```python
import torch

def _softmax(x, axis=-1):
    return torch.softmax(x, dim=axis)

def _gelu(x):
    return torch.nn.functional.gelu(x, approximate='tanh')

def _silu(x):
    return torch.nn.functional.silu(x)

def _sigmoid(x):
    return torch.sigmoid(x)

def _layernorm(x, axis=-1, eps=1e-05):
    m = x.mean(dim=axis, keepdim=True)
    v = x.var(dim=axis, keepdim=True, unbiased=False)
    return (x - m) / torch.sqrt(v + eps)

def _scatter_add(tgt, idx, src, axis=0):
    return tgt.clone().index_add_(axis, idx, src)

def _segment_sum(src, seg, n):
    out = torch.zeros((n,) + src.shape[1:], dtype=src.dtype)
    return out.index_add_(0, seg, src)

def _pad(x, pw):
    flat = tuple((v for pair in reversed(pw) for v in pair))
    return torch.nn.functional.pad(x, flat)
kernel_name = 'standalone__video_video_swin__vision'

def setup_inputs():

    _w = lambda a: (lambda t: t.to(torch.bfloat16) if t.is_floating_point() else t)(torch.as_tensor(a))
    T = 4
    H = W = 512 // 7 * 7
    C, win = (96, 7)
    return {'x': _w((lambda *s: torch.randn(*s, dtype=torch.bfloat16) * 0.1)(1, T, H, W, C)), 'wqkv_t': _w((lambda *s: torch.randn(*s, dtype=torch.bfloat16) * 0.1)(C, 3 * C)), 'wo_t': _w((lambda *s: torch.randn(*s, dtype=torch.bfloat16) * 0.1)(C, C)), 'wqkv_s': _w((lambda *s: torch.randn(*s, dtype=torch.bfloat16) * 0.1)(C, 3 * C)), 'wo_s': _w((lambda *s: torch.randn(*s, dtype=torch.bfloat16) * 0.05)(C, C))}

def reference(x, wqkv_t, wo_t, wqkv_s, wo_s):
    B, T, H, W, C = x.shape
    win = 7
    scale = C ** (-0.5)
    h = _layernorm(x, axis=-1)
    ht = (lambda x, *d: x.permute(*d))(h, 0, 2, 3, 1, 4).reshape(B * H * W, T, C)
    qkv = ht @ wqkv_t
    q, k, v = (qkv[..., :C].clone(), qkv[..., C:2 * C].clone(), qkv[..., 2 * C:].clone())
    p = _softmax(torch.einsum('btc,bsc->bts', q, k) * scale, axis=-1)
    ot = torch.einsum('bts,bsc->btc', p, v) @ wo_t
    ot = (lambda x, *d: x.permute(*d))(ot.reshape(B, H, W, T, C), 0, 3, 1, 2, 4)
    x = x + ot
    h = _layernorm(x, axis=-1)
    nh, nw = (H // win, W // win)
    hs = h.reshape(B * T, nh, win, nw, win, C)
    hw = (lambda x, *d: x.permute(*d))(hs, 0, 1, 3, 2, 4, 5).reshape(B * T * nh * nw, win * win, C)
    qkv = hw @ wqkv_s
    q, k, v = (qkv[..., :C].clone(), qkv[..., C:2 * C].clone(), qkv[..., 2 * C:].clone())
    p = _softmax(torch.einsum('bqc,bkc->bqk', q, k) * scale, axis=-1)
    os = torch.einsum('bqk,bkc->bqc', p, v) @ wo_s
    os = (lambda x, *d: x.permute(*d))(os.reshape(B * T, nh, nw, win, win, C), 0, 1, 3, 2, 4, 5)
    os = os.reshape(B, T, H, W, C)
    return x + os
```

```cpp
#define NVI 18
#define LTOK_ 208
#define FPAD 32
#define NPR 27
#define NWC 19
#define CFP 3
#include <hip/hip_runtime.h>
#include <hip/hip_bf16.h>

#ifdef __HIP_DEVICE_COMPILE__
#define NOPK __attribute__((target("no-packed-fp32-ops")))
#else
#define NOPK
#endif
#define DI __device__ __forceinline__ NOPK

using bf16x8 = __attribute__((ext_vector_type(8))) short;
using f32x16 = __attribute__((ext_vector_type(16))) float;
using f32x4 = __attribute__((ext_vector_type(4))) float;
using f32x2 = __attribute__((ext_vector_type(2))) float;
using u32x4 = __attribute__((ext_vector_type(4))) unsigned;
using u32x16 = __attribute__((ext_vector_type(16))) unsigned;
using u32x2 = __attribute__((ext_vector_type(2))) unsigned;
using bf16x4 = __attribute__((ext_vector_type(4))) short;
using bf2 = __attribute__((ext_vector_type(2))) __bf16;
typedef __attribute__((address_space(3))) char* lds_ptr;

constexpr int TT = 4, HH = 511, WW = 511, CC = 96, NWX = 73, NWIN = NWX * NWX;
constexpr int TOKB = CC * 2;
constexpr int ROWB = WW * TOKB;
constexpr unsigned FRAMEB = (unsigned)HH * ROWB;
constexpr unsigned TOTALB = FRAMEB * TT;
constexpr int LTOK = LTOK_;
constexpr int LFRAME = 48 * LTOK + FPAD;
constexpr int LBUF = 4 * LFRAME;
constexpr int WMAT = 18 * 1024;
constexpr int WAUX = 1024;
constexpr int LSTASH = 10 * 4 * LTOK;
constexpr int LSTG = 6208;
constexpr int LDS_AUX = (NWC < 18 ? 72 - NPR - NWC : 54 - NPR) * 1024, LDS_TAB = LDS_AUX + WAUX, LDS_BUF = LDS_TAB + 2048, LDS_STASH = LDS_BUF + 2 * LBUF, LDS_CNT = LDS_STASH + LSTASH, LDS_STG = LDS_CNT + 64, LDS_SCRAP = LDS_STG + 4 * LSTG, LDS_EXT = LDS_SCRAP + 512, LDS_BYTES = 163840;
constexpr int LEXT = 4608;
static_assert(LDS_EXT + 4 * LEXT <= LDS_BYTES && NPR * 1024 <= LBUF, "LDS");
constexpr float EPS = 1e-5f;
constexpr int GRID = 256;
#define PRODUCER_HALF 1

#define U2F(x) __builtin_bit_cast(float, (unsigned)(x))
#define F2U(x) __builtin_bit_cast(unsigned, (float)(x))
DI unsigned pk2(float a, float b) { f32x2 v = {a, b}; bf2 r = __builtin_convertvector(v, bf2); return __builtin_bit_cast(unsigned, r); }

#define LP(T, a) ((__attribute__((address_space(3))) T*)(size_t)(unsigned)(a))
DI int opaque_lane() { int t = threadIdx.x; asm volatile("" : "+v"(t)); return t & 63; }
#define MF16(a, b, c) __builtin_amdgcn_mfma_f32_16x16x32_bf16(__builtin_bit_cast(bf16x8, a), __builtin_bit_cast(bf16x8, b), (c), 0, 0, 0)
constexpr float SCM = 0.10206207261596575f * 1.4426950408889634f * 96.f, SCV = 9.797958971132712f;
constexpr float SCT = SCM * 1.0028f;
#define BAR() do { asm volatile("" ::: "memory"); __builtin_amdgcn_s_waitcnt(0xc07f); __builtin_amdgcn_s_barrier(); asm volatile("" ::: "memory"); } while (0)
#define SB() __builtin_amdgcn_sched_barrier(0)
#define PIN(v) asm volatile("" : "+v"(v))
typedef short s16x4 __attribute__((ext_vector_type(4)));
using u32x8 = __attribute__((ext_vector_type(8))) unsigned;
constexpr unsigned RAWM = 18432;
constexpr unsigned RAW_T = LDS_BUF;
constexpr unsigned RAW_SV = (36 - NPR) * 1024, RAW_SO = LDS_EXT, RAW_SQ = LDS_BUF + LBUF, RAW_SK = RAW_SQ + RAWM;
constexpr unsigned SCR = LDS_SCRAP;
static_assert(RAW_SV + RAWM <= LDS_AUX && RAW_SO + RAWM <= LDS_BYTES && RAW_SK + RAWM <= LDS_STASH && RAW_T + 4 * RAWM <= LDS_STASH && NWC * 1024 <= LBUF, "fold LDS");
struct FoldLane { unsigned ra[2], ta[2]; };
DI FoldLane fold_lane(int lane) {
    const int i = lane & 15, g = lane >> 4, qq = (lane >> 2) & 3, p = lane & 3;
    FoldLane f;
#pragma unroll
    for (int a = 0; a < 2; ++a) {
        f.ra[a] = (8 * (i >> 2) + 4 * a + (i & 3)) * 192 + ((g ^ ((2 * (i >> 2) + a) & 3)) * 16);
        f.ta[a] = (8 * g + 4 * a + qq) * 192 + ((p ^ ((2 * g + a) & 3)) * 16);
    }
    return f;
}
struct Job { unsigned l[2], r[2]; };
DI Job fold_job(const FoldLane& f, unsigned L, unsigned R, bool tr, int tau) {
    Job j;
#pragma unroll
    for (int a = 0; a < 2; ++a) { j.l[a] = L + f.ra[a]; j.r[a] = tr ? R + tau * 64 + f.ta[a] : R + tau * 6144 + f.ra[a]; PIN(j.l[a]); PIN(j.r[a]); }
    return j;
}
DI u32x4 rd128(unsigned a) { return *LP(u32x4, a); }
DI s16x4 rdtr(unsigned a) { return __builtin_amdgcn_ds_read_tr16_b64_v4i16(LP(s16x4, a)); }
DI u32x4 cat(s16x4 lo, s16x4 hi) { return __builtin_bit_cast(u32x4, __builtin_shufflevector(lo, hi, 0, 1, 2, 3, 4, 5, 6, 7)); }
DI float gsum(float v) {
    auto q = __builtin_amdgcn_permlane32_swap(F2U(v), F2U(v), false, false); v = U2F(q[0]) + U2F(q[1]);
    auto r = __builtin_amdgcn_permlane16_swap(F2U(v), F2U(v), false, false); return U2F(r[0]) + U2F(r[1]);
}
template <bool TR, class H> DI void fold_tile(const Job& j, f32x4 (&D)[6][2], H mid) {
    constexpr int NB = TR ? 2 : 1, NK = 6 + 2 * NB, NR = 3 * NK;
    u32x4 A[3][6], B[3][2]; s16x4 Bh[3][2][2];
    int nxt = 0;
    auto upto = [&](int n) NOPK {
#pragma unroll
        for (int r = 0; r < NR; ++r) if (r >= nxt && r < n) {
            const int K = r / NK, q = r % NK, isb = q < NB ? 0 : (q > NB && q <= 2 * NB) ? 1 : -1;
            if (isb >= 0) {
                if (TR) { const int h = isb ? q - NB - 1 : q; Bh[K][isb][h] = rdtr(j.r[h] + K * 6144 + isb * 8); if (h) B[K][isb] = cat(Bh[K][isb][0], Bh[K][isb][1]); }
                else B[K][isb] = rd128(j.r[isb] + K * 64);
            } else { const int s = q == NB ? 0 : q - 2 * NB; A[K][s] = rd128(j.l[s & 1] + (s >> 1) * 6144 + K * 64); }
        }
        if (n > nxt) nxt = n < NR ? n : NR;
    };
    upto(15); SB();
#pragma unroll
    for (int K = 0; K < 2; ++K)
#pragma unroll
        for (int s = 0; s < 6; ++s) {
            D[s][0] = MF16(A[K][s], B[K][0], K ? D[s][0] : f32x4{}); D[s][1] = MF16(A[K][s], B[K][1], K ? D[s][1] : f32x4{});
            upto(K * NK + (s ? s + 2 * NB : 2 * NB) + 15); SB();
        }
    upto(NR); mid(); SB();
#pragma unroll
    for (int s = 0; s < 6; ++s) D[s][0] = MF16(A[2][s], B[2][0], D[s][0]);
    SB();
#pragma unroll
    for (int s = 0; s < 6; ++s) D[s][1] = MF16(A[2][s], B[2][1], D[s][1]);
}
template <bool TR, class H1, class H2> DI void fold_vec(const Job& j, f32x4 (&D)[6], H1 mid1, H2 mid2) {
    constexpr int NB = TR ? 2 : 1, N1 = 18 * NB, NR = N1 + 18;
    u32x4 A[18], A2[18], Bf[3]; s16x4 Ah[18][2];
    const u32x4 ones = {0x3f803f80u, 0x3f803f80u, 0x3f803f80u, 0x3f803f80u};
    int nxt = 0;
    auto upto = [&](int n) NOPK {
#pragma unroll
        for (int r = 0; r < NR; ++r) if (r >= nxt && r < n) {
            if (r < N1) {
                const int e = r / NB, h = r % NB, K = e / 6, s = e % 6;
                if (TR) { Ah[e][h] = rdtr(j.r[h] + K * 6144 + (s >> 1) * 64 + (s & 1) * 8); if (h) A[e] = cat(Ah[e][0], Ah[e][1]); }
                else A[e] = rd128(j.r[s & 1] + (s >> 1) * 6144 + K * 64);
            } else { const int e = r - N1, K = e / 6, s = e % 6; A2[e] = rd128(j.l[s & 1] + (s >> 1) * 6144 + K * 64); }
        }
        if (n > nxt) nxt = n < NR ? n : NR;
    };
    upto(15); SB();
#pragma unroll
    for (int e = 0; e < 18; ++e) { D[e % 6] = MF16(A[e], ones, e >= 6 ? D[e % 6] : f32x4{}); upto((e + 1) * NB + 14); SB(); }
    upto(NR); SB();
    mid1();
#pragma unroll
    for (int S = 0; S < 3; ++S) Bf[S] = u32x4{pk2(D[2 * S][0], D[2 * S][1]), pk2(D[2 * S][2], D[2 * S][3]), pk2(D[2 * S + 1][0], D[2 * S + 1][1]), pk2(D[2 * S + 1][2], D[2 * S + 1][3])};
    mid2();
#pragma unroll
    for (int e = 0; e < 18; ++e) D[e % 6] = MF16(A2[e], Bf[e / 6], e >= 6 ? D[e % 6] : f32x4{});
    const f32x4 c4 = ((D[0] + D[1]) + (D[2] + D[3])) + (D[4] + D[5]);
    const float cs = gsum((c4[0] + c4[1]) + (c4[2] + c4[3])) * (1.f / 96.f);
#pragma unroll
    for (int s = 0; s < 6; ++s) D[s] -= cs;
}
DI void fold_center(f32x4 (&D)[6][2], float sc) {
#pragma unroll
    for (int tb = 0; tb < 2; ++tb) {
        const f32x4 c4 = ((D[0][tb] + D[1][tb]) + (D[2][tb] + D[3][tb])) + (D[4][tb] + D[5][tb]);
        const float nb = gsum((c4[0] + c4[1]) + (c4[2] + c4[3])) * (-sc / 96.f);
#pragma unroll
        for (int s = 0; s < 6; ++s) D[s][tb] = D[s][tb] * sc + nb;
    }
}
template <class H> DI void fold_rows(f32x4 (&D)[6][2], int g, H mid) {
    f32x4 q[6];
#pragma unroll
    for (int s = 0; s < 6; ++s) q[s] = *LP(f32x4, SCR + (32 * (s >> 1) + 8 * g + 4 * (s & 1)) * 4);
    SB(); mid(); SB();
#pragma unroll
    for (int s = 0; s < 6; ++s) { D[s][0] += q[s]; D[s][1] += q[s]; }
}
DI void fold_pack(const f32x4 (&D)[6][2], u32x4 (&O)[6]) {
#pragma unroll
    for (int n = 0; n < 6; ++n)
#pragma unroll
        for (int j = 0; j < 4; ++j) O[n][j] = pk2(D[2 * (n % 3) + (j >> 1)][n / 3][2 * (j & 1)], D[2 * (n % 3) + (j >> 1)][n / 3][2 * (j & 1) + 1]);
}
DI void fold_store(const u32x4 (&O)[6], int lane, int tau, int lim, unsigned b1, unsigned b2) {
#pragma unroll
    for (int n = 0; n < 6; ++n) { const int f = 6 * tau + n; *LP(u32x4, (f < lim ? b1 : b2) + f * 1024 + lane * 16) = O[n]; }
}
template <bool WIN> DI void fold_store_p(const u32x4 (&O)[6], int lane, int tau, int lim, unsigned b1, unsigned b2) {
    const int i = lane & 15;
#pragma unroll
    for (int tb = 0; tb < 2; ++tb) {
        const int c = 32 * tau + 8 * (i >> 2) + 4 * tb + (i & 3), r = c / 24, o = c - 24 * r, q = WIN ? o / 6 : o & 3, t = WIN ? o - 6 * q : o >> 2;
#pragma unroll
        for (int S = 0; S < 3; ++S) { const int f = 3 * t + S; *LP(u32x4, (f < lim ? b1 : b2) + f * 1024 + ((lane & 48) + 4 * q + r) * 16) = O[3 * tb + S]; }
    }
}
DI void row_means(const f32x4 (&D)[6], int lane, float sc) {
    if ((lane & 15) == 0) {
#pragma unroll
        for (int s = 0; s < 6; ++s) *LP(f32x4, SCR + (32 * (s >> 1) + 8 * (lane >> 4) + 4 * (s & 1)) * 4) = D[s] * (-sc / 96.f);
    }
}
struct RawLane { unsigned vo[3]; };
DI RawLane raw_lane(int lane, unsigned rs, int mode) {
    RawLane r;
#pragma unroll
    for (int m = 0; m < 3; ++m) {
        const int n = 64 * m + lane, row = n / 12, pc = n - 12 * row, src = mode == 0 ? row : mode == 1 ? 24 * (row >> 3) + (row & 7) : 24 * (row & 3) + 12 * ((row >> 2) & 1) + 2 * (row >> 3);
        r.vo[m] = src * rs + (pc ^ ((row >> 2) & 3)) * 16; PIN(r.vo[m]);
    }
    return r;
}
DI u32x4 make_srd(unsigned lo, unsigned hi, unsigned bytes) { return u32x4{lo, hi & 0xffffu, bytes, 0x00020000u}; }
DI void raw_dma(const RawLane& r, const u32x4& srd, unsigned rs, unsigned col, int half, unsigned dst, int mode) {
    const unsigned d = dst + half * 9216;
#pragma unroll
    for (int m = 0; m < 9; ++m) {
        const unsigned G = 3 * half + m / 3, so = (mode == 0 ? 16 * G : mode == 1 ? 48 * (G & 1) + 8 * (G >> 1) : 4 * (G >> 1) + (G & 1)) * rs + col;
        asm volatile("s_mov_b32 m0, %3\n\ts_nop 0\n\tbuffer_load_dwordx4 %0, %1, %2 offen lds" :: "v"(r.vo[m % 3]), "s"(srd), "s"(so), "s"(d + m * 1024) : "memory");
    }
}
DI int xpiece(int s) { return 3 * (s & 3) + (s >> 2); }
DI void csync(int idx) {
    unsigned a = LDS_CNT + 32 + 4 * idx, v = 1, t;
    asm volatile("s_waitcnt lgkmcnt(0)\n\ts_mov_b64 exec, 1\n\tds_add_u32 %0, %1\n\ts_mov_b64 exec, -1\n\ts_mov_b32 s41, 0x400000\n"
                 "CS%=:\n\tds_read_b32 %1, %0\n\ts_waitcnt lgkmcnt(0)\n\tv_readfirstlane_b32 %2, %1\n\ts_nop 3\n\ts_cmp_ge_i32 %2, 4\n\ts_cbranch_scc1 CD%=\n\ts_sleep 1\n\ts_sub_u32 s41, s41, 1\n\ts_cbranch_scc0 CS%=\n\ts_endpgm\nCD%=:"
                 : "+v"(a), "+v"(v), "=s"(t) :: "s41", "scc", "memory");
}

DI void csignal(int idx) {
    unsigned a = LDS_CNT + 32 + 4 * idx, v = 1;
    asm volatile("s_waitcnt lgkmcnt(0)\n\ts_mov_b64 exec, 1\n\tds_add_u32 %0, %1\n\ts_mov_b64 exec, -1" :: "v"(a), "v"(v) : "memory");
}
DI void cwait(int idx) {
    unsigned a = LDS_CNT + 32 + 4 * idx, v, t;
    asm volatile("s_mov_b32 s41, 0x400000\n"
                 "CS%=:\n\tds_read_b32 %0, %2\n\ts_waitcnt lgkmcnt(0)\n\tv_readfirstlane_b32 %1, %0\n\ts_nop 3\n\ts_cmp_ge_i32 %1, 1\n\ts_cbranch_scc1 CD%=\n\ts_sleep 1\n\ts_sub_u32 s41, s41, 1\n\ts_cbranch_scc0 CS%=\n\ts_endpgm\nCD%=:"
                 : "=&v"(v), "=s"(t) : "v"(a) : "s41", "scc", "memory");
}

#define CLOBBERS "v18", "v19", "v20", "v21", "v22", "v23", "v24", "v25", "v26", "v27", "v28", "v29", "v30", "v31", "v32", "v33", "v34", "v35", "v36", "v37", "v38", "v39", "v40", "v41", "v42", "v43", "v44", "v45", "v46", "v47", "v48", "v49", "v50", "v51", "v52", "v53", "v54", "v55", "v56", "v57", "v58", "v59", "v60", "v61", "v62", "v63", "v64", "v65", "v66", "v67", "v68", "v69", "v70", "v71", "v72", "v73", "v74", "v75", "v76", "v77", "v78", "v79", "v80", "v81", "v82", "v83", "v84", "v85", "v86", "v87", "v88", "v89", "v90", "v91", "v92", "v93", "v94", "v95", "v96", "v97", "v98", "v99", "v100", "v101", "v102", "v103", "v104", "v105", "v106", "v107", "v108", "v109", "v110", "v111", "v112", "v113", "v114", "v115", "v116", "v117", "v118", "v119", "v120", "v121", "v122", "v123", "v124", "v125", "v126", "v127", "v128", "v129", "v130", "v131", "v132", "v133", "v134", "v135", "v136", "v137", "v138", "v139", "v140", "v141", "v142", "v143", "v144", "v145", "v146", "v147", "v148", "v149", "v150", "v151", "v152", "v153", "v154", "v155", "v156", "v157", "v158", "v159", "v160", "v161", "v162", "v163", "v164", "v165", "v166", "v167", "v168", "v169", "v170", "v171", "v172", "v173", "v174", "v175", "v176", "v177", "v178", "v179", "v180", "v181", "v182", "v183", "v184", "v185", "v186", "v187", "v188", "v189", "v190", "v191", "v192", "v193", "v194", "v195", "v196", "v197", "v198", "v199", "v200", "v201", "v202", "v203", "v204", "v205", "v206", "v207", "v208", "v209", "v210", "v211", "v212", "v213", "v214", "v215", "v216", "v217", "v218", "v219", "v220", "v221", "v222", "v223", "v224", "v225", "v226", "v227", "v228", "v229", "v230", "v231", "v232", "v233", "v234", "v235", "v236", "v237", "v238", "v239", "v240", "v241", "v242", "v243", "v244", "v245", "v246", "v247", "v248", "v249", "v250", "v251", "v252", "v253", "v254", "v255", "s28", "s29", "s30", "s31", "s34", "s35", "s36", "s37", "s38", "s39", "s40", "s41", "s42", "s43", "s44", "s45", "s46", "s47", "s48", "s49", "s50", "s51", "s52", "s53", "s54", "s55", "s56", "s57", "s58", "s59", "s60", "s61", "s62", "s63", "s64", "s65", "s66", "s67", "s68", "s69", "s70", "s71", "s72", "s73", "s74", "s75", "s76", "s77", "vcc", "scc", "memory"
__global__ __launch_bounds__(512, 2) NOPK void swin_kernel(const char* __restrict__ x_, const char* __restrict__ wqkv_t, const char* __restrict__ wo_t, const char* __restrict__ wqkv_s,
                                                           const char* __restrict__ wo_s, char* __restrict__ out_, const char* wf) {
    u32x8 ka; u32x4 kb; u32x2 kp;
    const unsigned long long kap = (unsigned long long)__builtin_amdgcn_kernarg_segment_ptr();
    asm volatile("s_load_dwordx8 %0, %2, 0x0\n\ts_load_dwordx4 %1, %2, 0x20" : "=&s"(ka), "=&s"(kb) : "s"(kap));
    SB();
    const unsigned pj = (blockIdx.x >> 3) & 15, pm = ((blockIdx.x >> 7) & 1) * 16 + (pj >= 12 ? 16 : 8);
    asm volatile("s_load_dwordx2 %0, %1, %2" : "=&s"(kp) : "s"(kap), "s"(pm));
    SB();
    const int wid = __builtin_amdgcn_readfirstlane(threadIdx.x >> 6) & 7, k = wid & 3;
    const unsigned role = (wid >> 2) ^ (PRODUCER_HALF ^ 1);
    if (wid == 4) __builtin_amdgcn_s_setprio(3);
    const unsigned rsT = wid < 6 ? 576 : 192;
    const RawLane rl = raw_lane(opaque_lane(), rsT, wid < 6);
    const int b = blockIdx.x, vb = (b & 7) * (GRID / 8) + (b >> 3);
    const int w0 = (vb * NWIN) / GRID, nwin = ((vb + 1) * NWIN) / GRID - w0;
    const int wy0 = w0 / NWX, wx0 = w0 - wy0 * NWX;
    unsigned base0 = (unsigned)(wy0 * 7) * ROWB + wx0 * 7 * TOKB;
    asm volatile("" : "+s"(base0));
    unsigned vW = 0;
    unsigned psz = pj >= 12 ? 18432 : 55296, pso = (pj >= 12 ? pj - 12 : pj) * 4608;
    asm volatile("" : "+s"(psz), "+s"(pso));
    if (wid == 4) { const int lane = opaque_lane(); vW = lane < 36 ? lane * 128 : 0x7fffffffu; PIN(vW); }
    SB();
    asm volatile("s_waitcnt lgkmcnt(0)" : "+s"(ka), "+s"(kb), "+s"(kp));
#define TS(i)
#define TSF(i)
    TS(1);
    if (wid == 4) {
        asm volatile("s_mov_b32 m0, %3\n\ts_nop 0\n\tbuffer_load_dword %0, %1, %2 offen lds" :: "v"(vW), "s"(make_srd(kp[0], kp[1], psz)), "s"(pso), "s"(LDS_STASH) : "memory");
        __builtin_amdgcn_s_setprio(0);
    }
    const u32x4 srdT = wid < 6 ? make_srd(ka[2], ka[3], 55296) : make_srd(ka[4], ka[5], 18432);
    const unsigned long long xb = (((unsigned long long)ka[1] << 32) | ka[0]) - 2048;
    const u32x4 srd = role ? make_srd((unsigned)xb, (unsigned)(xb >> 32), TOTALB + 2048) : make_srd(kb[2], kb[3], TOTALB);
    const unsigned long long outp = ((unsigned long long)kb[3] << 32) | kb[2];
    if (!role) raw_dma(rl, srdT, 576, (wid >> 1) * 192, wid & 1, RAW_T + (wid >> 1) * RAWM, 1);
    TS(2);
    unsigned vi[NVI], si[8];
    if (role) {
        const int lane = opaque_lane(), p = (lane >> 2) & 3, t = lane & 3, g = lane >> 4;
        const unsigned wl = lane * 16, lm = p == g ? ~0u : 0u, idp = LDS_AUX + 576 + (t == g ? 10 + p : 0) * 16;
        const unsigned i0 = k, sidx = 0xdddd;
        unsigned vA[3], vB[3], vL[3];
#pragma unroll
        for (int m = 0; m < 3; ++m) {
            const int n = 64 * m + lane, tf = n / 96, rem = n % 96, q = rem / 12;
            const unsigned c = (unsigned)tf * FRAMEB + xpiece((rem % 12) ^ tf) * 16 + 2048 - 1024 * m;
            vA[m] = c + q * TOKB; vB[m] = vA[m] + (ROWB - 7 * TOKB);
            vL[m] = (c + 6 * ROWB + 6 * TOKB) | ((unsigned)(2 * (q & 3) + (q >> 2)) << 28);
        }
        const unsigned vA0 = vA[0], vA1 = vA[1], vA2 = vA[2], vB0 = vB[0], vB1 = vB[1], vB2 = vB[2];
        const unsigned dA = LDS_BUF + t * LFRAME + p * LTOK + g * 16;
        const unsigned rA = LDS_BUF + t * LFRAME + (p == g ? p : 4 + g) * LTOK + 192;
        const unsigned dL = 2 * p * (4 * LTOK) + t * LTOK + g * 16, rL = (p == g ? 2 * p : 2 * g + 1) * (4 * LTOK) + t * LTOK + 192;
        if (k == 0) *LP(u32x4, LDS_TAB + lane * 16) = u32x4{vL[0], vL[1], vL[2], dL | (rL << 16)};
        const unsigned cd0 = dA + k * 8 * LTOK, cr0 = rA + k * 8 * LTOK, ca0 = LDS_CNT, cv00 = 0;
        const unsigned stg = LDS_STG + k * LSTG;
        const unsigned xr = stg + (t >> 1) * 3104 + ((t & 1) * 8 + p) * TOKB + (g ^ (t & 1)) * 16;
        const unsigned axp = LDS_AUX + (t < 2 ? t : 2) * 192 + g * 16;
        vi[0] = wl; vi[1] = xr; vi[2] = axp; vi[3] = lm; vi[4] = idp; vi[5] = rA; vi[6] = dA; vi[7] = vA0; vi[8] = vA1; vi[9] = vA2; vi[10] = vB0; vi[11] = vB1; vi[12] = vB2; vi[13] = sidx; vi[14] = cd0; vi[15] = cr0; vi[16] = ca0; vi[17] = cv00; si[0] = stg; si[1] = i0; si[2] = 0; si[3] = 0;
    } else {
        const int lane = opaque_lane(), i = lane & 15, g = lane >> 4;
        auto phi = [](int v) NOPK { return v ^ (v >> 1); };
        const int dg = 16 * (2 * (g & 1) + (g >> 1)), pix = 4 * (i & 3) + phi(i >> 2);
        const unsigned wl = lane * 16, fr = LDS_BUF + k * LFRAME;
        const unsigned ta0 = fr + pix * LTOK + dg, t3b = LDS_STASH + k * LTOK + dg, zr = LDS_AUX + 384 + g * 16, t30 = zr, ex0 = zr, ca0 = LDS_CNT;
        const int ro = 96 * ((i >> 2) & 1) + 8 * (i >> 3);
        const unsigned xa0 = fr + (4 * (i & 3) + phi(g)) * LTOK + ro;
        const unsigned ga0 = phi(g) * TOKB + 48 * (i & 3) + 12 * (i >> 2), WR_ = ROWB - 7 * TOKB;
        const unsigned ga1 = ga0 + (phi(g) >= 1 ? WR_ : 0), ga2 = ga0 + (phi(g) >= 2 ? WR_ : 0), ga3 = ga0 + (phi(g) >= 3 ? WR_ : 0);
        const unsigned idp = LDS_AUX + 576 + (10 + (i & 3)) * 16;
        const unsigned pr = i ? zr : LDS_SCRAP + k * 128 + g * 16, pw = LDS_SCRAP + k * 128 + (g >> 1) * 64 + (i >> 2) * 16 + (g & 1) * 8 + (i & 3) * 2 + (lane == 49 ? 2 : 0);
        const unsigned ext = LDS_EXT + k * LEXT, t3g = (i & 7) * (4 * LTOK) + dg;
        const unsigned exqr = ext + g * 16, exvr = ext + 1536 + i * 24;
        const int w3 = (4 * g + (i & 3)) & 7;
        const unsigned x3g = w3 * (4 * LTOK) + ro;
        const unsigned rkd = fr + phi(g) * LTOK + 192 - xa0, rqd = fr + pix * LTOK + 192 - xa0;
        const unsigned rla0 = lane < 48 ? fr + (16 * g + pix) * LTOK + 192 : LDS_AUX + 384, tog = lane < 48 ? rla0 ^ (rla0 + LBUF) : 0;
        if (k == 2) *LP(u32x4, LDS_TAB + 1024 + lane * 16) = u32x4{t3g | (x3g << 16), (i & 7) * 192u | ((g & 1) * 1536u << 16), rkd, rqd};
        const unsigned kfr = (unsigned)k * FRAMEB, rab = LDS_STASH + k * LTOK + 192;
        vi[0] = wl; vi[1] = idp; vi[2] = tog; vi[3] = pw; vi[4] = pr; vi[5] = ta0; vi[6] = t30; vi[7] = xa0; vi[8] = rla0; vi[9] = exqr; vi[10] = exvr; vi[11] = ga0; vi[12] = ga1; vi[13] = ga2; vi[14] = ga3; vi[15] = ex0; vi[16] = t3b; vi[17] = ca0; si[0] = kfr; si[1] = rab; si[2] = 0; si[3] = 0;
    }
#pragma unroll
    for (int i = 0; i < NVI; ++i) PIN(vi[i]);
    unsigned vF[3];
    FoldLane f; Job jt;
    {
        const int lane = opaque_lane();
        f = fold_lane(lane);
        jt = fold_job(f, RAW_T + (role ? 2 * RAWM : 0), RAW_T + (role ? 3 * RAWM : RAWM), (role != 0) == (k < 3), k < 3 ? k : 0);
        if (!role && k < 3) {
#pragma unroll
            for (int a = 0; a < 2; ++a) { jt.r[a] = RAW_T + RAWM + k * 6144 + (16 * a + (lane & 15)) * 192 + (((lane >> 4) ^ ((lane >> 2) & 3)) * 16); PIN(jt.r[a]); }
        }
        if (role) {
#pragma unroll
            for (int m = 0; m < 3; ++m) {
                const int n = 64 * m + lane, tf = n / 96, rem = n % 96, q = rem / 12;
                vF[m] = 2048 + (unsigned)tf * FRAMEB + xpiece((rem % 12) ^ tf) * 16 + q * TOKB + (q + k >= 7 ? ROWB - 7 * TOKB : 0) + k * (ROWB + TOKB); PIN(vF[m]);
            }
        } else {
            if (threadIdx.x < 16) *LP(int, LDS_CNT + threadIdx.x * 4) = 0;
            if (wid == 1) {
                if (lane < 48) { *LP(unsigned, LDS_AUX + lane * 4) = 0x3f803f80u; *LP(unsigned, LDS_AUX + 384 + lane * 4) = 0u; }
#pragma unroll
                for (int q = 0; q < 2; ++q) { const int e = q * 64 + lane;
                    if (e < 112) *LP(unsigned, LDS_AUX + 576 + e * 4) = (e >= 40 && e < 72 && ((e & 3) == ((e - 40) >> 3))) ? ((e & 4) ? 0x3f800000u : 0x3f80u) : 0u; }
            }
        }
    }
    SB();
    {
        const int lane = opaque_lane(), g = lane >> 4;
        f32x4 D[6][2]; u32x4 O[6];
        auto none = []() NOPK {}; auto bar = []() NOPK { BAR(); };
        if (role) {
            raw_dma(rl, srdT, rsT, wid < 6 ? 384 : 0, wid & 1, RAW_T + (wid >> 1) * RAWM, wid < 6);
            const RawLane r2 = raw_lane(lane, rsT, wid < 6 ? 2 : 0);
            TS(3);
            BAR();
            TS(4);
            raw_dma(r2, wid < 6 ? make_srd(ka[6], ka[7], 55296) : make_srd(kb[0], kb[1], 18432), rsT, wid < 6 ? 384 : 0, wid & 1, wid < 6 ? RAW_SV : RAW_SO, wid < 6 ? 2 : 0);
            const unsigned stg = LDS_STG + k * LSTG, so1 = base0 + 2 * FRAMEB;
            asm volatile("s_mov_b32 m0, %5\n\ts_nop 0\n\tbuffer_load_dwordx4 %0, %3, %4 offen nt lds\n\ts_add_u32 m0, %5, 1024\n\ts_nop 0\n\tbuffer_load_dwordx4 %1, %3, %4 offen nt lds\n\t"
                         "s_add_u32 m0, %5, 2048\n\ts_nop 0\n\tbuffer_load_dwordx4 %2, %3, %4 offen nt lds" :: "v"(vF[0]), "v"(vF[1]), "v"(vF[2]), "s"(srd), "s"(base0), "s"(stg) : "memory", "scc");
            asm volatile("s_mov_b32 m0, %5\n\ts_nop 0\n\tbuffer_load_dwordx4 %0, %3, %4 offen nt lds\n\ts_add_u32 m0, %5, 1024\n\ts_nop 0\n\tbuffer_load_dwordx4 %1, %3, %4 offen nt lds\n\t"
                         "s_add_u32 m0, %5, 2048\n\ts_nop 0\n\tbuffer_load_dwordx4 %2, %3, %4 offen nt lds" :: "v"(vF[0]), "v"(vF[1]), "v"(vF[2]), "s"(srd), "s"(so1), "s"(stg + 3104) : "memory", "scc");
            TS(5);
            asm volatile("s_waitcnt vmcnt(15)" ::: "memory");
            TS(6);
            BAR();
            TS(7);
            if (k < 3) {
                fold_tile<true>(jt, D, bar); TS(8); fold_center(D, SCV); fold_pack(D, O);
                fold_store_p<false>(O, lane, k, NPR - 18, LDS_BUF + 18 * 1024, -(NPR - 18) * 1024);
            } else {
                f32x4 E[6]; fold_vec<false>(jt, E, none, bar);
#pragma unroll
                for (int s = 0; s < 6; ++s) E[s] *= SCV;
                if ((lane & 15) == 0) {
#pragma unroll
                    for (int S = 0; S < 3; ++S)
                        *LP(u32x4, LDS_AUX + 192 + (32 * S + 8 * g) * 2) = u32x4{pk2(E[2 * S][0], E[2 * S][1]), pk2(E[2 * S][2], E[2 * S][3]), pk2(E[2 * S + 1][0], E[2 * S + 1][1]), pk2(E[2 * S + 1][2], E[2 * S + 1][3])};
                }
            }
            asm volatile("s_waitcnt vmcnt(6)" ::: "memory");
            TS(9);
            BAR();
            TS(10);
        } else {
            asm volatile("s_waitcnt vmcnt(0)" ::: "memory");
            TS(3);
            BAR();
            TS(4);
            const u32x4 srdS = make_srd(ka[6], ka[7], 55296);
            if (k < 3) { fold_tile<false>(jt, D, none); BAR(); fold_center(D, SCT); TS(5); BAR(); TS(6); fold_rows(D, g, none); fold_pack(D, O); fold_store(O, lane, k, 18, LDS_BUF, 0); BAR(); }
            else {
                f32x4 E[6]; fold_vec<true>(jt, E, bar, none); row_means(E, lane, SCT); TS(5); BAR(); TS(6);
                const RawLane r5 = raw_lane(lane, 576, 2);
                BAR();
                __builtin_amdgcn_s_setprio(CFP);
                raw_dma(r5, srdS, 576, 192, 0, RAW_SK, 2); raw_dma(r5, srdS, 576, 192, 1, RAW_SK, 2); raw_dma(r5, srdS, 576, 0, 0, RAW_SQ, 2); raw_dma(r5, srdS, 576, 0, 1, RAW_SQ, 2);
            }
            TS(7);
            __builtin_amdgcn_s_setprio(CFP);
            u32x4 OV[6];
            if (k < 3) {
                fold_tile<true>(fold_job(f, RAW_SV, RAW_SO, true, k), D, none); fold_center(D, SCV); fold_pack(D, OV);
                TS(8);
                cwait(0);
                TS(9);
                fold_tile<false>(fold_job(f, RAW_SQ, RAW_SK, false, k), D, none); fold_center(D, SCM);
            } else {
                asm volatile("s_waitcnt vmcnt(0)" ::: "memory");
                csignal(0);
                TS(9);
                f32x4 E[6]; fold_vec<true>(fold_job(f, RAW_SQ, RAW_SK, true, 0), E, none, none); row_means(E, lane, SCM);
            }
            TSF(3);
            csync(2);
            TSF(4);
            if (k < 3) {
                fold_rows(D, g, [&]() NOPK { fold_store_p<true>(OV, lane, k, NWC - 18, LDS_BUF + LBUF + 18 * 1024, (36 - NPR - NWC + 18) * 1024); }); fold_pack(D, O);
                fold_store(O, lane, k, NWC, LDS_BUF + LBUF, (36 - NPR - NWC) * 1024);
            }
            TSF(5);
            csync(3);
            __builtin_amdgcn_s_setprio(0);
            TS(10);
        }
    }
    unsigned vo[NVI], so[8]; u32x4 srdo; char* outo;
#pragma unroll
    for (int i = 0; i < NVI; ++i) asm volatile("v_mov_b32 %0, %1" : "=v"(vo[i]) : "v"(vi[i]));
    si[4] = nwin; si[5] = base0; si[6] = wx0; si[7] = role;
#pragma unroll
    for (int i = 0; i < 8; ++i) asm volatile("s_mov_b32 %0, %1" : "=s"(so[i]) : "s"(__builtin_amdgcn_readfirstlane(si[i])));
    asm volatile("s_mov_b64 %0, %1" : "=s"(outo) : "s"(outp));
    { unsigned long long lo = ((unsigned long long)srd[1] << 32) | srd[0], l2; asm volatile("s_mov_b64 %0, %1" : "=s"(l2) : "s"(lo));
      srdo = u32x4{(unsigned)l2, (unsigned)(l2 >> 32), role ? TOTALB + 2048 : TOTALB, 0x00020000u}; }
    asm volatile(
        "s_cmp_eq_u32 %27, 0\n"
        "s_cbranch_scc1 CONS_%=\n"
        "v_add_u32 v166, 30720, %0\n"
        "ds_read_b128 v[18:21], v166 offset:0\n"
        "ds_read_b128 v[22:25], v166 offset:1024\n"
        "ds_read_b128 v[26:29], v166 offset:2048\n"
        "ds_read_b128 v[30:33], v166 offset:3072\n"
        "ds_read_b128 v[34:37], v166 offset:4096\n"
        "ds_read_b128 v[38:41], v166 offset:5120\n"
        "ds_read_b128 v[42:45], v166 offset:6144\n"
        "ds_read_b128 v[46:49], v166 offset:7168\n"
        "ds_read_b128 v[50:53], v166 offset:8192\n"
        "ds_read_b128 v[54:57], v166 offset:9216\n"
        "ds_read_b128 v[58:61], v166 offset:10240\n"
        "ds_read_b128 v[62:65], v166 offset:11264\n"
        "ds_read_b128 v[66:69], v166 offset:12288\n"
        "ds_read_b128 v[70:73], v166 offset:13312\n"
        "ds_read_b128 v[74:77], v166 offset:14336\n"
        "ds_read_b128 v[78:81], v166 offset:15360\n"
        "ds_read_b128 v[82:85], v166 offset:16384\n"
        "ds_read_b128 v[86:89], v166 offset:17408\n"
        "ds_read_b128 v[90:93], v166 offset:18432\n"
        "ds_read_b128 v[94:97], v166 offset:19456\n"
        "ds_read_b128 v[98:101], v166 offset:20480\n"
        "ds_read_b128 v[102:105], v166 offset:21504\n"
        "ds_read_b128 v[106:109], v166 offset:22528\n"
        "ds_read_b128 v[110:113], v166 offset:23552\n"
        "ds_read_b128 v[114:117], v166 offset:24576\n"
        "ds_read_b128 v[118:121], v166 offset:25600\n"
        "ds_read_b128 v[122:125], v166 offset:26624\n"
        "s_waitcnt vmcnt(0)\n"
        "ds_read_b128 v[128:131], %1 offset:0\n"
        "ds_read_b128 v[134:137], %1 offset:64\n"
        "ds_read_b128 v[140:143], %1 offset:128\n"
        "ds_read_b128 v[148:151], %1 offset:768\n"
        "ds_read_b128 v[154:157], %1 offset:832\n"
        "ds_read_b128 v[160:163], %1 offset:896\n"
        "ds_read_b64 v[220:221], %4\n"
        "ds_read_b64 v[214:215], %4\n"
        "ds_read_b64 v[224:225], %4\n"
        "ds_read_b64 v[228:229], %4\n"
        "s_mov_b32 s44, 0xf0000f\n"
        "s_mov_b32 s45, 0xf0000f00\n"
        "s_mov_b32 s46, 0xff000ff\n"
        "s_mov_b32 s47, 0xf00fff00\n"
        "s_mov_b32 s48, 0x22222222\n"
        "s_mov_b32 s49, 0x22222222\n"
        "s_mov_b32 s50, 0x44444444\n"
        "s_mov_b32 s51, 0x44444444\n"
        "s_mov_b32 s52, 0x88888888\n"
        "s_mov_b32 s53, 0x88888888\n"
        "s_mov_b64 vcc, s[44:45]\n"
        "s_mov_b32 s28, 0x3c2aaaab\n"
        "s_mov_b32 s29, 0x3a7ba882\n"
        "s_mov_b32 s30, 0\n"
        "s_mov_b32 s34, 0\n"
        "s_mov_b32 s31, %19\n"
        "s_mov_b32 s36, %19\n"
        "s_mov_b32 s37, %19\n"
        "s_add_u32 s71, %25, 588672\n"
        "s_sub_u32 s72, 73, %26\n"
        "s_add_u32 s73, %18, 3104\n"
        "s_mov_b32 s68, 4\n"
        "s_mov_b32 s41, 0x400000\n"
        "s_add_u32 s37, s37, 4\n"
        "s_cmp_ge_u32 s37, 49\n"
        "s_cselect_b32 s69, 49, 0\n"
        "s_sub_u32 s37, s37, s69\n"
        "s_sub_u32 s69, s37, 5\n"
        "s_cmp_lt_u32 s69, 4\n"
        "s_subb_u32 s36, s36, -4\n"
        "s_cmp_ge_u32 s36, 6\n"
        "s_cselect_b32 s69, 6, 0\n"
        "s_addc_u32 s34, s34, 0\n"
        "s_sub_u32 s36, s36, s69\n"
        "s_mul_i32 s38, s34, 1344\n"
        "s_cmp_ge_u32 s34, s72\n"
        "s_cselect_b32 s70, s71, %25\n"
        "s_add_u32 s38, s38, s70\n"
        "s_mul_i32 s69, s36, 98304\n"
        "s_add_u32 s38, s38, s69\n"
        "s_mov_b32 s35, s36\n"
        "s_mul_i32 s69, s36, -12\n"
        "s_add_u32 s69, s69, 60\n"
        "s_lshl_b64 s[54:55], -1, s69\n"
        "s_lshl_b64 s[56:57], s[54:55], 24\n"
        "s_ashr_i64 s[60:61], s[54:55], 8\n"
        "s_mov_b32 s58, s61\n"
        "s_mov_b32 s59, s56\n"
        "v_cndmask_b32_e64 %17, %7, %10, s[56:57]\n"
        "v_cndmask_b32_e64 v251, %8, %11, s[58:59]\n"
        "v_cndmask_b32_e64 v250, %9, %12, s[60:61]\n"
        "s_cmp_eq_u32 s37, 4\n"
        "s_cbranch_scc0 L11_%=\n"
        "s_nop 1\n"
        "ds_read_b32 %17, %0 offset:28672\n"
        "ds_read_b32 v251, %0 offset:28676\n"
        "ds_read_b32 v250, %0 offset:28680\n"
        "s_mov_b32 s35, 6\n"
        "s_mov_b32 s38, %25\n"
        "s_mov_b32 s69, 1344\n"
        "v_mov_b32 v169, 588672\n"
        "s_waitcnt lgkmcnt(0)\n"
        "v_lshrrev_b32 v168, 28, %17\n"
        "v_add_u32 v168, s34, v168\n"
        "v_cmp_le_u32 vcc, s72, v168\n"
        "v_and_b32 %17, 0x0fffffff, %17\n"
        "v_mad_u32_u24 %17, v168, s69, %17\n"
        "v_cndmask_b32 v168, 0, v169, vcc\n"
        "v_add_u32 %17, %17, v168\n"
        "v_lshrrev_b32 v168, 28, v251\n"
        "v_add_u32 v168, s34, v168\n"
        "v_cmp_le_u32 vcc, s72, v168\n"
        "v_and_b32 v251, 0x0fffffff, v251\n"
        "v_mad_u32_u24 v251, v168, s69, v251\n"
        "v_cndmask_b32 v168, 0, v169, vcc\n"
        "v_add_u32 v251, v251, v168\n"
        "v_lshrrev_b32 v168, 28, v250\n"
        "v_add_u32 v168, s34, v168\n"
        "v_cmp_le_u32 vcc, s72, v168\n"
        "v_and_b32 v250, 0x0fffffff, v250\n"
        "v_mad_u32_u24 v250, v168, s69, v250\n"
        "v_cndmask_b32 v168, 0, v169, vcc\n"
        "v_add_u32 v250, v250, v168\n"
        "s_mov_b64 vcc, s[44:45]\n"
        "L11_%=:\n"
        "s_cmp_lt_u32 s34, %24\n"
        "s_cselect_b32 s38, s38, 0x80000000\n"
        "s_add_u32 s39, s38, 100270464\n"
        "s_waitcnt lgkmcnt(0)\n"
        "s_mov_b64 exec, 1\n"
        "ds_inc_u32 %16, %13 offset:8\n"
        "s_mov_b64 exec, -1\n"
        "s_setprio 1\n"
        "PTOP_%=:\n"
        "s_waitcnt lgkmcnt(5)\n"
        "v_mfma_f32_16x16x32_bf16 v[144:147], v[128:131], v[128:131], 0\n"
        "ds_read_b128 v[164:167], %2\n"
        "v_cndmask_b32_e64 v255, v168, v169, s[48:49]\n"
        "s_waitcnt lgkmcnt(5)\n"
        "v_mfma_f32_16x16x32_bf16 v[172:175], v[148:151], v[148:151], 0\n"
        "ds_read_b128 v[176:179], %2 offset:64\n"
        "v_cndmask_b32_e64 v254, v180, v181, s[48:49]\n"
        "v_cndmask_b32_e64 v255, v255, v170, s[50:51]\n"
        "s_waitcnt lgkmcnt(5)\n"
        "v_mfma_f32_16x16x32_bf16 v[144:147], v[134:137], v[134:137], v[144:147]\n"
        "ds_read_b128 v[184:187], %2 offset:128\n"
        "v_cndmask_b32_e64 v254, v254, v182, s[50:51]\n"
        "v_cndmask_b32_e64 v255, v255, v171, s[52:53]\n"
        "s_waitcnt lgkmcnt(5)\n"
        "v_mfma_f32_16x16x32_bf16 v[172:175], v[154:157], v[154:157], v[172:175]\n"
        "ds_read_b32 v253, %16 offset:8\n"
        "s_mov_b32 m0, %18\n"
        "v_cndmask_b32_e64 v254, v254, v183, s[52:53]\n"
        "s_waitcnt lgkmcnt(5)\n"
        "v_mfma_f32_16x16x32_bf16 v[144:147], v[140:143], v[140:143], v[144:147]\n"
        "s_waitcnt lgkmcnt(4)\n"
        "buffer_load_dwordx4 %17, %22, s38 offen lds\n"
        "v_mfma_f32_16x16x32_bf16 v[172:175], v[160:163], v[160:163], v[172:175]\n"
        "s_waitcnt lgkmcnt(2)\n"
        "v_cndmask_b32_dpp v255, v254, v255, vcc row_ror:4 row_mask:0xf bank_mask:0xf\n"
        "v_mul_f32 v254, s28, v252\n"
        "v_mfma_f32_16x16x32_bf16 v[168:171], v[164:167], v[128:131], 0\n"
        "v_fma_f32 v254, -v254, v252, v255\n"
        "v_add_f32_e64 v254, s29, |v254|\n"
        "v_cndmask_b32_e64 v255, v144, v145, s[48:49]\n"
        "v_mfma_f32_16x16x32_bf16 v[168:171], v[176:179], v[134:137], v[168:171]\n"
        "s_waitcnt lgkmcnt(0)\n"
        "v_readfirstlane_b32 s40, v253\n"
        "s_cmp_lt_i32 s40, s68\n"
        "v_rsq_f32 v252, v254\n"
        "v_cndmask_b32_e64 v254, v172, v173, s[48:49]\n"
        "v_mfma_f32_16x16x32_bf16 v[168:171], v[184:187], v[140:143], v[168:171]\n"
        "buffer_load_dwordx4 v251, %22, s38 offen offset:1024 lds\n"
        "s_cbranch_scc1 L3_%=\n"
        "L4_%=:\n"
        "v_cndmask_b32_e64 v255, v255, v146, s[50:51]\n"
        "v_cndmask_b32_e64 v254, v254, v174, s[50:51]\n"
        "v_cndmask_b32_e64 v255, v255, v147, s[52:53]\n"
        "v_cndmask_b32_e64 v254, v254, v175, s[52:53]\n"
        "v_mfma_f32_16x16x32_bf16 v[144:147], v[184:187], v[160:163], 0\n"
        "s_mov_b64 exec, s[46:47]\n"
        "ds_write_b32 %15, v252\n"
        "s_mov_b64 exec, -1\n"
        "v_cndmask_b32_dpp v255, v254, v255, vcc row_ror:4 row_mask:0xf bank_mask:0xf\n"
        "v_mfma_f32_16x16x32_bf16 v[144:147], v[176:179], v[154:157], v[144:147]\n"
        "buffer_load_dwordx4 v250, %22, s38 offen offset:2048 lds\n"
        "v_mfma_f32_16x16x32_bf16 v[144:147], v[164:167], v[148:151], v[144:147]\n"
        "s_mov_b32 m0, s73\n"
        "ds_write_b128 %14, v[188:191]\n"
        "v_mfma_f32_16x16x32_bf16 v[164:167], v[18:21], v[148:151], 0\n"
        "ds_write_b128 %14, v[192:195] offset:832\n"
        "v_mfma_f32_16x16x32_bf16 v[164:167], v[22:25], v[154:157], v[164:167]\n"
        "buffer_load_dwordx4 %17, %22, s39 offen lds\n"
        "v_mfma_f32_16x16x32_bf16 v[164:167], v[26:29], v[160:163], v[164:167]\n"
        "ds_write_b128 %14, v[196:199] offset:64\n"
        "v_mfma_f32_16x16x32_bf16 v[172:175], v[26:29], v[140:143], 0\n"
        "v_cndmask_b32_dpp v254, v144, v168, vcc row_ror:4 row_mask:0xf bank_mask:0xf\n"
        "v_mul_f32 v253, s28, v254\n"
        "v_mfma_f32_16x16x32_bf16 v[172:175], v[22:25], v[134:137], v[172:175]\n"
        "buffer_load_dwordx4 v251, %22, s39 offen offset:1024 lds\n"
        "v_fma_f32 v253, -v253, v254, v255\n"
        "v_mfma_f32_16x16x32_bf16 v[172:175], v[18:21], v[128:131], v[172:175]\n"
        "ds_write_b128 %14, v[200:203] offset:896\n"
        "v_mfma_f32_16x16x32_bf16 v[176:179], v[30:33], v[128:131], 0\n"
        "v_add_f32_e64 v253, s29, |v253|\n"
        "v_rsq_f32 v255, v253\n"
        "v_mfma_f32_16x16x32_bf16 v[176:179], v[34:37], v[134:137], v[176:179]\n"
        "buffer_load_dwordx4 v250, %22, s39 offen offset:2048 lds\n"
        "v_cndmask_b32_dpp v253, v145, v169, vcc row_ror:4 row_mask:0xf bank_mask:0xf\n"
        "v_mfma_f32_16x16x32_bf16 v[176:179], v[38:41], v[140:143], v[176:179]\n"
        "ds_write_b128 %14, v[204:207] offset:128\n"
        "v_mfma_f32_16x16x32_bf16 v[168:171], v[38:41], v[160:163], 0\n"
        "v_mfma_f32_16x16x32_bf16 v[168:171], v[34:37], v[154:157], v[168:171]\n"
        "v_mfma_f32_16x16x32_bf16 v[168:171], v[30:33], v[148:151], v[168:171]\n"
        "ds_write_b128 %14, v[208:211] offset:960\n"
        "v_mfma_f32_16x16x32_bf16 v[180:183], v[42:45], v[148:151], 0\n"
        "v_mfma_f32_16x16x32_bf16 v[180:183], v[46:49], v[154:157], v[180:183]\n"
        "v_mfma_f32_16x16x32_bf16 v[180:183], v[50:53], v[160:163], v[180:183]\n"
        "s_mov_b64 exec, 1\n"
        "ds_inc_u32 %16, %13 offset:0\n"
        "s_mov_b64 exec, -1\n"
        "v_mfma_f32_16x16x32_bf16 v[188:191], v[50:53], v[140:143], 0\n"
        "v_mfma_f32_16x16x32_bf16 v[188:191], v[46:49], v[134:137], v[188:191]\n"
        "v_mfma_f32_16x16x32_bf16 v[188:191], v[42:45], v[128:131], v[188:191]\n"
        "v_mfma_f32_16x16x32_bf16 v[192:195], v[54:57], v[128:131], 0\n"
        "v_mfma_f32_16x16x32_bf16 v[192:195], v[58:61], v[134:137], v[192:195]\n"
        "v_mfma_f32_16x16x32_bf16 v[192:195], v[62:65], v[140:143], v[192:195]\n"
        "v_mfma_f32_16x16x32_bf16 v[184:187], v[62:65], v[160:163], 0\n"
        "v_mfma_f32_16x16x32_bf16 v[184:187], v[58:61], v[154:157], v[184:187]\n"
        "v_mfma_f32_16x16x32_bf16 v[184:187], v[54:57], v[148:151], v[184:187]\n"
        "v_mfma_f32_16x16x32_bf16 v[196:199], v[66:69], v[148:151], 0\n"
        "v_mfma_f32_16x16x32_bf16 v[196:199], v[70:73], v[154:157], v[196:199]\n"
        "v_mfma_f32_16x16x32_bf16 v[196:199], v[74:77], v[160:163], v[196:199]\n"
        "v_mfma_f32_16x16x32_bf16 v[204:207], v[74:77], v[140:143], 0\n"
        "v_mfma_f32_16x16x32_bf16 v[204:207], v[70:73], v[134:137], v[204:207]\n"
        "v_mfma_f32_16x16x32_bf16 v[204:207], v[66:69], v[128:131], v[204:207]\n"
        "v_mfma_f32_16x16x32_bf16 v[208:211], v[78:81], v[128:131], 0\n"
        "v_mfma_f32_16x16x32_bf16 v[208:211], v[82:85], v[134:137], v[208:211]\n"
        "v_mfma_f32_16x16x32_bf16 v[208:211], v[86:89], v[140:143], v[208:211]\n"
        "s_and_b32 s69, s30, 1\n"
        "v_mfma_f32_16x16x32_bf16 v[200:203], v[86:89], v[160:163], 0\n"
        "s_lshl2_add_u32 s70, s69, 119168\n"
        "v_mfma_f32_16x16x32_bf16 v[200:203], v[82:85], v[154:157], v[200:203]\n"
        "v_mov_b32 %16, s70\n"
        "v_mfma_f32_16x16x32_bf16 v[200:203], v[78:81], v[148:151], v[200:203]\n"
        "s_mul_i32 s69, s69, 40064\n"
        "v_mfma_f32_16x16x32_bf16 v[144:147], v[148:151], v[90:93], 0\n"
        "ds_read_b128 v[230:233], %4 offset:128\n"
        "s_mul_i32 s70, s31, 1664\n"
        "v_mfma_f32_16x16x32_bf16 v[144:147], v[154:157], v[94:97], v[144:147]\n"
        "s_add_u32 s69, s69, s70\n"
        "v_mfma_f32_16x16x32_bf16 v[144:147], v[160:163], v[98:101], v[144:147]\n"
        "v_add_u32 %14, s69, %6\n"
        "v_mfma_f32_16x16x32_bf16 v[234:237], v[140:143], v[98:101], 0\n"
        "ds_read_b128 v[238:241], %4 offset:128\n"
        "v_add_u32 %15, s69, %5\n"
        "v_mfma_f32_16x16x32_bf16 v[234:237], v[134:137], v[94:97], v[234:237]\n"
        "s_lshr_b32 s68, s30, 1\n"
        "v_mfma_f32_16x16x32_bf16 v[234:237], v[128:131], v[90:93], v[234:237]\n"
        "s_waitcnt lgkmcnt(1)\n"
        "v_cvt_pk_bf16_f32 v212, v144, v145\n"
        "v_cvt_pk_bf16_f32 v213, v146, v147\n"
        "s_lshl2_add_u32 s68, s68, 4\n"
        "v_smfmac_f32_16x16x64_bf16 v[230:233], v[128:131], v[172:179], %13\n"
        "ds_read_b128 v[144:147], %0\n"
        "s_cmp_eq_u32 s31, 6\n"
        "v_smfmac_f32_16x16x64_bf16 v[230:233], v[134:137], v[188:195], %13\n"
        "ds_read_b128 v[172:175], %0 offset:1024\n"
        "v_cvt_pk_bf16_f32 v218, v234, v235\n"
        "v_smfmac_f32_16x16x64_bf16 v[230:233], v[140:143], v[204:211], %13\n"
        "ds_read_b128 v[176:179], %0 offset:2048\n"
        "v_cvt_pk_bf16_f32 v219, v236, v237\n"
        "s_waitcnt lgkmcnt(3)\n"
        "v_smfmac_f32_16x16x64_bf16 v[238:241], v[148:151], v[164:171], %13\n"
        "s_cbranch_scc1 L5_%=\n"
        "L6_%=:\n"
        "s_mov_b32 s30, s34\n"
        "s_mov_b32 s31, s35\n"
        "v_smfmac_f32_16x16x64_bf16 v[238:241], v[154:157], v[180:187], %13\n"
        "s_add_u32 s37, s37, 4\n"
        "v_smfmac_f32_16x16x64_bf16 v[238:241], v[160:163], v[196:203], %13\n"
        "s_waitcnt lgkmcnt(2)\n"
        "s_cmp_ge_u32 s37, 49\n"
        "v_mfma_f32_16x16x32_bf16 v[164:167], v[128:131], v[144:147], 0\n"
        "s_waitcnt lgkmcnt(1)\n"
        "s_cselect_b32 s69, 49, 0\n"
        "v_mfma_f32_16x16x32_bf16 v[164:167], v[134:137], v[172:175], v[164:167]\n"
        "s_waitcnt lgkmcnt(0)\n"
        "s_sub_u32 s37, s37, s69\n"
        "v_cndmask_b32_dpp v249, v238, v230, vcc row_ror:4 row_mask:0xf bank_mask:0xf\n"
        "v_cndmask_b32_dpp v248, v239, v231, vcc row_ror:4 row_mask:0xf bank_mask:0xf\n"
        "v_mfma_f32_16x16x32_bf16 v[164:167], v[140:143], v[176:179], v[164:167]\n"
        "s_sub_u32 s69, s37, 5\n"
        "v_cndmask_b32_dpp v247, v240, v232, vcc row_ror:4 row_mask:0xf bank_mask:0xf\n"
        "v_mul_f32_dpp v249, v255, v249 quad_perm:[0,0,0,0] row_mask:0xf bank_mask:0xf\n"
        "v_mfma_f32_16x16x32_bf16 v[168:171], v[160:163], v[176:179], 0\n"
        "s_cmp_lt_u32 s69, 4\n"
        "v_mul_f32_dpp v248, v255, v248 quad_perm:[1,1,1,1] row_mask:0xf bank_mask:0xf\n"
        "v_mul_f32_dpp v247, v255, v247 quad_perm:[2,2,2,2] row_mask:0xf bank_mask:0xf\n"
        "v_cndmask_b32_dpp v246, v241, v233, vcc row_ror:4 row_mask:0xf bank_mask:0xf\n"
        "v_mfma_f32_16x16x32_bf16 v[168:171], v[154:157], v[172:175], v[168:171]\n"
        "s_subb_u32 s36, s36, -4\n"
        "v_mul_f32_dpp v246, v255, v246 quad_perm:[3,3,3,3] row_mask:0xf bank_mask:0xf\n"
        "v_max3_f32 v252, v249, v248, v247\n"
        "v_max_f32 v252, v252, v246\n"
        "v_mfma_f32_16x16x32_bf16 v[168:171], v[148:151], v[144:147], v[168:171]\n"
        "s_cmp_ge_u32 s36, 6\n"
        "v_mul_f32 v252, v252, v255\n"
        "v_fma_f32 v249, v249, v255, -v252\n"
        "v_fma_f32 v248, v248, v255, -v252\n"
        "v_mfma_f32_16x16x32_bf16 v[172:175], v[148:151], v[102:105], 0\n"
        "ds_read_b128 v[144:147], %0 offset:3072\n"
        "v_fma_f32 v247, v247, v255, -v252\n"
        "v_mfma_f32_16x16x32_bf16 v[172:175], v[154:157], v[106:109], v[172:175]\n"
        "ds_read_b128 v[176:179], %0 offset:4096\n"
        "v_fma_f32 v246, v246, v255, -v252\n"
        "v_cvt_pk_bf16_f32 v222, v164, v165\n"
        "v_mfma_f32_16x16x32_bf16 v[172:175], v[160:163], v[110:113], v[172:175]\n"
        "ds_read_b128 v[180:183], %0 offset:5120\n"
        "s_cselect_b32 s69, 6, 0\n"
        "v_cvt_pk_bf16_f32 v223, v166, v167\n"
        "v_mfma_f32_16x16x32_bf16 v[164:167], v[140:143], v[110:113], 0\n"
        "s_addc_u32 s34, s34, 0\n"
        "v_exp_f32 v249, v249\n"
        "v_cvt_pk_bf16_f32 v216, v168, v169\n"
        "v_mfma_f32_16x16x32_bf16 v[164:167], v[134:137], v[106:109], v[164:167]\n"
        "s_sub_u32 s36, s36, s69\n"
        "v_exp_f32 v248, v248\n"
        "v_cvt_pk_bf16_f32 v217, v170, v171\n"
        "v_mfma_f32_16x16x32_bf16 v[164:167], v[128:131], v[102:105], v[164:167]\n"
        "s_waitcnt lgkmcnt(1)\n"
        "s_mul_i32 s38, s34, 1344\n"
        "v_exp_f32 v247, v247\n"
        "v_add_f32 v252, v249, v248\n"
        "v_mfma_f32_16x16x32_bf16 v[168:171], v[128:131], v[144:147], 0\n"
        "s_cmp_ge_u32 s34, s72\n"
        "v_exp_f32 v246, v246\n"
        "v_mul_f32_dpp v249, v255, v249 quad_perm:[0,0,0,0] row_mask:0xf bank_mask:0xf\n"
        "v_add_f32 v251, v247, v246\n"
        "v_mfma_f32_16x16x32_bf16 v[168:171], v[134:137], v[176:179], v[168:171]\n"
        "s_waitcnt lgkmcnt(0)\n"
        "s_cselect_b32 s70, s71, %25\n"
        "v_add_f32 v252, v252, v251\n"
        "v_mul_f32_dpp v248, v255, v248 quad_perm:[1,1,1,1] row_mask:0xf bank_mask:0xf\n"
        "v_mfma_f32_16x16x32_bf16 v[168:171], v[140:143], v[180:183], v[168:171]\n"
        "s_add_u32 s38, s38, s70\n"
        "v_rcp_f32 v252, v252\n"
        "v_mul_f32_dpp v247, v255, v247 quad_perm:[2,2,2,2] row_mask:0xf bank_mask:0xf\n"
        "v_mul_f32_dpp v246, v255, v246 quad_perm:[3,3,3,3] row_mask:0xf bank_mask:0xf\n"
        "v_mfma_f32_16x16x32_bf16 v[184:187], v[160:163], v[180:183], 0\n"
        "s_mul_i32 s69, s36, 98304\n"
        "v_mul_f32 v249, v249, v252\n"
        "v_mul_f32 v248, v248, v252\n"
        "v_mul_f32 v247, v247, v252\n"
        "v_mul_f32 v246, v246, v252\n"
        "v_mfma_f32_16x16x32_bf16 v[184:187], v[154:157], v[176:179], v[184:187]\n"
        "s_add_u32 s38, s38, s69\n"
        "v_cvt_pk_bf16_f32 v255, v249, v248\n"
        "v_cvt_pk_bf16_f32 v252, v247, v246\n"
        "v_and_b32 v126, %3, v255\n"
        "v_and_b32 v127, %3, v252\n"
        "v_mfma_f32_16x16x32_bf16 v[184:187], v[148:151], v[144:147], v[184:187]\n"
        "s_mov_b32 s35, s36\n"
        "v_and_b32_dpp v146, v255, %3 row_ror:12 row_mask:0xf bank_mask:0xf\n"
        "v_and_b32_dpp v147, v252, %3 row_ror:12 row_mask:0xf bank_mask:0xf\n"
        "v_mfma_f32_16x16x32_bf16 v[176:179], v[218:221], v[126:129], 0\n"
        "s_mul_i32 s69, s36, -12\n"
        "v_mov_b64 v[138:139], v[126:127]\n"
        "v_mov_b64 v[158:159], v[146:147]\n"
        "v_mfma_f32_16x16x32_bf16 v[180:183], v[212:215], v[146:149], 0\n"
        "s_add_u32 s69, s69, 60\n"
        "v_mov_b64 v[132:133], v[126:127]\n"
        "v_cvt_pk_bf16_f32 v226, v164, v165\n"
        "v_mfma_f32_16x16x32_bf16 v[192:195], v[220:223], v[136:139], 0\n"
        "s_lshl_b64 s[54:55], -1, s69\n"
        "v_cvt_pk_bf16_f32 v227, v166, v167\n"
        "v_mov_b64 v[152:153], v[146:147]\n"
        "v_mfma_f32_16x16x32_bf16 v[164:167], v[214:217], v[156:159], 0\n"
        "s_lshl_b64 s[56:57], s[54:55], 24\n"
        "v_cvt_pk_bf16_f32 v230, v172, v173\n"
        "v_cvt_pk_bf16_f32 v231, v174, v175\n"
        "v_mfma_f32_16x16x32_bf16 v[172:175], v[224:227], v[130:133], 0\n"
        "s_ashr_i64 s[60:61], s[54:55], 8\n"
        "v_cvt_pk_bf16_f32 v222, v168, v169\n"
        "v_cvt_pk_bf16_f32 v223, v170, v171\n"
        "v_mfma_f32_16x16x32_bf16 v[168:171], v[228:231], v[150:153], 0\n"
        "s_mov_b32 s58, s61\n"
        "v_cvt_pk_bf16_f32 v226, v184, v185\n"
        "v_cvt_pk_bf16_f32 v227, v186, v187\n"
        "v_cvt_pk_bf16_f32 v188, v176, v177\n"
        "v_mfma_f32_16x16x32_bf16 v[184:187], v[222:225], v[138:141], 0\n"
        "s_mov_b32 s59, s56\n"
        "v_cndmask_b32_e64 %17, %7, %10, s[56:57]\n"
        "v_cndmask_b32_e64 v251, %8, %11, s[58:59]\n"
        "v_mfma_f32_16x16x32_bf16 v[204:207], v[226:229], v[158:161], 0\n"
        "v_cndmask_b32_e64 v250, %9, %12, s[60:61]\n"
        "v_cvt_pk_bf16_f32 v189, v178, v179\n"
        "v_mfma_f32_16x16x32_bf16 v[176:179], v[148:151], v[114:117], 0\n"
        "ds_read_b128 v[208:211], %0 offset:6144\n"
        "v_cvt_pk_bf16_f32 v190, v192, v193\n"
        "v_mfma_f32_16x16x32_bf16 v[176:179], v[154:157], v[118:121], v[176:179]\n"
        "ds_read_b128 v[200:203], %0 offset:7168\n"
        "v_cvt_pk_bf16_f32 v191, v194, v195\n"
        "v_mfma_f32_16x16x32_bf16 v[176:179], v[160:163], v[122:125], v[176:179]\n"
        "ds_read_b128 v[230:233], %0 offset:8192\n"
        "s_cmp_eq_u32 s37, 4\n"
        "v_mfma_f32_16x16x32_bf16 v[196:199], v[140:143], v[122:125], 0\n"
        "s_cbranch_scc1 L7_%=\n"
        "L8_%=:\n"
        "v_cvt_pk_bf16_f32 v192, v180, v181\n"
        "v_cvt_pk_bf16_f32 v193, v182, v183\n"
        "v_mfma_f32_16x16x32_bf16 v[196:199], v[134:137], v[118:121], v[196:199]\n"
        "s_cmp_lt_u32 s34, %24\n"
        "v_cvt_pk_bf16_f32 v212, v176, v177\n"
        "v_cvt_pk_bf16_f32 v213, v178, v179\n"
        "v_mfma_f32_16x16x32_bf16 v[196:199], v[128:131], v[114:117], v[196:199]\n"
        "s_waitcnt lgkmcnt(2)\n"
        "s_cselect_b32 s38, s38, 0x80000000\n"
        "v_cvt_pk_bf16_f32 v194, v164, v165\n"
        "v_mfma_f32_16x16x32_bf16 v[176:179], v[128:131], v[208:211], 0\n"
        "s_waitcnt lgkmcnt(1)\n"
        "s_add_u32 s39, s38, 100270464\n"
        "v_cvt_pk_bf16_f32 v195, v166, v167\n"
        "v_mfma_f32_16x16x32_bf16 v[176:179], v[134:137], v[200:203], v[176:179]\n"
        "s_waitcnt lgkmcnt(0)\n"
        "v_cvt_pk_bf16_f32 v218, v196, v197\n"
        "v_cvt_pk_bf16_f32 v219, v198, v199\n"
        "v_mov_b64 v[144:145], v[126:127]\n"
        "v_mfma_f32_16x16x32_bf16 v[176:179], v[140:143], v[230:233], v[176:179]\n"
        "v_mov_b64 v[164:165], v[146:147]\n"
        "v_cvt_pk_bf16_f32 v196, v172, v173\n"
        "v_mfma_f32_16x16x32_bf16 v[136:139], v[160:163], v[230:233], 0\n"
        "v_cvt_pk_bf16_f32 v197, v174, v175\n"
        "v_cvt_pk_bf16_f32 v198, v184, v185\n"
        "v_mfma_f32_16x16x32_bf16 v[136:139], v[154:157], v[200:203], v[136:139]\n"
        "v_cvt_pk_bf16_f32 v199, v186, v187\n"
        "v_cvt_pk_bf16_f32 v200, v168, v169\n"
        "v_cvt_pk_bf16_f32 v222, v176, v177\n"
        "v_mfma_f32_16x16x32_bf16 v[136:139], v[148:151], v[208:211], v[136:139]\n"
        "v_cvt_pk_bf16_f32 v223, v178, v179\n"
        "v_cvt_pk_bf16_f32 v201, v170, v171\n"
        "v_cvt_pk_bf16_f32 v202, v204, v205\n"
        "v_mfma_f32_16x16x32_bf16 v[158:161], v[218:221], v[132:135], 0\n"
        "s_waitcnt vmcnt(0)\n"
        "ds_read_b128 v[128:131], %1\n"
        "ds_read_b128 v[148:151], %1 offset:768\n"
        "v_cvt_pk_bf16_f32 v203, v206, v207\n"
        "v_cvt_pk_bf16_f32 v216, v136, v137\n"
        "v_mfma_f32_16x16x32_bf16 v[172:175], v[212:215], v[152:155], 0\n"
        "ds_read_b128 v[134:137], %1 offset:64\n"
        "ds_read_b128 v[154:157], %1 offset:832\n"
        "v_cvt_pk_bf16_f32 v217, v138, v139\n"
        "v_cvt_pk_bf16_f32 v204, v158, v159\n"
        "v_cvt_pk_bf16_f32 v205, v160, v161\n"
        "v_mfma_f32_16x16x32_bf16 v[168:171], v[188:191], v[188:191], 0\n"
        "v_mul_f32_dpp v252, v253, v249 quad_perm:[0,0,0,0] row_mask:0xf bank_mask:0xf\n"
        "v_mfma_f32_16x16x32_bf16 v[180:183], v[192:195], v[192:195], 0\n"
        "v_cvt_pk_bf16_f32 v208, v172, v173\n"
        "v_cvt_pk_bf16_f32 v209, v174, v175\n"
        "v_fmac_f32_dpp v252, v253, v248 quad_perm:[1,1,1,1] row_mask:0xf bank_mask:0xf\n"
        "v_mfma_f32_16x16x32_bf16 v[172:175], v[220:223], v[142:145], 0\n"
        "ds_read_b128 v[140:143], %1 offset:128\n"
        "v_fmac_f32_dpp v252, v253, v247 quad_perm:[2,2,2,2] row_mask:0xf bank_mask:0xf\n"
        "v_mfma_f32_16x16x32_bf16 v[144:147], v[214:217], v[162:165], 0\n"
        "ds_read_b128 v[160:163], %1 offset:896\n"
        "v_fmac_f32_dpp v252, v253, v246 quad_perm:[3,3,3,3] row_mask:0xf bank_mask:0xf\n"
        "v_add_f32 v252, v252, v254\n"
        "v_mfma_f32_16x16x32_bf16 v[168:171], v[196:199], v[196:199], v[168:171]\n"
        "v_mfma_f32_16x16x32_bf16 v[180:183], v[200:203], v[200:203], v[180:183]\n"
        "v_cvt_pk_bf16_f32 v206, v172, v173\n"
        "v_cvt_pk_bf16_f32 v207, v174, v175\n"
        "s_nop 1\n"
        "v_mfma_f32_16x16x32_bf16 v[168:171], v[204:207], v[204:207], v[168:171]\n"
        "v_cvt_pk_bf16_f32 v210, v144, v145\n"
        "v_cvt_pk_bf16_f32 v211, v146, v147\n"
        "s_cmp_lt_u32 s30, %24\n"
        "s_nop 0\n"
        "v_mfma_f32_16x16x32_bf16 v[180:183], v[208:211], v[208:211], v[180:183]\n"
        "s_cbranch_scc1 PTOP_%=\n"
        "s_waitcnt lgkmcnt(0)\n"
        "s_nop 15\n"
        "L9_%=:\n"
        "ds_read_b32 v255, %16 offset:8\n"
        "s_waitcnt lgkmcnt(0)\n"
        "v_readfirstlane_b32 s40, v255\n"
        "s_nop 3\n"
        "s_cmp_ge_i32 s40, s68\n"
        "s_cbranch_scc1 L10_%=\n"
        "s_sleep 1\n"
        "s_sub_u32 s41, s41, 1\n"
        "s_cbranch_scc0 L9_%=\n"
        "s_endpgm\n"
        "L10_%=:\n"
        "v_cndmask_b32_e64 v255, v168, v169, s[48:49]\n"
        "v_cndmask_b32_e64 v254, v180, v181, s[48:49]\n"
        "v_cndmask_b32_e64 v255, v255, v170, s[50:51]\n"
        "v_cndmask_b32_e64 v254, v254, v182, s[50:51]\n"
        "v_cndmask_b32_e64 v255, v255, v171, s[52:53]\n"
        "v_cndmask_b32_e64 v254, v254, v183, s[52:53]\n"
        "v_mul_f32 v253, s28, v252\n"
        "ds_write_b128 %14, v[188:191]\n"
        "v_cndmask_b32_dpp v255, v254, v255, vcc row_ror:4 row_mask:0xf bank_mask:0xf\n"
        "v_fma_f32 v253, -v253, v252, v255\n"
        "v_add_f32_e64 v253, s29, |v253|\n"
        "v_rsq_f32 v255, v253\n"
        "ds_write_b128 %14, v[192:195] offset:832\n"
        "ds_write_b128 %14, v[196:199] offset:64\n"
        "ds_write_b128 %14, v[200:203] offset:896\n"
        "ds_write_b128 %14, v[204:207] offset:128\n"
        "ds_write_b128 %14, v[208:211] offset:960\n"
        "s_mov_b64 exec, s[46:47]\n"
        "ds_write_b32 %15, v255\n"
        "s_mov_b64 exec, -1\n"
        "s_mov_b64 exec, 1\n"
        "ds_inc_u32 %16, %13 offset:0\n"
        "s_mov_b64 exec, -1\n"
        "s_endpgm\n"
        "L3_%=:\n"
        "s_nop 7\n"
        "s_nop 7\n"
        "L1_%=:\n"
        "ds_read_b32 v253, %16 offset:8\n"
        "s_waitcnt lgkmcnt(0)\n"
        "v_readfirstlane_b32 s40, v253\n"
        "s_nop 3\n"
        "s_cmp_ge_i32 s40, s68\n"
        "s_cbranch_scc1 L2_%=\n"
        "s_sleep 1\n"
        "s_sub_u32 s41, s41, 1\n"
        "s_cbranch_scc0 L1_%=\n"
        "s_endpgm\n"
        "L2_%=:\n"
        "s_nop 1\n"
        "s_branch L4_%=\n"
        "L5_%=:\n"
        "s_nop 7\n"
        "s_nop 7\n"
        "ds_read_u16 %14, %0 offset:28684\n"
        "ds_read_u16 %15, %0 offset:28686\n"
        "s_cmp_ge_u32 s30, 10\n"
        "s_cselect_b32 s69, 10, 0\n"
        "s_cmp_ge_u32 s30, 20\n"
        "s_cselect_b32 s69, 20, s69\n"
        "s_sub_u32 s69, s30, s69\n"
        "s_mul_i32 s69, s69, 832\n"
        "s_waitcnt lgkmcnt(0)\n"
        "v_add_u32 %14, s69, %14\n"
        "v_subrev_u32 v252, 8320, %14\n"
        "v_min_u32 %14, %14, v252\n"
        "v_add_u32 %14, 110848, %14\n"
        "v_add_u32 %15, s69, %15\n"
        "v_subrev_u32 v252, 8320, %15\n"
        "v_min_u32 %15, %15, v252\n"
        "v_add_u32 %15, 110848, %15\n"
        "s_nop 1\n"
        "s_branch L6_%=\n"
        "L7_%=:\n"
        "s_nop 7\n"
        "s_nop 7\n"
        "ds_read_b32 %17, %0 offset:28672\n"
        "ds_read_b32 v251, %0 offset:28676\n"
        "ds_read_b32 v250, %0 offset:28680\n"
        "s_mov_b32 s35, 6\n"
        "s_mov_b32 s38, %25\n"
        "s_mov_b32 s69, 1344\n"
        "v_mov_b32 v255, 588672\n"
        "s_waitcnt lgkmcnt(0)\n"
        "v_lshrrev_b32 v252, 28, %17\n"
        "v_add_u32 v252, s34, v252\n"
        "v_cmp_le_u32 vcc, s72, v252\n"
        "v_and_b32 %17, 0x0fffffff, %17\n"
        "v_mad_u32_u24 %17, v252, s69, %17\n"
        "v_cndmask_b32 v252, 0, v255, vcc\n"
        "v_add_u32 %17, %17, v252\n"
        "v_lshrrev_b32 v252, 28, v251\n"
        "v_add_u32 v252, s34, v252\n"
        "v_cmp_le_u32 vcc, s72, v252\n"
        "v_and_b32 v251, 0x0fffffff, v251\n"
        "v_mad_u32_u24 v251, v252, s69, v251\n"
        "v_cndmask_b32 v252, 0, v255, vcc\n"
        "v_add_u32 v251, v251, v252\n"
        "v_lshrrev_b32 v252, 28, v250\n"
        "v_add_u32 v252, s34, v252\n"
        "v_cmp_le_u32 vcc, s72, v252\n"
        "v_and_b32 v250, 0x0fffffff, v250\n"
        "v_mad_u32_u24 v250, v252, s69, v250\n"
        "v_cndmask_b32 v252, 0, v255, vcc\n"
        "v_add_u32 v250, v250, v252\n"
        "s_mov_b64 vcc, s[44:45]\n"
        "s_nop 1\n"
        "s_branch L8_%=\n"
        "CONS_%=:\n"
        "s_mov_b32 s41, 0x400000\n"
        "s_mov_b32 s48, 0xffff0000\n"
        "s_mov_b32 s49, 0xffffffff\n"
        "s_mov_b32 s50, 0xffff0000\n"
        "s_mov_b32 s51, 0x0\n"
        "s_mov_b32 s52, 0x0\n"
        "s_mov_b32 s53, 0xffff\n"
        "s_mov_b32 s54, 0x0\n"
        "s_mov_b32 s55, 0xffff0000\n"
        "s_mov_b32 s56, 0x0\n"
        "s_mov_b32 s57, 0xfffe0000\n"
        "s_mov_b32 s62, 0x11111111\n"
        "s_mov_b32 s63, 0x11111111\n"
        "s_mov_b64 s[44:45], %23\n"
        "s_and_b32 s45, s45, 0xffff\n"
        "s_mov_b32 s47, 0x20000\n"
        "s_mov_b32 s29, 0\n"
        "s_mov_b32 s30, 0\n"
        "s_mov_b32 s34, 0\n"
        "s_mov_b32 s73, %19\n"
        "s_mov_b32 s28, 0xffff0000\n"
        "s_mov_b32 s31, 0\n"
        "s_add_u32 s72, %25, %18\n"
        "s_add_u32 s70, s72, 588672\n"
        "s_sub_u32 s71, 73, %26\n"
        "s_mov_b32 s35, s72\n"
        "s_mov_b32 s36, 0x20000000\n"
        "s_mov_b32 s69, 40064\n"
        "v_add_u32 v94, -2, %3\n"
        "v_mov_b32 v95, 0x3f80\n"
        "s_mov_b32 exec_lo, 0\n"
        "s_mov_b32 exec_hi, 0x20000\n"
        "ds_write_b16 v94, v95\n"
        "s_mov_b64 exec, -1\n"
        "v_add_u32 v94, 70784, %0\n"
        "ds_read_b128 v[18:21], v94 offset:0\n"
        "ds_read_b128 v[22:25], v94 offset:1024\n"
        "ds_read_b128 v[26:29], v94 offset:2048\n"
        "ds_read_b128 v[30:33], v94 offset:3072\n"
        "ds_read_b128 v[34:37], v94 offset:4096\n"
        "ds_read_b128 v[38:41], v94 offset:5120\n"
        "ds_read_b128 v[42:45], v94 offset:6144\n"
        "ds_read_b128 v[46:49], v94 offset:7168\n"
        "ds_read_b128 v[50:53], v94 offset:8192\n"
        "ds_read_b128 v[54:57], v94 offset:9216\n"
        "ds_read_b128 v[58:61], v94 offset:10240\n"
        "ds_read_b128 v[62:65], v94 offset:11264\n"
        "ds_read_b128 v[66:69], v94 offset:12288\n"
        "ds_read_b128 v[70:73], v94 offset:13312\n"
        "ds_read_b128 v[74:77], v94 offset:14336\n"
        "ds_read_b128 v[78:81], v94 offset:15360\n"
        "ds_read_b128 v[82:85], v94 offset:16384\n"
        "ds_read_b128 v[86:89], v94 offset:17408\n"
        "ds_read_b128 v[90:93], v94 offset:18432\n"
        "s_waitcnt lgkmcnt(0)\n"
        "s_mov_b64 exec, 1\n"
        "ds_inc_u32 %17, %1 offset:12\n"
        "s_mov_b64 exec, -1\n"
        "L28_%=:\n"
        "ds_read_b32 v94, %17 offset:0\n"
        "s_waitcnt lgkmcnt(0)\n"
        "v_readfirstlane_b32 s40, v94\n"
        "s_nop 3\n"
        "s_cmp_ge_i32 s40, 11\n"
        "s_cbranch_scc1 L29_%=\n"
        "s_sleep 1\n"
        "s_sub_u32 s41, s41, 1\n"
        "s_cbranch_scc0 L28_%=\n"
        "s_endpgm\n"
        "L29_%=:\n"
        "ds_read_b128 v[176:179], %5 offset:0\n"
        "ds_read_b128 v[154:157], %5 offset:3328\n"
        "ds_read_b128 v[236:239], %5 offset:64\n"
        "ds_read_b128 v[180:183], %5 offset:3392\n"
        "ds_read_b128 v[244:247], %5 offset:128\n"
        "ds_read_b128 v[102:105], %5 offset:3456\n"
        "s_waitcnt lgkmcnt(0)\n"
        "CFULL_%=:\n"
        "s_waitcnt lgkmcnt(0)\n"
        "s_nop 7\n"
        "v_mfma_f32_16x16x32_bf16 v[94:97], v[18:21], v[176:179], 0\n"
        "s_sub_u32 s60, s73, 111040\n"
        "v_cvt_pk_bf16_f32 v114, v98, v106\n"
        "v_cvt_pk_bf16_f32 v115, v120, v132\n"
        "v_cvt_pk_bf16_f32 v116, v136, v140\n"
        "v_mfma_f32_16x16x32_bf16 v[94:97], v[22:25], v[236:239], v[94:97]\n"
        "s_add_u32 s37, s36, 196224\n"
        "v_cvt_pk_bf16_f32 v118, v99, v107\n"
        "v_cvt_pk_bf16_f32 v119, v121, v133\n"
        "v_cvt_pk_bf16_f32 v120, v137, v141\n"
        "v_mfma_f32_16x16x32_bf16 v[94:97], v[26:29], v[244:247], v[94:97]\n"
        "v_cvt_pk_bf16_f32 v132, v100, v108\n"
        "v_cvt_pk_bf16_f32 v133, v122, v134\n"
        "v_cvt_pk_bf16_f32 v134, v138, v142\n"
        "v_mfma_f32_16x16x32_bf16 v[110:113], v[26:29], v[102:105], 0\n"
        "v_cvt_pk_bf16_f32 v140, v101, v109\n"
        "v_cvt_pk_bf16_f32 v141, v123, v135\n"
        "v_cvt_pk_bf16_f32 v142, v139, v143\n"
        "v_mfma_f32_16x16x32_bf16 v[110:113], v[22:25], v[180:183], v[110:113]\n"
        "buffer_store_dwordx3 v[114:116], %11, %22, s37 offen offset:384 sc1 nt\n"
        "v_mfma_f32_16x16x32_bf16 v[110:113], v[18:21], v[154:157], v[110:113]\n"
        "v_cvt_pk_bf16_f32 v98, v94, v95\n"
        "v_cvt_pk_bf16_f32 v99, v96, v97\n"
        "v_cvt_pk_bf16_f32 v114, v124, v128\n"
        "v_mfma_f32_16x16x32_bf16 v[94:97], v[30:33], v[154:157], 0\n"
        "s_add_u32 s37, s36, 196224\n"
        "v_cvt_pk_bf16_f32 v115, v168, v172\n"
        "v_cvt_pk_bf16_f32 v116, v184, v188\n"
        "v_cvt_pk_bf16_f32 v144, v125, v129\n"
        "v_mfma_f32_16x16x32_bf16 v[94:97], v[34:37], v[180:183], v[94:97]\n"
        "v_cvt_pk_bf16_f32 v136, v110, v111\n"
        "v_cvt_pk_bf16_f32 v137, v112, v113\n"
        "v_cvt_pk_bf16_f32 v145, v169, v173\n"
        "v_mfma_f32_16x16x32_bf16 v[94:97], v[38:41], v[102:105], v[94:97]\n"
        "v_cvt_pk_bf16_f32 v146, v185, v189\n"
        "v_cvt_pk_bf16_f32 v148, v126, v130\n"
        "v_cvt_pk_bf16_f32 v149, v170, v174\n"
        "v_mfma_f32_16x16x32_bf16 v[106:109], v[38:41], v[244:247], 0\n"
        "buffer_store_dwordx3 v[118:120], %12, %22, s37 offen offset:1152 sc1 nt\n"
        "v_mfma_f32_16x16x32_bf16 v[106:109], v[34:37], v[236:239], v[106:109]\n"
        "ds_read_u16 v255, %0 offset:29696\n"
        "v_cvt_pk_bf16_f32 v150, v186, v190\n"
        "v_cvt_pk_bf16_f32 v138, v94, v95\n"
        "v_mfma_f32_16x16x32_bf16 v[106:109], v[30:33], v[176:179], v[106:109]\n"
        "s_add_u32 s37, s36, 294336\n"
        "v_cvt_pk_bf16_f32 v139, v96, v97\n"
        "v_cvt_pk_bf16_f32 v200, v127, v131\n"
        "v_cvt_pk_bf16_f32 v201, v171, v175\n"
        "v_mfma_f32_16x16x32_bf16 v[94:97], v[42:45], v[176:179], 0\n"
        "s_mov_b64 exec, s[62:63]\n"
        "v_add_u32 %15, s29, %9\n"
        "v_add_u32 %6, s34, %16\n"
        "s_mov_b64 exec, -1\n"
        "v_mfma_f32_16x16x32_bf16 v[94:97], v[46:49], v[236:239], v[94:97]\n"
        "v_cvt_pk_bf16_f32 v100, v106, v107\n"
        "v_cvt_pk_bf16_f32 v101, v108, v109\n"
        "v_cvt_pk_bf16_f32 v202, v187, v191\n"
        "v_mfma_f32_16x16x32_bf16 v[94:97], v[50:53], v[244:247], v[94:97]\n"
        "buffer_store_dwordx3 v[132:134], %11, %22, s37 offen offset:576 sc1 nt\n"
        "v_mfma_f32_16x16x32_bf16 v[106:109], v[50:53], v[102:105], 0\n"
        "s_waitcnt lgkmcnt(0)\n"
        "v_add_u32 v255, s60, v255\n"
        "v_subrev_u32 v254, 8320, v255\n"
        "v_min_u32 v255, v255, v254\n"
        "v_mfma_f32_16x16x32_bf16 v[106:109], v[46:49], v[180:183], v[106:109]\n"
        "s_add_u32 s37, s36, 392448\n"
        "v_writelane_b32 %8, s73, 48\n"
        "v_cvt_pk_bf16_f32 v122, v94, v95\n"
        "v_cvt_pk_bf16_f32 v123, v96, v97\n"
        "v_mfma_f32_16x16x32_bf16 v[106:109], v[42:45], v[154:157], v[106:109]\n"
        "v_add_u32 v255, 110848, v255\n"
        "v_mfma_f32_16x16x32_bf16 v[94:97], v[54:57], v[154:157], 0\n"
        "v_mfma_f32_16x16x32_bf16 v[94:97], v[58:61], v[180:183], v[94:97]\n"
        "buffer_store_dwordx3 v[140:142], %11, %22, s37 offen sc1 nt\n"
        "v_mfma_f32_16x16x32_bf16 v[94:97], v[62:65], v[102:105], v[94:97]\n"
        "ds_read_b32 v253, %8\n"
        "v_mfma_f32_16x16x32_bf16 v[110:113], v[62:65], v[244:247], 0\n"
        "s_add_u32 s37, s36, 392448\n"
        "v_cvt_pk_bf16_f32 v140, v106, v107\n"
        "v_cvt_pk_bf16_f32 v141, v108, v109\n"
        "v_mfma_f32_16x16x32_bf16 v[110:113], v[58:61], v[236:239], v[110:113]\n"
        "v_mfma_f32_16x16x32_bf16 v[110:113], v[54:57], v[176:179], v[110:113]\n"
        "v_mfma_f32_16x16x32_bf16 v[106:109], v[66:69], v[176:179], 0\n"
        "buffer_store_dwordx3 v[114:116], %14, %22, s37 offen offset:768 sc1 nt\n"
        "v_mfma_f32_16x16x32_bf16 v[106:109], v[70:73], v[236:239], v[106:109]\n"
        "v_cvt_pk_bf16_f32 v142, v94, v95\n"
        "v_cvt_pk_bf16_f32 v143, v96, v97\n"
        "v_mfma_f32_16x16x32_bf16 v[106:109], v[74:77], v[244:247], v[106:109]\n"
        "s_add_u32 s37, s36, 490560\n"
        "s_waitcnt lgkmcnt(0)\n"
        "v_and_b32 v174, s28, v253\n"
        "v_cvt_pk_bf16_f32 v124, v110, v111\n"
        "v_mfma_f32_16x16x32_bf16 v[94:97], v[74:77], v[102:105], 0\n"
        "v_cvt_pk_bf16_f32 v125, v112, v113\n"
        "v_sub_f32 v254, v253, v174\n"
        "v_and_b32 v152, s28, v253\n"
        "v_mfma_f32_16x16x32_bf16 v[94:97], v[70:73], v[180:183], v[94:97]\n"
        "v_cvt_pk_bf16_f32 v126, v106, v107\n"
        "v_cvt_pk_bf16_f32 v127, v108, v109\n"
        "v_cvt_pk_bf16_f32 v175, v254, v174\n"
        "v_mfma_f32_16x16x32_bf16 v[94:97], v[66:69], v[154:157], v[94:97]\n"
        "buffer_store_dwordx3 v[144:146], %11, %22, s37 offen offset:192 sc1 nt\n"
        "v_mfma_f32_16x16x32_bf16 v[128:131], v[78:81], v[154:157], 0\n"
        "v_cvt_pk_bf16_f32 v153, v152, v254\n"
        "v_readlane_b32 s58, v253, 48\n"
        "v_mfma_f32_16x16x32_bf16 v[128:131], v[82:85], v[180:183], v[128:131]\n"
        "s_add_u32 s37, s36, 490560\n"
        "v_mfma_f32_16x16x32_bf16 v[128:131], v[86:89], v[102:105], v[128:131]\n"
        "v_mfma_f32_16x16x32_bf16 v[132:135], v[86:89], v[244:247], 0\n"
        "v_cvt_pk_bf16_f32 v144, v94, v95\n"
        "v_cvt_pk_bf16_f32 v145, v96, v97\n"
        "v_mfma_f32_16x16x32_bf16 v[132:135], v[82:85], v[236:239], v[132:135]\n"
        "buffer_store_dwordx3 v[148:150], %13, %22, s37 offen offset:960 sc1 nt\n"
        "v_mfma_f32_16x16x4_4b_bf16 v[106:121], v[174:175], v[152:153], 0 blgp:4\n"
        "v_mfma_f32_16x16x32_bf16 v[132:135], v[78:81], v[176:179], v[132:135]\n"
        "v_mfma_f32_16x16x32_bf16 v[94:97], v[176:179], v[98:101], 0\n"
        "ds_read_b128 v[162:165], %5 offset:6656\n"
        "v_cvt_pk_bf16_f32 v146, v128, v129\n"
        "v_cvt_pk_bf16_f32 v147, v130, v131\n"
        "v_mfma_f32_16x16x32_bf16 v[94:97], v[236:239], v[122:125], v[94:97]\n"
        "ds_read_b128 v[166:169], %5 offset:6720\n"
        "s_add_u32 s37, s36, 588672\n"
        "ds_read_b128 v[170:173], %5 offset:6784\n"
        "v_cvt_pk_bf16_f32 v128, v132, v133\n"
        "v_cvt_pk_bf16_f32 v129, v134, v135\n"
        "s_nop 1\n"
        "v_mfma_f32_16x16x32_bf16 v[94:97], v[244:247], v[126:129], v[94:97]\n"
        "v_mfma_f32_16x16x32_bf16 v[158:161], v[102:105], v[126:129], 0\n"
        "ds_read_b128 v[184:187], v255 offset:128\n"
        "v_mfma_f32_16x16x32_bf16 v[158:161], v[180:183], v[122:125], v[158:161]\n"
        "ds_read_b128 v[188:191], v255 offset:64\n"
        "v_mfma_f32_16x16x32_bf16 v[158:161], v[154:157], v[98:101], v[158:161]\n"
        "ds_read_b128 v[192:195], v255\n"
        "s_waitcnt lgkmcnt(3)\n"
        "v_mfma_f32_16x16x32_bf16 v[196:199], v[162:165], v[98:101], 0\n"
        "buffer_store_dwordx3 v[200:202], %11, %22, s37 offen offset:384 sc1 nt\n"
        "v_mul_f32 v94, v94, v106\n"
        "v_mfma_f32_16x16x32_bf16 v[196:199], v[166:169], v[122:125], v[196:199]\n"
        "v_mul_f32 v95, v95, v107\n"
        "v_exp_f32 v94, v94\n"
        "v_mfma_f32_16x16x32_bf16 v[196:199], v[170:173], v[126:129], v[196:199]\n"
        "s_waitcnt lgkmcnt(1)\n"
        "v_exp_f32 v95, v95\n"
        "v_mul_f32 v96, v96, v108\n"
        "v_mfma_f32_16x16x32_bf16 v[200:203], v[184:187], v[126:129], 0\n"
        "v_exp_f32 v96, v96\n"
        "v_mul_f32 v97, v97, v109\n"
        "v_mfma_f32_16x16x32_bf16 v[200:203], v[188:191], v[122:125], v[200:203]\n"
        "s_waitcnt lgkmcnt(0)\n"
        "v_add_f32 v255, v94, v95\n"
        "v_mfma_f32_16x16x4_4b_bf16 v[120:135], v[174:175], v[152:153], 0 blgp:5\n"
        "v_exp_f32 v97, v97\n"
        "v_mul_f32 v158, v158, v110\n"
        "v_add_f32 v255, v255, v96\n"
        "v_exp_f32 v158, v158\n"
        "v_mul_f32 v159, v159, v111\n"
        "v_add_f32 v255, v255, v97\n"
        "v_exp_f32 v159, v159\n"
        "v_mfma_f32_16x16x32_bf16 v[200:203], v[192:195], v[98:101], v[200:203]\n"
        "v_mul_f32 v160, v160, v112\n"
        "v_add_f32 v255, v255, v158\n"
        "v_mul_f32 v161, v161, v113\n"
        "v_mfma_f32_16x16x32_bf16 v[98:101], v[176:179], v[136:139], 0\n"
        "v_exp_f32 v160, v160\n"
        "v_add_f32 v255, v255, v159\n"
        "v_mfma_f32_16x16x32_bf16 v[98:101], v[236:239], v[140:143], v[98:101]\n"
        "v_exp_f32 v161, v161\n"
        "v_mul_f32 v196, v196, v114\n"
        "v_mfma_f32_16x16x32_bf16 v[98:101], v[244:247], v[144:147], v[98:101]\n"
        "s_setprio 2\n"
        "v_add_f32 v255, v255, v160\n"
        "v_exp_f32 v196, v196\n"
        "v_mfma_f32_16x16x32_bf16 v[202:205], v[102:105], v[144:147], 0\n"
        "v_mul_f32 v197, v197, v115\n"
        "v_add_f32 v255, v255, v161\n"
        "v_mul_f32 v198, v198, v116\n"
        "v_mfma_f32_16x16x32_bf16 v[202:205], v[180:183], v[140:143], v[202:205]\n"
        "v_mul_f32 v98, v98, v120\n"
        "v_mul_f32 v99, v99, v121\n"
        "v_mul_f32 v100, v100, v122\n"
        "v_mfma_f32_16x16x32_bf16 v[202:205], v[154:157], v[136:139], v[202:205]\n"
        "v_exp_f32 v98, v98\n"
        "v_mul_f32 v101, v101, v123\n"
        "v_mfma_f32_16x16x32_bf16 v[206:209], v[162:165], v[136:139], 0\n"
        "v_exp_f32 v99, v99\n"
        "v_add_f32 v255, v255, v196\n"
        "v_mfma_f32_16x16x32_bf16 v[206:209], v[166:169], v[140:143], v[206:209]\n"
        "v_exp_f32 v100, v100\n"
        "v_add_f32 v254, v98, v99\n"
        "v_mfma_f32_16x16x32_bf16 v[206:209], v[170:173], v[144:147], v[206:209]\n"
        "ds_read_u16 v252, %0 offset:29700\n"
        "v_exp_f32 v101, v101\n"
        "v_mfma_f32_16x16x32_bf16 v[210:213], v[184:187], v[144:147], 0\n"
        "v_mul_f32 v202, v202, v124\n"
        "v_add_f32 v254, v254, v100\n"
        "v_mul_f32 v203, v203, v125\n"
        "v_mfma_f32_16x16x32_bf16 v[210:213], v[188:191], v[140:143], v[210:213]\n"
        "v_exp_f32 v202, v202\n"
        "v_add_f32 v254, v254, v101\n"
        "v_mfma_f32_16x16x32_bf16 v[210:213], v[192:195], v[136:139], v[210:213]\n"
        "v_exp_f32 v203, v203\n"
        "v_mul_f32 v204, v204, v126\n"
        "v_mfma_f32_16x16x32_bf16 v[106:109], v[18:21], v[162:165], 0\n"
        "s_waitcnt lgkmcnt(0)\n"
        "v_add_u32 v252, %9, v252\n"
        "v_add_f32 v254, v254, v202\n"
        "v_mul_f32 v205, v205, v127\n"
        "v_mfma_f32_16x16x32_bf16 v[106:109], v[22:25], v[166:169], v[106:109]\n"
        "v_exp_f32 v204, v204\n"
        "v_add_f32 v254, v254, v203\n"
        "v_mfma_f32_16x16x32_bf16 v[106:109], v[26:29], v[170:173], v[106:109]\n"
        "v_exp_f32 v205, v205\n"
        "v_mul_f32 v206, v206, v128\n"
        "v_mfma_f32_16x16x32_bf16 v[110:113], v[26:29], v[184:187], 0\n"
        "v_add_f32 v254, v254, v204\n"
        "v_exp_f32 v206, v206\n"
        "v_mfma_f32_16x16x32_bf16 v[110:113], v[22:25], v[188:191], v[110:113]\n"
        "v_mul_f32 v207, v207, v129\n"
        "v_exp_f32 v197, v197\n"
        "v_mfma_f32_16x16x32_bf16 v[110:113], v[18:21], v[192:195], v[110:113]\n"
        "v_cvt_pk_bf16_f32 v212, v106, v107\n"
        "v_cvt_pk_bf16_f32 v213, v108, v109\n"
        "v_add_f32 v254, v254, v205\n"
        "v_mfma_f32_16x16x32_bf16 v[106:109], v[30:33], v[192:195], 0\n"
        "v_exp_f32 v207, v207\n"
        "v_mul_f32 v208, v208, v130\n"
        "v_mfma_f32_16x16x32_bf16 v[106:109], v[34:37], v[188:191], v[106:109]\n"
        "v_exp_f32 v198, v198\n"
        "v_cvt_pk_bf16_f32 v114, v110, v111\n"
        "v_mfma_f32_16x16x32_bf16 v[106:109], v[38:41], v[184:187], v[106:109]\n"
        "v_cvt_pk_bf16_f32 v115, v112, v113\n"
        "v_mul_f32 v199, v199, v117\n"
        "v_add_f32 v254, v254, v206\n"
        "v_mfma_f32_16x16x32_bf16 v[110:113], v[38:41], v[170:173], 0\n"
        "v_exp_f32 v208, v208\n"
        "v_mul_f32 v209, v209, v131\n"
        "v_mfma_f32_16x16x32_bf16 v[110:113], v[34:37], v[166:169], v[110:113]\n"
        "v_add_f32 v255, v255, v197\n"
        "v_cvt_pk_bf16_f32 v116, v106, v107\n"
        "v_cvt_pk_bf16_f32 v117, v108, v109\n"
        "v_mfma_f32_16x16x32_bf16 v[110:113], v[30:33], v[162:165], v[110:113]\n"
        "v_exp_f32 v199, v199\n"
        "v_mul_f32 v200, v200, v118\n"
        "v_mfma_f32_16x16x32_bf16 v[106:109], v[42:45], v[162:165], 0\n"
        "v_add_f32 v254, v254, v207\n"
        "v_exp_f32 v209, v209\n"
        "v_mfma_f32_16x16x32_bf16 v[106:109], v[46:49], v[166:169], v[106:109]\n"
        "v_mul_f32 v210, v210, v132\n"
        "v_add_f32 v255, v255, v198\n"
        "v_cvt_pk_bf16_f32 v214, v110, v111\n"
        "v_mfma_f32_16x16x32_bf16 v[106:109], v[50:53], v[170:173], v[106:109]\n"
        "v_cvt_pk_bf16_f32 v215, v112, v113\n"
        "v_exp_f32 v200, v200\n"
        "v_mfma_f32_16x16x32_bf16 v[110:113], v[50:53], v[184:187], 0\n"
        "v_add_f32 v254, v254, v208\n"
        "v_exp_f32 v210, v210\n"
        "v_mfma_f32_16x16x32_bf16 v[110:113], v[46:49], v[188:191], v[110:113]\n"
        "v_add_f32 v255, v255, v199\n"
        "v_add_f32 v254, v254, v209\n"
        "v_cvt_pk_bf16_f32 v216, v106, v107\n"
        "v_mfma_f32_16x16x32_bf16 v[110:113], v[42:45], v[192:195], v[110:113]\n"
        "v_cvt_pk_bf16_f32 v217, v108, v109\n"
        "v_add_f32_dpp v255, v200, v255 quad_perm:[0,1,2,3] row_mask:0x1 bank_mask:0xf\n"
        "v_add_f32_dpp v254, v210, v254 quad_perm:[0,1,2,3] row_mask:0x1 bank_mask:0xf\n"
        "v_mfma_f32_16x16x32_bf16 v[106:109], v[54:57], v[192:195], 0\n"
        "ds_write_b128 v252, v[114:117]\n"
        "v_mfma_f32_16x16x32_bf16 v[106:109], v[58:61], v[188:191], v[106:109]\n"
        "v_permlane32_swap_b32 v255, v254\n"
        "v_add_f32 v255, v255, v254\n"
        "v_mfma_f32_16x16x32_bf16 v[106:109], v[62:65], v[184:187], v[106:109]\n"
        "v_cvt_pk_bf16_f32 v122, v110, v111\n"
        "v_cvt_pk_bf16_f32 v123, v112, v113\n"
        "v_mov_b32 v254, v255\n"
        "v_mfma_f32_16x16x32_bf16 v[110:113], v[62:65], v[170:173], 0\n"
        "v_mfma_f32_16x16x32_bf16 v[110:113], v[58:61], v[166:169], v[110:113]\n"
        "v_permlane16_swap_b32 v255, v254\n"
        "v_add_f32 v255, v255, v254\n"
        "v_mfma_f32_16x16x32_bf16 v[110:113], v[54:57], v[162:165], v[110:113]\n"
        "v_cvt_pk_bf16_f32 v124, v106, v107\n"
        "v_cvt_pk_bf16_f32 v125, v108, v109\n"
        "v_mfma_f32_16x16x32_bf16 v[138:141], v[66:69], v[162:165], 0\n"
        "v_rcp_f32 v254, v255\n"
        "v_mfma_f32_16x16x32_bf16 v[138:141], v[70:73], v[166:169], v[138:141]\n"
        "v_and_b32 v146, s28, v254\n"
        "v_sub_f32 v251, v254, v146\n"
        "v_cvt_pk_bf16_f32 v147, v146, v251\n"
        "v_mfma_f32_16x16x32_bf16 v[138:141], v[74:77], v[170:173], v[138:141]\n"
        "v_cvt_pk_bf16_f32 v218, v110, v111\n"
        "v_cvt_pk_bf16_f32 v219, v112, v113\n"
        "v_mfma_f32_16x16x4_4b_bf16 v[106:121], v[174:175], v[146:147], 0 blgp:4\n"
        "ds_write_b128 v252, v[122:125] offset:64\n"
        "v_min3_f32 v211, v255, v254, v253\n"
        "v_mfma_f32_16x16x4_4b_bf16 v[122:137], v[174:175], v[146:147], 0 blgp:6\n"
        "v_mfma_f32_16x16x32_bf16 v[142:145], v[74:77], v[184:187], 0\n"
        "v_mfma_f32_16x16x32_bf16 v[142:145], v[70:73], v[188:191], v[142:145]\n"
        "v_cvt_pk_bf16_f32 v220, v138, v139\n"
        "v_cvt_pk_bf16_f32 v221, v140, v141\n"
        "v_mfma_f32_16x16x32_bf16 v[142:145], v[66:69], v[192:195], v[142:145]\n"
        "v_mfma_f32_16x16x32_bf16 v[138:141], v[78:81], v[192:195], 0\n"
        "v_add_u32 v255, s30, %10\n"
        "v_mfma_f32_16x16x32_bf16 v[138:141], v[82:85], v[188:191], v[138:141]\n"
        "v_mfma_f32_16x16x32_bf16 v[138:141], v[86:89], v[184:187], v[138:141]\n"
        "v_mul_f32 v94, v94, v106\n"
        "v_mul_f32 v95, v95, v107\n"
        "v_mul_f32 v96, v96, v108\n"
        "v_mfma_f32_16x16x32_bf16 v[224:227], v[86:89], v[170:173], 0\n"
        "v_cvt_pk_bf16_f32 v106, v142, v143\n"
        "v_cvt_pk_bf16_f32 v107, v144, v145\n"
        "v_mul_f32 v97, v97, v109\n"
        "v_mfma_f32_16x16x32_bf16 v[224:227], v[82:85], v[166:169], v[224:227]\n"
        "v_cvt_pk_bf16_f32 v108, v138, v139\n"
        "v_cvt_pk_bf16_f32 v109, v140, v141\n"
        "v_mfma_f32_16x16x4_4b_bf16 v[136:151], v[174:175], v[152:153], 0 blgp:6\n"
        "ds_write_b128 v252, v[106:109] offset:128\n"
        "v_mul_f32 v158, v158, v110\n"
        "v_mul_f32 v159, v159, v111\n"
        "v_mul_f32 v160, v160, v112\n"
        "v_mul_f32 v161, v161, v113\n"
        "v_mfma_f32_16x16x32_bf16 v[224:227], v[78:81], v[162:165], v[224:227]\n"
        "v_cvt_pk_bf16_f32 v228, v94, v95\n"
        "v_cvt_pk_bf16_f32 v229, v96, v97\n"
        "v_cvt_pk_bf16_f32 v230, v158, v159\n"
        "v_mfma_f32_16x16x32_bf16 v[232:235], v[176:179], v[212:215], 0\n"
        "v_cvt_pk_bf16_f32 v231, v160, v161\n"
        "v_mul_f32 v196, v196, v114\n"
        "v_mul_f32 v197, v197, v115\n"
        "v_mfma_f32_16x16x32_bf16 v[232:235], v[236:239], v[216:219], v[232:235]\n"
        "v_cvt_pk_bf16_f32 v222, v224, v225\n"
        "v_cvt_pk_bf16_f32 v223, v226, v227\n"
        "v_mul_f32 v198, v198, v116\n"
        "v_mul_f32 v199, v199, v117\n"
        "v_mfma_f32_16x16x32_bf16 v[232:235], v[244:247], v[220:223], v[232:235]\n"
        "v_mul_f32 v200, v200, v118\n"
        "v_cvt_pk_bf16_f32 v224, v196, v197\n"
        "v_cvt_pk_bf16_f32 v225, v198, v199\n"
        "v_mfma_f32_16x16x32_bf16 v[240:243], v[102:105], v[220:223], 0\n"
        "v_cvt_pk_bf16_f32 v226, v200, 0\n"
        "v_mul_f32 v98, v98, v122\n"
        "v_mul_f32 v99, v99, v123\n"
        "v_mfma_f32_16x16x32_bf16 v[240:243], v[180:183], v[216:219], v[240:243]\n"
        "ds_read_u16 v227, %0 offset:29698\n"
        "v_mul_f32 v232, v232, v136\n"
        "v_mul_f32 v233, v233, v137\n"
        "v_mfma_f32_16x16x32_bf16 v[240:243], v[154:157], v[212:215], v[240:243]\n"
        "v_exp_f32 v232, v232\n"
        "v_mul_f32 v234, v234, v138\n"
        "v_mfma_f32_16x16x32_bf16 v[248:251], v[162:165], v[212:215], 0\n"
        "ds_read_b128 v[94:97], %15\n"
        "v_exp_f32 v233, v233\n"
        "v_mfma_f32_16x16x32_bf16 v[248:251], v[166:169], v[216:219], v[248:251]\n"
        "ds_read_b128 v[106:109], %15 offset:64\n"
        "v_exp_f32 v234, v234\n"
        "v_mfma_f32_16x16x32_bf16 v[248:251], v[170:173], v[220:223], v[248:251]\n"
        "ds_read_b128 v[110:113], %15 offset:128\n"
        "s_waitcnt lgkmcnt(3)\n"
        "v_add_u32 v227, s60, v227\n"
        "v_mfma_f32_16x16x32_bf16 v[150:153], v[184:187], v[220:223], 0\n"
        "v_subrev_u32 v254, 8320, v227\n"
        "v_min_u32 v227, v227, v254\n"
        "v_add_u32 v227, 110848, v227\n"
        "v_mfma_f32_16x16x32_bf16 v[150:153], v[188:191], v[216:219], v[150:153]\n"
        "v_mul_f32 v235, v235, v139\n"
        "v_add_f32 v252, v232, v233\n"
        "v_mul_f32 v240, v240, v140\n"
        "v_mfma_f32_16x16x32_bf16 v[150:153], v[192:195], v[212:215], v[150:153]\n"
        "s_waitcnt lgkmcnt(1)\n"
        "v_exp_f32 v235, v235\n"
        "v_add_f32 v252, v252, v234\n"
        "v_mfma_f32_16x16x32_bf16 v[114:117], v[94:97], v[176:179], 0\n"
        "v_exp_f32 v240, v240\n"
        "v_mul_f32 v241, v241, v141\n"
        "v_mfma_f32_16x16x32_bf16 v[114:117], v[106:109], v[236:239], v[114:117]\n"
        "s_waitcnt lgkmcnt(0)\n"
        "v_add_f32 v252, v252, v235\n"
        "v_exp_f32 v241, v241\n"
        "v_mfma_f32_16x16x32_bf16 v[114:117], v[110:113], v[244:247], v[114:117]\n"
        "ds_read_u16 v161, %0 offset:29702\n"
        "v_mul_f32 v242, v242, v142\n"
        "v_add_f32 v252, v252, v240\n"
        "v_mfma_f32_16x16x32_bf16 v[118:121], v[110:113], v[102:105], 0\n"
        "v_exp_f32 v242, v242\n"
        "v_mul_f32 v243, v243, v143\n"
        "v_mfma_f32_16x16x32_bf16 v[118:121], v[106:109], v[180:183], v[118:121]\n"
        "v_add_f32 v252, v252, v241\n"
        "v_exp_f32 v243, v243\n"
        "v_mfma_f32_16x16x32_bf16 v[118:121], v[94:97], v[154:157], v[118:121]\n"
        "ds_read_b64 v[122:123], v227\n"
        "v_mul_f32 v248, v248, v144\n"
        "v_add_f32 v252, v252, v242\n"
        "v_mfma_f32_16x16x32_bf16 v[136:139], v[94:97], v[162:165], 0\n"
        "ds_read_b64 v[152:153], %1\n"
        "s_waitcnt lgkmcnt(2)\n"
        "v_add_u32 v161, %10, v161\n"
        "v_mfma_f32_16x16x32_bf16 v[136:139], v[106:109], v[166:169], v[136:139]\n"
        "ds_read_b64 v[140:141], v227 offset:16\n"
        "v_cndmask_b32_e64 v254, v114, v118, s[50:51]\n"
        "v_mul_f32 v249, v249, v145\n"
        "v_mfma_f32_16x16x32_bf16 v[136:139], v[110:113], v[170:173], v[136:139]\n"
        "v_exp_f32 v248, v248\n"
        "v_add_f32 v252, v252, v243\n"
        "v_mfma_f32_16x16x32_bf16 v[114:117], v[110:113], v[184:187], 0\n"
        "ds_read_b128 v[118:121], %0 offset:9216\n"
        "v_exp_f32 v249, v249\n"
        "v_mfma_f32_16x16x32_bf16 v[114:117], v[106:109], v[188:191], v[114:117]\n"
        "ds_read_b128 v[110:113], %0 offset:10240\n"
        "v_mul_f32 v250, v250, v146\n"
        "v_cndmask_b32_e64 v254, v254, v136, s[52:53]\n"
        "v_mfma_f32_16x16x32_bf16 v[114:117], v[94:97], v[192:195], v[114:117]\n"
        "s_waitcnt lgkmcnt(2)\n"
        "v_add_f32 v252, v252, v248\n"
        "v_exp_f32 v250, v250\n"
        "v_mfma_f32_4x4x4_16b_bf16 v[94:97], v[122:123], v[152:153], 0\n"
        "v_mfma_f32_4x4x4_16b_bf16 v[106:109], v[140:141], v[152:153], 0\n"
        "v_mul_f32 v251, v251, v147\n"
        "v_add_f32 v252, v252, v249\n"
        "v_exp_f32 v251, v251\n"
        "v_mfma_f32_16x16x32_bf16 v[136:139], v[176:179], v[90:93], 0\n"
        "s_waitcnt lgkmcnt(1)\n"
        "v_cndmask_b32_e64 v254, v254, v114, s[54:55]\n"
        "v_mul_f32 v254, v254, v253\n"
        "v_mul_f32 v150, v150, v148\n"
        "v_mfma_f32_16x16x32_bf16 v[136:139], v[236:239], v[118:121], v[136:139]\n"
        "s_waitcnt lgkmcnt(0)\n"
        "v_add_f32 v252, v252, v250\n"
        "v_exp_f32 v150, v150\n"
        "v_mfma_f32_16x16x32_bf16 v[136:139], v[244:247], v[110:113], v[136:139]\n"
        "s_mov_b64 exec, s[56:57]\n"
        "v_mov_b32 v254, 0xff800000\n"
        "s_mov_b64 exec, -1\n"
        "v_mul_f32 v254, s58, v254\n"
        "v_mfma_f32_16x16x32_bf16 v[114:117], v[102:105], v[110:113], 0\n"
        "v_exp_f32 v254, v254\n"
        "v_add_f32 v252, v252, v251\n"
        "v_mfma_f32_16x16x32_bf16 v[114:117], v[180:183], v[118:121], v[114:117]\n"
        "v_cvt_pk_bf16_f32 v212, v136, v137\n"
        "v_cvt_pk_bf16_f32 v213, v138, v139\n"
        "v_add_f32_dpp v223, v254, v254 row_ror:8 row_mask:0xf bank_mask:0xf\n"
        "v_mfma_f32_16x16x32_bf16 v[114:117], v[154:157], v[90:93], v[114:117]\n"
        "v_add_f32_dpp v252, v150, v252 quad_perm:[0,1,2,3] row_mask:0x1 bank_mask:0xf\n"
        "v_add_f32_dpp v223, v223, v223 row_ror:4 row_mask:0xf bank_mask:0xf\n"
        "v_mul_f32 v254, v254, v253\n"
        "v_mfma_f32_16x16x32_bf16 v[136:139], v[162:165], v[90:93], 0\n"
        "ds_read_b128 v[140:143], %0 offset:11264\n"
        "v_add_f32_dpp v223, v223, v223 row_ror:2 row_mask:0xf bank_mask:0xf\n"
        "v_mul_f32 v100, v100, v124\n"
        "v_mfma_f32_16x16x32_bf16 v[136:139], v[166:169], v[118:121], v[136:139]\n"
        "ds_read_b128 v[144:147], %0 offset:12288\n"
        "v_cvt_pk_bf16_f32 v214, v114, v115\n"
        "v_cvt_pk_bf16_f32 v215, v116, v117\n"
        "v_mfma_f32_16x16x32_bf16 v[136:139], v[170:173], v[110:113], v[136:139]\n"
        "ds_read_b128 v[114:117], %0 offset:13312\n"
        "v_add_f32_dpp v223, v223, v223 row_ror:1 row_mask:0xf bank_mask:0xf\n"
        "v_mul_f32 v101, v101, v125\n"
        "v_mfma_f32_16x16x32_bf16 v[122:125], v[184:187], v[110:113], 0\n"
        "v_permlane32_swap_b32 v252, v223\n"
        "v_add_f32 v252, v252, v223\n"
        "v_mfma_f32_16x16x32_bf16 v[122:125], v[188:191], v[118:121], v[122:125]\n"
        "v_mov_b32 v253, v252\n"
        "v_cvt_pk_bf16_f32 v200, v136, v137\n"
        "v_cvt_pk_bf16_f32 v201, v138, v139\n"
        "v_mfma_f32_16x16x32_bf16 v[122:125], v[192:195], v[90:93], v[122:125]\n"
        "s_waitcnt lgkmcnt(1)\n"
        "v_permlane16_swap_b32 v252, v253\n"
        "v_add_f32 v252, v252, v253\n"
        "v_mfma_f32_16x16x32_bf16 v[110:113], v[192:195], v[140:143], 0\n"
        "v_rcp_f32 v253, v252\n"
        "v_mul_f32 v202, v202, v126\n"
        "v_mfma_f32_16x16x32_bf16 v[110:113], v[188:191], v[144:147], v[110:113]\n"
        "s_waitcnt lgkmcnt(0)\n"
        "v_cvt_pk_bf16_f32 v148, v122, v94\n"
        "v_cvt_pk_bf16_f32 v196, v123, v95\n"
        "v_cvt_pk_bf16_f32 v198, v124, v96\n"
        "v_mfma_f32_16x16x32_bf16 v[110:113], v[184:187], v[114:117], v[110:113]\n"
        "v_cvt_pk_bf16_f32 v222, v125, v97\n"
        "v_readlane_b32 s59, v253, 32\n"
        "v_min3_f32 v211, v211, v252, v253\n"
        "v_mfma_f32_16x16x32_bf16 v[94:97], v[170:173], v[114:117], 0\n"
        "v_mul_f32 v254, s59, v254\n"
        "v_cvt_pk_bf16_f32 v254, v254, 0\n"
        "v_cmp_nle_f32 vcc, 0x2b800000, v211\n"
        "v_mfma_f32_16x16x32_bf16 v[94:97], v[166:169], v[144:147], v[94:97]\n"
        "v_cvt_pk_bf16_f32 v149, v110, v106\n"
        "v_cvt_pk_bf16_f32 v197, v111, v107\n"
        "v_cvt_pk_bf16_f32 v199, v112, v108\n"
        "v_mfma_f32_16x16x32_bf16 v[94:97], v[162:165], v[140:143], v[94:97]\n"
        "ds_read_b64 v[118:119], v227 offset:32\n"
        "v_cvt_pk_bf16_f32 v223, v113, v109\n"
        "v_mul_f32 v203, v203, v127\n"
        "v_mfma_f32_16x16x32_bf16 v[106:109], v[154:157], v[140:143], 0\n"
        "s_andn2_b64 vcc, vcc, s[56:57]\n"
        "v_mul_f32 v204, v204, v128\n"
        "v_mul_f32 v205, v205, v129\n"
        "v_cvt_pk_bf16_f32 v216, v98, v99\n"
        "v_mfma_f32_16x16x32_bf16 v[106:109], v[180:183], v[144:147], v[106:109]\n"
        "ds_read_b64 v[120:121], v227 offset:48\n"
        "v_cvt_pk_bf16_f32 v220, v94, v95\n"
        "v_cvt_pk_bf16_f32 v221, v96, v97\n"
        "v_mfma_f32_16x16x32_bf16 v[106:109], v[102:105], v[114:117], v[106:109]\n"
        "ds_read_b128 v[136:139], %0 offset:14336\n"
        "v_cvt_pk_bf16_f32 v217, v100, v101\n"
        "v_cvt_pk_bf16_f32 v218, v202, v203\n"
        "v_mfma_f32_16x16x32_bf16 v[110:113], v[244:247], v[114:117], 0\n"
        "ds_read_b128 v[94:97], %0 offset:15360\n"
        "v_cvt_pk_bf16_f32 v219, v204, v205\n"
        "v_mul_f32 v206, v206, v130\n"
        "v_mfma_f32_16x16x32_bf16 v[110:113], v[236:239], v[144:147], v[110:113]\n"
        "ds_read_b128 v[202:205], %0 offset:16384\n"
        "v_cvt_pk_bf16_f32 v146, v106, v107\n"
        "v_cvt_pk_bf16_f32 v147, v108, v109\n"
        "v_mfma_f32_16x16x32_bf16 v[110:113], v[176:179], v[140:143], v[110:113]\n"
        "s_waitcnt lgkmcnt(3)\n"
        "v_mul_f32 v207, v207, v131\n"
        "v_mul_f32 v208, v208, v132\n"
        "v_mul_f32 v209, v209, v133\n"
        "v_mfma_f32_4x4x4_16b_bf16 v[140:143], v[118:119], v[152:153], 0\n"
        "v_mfma_f32_4x4x4_16b_bf16 v[98:101], v[120:121], v[152:153], 0\n"
        "v_mul_f32 v210, v210, v134\n"
        "v_cvt_pk_bf16_f32 v158, v206, v207\n"
        "v_cvt_pk_bf16_f32 v144, v110, v111\n"
        "s_waitcnt lgkmcnt(1)\n"
        "v_mfma_f32_16x16x32_bf16 v[122:125], v[176:179], v[136:139], 0\n"
        "s_cbranch_scc1 CREDOf_%=\n"
        "v_cvt_pk_bf16_f32 v145, v112, v113\n"
        "v_cvt_pk_bf16_f32 v159, v208, v209\n"
        "v_cvt_pk_bf16_f32 v160, v210, 0\n"
        "v_mfma_f32_16x16x32_bf16 v[122:125], v[236:239], v[94:97], v[122:125]\n"
        "s_waitcnt lgkmcnt(0)\n"
        "v_and_b32 v126, s28, v253\n"
        "v_sub_f32 v252, v253, v126\n"
        "v_cvt_pk_bf16_f32 v127, v126, v252\n"
        "v_mfma_f32_16x16x32_bf16 v[122:125], v[244:247], v[202:205], v[122:125]\n"
        "v_add_u32 %5, s69, %5\n"
        "v_mfma_f32_16x16x4_4b_bf16 v[106:121], v[174:175], v[126:127], 0 blgp:4\n"
        "ds_write_b64 v161, v[148:149]\n"
        "ds_write_b64 v161, v[196:197] offset:384\n"
        "ds_write_b64 v161, v[198:199] offset:768\n"
        "v_xor_b32 %8, %2, %8\n"
        "v_mfma_f32_16x16x32_bf16 v[206:209], v[102:105], v[202:205], 0\n"
        "v_mfma_f32_16x16x32_bf16 v[206:209], v[180:183], v[94:97], v[206:209]\n"
        "v_cvt_pk_bf16_f32 v196, v122, v123\n"
        "v_cvt_pk_bf16_f32 v197, v124, v125\n"
        "v_mfma_f32_16x16x32_bf16 v[206:209], v[154:157], v[136:139], v[206:209]\n"
        "v_mfma_f32_16x16x32_bf16 v[128:131], v[162:165], v[136:139], 0\n"
        "ds_read_b128 v[124:127], %0 offset:17408\n"
        "v_mfma_f32_16x16x32_bf16 v[128:131], v[166:169], v[94:97], v[128:131]\n"
        "ds_read_b128 v[132:135], %0 offset:18432\n"
        "v_mul_f32 v232, v232, v106\n"
        "v_mul_f32 v233, v233, v107\n"
        "v_mfma_f32_16x16x32_bf16 v[128:131], v[170:173], v[202:205], v[128:131]\n"
        "ds_read_b128 v[120:123], %0 offset:19456\n"
        "v_cvt_pk_bf16_f32 v198, v206, v207\n"
        "v_cvt_pk_bf16_f32 v199, v208, v209\n"
        "v_mfma_f32_16x16x32_bf16 v[206:209], v[184:187], v[202:205], 0\n"
        "v_mul_f32 v234, v234, v108\n"
        "v_mul_f32 v235, v235, v109\n"
        "v_mul_f32 v240, v240, v110\n"
        "v_mfma_f32_16x16x32_bf16 v[206:209], v[188:191], v[94:97], v[206:209]\n"
        "v_cvt_pk_bf16_f32 v148, v128, v129\n"
        "v_cvt_pk_bf16_f32 v149, v130, v131\n"
        "v_mul_f32 v241, v241, v111\n"
        "v_mfma_f32_16x16x32_bf16 v[206:209], v[192:195], v[136:139], v[206:209]\n"
        "s_waitcnt lgkmcnt(1)\n"
        "ds_write_b64 v161, v[222:223] offset:1152\n"
        "v_mul_f32 v242, v242, v112\n"
        "v_mfma_f32_16x16x32_bf16 v[106:109], v[192:195], v[124:127], 0\n"
        "ds_read2_b32 v[202:203], v255 offset0:0 offset1:0\n"
        "ds_read2_b32 v[222:223], v255 offset0:1 offset1:1\n"
        "v_mfma_f32_16x16x32_bf16 v[106:109], v[188:191], v[132:135], v[106:109]\n"
        "s_waitcnt lgkmcnt(3)\n"
        "v_cvt_pk_bf16_f32 v174, v206, v140\n"
        "v_cvt_pk_bf16_f32 v252, v207, v141\n"
        "v_cvt_pk_bf16_f32 v94, v208, v142\n"
        "v_mfma_f32_16x16x32_bf16 v[106:109], v[184:187], v[120:123], v[106:109]\n"
        "v_cvt_pk_bf16_f32 v96, v209, v143\n"
        "v_mul_f32 v243, v243, v113\n"
        "v_cvt_pk_bf16_f32 v204, v232, v233\n"
        "v_mfma_f32_16x16x32_bf16 v[110:113], v[170:173], v[120:123], 0\n"
        "v_cvt_pk_bf16_f32 v205, v234, v235\n"
        "v_cvt_pk_bf16_f32 v206, v240, v241\n"
        "v_cvt_pk_bf16_f32 v207, v242, v243\n"
        "v_mfma_f32_16x16x32_bf16 v[110:113], v[166:169], v[132:135], v[110:113]\n"
        "v_cvt_pk_bf16_f32 v175, v106, v98\n"
        "v_cvt_pk_bf16_f32 v253, v107, v99\n"
        "v_cvt_pk_bf16_f32 v95, v108, v100\n"
        "v_mfma_f32_16x16x32_bf16 v[110:113], v[162:165], v[124:127], v[110:113]\n"
        "ds_read_b64 v[128:129], v227 offset:64\n"
        "v_cvt_pk_bf16_f32 v97, v109, v101\n"
        "v_mul_f32 v248, v248, v114\n"
        "v_mfma_f32_16x16x32_bf16 v[98:101], v[154:157], v[124:127], 0\n"
        "v_mul_f32 v249, v249, v115\n"
        "v_mul_f32 v250, v250, v116\n"
        "v_mul_f32 v251, v251, v117\n"
        "v_mfma_f32_16x16x32_bf16 v[98:101], v[180:183], v[132:135], v[98:101]\n"
        "ds_read_b64 v[130:131], v227 offset:80\n"
        "v_cvt_pk_bf16_f32 v114, v110, v111\n"
        "v_cvt_pk_bf16_f32 v115, v112, v113\n"
        "v_mfma_f32_16x16x32_bf16 v[98:101], v[102:105], v[120:123], v[98:101]\n"
        "ds_read_b128 v[106:109], %0 offset:20480\n"
        "v_mul_f32 v150, v150, v118\n"
        "v_cvt_pk_bf16_f32 v208, v248, v249\n"
        "v_mfma_f32_16x16x32_bf16 v[110:113], v[244:247], v[120:123], 0\n"
        "ds_read_b128 v[116:119], %0 offset:21504\n"
        "v_cvt_pk_bf16_f32 v209, v250, v251\n"
        "v_cvt_pk_bf16_f32 v210, v150, 0\n"
        "v_mfma_f32_16x16x32_bf16 v[110:113], v[236:239], v[132:135], v[110:113]\n"
        "ds_read_b128 v[120:123], %0 offset:22528\n"
        "v_cvt_pk_bf16_f32 v234, v98, v99\n"
        "v_cvt_pk_bf16_f32 v235, v100, v101\n"
        "v_mfma_f32_16x16x32_bf16 v[110:113], v[176:179], v[124:127], v[110:113]\n"
        "s_waitcnt lgkmcnt(3)\n"
        "v_mfma_f32_4x4x4_16b_bf16 v[98:101], v[128:129], v[152:153], 0\n"
        "v_mfma_f32_4x4x4_16b_bf16 v[124:127], v[130:131], v[152:153], 0\n"
        "s_waitcnt lgkmcnt(2)\n"
        "v_mfma_f32_16x16x32_bf16 v[128:131], v[176:179], v[106:109], 0\n"
        "s_waitcnt lgkmcnt(1)\n"
        "v_mfma_f32_16x16x32_bf16 v[128:131], v[236:239], v[116:119], v[128:131]\n"
        "s_waitcnt lgkmcnt(0)\n"
        "v_cvt_pk_bf16_f32 v232, v110, v111\n"
        "v_cvt_pk_bf16_f32 v233, v112, v113\n"
        "v_mfma_f32_16x16x32_bf16 v[128:131], v[244:247], v[120:123], v[128:131]\n"
        "v_mfma_f32_16x16x32_bf16 v[110:113], v[102:105], v[120:123], 0\n"
        "v_mfma_f32_16x16x32_bf16 v[110:113], v[180:183], v[116:119], v[110:113]\n"
        "v_mfma_f32_16x16x32_bf16 v[110:113], v[154:157], v[106:109], v[110:113]\n"
        "v_mfma_f32_16x16x32_bf16 v[132:135], v[162:165], v[106:109], 0\n"
        "ds_read_b128 v[136:139], %0 offset:23552\n"
        "v_mfma_f32_16x16x32_bf16 v[132:135], v[166:169], v[116:119], v[132:135]\n"
        "ds_read_b128 v[140:143], %0 offset:24576\n"
        "v_mfma_f32_16x16x32_bf16 v[132:135], v[170:173], v[120:123], v[132:135]\n"
        "ds_read_b128 v[240:243], %0 offset:25600\n"
        "v_cvt_pk_bf16_f32 v248, v128, v129\n"
        "v_cvt_pk_bf16_f32 v249, v130, v131\n"
        "v_mfma_f32_16x16x32_bf16 v[128:131], v[184:187], v[120:123], 0\n"
        "ds_write_b64 v161, v[174:175] offset:8\n"
        "v_cvt_pk_bf16_f32 v250, v110, v111\n"
        "v_mfma_f32_16x16x32_bf16 v[128:131], v[188:191], v[116:119], v[128:131]\n"
        "ds_write_b64 v161, v[252:253] offset:392\n"
        "v_cvt_pk_bf16_f32 v251, v112, v113\n"
        "v_cvt_pk_bf16_f32 v110, v132, v133\n"
        "v_cvt_pk_bf16_f32 v111, v134, v135\n"
        "v_mfma_f32_16x16x32_bf16 v[128:131], v[192:195], v[106:109], v[128:131]\n"
        "s_waitcnt lgkmcnt(2)\n"
        "ds_write_b64 v161, v[94:95] offset:776\n"
        "v_mfma_f32_16x16x32_bf16 v[106:109], v[192:195], v[136:139], 0\n"
        "ds_read2_b32 v[150:151], v255 offset0:2 offset1:2\n"
        "ds_read2_b32 v[116:117], v255 offset0:3 offset1:3\n"
        "v_mfma_f32_16x16x32_bf16 v[106:109], v[188:191], v[140:143], v[106:109]\n"
        "ds_read_b64 v[112:113], %7\n"
        "v_mfma_f32_16x16x32_bf16 v[106:109], v[184:187], v[240:243], v[106:109]\n"
        "ds_write_b64 v161, v[96:97] offset:1160\n"
        "v_cvt_pk_bf16_f32 v134, v128, v98\n"
        "v_mfma_f32_16x16x32_bf16 v[94:97], v[170:173], v[240:243], 0\n"
        "ds_read_b64 v[122:123], %7 offset:16\n"
        "v_cvt_pk_bf16_f32 v170, v129, v99\n"
        "v_cvt_pk_bf16_f32 v172, v130, v100\n"
        "v_mfma_f32_16x16x32_bf16 v[94:97], v[166:169], v[140:143], v[94:97]\n"
        "v_cvt_pk_bf16_f32 v184, v131, v101\n"
        "v_cvt_pk_bf16_f32 v135, v106, v124\n"
        "v_cvt_pk_bf16_f32 v171, v107, v125\n"
        "v_mfma_f32_16x16x32_bf16 v[94:97], v[162:165], v[136:139], v[94:97]\n"
        "ds_read_b64 v[128:129], %7 offset:32\n"
        "v_cvt_pk_bf16_f32 v173, v108, v126\n"
        "v_cvt_pk_bf16_f32 v185, v109, v127\n"
        "v_mfma_f32_16x16x32_bf16 v[98:101], v[154:157], v[136:139], 0\n"
        "v_mfma_f32_16x16x32_bf16 v[98:101], v[180:183], v[140:143], v[98:101]\n"
        "ds_read_b64 v[126:127], %7 offset:48\n"
        "v_mfma_f32_16x16x32_bf16 v[98:101], v[102:105], v[240:243], v[98:101]\n"
        "v_mfma_f32_16x16x32_bf16 v[106:109], v[244:247], v[240:243], 0\n"
        "ds_read_b64 v[130:131], %7 offset:64\n"
        "v_cvt_pk_bf16_f32 v162, v94, v95\n"
        "v_cvt_pk_bf16_f32 v163, v96, v97\n"
        "v_mfma_f32_16x16x32_bf16 v[106:109], v[236:239], v[140:143], v[106:109]\n"
        "v_mfma_f32_16x16x32_bf16 v[106:109], v[176:179], v[136:139], v[106:109]\n"
        "ds_read_b64 v[140:141], %7 offset:80\n"
        "v_mov_b32 v227, 0\n"
        "s_waitcnt lgkmcnt(2)\n"
        "v_mfma_f32_4x4x4_16b_bf16 v[94:97], v[112:113], v[152:153], 0\n"
        "v_mfma_f32_4x4x4_16b_bf16 v[102:105], v[122:123], v[152:153], 0\n"
        "v_mfma_f32_4x4x4_16b_bf16 v[118:121], v[128:129], v[152:153], 0\n"
        "v_mfma_f32_4x4x4_16b_bf16 v[122:125], v[126:127], v[152:153], 0\n"
        "s_waitcnt lgkmcnt(1)\n"
        "v_mfma_f32_4x4x4_16b_bf16 v[126:129], v[130:131], v[152:153], 0\n"
        "s_waitcnt lgkmcnt(0)\n"
        "v_mfma_f32_4x4x4_16b_bf16 v[130:133], v[140:141], v[152:153], 0\n"
        "v_cvt_pk_bf16_f32 v192, v106, v107\n"
        "v_cvt_pk_bf16_f32 v193, v108, v109\n"
        "v_cvt_pk_bf16_f32 v194, v98, v99\n"
        "v_cvt_pk_bf16_f32 v195, v100, v101\n"
        "v_mfma_f32_16x16x32_bf16 v[94:97], v[228:231], v[212:215], v[94:97]\n"
        "v_mfma_f32_16x16x32_bf16 v[94:97], v[224:227], v[200:203], v[94:97]\n"
        "v_mfma_f32_16x16x32_bf16 v[102:105], v[224:227], v[220:223], v[102:105]\n"
        "v_mfma_f32_16x16x32_bf16 v[102:105], v[228:231], v[144:147], v[102:105]\n"
        "ds_write_b64 v161, v[134:135] offset:16\n"
        "ds_read2_b32 v[112:113], v255 offset0:4 offset1:4\n"
        "v_mfma_f32_16x16x32_bf16 v[118:121], v[228:231], v[196:199], v[118:121]\n"
        "ds_read2_b32 v[164:165], v255 offset0:5 offset1:5\n"
        "v_mfma_f32_16x16x32_bf16 v[118:121], v[224:227], v[148:151], v[118:121]\n"
        "ds_write_b16 %3, v254\n"
        "v_mfma_f32_16x16x32_bf16 v[122:125], v[224:227], v[114:117], v[122:125]\n"
        "ds_read_b128 v[154:157], %4\n"
        "ds_write_b64 v161, v[170:171] offset:400\n"
        "v_cvt_pk_bf16_f32 v134, v94, v102\n"
        "v_cvt_pk_bf16_f32 v170, v95, v103\n"
        "v_cvt_pk_bf16_f32 v174, v96, v104\n"
        "v_mfma_f32_16x16x32_bf16 v[122:125], v[228:231], v[232:235], v[122:125]\n"
        "ds_read_b128 v[166:169], %4 offset:64\n"
        "ds_write_b64 v161, v[172:173] offset:784\n"
        "v_cvt_pk_bf16_f32 v178, v97, v105\n"
        "v_mfma_f32_16x16x32_bf16 v[126:129], v[228:231], v[248:251], v[126:129]\n"
        "s_waitcnt lgkmcnt(4)\n"
        "ds_write_b64 v161, v[184:185] offset:1168\n"
        "v_mfma_f32_16x16x32_bf16 v[126:129], v[224:227], v[110:113], v[126:129]\n"
        "ds_read_b64 v[106:107], %7 offset:3328\n"
        "v_cvt_pk_bf16_f32 v135, v118, v122\n"
        "v_cvt_pk_bf16_f32 v171, v119, v123\n"
        "v_mfma_f32_16x16x32_bf16 v[130:133], v[224:227], v[162:165], v[130:133]\n"
        "v_cvt_pk_bf16_f32 v175, v120, v124\n"
        "v_cvt_pk_bf16_f32 v179, v121, v125\n"
        "v_mfma_f32_16x16x32_bf16 v[130:133], v[228:231], v[192:195], v[130:133]\n"
        "ds_read_b64 v[118:119], %7 offset:3344\n"
        "s_waitcnt lgkmcnt(3)\n"
        "v_mfma_f32_16x16x32_bf16 v[94:97], v[154:157], v[212:215], 0\n"
        "v_mfma_f32_16x16x32_bf16 v[94:97], v[166:169], v[200:203], v[94:97]\n"
        "ds_read_b64 v[124:125], %7 offset:3360\n"
        "v_mfma_f32_16x16x32_bf16 v[98:101], v[166:169], v[220:223], 0\n"
        "v_mfma_f32_16x16x32_bf16 v[98:101], v[154:157], v[144:147], v[98:101]\n"
        "ds_read_b64 v[138:139], %7 offset:3376\n"
        "v_cvt_pk_bf16_f32 v136, v126, v130\n"
        "v_cvt_pk_bf16_f32 v172, v127, v131\n"
        "v_mfma_f32_16x16x32_bf16 v[102:105], v[154:157], v[196:199], 0\n"
        "s_add_u32 s31, s31, 1\n"
        "v_cvt_pk_bf16_f32 v176, v128, v132\n"
        "v_cvt_pk_bf16_f32 v180, v129, v133\n"
        "v_mfma_f32_16x16x32_bf16 v[102:105], v[166:169], v[148:151], v[102:105]\n"
        "ds_read_b64 v[126:127], %7 offset:3392\n"
        "v_cvt_pk_bf16_f32 v182, v94, v98\n"
        "s_add_u32 s37, s35, 0\n"
        "v_mfma_f32_16x16x32_bf16 v[94:97], v[166:169], v[114:117], 0\n"
        "s_mul_i32 s68, s31, 3\n"
        "s_lshr_b32 s38, s31, 3\n"
        "v_mfma_f32_16x16x32_bf16 v[94:97], v[154:157], v[232:235], v[94:97]\n"
        "ds_read_b64 v[128:129], %7 offset:3408\n"
        "s_add_u32 s38, s38, 11\n"
        "s_waitcnt lgkmcnt(2)\n"
        "v_mfma_f32_4x4x4_16b_bf16 v[98:101], v[106:107], v[152:153], 0\n"
        "buffer_store_dwordx3 v[134:136], %11, %22, s37 offen sc1 nt\n"
        "v_mfma_f32_4x4x4_16b_bf16 v[106:109], v[118:119], v[152:153], 0\n"
        "v_mfma_f32_4x4x4_16b_bf16 v[120:123], v[124:125], v[152:153], 0\n"
        "v_mfma_f32_4x4x4_16b_bf16 v[132:135], v[138:139], v[152:153], 0\n"
        "s_waitcnt lgkmcnt(1)\n"
        "v_mfma_f32_4x4x4_16b_bf16 v[136:139], v[126:127], v[152:153], 0\n"
        "s_waitcnt lgkmcnt(0)\n"
        "v_mfma_f32_4x4x4_16b_bf16 v[140:143], v[128:129], v[152:153], 0\n"
        "s_add_u32 s37, s35, 0\n"
        "buffer_store_dwordx3 v[170:172], %14, %22, s37 offen offset:768 sc1 nt\n"
        "s_bitcmp1_b32 s31, 0\n"
        "s_cselect_b32 s38, 3, s38\n"
        "s_add_u32 s37, s35, 98112\n"
        "s_add_u32 s68, s68, s38\n"
        "v_mfma_f32_16x16x32_bf16 v[124:127], v[154:157], v[248:251], 0\n"
        "s_cmp_lt_u32 s31, %24\n"
        "buffer_store_dwordx3 v[174:176], %11, %22, s37 offen offset:192 sc1 nt\n"
        "s_cselect_b32 s68, s68, 0\n"
        "s_add_u32 s37, s35, 98112\n"
        "v_mfma_f32_16x16x32_bf16 v[124:127], v[166:169], v[110:113], v[124:127]\n"
        "ds_read_b64 v[104:105], %7 offset:6656\n"
        "s_and_b32 s38, s31, 7\n"
        "v_mov_b32 v161, 0\n"
        "v_cvt_pk_bf16_f32 v183, v102, v94\n"
        "s_mul_i32 s29, s38, 192\n"
        "v_mfma_f32_16x16x32_bf16 v[94:97], v[166:169], v[162:165], 0\n"
        "s_mul_i32 s30, s38, 384\n"
        "buffer_store_dwordx3 v[178:180], %13, %22, s37 offen offset:960 sc1 nt\n"
        "s_add_u32 s34, s34, 832\n"
        "v_mfma_f32_16x16x32_bf16 v[94:97], v[154:157], v[192:195], v[94:97]\n"
        "ds_read_b64 v[102:103], %7 offset:6672\n"
        "s_cmp_eq_u32 s34, 8320\n"
        "v_mfma_f32_16x16x32_bf16 v[98:101], v[216:219], v[212:215], v[98:101]\n"
        "s_cselect_b32 s34, 0, s34\n"
        "v_mfma_f32_16x16x32_bf16 v[98:101], v[158:161], v[200:203], v[98:101]\n"
        "ds_read_b64 v[118:119], %7 offset:6688\n"
        "s_add_u32 s73, s34, %19\n"
        "v_mfma_f32_16x16x32_bf16 v[106:109], v[158:161], v[220:223], v[106:109]\n"
        "s_mul_i32 s38, s31, 1344\n"
        "v_cvt_pk_bf16_f32 v184, v124, v94\n"
        "v_mfma_f32_16x16x32_bf16 v[106:109], v[216:219], v[144:147], v[106:109]\n"
        "ds_read_b64 v[94:95], %7 offset:6704\n"
        "s_cmp_ge_u32 s31, s71\n"
        "v_mfma_f32_16x16x32_bf16 v[120:123], v[216:219], v[196:199], v[120:123]\n"
        "s_cselect_b32 s39, s70, s72\n"
        "v_mfma_f32_16x16x32_bf16 v[120:123], v[158:161], v[148:151], v[120:123]\n"
        "ds_read_b64 v[96:97], %7 offset:6720\n"
        "s_add_u32 s37, s35, 588672\n"
        "s_add_u32 s39, s39, s38\n"
        "v_mfma_f32_16x16x32_bf16 v[132:135], v[158:161], v[114:117], v[132:135]\n"
        "s_add_u32 s46, s37, 1344\n"
        "v_mfma_f32_16x16x32_bf16 v[132:135], v[216:219], v[232:235], v[132:135]\n"
        "ds_read_b64 v[154:155], %7 offset:6736\n"
        "v_add_u32 %7, s69, %7\n"
        "s_waitcnt lgkmcnt(2)\n"
        "v_mfma_f32_4x4x4_16b_bf16 v[124:127], v[104:105], v[152:153], 0\n"
        "buffer_store_dwordx3 v[182:184], %11, s[44:47], s37 offen offset:1152 sc1 nt\n"
        "v_mfma_f32_4x4x4_16b_bf16 v[128:131], v[102:103], v[152:153], 0\n"
        "v_mfma_f32_4x4x4_16b_bf16 v[168:171], v[118:119], v[152:153], 0\n"
        "v_mfma_f32_4x4x4_16b_bf16 v[172:175], v[94:95], v[152:153], 0\n"
        "s_waitcnt lgkmcnt(1)\n"
        "v_mfma_f32_4x4x4_16b_bf16 v[184:187], v[96:97], v[152:153], 0\n"
        "s_waitcnt lgkmcnt(0)\n"
        "v_mfma_f32_4x4x4_16b_bf16 v[188:191], v[154:155], v[152:153], 0\n"
        "v_xor_b32 v255, 4, %17\n"
        "v_mov_b32 v211, 0\n"
        "v_mfma_f32_16x16x32_bf16 v[136:139], v[216:219], v[248:251], v[136:139]\n"
        "v_mfma_f32_16x16x32_bf16 v[136:139], v[158:161], v[110:113], v[136:139]\n"
        "v_mfma_f32_16x16x32_bf16 v[140:143], v[158:161], v[162:165], v[140:143]\n"
        "ds_read_b32 v254, v255\n"
        "v_mfma_f32_16x16x32_bf16 v[140:143], v[216:219], v[192:195], v[140:143]\n"
        "v_mfma_f32_16x16x32_bf16 v[124:127], v[204:207], v[212:215], v[124:127]\n"
        "v_mfma_f32_16x16x32_bf16 v[124:127], v[208:211], v[200:203], v[124:127]\n"
        "v_mfma_f32_16x16x32_bf16 v[128:131], v[208:211], v[220:223], v[128:131]\n"
        "v_mfma_f32_16x16x32_bf16 v[128:131], v[204:207], v[144:147], v[128:131]\n"
        "s_waitcnt lgkmcnt(0)\n"
        "v_readfirstlane_b32 s40, v254\n"
        "v_mfma_f32_16x16x32_bf16 v[168:171], v[204:207], v[196:199], v[168:171]\n"
        "s_cmp_lt_i32 s40, s68\n"
        "v_mfma_f32_16x16x32_bf16 v[168:171], v[208:211], v[148:151], v[168:171]\n"
        "s_cbranch_scc1 L18_%=\n"
        "L19_%=:\n"
        "ds_read_b128 v[176:179], %5\n"
        "ds_read_b128 v[154:157], %5 offset:3328\n"
        "v_mfma_f32_16x16x32_bf16 v[172:175], v[208:211], v[114:117], v[172:175]\n"
        "ds_read_b128 v[236:239], %5 offset:64\n"
        "ds_read_b128 v[180:183], %5 offset:3392\n"
        "ds_read_b128 v[244:247], %5 offset:128\n"
        "v_mfma_f32_16x16x32_bf16 v[172:175], v[204:207], v[232:235], v[172:175]\n"
        "ds_read_b128 v[102:105], %5 offset:3456\n"
        "v_mfma_f32_16x16x32_bf16 v[184:187], v[204:207], v[248:251], v[184:187]\n"
        "s_sub_u32 s69, 0, s69\n"
        "v_mfma_f32_16x16x32_bf16 v[184:187], v[208:211], v[110:113], v[184:187]\n"
        "s_mov_b64 exec, 1\n"
        "ds_inc_u32 %17, %1 offset:8\n"
        "s_mov_b64 exec, -1\n"
        "v_mfma_f32_16x16x32_bf16 v[188:191], v[208:211], v[162:165], v[188:191]\n"
        "s_mov_b32 s36, s35\n"
        "v_xor_b32 %17, 4, %17\n"
        "s_mov_b32 s35, s39\n"
        "v_mfma_f32_16x16x32_bf16 v[188:191], v[204:207], v[192:195], v[188:191]\n"
        "s_setprio 0\n"
        "s_waitcnt lgkmcnt(0)\n"
        "s_nop 7\n"
        "s_cmp_ge_u32 s31, %24\n"
        "s_cbranch_scc1 CEND_%=\n"
        "CTOP_%=:\n"
        "s_waitcnt lgkmcnt(5)\n"
        "v_mfma_f32_16x16x32_bf16 v[94:97], v[18:21], v[176:179], 0\n"
        "v_cvt_pk_bf16_f32 v110, v98, v106\n"
        "v_cvt_pk_bf16_f32 v111, v120, v132\n"
        "s_waitcnt lgkmcnt(3)\n"
        "v_mfma_f32_16x16x32_bf16 v[94:97], v[22:25], v[236:239], v[94:97]\n"
        "s_add_u32 s37, s36, 196224\n"
        "v_cvt_pk_bf16_f32 v112, v136, v140\n"
        "v_cvt_pk_bf16_f32 v114, v99, v107\n"
        "s_waitcnt lgkmcnt(1)\n"
        "v_mfma_f32_16x16x32_bf16 v[94:97], v[26:29], v[244:247], v[94:97]\n"
        "v_cvt_pk_bf16_f32 v115, v121, v133\n"
        "v_cvt_pk_bf16_f32 v116, v137, v141\n"
        "s_waitcnt lgkmcnt(0)\n"
        "v_mfma_f32_16x16x32_bf16 v[118:121], v[26:29], v[102:105], 0\n"
        "v_cvt_pk_bf16_f32 v98, v100, v108\n"
        "v_cvt_pk_bf16_f32 v99, v122, v134\n"
        "v_mfma_f32_16x16x32_bf16 v[118:121], v[22:25], v[180:183], v[118:121]\n"
        "buffer_store_dwordx3 v[110:112], %11, %22, s37 offen offset:384 sc1 nt\n"
        "v_mfma_f32_16x16x32_bf16 v[118:121], v[18:21], v[154:157], v[118:121]\n"
        "v_cvt_pk_bf16_f32 v100, v138, v142\n"
        "v_cvt_pk_bf16_f32 v106, v94, v95\n"
        "v_cvt_pk_bf16_f32 v107, v96, v97\n"
        "v_mfma_f32_16x16x32_bf16 v[94:97], v[30:33], v[154:157], 0\n"
        "s_add_u32 s37, s36, 196224\n"
        "v_cvt_pk_bf16_f32 v110, v101, v109\n"
        "v_cvt_pk_bf16_f32 v111, v123, v135\n"
        "v_mfma_f32_16x16x32_bf16 v[94:97], v[34:37], v[180:183], v[94:97]\n"
        "v_cvt_pk_bf16_f32 v140, v118, v119\n"
        "v_cvt_pk_bf16_f32 v141, v120, v121\n"
        "v_mfma_f32_16x16x32_bf16 v[94:97], v[38:41], v[102:105], v[94:97]\n"
        "v_cvt_pk_bf16_f32 v112, v139, v143\n"
        "v_cvt_pk_bf16_f32 v118, v124, v128\n"
        "v_mfma_f32_16x16x32_bf16 v[132:135], v[38:41], v[244:247], 0\n"
        "buffer_store_dwordx3 v[114:116], %12, %22, s37 offen offset:1152 sc1 nt\n"
        "v_mfma_f32_16x16x32_bf16 v[132:135], v[34:37], v[236:239], v[132:135]\n"
        "s_mov_b64 exec, s[62:63]\n"
        "v_add_u32 %15, s29, %9\n"
        "v_add_u32 %6, s34, %16\n"
        "s_mov_b64 exec, -1\n"
        "v_mfma_f32_16x16x32_bf16 v[132:135], v[30:33], v[176:179], v[132:135]\n"
        "s_add_u32 s37, s36, 294336\n"
        "v_cvt_pk_bf16_f32 v142, v94, v95\n"
        "v_cvt_pk_bf16_f32 v143, v96, v97\n"
        "v_mfma_f32_16x16x32_bf16 v[94:97], v[42:45], v[176:179], 0\n"
        "v_cvt_pk_bf16_f32 v119, v168, v172\n"
        "v_cvt_pk_bf16_f32 v120, v184, v188\n"
        "v_mfma_f32_16x16x32_bf16 v[94:97], v[46:49], v[236:239], v[94:97]\n"
        "v_cvt_pk_bf16_f32 v114, v125, v129\n"
        "v_cvt_pk_bf16_f32 v108, v132, v133\n"
        "v_mfma_f32_16x16x32_bf16 v[94:97], v[50:53], v[244:247], v[94:97]\n"
        "buffer_store_dwordx3 v[98:100], %11, %22, s37 offen offset:576 sc1 nt\n"
        "v_mfma_f32_16x16x32_bf16 v[122:125], v[50:53], v[102:105], 0\n"
        "v_cvt_pk_bf16_f32 v109, v134, v135\n"
        "v_cvt_pk_bf16_f32 v115, v169, v173\n"
        "v_cvt_pk_bf16_f32 v116, v185, v189\n"
        "v_mfma_f32_16x16x32_bf16 v[122:125], v[46:49], v[180:183], v[122:125]\n"
        "s_add_u32 s37, s36, 392448\n"
        "v_writelane_b32 %8, s73, 48\n"
        "v_cvt_pk_bf16_f32 v144, v94, v95\n"
        "v_mfma_f32_16x16x32_bf16 v[122:125], v[42:45], v[154:157], v[122:125]\n"
        "v_cvt_pk_bf16_f32 v145, v96, v97\n"
        "v_cvt_pk_bf16_f32 v94, v126, v130\n"
        "v_mfma_f32_16x16x32_bf16 v[98:101], v[54:57], v[154:157], 0\n"
        "v_cvt_pk_bf16_f32 v95, v170, v174\n"
        "v_cvt_pk_bf16_f32 v96, v186, v190\n"
        "v_mfma_f32_16x16x32_bf16 v[98:101], v[58:61], v[180:183], v[98:101]\n"
        "buffer_store_dwordx3 v[110:112], %11, %22, s37 offen sc1 nt\n"
        "v_mfma_f32_16x16x32_bf16 v[98:101], v[62:65], v[102:105], v[98:101]\n"
        "ds_read_b32 v153, %8\n"
        "v_cvt_pk_bf16_f32 v166, v122, v123\n"
        "v_cvt_pk_bf16_f32 v167, v124, v125\n"
        "v_mfma_f32_16x16x32_bf16 v[122:125], v[62:65], v[244:247], 0\n"
        "s_add_u32 s37, s36, 392448\n"
        "v_cvt_pk_bf16_f32 v172, v127, v131\n"
        "v_cvt_pk_bf16_f32 v173, v171, v175\n"
        "v_mfma_f32_16x16x32_bf16 v[122:125], v[58:61], v[236:239], v[122:125]\n"
        "v_cvt_pk_bf16_f32 v168, v98, v99\n"
        "v_cvt_pk_bf16_f32 v169, v100, v101\n"
        "v_mfma_f32_16x16x32_bf16 v[122:125], v[54:57], v[176:179], v[122:125]\n"
        "v_cvt_pk_bf16_f32 v174, v187, v191\n"
        "v_mfma_f32_16x16x32_bf16 v[98:101], v[66:69], v[176:179], 0\n"
        "buffer_store_dwordx3 v[118:120], %14, %22, s37 offen offset:768 sc1 nt\n"
        "v_mfma_f32_16x16x32_bf16 v[98:101], v[70:73], v[236:239], v[98:101]\n"
        "s_waitcnt lgkmcnt(0)\n"
        "v_and_b32 v110, s28, v153\n"
        "v_sub_f32 v255, v153, v110\n"
        "v_mfma_f32_16x16x32_bf16 v[98:101], v[74:77], v[244:247], v[98:101]\n"
        "s_add_u32 s37, s36, 490560\n"
        "v_cvt_pk_bf16_f32 v146, v122, v123\n"
        "v_cvt_pk_bf16_f32 v147, v124, v125\n"
        "v_and_b32 v118, s28, v153\n"
        "v_mfma_f32_16x16x32_bf16 v[120:123], v[74:77], v[102:105], 0\n"
        "v_cvt_pk_bf16_f32 v111, v255, v110\n"
        "v_cvt_pk_bf16_f32 v119, v118, v255\n"
        "v_mfma_f32_16x16x32_bf16 v[120:123], v[70:73], v[180:183], v[120:123]\n"
        "v_cvt_pk_bf16_f32 v184, v98, v99\n"
        "v_cvt_pk_bf16_f32 v185, v100, v101\n"
        "v_mfma_f32_16x16x32_bf16 v[120:123], v[66:69], v[154:157], v[120:123]\n"
        "buffer_store_dwordx3 v[114:116], %11, %22, s37 offen offset:192 sc1 nt\n"
        "v_mfma_f32_16x16x32_bf16 v[98:101], v[78:81], v[154:157], 0\n"
        "v_readlane_b32 s58, v153, 48\n"
        "v_mfma_f32_16x16x32_bf16 v[98:101], v[82:85], v[180:183], v[98:101]\n"
        "s_add_u32 s37, s36, 490560\n"
        "v_mfma_f32_16x16x32_bf16 v[98:101], v[86:89], v[102:105], v[98:101]\n"
        "v_mfma_f32_16x16x32_bf16 v[148:151], v[86:89], v[244:247], 0\n"
        "v_mfma_f32_16x16x32_bf16 v[148:151], v[82:85], v[236:239], v[148:151]\n"
        "buffer_store_dwordx3 v[94:96], %13, %22, s37 offen offset:960 sc1 nt\n"
        "v_mfma_f32_16x16x4_4b_bf16 v[124:139], v[110:111], v[118:119], 0 blgp:4\n"
        "v_cvt_pk_bf16_f32 v206, v120, v121\n"
        "v_cvt_pk_bf16_f32 v207, v122, v123\n"
        "v_mfma_f32_16x16x32_bf16 v[148:151], v[78:81], v[176:179], v[148:151]\n"
        "v_mfma_f32_16x16x32_bf16 v[112:115], v[176:179], v[106:109], 0\n"
        "ds_read_b128 v[202:205], %5 offset:6656\n"
        "v_cvt_pk_bf16_f32 v208, v98, v99\n"
        "v_cvt_pk_bf16_f32 v209, v100, v101\n"
        "v_mfma_f32_16x16x32_bf16 v[112:115], v[236:239], v[144:147], v[112:115]\n"
        "ds_read_b128 v[158:161], %5 offset:6720\n"
        "s_add_u32 s37, s36, 588672\n"
        "ds_read_b128 v[162:165], %5 offset:6784\n"
        "v_cvt_pk_bf16_f32 v186, v148, v149\n"
        "v_cvt_pk_bf16_f32 v187, v150, v151\n"
        "s_nop 1\n"
        "v_mfma_f32_16x16x32_bf16 v[112:115], v[244:247], v[184:187], v[112:115]\n"
        "v_mfma_f32_16x16x32_bf16 v[98:101], v[102:105], v[184:187], 0\n"
        "ds_read_b128 v[120:123], %6 offset:128\n"
        "v_mfma_f32_16x16x32_bf16 v[98:101], v[180:183], v[144:147], v[98:101]\n"
        "ds_read_b128 v[226:229], %6 offset:64\n"
        "v_mfma_f32_16x16x32_bf16 v[98:101], v[154:157], v[106:109], v[98:101]\n"
        "ds_read_b128 v[148:151], %6\n"
        "s_waitcnt lgkmcnt(3)\n"
        "v_mfma_f32_16x16x32_bf16 v[248:251], v[202:205], v[106:109], 0\n"
        "buffer_store_dwordx3 v[172:174], %11, %22, s37 offen offset:384 sc1 nt\n"
        "v_mul_f32 v112, v112, v124\n"
        "v_mfma_f32_16x16x32_bf16 v[248:251], v[158:161], v[144:147], v[248:251]\n"
        "v_mul_f32 v113, v113, v125\n"
        "v_mul_f32 v114, v114, v126\n"
        "v_mfma_f32_16x16x32_bf16 v[248:251], v[162:165], v[184:187], v[248:251]\n"
        "s_waitcnt lgkmcnt(1)\n"
        "v_exp_f32 v112, v112\n"
        "v_mfma_f32_16x16x32_bf16 v[230:233], v[120:123], v[184:187], 0\n"
        "v_exp_f32 v113, v113\n"
        "v_mul_f32 v115, v115, v127\n"
        "v_mfma_f32_16x16x32_bf16 v[230:233], v[226:229], v[144:147], v[230:233]\n"
        "s_waitcnt lgkmcnt(0)\n"
        "v_exp_f32 v114, v114\n"
        "v_mfma_f32_16x16x4_4b_bf16 v[184:199], v[110:111], v[118:119], 0 blgp:5\n"
        "v_add_f32 v152, v112, v113\n"
        "v_exp_f32 v115, v115\n"
        "v_mul_f32 v98, v98, v128\n"
        "v_add_f32 v152, v152, v114\n"
        "v_exp_f32 v98, v98\n"
        "v_mul_f32 v99, v99, v129\n"
        "v_add_f32 v152, v152, v115\n"
        "v_mfma_f32_16x16x32_bf16 v[230:233], v[148:151], v[106:109], v[230:233]\n"
        "v_exp_f32 v99, v99\n"
        "v_mfma_f32_16x16x32_bf16 v[252:255], v[176:179], v[140:143], 0\n"
        "v_mul_f32 v100, v100, v130\n"
        "v_add_f32 v152, v152, v98\n"
        "v_mul_f32 v101, v101, v131\n"
        "v_mfma_f32_16x16x32_bf16 v[252:255], v[236:239], v[166:169], v[252:255]\n"
        "v_exp_f32 v100, v100\n"
        "v_mfma_f32_16x16x32_bf16 v[252:255], v[244:247], v[206:209], v[252:255]\n"
        "s_setprio 2\n"
        "v_add_f32 v152, v152, v99\n"
        "v_mul_f32 v248, v248, v132\n"
        "v_mfma_f32_16x16x32_bf16 v[232:235], v[102:105], v[206:209], 0\n"
        "v_exp_f32 v101, v101\n"
        "v_add_f32 v152, v152, v100\n"
        "v_mfma_f32_16x16x32_bf16 v[232:235], v[180:183], v[166:169], v[232:235]\n"
        "v_exp_f32 v248, v248\n"
        "v_mfma_f32_16x16x32_bf16 v[232:235], v[154:157], v[140:143], v[232:235]\n"
        "v_mul_f32 v252, v252, v184\n"
        "v_mul_f32 v253, v253, v185\n"
        "v_mfma_f32_16x16x32_bf16 v[106:109], v[202:205], v[140:143], 0\n"
        "v_exp_f32 v252, v252\n"
        "v_mfma_f32_16x16x32_bf16 v[106:109], v[158:161], v[166:169], v[106:109]\n"
        "v_exp_f32 v253, v253\n"
        "v_mul_f32 v254, v254, v186\n"
        "v_mfma_f32_16x16x32_bf16 v[106:109], v[162:165], v[206:209], v[106:109]\n"
        "v_exp_f32 v254, v254\n"
        "v_mfma_f32_16x16x32_bf16 v[170:173], v[120:123], v[206:209], 0\n"
        "v_mul_f32 v255, v255, v187\n"
        "v_add_f32 v243, v252, v253\n"
        "v_mfma_f32_16x16x32_bf16 v[170:173], v[226:229], v[166:169], v[170:173]\n"
        "v_exp_f32 v255, v255\n"
        "v_mul_f32 v232, v232, v188\n"
        "v_mfma_f32_16x16x32_bf16 v[170:173], v[148:151], v[140:143], v[170:173]\n"
        "v_add_f32 v243, v243, v254\n"
        "v_mul_f32 v233, v233, v189\n"
        "v_mfma_f32_16x16x32_bf16 v[94:97], v[18:21], v[202:205], 0\n"
        "v_exp_f32 v232, v232\n"
        "v_mfma_f32_16x16x32_bf16 v[94:97], v[22:25], v[158:161], v[94:97]\n"
        "v_add_f32 v243, v243, v255\n"
        "v_exp_f32 v233, v233\n"
        "v_mfma_f32_16x16x32_bf16 v[94:97], v[26:29], v[162:165], v[94:97]\n"
        "v_mul_f32 v234, v234, v190\n"
        "v_add_f32 v243, v243, v232\n"
        "v_mfma_f32_16x16x32_bf16 v[124:127], v[38:41], v[162:165], 0\n"
        "v_exp_f32 v234, v234\n"
        "v_mfma_f32_16x16x32_bf16 v[124:127], v[34:37], v[158:161], v[124:127]\n"
        "v_mul_f32 v235, v235, v191\n"
        "v_add_f32 v243, v243, v233\n"
        "v_mul_f32 v106, v106, v192\n"
        "v_mfma_f32_16x16x32_bf16 v[124:127], v[30:33], v[202:205], v[124:127]\n"
        "v_cvt_pk_bf16_f32 v138, v94, v95\n"
        "v_cvt_pk_bf16_f32 v139, v96, v97\n"
        "v_mfma_f32_16x16x32_bf16 v[94:97], v[42:45], v[202:205], 0\n"
        "v_exp_f32 v235, v235\n"
        "v_mfma_f32_16x16x32_bf16 v[94:97], v[46:49], v[158:161], v[94:97]\n"
        "v_mul_f32 v249, v249, v133\n"
        "v_add_f32 v243, v243, v234\n"
        "v_mul_f32 v107, v107, v193\n"
        "v_mfma_f32_16x16x32_bf16 v[94:97], v[50:53], v[162:165], v[94:97]\n"
        "v_cvt_pk_bf16_f32 v140, v124, v125\n"
        "v_cvt_pk_bf16_f32 v141, v126, v127\n"
        "v_mfma_f32_16x16x32_bf16 v[124:127], v[62:65], v[162:165], 0\n"
        "v_exp_f32 v106, v106\n"
        "v_mfma_f32_16x16x32_bf16 v[124:127], v[58:61], v[158:161], v[124:127]\n"
        "v_add_f32 v152, v152, v101\n"
        "v_exp_f32 v249, v249\n"
        "v_mfma_f32_16x16x32_bf16 v[124:127], v[54:57], v[202:205], v[124:127]\n"
        "v_cvt_pk_bf16_f32 v142, v94, v95\n"
        "v_cvt_pk_bf16_f32 v143, v96, v97\n"
        "v_mfma_f32_16x16x32_bf16 v[94:97], v[66:69], v[202:205], 0\n"
        "v_mul_f32 v250, v250, v134\n"
        "v_add_f32 v243, v243, v235\n"
        "v_mfma_f32_16x16x32_bf16 v[94:97], v[70:73], v[158:161], v[94:97]\n"
        "v_exp_f32 v107, v107\n"
        "v_mul_f32 v108, v108, v194\n"
        "v_mfma_f32_16x16x32_bf16 v[94:97], v[74:77], v[162:165], v[94:97]\n"
        "v_cvt_pk_bf16_f32 v144, v124, v125\n"
        "v_cvt_pk_bf16_f32 v145, v126, v127\n"
        "v_mfma_f32_16x16x32_bf16 v[128:131], v[86:89], v[162:165], 0\n"
        "v_add_f32 v152, v152, v248\n"
        "v_mul_f32 v251, v251, v135\n"
        "v_mfma_f32_16x16x32_bf16 v[128:131], v[82:85], v[158:161], v[128:131]\n"
        "v_exp_f32 v250, v250\n"
        "v_mfma_f32_16x16x4_4b_bf16 v[210:225], v[110:111], v[118:119], 0 blgp:6\n"
        "v_cvt_pk_bf16_f32 v132, v94, v95\n"
        "v_cvt_pk_bf16_f32 v133, v96, v97\n"
        "v_add_f32 v243, v243, v106\n"
        "v_exp_f32 v108, v108\n"
        "v_mul_f32 v109, v109, v195\n"
        "v_add_f32 v152, v152, v249\n"
        "v_exp_f32 v251, v251\n"
        "v_mfma_f32_16x16x32_bf16 v[128:131], v[78:81], v[202:205], v[128:131]\n"
        "v_mul_f32 v230, v230, v136\n"
        "v_add_f32 v243, v243, v107\n"
        "v_mul_f32 v170, v170, v196\n"
        "v_mfma_f32_16x16x32_bf16 v[124:127], v[176:179], v[138:141], 0\n"
        "v_exp_f32 v109, v109\n"
        "v_mfma_f32_16x16x32_bf16 v[124:127], v[236:239], v[142:145], v[124:127]\n"
        "v_add_f32 v152, v152, v250\n"
        "v_exp_f32 v230, v230\n"
        "v_cvt_pk_bf16_f32 v134, v128, v129\n"
        "v_cvt_pk_bf16_f32 v135, v130, v131\n"
        "v_add_f32 v243, v243, v108\n"
        "v_exp_f32 v170, v170\n"
        "v_mfma_f32_16x16x32_bf16 v[124:127], v[244:247], v[132:135], v[124:127]\n"
        "v_add_f32 v152, v152, v251\n"
        "v_add_f32 v243, v243, v109\n"
        "v_mfma_f32_16x16x32_bf16 v[128:131], v[102:105], v[132:135], 0\n"
        "v_add_f32_dpp v152, v230, v152 quad_perm:[0,1,2,3] row_mask:0x1 bank_mask:0xf\n"
        "v_add_f32_dpp v243, v170, v243 quad_perm:[0,1,2,3] row_mask:0x1 bank_mask:0xf\n"
        "v_mfma_f32_16x16x32_bf16 v[128:131], v[180:183], v[142:145], v[128:131]\n"
        "v_mfma_f32_16x16x32_bf16 v[128:131], v[154:157], v[138:141], v[128:131]\n"
        "v_permlane32_swap_b32 v152, v243\n"
        "v_mfma_f32_16x16x32_bf16 v[116:119], v[202:205], v[138:141], 0\n"
        "ds_read_b128 v[206:209], %15\n"
        "v_add_f32 v152, v152, v243\n"
        "v_mfma_f32_16x16x32_bf16 v[116:119], v[158:161], v[142:145], v[116:119]\n"
        "ds_read_b128 v[240:243], %15 offset:64\n"
        "v_mov_b32 v231, v152\n"
        "v_mul_f32 v124, v124, v210\n"
        "v_mfma_f32_16x16x32_bf16 v[116:119], v[162:165], v[132:135], v[116:119]\n"
        "ds_read_b128 v[94:97], %15 offset:128\n"
        "v_mul_f32 v125, v125, v211\n"
        "v_mfma_f32_16x16x32_bf16 v[166:169], v[120:123], v[132:135], 0\n"
        "v_permlane16_swap_b32 v152, v231\n"
        "v_mfma_f32_16x16x32_bf16 v[166:169], v[226:229], v[142:145], v[166:169]\n"
        "v_add_f32 v152, v152, v231\n"
        "v_mul_f32 v126, v126, v212\n"
        "v_mfma_f32_16x16x32_bf16 v[166:169], v[148:151], v[138:141], v[166:169]\n"
        "s_waitcnt lgkmcnt(2)\n"
        "v_rcp_f32 v231, v152\n"
        "v_mul_f32 v127, v127, v213\n"
        "v_mfma_f32_16x16x32_bf16 v[210:213], v[206:209], v[176:179], 0\n"
        "s_waitcnt lgkmcnt(1)\n"
        "v_add_u32 v171, s30, %10\n"
        "v_and_b32 v172, s28, v231\n"
        "v_mfma_f32_16x16x32_bf16 v[210:213], v[240:243], v[236:239], v[210:213]\n"
        "s_waitcnt lgkmcnt(0)\n"
        "v_sub_f32 v225, v231, v172\n"
        "v_cvt_pk_bf16_f32 v173, v172, v225\n"
        "v_mfma_f32_16x16x32_bf16 v[210:213], v[94:97], v[244:247], v[210:213]\n"
        "v_exp_f32 v124, v124\n"
        "v_mfma_f32_16x16x4_4b_bf16 v[184:199], v[110:111], v[172:173], 0 blgp:4\n"
        "v_exp_f32 v125, v125\n"
        "v_exp_f32 v126, v126\n"
        "v_add_f32 v169, v124, v125\n"
        "v_exp_f32 v127, v127\n"
        "v_mul_f32 v128, v128, v214\n"
        "v_add_f32 v169, v169, v126\n"
        "v_mul_f32 v129, v129, v215\n"
        "v_mfma_f32_16x16x4_4b_bf16 v[132:147], v[110:111], v[172:173], 0 blgp:6\n"
        "v_exp_f32 v128, v128\n"
        "v_add_f32 v169, v169, v127\n"
        "v_exp_f32 v129, v129\n"
        "v_mul_f32 v130, v130, v216\n"
        "v_add_f32 v169, v169, v128\n"
        "v_exp_f32 v130, v130\n"
        "v_mul_f32 v131, v131, v217\n"
        "v_mfma_f32_16x16x32_bf16 v[198:201], v[94:97], v[102:105], 0\n"
        "v_add_f32 v169, v169, v129\n"
        "v_mul_f32 v116, v116, v218\n"
        "v_mfma_f32_16x16x32_bf16 v[198:201], v[240:243], v[180:183], v[198:201]\n"
        "v_exp_f32 v131, v131\n"
        "v_mfma_f32_16x16x32_bf16 v[198:201], v[206:209], v[154:157], v[198:201]\n"
        "v_add_f32 v169, v169, v130\n"
        "v_mul_f32 v117, v117, v219\n"
        "v_mfma_f32_16x16x32_bf16 v[216:219], v[206:209], v[202:205], 0\n"
        "v_exp_f32 v116, v116\n"
        "v_add_f32 v169, v169, v131\n"
        "v_mfma_f32_16x16x32_bf16 v[216:219], v[240:243], v[158:161], v[216:219]\n"
        "ds_read_b128 v[172:175], %0 offset:9216\n"
        "v_mul_f32 v118, v118, v220\n"
        "v_mfma_f32_16x16x32_bf16 v[216:219], v[94:97], v[162:165], v[216:219]\n"
        "ds_read_b128 v[212:215], %0 offset:10240\n"
        "v_cndmask_b32_e64 v168, v210, v198, s[50:51]\n"
        "v_mfma_f32_16x16x32_bf16 v[198:201], v[94:97], v[120:123], 0\n"
        "v_exp_f32 v117, v117\n"
        "v_mfma_f32_16x16x32_bf16 v[198:201], v[240:243], v[226:229], v[198:201]\n"
        "v_add_f32 v169, v169, v116\n"
        "v_mul_f32 v119, v119, v221\n"
        "v_mfma_f32_16x16x32_bf16 v[198:201], v[206:209], v[148:151], v[198:201]\n"
        "v_cndmask_b32_e64 v168, v168, v216, s[52:53]\n"
        "v_exp_f32 v118, v118\n"
        "v_mfma_f32_16x16x32_bf16 v[94:97], v[176:179], v[90:93], 0\n"
        "s_waitcnt lgkmcnt(1)\n"
        "v_mul_f32 v112, v112, v184\n"
        "v_mul_f32 v113, v113, v185\n"
        "v_mfma_f32_16x16x32_bf16 v[94:97], v[236:239], v[172:175], v[94:97]\n"
        "s_waitcnt lgkmcnt(0)\n"
        "v_cndmask_b32_e64 v168, v168, v198, s[54:55]\n"
        "v_mul_f32 v168, v168, v153\n"
        "v_mfma_f32_16x16x32_bf16 v[94:97], v[244:247], v[212:215], v[94:97]\n"
        "s_mov_b64 exec, s[56:57]\n"
        "v_mov_b32 v168, 0xff800000\n"
        "s_mov_b64 exec, -1\n"
        "v_mfma_f32_16x16x32_bf16 v[120:123], v[102:105], v[212:215], 0\n"
        "ds_read_b128 v[198:201], %0 offset:13312\n"
        "v_mul_f32 v168, s58, v168\n"
        "v_mul_f32 v114, v114, v186\n"
        "v_mfma_f32_16x16x32_bf16 v[120:123], v[180:183], v[172:175], v[120:123]\n"
        "ds_read_b128 v[224:227], %0 offset:12288\n"
        "v_cvt_pk_bf16_f32 v146, v94, v95\n"
        "v_mfma_f32_16x16x32_bf16 v[120:123], v[154:157], v[90:93], v[120:123]\n"
        "ds_read_b128 v[240:243], %0 offset:11264\n"
        "v_cvt_pk_bf16_f32 v147, v96, v97\n"
        "v_mfma_f32_16x16x32_bf16 v[94:97], v[202:205], v[90:93], 0\n"
        "v_exp_f32 v168, v168\n"
        "v_mfma_f32_16x16x32_bf16 v[94:97], v[158:161], v[172:175], v[94:97]\n"
        "v_mul_f32 v115, v115, v187\n"
        "v_mul_f32 v98, v98, v188\n"
        "v_mfma_f32_16x16x32_bf16 v[94:97], v[162:165], v[212:215], v[94:97]\n"
        "s_waitcnt lgkmcnt(2)\n"
        "v_cvt_pk_bf16_f32 v148, v120, v121\n"
        "v_cvt_pk_bf16_f32 v149, v122, v123\n"
        "v_mul_f32 v99, v99, v189\n"
        "v_mfma_f32_16x16x32_bf16 v[120:123], v[162:165], v[198:201], 0\n"
        "ds_read2_b32 v[208:209], v171 offset0:0 offset1:0\n"
        "s_waitcnt lgkmcnt(1)\n"
        "v_mfma_f32_16x16x32_bf16 v[120:123], v[158:161], v[224:227], v[120:123]\n"
        "v_cvt_pk_bf16_f32 v206, v94, v95\n"
        "v_cvt_pk_bf16_f32 v207, v96, v97\n"
        "v_mfma_f32_16x16x32_bf16 v[120:123], v[202:205], v[240:243], v[120:123]\n"
        "v_mul_f32 v100, v100, v190\n"
        "v_mul_f32 v101, v101, v191\n"
        "v_mfma_f32_16x16x32_bf16 v[188:191], v[154:157], v[240:243], 0\n"
        "ds_read_b128 v[210:213], %0 offset:14336\n"
        "v_add_f32_dpp v167, v168, v168 row_ror:8 row_mask:0xf bank_mask:0xf\n"
        "v_add_f32 v169, v169, v117\n"
        "v_mfma_f32_16x16x32_bf16 v[188:191], v[180:183], v[224:227], v[188:191]\n"
        "ds_read_b128 v[94:97], %0 offset:15360\n"
        "v_cvt_pk_bf16_f32 v218, v120, v121\n"
        "v_mfma_f32_16x16x32_bf16 v[188:191], v[102:105], v[198:201], v[188:191]\n"
        "ds_read_b128 v[184:187], %0 offset:16384\n"
        "v_cvt_pk_bf16_f32 v219, v122, v123\n"
        "v_mfma_f32_16x16x32_bf16 v[120:123], v[244:247], v[198:201], 0\n"
        "v_exp_f32 v119, v119\n"
        "v_mfma_f32_16x16x32_bf16 v[120:123], v[236:239], v[224:227], v[120:123]\n"
        "v_mul_f32 v166, v166, v222\n"
        "v_cvt_pk_bf16_f32 v172, v112, v113\n"
        "v_cvt_pk_bf16_f32 v173, v114, v115\n"
        "v_mfma_f32_16x16x32_bf16 v[120:123], v[176:179], v[240:243], v[120:123]\n"
        "s_waitcnt lgkmcnt(2)\n"
        "v_cvt_pk_bf16_f32 v200, v188, v189\n"
        "v_cvt_pk_bf16_f32 v201, v190, v191\n"
        "v_mfma_f32_16x16x32_bf16 v[112:115], v[176:179], v[210:213], 0\n"
        "ds_read2_b32 v[220:221], v171 offset0:1 offset1:1\n"
        "s_waitcnt lgkmcnt(1)\n"
        "v_mfma_f32_16x16x32_bf16 v[112:115], v[236:239], v[94:97], v[112:115]\n"
        "v_cvt_pk_bf16_f32 v174, v98, v99\n"
        "v_cvt_pk_bf16_f32 v198, v120, v121\n"
        "v_mfma_f32_16x16x32_bf16 v[112:115], v[244:247], v[184:187], v[112:115]\n"
        "v_cvt_pk_bf16_f32 v199, v122, v123\n"
        "v_cvt_pk_bf16_f32 v175, v100, v101\n"
        "v_add_f32_dpp v167, v167, v167 row_ror:4 row_mask:0xf bank_mask:0xf\n"
        "v_mfma_f32_16x16x32_bf16 v[188:191], v[102:105], v[184:187], 0\n"
        "ds_read_b128 v[240:243], %0 offset:19456\n"
        "v_add_f32 v169, v169, v118\n"
        "v_mfma_f32_16x16x32_bf16 v[188:191], v[180:183], v[94:97], v[188:191]\n"
        "ds_read_b128 v[98:101], %0 offset:18432\n"
        "v_cvt_pk_bf16_f32 v214, v112, v113\n"
        "v_mfma_f32_16x16x32_bf16 v[188:191], v[154:157], v[210:213], v[188:191]\n"
        "ds_read_b128 v[120:123], %0 offset:17408\n"
        "v_cvt_pk_bf16_f32 v215, v114, v115\n"
        "v_mfma_f32_16x16x32_bf16 v[112:115], v[202:205], v[210:213], 0\n"
        "v_exp_f32 v166, v166\n"
        "v_mul_f32 v248, v248, v192\n"
        "v_mfma_f32_16x16x32_bf16 v[112:115], v[158:161], v[94:97], v[112:115]\n"
        "v_mul_f32 v249, v249, v193\n"
        "v_mul_f32 v250, v250, v194\n"
        "v_mfma_f32_16x16x32_bf16 v[112:115], v[162:165], v[184:187], v[112:115]\n"
        "s_waitcnt lgkmcnt(2)\n"
        "v_cvt_pk_bf16_f32 v216, v188, v189\n"
        "v_cvt_pk_bf16_f32 v217, v190, v191\n"
        "v_mfma_f32_16x16x32_bf16 v[184:187], v[162:165], v[240:243], 0\n"
        "ds_read2_b32 v[96:97], v171 offset0:2 offset1:2\n"
        "s_waitcnt lgkmcnt(1)\n"
        "v_mfma_f32_16x16x32_bf16 v[184:187], v[158:161], v[98:101], v[184:187]\n"
        "v_mul_f32 v251, v251, v195\n"
        "v_cvt_pk_bf16_f32 v94, v112, v113\n"
        "v_cvt_pk_bf16_f32 v95, v114, v115\n"
        "v_mfma_f32_16x16x32_bf16 v[184:187], v[202:205], v[120:123], v[184:187]\n"
        "v_mul_f32 v230, v230, v196\n"
        "v_add_f32_dpp v167, v167, v167 row_ror:2 row_mask:0xf bank_mask:0xf\n"
        "v_mfma_f32_16x16x32_bf16 v[188:191], v[154:157], v[120:123], 0\n"
        "ds_read_b128 v[194:197], %0 offset:20480\n"
        "v_add_f32 v169, v169, v119\n"
        "v_mfma_f32_16x16x32_bf16 v[188:191], v[180:183], v[98:101], v[188:191]\n"
        "ds_read_b128 v[222:225], %0 offset:21504\n"
        "v_cvt_pk_bf16_f32 v210, v248, v249\n"
        "v_mfma_f32_16x16x32_bf16 v[188:191], v[102:105], v[240:243], v[188:191]\n"
        "ds_read_b128 v[226:229], %0 offset:22528\n"
        "v_cvt_pk_bf16_f32 v248, v184, v185\n"
        "v_cvt_pk_bf16_f32 v249, v186, v187\n"
        "v_mfma_f32_16x16x32_bf16 v[184:187], v[244:247], v[240:243], 0\n"
        "v_cvt_pk_bf16_f32 v211, v250, v251\n"
        "v_cvt_pk_bf16_f32 v212, v230, 0\n"
        "v_mfma_f32_16x16x32_bf16 v[184:187], v[236:239], v[98:101], v[184:187]\n"
        "v_add_f32_dpp v167, v167, v167 row_ror:1 row_mask:0xf bank_mask:0xf\n"
        "v_cvt_pk_bf16_f32 v114, v188, v189\n"
        "v_mfma_f32_16x16x32_bf16 v[184:187], v[176:179], v[120:123], v[184:187]\n"
        "s_waitcnt lgkmcnt(2)\n"
        "v_cvt_pk_bf16_f32 v115, v190, v191\n"
        "v_add_f32_dpp v169, v166, v169 quad_perm:[0,1,2,3] row_mask:0x1 bank_mask:0xf\n"
        "v_mfma_f32_16x16x32_bf16 v[240:243], v[176:179], v[194:197], 0\n"
        "ds_read2_b32 v[250:251], v171 offset0:3 offset1:3\n"
        "v_mul_f32 v168, v168, v153\n"
        "s_waitcnt lgkmcnt(1)\n"
        "v_mfma_f32_16x16x32_bf16 v[240:243], v[236:239], v[222:225], v[240:243]\n"
        "v_cvt_pk_bf16_f32 v112, v184, v185\n"
        "v_cvt_pk_bf16_f32 v113, v186, v187\n"
        "v_mfma_f32_16x16x32_bf16 v[240:243], v[244:247], v[226:229], v[240:243]\n"
        "v_permlane32_swap_b32 v169, v167\n"
        "v_mfma_f32_16x16x32_bf16 v[98:101], v[102:105], v[226:229], 0\n"
        "ds_read_b128 v[120:123], %0 offset:25600\n"
        "v_add_f32 v169, v169, v167\n"
        "v_mov_b32 v230, v169\n"
        "v_mfma_f32_16x16x32_bf16 v[98:101], v[180:183], v[222:225], v[98:101]\n"
        "ds_read_b128 v[184:187], %0 offset:24576\n"
        "v_min3_f32 v167, v152, v231, v153\n"
        "v_mfma_f32_16x16x32_bf16 v[98:101], v[154:157], v[194:197], v[98:101]\n"
        "ds_read_b128 v[150:153], %0 offset:23552\n"
        "v_cvt_pk_bf16_f32 v192, v240, v241\n"
        "v_mfma_f32_16x16x32_bf16 v[188:191], v[202:205], v[194:197], 0\n"
        "v_cvt_pk_bf16_f32 v193, v242, v243\n"
        "v_mul_f32 v252, v252, v132\n"
        "v_mfma_f32_16x16x32_bf16 v[188:191], v[158:161], v[222:225], v[188:191]\n"
        "ds_read_b64 v[196:197], %7\n"
        "v_permlane16_swap_b32 v169, v230\n"
        "v_mfma_f32_16x16x32_bf16 v[188:191], v[162:165], v[226:229], v[188:191]\n"
        "s_waitcnt lgkmcnt(3)\n"
        "v_cvt_pk_bf16_f32 v194, v98, v99\n"
        "v_cvt_pk_bf16_f32 v195, v100, v101\n"
        "v_mfma_f32_16x16x32_bf16 v[98:101], v[162:165], v[120:123], 0\n"
        "ds_read2_b32 v[224:225], v171 offset0:4 offset1:4\n"
        "ds_read_b64 v[162:163], %7 offset:16\n"
        "s_waitcnt lgkmcnt(3)\n"
        "v_mfma_f32_16x16x32_bf16 v[98:101], v[158:161], v[184:187], v[98:101]\n"
        "v_cvt_pk_bf16_f32 v222, v188, v189\n"
        "v_cvt_pk_bf16_f32 v223, v190, v191\n"
        "v_add_f32 v169, v169, v230\n"
        "v_mfma_f32_16x16x32_bf16 v[98:101], v[202:205], v[150:153], v[98:101]\n"
        "ds_read_b64 v[158:159], %7 offset:32\n"
        "v_mul_f32 v253, v253, v133\n"
        "v_mfma_f32_16x16x32_bf16 v[188:191], v[154:157], v[150:153], 0\n"
        "ds_read_b64 v[202:203], %1\n"
        "v_mul_f32 v254, v254, v134\n"
        "v_mfma_f32_16x16x32_bf16 v[188:191], v[180:183], v[184:187], v[188:191]\n"
        "ds_read_b64 v[132:133], %7 offset:48\n"
        "v_mul_f32 v255, v255, v135\n"
        "v_mfma_f32_16x16x32_bf16 v[188:191], v[102:105], v[120:123], v[188:191]\n"
        "v_cvt_pk_bf16_f32 v226, v98, v99\n"
        "v_cvt_pk_bf16_f32 v227, v100, v101\n"
        "v_mul_f32 v232, v232, v136\n"
        "v_mfma_f32_16x16x32_bf16 v[98:101], v[244:247], v[120:123], 0\n"
        "ds_read_b64 v[102:103], %7 offset:64\n"
        "v_mul_f32 v233, v233, v137\n"
        "v_mfma_f32_16x16x32_bf16 v[98:101], v[236:239], v[184:187], v[98:101]\n"
        "v_rcp_f32 v247, v169\n"
        "v_mfma_f32_16x16x32_bf16 v[98:101], v[176:179], v[150:153], v[98:101]\n"
        "ds_read_b64 v[104:105], %7 offset:80\n"
        "v_mov_b32 v213, 0\n"
        "s_waitcnt lgkmcnt(2)\n"
        "v_mfma_f32_4x4x4_16b_bf16 v[120:123], v[196:197], v[202:203], 0\n"
        "ds_read2_b32 v[228:229], v171 offset0:5 offset1:5\n"
        "v_mfma_f32_4x4x4_16b_bf16 v[134:137], v[162:163], v[202:203], 0\n"
        "v_mfma_f32_4x4x4_16b_bf16 v[150:153], v[158:159], v[202:203], 0\n"
        "v_mfma_f32_4x4x4_16b_bf16 v[154:157], v[132:133], v[202:203], 0\n"
        "s_waitcnt lgkmcnt(2)\n"
        "v_mfma_f32_4x4x4_16b_bf16 v[158:161], v[102:103], v[202:203], 0\n"
        "s_waitcnt lgkmcnt(1)\n"
        "v_mfma_f32_4x4x4_16b_bf16 v[162:165], v[104:105], v[202:203], 0\n"
        "v_cvt_pk_bf16_f32 v240, v98, v99\n"
        "v_cvt_pk_bf16_f32 v241, v100, v101\n"
        "v_cvt_pk_bf16_f32 v242, v188, v189\n"
        "v_mfma_f32_16x16x32_bf16 v[120:123], v[172:175], v[146:149], v[120:123]\n"
        "v_cvt_pk_bf16_f32 v243, v190, v191\n"
        "v_readlane_b32 s59, v247, 32\n"
        "v_min3_f32 v167, v167, v169, v247\n"
        "v_mfma_f32_16x16x32_bf16 v[120:123], v[210:213], v[206:209], v[120:123]\n"
        "v_mul_f32 v168, s59, v168\n"
        "v_cvt_pk_bf16_f32 v168, v168, 0\n"
        "v_mfma_f32_16x16x32_bf16 v[134:137], v[210:213], v[218:221], v[134:137]\n"
        "v_cmp_nle_f32 vcc, 0x2b800000, v167\n"
        "v_mul_f32 v234, v234, v138\n"
        "v_mfma_f32_16x16x32_bf16 v[134:137], v[172:175], v[198:201], v[134:137]\n"
        "s_andn2_b64 vcc, vcc, s[56:57]\n"
        "v_mul_f32 v235, v235, v139\n"
        "v_cvt_pk_bf16_f32 v102, v252, v253\n"
        "v_mfma_f32_16x16x32_bf16 v[150:153], v[172:175], v[214:217], v[150:153]\n"
        "s_cbranch_scc1 CREDOs_%=\n"
        "v_cvt_pk_bf16_f32 v103, v254, v255\n"
        "v_cvt_pk_bf16_f32 v104, v232, v233\n"
        "v_cvt_pk_bf16_f32 v105, v234, v235\n"
        "v_mfma_f32_16x16x32_bf16 v[150:153], v[210:213], v[94:97], v[150:153]\n"
        "v_cvt_pk_bf16_f32 v132, v120, v134\n"
        "v_cvt_pk_bf16_f32 v138, v121, v135\n"
        "v_mfma_f32_16x16x32_bf16 v[154:157], v[210:213], v[248:251], v[154:157]\n"
        "ds_write_b16 %3, v168\n"
        "ds_read_b128 v[176:179], %4\n"
        "v_mfma_f32_16x16x32_bf16 v[154:157], v[172:175], v[112:115], v[154:157]\n"
        "ds_read_b128 v[180:183], %4 offset:64\n"
        "v_cvt_pk_bf16_f32 v184, v122, v136\n"
        "v_mfma_f32_16x16x32_bf16 v[158:161], v[172:175], v[192:195], v[158:161]\n"
        "v_cvt_pk_bf16_f32 v188, v123, v137\n"
        "v_mul_f32 v106, v106, v140\n"
        "v_mfma_f32_16x16x32_bf16 v[158:161], v[210:213], v[222:225], v[158:161]\n"
        "ds_read_b64 v[120:121], %7 offset:3328\n"
        "v_mul_f32 v107, v107, v141\n"
        "s_waitcnt lgkmcnt(4)\n"
        "v_mfma_f32_16x16x32_bf16 v[162:165], v[210:213], v[226:229], v[162:165]\n"
        "v_cvt_pk_bf16_f32 v133, v150, v154\n"
        "v_cvt_pk_bf16_f32 v139, v151, v155\n"
        "v_mfma_f32_16x16x32_bf16 v[162:165], v[172:175], v[240:243], v[162:165]\n"
        "ds_read_b64 v[136:137], %7 offset:3344\n"
        "s_waitcnt lgkmcnt(2)\n"
        "v_mfma_f32_16x16x32_bf16 v[98:101], v[176:179], v[146:149], 0\n"
        "v_cvt_pk_bf16_f32 v185, v152, v156\n"
        "v_cvt_pk_bf16_f32 v189, v153, v157\n"
        "v_mfma_f32_16x16x32_bf16 v[98:101], v[180:183], v[206:209], v[98:101]\n"
        "ds_read_b64 v[168:169], %7 offset:3360\n"
        "v_mul_f32 v108, v108, v142\n"
        "v_cvt_pk_bf16_f32 v134, v158, v162\n"
        "v_mfma_f32_16x16x32_bf16 v[150:153], v[180:183], v[218:221], 0\n"
        "v_cvt_pk_bf16_f32 v140, v159, v163\n"
        "v_cvt_pk_bf16_f32 v186, v160, v164\n"
        "v_mfma_f32_16x16x32_bf16 v[150:153], v[176:179], v[198:201], v[150:153]\n"
        "ds_read_b64 v[172:173], %7 offset:3376\n"
        "v_cvt_pk_bf16_f32 v190, v161, v165\n"
        "v_mfma_f32_16x16x32_bf16 v[210:213], v[176:179], v[214:217], 0\n"
        "s_add_u32 s31, s31, 1\n"
        "v_mul_f32 v109, v109, v143\n"
        "v_mul_f32 v170, v170, v144\n"
        "v_cvt_pk_bf16_f32 v230, v106, v107\n"
        "v_mfma_f32_16x16x32_bf16 v[210:213], v[180:183], v[94:97], v[210:213]\n"
        "ds_read_b64 v[144:145], %7 offset:3392\n"
        "v_cvt_pk_bf16_f32 v231, v108, v109\n"
        "v_cvt_pk_bf16_f32 v232, v170, 0\n"
        "v_mfma_f32_16x16x32_bf16 v[106:109], v[180:183], v[248:251], 0\n"
        "s_add_u32 s37, s35, 0\n"
        "v_cvt_pk_bf16_f32 v234, v98, v150\n"
        "v_and_b32 v122, s28, v247\n"
        "v_sub_f32 v255, v247, v122\n"
        "v_mfma_f32_16x16x32_bf16 v[106:109], v[176:179], v[112:115], v[106:109]\n"
        "ds_read_b64 v[170:171], %7 offset:3408\n"
        "s_mul_i32 s68, s31, 3\n"
        "v_cvt_pk_bf16_f32 v123, v122, v255\n"
        "s_waitcnt lgkmcnt(2)\n"
        "v_mfma_f32_4x4x4_16b_bf16 v[98:101], v[120:121], v[202:203], 0\n"
        "buffer_store_dwordx3 v[132:134], %11, %22, s37 offen sc1 nt\n"
        "v_mfma_f32_16x16x4_4b_bf16 v[150:165], v[110:111], v[122:123], 0 blgp:4\n"
        "s_lshr_b32 s38, s31, 3\n"
        "v_cvt_pk_bf16_f32 v235, v210, v106\n"
        "v_add_u32 %5, s69, %5\n"
        "v_xor_b32 %8, %2, %8\n"
        "s_add_u32 s37, s35, 0\n"
        "s_add_u32 s38, s38, 11\n"
        "v_mfma_f32_4x4x4_16b_bf16 v[106:109], v[136:137], v[202:203], 0\n"
        "v_mfma_f32_4x4x4_16b_bf16 v[120:123], v[168:169], v[202:203], 0\n"
        "buffer_store_dwordx3 v[138:140], %14, %22, s37 offen offset:768 sc1 nt\n"
        "v_mfma_f32_4x4x4_16b_bf16 v[132:135], v[172:173], v[202:203], 0\n"
        "s_waitcnt lgkmcnt(0)\n"
        "v_mfma_f32_4x4x4_16b_bf16 v[136:139], v[144:145], v[202:203], 0\n"
        "v_mfma_f32_4x4x4_16b_bf16 v[140:143], v[170:171], v[202:203], 0\n"
        "s_bitcmp1_b32 s31, 0\n"
        "s_cselect_b32 s38, 3, s38\n"
        "s_add_u32 s37, s35, 98112\n"
        "s_add_u32 s68, s68, s38\n"
        "s_cmp_lt_u32 s31, %24\n"
        "v_mov_b32 v233, 0\n"
        "v_mfma_f32_16x16x32_bf16 v[168:171], v[176:179], v[192:195], 0\n"
        "s_cselect_b32 s68, s68, 0\n"
        "buffer_store_dwordx3 v[184:186], %11, %22, s37 offen offset:192 sc1 nt\n"
        "s_add_u32 s37, s35, 98112\n"
        "v_mul_f32 v124, v124, v150\n"
        "v_mfma_f32_16x16x32_bf16 v[168:171], v[180:183], v[222:225], v[168:171]\n"
        "ds_read_b64 v[110:111], %7 offset:6656\n"
        "s_and_b32 s38, s31, 7\n"
        "v_mul_f32 v125, v125, v151\n"
        "v_mul_f32 v126, v126, v152\n"
        "v_mul_f32 v127, v127, v153\n"
        "v_mfma_f32_16x16x32_bf16 v[150:153], v[180:183], v[226:229], 0\n"
        "s_mul_i32 s29, s38, 192\n"
        "buffer_store_dwordx3 v[188:190], %13, %22, s37 offen offset:960 sc1 nt\n"
        "v_mul_f32 v128, v128, v154\n"
        "v_mfma_f32_16x16x32_bf16 v[150:153], v[176:179], v[240:243], v[150:153]\n"
        "ds_read_b64 v[144:145], %7 offset:6672\n"
        "s_mul_i32 s30, s38, 384\n"
        "v_mfma_f32_16x16x32_bf16 v[98:101], v[102:105], v[146:149], v[98:101]\n"
        "s_add_u32 s34, s34, 832\n"
        "v_mul_f32 v129, v129, v155\n"
        "v_mul_f32 v130, v130, v156\n"
        "v_mfma_f32_16x16x32_bf16 v[98:101], v[230:233], v[206:209], v[98:101]\n"
        "ds_read_b64 v[154:155], %7 offset:6688\n"
        "s_cmp_eq_u32 s34, 8320\n"
        "v_mfma_f32_16x16x32_bf16 v[106:109], v[230:233], v[218:221], v[106:109]\n"
        "s_cselect_b32 s34, 0, s34\n"
        "v_cvt_pk_bf16_f32 v236, v168, v150\n"
        "v_mul_f32 v131, v131, v157\n"
        "v_mfma_f32_16x16x32_bf16 v[106:109], v[102:105], v[198:201], v[106:109]\n"
        "ds_read_b64 v[150:151], %7 offset:6704\n"
        "s_add_u32 s73, s34, %19\n"
        "v_mfma_f32_16x16x32_bf16 v[120:123], v[102:105], v[214:217], v[120:123]\n"
        "s_mul_i32 s38, s31, 1344\n"
        "v_cvt_pk_bf16_f32 v210, v124, v125\n"
        "v_cvt_pk_bf16_f32 v211, v126, v127\n"
        "v_mfma_f32_16x16x32_bf16 v[120:123], v[230:233], v[94:97], v[120:123]\n"
        "ds_read_b64 v[152:153], %7 offset:6720\n"
        "s_cmp_ge_u32 s31, s71\n"
        "v_mfma_f32_16x16x32_bf16 v[132:135], v[230:233], v[248:251], v[132:135]\n"
        "s_cselect_b32 s39, s70, s72\n"
        "v_cvt_pk_bf16_f32 v212, v128, v129\n"
        "v_cvt_pk_bf16_f32 v213, v130, v131\n"
        "v_mul_f32 v116, v116, v158\n"
        "v_mfma_f32_16x16x32_bf16 v[132:135], v[102:105], v[112:115], v[132:135]\n"
        "ds_read_b64 v[156:157], %7 offset:6736\n"
        "s_add_u32 s37, s35, 588672\n"
        "s_waitcnt lgkmcnt(2)\n"
        "v_mfma_f32_4x4x4_16b_bf16 v[124:127], v[110:111], v[202:203], 0\n"
        "v_mfma_f32_4x4x4_16b_bf16 v[128:131], v[144:145], v[202:203], 0\n"
        "v_mfma_f32_4x4x4_16b_bf16 v[168:171], v[154:155], v[202:203], 0\n"
        "v_mfma_f32_4x4x4_16b_bf16 v[172:175], v[150:151], v[202:203], 0\n"
        "s_waitcnt lgkmcnt(1)\n"
        "v_mfma_f32_4x4x4_16b_bf16 v[184:187], v[152:153], v[202:203], 0\n"
        "s_waitcnt lgkmcnt(0)\n"
        "v_mfma_f32_4x4x4_16b_bf16 v[188:191], v[156:157], v[202:203], 0\n"
        "s_add_u32 s46, s37, 1344\n"
        "s_add_u32 s39, s39, s38\n"
        "v_xor_b32 v255, 4, %17\n"
        "v_mul_f32 v117, v117, v159\n"
        "v_mul_f32 v118, v118, v160\n"
        "v_mul_f32 v119, v119, v161\n"
        "v_mfma_f32_16x16x32_bf16 v[136:139], v[102:105], v[192:195], v[136:139]\n"
        "s_mov_b64 exec, 1\n"
        "ds_inc_u32 %17, %1 offset:8\n"
        "s_mov_b64 exec, -1\n"
        "v_mul_f32 v166, v166, v162\n"
        "v_mfma_f32_16x16x32_bf16 v[136:139], v[230:233], v[222:225], v[136:139]\n"
        "buffer_store_dwordx3 v[234:236], %11, s[44:47], s37 offen offset:1152 sc1 nt\n"
        "v_cvt_pk_bf16_f32 v150, v116, v117\n"
        "v_mfma_f32_16x16x32_bf16 v[140:143], v[230:233], v[226:229], v[140:143]\n"
        "ds_read_b32 v254, v255\n"
        "v_cvt_pk_bf16_f32 v151, v118, v119\n"
        "v_mfma_f32_16x16x32_bf16 v[140:143], v[102:105], v[240:243], v[140:143]\n"
        "v_cvt_pk_bf16_f32 v152, v166, 0\n"
        "v_mov_b32 v153, 0\n"
        "v_mfma_f32_16x16x32_bf16 v[124:127], v[210:213], v[146:149], v[124:127]\n"
        "v_add_u32 %7, s69, %7\n"
        "v_xor_b32 %17, 4, %17\n"
        "v_mfma_f32_16x16x32_bf16 v[124:127], v[150:153], v[206:209], v[124:127]\n"
        "v_mfma_f32_16x16x32_bf16 v[128:131], v[150:153], v[218:221], v[128:131]\n"
        "v_mfma_f32_16x16x32_bf16 v[128:131], v[210:213], v[198:201], v[128:131]\n"
        "s_waitcnt lgkmcnt(0)\n"
        "v_readfirstlane_b32 s40, v254\n"
        "v_mfma_f32_16x16x32_bf16 v[168:171], v[210:213], v[214:217], v[168:171]\n"
        "s_cmp_lt_i32 s40, s68\n"
        "v_mfma_f32_16x16x32_bf16 v[168:171], v[150:153], v[94:97], v[168:171]\n"
        "s_cbranch_scc1 L14_%=\n"
        "L15_%=:\n"
        "ds_read_b128 v[176:179], %5\n"
        "ds_read_b128 v[154:157], %5 offset:3328\n"
        "v_mfma_f32_16x16x32_bf16 v[172:175], v[150:153], v[248:251], v[172:175]\n"
        "ds_read_b128 v[236:239], %5 offset:64\n"
        "ds_read_b128 v[180:183], %5 offset:3392\n"
        "ds_read_b128 v[244:247], %5 offset:128\n"
        "v_mfma_f32_16x16x32_bf16 v[172:175], v[210:213], v[112:115], v[172:175]\n"
        "ds_read_b128 v[102:105], %5 offset:3456\n"
        "v_mfma_f32_16x16x32_bf16 v[184:187], v[210:213], v[192:195], v[184:187]\n"
        "s_sub_u32 s69, 0, s69\n"
        "v_mfma_f32_16x16x32_bf16 v[184:187], v[150:153], v[222:225], v[184:187]\n"
        "s_mov_b32 s36, s35\n"
        "v_mfma_f32_16x16x32_bf16 v[188:191], v[150:153], v[226:229], v[188:191]\n"
        "s_mov_b32 s35, s39\n"
        "v_mfma_f32_16x16x32_bf16 v[188:191], v[210:213], v[240:243], v[188:191]\n"
        "s_setprio 0\n"
        "s_cmp_ge_u32 s31, %24\n"
        "s_cbranch_scc1 CEND_%=\n"
        "s_cmp_lg_u32 s29, 0\n"
        "s_cbranch_scc1 CTOP_%=\n"
        "s_branch CFULL_%=\n"
        "CEND_%=:\n"
        "s_waitcnt lgkmcnt(0)\n"
        "s_nop 15\n"
        "v_cvt_pk_bf16_f32 v18, v98, v106\n"
        "v_cvt_pk_bf16_f32 v19, v120, v132\n"
        "v_cvt_pk_bf16_f32 v20, v136, v140\n"
        "s_add_u32 s37, s36, 196224\n"
        "buffer_store_dwordx3 v[18:20], %11, %22, s37 offen offset:384 sc1 nt\n"
        "v_cvt_pk_bf16_f32 v22, v99, v107\n"
        "v_cvt_pk_bf16_f32 v23, v121, v133\n"
        "v_cvt_pk_bf16_f32 v24, v137, v141\n"
        "s_add_u32 s37, s36, 196224\n"
        "buffer_store_dwordx3 v[22:24], %12, %22, s37 offen offset:1152 sc1 nt\n"
        "v_cvt_pk_bf16_f32 v18, v100, v108\n"
        "v_cvt_pk_bf16_f32 v19, v122, v134\n"
        "v_cvt_pk_bf16_f32 v20, v138, v142\n"
        "s_add_u32 s37, s36, 294336\n"
        "buffer_store_dwordx3 v[18:20], %11, %22, s37 offen offset:576 sc1 nt\n"
        "v_cvt_pk_bf16_f32 v22, v101, v109\n"
        "v_cvt_pk_bf16_f32 v23, v123, v135\n"
        "v_cvt_pk_bf16_f32 v24, v139, v143\n"
        "s_add_u32 s37, s36, 392448\n"
        "buffer_store_dwordx3 v[22:24], %11, %22, s37 offen sc1 nt\n"
        "v_cvt_pk_bf16_f32 v18, v124, v128\n"
        "v_cvt_pk_bf16_f32 v19, v168, v172\n"
        "v_cvt_pk_bf16_f32 v20, v184, v188\n"
        "s_add_u32 s37, s36, 392448\n"
        "buffer_store_dwordx3 v[18:20], %14, %22, s37 offen offset:768 sc1 nt\n"
        "v_cvt_pk_bf16_f32 v22, v125, v129\n"
        "v_cvt_pk_bf16_f32 v23, v169, v173\n"
        "v_cvt_pk_bf16_f32 v24, v185, v189\n"
        "s_add_u32 s37, s36, 490560\n"
        "buffer_store_dwordx3 v[22:24], %11, %22, s37 offen offset:192 sc1 nt\n"
        "v_cvt_pk_bf16_f32 v18, v126, v130\n"
        "v_cvt_pk_bf16_f32 v19, v170, v174\n"
        "v_cvt_pk_bf16_f32 v20, v186, v190\n"
        "s_add_u32 s37, s36, 490560\n"
        "buffer_store_dwordx3 v[18:20], %13, %22, s37 offen offset:960 sc1 nt\n"
        "s_add_u32 s37, s36, 588672\n"
        "v_cvt_pk_bf16_f32 v22, v127, v131\n"
        "v_cvt_pk_bf16_f32 v23, v171, v175\n"
        "v_cvt_pk_bf16_f32 v24, v187, v191\n"
        "buffer_store_dwordx3 v[22:24], %11, %22, s37 offen offset:384 sc1 nt\n"
        "s_nop 1\n"
        "s_endpgm\n"
        "L14_%=:\n"
        "s_nop 7\n"
        "s_nop 7\n"
        "L12_%=:\n"
        "ds_read_b32 v254, v255 offset:0\n"
        "s_waitcnt lgkmcnt(0)\n"
        "v_readfirstlane_b32 s40, v254\n"
        "s_nop 3\n"
        "s_cmp_ge_i32 s40, s68\n"
        "s_cbranch_scc1 L13_%=\n"
        "s_sleep 1\n"
        "s_sub_u32 s41, s41, 1\n"
        "s_cbranch_scc0 L12_%=\n"
        "s_endpgm\n"
        "L13_%=:\n"
        "s_nop 1\n"
        "s_branch L15_%=\n"
        "L18_%=:\n"
        "s_nop 7\n"
        "s_nop 7\n"
        "L16_%=:\n"
        "ds_read_b32 v254, v255 offset:0\n"
        "s_waitcnt lgkmcnt(0)\n"
        "v_readfirstlane_b32 s40, v254\n"
        "s_nop 3\n"
        "s_cmp_ge_i32 s40, s68\n"
        "s_cbranch_scc1 L17_%=\n"
        "s_sleep 1\n"
        "s_sub_u32 s41, s41, 1\n"
        "s_cbranch_scc0 L16_%=\n"
        "s_endpgm\n"
        "L17_%=:\n"
        "s_nop 1\n"
        "s_branch L19_%=\n"
        "L22_%=:\n"
        "s_nop 7\n"
        "s_nop 7\n"
        "L20_%=:\n"
        "ds_read_b32 v254, v255 offset:0\n"
        "s_waitcnt lgkmcnt(0)\n"
        "v_readfirstlane_b32 s40, v254\n"
        "s_nop 3\n"
        "s_cmp_ge_i32 s40, s68\n"
        "s_cbranch_scc1 L21_%=\n"
        "s_sleep 1\n"
        "s_sub_u32 s41, s41, 1\n"
        "s_cbranch_scc0 L20_%=\n"
        "s_endpgm\n"
        "L21_%=:\n"
        "s_nop 1\n"
        "s_branch L23_%=\n"
        "L26_%=:\n"
        "s_nop 7\n"
        "s_nop 7\n"
        "L24_%=:\n"
        "ds_read_b32 v254, v255 offset:0\n"
        "s_waitcnt lgkmcnt(0)\n"
        "v_readfirstlane_b32 s40, v254\n"
        "s_nop 3\n"
        "s_cmp_ge_i32 s40, s68\n"
        "s_cbranch_scc1 L25_%=\n"
        "s_sleep 1\n"
        "s_sub_u32 s41, s41, 1\n"
        "s_cbranch_scc0 L24_%=\n"
        "s_endpgm\n"
        "L25_%=:\n"
        "s_nop 1\n"
        "s_branch L27_%=\n"
        "CREDOs_%=:\n"
        "s_waitcnt lgkmcnt(0)\n"
        "s_nop 15\n"
        "s_mov_b32 s36, 0x20000000\n"
        "ds_read_b128 v[176:179], %5 offset:0\n"
        "ds_read_b128 v[154:157], %5 offset:3328\n"
        "ds_read_b128 v[236:239], %5 offset:64\n"
        "ds_read_b128 v[180:183], %5 offset:3392\n"
        "ds_read_b128 v[244:247], %5 offset:128\n"
        "ds_read_b128 v[102:105], %5 offset:3456\n"
        "s_waitcnt lgkmcnt(0)\n"
        "s_branch MTOP_%=\n"
        "CREDOf_%=:\n"
        "s_waitcnt lgkmcnt(0)\n"
        "s_nop 15\n"
        "s_mov_b32 s36, 0x20000000\n"
        "ds_read_b128 v[176:179], %5 offset:0\n"
        "ds_read_b128 v[154:157], %5 offset:3328\n"
        "ds_read_b128 v[236:239], %5 offset:64\n"
        "ds_read_b128 v[180:183], %5 offset:3392\n"
        "ds_read_b128 v[244:247], %5 offset:128\n"
        "ds_read_b128 v[102:105], %5 offset:3456\n"
        "s_waitcnt lgkmcnt(0)\n"
        "s_branch MFULL_%=\n"
        "MFULL_%=:\n"
        "s_waitcnt lgkmcnt(0)\n"
        "s_nop 7\n"
        "v_mfma_f32_16x16x32_bf16 v[94:97], v[18:21], v[176:179], 0\n"
        "s_sub_u32 s60, s73, 111040\n"
        "v_cvt_pk_bf16_f32 v110, v98, v106\n"
        "v_cvt_pk_bf16_f32 v111, v120, v132\n"
        "v_cvt_pk_bf16_f32 v112, v136, v140\n"
        "v_cvt_pk_bf16_f32 v144, v99, v107\n"
        "v_cvt_pk_bf16_f32 v145, v121, v133\n"
        "v_mfma_f32_16x16x32_bf16 v[94:97], v[22:25], v[236:239], v[94:97]\n"
        "s_add_u32 s37, s36, 196224\n"
        "v_cvt_pk_bf16_f32 v146, v137, v141\n"
        "v_cvt_pk_bf16_f32 v132, v100, v108\n"
        "v_cvt_pk_bf16_f32 v133, v122, v134\n"
        "v_cvt_pk_bf16_f32 v134, v138, v142\n"
        "v_cvt_pk_bf16_f32 v136, v101, v109\n"
        "v_mfma_f32_16x16x32_bf16 v[94:97], v[26:29], v[244:247], v[94:97]\n"
        "v_cvt_pk_bf16_f32 v137, v123, v135\n"
        "v_cvt_pk_bf16_f32 v138, v139, v143\n"
        "v_cvt_pk_bf16_f32 v140, v124, v128\n"
        "v_cvt_pk_bf16_f32 v141, v168, v172\n"
        "v_cvt_pk_bf16_f32 v142, v184, v188\n"
        "v_mfma_f32_16x16x32_bf16 v[98:101], v[26:29], v[102:105], 0\n"
        "v_cvt_pk_bf16_f32 v148, v125, v129\n"
        "v_cvt_pk_bf16_f32 v149, v169, v173\n"
        "v_cvt_pk_bf16_f32 v116, v94, v95\n"
        "v_cvt_pk_bf16_f32 v117, v96, v97\n"
        "v_cvt_pk_bf16_f32 v150, v185, v189\n"
        "v_mfma_f32_16x16x32_bf16 v[98:101], v[22:25], v[180:183], v[98:101]\n"
        "buffer_store_dwordx3 v[110:112], %11, %22, s37 offen offset:384 sc1 nt\n"
        "v_cvt_pk_bf16_f32 v158, v126, v130\n"
        "v_mfma_f32_16x16x32_bf16 v[98:101], v[18:21], v[154:157], v[98:101]\n"
        "v_cvt_pk_bf16_f32 v159, v170, v174\n"
        "v_cvt_pk_bf16_f32 v160, v186, v190\n"
        "v_cvt_pk_bf16_f32 v188, v127, v131\n"
        "v_cvt_pk_bf16_f32 v189, v171, v175\n"
        "v_cvt_pk_bf16_f32 v190, v187, v191\n"
        "v_mfma_f32_16x16x32_bf16 v[94:97], v[30:33], v[154:157], 0\n"
        "s_add_u32 s37, s36, 196224\n"
        "v_mfma_f32_16x16x32_bf16 v[94:97], v[34:37], v[180:183], v[94:97]\n"
        "v_cvt_pk_bf16_f32 v120, v98, v99\n"
        "v_cvt_pk_bf16_f32 v121, v100, v101\n"
        "v_mfma_f32_16x16x32_bf16 v[94:97], v[38:41], v[102:105], v[94:97]\n"
        "v_mfma_f32_16x16x32_bf16 v[98:101], v[38:41], v[244:247], 0\n"
        "buffer_store_dwordx3 v[144:146], %12, %22, s37 offen offset:1152 sc1 nt\n"
        "v_mfma_f32_16x16x32_bf16 v[98:101], v[34:37], v[236:239], v[98:101]\n"
        "ds_read_u16 v255, %0 offset:29696\n"
        "s_mov_b64 exec, s[62:63]\n"
        "v_add_u32 %15, s29, %9\n"
        "v_add_u32 %6, s34, %16\n"
        "s_mov_b64 exec, -1\n"
        "v_mfma_f32_16x16x32_bf16 v[98:101], v[30:33], v[176:179], v[98:101]\n"
        "s_add_u32 s37, s36, 294336\n"
        "v_cvt_pk_bf16_f32 v122, v94, v95\n"
        "v_cvt_pk_bf16_f32 v123, v96, v97\n"
        "v_mfma_f32_16x16x32_bf16 v[106:109], v[42:45], v[176:179], 0\n"
        "v_mfma_f32_16x16x32_bf16 v[106:109], v[46:49], v[236:239], v[106:109]\n"
        "v_mfma_f32_16x16x32_bf16 v[106:109], v[50:53], v[244:247], v[106:109]\n"
        "buffer_store_dwordx3 v[132:134], %11, %22, s37 offen offset:576 sc1 nt\n"
        "v_mfma_f32_16x16x32_bf16 v[94:97], v[50:53], v[102:105], 0\n"
        "v_cvt_pk_bf16_f32 v118, v98, v99\n"
        "v_cvt_pk_bf16_f32 v119, v100, v101\n"
        "s_waitcnt lgkmcnt(0)\n"
        "v_add_u32 v255, s60, v255\n"
        "v_subrev_u32 v254, 8320, v255\n"
        "v_mfma_f32_16x16x32_bf16 v[94:97], v[46:49], v[180:183], v[94:97]\n"
        "s_add_u32 s37, s36, 392448\n"
        "v_writelane_b32 %8, s73, 48\n"
        "v_cvt_pk_bf16_f32 v98, v106, v107\n"
        "v_cvt_pk_bf16_f32 v99, v108, v109\n"
        "v_min_u32 v255, v255, v254\n"
        "v_add_u32 v255, 110848, v255\n"
        "v_mfma_f32_16x16x32_bf16 v[94:97], v[42:45], v[154:157], v[94:97]\n"
        "v_mfma_f32_16x16x32_bf16 v[106:109], v[54:57], v[154:157], 0\n"
        "v_mfma_f32_16x16x32_bf16 v[106:109], v[58:61], v[180:183], v[106:109]\n"
        "buffer_store_dwordx3 v[136:138], %11, %22, s37 offen sc1 nt\n"
        "v_mfma_f32_16x16x32_bf16 v[106:109], v[62:65], v[102:105], v[106:109]\n"
        "ds_read_b32 v253, %8\n"
        "v_xor_b32 %8, %2, %8\n"
        "v_mfma_f32_16x16x32_bf16 v[110:113], v[62:65], v[244:247], 0\n"
        "s_add_u32 s37, s36, 392448\n"
        "v_cvt_pk_bf16_f32 v124, v94, v95\n"
        "v_cvt_pk_bf16_f32 v125, v96, v97\n"
        "v_mfma_f32_16x16x32_bf16 v[110:113], v[58:61], v[236:239], v[110:113]\n"
        "v_mfma_f32_16x16x32_bf16 v[110:113], v[54:57], v[176:179], v[110:113]\n"
        "v_cvt_pk_bf16_f32 v126, v106, v107\n"
        "v_cvt_pk_bf16_f32 v127, v108, v109\n"
        "v_mfma_f32_16x16x32_bf16 v[94:97], v[66:69], v[176:179], 0\n"
        "buffer_store_dwordx3 v[140:142], %14, %22, s37 offen offset:768 sc1 nt\n"
        "v_mfma_f32_16x16x32_bf16 v[94:97], v[70:73], v[236:239], v[94:97]\n"
        "s_waitcnt lgkmcnt(0)\n"
        "v_readlane_b32 s58, v253, 48\n"
        "v_mfma_f32_16x16x32_bf16 v[94:97], v[74:77], v[244:247], v[94:97]\n"
        "s_add_u32 s37, s36, 490560\n"
        "v_cvt_pk_bf16_f32 v100, v110, v111\n"
        "v_cvt_pk_bf16_f32 v101, v112, v113\n"
        "v_mfma_f32_16x16x32_bf16 v[106:109], v[74:77], v[102:105], 0\n"
        "v_mfma_f32_16x16x32_bf16 v[106:109], v[70:73], v[180:183], v[106:109]\n"
        "v_mfma_f32_16x16x32_bf16 v[106:109], v[66:69], v[154:157], v[106:109]\n"
        "buffer_store_dwordx3 v[148:150], %11, %22, s37 offen offset:192 sc1 nt\n"
        "v_mfma_f32_16x16x32_bf16 v[110:113], v[78:81], v[154:157], 0\n"
        "ds_read_b32 v254, %0 offset:29704\n"
        "v_cvt_pk_bf16_f32 v128, v94, v95\n"
        "v_cvt_pk_bf16_f32 v129, v96, v97\n"
        "v_mfma_f32_16x16x32_bf16 v[110:113], v[82:85], v[180:183], v[110:113]\n"
        "ds_read_b32 v252, %0 offset:29708\n"
        "s_add_u32 s37, s36, 490560\n"
        "v_cvt_pk_bf16_f32 v94, v106, v107\n"
        "v_cvt_pk_bf16_f32 v95, v108, v109\n"
        "v_mfma_f32_16x16x32_bf16 v[110:113], v[86:89], v[102:105], v[110:113]\n"
        "v_mfma_f32_16x16x32_bf16 v[106:109], v[86:89], v[244:247], 0\n"
        "v_mfma_f32_16x16x32_bf16 v[106:109], v[82:85], v[236:239], v[106:109]\n"
        "buffer_store_dwordx3 v[158:160], %13, %22, s37 offen offset:960 sc1 nt\n"
        "v_mfma_f32_16x16x32_bf16 v[106:109], v[78:81], v[176:179], v[106:109]\n"
        "s_waitcnt lgkmcnt(0)\n"
        "v_add_u32 v254, %7, v254\n"
        "v_add_u32 v252, %7, v252\n"
        "v_mfma_f32_16x16x32_bf16 v[132:135], v[176:179], v[116:119], 0\n"
        "ds_read_b128 v[136:139], %5 offset:6656\n"
        "v_cvt_pk_bf16_f32 v96, v110, v111\n"
        "v_cvt_pk_bf16_f32 v97, v112, v113\n"
        "s_add_u32 s37, s36, 588672\n"
        "v_cvt_pk_bf16_f32 v130, v106, v107\n"
        "v_mfma_f32_16x16x32_bf16 v[132:135], v[236:239], v[98:101], v[132:135]\n"
        "ds_read_b128 v[140:143], %5 offset:6720\n"
        "v_cvt_pk_bf16_f32 v131, v108, v109\n"
        "ds_read_b128 v[144:147], %5 offset:6784\n"
        "v_add_u32 %5, s69, %5\n"
        "v_mfma_f32_16x16x32_bf16 v[132:135], v[244:247], v[128:131], v[132:135]\n"
        "v_mfma_f32_16x16x32_bf16 v[148:151], v[102:105], v[128:131], 0\n"
        "ds_read_b128 v[158:161], v255 offset:128\n"
        "v_mfma_f32_16x16x32_bf16 v[148:151], v[180:183], v[98:101], v[148:151]\n"
        "ds_read_b128 v[162:165], v255 offset:64\n"
        "ds_read_b32 v106, v254\n"
        "v_mfma_f32_16x16x32_bf16 v[148:151], v[154:157], v[116:119], v[148:151]\n"
        "ds_read_b128 v[166:169], v255\n"
        "ds_read_b32 v107, v254 offset:832\n"
        "s_waitcnt lgkmcnt(5)\n"
        "v_mfma_f32_16x16x32_bf16 v[170:173], v[136:139], v[116:119], 0\n"
        "ds_read_b32 v108, v254 offset:1664\n"
        "ds_read_b32 v109, v254 offset:2496\n"
        "v_mfma_f32_16x16x32_bf16 v[170:173], v[140:143], v[98:101], v[170:173]\n"
        "ds_read_b32 v110, v254 offset:3328\n"
        "ds_read_b32 v111, v254 offset:4160\n"
        "s_waitcnt lgkmcnt(6)\n"
        "v_mul_f32 v132, v132, v106\n"
        "v_mfma_f32_16x16x32_bf16 v[170:173], v[144:147], v[128:131], v[170:173]\n"
        "ds_read_b32 v112, v254 offset:4992\n"
        "s_waitcnt lgkmcnt(5)\n"
        "v_mul_f32 v133, v133, v107\n"
        "v_mfma_f32_16x16x32_bf16 v[184:187], v[158:161], v[128:131], 0\n"
        "ds_read_b32 v113, v254 offset:5824\n"
        "s_waitcnt lgkmcnt(5)\n"
        "v_mul_f32 v134, v134, v108\n"
        "s_waitcnt lgkmcnt(4)\n"
        "v_mul_f32 v135, v135, v109\n"
        "v_mfma_f32_16x16x32_bf16 v[184:187], v[162:165], v[98:101], v[184:187]\n"
        "ds_read_b32 v114, v254 offset:6656\n"
        "s_waitcnt lgkmcnt(3)\n"
        "v_mul_f32 v148, v148, v110\n"
        "v_max3_f32 v251, v132, v133, v134\n"
        "v_mul_f32 v149, v149, v111\n"
        "v_mfma_f32_16x16x32_bf16 v[184:187], v[166:169], v[116:119], v[184:187]\n"
        "ds_read_b32 v115, v254 offset:7488\n"
        "s_waitcnt lgkmcnt(3)\n"
        "v_mul_f32 v150, v150, v112\n"
        "v_max3_f32 v251, v251, v135, v148\n"
        "v_max3_f32 v251, v251, v149, v150\n"
        "v_mfma_f32_16x16x32_bf16 v[98:101], v[176:179], v[120:123], 0\n"
        "ds_read_b32 v116, v254 offset:8320\n"
        "s_waitcnt lgkmcnt(3)\n"
        "v_mul_f32 v151, v151, v113\n"
        "s_waitcnt lgkmcnt(2)\n"
        "v_mul_f32 v170, v170, v114\n"
        "v_mfma_f32_16x16x32_bf16 v[98:101], v[236:239], v[124:127], v[98:101]\n"
        "ds_read_b32 v117, v254 offset:9152\n"
        "v_mul_f32 v184, s58, v184\n"
        "v_max3_f32 v251, v251, v151, v170\n"
        "s_waitcnt lgkmcnt(2)\n"
        "v_mul_f32 v171, v171, v115\n"
        "v_mfma_f32_16x16x32_bf16 v[98:101], v[244:247], v[94:97], v[98:101]\n"
        "s_setprio 2\n"
        "buffer_store_dwordx3 v[188:190], %11, %22, s37 offen offset:384 sc1 nt\n"
        "v_mfma_f32_16x16x32_bf16 v[128:131], v[102:105], v[94:97], 0\n"
        "ds_read_b32 v254, v252\n"
        "s_waitcnt lgkmcnt(2)\n"
        "v_mul_f32 v172, v172, v116\n"
        "v_max3_f32 v251, v251, v171, v172\n"
        "v_mfma_f32_16x16x32_bf16 v[128:131], v[180:183], v[124:127], v[128:131]\n"
        "ds_read_b32 v250, v252 offset:3328\n"
        "v_mul_f32 v98, v98, v106\n"
        "v_mul_f32 v99, v99, v107\n"
        "v_mul_f32 v100, v100, v108\n"
        "v_mul_f32 v101, v101, v109\n"
        "v_mfma_f32_16x16x32_bf16 v[128:131], v[154:157], v[120:123], v[128:131]\n"
        "ds_read_b32 v249, v252 offset:6656\n"
        "v_max3_f32 v252, v98, v99, v100\n"
        "s_waitcnt lgkmcnt(3)\n"
        "v_mul_f32 v173, v173, v117\n"
        "v_mfma_f32_16x16x32_bf16 v[186:189], v[136:139], v[120:123], 0\n"
        "s_mov_b64 exec, s[48:49]\n"
        "v_mov_b32 v184, 0xff800000\n"
        "s_mov_b64 exec, -1\n"
        "v_mul_f32 v128, v128, v110\n"
        "v_mul_f32 v129, v129, v111\n"
        "v_mul_f32 v130, v130, v112\n"
        "v_mfma_f32_16x16x32_bf16 v[186:189], v[140:143], v[124:127], v[186:189]\n"
        "v_max3_f32 v252, v252, v101, v128\n"
        "v_mul_f32 v131, v131, v113\n"
        "v_max3_f32 v252, v252, v129, v130\n"
        "v_max3_f32 v251, v251, v173, v184\n"
        "v_mfma_f32_16x16x32_bf16 v[186:189], v[144:147], v[94:97], v[186:189]\n"
        "ds_read_u16 v248, %0 offset:29700\n"
        "v_mfma_f32_16x16x32_bf16 v[190:193], v[158:161], v[94:97], 0\n"
        "v_mfma_f32_16x16x32_bf16 v[190:193], v[162:165], v[124:127], v[190:193]\n"
        "v_mfma_f32_16x16x32_bf16 v[190:193], v[166:169], v[120:123], v[190:193]\n"
        "v_mfma_f32_16x16x32_bf16 v[94:97], v[18:21], v[136:139], 0\n"
        "v_mfma_f32_16x16x32_bf16 v[94:97], v[22:25], v[140:143], v[94:97]\n"
        "s_waitcnt lgkmcnt(0)\n"
        "v_add_u32 v248, %9, v248\n"
        "v_mul_f32 v186, v186, v114\n"
        "v_mul_f32 v187, v187, v115\n"
        "v_mul_f32 v188, v188, v116\n"
        "v_max3_f32 v252, v252, v131, v186\n"
        "v_mfma_f32_16x16x32_bf16 v[94:97], v[26:29], v[144:147], v[94:97]\n"
        "v_mul_f32 v190, s58, v190\n"
        "v_mul_f32 v189, v189, v117\n"
        "v_max3_f32 v252, v252, v187, v188\n"
        "v_mfma_f32_16x16x32_bf16 v[118:121], v[26:29], v[158:161], 0\n"
        "s_mov_b64 exec, s[48:49]\n"
        "v_mov_b32 v190, 0xff800000\n"
        "s_mov_b64 exec, -1\n"
        "v_max3_f32 v252, v252, v189, v190\n"
        "v_cvt_pk_bf16_f32 v122, v94, v95\n"
        "v_cvt_pk_bf16_f32 v123, v96, v97\n"
        "v_mfma_f32_16x16x32_bf16 v[118:121], v[22:25], v[162:165], v[118:121]\n"
        "v_permlane32_swap_b32 v251, v252\n"
        "v_max_f32 v251, v251, v252\n"
        "v_mov_b32 v255, v251\n"
        "v_mfma_f32_16x16x32_bf16 v[118:121], v[18:21], v[166:169], v[118:121]\n"
        "v_mfma_f32_16x16x32_bf16 v[94:97], v[30:33], v[166:169], 0\n"
        "v_permlane16_swap_b32 v251, v255\n"
        "v_max_f32 v251, v251, v255\n"
        "v_mov_b32 v252, v251\n"
        "v_mfma_f32_16x16x32_bf16 v[94:97], v[34:37], v[162:165], v[94:97]\n"
        "v_mfma_f32_16x16x32_bf16 v[94:97], v[38:41], v[158:161], v[94:97]\n"
        "v_permlane32_swap_b32 v251, v252\n"
        "v_mul_f32 v251, v251, v254\n"
        "v_cvt_pk_bf16_f32 v192, v118, v119\n"
        "v_cvt_pk_bf16_f32 v193, v120, v121\n"
        "v_mfma_f32_16x16x32_bf16 v[118:121], v[38:41], v[144:147], 0\n"
        "v_mul_f32 v252, v252, v250\n"
        "v_fma_f32 v132, v132, v254, -v251\n"
        "v_fma_f32 v133, v133, v254, -v251\n"
        "v_cvt_pk_bf16_f32 v194, v94, v95\n"
        "v_cvt_pk_bf16_f32 v195, v96, v97\n"
        "v_mfma_f32_16x16x32_bf16 v[118:121], v[34:37], v[140:143], v[118:121]\n"
        "v_fma_f32 v98, v98, v250, -v252\n"
        "v_fma_f32 v99, v99, v250, -v252\n"
        "v_exp_f32 v132, v132\n"
        "v_fma_f32 v134, v134, v254, -v251\n"
        "v_mfma_f32_16x16x32_bf16 v[118:121], v[30:33], v[136:139], v[118:121]\n"
        "v_exp_f32 v133, v133\n"
        "v_exp_f32 v98, v98\n"
        "v_fma_f32 v100, v100, v250, -v252\n"
        "v_mfma_f32_16x16x32_bf16 v[94:97], v[42:45], v[136:139], 0\n"
        "v_exp_f32 v99, v99\n"
        "v_exp_f32 v134, v134\n"
        "v_fma_f32 v135, v135, v254, -v251\n"
        "v_mfma_f32_16x16x32_bf16 v[94:97], v[46:49], v[140:143], v[94:97]\n"
        "v_cvt_pk_bf16_f32 v124, v118, v119\n"
        "v_cvt_pk_bf16_f32 v125, v120, v121\n"
        "v_exp_f32 v100, v100\n"
        "v_fma_f32 v101, v101, v250, -v252\n"
        "v_mfma_f32_16x16x32_bf16 v[94:97], v[50:53], v[144:147], v[94:97]\n"
        "v_add_f32 v243, v132, v133\n"
        "v_exp_f32 v135, v135\n"
        "v_fma_f32 v148, v148, v254, -v251\n"
        "v_add_f32 v242, v98, v99\n"
        "v_mfma_f32_16x16x32_bf16 v[118:121], v[50:53], v[158:161], 0\n"
        "v_exp_f32 v101, v101\n"
        "v_fma_f32 v128, v128, v250, -v252\n"
        "v_add_f32 v243, v243, v134\n"
        "v_cvt_pk_bf16_f32 v196, v94, v95\n"
        "v_mfma_f32_16x16x32_bf16 v[118:121], v[46:49], v[162:165], v[118:121]\n"
        "v_cvt_pk_bf16_f32 v197, v96, v97\n"
        "v_exp_f32 v148, v148\n"
        "v_fma_f32 v149, v149, v254, -v251\n"
        "v_add_f32 v242, v242, v100\n"
        "v_mfma_f32_16x16x32_bf16 v[118:121], v[42:45], v[166:169], v[118:121]\n"
        "v_exp_f32 v128, v128\n"
        "v_fma_f32 v129, v129, v250, -v252\n"
        "v_add_f32 v243, v243, v135\n"
        "v_fma_f32 v150, v150, v254, -v251\n"
        "v_mfma_f32_16x16x32_bf16 v[94:97], v[54:57], v[166:169], 0\n"
        "ds_write_b128 v248, v[192:195]\n"
        "v_mfma_f32_16x16x32_bf16 v[94:97], v[58:61], v[162:165], v[94:97]\n"
        "v_exp_f32 v149, v149\n"
        "v_cvt_pk_bf16_f32 v192, v118, v119\n"
        "v_cvt_pk_bf16_f32 v193, v120, v121\n"
        "v_add_f32 v242, v242, v101\n"
        "v_mfma_f32_16x16x32_bf16 v[94:97], v[62:65], v[158:161], v[94:97]\n"
        "v_exp_f32 v129, v129\n"
        "v_fma_f32 v130, v130, v250, -v252\n"
        "v_add_f32 v243, v243, v148\n"
        "v_fma_f32 v151, v151, v254, -v251\n"
        "v_mfma_f32_16x16x32_bf16 v[118:121], v[62:65], v[144:147], 0\n"
        "v_exp_f32 v150, v150\n"
        "v_add_f32 v242, v242, v128\n"
        "v_exp_f32 v130, v130\n"
        "v_mfma_f32_16x16x32_bf16 v[118:121], v[58:61], v[140:143], v[118:121]\n"
        "v_cvt_pk_bf16_f32 v194, v94, v95\n"
        "v_cvt_pk_bf16_f32 v195, v96, v97\n"
        "v_fma_f32 v131, v131, v250, -v252\n"
        "v_add_f32 v243, v243, v149\n"
        "v_fma_f32 v170, v170, v254, -v251\n"
        "v_mfma_f32_16x16x32_bf16 v[118:121], v[54:57], v[136:139], v[118:121]\n"
        "v_exp_f32 v151, v151\n"
        "v_add_f32 v242, v242, v129\n"
        "v_exp_f32 v131, v131\n"
        "v_mfma_f32_16x16x32_bf16 v[94:97], v[66:69], v[136:139], 0\n"
        "v_fma_f32 v186, v186, v250, -v252\n"
        "v_add_f32 v243, v243, v150\n"
        "v_exp_f32 v170, v170\n"
        "v_fma_f32 v171, v171, v254, -v251\n"
        "v_mfma_f32_16x16x32_bf16 v[94:97], v[70:73], v[140:143], v[94:97]\n"
        "v_cvt_pk_bf16_f32 v198, v118, v119\n"
        "v_cvt_pk_bf16_f32 v199, v120, v121\n"
        "v_add_f32 v242, v242, v130\n"
        "v_exp_f32 v186, v186\n"
        "v_mfma_f32_16x16x32_bf16 v[94:97], v[74:77], v[144:147], v[94:97]\n"
        "v_fma_f32 v187, v187, v250, -v252\n"
        "v_add_f32 v243, v243, v151\n"
        "v_exp_f32 v171, v171\n"
        "v_fma_f32 v172, v172, v254, -v251\n"
        "v_mfma_f32_16x16x32_bf16 v[118:121], v[74:77], v[158:161], 0\n"
        "v_add_f32 v242, v242, v131\n"
        "v_exp_f32 v187, v187\n"
        "v_fma_f32 v188, v188, v250, -v252\n"
        "v_cvt_pk_bf16_f32 v200, v94, v95\n"
        "v_mfma_f32_16x16x32_bf16 v[118:121], v[70:73], v[162:165], v[118:121]\n"
        "v_cvt_pk_bf16_f32 v201, v96, v97\n"
        "v_add_f32 v243, v243, v170\n"
        "v_exp_f32 v172, v172\n"
        "v_fma_f32 v173, v173, v254, -v251\n"
        "v_mfma_f32_16x16x32_bf16 v[118:121], v[66:69], v[166:169], v[118:121]\n"
        "v_add_f32 v242, v242, v186\n"
        "v_exp_f32 v188, v188\n"
        "v_fma_f32 v189, v189, v250, -v252\n"
        "v_add_f32 v243, v243, v171\n"
        "v_mfma_f32_16x16x32_bf16 v[94:97], v[78:81], v[166:169], 0\n"
        "ds_write_b128 v248, v[192:195] offset:64\n"
        "v_mfma_f32_16x16x32_bf16 v[94:97], v[82:85], v[162:165], v[94:97]\n"
        "v_add_u32 v255, s30, %10\n"
        "v_cvt_pk_bf16_f32 v192, v118, v119\n"
        "v_cvt_pk_bf16_f32 v193, v120, v121\n"
        "v_exp_f32 v173, v173\n"
        "v_mfma_f32_16x16x32_bf16 v[94:97], v[86:89], v[158:161], v[94:97]\n"
        "v_fma_f32 v184, v184, v254, -v251\n"
        "v_add_f32 v242, v242, v187\n"
        "v_exp_f32 v189, v189\n"
        "v_fma_f32 v190, v190, v250, -v252\n"
        "v_mfma_f32_16x16x32_bf16 v[118:121], v[86:89], v[144:147], 0\n"
        "v_add_f32 v243, v243, v172\n"
        "v_exp_f32 v184, v184\n"
        "v_add_f32 v242, v242, v188\n"
        "v_cvt_pk_bf16_f32 v194, v94, v95\n"
        "v_mfma_f32_16x16x32_bf16 v[118:121], v[82:85], v[140:143], v[118:121]\n"
        "v_cvt_pk_bf16_f32 v195, v96, v97\n"
        "v_exp_f32 v190, v190\n"
        "v_add_f32 v243, v243, v173\n"
        "v_add_f32 v242, v242, v189\n"
        "v_mfma_f32_16x16x32_bf16 v[118:121], v[78:81], v[136:139], v[118:121]\n"
        "v_add_f32 v243, v243, v184\n"
        "v_add_f32 v242, v242, v190\n"
        "v_mul_f32 v132, v132, v106\n"
        "v_mul_f32 v133, v133, v107\n"
        "v_mul_f32 v134, v134, v108\n"
        "v_mfma_f32_16x16x32_bf16 v[204:207], v[176:179], v[122:125], 0\n"
        "v_permlane32_swap_b32 v243, v242\n"
        "v_add_f32 v243, v243, v242\n"
        "v_cvt_pk_bf16_f32 v202, v118, v119\n"
        "v_cvt_pk_bf16_f32 v203, v120, v121\n"
        "v_mfma_f32_16x16x32_bf16 v[204:207], v[236:239], v[196:199], v[204:207]\n"
        "v_mov_b32 v254, v243\n"
        "v_mul_f32 v135, v135, v109\n"
        "v_mul_f32 v148, v148, v110\n"
        "v_permlane16_swap_b32 v243, v254\n"
        "v_mfma_f32_16x16x32_bf16 v[204:207], v[244:247], v[200:203], v[204:207]\n"
        "v_add_f32 v243, v243, v254\n"
        "v_rcp_f32 v243, v243\n"
        "v_mul_f32 v149, v149, v111\n"
        "v_mov_b32 v242, v243\n"
        "v_mfma_f32_16x16x32_bf16 v[118:121], v[102:105], v[200:203], 0\n"
        "v_mul_f32 v150, v150, v112\n"
        "v_permlane32_swap_b32 v243, v242\n"
        "v_mul_f32 v151, v151, v113\n"
        "v_mul_f32 v204, v204, v106\n"
        "v_mfma_f32_16x16x32_bf16 v[118:121], v[180:183], v[196:199], v[118:121]\n"
        "ds_read_u16 v252, %0 offset:29698\n"
        "v_mul_f32 v205, v205, v107\n"
        "v_mul_f32 v206, v206, v108\n"
        "v_mul_f32 v207, v207, v109\n"
        "v_max3_f32 v251, v204, v205, v206\n"
        "v_mfma_f32_16x16x32_bf16 v[118:121], v[154:157], v[122:125], v[118:121]\n"
        "ds_write_b128 v248, v[192:195] offset:128\n"
        "v_mfma_f32_16x16x32_bf16 v[192:195], v[136:139], v[122:125], 0\n"
        "ds_read_b128 v[94:97], %15\n"
        "v_mul_f32 v132, v132, v243\n"
        "s_waitcnt lgkmcnt(2)\n"
        "v_add_u32 v252, s60, v252\n"
        "v_subrev_u32 v254, 8320, v252\n"
        "v_mfma_f32_16x16x32_bf16 v[192:195], v[140:143], v[196:199], v[192:195]\n"
        "ds_read_b128 v[208:211], %15 offset:64\n"
        "v_min_u32 v252, v252, v254\n"
        "v_add_u32 v252, 110848, v252\n"
        "v_mul_f32 v118, v118, v110\n"
        "v_mul_f32 v119, v119, v111\n"
        "v_mfma_f32_16x16x32_bf16 v[192:195], v[144:147], v[200:203], v[192:195]\n"
        "ds_read_b128 v[212:215], %15 offset:128\n"
        "v_mul_f32 v120, v120, v112\n"
        "v_max3_f32 v251, v251, v207, v118\n"
        "v_mul_f32 v121, v121, v113\n"
        "v_max3_f32 v251, v251, v119, v120\n"
        "v_mfma_f32_16x16x32_bf16 v[216:219], v[158:161], v[200:203], 0\n"
        "v_mul_f32 v133, v133, v243\n"
        "v_mul_f32 v134, v134, v243\n"
        "v_mul_f32 v192, v192, v114\n"
        "v_mul_f32 v193, v193, v115\n"
        "v_mul_f32 v194, v194, v116\n"
        "v_mfma_f32_16x16x32_bf16 v[216:219], v[162:165], v[196:199], v[216:219]\n"
        "v_max3_f32 v251, v251, v121, v192\n"
        "v_mul_f32 v195, v195, v117\n"
        "v_max3_f32 v251, v251, v193, v194\n"
        "v_mul_f32 v135, v135, v243\n"
        "v_mul_f32 v148, v148, v243\n"
        "v_mfma_f32_16x16x32_bf16 v[216:219], v[166:169], v[122:125], v[216:219]\n"
        "s_waitcnt lgkmcnt(0)\n"
        "v_mul_f32 v149, v149, v243\n"
        "v_mul_f32 v150, v150, v243\n"
        "v_mul_f32 v151, v151, v243\n"
        "v_cvt_pk_bf16_f32 v122, v132, v133\n"
        "v_cvt_pk_bf16_f32 v123, v134, v135\n"
        "v_mfma_f32_16x16x32_bf16 v[132:135], v[94:97], v[176:179], 0\n"
        "v_cvt_pk_bf16_f32 v124, v148, v149\n"
        "v_mul_f32 v216, s58, v216\n"
        "v_cvt_pk_bf16_f32 v125, v150, v151\n"
        "v_mul_f32 v170, v170, v114\n"
        "v_mul_f32 v171, v171, v115\n"
        "v_mfma_f32_16x16x32_bf16 v[132:135], v[208:211], v[236:239], v[132:135]\n"
        "s_mov_b64 exec, s[48:49]\n"
        "v_mov_b32 v216, 0xff800000\n"
        "s_mov_b64 exec, -1\n"
        "v_max3_f32 v251, v251, v195, v216\n"
        "v_mul_f32 v172, v172, v116\n"
        "v_mul_f32 v173, v173, v117\n"
        "v_mfma_f32_16x16x32_bf16 v[132:135], v[212:215], v[244:247], v[132:135]\n"
        "ds_read_u16 v254, %0 offset:29702\n"
        "v_mul_f32 v184, s58, v184\n"
        "v_mul_f32 v170, v170, v243\n"
        "v_mul_f32 v171, v171, v243\n"
        "v_mul_f32 v172, v172, v243\n"
        "v_mfma_f32_16x16x32_bf16 v[148:151], v[212:215], v[102:105], 0\n"
        "v_mul_f32 v173, v173, v243\n"
        "v_mul_f32 v184, v184, v243\n"
        "v_cvt_pk_bf16_f32 v196, v170, v171\n"
        "v_cvt_pk_bf16_f32 v197, v172, v173\n"
        "v_cvt_pk_bf16_f32 v198, v184, 0\n"
        "v_mfma_f32_16x16x32_bf16 v[148:151], v[208:211], v[180:183], v[148:151]\n"
        "v_mul_f32 v98, v98, v106\n"
        "v_mul_f32 v99, v99, v107\n"
        "v_mul_f32 v100, v100, v108\n"
        "s_waitcnt lgkmcnt(0)\n"
        "v_add_u32 v254, %10, v254\n"
        "v_mfma_f32_16x16x32_bf16 v[148:151], v[94:97], v[154:157], v[148:151]\n"
        "ds_read_b64 v[134:135], v252\n"
        "v_mul_f32 v101, v101, v109\n"
        "v_mul_f32 v128, v128, v110\n"
        "v_mul_f32 v129, v129, v111\n"
        "v_mul_f32 v130, v130, v112\n"
        "v_mfma_f32_16x16x32_bf16 v[170:173], v[94:97], v[136:139], 0\n"
        "ds_read_b64 v[234:235], %1\n"
        "v_mul_f32 v131, v131, v113\n"
        "v_cndmask_b32_e64 v250, v132, v148, s[50:51]\n"
        "v_mul_f32 v98, v98, v242\n"
        "v_mul_f32 v99, v99, v242\n"
        "v_mfma_f32_16x16x32_bf16 v[170:173], v[208:211], v[140:143], v[170:173]\n"
        "ds_read_b64 v[152:153], v252 offset:16\n"
        "v_mul_f32 v100, v100, v242\n"
        "v_mul_f32 v101, v101, v242\n"
        "v_mul_f32 v128, v128, v242\n"
        "v_mul_f32 v129, v129, v242\n"
        "v_mfma_f32_16x16x32_bf16 v[170:173], v[212:215], v[144:147], v[170:173]\n"
        "v_mul_f32 v130, v130, v242\n"
        "v_mul_f32 v131, v131, v242\n"
        "v_cvt_pk_bf16_f32 v148, v98, v99\n"
        "v_cvt_pk_bf16_f32 v149, v100, v101\n"
        "v_cvt_pk_bf16_f32 v150, v128, v129\n"
        "v_mfma_f32_16x16x32_bf16 v[98:101], v[212:215], v[158:161], 0\n"
        "ds_read_b128 v[126:129], %0 offset:9216\n"
        "v_cvt_pk_bf16_f32 v151, v130, v131\n"
        "v_cndmask_b32_e64 v250, v250, v170, s[52:53]\n"
        "v_mul_f32 v186, v186, v114\n"
        "v_mul_f32 v187, v187, v115\n"
        "v_mfma_f32_16x16x32_bf16 v[98:101], v[208:211], v[162:165], v[98:101]\n"
        "ds_read_b128 v[130:133], %0 offset:10240\n"
        "v_mul_f32 v188, v188, v116\n"
        "v_mul_f32 v189, v189, v117\n"
        "v_mul_f32 v190, s58, v190\n"
        "v_mul_f32 v186, v186, v242\n"
        "v_mfma_f32_16x16x32_bf16 v[98:101], v[94:97], v[166:169], v[98:101]\n"
        "s_waitcnt lgkmcnt(2)\n"
        "v_mul_f32 v187, v187, v242\n"
        "v_mul_f32 v188, v188, v242\n"
        "v_mul_f32 v189, v189, v242\n"
        "v_mul_f32 v190, v190, v242\n"
        "v_cvt_pk_bf16_f32 v200, v186, v187\n"
        "v_mfma_f32_4x4x4_16b_bf16 v[94:97], v[134:135], v[234:235], 0\n"
        "v_mfma_f32_4x4x4_16b_bf16 v[170:173], v[152:153], v[234:235], 0\n"
        "v_cndmask_b32_e64 v250, v250, v98, s[54:55]\n"
        "v_mul_f32 v250, v250, v253\n"
        "v_cvt_pk_bf16_f32 v201, v188, v189\n"
        "v_cvt_pk_bf16_f32 v202, v190, 0\n"
        "v_mfma_f32_16x16x32_bf16 v[98:101], v[176:179], v[90:93], 0\n"
        "s_waitcnt lgkmcnt(0)\n"
        "s_mov_b64 exec, s[56:57]\n"
        "v_mov_b32 v250, 0xff800000\n"
        "s_mov_b64 exec, -1\n"
        "v_mfma_f32_16x16x32_bf16 v[98:101], v[236:239], v[126:129], v[98:101]\n"
        "v_mfma_f32_16x16x32_bf16 v[98:101], v[244:247], v[130:133], v[98:101]\n"
        "v_max_f32_dpp v248, v250, v250 row_ror:8 row_mask:0xf bank_mask:0xf\n"
        "v_mfma_f32_16x16x32_bf16 v[184:187], v[102:105], v[130:133], 0\n"
        "v_mfma_f32_16x16x32_bf16 v[184:187], v[180:183], v[126:129], v[184:187]\n"
        "v_max_f32_dpp v248, v248, v248 row_ror:4 row_mask:0xf bank_mask:0xf\n"
        "v_mfma_f32_16x16x32_bf16 v[184:187], v[154:157], v[90:93], v[184:187]\n"
        "v_mfma_f32_16x16x32_bf16 v[188:191], v[136:139], v[90:93], 0\n"
        "ds_read_b128 v[208:211], %0 offset:11264\n"
        "v_max_f32_dpp v248, v248, v248 row_ror:2 row_mask:0xf bank_mask:0xf\n"
        "v_cvt_pk_bf16_f32 v212, v98, v99\n"
        "v_cvt_pk_bf16_f32 v213, v100, v101\n"
        "v_max_f32_dpp v248, v248, v248 row_ror:1 row_mask:0xf bank_mask:0xf\n"
        "v_mfma_f32_16x16x32_bf16 v[188:191], v[140:143], v[126:129], v[188:191]\n"
        "ds_read_b128 v[98:101], %0 offset:12288\n"
        "v_cvt_pk_bf16_f32 v214, v184, v185\n"
        "v_cvt_pk_bf16_f32 v215, v186, v187\n"
        "v_permlane32_swap_b32 v251, v248\n"
        "v_mfma_f32_16x16x32_bf16 v[188:191], v[144:147], v[130:133], v[188:191]\n"
        "ds_read_b128 v[184:187], %0 offset:13312\n"
        "v_max_f32 v251, v251, v248\n"
        "v_mov_b32 v248, v251\n"
        "v_mfma_f32_16x16x32_bf16 v[218:221], v[158:161], v[130:133], 0\n"
        "v_mfma_f32_16x16x32_bf16 v[218:221], v[162:165], v[126:129], v[218:221]\n"
        "v_permlane16_swap_b32 v251, v248\n"
        "v_max_f32 v251, v251, v248\n"
        "v_mov_b32 v248, v251\n"
        "v_cvt_pk_bf16_f32 v222, v188, v189\n"
        "v_mfma_f32_16x16x32_bf16 v[218:221], v[166:169], v[90:93], v[218:221]\n"
        "s_waitcnt lgkmcnt(1)\n"
        "v_cvt_pk_bf16_f32 v223, v190, v191\n"
        "v_permlane32_swap_b32 v251, v248\n"
        "v_mul_f32 v251, v251, v249\n"
        "v_fma_f32 v204, v204, v249, -v251\n"
        "v_mfma_f32_16x16x32_bf16 v[126:129], v[166:169], v[208:211], 0\n"
        "v_fma_f32 v205, v205, v249, -v251\n"
        "v_exp_f32 v204, v204\n"
        "v_cvt_pk_bf16_f32 v130, v218, v94\n"
        "v_cvt_pk_bf16_f32 v132, v219, v95\n"
        "v_mfma_f32_16x16x32_bf16 v[126:129], v[162:165], v[98:101], v[126:129]\n"
        "s_waitcnt lgkmcnt(0)\n"
        "v_cvt_pk_bf16_f32 v134, v220, v96\n"
        "v_cvt_pk_bf16_f32 v152, v221, v97\n"
        "v_exp_f32 v205, v205\n"
        "v_fma_f32 v206, v206, v249, -v251\n"
        "v_mfma_f32_16x16x32_bf16 v[126:129], v[158:161], v[184:187], v[126:129]\n"
        "v_exp_f32 v206, v206\n"
        "v_fma_f32 v207, v207, v249, -v251\n"
        "v_add_f32 v217, v204, v205\n"
        "v_fma_f32 v118, v118, v249, -v251\n"
        "v_mfma_f32_16x16x32_bf16 v[94:97], v[144:147], v[184:187], 0\n"
        "v_exp_f32 v207, v207\n"
        "v_add_f32 v217, v217, v206\n"
        "v_exp_f32 v118, v118\n"
        "v_mfma_f32_16x16x32_bf16 v[94:97], v[140:143], v[98:101], v[94:97]\n"
        "v_cvt_pk_bf16_f32 v131, v126, v170\n"
        "v_cvt_pk_bf16_f32 v133, v127, v171\n"
        "v_cvt_pk_bf16_f32 v135, v128, v172\n"
        "v_cvt_pk_bf16_f32 v153, v129, v173\n"
        "v_fma_f32 v119, v119, v249, -v251\n"
        "v_mfma_f32_16x16x32_bf16 v[94:97], v[136:139], v[208:211], v[94:97]\n"
        "ds_read_b64 v[174:175], v252 offset:32\n"
        "ds_write_b64 v254, v[130:131]\n"
        "v_add_f32 v217, v217, v207\n"
        "v_mfma_f32_16x16x32_bf16 v[126:129], v[154:157], v[208:211], 0\n"
        "ds_write_b64 v254, v[132:133] offset:384\n"
        "v_exp_f32 v119, v119\n"
        "v_mfma_f32_16x16x32_bf16 v[126:129], v[180:183], v[98:101], v[126:129]\n"
        "ds_read_b64 v[188:189], v252 offset:48\n"
        "v_cvt_pk_bf16_f32 v218, v94, v95\n"
        "v_cvt_pk_bf16_f32 v219, v96, v97\n"
        "v_fma_f32 v120, v120, v249, -v251\n"
        "v_add_f32 v217, v217, v118\n"
        "v_mfma_f32_16x16x32_bf16 v[126:129], v[102:105], v[184:187], v[126:129]\n"
        "ds_read_b128 v[94:97], %0 offset:14336\n"
        "ds_write_b64 v254, v[134:135] offset:768\n"
        "v_fma_f32 v121, v121, v249, -v251\n"
        "v_mfma_f32_16x16x32_bf16 v[130:133], v[244:247], v[184:187], 0\n"
        "ds_read_b128 v[170:173], %0 offset:15360\n"
        "ds_write_b64 v254, v[152:153] offset:1152\n"
        "v_sub_f32 v250, v250, v248\n"
        "v_mfma_f32_16x16x32_bf16 v[130:133], v[236:239], v[98:101], v[130:133]\n"
        "ds_read_b128 v[184:187], %0 offset:16384\n"
        "v_cvt_pk_bf16_f32 v228, v126, v127\n"
        "v_cvt_pk_bf16_f32 v229, v128, v129\n"
        "v_exp_f32 v120, v120\n"
        "v_mfma_f32_16x16x32_bf16 v[130:133], v[176:179], v[208:211], v[130:133]\n"
        "s_waitcnt lgkmcnt(3)\n"
        "v_add_f32 v217, v217, v119\n"
        "v_exp_f32 v121, v121\n"
        "v_fma_f32 v192, v192, v249, -v251\n"
        "v_mul_f32 v250, s58, v250\n"
        "v_mfma_f32_4x4x4_16b_bf16 v[98:101], v[174:175], v[234:235], 0\n"
        "v_mfma_f32_4x4x4_16b_bf16 v[126:129], v[188:189], v[234:235], 0\n"
        "v_add_f32 v217, v217, v120\n"
        "v_cvt_pk_bf16_f32 v226, v130, v131\n"
        "v_cvt_pk_bf16_f32 v227, v132, v133\n"
        "v_exp_f32 v192, v192\n"
        "v_fma_f32 v193, v193, v249, -v251\n"
        "v_mfma_f32_16x16x32_bf16 v[130:133], v[176:179], v[94:97], 0\n"
        "s_waitcnt lgkmcnt(0)\n"
        "v_exp_f32 v250, v250\n"
        "v_add_f32 v217, v217, v121\n"
        "v_exp_f32 v193, v193\n"
        "v_mfma_f32_16x16x32_bf16 v[130:133], v[236:239], v[170:173], v[130:133]\n"
        "v_fma_f32 v194, v194, v249, -v251\n"
        "v_add_f32 v217, v217, v192\n"
        "v_exp_f32 v194, v194\n"
        "v_fma_f32 v195, v195, v249, -v251\n"
        "v_mfma_f32_16x16x32_bf16 v[130:133], v[244:247], v[184:187], v[130:133]\n"
        "v_add_f32_dpp v248, v250, v250 row_ror:8 row_mask:0xf bank_mask:0xf\n"
        "v_add_f32 v217, v217, v193\n"
        "v_exp_f32 v195, v195\n"
        "v_fma_f32 v216, v216, v249, -v251\n"
        "v_mfma_f32_16x16x32_bf16 v[188:191], v[102:105], v[184:187], 0\n"
        "v_add_f32_dpp v248, v248, v248 row_ror:4 row_mask:0xf bank_mask:0xf\n"
        "v_add_f32 v217, v217, v194\n"
        "v_exp_f32 v216, v216\n"
        "v_cvt_pk_bf16_f32 v208, v130, v131\n"
        "v_mfma_f32_16x16x32_bf16 v[188:191], v[180:183], v[170:173], v[188:191]\n"
        "v_cvt_pk_bf16_f32 v209, v132, v133\n"
        "v_add_f32_dpp v248, v248, v248 row_ror:2 row_mask:0xf bank_mask:0xf\n"
        "v_add_f32 v217, v217, v195\n"
        "v_add_f32 v217, v217, v216\n"
        "v_add_f32_dpp v248, v248, v248 row_ror:1 row_mask:0xf bank_mask:0xf\n"
        "v_mfma_f32_16x16x32_bf16 v[188:191], v[154:157], v[94:97], v[188:191]\n"
        "v_mul_f32 v250, v250, v253\n"
        "v_permlane32_swap_b32 v217, v248\n"
        "v_add_f32 v217, v217, v248\n"
        "v_mov_b32 v253, v217\n"
        "v_mfma_f32_16x16x32_bf16 v[130:133], v[136:139], v[94:97], 0\n"
        "ds_read_b128 v[230:233], %0 offset:17408\n"
        "v_permlane16_swap_b32 v217, v253\n"
        "v_add_f32 v217, v217, v253\n"
        "v_cvt_pk_bf16_f32 v210, v188, v189\n"
        "v_mfma_f32_16x16x32_bf16 v[130:133], v[140:143], v[170:173], v[130:133]\n"
        "ds_read_b128 v[240:243], %0 offset:18432\n"
        "v_cvt_pk_bf16_f32 v211, v190, v191\n"
        "v_rcp_f32 v217, v217\n"
        "v_mul_f32 v204, v204, v106\n"
        "v_mfma_f32_16x16x32_bf16 v[130:133], v[144:147], v[184:187], v[130:133]\n"
        "ds_read_b128 v[188:191], %0 offset:19456\n"
        "v_mov_b32 v248, v217\n"
        "v_mul_f32 v205, v205, v107\n"
        "v_mul_f32 v206, v206, v108\n"
        "v_mul_f32 v207, v207, v109\n"
        "v_mfma_f32_16x16x32_bf16 v[106:109], v[158:161], v[184:187], 0\n"
        "v_permlane32_swap_b32 v217, v248\n"
        "v_mul_f32 v250, v250, v248\n"
        "v_cvt_pk_bf16_f32 v248, v130, v131\n"
        "v_cvt_pk_bf16_f32 v249, v132, v133\n"
        "v_mfma_f32_16x16x32_bf16 v[106:109], v[162:165], v[170:173], v[106:109]\n"
        "v_cvt_pk_bf16_f32 v250, v250, 0\n"
        "ds_write_b16 %3, v250\n"
        "v_mul_f32 v118, v118, v110\n"
        "v_mfma_f32_16x16x32_bf16 v[106:109], v[166:169], v[94:97], v[106:109]\n"
        "s_waitcnt lgkmcnt(1)\n"
        "v_mul_f32 v119, v119, v111\n"
        "v_mul_f32 v120, v120, v112\n"
        "v_mul_f32 v121, v121, v113\n"
        "v_mul_f32 v204, v204, v217\n"
        "v_mul_f32 v205, v205, v217\n"
        "v_mfma_f32_16x16x32_bf16 v[110:113], v[166:169], v[230:233], 0\n"
        "ds_read2_b32 v[224:225], v255 offset0:0 offset1:0\n"
        "ds_read2_b32 v[220:221], v255 offset0:1 offset1:1\n"
        "v_cvt_pk_bf16_f32 v130, v106, v98\n"
        "v_cvt_pk_bf16_f32 v106, v107, v99\n"
        "v_mfma_f32_16x16x32_bf16 v[110:113], v[162:165], v[240:243], v[110:113]\n"
        "v_cvt_pk_bf16_f32 v132, v108, v100\n"
        "v_cvt_pk_bf16_f32 v134, v109, v101\n"
        "v_mul_f32 v206, v206, v217\n"
        "v_mul_f32 v207, v207, v217\n"
        "v_mul_f32 v118, v118, v217\n"
        "v_mfma_f32_16x16x32_bf16 v[110:113], v[158:161], v[188:191], v[110:113]\n"
        "v_mul_f32 v119, v119, v217\n"
        "v_mul_f32 v120, v120, v217\n"
        "v_mul_f32 v121, v121, v217\n"
        "v_cvt_pk_bf16_f32 v94, v204, v205\n"
        "v_cvt_pk_bf16_f32 v95, v206, v207\n"
        "v_mfma_f32_16x16x32_bf16 v[98:101], v[144:147], v[188:191], 0\n"
        "v_cvt_pk_bf16_f32 v96, v118, v119\n"
        "v_cvt_pk_bf16_f32 v97, v120, v121\n"
        "v_cvt_pk_bf16_f32 v131, v110, v126\n"
        "v_cvt_pk_bf16_f32 v107, v111, v127\n"
        "v_cvt_pk_bf16_f32 v133, v112, v128\n"
        "v_mfma_f32_16x16x32_bf16 v[98:101], v[140:143], v[240:243], v[98:101]\n"
        "v_cvt_pk_bf16_f32 v135, v113, v129\n"
        "ds_write_b64 v254, v[130:131] offset:8\n"
        "v_mul_f32 v192, v192, v114\n"
        "v_mfma_f32_16x16x32_bf16 v[98:101], v[136:139], v[230:233], v[98:101]\n"
        "ds_read_b64 v[120:121], v252 offset:64\n"
        "ds_write_b64 v254, v[106:107] offset:392\n"
        "v_mul_f32 v193, v193, v115\n"
        "v_mfma_f32_16x16x32_bf16 v[106:109], v[154:157], v[230:233], 0\n"
        "ds_write_b64 v254, v[132:133] offset:776\n"
        "v_mul_f32 v194, v194, v116\n"
        "v_mul_f32 v195, v195, v117\n"
        "v_mfma_f32_16x16x32_bf16 v[106:109], v[180:183], v[240:243], v[106:109]\n"
        "ds_read_b64 v[152:153], v252 offset:80\n"
        "v_cvt_pk_bf16_f32 v114, v98, v99\n"
        "v_cvt_pk_bf16_f32 v115, v100, v101\n"
        "v_mul_f32 v216, s58, v216\n"
        "v_mul_f32 v192, v192, v217\n"
        "v_mfma_f32_16x16x32_bf16 v[106:109], v[102:105], v[188:191], v[106:109]\n"
        "ds_read_b128 v[110:113], %0 offset:20480\n"
        "ds_write_b64 v254, v[134:135] offset:1160\n"
        "v_mul_f32 v193, v193, v217\n"
        "v_mfma_f32_16x16x32_bf16 v[98:101], v[244:247], v[188:191], 0\n"
        "ds_read_b128 v[116:119], %0 offset:21504\n"
        "v_mul_f32 v194, v194, v217\n"
        "v_mul_f32 v195, v195, v217\n"
        "v_mul_f32 v216, v216, v217\n"
        "v_cvt_pk_bf16_f32 v206, v106, v107\n"
        "v_mfma_f32_16x16x32_bf16 v[98:101], v[236:239], v[240:243], v[98:101]\n"
        "ds_read_b128 v[126:129], %0 offset:22528\n"
        "v_cvt_pk_bf16_f32 v207, v108, v109\n"
        "v_cvt_pk_bf16_f32 v240, v192, v193\n"
        "v_cvt_pk_bf16_f32 v241, v194, v195\n"
        "v_cvt_pk_bf16_f32 v242, v216, 0\n"
        "v_mfma_f32_16x16x32_bf16 v[98:101], v[176:179], v[230:233], v[98:101]\n"
        "s_waitcnt lgkmcnt(2)\n"
        "v_mfma_f32_4x4x4_16b_bf16 v[106:109], v[120:121], v[234:235], 0\n"
        "v_mfma_f32_4x4x4_16b_bf16 v[130:133], v[152:153], v[234:235], 0\n"
        "v_mfma_f32_16x16x32_bf16 v[170:173], v[176:179], v[110:113], 0\n"
        "s_waitcnt lgkmcnt(1)\n"
        "v_mfma_f32_16x16x32_bf16 v[170:173], v[236:239], v[116:119], v[170:173]\n"
        "s_waitcnt lgkmcnt(0)\n"
        "v_mfma_f32_16x16x32_bf16 v[170:173], v[244:247], v[126:129], v[170:173]\n"
        "v_cvt_pk_bf16_f32 v204, v98, v99\n"
        "v_cvt_pk_bf16_f32 v205, v100, v101\n"
        "v_mfma_f32_16x16x32_bf16 v[98:101], v[102:105], v[126:129], 0\n"
        "v_mfma_f32_16x16x32_bf16 v[98:101], v[180:183], v[116:119], v[98:101]\n"
        "v_mfma_f32_16x16x32_bf16 v[98:101], v[154:157], v[110:113], v[98:101]\n"
        "v_mfma_f32_16x16x32_bf16 v[184:187], v[136:139], v[110:113], 0\n"
        "ds_read_b128 v[188:191], %0 offset:23552\n"
        "v_mfma_f32_16x16x32_bf16 v[184:187], v[140:143], v[116:119], v[184:187]\n"
        "ds_read_b128 v[192:195], %0 offset:24576\n"
        "v_cvt_pk_bf16_f32 v230, v170, v171\n"
        "v_cvt_pk_bf16_f32 v231, v172, v173\n"
        "v_mfma_f32_16x16x32_bf16 v[184:187], v[144:147], v[126:129], v[184:187]\n"
        "ds_read_b128 v[170:173], %0 offset:25600\n"
        "v_cvt_pk_bf16_f32 v232, v98, v99\n"
        "v_cvt_pk_bf16_f32 v233, v100, v101\n"
        "v_mfma_f32_16x16x32_bf16 v[98:101], v[158:161], v[126:129], 0\n"
        "v_mfma_f32_16x16x32_bf16 v[98:101], v[162:165], v[116:119], v[98:101]\n"
        "v_mfma_f32_16x16x32_bf16 v[98:101], v[166:169], v[110:113], v[98:101]\n"
        "s_waitcnt lgkmcnt(1)\n"
        "v_mfma_f32_16x16x32_bf16 v[118:121], v[166:169], v[188:191], 0\n"
        "ds_read2_b32 v[250:251], v255 offset0:2 offset1:2\n"
        "ds_read2_b32 v[116:117], v255 offset0:3 offset1:3\n"
        "v_cvt_pk_bf16_f32 v110, v184, v185\n"
        "v_cvt_pk_bf16_f32 v111, v186, v187\n"
        "v_mfma_f32_16x16x32_bf16 v[118:121], v[162:165], v[192:195], v[118:121]\n"
        "ds_read_b64 v[112:113], %7\n"
        "v_cvt_pk_bf16_f32 v126, v98, v106\n"
        "v_cvt_pk_bf16_f32 v128, v99, v107\n"
        "v_cvt_pk_bf16_f32 v152, v100, v108\n"
        "s_waitcnt lgkmcnt(3)\n"
        "v_mfma_f32_16x16x32_bf16 v[118:121], v[158:161], v[170:173], v[118:121]\n"
        "v_cvt_pk_bf16_f32 v162, v101, v109\n"
        "v_mfma_f32_16x16x32_bf16 v[98:101], v[144:147], v[170:173], 0\n"
        "ds_read_b64 v[134:135], %7 offset:16\n"
        "v_mfma_f32_16x16x32_bf16 v[98:101], v[140:143], v[192:195], v[98:101]\n"
        "v_mfma_f32_16x16x32_bf16 v[98:101], v[136:139], v[188:191], v[98:101]\n"
        "ds_read_b64 v[140:141], %7 offset:32\n"
        "v_mfma_f32_16x16x32_bf16 v[106:109], v[154:157], v[188:191], 0\n"
        "v_mfma_f32_16x16x32_bf16 v[106:109], v[180:183], v[192:195], v[106:109]\n"
        "ds_read_b64 v[136:137], %7 offset:48\n"
        "v_cvt_pk_bf16_f32 v127, v118, v130\n"
        "v_cvt_pk_bf16_f32 v129, v119, v131\n"
        "v_cvt_pk_bf16_f32 v153, v120, v132\n"
        "v_cvt_pk_bf16_f32 v163, v121, v133\n"
        "v_mfma_f32_16x16x32_bf16 v[106:109], v[102:105], v[170:173], v[106:109]\n"
        "v_cvt_pk_bf16_f32 v144, v98, v99\n"
        "v_cvt_pk_bf16_f32 v145, v100, v101\n"
        "v_mfma_f32_16x16x32_bf16 v[98:101], v[244:247], v[170:173], 0\n"
        "ds_read_b64 v[138:139], %7 offset:64\n"
        "ds_write_b64 v254, v[126:127] offset:16\n"
        "v_mfma_f32_16x16x32_bf16 v[98:101], v[236:239], v[192:195], v[98:101]\n"
        "ds_write_b64 v254, v[128:129] offset:400\n"
        "v_mfma_f32_16x16x32_bf16 v[98:101], v[176:179], v[188:191], v[98:101]\n"
        "ds_read_b64 v[142:143], %7 offset:80\n"
        "v_mov_b32 v199, 0\n"
        "v_cvt_pk_bf16_f32 v160, v106, v107\n"
        "v_cvt_pk_bf16_f32 v161, v108, v109\n"
        "s_waitcnt lgkmcnt(4)\n"
        "v_mfma_f32_4x4x4_16b_bf16 v[102:105], v[112:113], v[234:235], 0\n"
        "v_mfma_f32_4x4x4_16b_bf16 v[106:109], v[134:135], v[234:235], 0\n"
        "v_mfma_f32_4x4x4_16b_bf16 v[118:121], v[140:141], v[234:235], 0\n"
        "v_mfma_f32_4x4x4_16b_bf16 v[126:129], v[136:137], v[234:235], 0\n"
        "s_waitcnt lgkmcnt(2)\n"
        "v_mfma_f32_4x4x4_16b_bf16 v[130:133], v[138:139], v[234:235], 0\n"
        "s_waitcnt lgkmcnt(0)\n"
        "v_mfma_f32_4x4x4_16b_bf16 v[134:137], v[142:143], v[234:235], 0\n"
        "v_cvt_pk_bf16_f32 v158, v98, v99\n"
        "v_cvt_pk_bf16_f32 v159, v100, v101\n"
        "ds_write_b64 v254, v[152:153] offset:784\n"
        "v_mfma_f32_16x16x32_bf16 v[102:105], v[122:125], v[212:215], v[102:105]\n"
        "ds_write_b64 v254, v[162:163] offset:1168\n"
        "v_mfma_f32_16x16x32_bf16 v[102:105], v[196:199], v[222:225], v[102:105]\n"
        "v_mfma_f32_16x16x32_bf16 v[106:109], v[196:199], v[218:221], v[106:109]\n"
        "v_mfma_f32_16x16x32_bf16 v[106:109], v[122:125], v[226:229], v[106:109]\n"
        "ds_read2_b32 v[112:113], v255 offset0:4 offset1:4\n"
        "v_mfma_f32_16x16x32_bf16 v[118:121], v[122:125], v[208:211], v[118:121]\n"
        "ds_read2_b32 v[146:147], v255 offset0:5 offset1:5\n"
        "v_mfma_f32_16x16x32_bf16 v[118:121], v[196:199], v[248:251], v[118:121]\n"
        "v_mfma_f32_16x16x32_bf16 v[126:129], v[196:199], v[114:117], v[126:129]\n"
        "ds_read_b128 v[152:155], %4\n"
        "v_mfma_f32_16x16x32_bf16 v[126:129], v[122:125], v[204:207], v[126:129]\n"
        "ds_read_b128 v[162:165], %4 offset:64\n"
        "v_cvt_pk_bf16_f32 v138, v102, v106\n"
        "v_cvt_pk_bf16_f32 v166, v103, v107\n"
        "v_cvt_pk_bf16_f32 v170, v104, v108\n"
        "v_cvt_pk_bf16_f32 v174, v105, v109\n"
        "v_mfma_f32_16x16x32_bf16 v[130:133], v[122:125], v[230:233], v[130:133]\n"
        "s_waitcnt lgkmcnt(2)\n"
        "v_mfma_f32_16x16x32_bf16 v[130:133], v[196:199], v[110:113], v[130:133]\n"
        "ds_read_b64 v[106:107], %7 offset:3328\n"
        "v_cvt_pk_bf16_f32 v139, v118, v126\n"
        "v_cvt_pk_bf16_f32 v167, v119, v127\n"
        "v_cvt_pk_bf16_f32 v171, v120, v128\n"
        "v_cvt_pk_bf16_f32 v175, v121, v129\n"
        "v_mfma_f32_16x16x32_bf16 v[134:137], v[196:199], v[144:147], v[134:137]\n"
        "v_mfma_f32_16x16x32_bf16 v[134:137], v[122:125], v[158:161], v[134:137]\n"
        "ds_read_b64 v[126:127], %7 offset:3344\n"
        "s_waitcnt lgkmcnt(2)\n"
        "v_mfma_f32_16x16x32_bf16 v[98:101], v[152:155], v[212:215], 0\n"
        "v_mfma_f32_16x16x32_bf16 v[98:101], v[162:165], v[222:225], v[98:101]\n"
        "ds_read_b64 v[124:125], %7 offset:3360\n"
        "v_mfma_f32_16x16x32_bf16 v[102:105], v[162:165], v[218:221], 0\n"
        "v_mfma_f32_16x16x32_bf16 v[102:105], v[152:155], v[226:229], v[102:105]\n"
        "ds_read_b64 v[128:129], %7 offset:3376\n"
        "v_cvt_pk_bf16_f32 v140, v130, v134\n"
        "v_cvt_pk_bf16_f32 v168, v131, v135\n"
        "v_cvt_pk_bf16_f32 v172, v132, v136\n"
        "v_cvt_pk_bf16_f32 v176, v133, v137\n"
        "v_mfma_f32_16x16x32_bf16 v[118:121], v[152:155], v[208:211], 0\n"
        "s_add_u32 s31, s31, 1\n"
        "v_mfma_f32_16x16x32_bf16 v[118:121], v[162:165], v[248:251], v[118:121]\n"
        "ds_read_b64 v[130:131], %7 offset:3392\n"
        "v_cvt_pk_bf16_f32 v178, v98, v102\n"
        "s_add_u32 s37, s35, 0\n"
        "s_mul_i32 s68, s31, 3\n"
        "v_mfma_f32_16x16x32_bf16 v[102:105], v[162:165], v[114:117], 0\n"
        "s_lshr_b32 s38, s31, 3\n"
        "v_mfma_f32_16x16x32_bf16 v[102:105], v[152:155], v[204:207], v[102:105]\n"
        "ds_read_b64 v[156:157], %7 offset:3408\n"
        "s_add_u32 s38, s38, 11\n"
        "s_waitcnt lgkmcnt(2)\n"
        "v_mfma_f32_4x4x4_16b_bf16 v[98:101], v[106:107], v[234:235], 0\n"
        "buffer_store_dwordx3 v[138:140], %11, %22, s37 offen sc1 nt\n"
        "v_mfma_f32_4x4x4_16b_bf16 v[106:109], v[126:127], v[234:235], 0\n"
        "v_mfma_f32_4x4x4_16b_bf16 v[120:123], v[124:125], v[234:235], 0\n"
        "v_mfma_f32_4x4x4_16b_bf16 v[132:135], v[128:129], v[234:235], 0\n"
        "s_waitcnt lgkmcnt(1)\n"
        "v_mfma_f32_4x4x4_16b_bf16 v[136:139], v[130:131], v[234:235], 0\n"
        "s_waitcnt lgkmcnt(0)\n"
        "v_mfma_f32_4x4x4_16b_bf16 v[140:143], v[156:157], v[234:235], 0\n"
        "s_add_u32 s37, s35, 0\n"
        "buffer_store_dwordx3 v[166:168], %14, %22, s37 offen offset:768 sc1 nt\n"
        "s_bitcmp1_b32 s31, 0\n"
        "s_cselect_b32 s38, 3, s38\n"
        "s_add_u32 s37, s35, 98112\n"
        "s_add_u32 s68, s68, s38\n"
        "v_mfma_f32_16x16x32_bf16 v[124:127], v[152:155], v[230:233], 0\n"
        "s_cmp_lt_u32 s31, %24\n"
        "buffer_store_dwordx3 v[170:172], %11, %22, s37 offen offset:192 sc1 nt\n"
        "s_cselect_b32 s68, s68, 0\n"
        "s_add_u32 s37, s35, 98112\n"
        "v_mfma_f32_16x16x32_bf16 v[124:127], v[162:165], v[110:113], v[124:127]\n"
        "ds_read_b64 v[128:129], %7 offset:6656\n"
        "s_and_b32 s38, s31, 7\n"
        "v_mov_b32 v203, 0\n"
        "v_cvt_pk_bf16_f32 v179, v118, v102\n"
        "s_mul_i32 s29, s38, 192\n"
        "v_mfma_f32_16x16x32_bf16 v[102:105], v[162:165], v[144:147], 0\n"
        "s_mul_i32 s30, s38, 384\n"
        "buffer_store_dwordx3 v[174:176], %13, %22, s37 offen offset:960 sc1 nt\n"
        "s_add_u32 s34, s34, 832\n"
        "v_mfma_f32_16x16x32_bf16 v[102:105], v[152:155], v[158:161], v[102:105]\n"
        "ds_read_b64 v[118:119], %7 offset:6672\n"
        "s_cmp_eq_u32 s34, 8320\n"
        "v_mfma_f32_16x16x32_bf16 v[98:101], v[148:151], v[212:215], v[98:101]\n"
        "s_cselect_b32 s34, 0, s34\n"
        "v_mfma_f32_16x16x32_bf16 v[98:101], v[200:203], v[222:225], v[98:101]\n"
        "ds_read_b64 v[152:153], %7 offset:6688\n"
        "s_add_u32 s73, s34, %19\n"
        "v_mfma_f32_16x16x32_bf16 v[106:109], v[200:203], v[218:221], v[106:109]\n"
        "s_mul_i32 s38, s31, 1344\n"
        "v_cvt_pk_bf16_f32 v180, v124, v102\n"
        "v_mfma_f32_16x16x32_bf16 v[106:109], v[148:151], v[226:229], v[106:109]\n"
        "ds_read_b64 v[102:103], %7 offset:6704\n"
        "s_cmp_ge_u32 s31, s71\n"
        "v_mfma_f32_16x16x32_bf16 v[120:123], v[148:151], v[208:211], v[120:123]\n"
        "s_cselect_b32 s39, s70, s72\n"
        "v_mfma_f32_16x16x32_bf16 v[120:123], v[200:203], v[248:251], v[120:123]\n"
        "ds_read_b64 v[104:105], %7 offset:6720\n"
        "s_add_u32 s37, s35, 588672\n"
        "s_add_u32 s39, s39, s38\n"
        "v_mfma_f32_16x16x32_bf16 v[132:135], v[200:203], v[114:117], v[132:135]\n"
        "s_add_u32 s46, s37, 1344\n"
        "v_mfma_f32_16x16x32_bf16 v[132:135], v[148:151], v[204:207], v[132:135]\n"
        "ds_read_b64 v[154:155], %7 offset:6736\n"
        "v_add_u32 %7, s69, %7\n"
        "s_waitcnt lgkmcnt(2)\n"
        "v_mfma_f32_4x4x4_16b_bf16 v[124:127], v[128:129], v[234:235], 0\n"
        "buffer_store_dwordx3 v[178:180], %11, s[44:47], s37 offen offset:1152 sc1 nt\n"
        "v_mfma_f32_4x4x4_16b_bf16 v[128:131], v[118:119], v[234:235], 0\n"
        "v_mfma_f32_4x4x4_16b_bf16 v[168:171], v[152:153], v[234:235], 0\n"
        "v_mfma_f32_4x4x4_16b_bf16 v[172:175], v[102:103], v[234:235], 0\n"
        "s_waitcnt lgkmcnt(1)\n"
        "v_mfma_f32_4x4x4_16b_bf16 v[184:187], v[104:105], v[234:235], 0\n"
        "s_waitcnt lgkmcnt(0)\n"
        "v_mfma_f32_4x4x4_16b_bf16 v[188:191], v[154:155], v[234:235], 0\n"
        "v_xor_b32 v255, 4, %17\n"
        "v_mov_b32 v243, 0\n"
        "v_mfma_f32_16x16x32_bf16 v[136:139], v[148:151], v[230:233], v[136:139]\n"
        "s_mov_b64 exec, 1\n"
        "ds_inc_u32 %17, %1 offset:8\n"
        "s_mov_b64 exec, -1\n"
        "v_xor_b32 %17, 4, %17\n"
        "v_mfma_f32_16x16x32_bf16 v[136:139], v[200:203], v[110:113], v[136:139]\n"
        "v_mfma_f32_16x16x32_bf16 v[140:143], v[200:203], v[144:147], v[140:143]\n"
        "ds_read_b32 v254, v255\n"
        "v_mfma_f32_16x16x32_bf16 v[140:143], v[148:151], v[158:161], v[140:143]\n"
        "v_mfma_f32_16x16x32_bf16 v[124:127], v[94:97], v[212:215], v[124:127]\n"
        "v_mfma_f32_16x16x32_bf16 v[124:127], v[240:243], v[222:225], v[124:127]\n"
        "v_mfma_f32_16x16x32_bf16 v[128:131], v[240:243], v[218:221], v[128:131]\n"
        "v_mfma_f32_16x16x32_bf16 v[128:131], v[94:97], v[226:229], v[128:131]\n"
        "s_waitcnt lgkmcnt(0)\n"
        "v_readfirstlane_b32 s40, v254\n"
        "v_mfma_f32_16x16x32_bf16 v[168:171], v[94:97], v[208:211], v[168:171]\n"
        "s_cmp_lt_i32 s40, s68\n"
        "v_mfma_f32_16x16x32_bf16 v[168:171], v[240:243], v[248:251], v[168:171]\n"
        "s_cbranch_scc1 L26_%=\n"
        "L27_%=:\n"
        "ds_read_b128 v[176:179], %5\n"
        "ds_read_b128 v[154:157], %5 offset:3328\n"
        "v_mfma_f32_16x16x32_bf16 v[172:175], v[240:243], v[114:117], v[172:175]\n"
        "ds_read_b128 v[236:239], %5 offset:64\n"
        "ds_read_b128 v[180:183], %5 offset:3392\n"
        "ds_read_b128 v[244:247], %5 offset:128\n"
        "v_mfma_f32_16x16x32_bf16 v[172:175], v[94:97], v[204:207], v[172:175]\n"
        "ds_read_b128 v[102:105], %5 offset:3456\n"
        "v_mfma_f32_16x16x32_bf16 v[184:187], v[94:97], v[230:233], v[184:187]\n"
        "s_sub_u32 s69, 0, s69\n"
        "v_mfma_f32_16x16x32_bf16 v[184:187], v[240:243], v[110:113], v[184:187]\n"
        "s_mov_b32 s36, s35\n"
        "v_mfma_f32_16x16x32_bf16 v[188:191], v[240:243], v[144:147], v[188:191]\n"
        "s_mov_b32 s35, s39\n"
        "v_mfma_f32_16x16x32_bf16 v[188:191], v[94:97], v[158:161], v[188:191]\n"
        "s_setprio 0\n"
        "s_waitcnt lgkmcnt(0)\n"
        "s_nop 7\n"
        "s_cmp_ge_u32 s31, %24\n"
        "s_cbranch_scc1 CEND_%=\n"
        "MTOP_%=:\n"
        "s_waitcnt lgkmcnt(5)\n"
        "v_mfma_f32_16x16x32_bf16 v[94:97], v[18:21], v[176:179], 0\n"
        "v_cvt_pk_bf16_f32 v110, v98, v106\n"
        "v_cvt_pk_bf16_f32 v111, v120, v132\n"
        "v_cvt_pk_bf16_f32 v112, v136, v140\n"
        "v_cvt_pk_bf16_f32 v144, v99, v107\n"
        "v_cvt_pk_bf16_f32 v145, v121, v133\n"
        "s_waitcnt lgkmcnt(3)\n"
        "v_mfma_f32_16x16x32_bf16 v[94:97], v[22:25], v[236:239], v[94:97]\n"
        "s_add_u32 s37, s36, 196224\n"
        "v_cvt_pk_bf16_f32 v146, v137, v141\n"
        "v_cvt_pk_bf16_f32 v132, v100, v108\n"
        "v_cvt_pk_bf16_f32 v133, v122, v134\n"
        "v_cvt_pk_bf16_f32 v134, v138, v142\n"
        "v_cvt_pk_bf16_f32 v136, v101, v109\n"
        "s_waitcnt lgkmcnt(1)\n"
        "v_mfma_f32_16x16x32_bf16 v[94:97], v[26:29], v[244:247], v[94:97]\n"
        "v_cvt_pk_bf16_f32 v137, v123, v135\n"
        "v_cvt_pk_bf16_f32 v138, v139, v143\n"
        "v_cvt_pk_bf16_f32 v140, v124, v128\n"
        "v_cvt_pk_bf16_f32 v141, v168, v172\n"
        "v_cvt_pk_bf16_f32 v142, v184, v188\n"
        "s_waitcnt lgkmcnt(0)\n"
        "v_mfma_f32_16x16x32_bf16 v[98:101], v[26:29], v[102:105], 0\n"
        "v_cvt_pk_bf16_f32 v148, v125, v129\n"
        "v_cvt_pk_bf16_f32 v149, v169, v173\n"
        "v_cvt_pk_bf16_f32 v116, v94, v95\n"
        "v_cvt_pk_bf16_f32 v117, v96, v97\n"
        "v_cvt_pk_bf16_f32 v150, v185, v189\n"
        "v_mfma_f32_16x16x32_bf16 v[98:101], v[22:25], v[180:183], v[98:101]\n"
        "buffer_store_dwordx3 v[110:112], %11, %22, s37 offen offset:384 sc1 nt\n"
        "v_cvt_pk_bf16_f32 v158, v126, v130\n"
        "v_mfma_f32_16x16x32_bf16 v[98:101], v[18:21], v[154:157], v[98:101]\n"
        "v_cvt_pk_bf16_f32 v159, v170, v174\n"
        "v_cvt_pk_bf16_f32 v160, v186, v190\n"
        "v_cvt_pk_bf16_f32 v188, v127, v131\n"
        "v_cvt_pk_bf16_f32 v189, v171, v175\n"
        "v_cvt_pk_bf16_f32 v190, v187, v191\n"
        "v_mfma_f32_16x16x32_bf16 v[94:97], v[30:33], v[154:157], 0\n"
        "s_add_u32 s37, s36, 196224\n"
        "v_mfma_f32_16x16x32_bf16 v[94:97], v[34:37], v[180:183], v[94:97]\n"
        "v_cvt_pk_bf16_f32 v120, v98, v99\n"
        "v_cvt_pk_bf16_f32 v121, v100, v101\n"
        "v_mfma_f32_16x16x32_bf16 v[94:97], v[38:41], v[102:105], v[94:97]\n"
        "v_mfma_f32_16x16x32_bf16 v[98:101], v[38:41], v[244:247], 0\n"
        "buffer_store_dwordx3 v[144:146], %12, %22, s37 offen offset:1152 sc1 nt\n"
        "v_mfma_f32_16x16x32_bf16 v[98:101], v[34:37], v[236:239], v[98:101]\n"
        "s_mov_b64 exec, s[62:63]\n"
        "v_add_u32 %15, s29, %9\n"
        "v_add_u32 %6, s34, %16\n"
        "s_mov_b64 exec, -1\n"
        "v_mfma_f32_16x16x32_bf16 v[98:101], v[30:33], v[176:179], v[98:101]\n"
        "s_add_u32 s37, s36, 294336\n"
        "v_cvt_pk_bf16_f32 v122, v94, v95\n"
        "v_cvt_pk_bf16_f32 v123, v96, v97\n"
        "v_mfma_f32_16x16x32_bf16 v[106:109], v[42:45], v[176:179], 0\n"
        "v_mfma_f32_16x16x32_bf16 v[106:109], v[46:49], v[236:239], v[106:109]\n"
        "v_mfma_f32_16x16x32_bf16 v[106:109], v[50:53], v[244:247], v[106:109]\n"
        "buffer_store_dwordx3 v[132:134], %11, %22, s37 offen offset:576 sc1 nt\n"
        "v_mfma_f32_16x16x32_bf16 v[94:97], v[50:53], v[102:105], 0\n"
        "v_cvt_pk_bf16_f32 v118, v98, v99\n"
        "v_cvt_pk_bf16_f32 v119, v100, v101\n"
        "v_mfma_f32_16x16x32_bf16 v[94:97], v[46:49], v[180:183], v[94:97]\n"
        "s_add_u32 s37, s36, 392448\n"
        "v_writelane_b32 %8, s73, 48\n"
        "v_mfma_f32_16x16x32_bf16 v[94:97], v[42:45], v[154:157], v[94:97]\n"
        "v_cvt_pk_bf16_f32 v98, v106, v107\n"
        "v_cvt_pk_bf16_f32 v99, v108, v109\n"
        "v_mfma_f32_16x16x32_bf16 v[106:109], v[54:57], v[154:157], 0\n"
        "v_mfma_f32_16x16x32_bf16 v[106:109], v[58:61], v[180:183], v[106:109]\n"
        "buffer_store_dwordx3 v[136:138], %11, %22, s37 offen sc1 nt\n"
        "v_mfma_f32_16x16x32_bf16 v[106:109], v[62:65], v[102:105], v[106:109]\n"
        "ds_read_b32 v255, %8\n"
        "v_xor_b32 %8, %2, %8\n"
        "v_cvt_pk_bf16_f32 v124, v94, v95\n"
        "v_cvt_pk_bf16_f32 v125, v96, v97\n"
        "v_mfma_f32_16x16x32_bf16 v[94:97], v[62:65], v[244:247], 0\n"
        "s_add_u32 s37, s36, 392448\n"
        "v_mfma_f32_16x16x32_bf16 v[94:97], v[58:61], v[236:239], v[94:97]\n"
        "v_mfma_f32_16x16x32_bf16 v[94:97], v[54:57], v[176:179], v[94:97]\n"
        "v_cvt_pk_bf16_f32 v126, v106, v107\n"
        "v_cvt_pk_bf16_f32 v127, v108, v109\n"
        "v_mfma_f32_16x16x32_bf16 v[106:109], v[66:69], v[176:179], 0\n"
        "buffer_store_dwordx3 v[140:142], %14, %22, s37 offen offset:768 sc1 nt\n"
        "v_mfma_f32_16x16x32_bf16 v[106:109], v[70:73], v[236:239], v[106:109]\n"
        "s_waitcnt lgkmcnt(0)\n"
        "v_readlane_b32 s58, v255, 48\n"
        "v_mfma_f32_16x16x32_bf16 v[106:109], v[74:77], v[244:247], v[106:109]\n"
        "s_add_u32 s37, s36, 490560\n"
        "v_cvt_pk_bf16_f32 v100, v94, v95\n"
        "v_cvt_pk_bf16_f32 v101, v96, v97\n"
        "v_mfma_f32_16x16x32_bf16 v[94:97], v[74:77], v[102:105], 0\n"
        "v_mfma_f32_16x16x32_bf16 v[94:97], v[70:73], v[180:183], v[94:97]\n"
        "v_mfma_f32_16x16x32_bf16 v[94:97], v[66:69], v[154:157], v[94:97]\n"
        "buffer_store_dwordx3 v[148:150], %11, %22, s37 offen offset:192 sc1 nt\n"
        "v_mfma_f32_16x16x32_bf16 v[110:113], v[78:81], v[154:157], 0\n"
        "ds_read_b32 v254, %0 offset:29704\n"
        "v_cvt_pk_bf16_f32 v128, v106, v107\n"
        "v_cvt_pk_bf16_f32 v129, v108, v109\n"
        "v_mfma_f32_16x16x32_bf16 v[110:113], v[82:85], v[180:183], v[110:113]\n"
        "ds_read_b32 v253, %0 offset:29708\n"
        "s_add_u32 s37, s36, 490560\n"
        "v_cvt_pk_bf16_f32 v132, v94, v95\n"
        "v_cvt_pk_bf16_f32 v133, v96, v97\n"
        "v_mfma_f32_16x16x32_bf16 v[110:113], v[86:89], v[102:105], v[110:113]\n"
        "v_mfma_f32_16x16x32_bf16 v[94:97], v[86:89], v[244:247], 0\n"
        "v_mfma_f32_16x16x32_bf16 v[94:97], v[82:85], v[236:239], v[94:97]\n"
        "buffer_store_dwordx3 v[158:160], %13, %22, s37 offen offset:960 sc1 nt\n"
        "v_mfma_f32_16x16x32_bf16 v[94:97], v[78:81], v[176:179], v[94:97]\n"
        "s_waitcnt lgkmcnt(0)\n"
        "v_add_u32 v254, %7, v254\n"
        "v_add_u32 v253, %7, v253\n"
        "v_mfma_f32_16x16x32_bf16 v[136:139], v[176:179], v[116:119], 0\n"
        "ds_read_b128 v[140:143], %5 offset:6656\n"
        "v_cvt_pk_bf16_f32 v134, v110, v111\n"
        "v_cvt_pk_bf16_f32 v135, v112, v113\n"
        "s_add_u32 s37, s36, 588672\n"
        "v_cvt_pk_bf16_f32 v130, v94, v95\n"
        "v_mfma_f32_16x16x32_bf16 v[136:139], v[236:239], v[98:101], v[136:139]\n"
        "ds_read_b128 v[144:147], %5 offset:6720\n"
        "v_cvt_pk_bf16_f32 v131, v96, v97\n"
        "ds_read_b128 v[94:97], %5 offset:6784\n"
        "v_add_u32 %5, s69, %5\n"
        "v_mfma_f32_16x16x32_bf16 v[136:139], v[244:247], v[128:131], v[136:139]\n"
        "v_mfma_f32_16x16x32_bf16 v[148:151], v[102:105], v[128:131], 0\n"
        "ds_read_b128 v[158:161], %6 offset:128\n"
        "v_mfma_f32_16x16x32_bf16 v[148:151], v[180:183], v[98:101], v[148:151]\n"
        "ds_read_b128 v[162:165], %6 offset:64\n"
        "ds_read_b32 v106, v254\n"
        "v_mfma_f32_16x16x32_bf16 v[148:151], v[154:157], v[116:119], v[148:151]\n"
        "ds_read_b128 v[166:169], %6\n"
        "ds_read_b32 v107, v254 offset:832\n"
        "s_waitcnt lgkmcnt(5)\n"
        "v_mfma_f32_16x16x32_bf16 v[170:173], v[140:143], v[116:119], 0\n"
        "ds_read_b32 v108, v254 offset:1664\n"
        "ds_read_b32 v109, v254 offset:2496\n"
        "v_mfma_f32_16x16x32_bf16 v[170:173], v[144:147], v[98:101], v[170:173]\n"
        "ds_read_b32 v110, v254 offset:3328\n"
        "ds_read_b32 v111, v254 offset:4160\n"
        "s_waitcnt lgkmcnt(6)\n"
        "v_mul_f32 v136, v136, v106\n"
        "v_mfma_f32_16x16x32_bf16 v[170:173], v[94:97], v[128:131], v[170:173]\n"
        "ds_read_b32 v112, v254 offset:4992\n"
        "s_waitcnt lgkmcnt(5)\n"
        "v_mul_f32 v137, v137, v107\n"
        "v_mfma_f32_16x16x32_bf16 v[184:187], v[158:161], v[128:131], 0\n"
        "ds_read_b32 v113, v254 offset:5824\n"
        "s_waitcnt lgkmcnt(5)\n"
        "v_mul_f32 v138, v138, v108\n"
        "s_waitcnt lgkmcnt(4)\n"
        "v_mul_f32 v139, v139, v109\n"
        "v_mfma_f32_16x16x32_bf16 v[184:187], v[162:165], v[98:101], v[184:187]\n"
        "ds_read_b32 v114, v254 offset:6656\n"
        "s_waitcnt lgkmcnt(3)\n"
        "v_mul_f32 v148, v148, v110\n"
        "v_max3_f32 v252, v136, v137, v138\n"
        "v_mul_f32 v149, v149, v111\n"
        "v_mfma_f32_16x16x32_bf16 v[184:187], v[166:169], v[116:119], v[184:187]\n"
        "ds_read_b32 v115, v254 offset:7488\n"
        "s_waitcnt lgkmcnt(3)\n"
        "v_mul_f32 v150, v150, v112\n"
        "v_max3_f32 v252, v252, v139, v148\n"
        "v_max3_f32 v252, v252, v149, v150\n"
        "v_mfma_f32_16x16x32_bf16 v[98:101], v[176:179], v[120:123], 0\n"
        "ds_read_b32 v116, v254 offset:8320\n"
        "s_waitcnt lgkmcnt(3)\n"
        "v_mul_f32 v151, v151, v113\n"
        "s_waitcnt lgkmcnt(2)\n"
        "v_mul_f32 v170, v170, v114\n"
        "v_mfma_f32_16x16x32_bf16 v[98:101], v[236:239], v[124:127], v[98:101]\n"
        "ds_read_b32 v117, v254 offset:9152\n"
        "v_mul_f32 v184, s58, v184\n"
        "v_max3_f32 v252, v252, v151, v170\n"
        "s_waitcnt lgkmcnt(2)\n"
        "v_mul_f32 v171, v171, v115\n"
        "v_mfma_f32_16x16x32_bf16 v[98:101], v[244:247], v[132:135], v[98:101]\n"
        "s_setprio 2\n"
        "buffer_store_dwordx3 v[188:190], %11, %22, s37 offen offset:384 sc1 nt\n"
        "v_mfma_f32_16x16x32_bf16 v[128:131], v[102:105], v[132:135], 0\n"
        "ds_read_b32 v254, v253\n"
        "s_waitcnt lgkmcnt(2)\n"
        "v_mul_f32 v172, v172, v116\n"
        "v_max3_f32 v252, v252, v171, v172\n"
        "v_mfma_f32_16x16x32_bf16 v[128:131], v[180:183], v[124:127], v[128:131]\n"
        "ds_read_b32 v251, v253 offset:3328\n"
        "v_mul_f32 v98, v98, v106\n"
        "v_mul_f32 v99, v99, v107\n"
        "v_mul_f32 v100, v100, v108\n"
        "v_mul_f32 v101, v101, v109\n"
        "v_mfma_f32_16x16x32_bf16 v[128:131], v[154:157], v[120:123], v[128:131]\n"
        "ds_read_b32 v250, v253 offset:6656\n"
        "v_max3_f32 v253, v98, v99, v100\n"
        "s_waitcnt lgkmcnt(3)\n"
        "v_mul_f32 v173, v173, v117\n"
        "v_mfma_f32_16x16x32_bf16 v[186:189], v[140:143], v[120:123], 0\n"
        "s_mov_b64 exec, s[48:49]\n"
        "v_mov_b32 v184, 0xff800000\n"
        "s_mov_b64 exec, -1\n"
        "v_mul_f32 v128, v128, v110\n"
        "v_mul_f32 v129, v129, v111\n"
        "v_mul_f32 v130, v130, v112\n"
        "v_mfma_f32_16x16x32_bf16 v[186:189], v[144:147], v[124:127], v[186:189]\n"
        "v_max3_f32 v253, v253, v101, v128\n"
        "v_mul_f32 v131, v131, v113\n"
        "v_max3_f32 v253, v253, v129, v130\n"
        "v_max3_f32 v252, v252, v173, v184\n"
        "v_mfma_f32_16x16x32_bf16 v[186:189], v[94:97], v[132:135], v[186:189]\n"
        "v_mfma_f32_16x16x32_bf16 v[190:193], v[158:161], v[132:135], 0\n"
        "v_mfma_f32_16x16x32_bf16 v[190:193], v[162:165], v[124:127], v[190:193]\n"
        "v_mfma_f32_16x16x32_bf16 v[190:193], v[166:169], v[120:123], v[190:193]\n"
        "v_mfma_f32_16x16x32_bf16 v[124:127], v[18:21], v[140:143], 0\n"
        "v_mfma_f32_16x16x32_bf16 v[124:127], v[22:25], v[144:147], v[124:127]\n"
        "v_mfma_f32_16x16x32_bf16 v[124:127], v[26:29], v[94:97], v[124:127]\n"
        "v_mfma_f32_16x16x32_bf16 v[118:121], v[38:41], v[94:97], 0\n"
        "v_mfma_f32_16x16x32_bf16 v[118:121], v[34:37], v[144:147], v[118:121]\n"
        "v_mul_f32 v186, v186, v114\n"
        "v_mul_f32 v187, v187, v115\n"
        "v_mul_f32 v188, v188, v116\n"
        "v_mul_f32 v190, s58, v190\n"
        "v_max3_f32 v253, v253, v131, v186\n"
        "v_mfma_f32_16x16x32_bf16 v[118:121], v[30:33], v[140:143], v[118:121]\n"
        "s_mov_b64 exec, s[48:49]\n"
        "v_mov_b32 v190, 0xff800000\n"
        "s_mov_b64 exec, -1\n"
        "v_cvt_pk_bf16_f32 v122, v124, v125\n"
        "v_cvt_pk_bf16_f32 v123, v126, v127\n"
        "v_mul_f32 v189, v189, v117\n"
        "v_mfma_f32_16x16x32_bf16 v[132:135], v[42:45], v[140:143], 0\n"
        "v_max3_f32 v253, v253, v187, v188\n"
        "v_cvt_pk_bf16_f32 v124, v118, v119\n"
        "v_cvt_pk_bf16_f32 v125, v120, v121\n"
        "v_max3_f32 v253, v253, v189, v190\n"
        "v_mfma_f32_16x16x32_bf16 v[132:135], v[46:49], v[144:147], v[132:135]\n"
        "v_mfma_f32_16x16x32_bf16 v[132:135], v[50:53], v[94:97], v[132:135]\n"
        "v_permlane32_swap_b32 v252, v253\n"
        "v_max_f32 v252, v252, v253\n"
        "v_mov_b32 v253, v252\n"
        "v_mfma_f32_16x16x32_bf16 v[118:121], v[62:65], v[94:97], 0\n"
        "v_mfma_f32_16x16x32_bf16 v[118:121], v[58:61], v[144:147], v[118:121]\n"
        "v_permlane16_swap_b32 v252, v253\n"
        "v_max_f32 v252, v252, v253\n"
        "v_mov_b32 v253, v252\n"
        "v_cvt_pk_bf16_f32 v192, v132, v133\n"
        "v_mfma_f32_16x16x32_bf16 v[118:121], v[54:57], v[140:143], v[118:121]\n"
        "v_cvt_pk_bf16_f32 v193, v134, v135\n"
        "v_permlane32_swap_b32 v252, v253\n"
        "s_waitcnt lgkmcnt(0)\n"
        "v_mul_f32 v252, v252, v254\n"
        "v_mfma_f32_16x16x32_bf16 v[132:135], v[66:69], v[140:143], 0\n"
        "v_mul_f32 v253, v253, v251\n"
        "v_fma_f32 v136, v136, v254, -v252\n"
        "v_fma_f32 v137, v137, v254, -v252\n"
        "v_cvt_pk_bf16_f32 v194, v118, v119\n"
        "v_cvt_pk_bf16_f32 v195, v120, v121\n"
        "v_mfma_f32_16x16x32_bf16 v[132:135], v[70:73], v[144:147], v[132:135]\n"
        "v_fma_f32 v98, v98, v251, -v253\n"
        "v_fma_f32 v99, v99, v251, -v253\n"
        "v_exp_f32 v136, v136\n"
        "v_fma_f32 v138, v138, v254, -v252\n"
        "v_mfma_f32_16x16x32_bf16 v[132:135], v[74:77], v[94:97], v[132:135]\n"
        "v_exp_f32 v137, v137\n"
        "v_exp_f32 v98, v98\n"
        "v_fma_f32 v100, v100, v251, -v253\n"
        "v_mfma_f32_16x16x32_bf16 v[118:121], v[86:89], v[94:97], 0\n"
        "v_exp_f32 v99, v99\n"
        "v_exp_f32 v138, v138\n"
        "v_fma_f32 v139, v139, v254, -v252\n"
        "v_mfma_f32_16x16x32_bf16 v[118:121], v[82:85], v[144:147], v[118:121]\n"
        "v_cvt_pk_bf16_f32 v196, v132, v133\n"
        "v_cvt_pk_bf16_f32 v197, v134, v135\n"
        "v_exp_f32 v100, v100\n"
        "v_fma_f32 v101, v101, v251, -v253\n"
        "v_mfma_f32_16x16x32_bf16 v[118:121], v[78:81], v[140:143], v[118:121]\n"
        "v_add_f32 v249, v136, v137\n"
        "v_exp_f32 v139, v139\n"
        "v_fma_f32 v148, v148, v254, -v252\n"
        "v_add_f32 v248, v98, v99\n"
        "v_mfma_f32_16x16x32_bf16 v[132:135], v[176:179], v[122:125], 0\n"
        "v_exp_f32 v101, v101\n"
        "v_fma_f32 v128, v128, v251, -v253\n"
        "v_add_f32 v249, v249, v138\n"
        "v_cvt_pk_bf16_f32 v198, v118, v119\n"
        "v_mfma_f32_16x16x32_bf16 v[132:135], v[236:239], v[192:195], v[132:135]\n"
        "v_cvt_pk_bf16_f32 v199, v120, v121\n"
        "v_exp_f32 v148, v148\n"
        "v_fma_f32 v149, v149, v254, -v252\n"
        "v_add_f32 v248, v248, v100\n"
        "v_mfma_f32_16x16x32_bf16 v[132:135], v[244:247], v[196:199], v[132:135]\n"
        "v_exp_f32 v128, v128\n"
        "v_fma_f32 v129, v129, v251, -v253\n"
        "v_add_f32 v249, v249, v139\n"
        "v_fma_f32 v150, v150, v254, -v252\n"
        "v_mfma_f32_16x16x32_bf16 v[118:121], v[102:105], v[196:199], 0\n"
        "v_exp_f32 v149, v149\n"
        "v_add_f32 v248, v248, v101\n"
        "v_exp_f32 v129, v129\n"
        "v_mfma_f32_16x16x32_bf16 v[118:121], v[180:183], v[192:195], v[118:121]\n"
        "v_mul_f32 v132, v132, v106\n"
        "v_mul_f32 v133, v133, v107\n"
        "v_mul_f32 v134, v134, v108\n"
        "v_mul_f32 v135, v135, v109\n"
        "v_max3_f32 v243, v132, v133, v134\n"
        "v_mfma_f32_16x16x32_bf16 v[118:121], v[154:157], v[122:125], v[118:121]\n"
        "v_fma_f32 v130, v130, v251, -v253\n"
        "v_add_f32 v249, v249, v148\n"
        "v_exp_f32 v150, v150\n"
        "v_fma_f32 v151, v151, v254, -v252\n"
        "v_mfma_f32_16x16x32_bf16 v[200:203], v[140:143], v[122:125], 0\n"
        "ds_read_b128 v[204:207], %15\n"
        "v_add_f32 v248, v248, v128\n"
        "v_exp_f32 v130, v130\n"
        "v_mul_f32 v118, v118, v110\n"
        "v_mfma_f32_16x16x32_bf16 v[200:203], v[144:147], v[192:195], v[200:203]\n"
        "ds_read_b128 v[208:211], %15 offset:64\n"
        "v_mul_f32 v119, v119, v111\n"
        "v_mul_f32 v120, v120, v112\n"
        "v_max3_f32 v243, v243, v135, v118\n"
        "v_mul_f32 v121, v121, v113\n"
        "v_mfma_f32_16x16x32_bf16 v[200:203], v[94:97], v[196:199], v[200:203]\n"
        "ds_read_b128 v[212:215], %15 offset:128\n"
        "v_max3_f32 v243, v243, v119, v120\n"
        "v_fma_f32 v131, v131, v251, -v253\n"
        "v_add_f32 v249, v249, v149\n"
        "v_fma_f32 v170, v170, v254, -v252\n"
        "v_mfma_f32_16x16x32_bf16 v[216:219], v[158:161], v[196:199], 0\n"
        "v_exp_f32 v151, v151\n"
        "v_add_f32 v248, v248, v129\n"
        "v_mul_f32 v200, v200, v114\n"
        "v_mul_f32 v201, v201, v115\n"
        "v_mfma_f32_16x16x32_bf16 v[216:219], v[162:165], v[192:195], v[216:219]\n"
        "v_mul_f32 v202, v202, v116\n"
        "v_max3_f32 v243, v243, v121, v200\n"
        "v_mul_f32 v203, v203, v117\n"
        "v_max3_f32 v243, v243, v201, v202\n"
        "v_fma_f32 v186, v186, v251, -v253\n"
        "v_mfma_f32_16x16x32_bf16 v[216:219], v[166:169], v[122:125], v[216:219]\n"
        "s_waitcnt lgkmcnt(0)\n"
        "v_exp_f32 v131, v131\n"
        "v_add_f32 v249, v249, v150\n"
        "v_exp_f32 v170, v170\n"
        "v_mfma_f32_16x16x32_bf16 v[122:125], v[204:207], v[176:179], 0\n"
        "v_add_u32 v235, s30, %10\n"
        "v_fma_f32 v171, v171, v254, -v252\n"
        "v_add_f32 v248, v248, v130\n"
        "v_mul_f32 v216, s58, v216\n"
        "v_fma_f32 v187, v187, v251, -v253\n"
        "v_mfma_f32_16x16x32_bf16 v[122:125], v[208:211], v[236:239], v[122:125]\n"
        "s_mov_b64 exec, s[48:49]\n"
        "v_mov_b32 v216, 0xff800000\n"
        "s_mov_b64 exec, -1\n"
        "v_max3_f32 v243, v243, v203, v216\n"
        "v_exp_f32 v186, v186\n"
        "v_mfma_f32_16x16x32_bf16 v[122:125], v[212:215], v[244:247], v[122:125]\n"
        "v_add_f32 v249, v249, v151\n"
        "v_exp_f32 v171, v171\n"
        "v_fma_f32 v172, v172, v254, -v252\n"
        "v_add_f32 v248, v248, v131\n"
        "v_mfma_f32_16x16x32_bf16 v[192:195], v[212:215], v[102:105], 0\n"
        "v_exp_f32 v187, v187\n"
        "v_fma_f32 v188, v188, v251, -v253\n"
        "v_add_f32 v249, v249, v170\n"
        "v_fma_f32 v173, v173, v254, -v252\n"
        "v_mfma_f32_16x16x32_bf16 v[192:195], v[208:211], v[180:183], v[192:195]\n"
        "v_exp_f32 v172, v172\n"
        "v_add_f32 v248, v248, v186\n"
        "v_exp_f32 v188, v188\n"
        "v_mfma_f32_16x16x32_bf16 v[192:195], v[204:207], v[154:157], v[192:195]\n"
        "v_fma_f32 v189, v189, v251, -v253\n"
        "v_add_f32 v249, v249, v171\n"
        "v_exp_f32 v173, v173\n"
        "v_fma_f32 v184, v184, v254, -v252\n"
        "v_mfma_f32_16x16x32_bf16 v[124:127], v[204:207], v[140:143], 0\n"
        "v_add_f32 v248, v248, v187\n"
        "v_exp_f32 v189, v189\n"
        "v_fma_f32 v190, v190, v251, -v253\n"
        "v_cndmask_b32_e64 v254, v122, v192, s[50:51]\n"
        "v_mfma_f32_16x16x32_bf16 v[124:127], v[208:211], v[144:147], v[124:127]\n"
        "ds_read_b128 v[192:195], %0 offset:9216\n"
        "v_add_f32 v249, v249, v172\n"
        "v_exp_f32 v184, v184\n"
        "v_add_f32 v248, v248, v188\n"
        "v_mfma_f32_16x16x32_bf16 v[124:127], v[212:215], v[94:97], v[124:127]\n"
        "ds_read_b128 v[196:199], %0 offset:10240\n"
        "v_exp_f32 v190, v190\n"
        "v_add_f32 v249, v249, v173\n"
        "v_add_f32 v248, v248, v189\n"
        "v_mfma_f32_16x16x32_bf16 v[218:221], v[212:215], v[158:161], 0\n"
        "v_add_f32 v249, v249, v184\n"
        "v_add_f32 v248, v248, v190\n"
        "v_mul_f32 v136, v136, v106\n"
        "v_cndmask_b32_e64 v254, v254, v124, s[52:53]\n"
        "v_mul_f32 v137, v137, v107\n"
        "v_mfma_f32_16x16x32_bf16 v[218:221], v[208:211], v[162:165], v[218:221]\n"
        "v_permlane32_swap_b32 v249, v248\n"
        "v_add_f32 v249, v249, v248\n"
        "v_mov_b32 v253, v249\n"
        "v_mul_f32 v138, v138, v108\n"
        "v_mfma_f32_16x16x32_bf16 v[218:221], v[204:207], v[166:169], v[218:221]\n"
        "v_permlane16_swap_b32 v249, v253\n"
        "v_add_f32 v249, v249, v253\n"
        "v_rcp_f32 v249, v249\n"
        "v_mfma_f32_16x16x32_bf16 v[122:125], v[176:179], v[90:93], 0\n"
        "s_waitcnt lgkmcnt(0)\n"
        "v_mov_b32 v248, v249\n"
        "v_mul_f32 v139, v139, v109\n"
        "v_mul_f32 v148, v148, v110\n"
        "v_cndmask_b32_e64 v254, v254, v218, s[54:55]\n"
        "v_mul_f32 v254, v254, v255\n"
        "v_mfma_f32_16x16x32_bf16 v[122:125], v[236:239], v[192:195], v[122:125]\n"
        "s_mov_b64 exec, s[56:57]\n"
        "v_mov_b32 v254, 0xff800000\n"
        "s_mov_b64 exec, -1\n"
        "v_permlane32_swap_b32 v249, v248\n"
        "v_mul_f32 v149, v149, v111\n"
        "v_mfma_f32_16x16x32_bf16 v[122:125], v[244:247], v[196:199], v[122:125]\n"
        "v_max_f32_dpp v253, v254, v254 row_ror:8 row_mask:0xf bank_mask:0xf\n"
        "v_mul_f32 v150, v150, v112\n"
        "v_mul_f32 v151, v151, v113\n"
        "v_max_f32_dpp v253, v253, v253 row_ror:4 row_mask:0xf bank_mask:0xf\n"
        "v_mul_f32 v136, v136, v249\n"
        "v_mfma_f32_16x16x32_bf16 v[158:161], v[102:105], v[196:199], 0\n"
        "ds_read_b128 v[162:165], %0 offset:13312\n"
        "v_max_f32_dpp v253, v253, v253 row_ror:2 row_mask:0xf bank_mask:0xf\n"
        "v_cvt_pk_bf16_f32 v204, v122, v123\n"
        "v_cvt_pk_bf16_f32 v205, v124, v125\n"
        "v_max_f32_dpp v253, v253, v253 row_ror:1 row_mask:0xf bank_mask:0xf\n"
        "v_mfma_f32_16x16x32_bf16 v[158:161], v[180:183], v[192:195], v[158:161]\n"
        "ds_read_b128 v[122:125], %0 offset:12288\n"
        "v_permlane32_swap_b32 v243, v253\n"
        "v_max_f32 v243, v243, v253\n"
        "v_mov_b32 v253, v243\n"
        "v_mfma_f32_16x16x32_bf16 v[158:161], v[154:157], v[90:93], v[158:161]\n"
        "ds_read_b128 v[166:169], %0 offset:11264\n"
        "v_permlane16_swap_b32 v243, v253\n"
        "v_max_f32 v243, v243, v253\n"
        "v_mov_b32 v253, v243\n"
        "v_mfma_f32_16x16x32_bf16 v[208:211], v[140:143], v[90:93], 0\n"
        "v_mul_f32 v137, v137, v249\n"
        "v_permlane32_swap_b32 v243, v253\n"
        "v_mul_f32 v243, v243, v250\n"
        "v_cvt_pk_bf16_f32 v206, v158, v159\n"
        "v_mfma_f32_16x16x32_bf16 v[208:211], v[144:147], v[192:195], v[208:211]\n"
        "v_cvt_pk_bf16_f32 v207, v160, v161\n"
        "v_fma_f32 v132, v132, v250, -v243\n"
        "v_fma_f32 v133, v133, v250, -v243\n"
        "v_exp_f32 v132, v132\n"
        "v_mfma_f32_16x16x32_bf16 v[208:211], v[94:97], v[196:199], v[208:211]\n"
        "s_waitcnt lgkmcnt(0)\n"
        "v_exp_f32 v133, v133\n"
        "v_fma_f32 v134, v134, v250, -v243\n"
        "v_exp_f32 v134, v134\n"
        "v_mfma_f32_16x16x32_bf16 v[158:161], v[94:97], v[162:165], 0\n"
        "ds_read2_b32 v[194:195], v235 offset0:0 offset1:0\n"
        "v_fma_f32 v135, v135, v250, -v243\n"
        "v_add_f32 v252, v132, v133\n"
        "v_cvt_pk_bf16_f32 v192, v208, v209\n"
        "v_cvt_pk_bf16_f32 v193, v210, v211\n"
        "v_mfma_f32_16x16x32_bf16 v[158:161], v[144:147], v[122:125], v[158:161]\n"
        "v_exp_f32 v135, v135\n"
        "v_fma_f32 v118, v118, v250, -v243\n"
        "v_add_f32 v252, v252, v134\n"
        "v_fma_f32 v119, v119, v250, -v243\n"
        "v_mfma_f32_16x16x32_bf16 v[158:161], v[140:143], v[166:169], v[158:161]\n"
        "v_exp_f32 v118, v118\n"
        "v_add_f32 v252, v252, v135\n"
        "v_exp_f32 v119, v119\n"
        "v_mfma_f32_16x16x32_bf16 v[196:199], v[154:157], v[166:169], 0\n"
        "ds_read_b128 v[208:211], %0 offset:14336\n"
        "v_fma_f32 v120, v120, v250, -v243\n"
        "v_add_f32 v252, v252, v118\n"
        "v_exp_f32 v120, v120\n"
        "v_mfma_f32_16x16x32_bf16 v[196:199], v[180:183], v[122:125], v[196:199]\n"
        "ds_read_b128 v[212:215], %0 offset:15360\n"
        "v_cvt_pk_bf16_f32 v218, v158, v159\n"
        "v_cvt_pk_bf16_f32 v219, v160, v161\n"
        "v_fma_f32 v121, v121, v250, -v243\n"
        "v_sub_f32 v254, v254, v253\n"
        "v_mfma_f32_16x16x32_bf16 v[196:199], v[102:105], v[162:165], v[196:199]\n"
        "ds_read_b128 v[158:161], %0 offset:16384\n"
        "v_add_f32 v252, v252, v119\n"
        "v_exp_f32 v121, v121\n"
        "v_fma_f32 v200, v200, v250, -v243\n"
        "v_mfma_f32_16x16x32_bf16 v[222:225], v[244:247], v[162:165], 0\n"
        "v_mul_f32 v254, s58, v254\n"
        "v_add_f32 v252, v252, v120\n"
        "v_exp_f32 v200, v200\n"
        "v_cvt_pk_bf16_f32 v164, v196, v197\n"
        "v_mfma_f32_16x16x32_bf16 v[222:225], v[236:239], v[122:125], v[222:225]\n"
        "v_cvt_pk_bf16_f32 v165, v198, v199\n"
        "v_fma_f32 v201, v201, v250, -v243\n"
        "v_exp_f32 v254, v254\n"
        "v_add_f32 v252, v252, v121\n"
        "v_mfma_f32_16x16x32_bf16 v[222:225], v[176:179], v[166:169], v[222:225]\n"
        "s_waitcnt lgkmcnt(0)\n"
        "v_exp_f32 v201, v201\n"
        "v_fma_f32 v202, v202, v250, -v243\n"
        "v_add_f32 v252, v252, v200\n"
        "v_fma_f32 v203, v203, v250, -v243\n"
        "v_mfma_f32_16x16x32_bf16 v[122:125], v[176:179], v[208:211], 0\n"
        "ds_read2_b32 v[220:221], v235 offset0:1 offset1:1\n"
        "v_exp_f32 v202, v202\n"
        "v_cvt_pk_bf16_f32 v162, v222, v223\n"
        "v_cvt_pk_bf16_f32 v163, v224, v225\n"
        "v_mfma_f32_16x16x32_bf16 v[122:125], v[236:239], v[212:215], v[122:125]\n"
        "v_add_f32_dpp v253, v254, v254 row_ror:8 row_mask:0xf bank_mask:0xf\n"
        "v_add_f32 v252, v252, v201\n"
        "v_exp_f32 v203, v203\n"
        "v_fma_f32 v216, v216, v250, -v243\n"
        "v_mfma_f32_16x16x32_bf16 v[122:125], v[244:247], v[158:161], v[122:125]\n"
        "v_add_f32_dpp v253, v253, v253 row_ror:4 row_mask:0xf bank_mask:0xf\n"
        "v_add_f32 v252, v252, v202\n"
        "v_exp_f32 v216, v216\n"
        "v_mul_f32 v138, v138, v249\n"
        "v_mfma_f32_16x16x32_bf16 v[166:169], v[102:105], v[158:161], 0\n"
        "ds_read_b128 v[196:199], %0 offset:19456\n"
        "v_mul_f32 v139, v139, v249\n"
        "v_mul_f32 v148, v148, v249\n"
        "v_cvt_pk_bf16_f32 v222, v122, v123\n"
        "v_cvt_pk_bf16_f32 v223, v124, v125\n"
        "v_mfma_f32_16x16x32_bf16 v[166:169], v[180:183], v[212:215], v[166:169]\n"
        "ds_read_b128 v[122:125], %0 offset:18432\n"
        "v_mul_f32 v149, v149, v249\n"
        "v_mul_f32 v150, v150, v249\n"
        "v_mul_f32 v151, v151, v249\n"
        "v_add_f32_dpp v253, v253, v253 row_ror:2 row_mask:0xf bank_mask:0xf\n"
        "v_mfma_f32_16x16x32_bf16 v[166:169], v[154:157], v[208:211], v[166:169]\n"
        "ds_read_b128 v[226:229], %0 offset:17408\n"
        "v_add_f32 v252, v252, v203\n"
        "v_cvt_pk_bf16_f32 v230, v136, v137\n"
        "v_cvt_pk_bf16_f32 v231, v138, v139\n"
        "v_cvt_pk_bf16_f32 v232, v148, v149\n"
        "v_mfma_f32_16x16x32_bf16 v[136:139], v[140:143], v[208:211], 0\n"
        "v_cvt_pk_bf16_f32 v233, v150, v151\n"
        "v_mul_f32 v170, v170, v114\n"
        "v_cvt_pk_bf16_f32 v224, v166, v167\n"
        "v_cvt_pk_bf16_f32 v225, v168, v169\n"
        "v_mul_f32 v171, v171, v115\n"
        "v_mfma_f32_16x16x32_bf16 v[136:139], v[144:147], v[212:215], v[136:139]\n"
        "v_mul_f32 v172, v172, v116\n"
        "v_mul_f32 v173, v173, v117\n"
        "v_mul_f32 v184, s58, v184\n"
        "v_add_f32_dpp v253, v253, v253 row_ror:1 row_mask:0xf bank_mask:0xf\n"
        "v_add_f32 v252, v252, v216\n"
        "v_mfma_f32_16x16x32_bf16 v[136:139], v[94:97], v[158:161], v[136:139]\n"
        "s_waitcnt lgkmcnt(0)\n"
        "v_mul_f32 v170, v170, v249\n"
        "v_mul_f32 v171, v171, v249\n"
        "v_mul_f32 v172, v172, v249\n"
        "v_mul_f32 v173, v173, v249\n"
        "v_mul_f32 v184, v184, v249\n"
        "v_mfma_f32_16x16x32_bf16 v[148:151], v[94:97], v[196:199], 0\n"
        "ds_read2_b32 v[160:161], v235 offset0:2 offset1:2\n"
        "v_cvt_pk_bf16_f32 v158, v136, v137\n"
        "v_cvt_pk_bf16_f32 v159, v138, v139\n"
        "v_permlane32_swap_b32 v252, v253\n"
        "v_mfma_f32_16x16x32_bf16 v[148:151], v[144:147], v[122:125], v[148:151]\n"
        "v_cvt_pk_bf16_f32 v136, v170, v171\n"
        "v_cvt_pk_bf16_f32 v137, v172, v173\n"
        "v_cvt_pk_bf16_f32 v138, v184, 0\n"
        "v_add_f32 v252, v252, v253\n"
        "v_mov_b32 v253, v252\n"
        "v_mfma_f32_16x16x32_bf16 v[148:151], v[140:143], v[226:229], v[148:151]\n"
        "v_mul_f32 v254, v254, v255\n"
        "v_permlane16_swap_b32 v252, v253\n"
        "v_add_f32 v252, v252, v253\n"
        "v_mul_f32 v98, v98, v106\n"
        "v_mfma_f32_16x16x32_bf16 v[166:169], v[154:157], v[226:229], 0\n"
        "ds_read_b128 v[170:173], %0 offset:20480\n"
        "v_rcp_f32 v252, v252\n"
        "v_mul_f32 v99, v99, v107\n"
        "v_cvt_pk_bf16_f32 v208, v148, v149\n"
        "v_mfma_f32_16x16x32_bf16 v[166:169], v[180:183], v[122:125], v[166:169]\n"
        "ds_read_b128 v[212:215], %0 offset:21504\n"
        "v_cvt_pk_bf16_f32 v209, v150, v151\n"
        "v_mov_b32 v253, v252\n"
        "v_mul_f32 v100, v100, v108\n"
        "v_mul_f32 v101, v101, v109\n"
        "v_mfma_f32_16x16x32_bf16 v[166:169], v[102:105], v[196:199], v[166:169]\n"
        "ds_read_b128 v[148:151], %0 offset:22528\n"
        "v_permlane32_swap_b32 v252, v253\n"
        "v_mul_f32 v254, v254, v253\n"
        "v_cvt_pk_bf16_f32 v254, v254, 0\n"
        "v_mfma_f32_16x16x32_bf16 v[240:243], v[244:247], v[196:199], 0\n"
        "ds_write_b16 %3, v254\n"
        "v_mul_f32 v128, v128, v110\n"
        "v_mul_f32 v129, v129, v111\n"
        "v_mfma_f32_16x16x32_bf16 v[240:243], v[236:239], v[122:125], v[240:243]\n"
        "v_cvt_pk_bf16_f32 v198, v166, v167\n"
        "v_cvt_pk_bf16_f32 v199, v168, v169\n"
        "v_mul_f32 v130, v130, v112\n"
        "v_mul_f32 v131, v131, v113\n"
        "v_mul_f32 v98, v98, v248\n"
        "v_mfma_f32_16x16x32_bf16 v[240:243], v[176:179], v[226:229], v[240:243]\n"
        "s_waitcnt lgkmcnt(1)\n"
        "v_mul_f32 v99, v99, v248\n"
        "v_mul_f32 v100, v100, v248\n"
        "v_mul_f32 v101, v101, v248\n"
        "v_mul_f32 v128, v128, v248\n"
        "v_mul_f32 v129, v129, v248\n"
        "v_mfma_f32_16x16x32_bf16 v[122:125], v[176:179], v[170:173], 0\n"
        "ds_read2_b32 v[210:211], v235 offset0:3 offset1:3\n"
        "v_cvt_pk_bf16_f32 v196, v240, v241\n"
        "v_cvt_pk_bf16_f32 v197, v242, v243\n"
        "v_mul_f32 v130, v130, v248\n"
        "v_mul_f32 v131, v131, v248\n"
        "v_mfma_f32_16x16x32_bf16 v[122:125], v[236:239], v[212:215], v[122:125]\n"
        "v_cvt_pk_bf16_f32 v226, v98, v99\n"
        "v_cvt_pk_bf16_f32 v227, v100, v101\n"
        "v_cvt_pk_bf16_f32 v228, v128, v129\n"
        "v_cvt_pk_bf16_f32 v229, v130, v131\n"
        "v_mul_f32 v186, v186, v114\n"
        "v_mfma_f32_16x16x32_bf16 v[122:125], v[244:247], v[148:151], v[122:125]\n"
        "v_mul_f32 v187, v187, v115\n"
        "v_mul_f32 v188, v188, v116\n"
        "v_mul_f32 v189, v189, v117\n"
        "v_mul_f32 v190, s58, v190\n"
        "v_mul_f32 v186, v186, v248\n"
        "v_mfma_f32_16x16x32_bf16 v[98:101], v[102:105], v[148:151], 0\n"
        "ds_read_b128 v[126:129], %0 offset:25600\n"
        "v_mul_f32 v187, v187, v248\n"
        "v_cvt_pk_bf16_f32 v240, v122, v123\n"
        "v_cvt_pk_bf16_f32 v241, v124, v125\n"
        "v_mul_f32 v188, v188, v248\n"
        "v_mfma_f32_16x16x32_bf16 v[98:101], v[180:183], v[212:215], v[98:101]\n"
        "ds_read_b128 v[122:125], %0 offset:24576\n"
        "v_mul_f32 v189, v189, v248\n"
        "v_mul_f32 v190, v190, v248\n"
        "v_cvt_pk_bf16_f32 v248, v186, v187\n"
        "v_cvt_pk_bf16_f32 v249, v188, v189\n"
        "v_mfma_f32_16x16x32_bf16 v[98:101], v[154:157], v[170:173], v[98:101]\n"
        "ds_read_b128 v[166:169], %0 offset:23552\n"
        "v_cvt_pk_bf16_f32 v250, v190, 0\n"
        "v_mul_f32 v132, v132, v106\n"
        "v_mul_f32 v133, v133, v107\n"
        "v_mul_f32 v134, v134, v108\n"
        "v_mfma_f32_16x16x32_bf16 v[184:187], v[140:143], v[170:173], 0\n"
        "v_mul_f32 v135, v135, v109\n"
        "v_mul_f32 v118, v118, v110\n"
        "v_cvt_pk_bf16_f32 v242, v98, v99\n"
        "v_cvt_pk_bf16_f32 v243, v100, v101\n"
        "v_mul_f32 v119, v119, v111\n"
        "v_mfma_f32_16x16x32_bf16 v[184:187], v[144:147], v[212:215], v[184:187]\n"
        "ds_read_b64 v[130:131], %7\n"
        "v_mul_f32 v120, v120, v112\n"
        "v_mul_f32 v121, v121, v113\n"
        "v_mul_f32 v132, v132, v252\n"
        "v_mul_f32 v133, v133, v252\n"
        "v_mfma_f32_16x16x32_bf16 v[184:187], v[94:97], v[148:151], v[184:187]\n"
        "s_waitcnt lgkmcnt(1)\n"
        "v_mul_f32 v134, v134, v252\n"
        "v_mul_f32 v135, v135, v252\n"
        "v_mul_f32 v118, v118, v252\n"
        "v_mul_f32 v119, v119, v252\n"
        "v_mul_f32 v120, v120, v252\n"
        "v_mfma_f32_16x16x32_bf16 v[98:101], v[94:97], v[126:129], 0\n"
        "ds_read2_b32 v[112:113], v235 offset0:4 offset1:4\n"
        "ds_read_b64 v[148:149], %7 offset:16\n"
        "v_cvt_pk_bf16_f32 v110, v184, v185\n"
        "v_cvt_pk_bf16_f32 v111, v186, v187\n"
        "v_mfma_f32_16x16x32_bf16 v[98:101], v[144:147], v[122:125], v[98:101]\n"
        "v_mul_f32 v121, v121, v252\n"
        "v_cvt_pk_bf16_f32 v94, v132, v133\n"
        "v_cvt_pk_bf16_f32 v95, v134, v135\n"
        "v_cvt_pk_bf16_f32 v96, v118, v119\n"
        "v_cvt_pk_bf16_f32 v97, v120, v121\n"
        "v_mfma_f32_16x16x32_bf16 v[98:101], v[140:143], v[166:169], v[98:101]\n"
        "ds_read_b64 v[132:133], %7 offset:32\n"
        "v_mul_f32 v200, v200, v114\n"
        "v_mul_f32 v201, v201, v115\n"
        "v_mul_f32 v202, v202, v116\n"
        "v_mul_f32 v203, v203, v117\n"
        "v_mfma_f32_16x16x32_bf16 v[106:109], v[154:157], v[166:169], 0\n"
        "ds_read_b64 v[212:213], %1\n"
        "v_mul_f32 v216, s58, v216\n"
        "v_cvt_pk_bf16_f32 v114, v98, v99\n"
        "v_cvt_pk_bf16_f32 v115, v100, v101\n"
        "v_mul_f32 v200, v200, v252\n"
        "v_mfma_f32_16x16x32_bf16 v[106:109], v[180:183], v[122:125], v[106:109]\n"
        "ds_read_b64 v[134:135], %7 offset:48\n"
        "v_mul_f32 v201, v201, v252\n"
        "v_mul_f32 v202, v202, v252\n"
        "v_mul_f32 v203, v203, v252\n"
        "v_mul_f32 v216, v216, v252\n"
        "v_mfma_f32_16x16x32_bf16 v[106:109], v[102:105], v[126:129], v[106:109]\n"
        "v_cvt_pk_bf16_f32 v144, v200, v201\n"
        "v_cvt_pk_bf16_f32 v145, v202, v203\n"
        "v_cvt_pk_bf16_f32 v146, v216, 0\n"
        "v_mfma_f32_16x16x32_bf16 v[98:101], v[244:247], v[126:129], 0\n"
        "ds_read_b64 v[140:141], %7 offset:64\n"
        "v_mfma_f32_16x16x32_bf16 v[98:101], v[236:239], v[122:125], v[98:101]\n"
        "v_mfma_f32_16x16x32_bf16 v[98:101], v[176:179], v[166:169], v[98:101]\n"
        "ds_read_b64 v[142:143], %7 offset:80\n"
        "v_mov_b32 v139, 0\n"
        "v_cvt_pk_bf16_f32 v150, v106, v107\n"
        "v_cvt_pk_bf16_f32 v151, v108, v109\n"
        "s_waitcnt lgkmcnt(2)\n"
        "v_mfma_f32_4x4x4_16b_bf16 v[102:105], v[130:131], v[212:213], 0\n"
        "ds_read2_b32 v[116:117], v235 offset0:5 offset1:5\n"
        "v_mfma_f32_4x4x4_16b_bf16 v[106:109], v[148:149], v[212:213], 0\n"
        "v_mfma_f32_4x4x4_16b_bf16 v[118:121], v[132:133], v[212:213], 0\n"
        "v_mfma_f32_4x4x4_16b_bf16 v[122:125], v[134:135], v[212:213], 0\n"
        "s_waitcnt lgkmcnt(2)\n"
        "v_mfma_f32_4x4x4_16b_bf16 v[126:129], v[140:141], v[212:213], 0\n"
        "s_waitcnt lgkmcnt(1)\n"
        "v_mfma_f32_4x4x4_16b_bf16 v[130:133], v[142:143], v[212:213], 0\n"
        "v_cvt_pk_bf16_f32 v148, v98, v99\n"
        "v_cvt_pk_bf16_f32 v149, v100, v101\n"
        "v_mfma_f32_16x16x32_bf16 v[102:105], v[230:233], v[204:207], v[102:105]\n"
        "v_mfma_f32_16x16x32_bf16 v[102:105], v[136:139], v[192:195], v[102:105]\n"
        "v_mfma_f32_16x16x32_bf16 v[106:109], v[136:139], v[218:221], v[106:109]\n"
        "v_mfma_f32_16x16x32_bf16 v[106:109], v[230:233], v[162:165], v[106:109]\n"
        "v_mfma_f32_16x16x32_bf16 v[118:121], v[230:233], v[222:225], v[118:121]\n"
        "v_mfma_f32_16x16x32_bf16 v[118:121], v[136:139], v[158:161], v[118:121]\n"
        "v_mfma_f32_16x16x32_bf16 v[122:125], v[136:139], v[208:211], v[122:125]\n"
        "ds_read_b128 v[152:155], %4\n"
        "v_mfma_f32_16x16x32_bf16 v[122:125], v[230:233], v[196:199], v[122:125]\n"
        "ds_read_b128 v[166:169], %4 offset:64\n"
        "v_mfma_f32_16x16x32_bf16 v[126:129], v[230:233], v[240:243], v[126:129]\n"
        "v_mfma_f32_16x16x32_bf16 v[126:129], v[136:139], v[110:113], v[126:129]\n"
        "ds_read_b64 v[140:141], %7 offset:3328\n"
        "v_cvt_pk_bf16_f32 v134, v102, v106\n"
        "v_cvt_pk_bf16_f32 v170, v103, v107\n"
        "v_cvt_pk_bf16_f32 v174, v104, v108\n"
        "s_waitcnt lgkmcnt(3)\n"
        "v_mfma_f32_16x16x32_bf16 v[130:133], v[136:139], v[114:117], v[130:133]\n"
        "v_cvt_pk_bf16_f32 v135, v118, v122\n"
        "v_cvt_pk_bf16_f32 v171, v119, v123\n"
        "v_cvt_pk_bf16_f32 v175, v120, v124\n"
        "v_cvt_pk_bf16_f32 v178, v105, v109\n"
        "v_cvt_pk_bf16_f32 v179, v121, v125\n"
        "v_mfma_f32_16x16x32_bf16 v[130:133], v[230:233], v[148:151], v[130:133]\n"
        "ds_read_b64 v[124:125], %7 offset:3344\n"
        "s_waitcnt lgkmcnt(2)\n"
        "v_mfma_f32_16x16x32_bf16 v[98:101], v[152:155], v[204:207], 0\n"
        "v_mfma_f32_16x16x32_bf16 v[98:101], v[166:169], v[192:195], v[98:101]\n"
        "ds_read_b64 v[138:139], %7 offset:3360\n"
        "v_mfma_f32_16x16x32_bf16 v[102:105], v[166:169], v[218:221], 0\n"
        "v_mfma_f32_16x16x32_bf16 v[102:105], v[152:155], v[162:165], v[102:105]\n"
        "ds_read_b64 v[142:143], %7 offset:3376\n"
        "v_cvt_pk_bf16_f32 v136, v126, v130\n"
        "v_cvt_pk_bf16_f32 v172, v127, v131\n"
        "v_cvt_pk_bf16_f32 v176, v128, v132\n"
        "v_cvt_pk_bf16_f32 v180, v129, v133\n"
        "v_mfma_f32_16x16x32_bf16 v[118:121], v[152:155], v[222:225], 0\n"
        "s_add_u32 s31, s31, 1\n"
        "v_mfma_f32_16x16x32_bf16 v[118:121], v[166:169], v[158:161], v[118:121]\n"
        "ds_read_b64 v[126:127], %7 offset:3392\n"
        "v_cvt_pk_bf16_f32 v182, v98, v102\n"
        "s_add_u32 s37, s35, 0\n"
        "s_mul_i32 s68, s31, 3\n"
        "v_mfma_f32_16x16x32_bf16 v[102:105], v[166:169], v[208:211], 0\n"
        "s_lshr_b32 s38, s31, 3\n"
        "v_mfma_f32_16x16x32_bf16 v[102:105], v[152:155], v[196:199], v[102:105]\n"
        "ds_read_b64 v[128:129], %7 offset:3408\n"
        "s_add_u32 s38, s38, 11\n"
        "s_waitcnt lgkmcnt(2)\n"
        "v_mfma_f32_4x4x4_16b_bf16 v[98:101], v[140:141], v[212:213], 0\n"
        "buffer_store_dwordx3 v[134:136], %11, %22, s37 offen sc1 nt\n"
        "v_mfma_f32_4x4x4_16b_bf16 v[106:109], v[124:125], v[212:213], 0\n"
        "v_mfma_f32_4x4x4_16b_bf16 v[120:123], v[138:139], v[212:213], 0\n"
        "v_mfma_f32_4x4x4_16b_bf16 v[132:135], v[142:143], v[212:213], 0\n"
        "s_waitcnt lgkmcnt(1)\n"
        "v_mfma_f32_4x4x4_16b_bf16 v[136:139], v[126:127], v[212:213], 0\n"
        "s_waitcnt lgkmcnt(0)\n"
        "v_mfma_f32_4x4x4_16b_bf16 v[140:143], v[128:129], v[212:213], 0\n"
        "s_add_u32 s37, s35, 0\n"
        "buffer_store_dwordx3 v[170:172], %14, %22, s37 offen offset:768 sc1 nt\n"
        "s_bitcmp1_b32 s31, 0\n"
        "s_cselect_b32 s38, 3, s38\n"
        "s_add_u32 s37, s35, 98112\n"
        "s_add_u32 s68, s68, s38\n"
        "v_mfma_f32_16x16x32_bf16 v[124:127], v[152:155], v[240:243], 0\n"
        "s_cmp_lt_u32 s31, %24\n"
        "buffer_store_dwordx3 v[174:176], %11, %22, s37 offen offset:192 sc1 nt\n"
        "s_cselect_b32 s68, s68, 0\n"
        "s_add_u32 s37, s35, 98112\n"
        "v_mfma_f32_16x16x32_bf16 v[124:127], v[166:169], v[110:113], v[124:127]\n"
        "ds_read_b64 v[128:129], %7 offset:6656\n"
        "s_and_b32 s38, s31, 7\n"
        "v_mov_b32 v251, 0\n"
        "v_cvt_pk_bf16_f32 v183, v118, v102\n"
        "s_mul_i32 s29, s38, 192\n"
        "v_mfma_f32_16x16x32_bf16 v[102:105], v[166:169], v[114:117], 0\n"
        "s_mul_i32 s30, s38, 384\n"
        "buffer_store_dwordx3 v[178:180], %13, %22, s37 offen offset:960 sc1 nt\n"
        "s_add_u32 s34, s34, 832\n"
        "v_mfma_f32_16x16x32_bf16 v[102:105], v[152:155], v[148:151], v[102:105]\n"
        "ds_read_b64 v[118:119], %7 offset:6672\n"
        "s_cmp_eq_u32 s34, 8320\n"
        "v_mfma_f32_16x16x32_bf16 v[98:101], v[226:229], v[204:207], v[98:101]\n"
        "s_cselect_b32 s34, 0, s34\n"
        "v_mfma_f32_16x16x32_bf16 v[98:101], v[248:251], v[192:195], v[98:101]\n"
        "ds_read_b64 v[152:153], %7 offset:6688\n"
        "s_add_u32 s73, s34, %19\n"
        "v_mfma_f32_16x16x32_bf16 v[106:109], v[248:251], v[218:221], v[106:109]\n"
        "s_mul_i32 s38, s31, 1344\n"
        "v_cvt_pk_bf16_f32 v184, v124, v102\n"
        "v_mfma_f32_16x16x32_bf16 v[106:109], v[226:229], v[162:165], v[106:109]\n"
        "ds_read_b64 v[102:103], %7 offset:6704\n"
        "s_cmp_ge_u32 s31, s71\n"
        "v_mfma_f32_16x16x32_bf16 v[120:123], v[226:229], v[222:225], v[120:123]\n"
        "s_cselect_b32 s39, s70, s72\n"
        "v_mfma_f32_16x16x32_bf16 v[120:123], v[248:251], v[158:161], v[120:123]\n"
        "ds_read_b64 v[104:105], %7 offset:6720\n"
        "s_add_u32 s37, s35, 588672\n"
        "s_add_u32 s39, s39, s38\n"
        "v_mfma_f32_16x16x32_bf16 v[132:135], v[248:251], v[208:211], v[132:135]\n"
        "s_add_u32 s46, s37, 1344\n"
        "v_mfma_f32_16x16x32_bf16 v[132:135], v[226:229], v[196:199], v[132:135]\n"
        "ds_read_b64 v[154:155], %7 offset:6736\n"
        "v_add_u32 %7, s69, %7\n"
        "s_waitcnt lgkmcnt(2)\n"
        "v_mfma_f32_4x4x4_16b_bf16 v[124:127], v[128:129], v[212:213], 0\n"
        "buffer_store_dwordx3 v[182:184], %11, s[44:47], s37 offen offset:1152 sc1 nt\n"
        "v_mfma_f32_4x4x4_16b_bf16 v[128:131], v[118:119], v[212:213], 0\n"
        "v_mfma_f32_4x4x4_16b_bf16 v[168:171], v[152:153], v[212:213], 0\n"
        "v_mfma_f32_4x4x4_16b_bf16 v[172:175], v[102:103], v[212:213], 0\n"
        "s_waitcnt lgkmcnt(1)\n"
        "v_mfma_f32_4x4x4_16b_bf16 v[184:187], v[104:105], v[212:213], 0\n"
        "s_waitcnt lgkmcnt(0)\n"
        "v_mfma_f32_4x4x4_16b_bf16 v[188:191], v[154:155], v[212:213], 0\n"
        "v_xor_b32 v255, 4, %17\n"
        "v_mov_b32 v147, 0\n"
        "v_mfma_f32_16x16x32_bf16 v[136:139], v[226:229], v[240:243], v[136:139]\n"
        "s_mov_b64 exec, 1\n"
        "ds_inc_u32 %17, %1 offset:8\n"
        "s_mov_b64 exec, -1\n"
        "v_xor_b32 %17, 4, %17\n"
        "v_mfma_f32_16x16x32_bf16 v[136:139], v[248:251], v[110:113], v[136:139]\n"
        "v_mfma_f32_16x16x32_bf16 v[140:143], v[248:251], v[114:117], v[140:143]\n"
        "ds_read_b32 v254, v255\n"
        "v_mfma_f32_16x16x32_bf16 v[140:143], v[226:229], v[148:151], v[140:143]\n"
        "v_mfma_f32_16x16x32_bf16 v[124:127], v[94:97], v[204:207], v[124:127]\n"
        "v_mfma_f32_16x16x32_bf16 v[124:127], v[144:147], v[192:195], v[124:127]\n"
        "v_mfma_f32_16x16x32_bf16 v[128:131], v[144:147], v[218:221], v[128:131]\n"
        "v_mfma_f32_16x16x32_bf16 v[128:131], v[94:97], v[162:165], v[128:131]\n"
        "s_waitcnt lgkmcnt(0)\n"
        "v_readfirstlane_b32 s40, v254\n"
        "v_mfma_f32_16x16x32_bf16 v[168:171], v[94:97], v[222:225], v[168:171]\n"
        "s_cmp_lt_i32 s40, s68\n"
        "v_mfma_f32_16x16x32_bf16 v[168:171], v[144:147], v[158:161], v[168:171]\n"
        "s_cbranch_scc1 L22_%=\n"
        "L23_%=:\n"
        "ds_read_b128 v[176:179], %5\n"
        "ds_read_b128 v[154:157], %5 offset:3328\n"
        "v_mfma_f32_16x16x32_bf16 v[172:175], v[144:147], v[208:211], v[172:175]\n"
        "ds_read_b128 v[236:239], %5 offset:64\n"
        "ds_read_b128 v[180:183], %5 offset:3392\n"
        "ds_read_b128 v[244:247], %5 offset:128\n"
        "v_mfma_f32_16x16x32_bf16 v[172:175], v[94:97], v[196:199], v[172:175]\n"
        "ds_read_b128 v[102:105], %5 offset:3456\n"
        "v_mfma_f32_16x16x32_bf16 v[184:187], v[94:97], v[240:243], v[184:187]\n"
        "s_sub_u32 s69, 0, s69\n"
        "v_mfma_f32_16x16x32_bf16 v[184:187], v[144:147], v[110:113], v[184:187]\n"
        "s_mov_b32 s36, s35\n"
        "v_mfma_f32_16x16x32_bf16 v[188:191], v[144:147], v[114:117], v[188:191]\n"
        "s_mov_b32 s35, s39\n"
        "v_mfma_f32_16x16x32_bf16 v[188:191], v[94:97], v[148:151], v[188:191]\n"
        "s_setprio 0\n"
        "s_cmp_ge_u32 s31, %24\n"
        "s_cbranch_scc1 CEND_%=\n"
        "s_cmp_lg_u32 s29, 0\n"
        "s_cbranch_scc1 MTOP_%=\n"
        "s_branch MFULL_%=\n"
        :: "v"(vo[0]), "v"(vo[1]), "v"(vo[2]), "v"(vo[3]), "v"(vo[4]), "v"(vo[5]), "v"(vo[6]), "v"(vo[7]), "v"(vo[8]), "v"(vo[9]), "v"(vo[10]), "v"(vo[11]), "v"(vo[12]), "v"(vo[13]), "v"(vo[14]), "v"(vo[15]), "v"(vo[16]), "v"(vo[17]), "s"(so[0]), "s"(so[1]), "s"(so[2]), "s"(so[3]), "s"(srdo), "s"(outo), "s"(so[4]), "s"(so[5]), "s"(so[6]), "s"(so[7]) : CLOBBERS);
}

extern "C" void kernel_launch(void* const* d_in, const int* in_sizes, int n_in, void* d_out, int out_size, void* d_ws, size_t ws_size, hipStream_t stream) {
    (void)in_sizes; (void)n_in; (void)out_size; (void)ws_size;
    (void)hipFuncSetAttribute((const void*)swin_kernel, hipFuncAttributeMaxDynamicSharedMemorySize, LDS_BYTES);
    swin_kernel<<<GRID, 512, LDS_BYTES, stream>>>((const char*)d_in[0], (const char*)d_in[1], (const char*)d_in[2], (const char*)d_in[3], (const char*)d_in[4], (char*)d_out, (const char*)d_ws);
}
```

```cpp
#define LTOK_ 208
#define FPAD 32
#define NPR 30
#define NWC 22
#define LK0 3
#define LK1 2
#define LK2 1
#define LK3 0
#include <hip/hip_runtime.h>
#include <hip/hip_bf16.h>

constexpr int TOKB = 192, ROWB = 511 * TOKB;
constexpr unsigned FRAMEB = 511u * ROWB;
constexpr int LTOK = LTOK_;
constexpr int LFRAME = 48 * LTOK + FPAD;
constexpr int LBUF = 4 * LFRAME;
constexpr int LSTASH = 10 * 4 * LTOK;
constexpr int LSTG = 6208;
constexpr int VST = 392, LEXT = 1536 + 8 * VST;
constexpr int WT = 3088;
constexpr int LDS_AUX = (54 - NPR) / 3 * WT, LDS_TAB = LDS_AUX + 1024, LDS_BUF = LDS_TAB + 2048, LDS_STASH = LDS_BUF + 2 * LBUF, LDS_CNT = LDS_STASH + LSTASH,
              LDS_STG = LDS_CNT + 64, LDS_SCRAP = LDS_STG + 4 * LSTG, LDS_EXT = LDS_SCRAP + 512, LDS_BYTES = 163840;
static_assert(NWC >= 18 && NPR % 3 == 0 && LDS_EXT + 4 * LEXT <= LDS_BYTES && NPR * 1024 <= LBUF, "LDS");
constexpr unsigned RAWM = 18432, RAW_T = LDS_BUF, RAW_SV = (36 - NPR) / 3 * WT, RAW_SO = LDS_EXT, RAW_SQ = LDS_BUF + LBUF, RAW_SK = RAW_SQ + RAWM;
static_assert(RAW_SV + RAWM <= LDS_AUX && RAW_SO + RAWM <= LDS_BYTES && RAW_SK + RAWM <= LDS_STASH && RAW_T + 4 * RAWM <= LDS_STASH, "fold LDS");

constexpr int phi(int v) { return v ^ (v >> 1); }
struct Tab {
    unsigned img[3][64][4];
    unsigned p[4][6][64][4];
    unsigned c[4][6][64][4];
    unsigned d[4][6][64][4];
    static constexpr void job(unsigned (*q)[64][4], int lane, unsigned L, unsigned R, int mode, int tau, unsigned st0, unsigned st1) {
        const int i = lane & 15, g = lane >> 4;
        for (int a = 0; a < 2; ++a) {
            const unsigned ra = (8 * (i >> 2) + 4 * a + (i & 3)) * 192 + ((g ^ ((2 * (i >> 2) + a) & 3)) * 16);
            q[0][lane][a] = L + ra;
            q[0][lane][2 + a] = mode == 0 ? R + tau * 6144 + ra : mode == 2 ? R + tau * 6144 + (16 * a + i) * 192 + ((g ^ (i >> 2)) * 16)
                                          : R + tau * 64 + (8 * g + 4 * a + (i >> 2)) * 192 + (((i & 3) ^ ((2 * g + a) & 3)) * 16);
        }
        q[1][lane][0] = st0; q[1][lane][1] = st1; q[1][lane][2] = LDS_SCRAP + g * 32;
    }
    static constexpr unsigned scat(bool win, int lane, int tau, int tb) {
        const int i = lane & 15, c = 32 * tau + 8 * (i >> 2) + 4 * tb + (i & 3), r = c / 24, o = c % 24, q = win ? o / 6 : o & 3, t = win ? o % 6 : o >> 2;
        return (win ? RAW_SV : 3 * t < NPR - 18 ? LDS_BUF + 18 * 1024 : -(NPR - 18) / 3 * WT) + t * WT + ((lane & 48) + 4 * q + r) * 16;
    }
    constexpr Tab() : img{}, p{}, c{}, d{} {
        for (int e = 0; e < 48; ++e) img[0][e >> 2][e & 3] = 0x3f803f80u;
        for (int e = 0; e < 8; ++e) img[0][36 + 10 + e][e >> 1] = e & 1 ? 0x3f800000u : 0x3f80u;
        for (int k = 0; k < 4; ++k) for (int lane = 0; lane < 64; ++lane) {
            const int i = lane & 15, g = lane >> 4, rowv = 8 * (i >> 2) + (i & 3);
            unsigned vi[16] = {};
            {
                const int pp = i >> 2, t = i & 3;
                const int t1 = (k + 3 - LK1) & 3;
                if (t1 < 3) job(p[k], lane, RAW_T + 2 * RAWM, RAW_T + 3 * RAWM, 1, t1, scat(false, lane, t1, 0), scat(false, lane, t1, 1));
                else job(p[k], lane, RAW_T + 2 * RAWM, RAW_T + 3 * RAWM, 0, 0, LDS_AUX + 192 + rowv * 2, 0);
                const unsigned wl = lane * 16, lm = pp == g ? ~0u : 0u, idp = LDS_AUX + 576 + (t == g ? 10 + pp : 0) * 16, sidx = 0xdddd;
                unsigned vA[3] = {};
                for (int m = 0; m < 3; ++m) {
                    const int n = 64 * m + lane, tf = n / 96, q = n % 96 / 12, s = (n % 12) ^ tf;
                    const unsigned cc = tf * FRAMEB + (3 * (s & 3) + (s >> 2)) * 16 + 2048 - 1024 * m;
                    vA[m] = cc + q * TOKB;
                    img[1][lane][m] = (cc + 6 * ROWB + 6 * TOKB) | ((unsigned)(2 * (q & 3) + (q >> 2)) << 28);
                }
                const unsigned vA0 = vA[0], vA1 = vA[1], vA2 = vA[2], vB0 = vA0 + (ROWB - 7 * TOKB), vB1 = vA1 + (ROWB - 7 * TOKB), vB2 = vA2 + (ROWB - 7 * TOKB);
                const unsigned dA = LDS_BUF + t * LFRAME + pp * LTOK + g * 16, rA = LDS_BUF + t * LFRAME + (pp == g ? pp : 4 + g) * LTOK + 192;
                img[1][lane][3] = (2 * pp * (4 * LTOK) + t * LTOK + g * 16) | (((pp == g ? 2 * pp : 2 * g + 1) * (4 * LTOK) + t * LTOK + 192) << 16);
                const unsigned cd0 = dA + k * 8 * LTOK, cr0 = rA + k * 8 * LTOK;
                const unsigned xr = LDS_STG + k * LSTG + (t >> 1) * 3104 + ((t & 1) * 8 + pp) * TOKB + (g ^ (t & 1)) * 16;
                const unsigned axp = LDS_AUX + (t < 2 ? t : 2) * 192 + g * 16;
                vi[0] = wl; vi[1] = xr; vi[2] = axp; vi[3] = lm; vi[4] = idp; vi[5] = rA; vi[6] = dA; vi[7] = vA0; vi[8] = vA1; vi[9] = vA2; vi[10] = vB0; vi[11] = vB1; vi[12] = vB2; vi[13] = sidx; vi[14] = cd0; vi[15] = cr0;
                for (int e = 0; e < 16; ++e) p[k][2 + (e >> 2)][lane][e & 3] = vi[e];
            }
            {
                const int t0 = (k + 3 - LK0) & 3, t2 = (k + 3 - LK2) & 3, t3 = (k + 3 - LK3) & 3;
                const unsigned s0 = LDS_BUF + t0 * 6144 + lane * 16, s3 = LDS_BUF + LBUF + t3 * 6144 + lane * 16;
                if (t0 < 3) job(c[k], lane, RAW_T, RAW_T + RAWM, 2, t0, s0, s0 + 3072); else job(c[k], lane, RAW_T, RAW_T + RAWM, 1, 0, LDS_SCRAP + rowv * 4, 0);
                if (t2 < 3) job(c[k] + 4, lane, RAW_SV, RAW_SO, 1, t2, scat(true, lane, t2, 0), scat(true, lane, t2, 1));
                if (t3 < 3) job(d[k], lane, RAW_SQ, RAW_SK, 0, t3, s3, s3 + 3072); else job(d[k], lane, RAW_SQ, RAW_SK, 1, 0, LDS_SCRAP + rowv * 4, 0);
                for (int m = 0; m < 3; ++m) {
                    const int n = 64 * m + lane, row = n / 12, pc = ((n % 12) ^ ((row >> 2) & 3)) * 16;
                    c[k][3][lane][m] = (24 * (row & 3) + 12 * ((row >> 2) & 1) + 2 * (row >> 3)) * 576 + pc;
                    c[k][2][lane][m] = row * 192 + pc;
                }
                const int dg = 16 * (2 * (g & 1) + (g >> 1)), pix = 4 * (i & 3) + phi(i >> 2);
                const unsigned wl = lane * 16, fr = LDS_BUF + k * LFRAME, ext = LDS_EXT + k * LEXT;
                const unsigned ta0 = fr + pix * LTOK + dg, zr = LDS_AUX + 384 + g * 16, t30 = i & 3 ? zr : LDS_STASH + k * LTOK + dg;
                const unsigned xa0 = fr + (4 * (i >> 2) + phi(g)) * LTOK + 96 * (i & 1) + 8 * ((i >> 1) & 1);
                const int sg = 8 * ((i >> 1) & 1) + 4 * (i >> 3) + 2 * ((i >> 2) & 1) + (i & 1);
                const unsigned tv0 = fr + (32 + 4 * (sg & 3) + phi(sg >> 2)) * LTOK + dg, c1f = (i >> 2) == 2 * g ? 0x3f800000u : 0u;
                const unsigned sidx = g < 2 ? 0x0e0e0404u | ((i >> 2) == 2 * g ? (i & 3) * 0x00100010u : (i >> 2) == 2 * g + 1 ? (i & 3) * 0x40004000u : 0u) : 0xeeee4444u;
                const unsigned ga0 = phi(g) * TOKB + 48 * (i & 3) + 12 * (i >> 2), wr = ROWB - 7 * TOKB;
                const unsigned ga1 = ga0 + (phi(g) >= 1 ? wr : 0), ga2 = ga0 + (phi(g) >= 2 ? wr : 0), ga3 = ga0 + (phi(g) >= 3 ? wr : 0);
                const unsigned pr = i ? zr : LDS_SCRAP + k * 128 + g * 16;
                const unsigned pw = LDS_SCRAP + k * 128 + (i >> 2) * 16 + (g < 2 ? g * 8 + (i & 3) * 2 : 64 + (g & 1) * 4 + ((i >> 1) & 1) * 8 + (i & 1) * 2 + (lane == 57 ? 6 : 0));
                const unsigned ex0 = i & 3 ? zr : ext + g * 16, exv0 = ext + 1536 + i * 24;
                const unsigned rla0 = lane < 48 ? fr + (16 * g + pix) * LTOK + 192 : lane == 56 ? LDS_STASH + k * LTOK + 192 : LDS_AUX + 384;
                img[2][lane][0] = ((i & 7) * (4 * LTOK) + dg) | ((((4 * g + (i & 3)) & 7) * (4 * LTOK) + 96 * ((i >> 2) & 1) + 8 * (i >> 3)) << 16);
                img[2][lane][1] = ((i & 7) * 192u + g * 16) | ((1536u + i * 24 + (g & 1) * 4 * VST) << 16);
                img[2][lane][2] = ((fr + phi(g) * LTOK + 192 - xa0) & 0xffffu) | ((fr + pix * LTOK + 192 - xa0) << 16);
                img[2][lane][3] = (LDS_AUX + 576 + (10 + (i & 3)) * 16) | ((1536u + (i & 7) * VST + 384) << 16);
                vi[0] = wl; vi[1] = pw; vi[2] = pr; vi[3] = ta0; vi[4] = t30; vi[5] = xa0; vi[6] = rla0; vi[7] = exv0; vi[8] = ga0; vi[9] = ga1; vi[10] = ga2; vi[11] = ga3; vi[12] = ex0; vi[13] = tv0; vi[14] = sidx; vi[15] = c1f;
                for (int e = 0; e < 16; ++e) d[k][2 + (e >> 2)][lane][e & 3] = vi[e];
            }
        }
    }
};
static __device__ const Tab TAB __attribute__((aligned(128))) = Tab();

#define CLOBBERS "v1", "v2", "v3", "v4", "v5", "v6", "v7", "v8", "v9", "v10", "v11", "v12", "v13", "v14", "v15", "v16", "v17", "v18", "v19", "v20", "v21", "v22", "v23", "v24", "v25", "v26", "v27", "v28", "v29", "v30", "v31", "v32", "v33", "v34", "v35", "v36", "v37", "v38", "v39", "v40", "v41", "v42", "v43", "v44", "v45", "v46", "v47", "v48", "v49", "v50", "v51", "v52", "v53", "v54", "v55", "v56", "v57", "v58", "v59", "v60", "v61", "v62", "v63", "v64", "v65", "v66", "v67", "v68", "v69", "v70", "v71", "v72", "v73", "v74", "v75", "v76", "v77", "v78", "v79", "v80", "v81", "v82", "v83", "v84", "v85", "v86", "v87", "v88", "v89", "v90", "v91", "v92", "v93", "v94", "v95", "v96", "v97", "v98", "v99", "v100", "v101", "v102", "v103", "v104", "v105", "v106", "v107", "v108", "v109", "v110", "v111", "v112", "v113", "v114", "v115", "v116", "v117", "v118", "v119", "v120", "v121", "v122", "v123", "v124", "v125", "v126", "v127", "v128", "v129", "v130", "v131", "v132", "v133", "v134", "v135", "v136", "v137", "v138", "v139", "v140", "v141", "v142", "v143", "v144", "v145", "v146", "v147", "v148", "v149", "v150", "v151", "v152", "v153", "v154", "v155", "v156", "v157", "v158", "v159", "v160", "v161", "v162", "v163", "v164", "v165", "v166", "v167", "v168", "v169", "v170", "v171", "v172", "v173", "v174", "v175", "v176", "v177", "v178", "v179", "v180", "v181", "v182", "v183", "v184", "v185", "v186", "v187", "v188", "v189", "v190", "v191", "v192", "v193", "v194", "v195", "v196", "v197", "v198", "v199", "v200", "v201", "v202", "v203", "v204", "v205", "v206", "v207", "v208", "v209", "v210", "v211", "v212", "v213", "v214", "v215", "v216", "v217", "v218", "v219", "v220", "v221", "v222", "v223", "v224", "v225", "v226", "v227", "v228", "v229", "v230", "v231", "v232", "v233", "v234", "v235", "v236", "v237", "v238", "v239", "v240", "v241", "v242", "v243", "v244", "v245", "v246", "v247", "v248", "v249", "v250", "v251", "v252", "v253", "v254", "v255", "s3", "s6", "s7", "s8", "s9", "s10", "s11", "s12", "s13", "s14", "s15", "s16", "s17", "s18", "s19", "s20", "s21", "s22", "s23", "s24", "s25", "s26", "s27", "s28", "s29", "s30", "s31", "s34", "s35", "s36", "s37", "s38", "s39", "s40", "s41", "s42", "s43", "s44", "s45", "s46", "s47", "s48", "s49", "s50", "s51", "s52", "s53", "s54", "s55", "s56", "s57", "s58", "s59", "s60", "s61", "s62", "s63", "s64", "s65", "s66", "s67", "s68", "s69", "s70", "s71", "s72", "s73", "s74", "s75", "s76", "s77", "vcc", "scc", "memory"
__global__ __launch_bounds__(512, 2) void swin_kernel(const char* x, const char* wqkv_t, const char* wo_t, const char* wqkv_s, const char* wo_s, char* out, const char* ws) {
    const unsigned long long kap = (unsigned long long)__builtin_amdgcn_kernarg_segment_ptr(), tab = (unsigned long long)&TAB;
    const unsigned tid = threadIdx.x, bid = blockIdx.x;
    asm volatile(
        "s_load_dwordx8 s[44:51], s[0:1], 0x0\n"
        "s_load_dwordx4 s[52:55], s[0:1], 0x20\n"
        "v_readfirstlane_b32 s3, v0\n"
        "v_and_b32 v16, 63, v0\n"
        "v_lshlrev_b32 v17, 4, v16\n"
        "s_bfe_u32 s3, s3, 0x30006\n"
        "s_and_b32 s6, s3, 3\n"
        "s_mul_i32 s64, s3, 9216\n"
        "s_add_u32 s64, s64, 27776\n"
        "s_mov_b32 s27, 0x20000\n"
        "s_cmp_lt_u32 s3, 4\n"
        "s_cbranch_scc0 PPRO_%=\n"
        "s_mul_i32 s7, s6, 6144\n"
        "s_add_u32 s7, s7, 29696\n"
        "s_add_u32 s22, s4, s7\n"
        "s_addc_u32 s23, s5, 0\n"
        "global_load_dwordx4 v[240:243], v17, s[22:23] offset:-2048\n"
        "global_load_dwordx4 v[244:247], v17, s[22:23] offset:-1024\n"
        "global_load_dwordx4 v[36:39], v17, s[22:23] offset:1024\n"
        "global_load_dwordx4 v[48:51], v17, s[22:23] offset:2048\n"
        "global_load_dwordx4 v[52:55], v17, s[22:23] offset:3072\n"
        "s_mov_b32 s65, 13824\n"
        "v_add_u32 v24, 0, v16\n"
        "v_mul_u32_u24 v21, 171, v24\n"
        "v_lshrrev_b32 v21, 11, v21\n"
        "v_mad_i32_i24 v24, v21, -12, v24\n"
        "v_bfe_u32 v18, v21, 2, 2\n"
        "v_xor_b32 v18, v24, v18\n"
        "v_and_b32 v27, 7, v21\n"
        "v_mul_u32_u24 v27, 576, v27\n"
        "v_lshl_add_u32 v18, v18, 4, v27\n"
        "v_add_u32 v25, 64, v16\n"
        "v_mul_u32_u24 v22, 171, v25\n"
        "v_lshrrev_b32 v22, 11, v22\n"
        "v_mad_i32_i24 v25, v22, -12, v25\n"
        "v_bfe_u32 v19, v22, 2, 2\n"
        "v_xor_b32 v19, v25, v19\n"
        "v_and_b32 v27, 7, v22\n"
        "v_mul_u32_u24 v27, 576, v27\n"
        "v_lshl_add_u32 v19, v19, 4, v27\n"
        "v_lshrrev_b32 v27, 3, v22\n"
        "v_mad_u32_u24 v19, v27, s65, v19\n"
        "v_add_u32 v26, 128, v16\n"
        "v_mul_u32_u24 v23, 171, v26\n"
        "v_lshrrev_b32 v23, 11, v23\n"
        "v_mad_i32_i24 v26, v23, -12, v26\n"
        "v_bfe_u32 v20, v23, 2, 2\n"
        "v_xor_b32 v20, v26, v20\n"
        "v_and_b32 v27, 7, v23\n"
        "v_mul_u32_u24 v27, 576, v27\n"
        "v_lshl_add_u32 v20, v20, 4, v27\n"
        "v_add_u32 v20, s65, v20\n"
        "s_lshr_b32 s61, s6, 1\n"
        "s_mul_i32 s61, s61, 192\n"
        "s_mov_b32 s62, 27648\n"
        "s_mov_b32 s63, 4608\n"
        "s_add_u32 s7, s62, s63\n"
        "s_lshl_b32 s29, s63, 1\n"
        "s_add_u32 s60, s7, s63\n"
        "s_bitcmp1_b32 s6, 0\n"
        "s_cselect_b32 s28, s7, 0\n"
        "s_cselect_b32 s29, s29, s62\n"
        "s_cselect_b32 s60, s60, s63\n"
        "s_add_u32 s28, s28, s61\n"
        "s_add_u32 s29, s29, s61\n"
        "s_add_u32 s60, s60, s61\n"
        "s_mov_b32 s26, 55296\n"
        "s_waitcnt lgkmcnt(0)\n"
        "s_mov_b32 s24, s46\n"
        "s_and_b32 s25, s47, 0xffff\n"
        "s_add_u32 m0, s64, 0\n"
        "s_nop 0\n"
        "buffer_load_dwordx4 v18, s[24:27], s28 offen lds\n"
        "s_add_u32 m0, s64, 1024\n"
        "s_nop 0\n"
        "buffer_load_dwordx4 v19, s[24:27], s28 offen lds\n"
        "s_add_u32 m0, s64, 2048\n"
        "s_nop 0\n"
        "buffer_load_dwordx4 v20, s[24:27], s28 offen lds\n"
        "s_add_u32 m0, s64, 3072\n"
        "s_nop 0\n"
        "buffer_load_dwordx4 v18, s[24:27], s29 offen lds\n"
        "s_add_u32 m0, s64, 4096\n"
        "s_nop 0\n"
        "buffer_load_dwordx4 v19, s[24:27], s29 offen lds\n"
        "s_add_u32 m0, s64, 5120\n"
        "s_nop 0\n"
        "buffer_load_dwordx4 v20, s[24:27], s29 offen lds\n"
        "s_add_u32 m0, s64, 6144\n"
        "s_nop 0\n"
        "buffer_load_dwordx4 v18, s[24:27], s60 offen lds\n"
        "s_add_u32 m0, s64, 7168\n"
        "s_nop 0\n"
        "buffer_load_dwordx4 v19, s[24:27], s60 offen lds\n"
        "s_add_u32 m0, s64, 8192\n"
        "s_nop 0\n"
        "buffer_load_dwordx4 v20, s[24:27], s60 offen lds\n"
        "s_barrier\n"
        "s_setprio 1\n"
        "v_mov_b32 v250, 116224\n"
        "s_mov_b32 s41, 0x400000\n"
        "s_and_b32 s7, s2, 7\n"
        "s_lshl_b32 s7, s7, 5\n"
        "s_lshr_b32 s14, s2, 3\n"
        "s_add_u32 s7, s7, s14\n"
        "s_mul_i32 s7, s7, 5329\n"
        "s_add_u32 s14, s7, 5329\n"
        "s_lshr_b32 s7, s7, 8\n"
        "s_lshr_b32 s14, s14, 8\n"
        "s_sub_u32 s14, s14, s7\n"
        "s_mul_i32 s15, s7, 57457\n"
        "s_lshr_b32 s15, s15, 22\n"
        "s_mul_i32 s16, s15, 73\n"
        "s_sub_u32 s16, s7, s16\n"
        "s_mul_i32 s15, s15, 686784\n"
        "s_mul_i32 s7, s16, 1344\n"
        "s_add_u32 s15, s15, s7\n"
        "s_mov_b32 s8, s54\n"
        "s_and_b32 s9, s55, 0xffff\n"
        "s_mov_b32 s10, 200540928\n"
        "s_mov_b32 s11, 0x20000\n"
        "s_mov_b64 s[12:13], s[54:55]\n"
        "s_mul_i32 s17, s6, 50135232\n"
        "s_mul_i32 s18, s6, 208\n"
        "s_add_u32 s18, s18, 115584\n"
        "s_mul_i32 s19, s6, 4672\n"
        "s_add_u32 s19, s19, 141632\n"
        "s_mul_i32 s7, s6, 6144\n"
        "s_add_u32 s7, s7, 54272\n"
        "s_add_u32 s22, s4, s7\n"
        "s_addc_u32 s23, s5, 0\n"
        "s_waitcnt vmcnt(0)\n"
        "s_mov_b64 exec, 1\n"
        "ds_inc_u32 v250, v250 offset:16\n"
        "s_mov_b64 exec, -1\n"
        "L31_%=:\n"
        "ds_read_b32 v251, v250 offset:16\n"
        "s_waitcnt lgkmcnt(0)\n"
        "v_readfirstlane_b32 s40, v251\n"
        "s_nop 3\n"
        "s_cmp_ge_i32 s40, 4\n"
        "s_cbranch_scc1 L32_%=\n"
        "s_nop 0\n"
        "s_sub_u32 s41, s41, 1\n"
        "s_cbranch_scc0 L31_%=\n"
        "s_endpgm\n"
        "L32_%=:\n"
        "s_cmp_eq_u32 s6, 3\n"
        "s_cbranch_scc1 L33_%=\n"
        "s_mov_b32 s34, 0x4162ccfd\n"
        "s_mov_b32 s35, 116248\n"
        "s_mov_b32 s36, 116248\n"
        "s_mov_b32 s37, 116252\n"
        "s_call_b64 s[30:31], FOLDM_%=\n"
        "s_branch L34_%=\n"
        "L33_%=:\n"
        "s_mov_b32 s34, 0xbe173353\n"
        "s_mov_b32 s35, 116280\n"
        "s_mov_b32 s36, 116280\n"
        "s_mov_b32 s37, 116248\n"
        "s_call_b64 s[30:31], FOLDR_%=\n"
        "L34_%=:\n"
        "s_setprio 3\n"
        "v_mov_b64 v[240:241], v[48:49]\n"
        "v_mov_b64 v[242:243], v[50:51]\n"
        "v_mov_b64 v[244:245], v[52:53]\n"
        "v_mov_b64 v[246:247], v[54:55]\n"
        "L35_%=:\n"
        "ds_read_b32 v251, v250 offset:32\n"
        "s_waitcnt lgkmcnt(0)\n"
        "v_readfirstlane_b32 s40, v251\n"
        "s_nop 3\n"
        "s_cmp_ge_i32 s40, 4\n"
        "s_cbranch_scc1 L36_%=\n"
        "s_nop 0\n"
        "s_sub_u32 s41, s41, 1\n"
        "s_cbranch_scc0 L35_%=\n"
        "s_endpgm\n"
        "L36_%=:\n"
        "s_cmp_eq_u32 s6, 1\n"
        "s_cbranch_scc1 L37_%=\n"
        "s_mov_b32 s34, 0x411cc471\n"
        "s_mov_b32 s35, 116264\n"
        "s_mov_b32 s36, 116264\n"
        "s_mov_b32 s37, 116276\n"
        "s_call_b64 s[30:31], FOLDW_%=\n"
        "s_branch L38_%=\n"
        "L37_%=:\n"
        "s_mov_b64 exec, 1\n"
        "ds_inc_u32 v250, v250 offset:40\n"
        "s_mov_b64 exec, -1\n"
        "s_mov_b32 s24, s50\n"
        "s_and_b32 s25, s51, 0xffff\n"
        "s_mov_b32 s26, 55296\n"
        "s_mov_b32 m0, 67840\n"
        "s_mov_b32 s28, 0\n"
        "buffer_load_dwordx4 v36, s[24:27], s28 offen lds\n"
        "s_mov_b32 m0, 68864\n"
        "s_nop 0\n"
        "buffer_load_dwordx4 v37, s[24:27], s28 offen lds\n"
        "s_mov_b32 m0, 69888\n"
        "s_nop 0\n"
        "buffer_load_dwordx4 v38, s[24:27], s28 offen lds\n"
        "s_mov_b32 m0, 70912\n"
        "s_mov_b32 s28, 576\n"
        "buffer_load_dwordx4 v36, s[24:27], s28 offen lds\n"
        "s_mov_b32 m0, 71936\n"
        "s_nop 0\n"
        "buffer_load_dwordx4 v37, s[24:27], s28 offen lds\n"
        "s_mov_b32 m0, 72960\n"
        "s_nop 0\n"
        "buffer_load_dwordx4 v38, s[24:27], s28 offen lds\n"
        "s_mov_b32 m0, 73984\n"
        "s_mov_b32 s28, 2304\n"
        "buffer_load_dwordx4 v36, s[24:27], s28 offen lds\n"
        "s_mov_b32 m0, 75008\n"
        "s_nop 0\n"
        "buffer_load_dwordx4 v37, s[24:27], s28 offen lds\n"
        "s_mov_b32 m0, 76032\n"
        "s_nop 0\n"
        "buffer_load_dwordx4 v38, s[24:27], s28 offen lds\n"
        "s_mov_b32 m0, 77056\n"
        "s_mov_b32 s28, 2880\n"
        "buffer_load_dwordx4 v36, s[24:27], s28 offen lds\n"
        "s_mov_b32 m0, 78080\n"
        "s_nop 0\n"
        "buffer_load_dwordx4 v37, s[24:27], s28 offen lds\n"
        "s_mov_b32 m0, 79104\n"
        "s_nop 0\n"
        "buffer_load_dwordx4 v38, s[24:27], s28 offen lds\n"
        "s_mov_b32 m0, 80128\n"
        "s_mov_b32 s28, 4608\n"
        "buffer_load_dwordx4 v36, s[24:27], s28 offen lds\n"
        "s_mov_b32 m0, 81152\n"
        "s_nop 0\n"
        "buffer_load_dwordx4 v37, s[24:27], s28 offen lds\n"
        "s_mov_b32 m0, 82176\n"
        "s_nop 0\n"
        "buffer_load_dwordx4 v38, s[24:27], s28 offen lds\n"
        "s_mov_b32 m0, 83200\n"
        "s_mov_b32 s28, 5184\n"
        "buffer_load_dwordx4 v36, s[24:27], s28 offen lds\n"
        "s_mov_b32 m0, 84224\n"
        "s_nop 0\n"
        "buffer_load_dwordx4 v37, s[24:27], s28 offen lds\n"
        "s_mov_b32 m0, 85248\n"
        "s_nop 0\n"
        "buffer_load_dwordx4 v38, s[24:27], s28 offen lds\n"
        "s_mov_b32 s24, s50\n"
        "s_and_b32 s25, s51, 0xffff\n"
        "s_mov_b32 s26, 55296\n"
        "s_mov_b32 m0, 86272\n"
        "s_mov_b32 s28, 192\n"
        "buffer_load_dwordx4 v36, s[24:27], s28 offen lds\n"
        "s_mov_b32 m0, 87296\n"
        "s_nop 0\n"
        "buffer_load_dwordx4 v37, s[24:27], s28 offen lds\n"
        "s_mov_b32 m0, 88320\n"
        "s_nop 0\n"
        "buffer_load_dwordx4 v38, s[24:27], s28 offen lds\n"
        "s_mov_b32 m0, 89344\n"
        "s_mov_b32 s28, 768\n"
        "buffer_load_dwordx4 v36, s[24:27], s28 offen lds\n"
        "s_mov_b32 m0, 90368\n"
        "s_nop 0\n"
        "buffer_load_dwordx4 v37, s[24:27], s28 offen lds\n"
        "s_mov_b32 m0, 91392\n"
        "s_nop 0\n"
        "buffer_load_dwordx4 v38, s[24:27], s28 offen lds\n"
        "s_mov_b32 m0, 92416\n"
        "s_mov_b32 s28, 2496\n"
        "buffer_load_dwordx4 v36, s[24:27], s28 offen lds\n"
        "s_mov_b32 m0, 93440\n"
        "s_nop 0\n"
        "buffer_load_dwordx4 v37, s[24:27], s28 offen lds\n"
        "s_mov_b32 m0, 94464\n"
        "s_nop 0\n"
        "buffer_load_dwordx4 v38, s[24:27], s28 offen lds\n"
        "s_mov_b32 m0, 95488\n"
        "s_mov_b32 s28, 3072\n"
        "buffer_load_dwordx4 v36, s[24:27], s28 offen lds\n"
        "s_mov_b32 m0, 96512\n"
        "s_nop 0\n"
        "buffer_load_dwordx4 v37, s[24:27], s28 offen lds\n"
        "s_mov_b32 m0, 97536\n"
        "s_nop 0\n"
        "buffer_load_dwordx4 v38, s[24:27], s28 offen lds\n"
        "s_mov_b32 m0, 98560\n"
        "s_mov_b32 s28, 4800\n"
        "buffer_load_dwordx4 v36, s[24:27], s28 offen lds\n"
        "s_mov_b32 m0, 99584\n"
        "s_nop 0\n"
        "buffer_load_dwordx4 v37, s[24:27], s28 offen lds\n"
        "s_mov_b32 m0, 100608\n"
        "s_nop 0\n"
        "buffer_load_dwordx4 v38, s[24:27], s28 offen lds\n"
        "s_mov_b32 m0, 101632\n"
        "s_mov_b32 s28, 5376\n"
        "buffer_load_dwordx4 v36, s[24:27], s28 offen lds\n"
        "s_mov_b32 m0, 102656\n"
        "s_nop 0\n"
        "buffer_load_dwordx4 v37, s[24:27], s28 offen lds\n"
        "s_mov_b32 m0, 103680\n"
        "s_nop 0\n"
        "buffer_load_dwordx4 v38, s[24:27], s28 offen lds\n"
        "global_load_dwordx4 v[40:43], v17, s[22:23] offset:-2048\n"
        "global_load_dwordx4 v[44:47], v17, s[22:23] offset:-1024\n"
        "global_load_dwordx4 v[0:3], v17, s[22:23] offset:0\n"
        "global_load_dwordx4 v[4:7], v17, s[22:23] offset:1024\n"
        "global_load_dwordx4 v[8:11], v17, s[22:23] offset:2048\n"
        "global_load_dwordx4 v[12:15], v17, s[22:23] offset:3072\n"
        "L38_%=:\n"
        "s_waitcnt vmcnt(0)\n"
        "s_mov_b64 exec, 1\n"
        "ds_inc_u32 v250, v250 offset:44\n"
        "s_mov_b64 exec, -1\n"
        "v_mov_b64 v[240:241], v[40:41]\n"
        "v_mov_b64 v[242:243], v[42:43]\n"
        "v_mov_b64 v[244:245], v[44:45]\n"
        "v_mov_b64 v[246:247], v[46:47]\n"
        "L39_%=:\n"
        "ds_read_b32 v251, v250 offset:44\n"
        "s_waitcnt lgkmcnt(0)\n"
        "v_readfirstlane_b32 s40, v251\n"
        "s_nop 3\n"
        "s_cmp_ge_i32 s40, 4\n"
        "s_cbranch_scc1 L40_%=\n"
        "s_nop 0\n"
        "s_sub_u32 s41, s41, 1\n"
        "s_cbranch_scc0 L39_%=\n"
        "s_endpgm\n"
        "L40_%=:\n"
        "s_cmp_eq_u32 s6, 0\n"
        "s_cbranch_scc1 L41_%=\n"
        "s_mov_b32 s34, 0x41622adf\n"
        "s_mov_b32 s35, 116272\n"
        "s_mov_b32 s36, 116272\n"
        "s_mov_b32 s37, 116276\n"
        "s_call_b64 s[30:31], FOLDM_%=\n"
        "s_branch L42_%=\n"
        "L41_%=:\n"
        "s_mov_b32 s34, 0xbe16c740\n"
        "s_mov_b32 s35, 116280\n"
        "s_mov_b32 s36, 116280\n"
        "s_mov_b32 s37, 116272\n"
        "s_call_b64 s[30:31], FOLDR_%=\n"
        "L42_%=:\n"
        "s_branch CONS_%=\n"
        "FOLDM_%=:\n"
        "ds_read_b128 v[88:91], v242\n"
        "ds_read_b128 v[168:171], v240\n"
        "ds_read_b128 v[184:187], v242 offset:64\n"
        "ds_read_b128 v[172:175], v240 offset:64\n"
        "ds_read_b128 v[180:183], v242 offset:128\n"
        "ds_read_b128 v[104:107], v240 offset:128\n"
        "ds_read_b128 v[92:95], v241\n"
        "ds_read_b128 v[96:99], v241 offset:64\n"
        "ds_read_b128 v[144:147], v241 offset:128\n"
        "ds_read_b128 v[100:103], v240 offset:6144\n"
        "ds_read_b128 v[132:135], v240 offset:6208\n"
        "ds_read_b128 v[140:143], v240 offset:6272\n"
        "ds_read_b128 v[128:131], v241 offset:6144\n"
        "ds_read_b128 v[136:139], v241 offset:6208\n"
        "ds_read_b128 v[108:111], v241 offset:6272\n"
        "s_waitcnt lgkmcnt(11)\n"
        "v_mfma_f32_16x16x32_bf16 v[64:67], v[168:171], v[88:91], 0\n"
        "ds_read_b128 v[156:159], v240 offset:12288\n"
        "v_mfma_f32_16x16x32_bf16 v[64:67], v[172:175], v[184:187], v[64:67]\n"
        "ds_read_b128 v[160:163], v240 offset:12352\n"
        "s_waitcnt lgkmcnt(9)\n"
        "v_mfma_f32_16x16x32_bf16 v[64:67], v[104:107], v[180:183], v[64:67]\n"
        "ds_read_b128 v[112:115], v240 offset:12416\n"
        "v_mfma_f32_16x16x32_bf16 v[68:71], v[92:95], v[88:91], 0\n"
        "ds_read_b128 v[116:119], v241 offset:12288\n"
        "v_mfma_f32_16x16x32_bf16 v[68:71], v[96:99], v[184:187], v[68:71]\n"
        "ds_read_b128 v[120:123], v241 offset:12352\n"
        "s_waitcnt lgkmcnt(6)\n"
        "v_mfma_f32_16x16x32_bf16 v[68:71], v[144:147], v[180:183], v[68:71]\n"
        "ds_read_b128 v[124:127], v241 offset:12416\n"
        "v_mfma_f32_16x16x32_bf16 v[72:75], v[100:103], v[88:91], 0\n"
        "ds_read_b128 v[148:151], v243\n"
        "v_mfma_f32_16x16x32_bf16 v[72:75], v[132:135], v[184:187], v[72:75]\n"
        "ds_read_b128 v[152:155], v243 offset:64\n"
        "v_mfma_f32_16x16x32_bf16 v[72:75], v[140:143], v[180:183], v[72:75]\n"
        "ds_read_b128 v[164:167], v243 offset:128\n"
        "v_mfma_f32_16x16x32_bf16 v[76:79], v[128:131], v[88:91], 0\n"
        "v_add_f32 v176, v64, v68\n"
        "v_add_f32 v177, v65, v69\n"
        "v_add_f32 v178, v66, v70\n"
        "v_add_f32 v179, v67, v71\n"
        "v_mfma_f32_16x16x32_bf16 v[76:79], v[136:139], v[184:187], v[76:79]\n"
        "s_waitcnt lgkmcnt(4)\n"
        "v_add_f32 v176, v176, v72\n"
        "v_add_f32 v177, v177, v73\n"
        "v_add_f32 v178, v178, v74\n"
        "v_add_f32 v179, v179, v75\n"
        "v_mfma_f32_16x16x32_bf16 v[76:79], v[108:111], v[180:183], v[76:79]\n"
        "v_mfma_f32_16x16x32_bf16 v[80:83], v[156:159], v[88:91], 0\n"
        "v_mfma_f32_16x16x32_bf16 v[80:83], v[160:163], v[184:187], v[80:83]\n"
        "v_mfma_f32_16x16x32_bf16 v[80:83], v[112:115], v[180:183], v[80:83]\n"
        "v_mfma_f32_16x16x32_bf16 v[84:87], v[116:119], v[88:91], 0\n"
        "v_mfma_f32_16x16x32_bf16 v[84:87], v[120:123], v[184:187], v[84:87]\n"
        "s_waitcnt lgkmcnt(0)\n"
        "v_mfma_f32_16x16x32_bf16 v[84:87], v[124:127], v[180:183], v[84:87]\n"
        "v_mov_b32 v91, s35\n"
        "v_add_f32 v176, v176, v76\n"
        "v_add_f32 v177, v177, v77\n"
        "v_add_f32 v178, v178, v78\n"
        "v_add_f32 v179, v179, v79\n"
        "v_add_f32 v176, v176, v80\n"
        "v_add_f32 v177, v177, v81\n"
        "v_add_f32 v178, v178, v82\n"
        "v_add_f32 v179, v179, v83\n"
        "v_add_f32 v176, v176, v84\n"
        "v_add_f32 v177, v177, v85\n"
        "v_add_f32 v178, v178, v86\n"
        "v_add_f32 v179, v179, v87\n"
        "v_add_f32 v176, v176, v177\n"
        "v_add_f32 v178, v178, v179\n"
        "v_add_f32 v176, v176, v178\n"
        "v_mov_b32 v177, v176\n"
        "s_waitcnt vmcnt(6)\n"
        "s_mov_b64 exec, 1\n"
        "ds_inc_u32 v91, v240 offset:0\n"
        "s_mov_b64 exec, -1\n"
        "v_permlane32_swap_b32 v176, v177\n"
        "v_add_f32 v176, v176, v177\n"
        "v_mov_b32 v177, v176\n"
        "v_mfma_f32_16x16x32_bf16 v[88:91], v[168:171], v[148:151], 0\n"
        "v_mfma_f32_16x16x32_bf16 v[88:91], v[172:175], v[152:155], v[88:91]\n"
        "v_permlane16_swap_b32 v176, v177\n"
        "v_add_f32 v176, v176, v177\n"
        "v_mul_f32 v176, 0xbc2aaaab, v176\n"
        "v_mul_f32 v176, s34, v176\n"
        "v_fma_f32 v64, v64, s34, v176\n"
        "v_fma_f32 v65, v65, s34, v176\n"
        "v_fma_f32 v66, v66, s34, v176\n"
        "v_fma_f32 v67, v67, s34, v176\n"
        "v_fma_f32 v68, v68, s34, v176\n"
        "v_fma_f32 v69, v69, s34, v176\n"
        "v_fma_f32 v70, v70, s34, v176\n"
        "v_fma_f32 v71, v71, s34, v176\n"
        "v_fma_f32 v72, v72, s34, v176\n"
        "v_fma_f32 v73, v73, s34, v176\n"
        "v_fma_f32 v74, v74, s34, v176\n"
        "v_fma_f32 v75, v75, s34, v176\n"
        "v_fma_f32 v76, v76, s34, v176\n"
        "v_fma_f32 v77, v77, s34, v176\n"
        "v_fma_f32 v78, v78, s34, v176\n"
        "v_fma_f32 v79, v79, s34, v176\n"
        "v_fma_f32 v80, v80, s34, v176\n"
        "v_fma_f32 v81, v81, s34, v176\n"
        "v_fma_f32 v82, v82, s34, v176\n"
        "v_fma_f32 v83, v83, s34, v176\n"
        "v_fma_f32 v84, v84, s34, v176\n"
        "v_fma_f32 v85, v85, s34, v176\n"
        "v_fma_f32 v86, v86, s34, v176\n"
        "v_mfma_f32_16x16x32_bf16 v[88:91], v[104:107], v[164:167], v[88:91]\n"
        "v_fma_f32 v87, v87, s34, v176\n"
        "v_mfma_f32_16x16x32_bf16 v[104:107], v[92:95], v[148:151], 0\n"
        "v_mfma_f32_16x16x32_bf16 v[104:107], v[96:99], v[152:155], v[104:107]\n"
        "v_mfma_f32_16x16x32_bf16 v[104:107], v[144:147], v[164:167], v[104:107]\n"
        "v_mfma_f32_16x16x32_bf16 v[96:99], v[100:103], v[148:151], 0\n"
        "v_mfma_f32_16x16x32_bf16 v[96:99], v[132:135], v[152:155], v[96:99]\n"
        "v_mfma_f32_16x16x32_bf16 v[96:99], v[140:143], v[164:167], v[96:99]\n"
        "v_mfma_f32_16x16x32_bf16 v[100:103], v[128:131], v[148:151], 0\n"
        "v_mfma_f32_16x16x32_bf16 v[100:103], v[136:139], v[152:155], v[100:103]\n"
        "v_mfma_f32_16x16x32_bf16 v[100:103], v[108:111], v[164:167], v[100:103]\n"
        "v_mfma_f32_16x16x32_bf16 v[92:95], v[156:159], v[148:151], 0\n"
        "v_mfma_f32_16x16x32_bf16 v[92:95], v[160:163], v[152:155], v[92:95]\n"
        "v_add_f32 v156, v88, v104\n"
        "v_add_f32 v157, v89, v105\n"
        "v_add_f32 v158, v90, v106\n"
        "v_add_f32 v159, v91, v107\n"
        "v_add_f32 v156, v156, v96\n"
        "v_add_f32 v157, v157, v97\n"
        "v_add_f32 v158, v158, v98\n"
        "v_add_f32 v159, v159, v99\n"
        "v_add_f32 v156, v156, v100\n"
        "v_add_f32 v157, v157, v101\n"
        "v_add_f32 v158, v158, v102\n"
        "v_add_f32 v159, v159, v103\n"
        "v_mfma_f32_16x16x32_bf16 v[92:95], v[112:115], v[164:167], v[92:95]\n"
        "v_mfma_f32_16x16x32_bf16 v[108:111], v[116:119], v[148:151], 0\n"
        "v_mfma_f32_16x16x32_bf16 v[108:111], v[120:123], v[152:155], v[108:111]\n"
        "v_mfma_f32_16x16x32_bf16 v[108:111], v[124:127], v[164:167], v[108:111]\n"
        "v_mov_b32 v155, s37\n"
        "s_nop 3\n"
        "v_add_f32 v156, v156, v92\n"
        "v_add_f32 v157, v157, v93\n"
        "v_add_f32 v158, v158, v94\n"
        "v_add_f32 v159, v159, v95\n"
        "v_add_f32 v156, v156, v108\n"
        "v_add_f32 v157, v157, v109\n"
        "v_add_f32 v158, v158, v110\n"
        "v_add_f32 v159, v159, v111\n"
        "v_add_f32 v156, v156, v157\n"
        "v_add_f32 v158, v158, v159\n"
        "v_add_f32 v156, v156, v158\n"
        "v_mov_b32 v157, v156\n"
        "s_nop 1\n"
        "v_permlane32_swap_b32 v156, v157\n"
        "v_add_f32 v156, v156, v157\n"
        "v_mov_b32 v157, v156\n"
        "s_nop 1\n"
        "v_permlane16_swap_b32 v156, v157\n"
        "v_add_f32 v156, v156, v157\n"
        "v_mul_f32 v156, 0xbc2aaaab, v156\n"
        "v_mul_f32 v156, s34, v156\n"
        "v_fma_f32 v88, v88, s34, v156\n"
        "v_fma_f32 v89, v89, s34, v156\n"
        "v_fma_f32 v90, v90, s34, v156\n"
        "v_fma_f32 v91, v91, s34, v156\n"
        "v_fma_f32 v104, v104, s34, v156\n"
        "v_fma_f32 v105, v105, s34, v156\n"
        "v_fma_f32 v106, v106, s34, v156\n"
        "v_fma_f32 v107, v107, s34, v156\n"
        "v_fma_f32 v96, v96, s34, v156\n"
        "v_fma_f32 v97, v97, s34, v156\n"
        "v_fma_f32 v98, v98, s34, v156\n"
        "v_fma_f32 v99, v99, s34, v156\n"
        "v_fma_f32 v100, v100, s34, v156\n"
        "v_fma_f32 v101, v101, s34, v156\n"
        "v_fma_f32 v102, v102, s34, v156\n"
        "v_fma_f32 v103, v103, s34, v156\n"
        "v_fma_f32 v92, v92, s34, v156\n"
        "v_fma_f32 v93, v93, s34, v156\n"
        "v_fma_f32 v94, v94, s34, v156\n"
        "v_fma_f32 v95, v95, s34, v156\n"
        "v_fma_f32 v108, v108, s34, v156\n"
        "v_fma_f32 v109, v109, s34, v156\n"
        "v_fma_f32 v110, v110, s34, v156\n"
        "v_fma_f32 v111, v111, s34, v156\n"
        "v_mov_b32 v127, s36\n"
        "L27_%=:\n"
        "ds_read_b32 v156, v127 offset:0\n"
        "s_waitcnt lgkmcnt(0)\n"
        "v_readfirstlane_b32 s40, v156\n"
        "s_nop 3\n"
        "s_cmp_ge_i32 s40, 4\n"
        "s_cbranch_scc1 L28_%=\n"
        "s_nop 0\n"
        "s_sub_u32 s41, s41, 1\n"
        "s_cbranch_scc0 L27_%=\n"
        "s_endpgm\n"
        "L28_%=:\n"
        "ds_read_b128 v[128:131], v246\n"
        "ds_read_b128 v[124:127], v246 offset:16\n"
        "ds_read_b128 v[120:123], v246 offset:128\n"
        "ds_read_b128 v[116:119], v246 offset:144\n"
        "ds_read_b128 v[112:115], v246 offset:256\n"
        "ds_read_b128 v[156:159], v246 offset:272\n"
        "s_waitcnt lgkmcnt(5)\n"
        "v_add_f32 v64, v64, v128\n"
        "v_add_f32 v65, v65, v129\n"
        "v_add_f32 v66, v66, v130\n"
        "v_add_f32 v67, v67, v131\n"
        "v_add_f32 v88, v88, v128\n"
        "v_add_f32 v89, v89, v129\n"
        "v_add_f32 v90, v90, v130\n"
        "v_add_f32 v91, v91, v131\n"
        "s_waitcnt lgkmcnt(1)\n"
        "v_add_f32 v68, v68, v124\n"
        "v_add_f32 v69, v69, v125\n"
        "v_add_f32 v70, v70, v126\n"
        "v_add_f32 v71, v71, v127\n"
        "v_add_f32 v104, v104, v124\n"
        "v_add_f32 v105, v105, v125\n"
        "v_add_f32 v106, v106, v126\n"
        "v_add_f32 v107, v107, v127\n"
        "v_add_f32 v72, v72, v120\n"
        "v_add_f32 v73, v73, v121\n"
        "v_add_f32 v74, v74, v122\n"
        "v_add_f32 v75, v75, v123\n"
        "v_add_f32 v96, v96, v120\n"
        "v_add_f32 v97, v97, v121\n"
        "v_add_f32 v98, v98, v122\n"
        "v_add_f32 v99, v99, v123\n"
        "v_add_f32 v76, v76, v116\n"
        "v_add_f32 v77, v77, v117\n"
        "v_add_f32 v78, v78, v118\n"
        "v_add_f32 v79, v79, v119\n"
        "v_add_f32 v100, v100, v116\n"
        "v_add_f32 v101, v101, v117\n"
        "v_add_f32 v102, v102, v118\n"
        "v_add_f32 v103, v103, v119\n"
        "v_add_f32 v80, v80, v112\n"
        "v_add_f32 v81, v81, v113\n"
        "v_add_f32 v82, v82, v114\n"
        "v_add_f32 v83, v83, v115\n"
        "v_add_f32 v92, v92, v112\n"
        "v_add_f32 v93, v93, v113\n"
        "v_add_f32 v94, v94, v114\n"
        "v_add_f32 v95, v95, v115\n"
        "s_waitcnt lgkmcnt(0)\n"
        "v_add_f32 v84, v84, v156\n"
        "v_add_f32 v85, v85, v157\n"
        "v_add_f32 v86, v86, v158\n"
        "v_add_f32 v87, v87, v159\n"
        "v_add_f32 v108, v108, v156\n"
        "v_add_f32 v109, v109, v157\n"
        "v_add_f32 v110, v110, v158\n"
        "v_add_f32 v111, v111, v159\n"
        "v_cvt_pk_bf16_f32 v156, v64, v65\n"
        "v_cvt_pk_bf16_f32 v157, v66, v67\n"
        "v_cvt_pk_bf16_f32 v158, v68, v69\n"
        "v_cvt_pk_bf16_f32 v159, v70, v71\n"
        "v_cvt_pk_bf16_f32 v64, v88, v89\n"
        "v_cvt_pk_bf16_f32 v65, v90, v91\n"
        "v_cvt_pk_bf16_f32 v66, v104, v105\n"
        "v_cvt_pk_bf16_f32 v67, v106, v107\n"
        "v_cvt_pk_bf16_f32 v88, v72, v73\n"
        "v_cvt_pk_bf16_f32 v89, v74, v75\n"
        "v_cvt_pk_bf16_f32 v90, v76, v77\n"
        "v_cvt_pk_bf16_f32 v91, v78, v79\n"
        "v_cvt_pk_bf16_f32 v104, v96, v97\n"
        "v_cvt_pk_bf16_f32 v105, v98, v99\n"
        "v_cvt_pk_bf16_f32 v106, v100, v101\n"
        "v_cvt_pk_bf16_f32 v107, v102, v103\n"
        "v_cvt_pk_bf16_f32 v96, v80, v81\n"
        "v_cvt_pk_bf16_f32 v97, v82, v83\n"
        "v_cvt_pk_bf16_f32 v98, v84, v85\n"
        "v_cvt_pk_bf16_f32 v99, v86, v87\n"
        "v_cvt_pk_bf16_f32 v100, v92, v93\n"
        "v_cvt_pk_bf16_f32 v101, v94, v95\n"
        "v_cvt_pk_bf16_f32 v102, v108, v109\n"
        "v_cvt_pk_bf16_f32 v103, v110, v111\n"
        "ds_write_b128 v244, v[156:159]\n"
        "ds_write_b128 v245, v[64:67]\n"
        "ds_write_b128 v244, v[88:91] offset:1024\n"
        "ds_write_b128 v245, v[104:107] offset:1024\n"
        "ds_write_b128 v244, v[96:99] offset:2048\n"
        "ds_write_b128 v245, v[100:103] offset:2048\n"
        "s_mov_b64 exec, 1\n"
        "ds_inc_u32 v155, v240 offset:0\n"
        "s_mov_b64 exec, -1\n"
        "s_setpc_b64 s[30:31]\n"
        "FOLDR_%=:\n"
        "ds_read_b64_tr_b16 v[88:89], v242 offset:0\n"
        "ds_read_b64_tr_b16 v[90:91], v243 offset:0\n"
        "ds_read_b64_tr_b16 v[84:85], v242 offset:6144\n"
        "ds_read_b64_tr_b16 v[86:87], v243 offset:6144\n"
        "ds_read_b64_tr_b16 v[80:81], v242 offset:12288\n"
        "ds_read_b64_tr_b16 v[82:83], v243 offset:12288\n"
        "ds_read_b64_tr_b16 v[76:77], v242 offset:8\n"
        "ds_read_b64_tr_b16 v[78:79], v243 offset:8\n"
        "ds_read_b64_tr_b16 v[92:93], v242 offset:6152\n"
        "ds_read_b64_tr_b16 v[94:95], v243 offset:6152\n"
        "ds_read_b64_tr_b16 v[70:71], v242 offset:12296\n"
        "ds_read_b64_tr_b16 v[72:73], v243 offset:12296\n"
        "v_mov_b32 v64, 0x3f803f80\n"
        "v_mov_b32 v65, 0x3f803f80\n"
        "v_mov_b32 v66, 0x3f803f80\n"
        "v_mov_b32 v67, 0x3f803f80\n"
        "ds_read_b64_tr_b16 v[96:97], v242 offset:64\n"
        "ds_read_b64_tr_b16 v[98:99], v243 offset:64\n"
        "s_waitcnt lgkmcnt(9)\n"
        "v_mfma_f32_16x16x32_bf16 v[108:111], v[88:91], v[64:67], 0\n"
        "v_mfma_f32_16x16x32_bf16 v[108:111], v[84:87], v[64:67], v[108:111]\n"
        "ds_read_b64_tr_b16 v[88:89], v242 offset:6208\n"
        "ds_read_b64_tr_b16 v[90:91], v243 offset:6208\n"
        "s_waitcnt lgkmcnt(6)\n"
        "v_mfma_f32_16x16x32_bf16 v[108:111], v[80:83], v[64:67], v[108:111]\n"
        "ds_read_b64_tr_b16 v[84:85], v242 offset:12352\n"
        "ds_read_b64_tr_b16 v[86:87], v243 offset:12352\n"
        "v_mfma_f32_16x16x32_bf16 v[116:119], v[76:79], v[64:67], 0\n"
        "ds_read_b64_tr_b16 v[80:81], v242 offset:72\n"
        "ds_read_b64_tr_b16 v[82:83], v243 offset:72\n"
        "v_mfma_f32_16x16x32_bf16 v[116:119], v[92:95], v[64:67], v[116:119]\n"
        "ds_read_b64_tr_b16 v[76:77], v242 offset:6216\n"
        "ds_read_b64_tr_b16 v[78:79], v243 offset:6216\n"
        "v_cvt_pk_bf16_f32 v68, v108, v109\n"
        "v_cvt_pk_bf16_f32 v69, v110, v111\n"
        "s_waitcnt lgkmcnt(8)\n"
        "v_mfma_f32_16x16x32_bf16 v[116:119], v[70:73], v[64:67], v[116:119]\n"
        "ds_read_b64_tr_b16 v[92:93], v242 offset:12360\n"
        "ds_read_b64_tr_b16 v[94:95], v243 offset:12360\n"
        "v_mfma_f32_16x16x32_bf16 v[108:111], v[96:99], v[64:67], 0\n"
        "ds_read_b64_tr_b16 v[72:73], v242 offset:128\n"
        "ds_read_b64_tr_b16 v[74:75], v243 offset:128\n"
        "s_waitcnt lgkmcnt(8)\n"
        "v_mfma_f32_16x16x32_bf16 v[108:111], v[88:91], v[64:67], v[108:111]\n"
        "ds_read_b64_tr_b16 v[96:97], v242 offset:6272\n"
        "ds_read_b64_tr_b16 v[98:99], v243 offset:6272\n"
        "v_cvt_pk_bf16_f32 v70, v116, v117\n"
        "v_cvt_pk_bf16_f32 v71, v118, v119\n"
        "v_mfma_f32_16x16x32_bf16 v[108:111], v[84:87], v[64:67], v[108:111]\n"
        "ds_read_b64_tr_b16 v[88:89], v242 offset:12416\n"
        "ds_read_b64_tr_b16 v[90:91], v243 offset:12416\n"
        "s_waitcnt lgkmcnt(8)\n"
        "v_mfma_f32_16x16x32_bf16 v[116:119], v[80:83], v[64:67], 0\n"
        "ds_read_b64_tr_b16 v[84:85], v242 offset:136\n"
        "ds_read_b64_tr_b16 v[86:87], v243 offset:136\n"
        "v_mfma_f32_16x16x32_bf16 v[116:119], v[76:79], v[64:67], v[116:119]\n"
        "ds_read_b64_tr_b16 v[104:105], v242 offset:6280\n"
        "ds_read_b64_tr_b16 v[106:107], v243 offset:6280\n"
        "v_cvt_pk_bf16_f32 v80, v108, v109\n"
        "v_cvt_pk_bf16_f32 v81, v110, v111\n"
        "s_waitcnt lgkmcnt(7)\n"
        "v_mfma_f32_16x16x32_bf16 v[116:119], v[92:95], v[64:67], v[116:119]\n"
        "ds_read_b64_tr_b16 v[108:109], v242 offset:12424\n"
        "ds_read_b64_tr_b16 v[110:111], v243 offset:12424\n"
        "v_mfma_f32_16x16x32_bf16 v[120:123], v[72:75], v[64:67], 0\n"
        "ds_read_b128 v[112:115], v240\n"
        "s_waitcnt lgkmcnt(7)\n"
        "v_mfma_f32_16x16x32_bf16 v[120:123], v[96:99], v[64:67], v[120:123]\n"
        "ds_read_b128 v[76:79], v241\n"
        "v_mfma_f32_16x16x32_bf16 v[120:123], v[88:91], v[64:67], v[120:123]\n"
        "ds_read_b128 v[100:103], v240 offset:6144\n"
        "v_cvt_pk_bf16_f32 v82, v116, v117\n"
        "v_cvt_pk_bf16_f32 v83, v118, v119\n"
        "s_waitcnt lgkmcnt(6)\n"
        "v_mfma_f32_16x16x32_bf16 v[116:119], v[84:87], v[64:67], 0\n"
        "ds_read_b128 v[88:91], v241 offset:6144\n"
        "s_waitcnt lgkmcnt(6)\n"
        "v_mfma_f32_16x16x32_bf16 v[116:119], v[104:107], v[64:67], v[116:119]\n"
        "ds_read_b128 v[96:99], v240 offset:12288\n"
        "v_cvt_pk_bf16_f32 v92, v120, v121\n"
        "v_cvt_pk_bf16_f32 v93, v122, v123\n"
        "s_waitcnt lgkmcnt(5)\n"
        "v_mfma_f32_16x16x32_bf16 v[116:119], v[108:111], v[64:67], v[116:119]\n"
        "ds_read_b128 v[104:107], v241 offset:12288\n"
        "s_waitcnt lgkmcnt(5)\n"
        "v_mfma_f32_16x16x32_bf16 v[64:67], v[68:71], v[112:115], 0\n"
        "ds_read_b128 v[108:111], v240 offset:64\n"
        "s_waitcnt lgkmcnt(5)\n"
        "v_mfma_f32_16x16x32_bf16 v[72:75], v[68:71], v[76:79], 0\n"
        "ds_read_b128 v[120:123], v241 offset:64\n"
        "s_waitcnt lgkmcnt(5)\n"
        "v_mfma_f32_16x16x32_bf16 v[76:79], v[68:71], v[100:103], 0\n"
        "ds_read_b128 v[112:115], v240 offset:6208\n"
        "v_cvt_pk_bf16_f32 v94, v116, v117\n"
        "v_cvt_pk_bf16_f32 v95, v118, v119\n"
        "s_waitcnt lgkmcnt(5)\n"
        "v_mfma_f32_16x16x32_bf16 v[84:87], v[68:71], v[88:91], 0\n"
        "ds_read_b128 v[100:103], v241 offset:6208\n"
        "s_waitcnt lgkmcnt(5)\n"
        "v_mfma_f32_16x16x32_bf16 v[88:91], v[68:71], v[96:99], 0\n"
        "ds_read_b128 v[124:127], v240 offset:12352\n"
        "s_waitcnt lgkmcnt(5)\n"
        "v_mfma_f32_16x16x32_bf16 v[96:99], v[68:71], v[104:107], 0\n"
        "ds_read_b128 v[116:119], v241 offset:12352\n"
        "s_waitcnt lgkmcnt(5)\n"
        "v_mfma_f32_16x16x32_bf16 v[64:67], v[80:83], v[108:111], v[64:67]\n"
        "ds_read_b128 v[68:71], v240 offset:128\n"
        "s_waitcnt lgkmcnt(5)\n"
        "v_mfma_f32_16x16x32_bf16 v[72:75], v[80:83], v[120:123], v[72:75]\n"
        "ds_read_b128 v[108:111], v241 offset:128\n"
        "s_waitcnt lgkmcnt(5)\n"
        "v_mfma_f32_16x16x32_bf16 v[76:79], v[80:83], v[112:115], v[76:79]\n"
        "ds_read_b128 v[120:123], v240 offset:6272\n"
        "s_waitcnt lgkmcnt(5)\n"
        "v_mfma_f32_16x16x32_bf16 v[84:87], v[80:83], v[100:103], v[84:87]\n"
        "ds_read_b128 v[112:115], v241 offset:6272\n"
        "s_waitcnt lgkmcnt(5)\n"
        "v_mfma_f32_16x16x32_bf16 v[88:91], v[80:83], v[124:127], v[88:91]\n"
        "ds_read_b128 v[100:103], v240 offset:12416\n"
        "s_waitcnt lgkmcnt(5)\n"
        "v_mfma_f32_16x16x32_bf16 v[96:99], v[80:83], v[116:119], v[96:99]\n"
        "ds_read_b128 v[124:127], v241 offset:12416\n"
        "s_waitcnt lgkmcnt(5)\n"
        "v_mfma_f32_16x16x32_bf16 v[64:67], v[92:95], v[68:71], v[64:67]\n"
        "s_waitcnt lgkmcnt(4)\n"
        "v_mfma_f32_16x16x32_bf16 v[72:75], v[92:95], v[108:111], v[72:75]\n"
        "v_mov_b32 v81, s35\n"
        "s_waitcnt lgkmcnt(3)\n"
        "v_mfma_f32_16x16x32_bf16 v[76:79], v[92:95], v[120:123], v[76:79]\n"
        "s_waitcnt vmcnt(6)\n"
        "s_mov_b64 exec, 1\n"
        "ds_inc_u32 v81, v240 offset:0\n"
        "s_mov_b64 exec, -1\n"
        "s_waitcnt lgkmcnt(3)\n"
        "v_mfma_f32_16x16x32_bf16 v[84:87], v[92:95], v[112:115], v[84:87]\n"
        "s_waitcnt lgkmcnt(2)\n"
        "v_add_f32 v81, v64, v72\n"
        "v_add_f32 v81, v81, v76\n"
        "v_mfma_f32_16x16x32_bf16 v[88:91], v[92:95], v[100:103], v[88:91]\n"
        "s_waitcnt lgkmcnt(1)\n"
        "v_mfma_f32_16x16x32_bf16 v[96:99], v[92:95], v[124:127], v[96:99]\n"
        "v_mov_b32 v73, s37\n"
        "s_nop 0\n"
        "v_add_f32 v81, v81, v84\n"
        "s_nop 2\n"
        "v_add_f32 v81, v81, v88\n"
        "s_nop 0\n"
        "v_add_f32 v81, v81, v96\n"
        "s_nop 1\n"
        "v_add_f32_dpp v81, v81, v81 row_ror:8 row_mask:0xf bank_mask:0xf\n"
        "s_nop 1\n"
        "v_add_f32_dpp v81, v81, v81 row_ror:4 row_mask:0xf bank_mask:0xf\n"
        "s_nop 1\n"
        "v_add_f32_dpp v81, v81, v81 row_ror:2 row_mask:0xf bank_mask:0xf\n"
        "s_nop 1\n"
        "v_add_f32_dpp v81, v81, v81 row_ror:1 row_mask:0xf bank_mask:0xf\n"
        "v_mul_f32 v81, 0xbc2aaaab, v81\n"
        "v_mul_f32 v81, s34, v81\n"
        "v_fma_f32 v64, v64, s34, v81\n"
        "v_fma_f32 v72, v72, s34, v81\n"
        "v_fma_f32 v76, v76, s34, v81\n"
        "v_fma_f32 v84, v84, s34, v81\n"
        "v_fma_f32 v88, v88, s34, v81\n"
        "v_fma_f32 v96, v96, s34, v81\n"
        "ds_write_b32 v244, v64\n"
        "ds_write_b32 v244, v72 offset:16\n"
        "ds_write_b32 v244, v76 offset:128\n"
        "ds_write_b32 v244, v84 offset:144\n"
        "ds_write_b32 v244, v88 offset:256\n"
        "ds_write_b32 v244, v96 offset:272\n"
        "s_mov_b64 exec, 1\n"
        "ds_inc_u32 v73, v240 offset:0\n"
        "s_mov_b64 exec, -1\n"
        "s_setpc_b64 s[30:31]\n"
        "FOLDW_%=:\n"
        "ds_read_b64_tr_b16 v[64:65], v242 offset:0\n"
        "ds_read_b64_tr_b16 v[66:67], v243 offset:0\n"
        "ds_read_b128 v[176:179], v240\n"
        "ds_read_b64_tr_b16 v[180:181], v242 offset:6144\n"
        "ds_read_b64_tr_b16 v[182:183], v243 offset:6144\n"
        "ds_read_b128 v[116:119], v240 offset:64\n"
        "ds_read_b64_tr_b16 v[184:185], v242 offset:12288\n"
        "ds_read_b64_tr_b16 v[186:187], v243 offset:12288\n"
        "ds_read_b128 v[172:175], v240 offset:128\n"
        "ds_read_b128 v[148:151], v241\n"
        "ds_read_b128 v[168:171], v241 offset:64\n"
        "ds_read_b128 v[144:147], v241 offset:128\n"
        "ds_read_b128 v[132:135], v240 offset:6144\n"
        "ds_read_b128 v[100:103], v240 offset:6208\n"
        "ds_read_b128 v[108:111], v240 offset:6272\n"
        "ds_read_b128 v[88:91], v241 offset:6144\n"
        "ds_read_b128 v[92:95], v241 offset:6208\n"
        "ds_read_b128 v[84:87], v241 offset:6272\n"
        "s_waitcnt lgkmcnt(11)\n"
        "v_mfma_f32_16x16x32_bf16 v[128:131], v[176:179], v[64:67], 0\n"
        "ds_read_b128 v[96:99], v240 offset:12288\n"
        "v_mfma_f32_16x16x32_bf16 v[128:131], v[116:119], v[180:183], v[128:131]\n"
        "ds_read_b128 v[104:107], v240 offset:12352\n"
        "s_waitcnt lgkmcnt(9)\n"
        "v_mfma_f32_16x16x32_bf16 v[128:131], v[172:175], v[184:187], v[128:131]\n"
        "ds_read_b128 v[80:83], v240 offset:12416\n"
        "v_mfma_f32_16x16x32_bf16 v[136:139], v[148:151], v[64:67], 0\n"
        "ds_read_b128 v[68:71], v241 offset:12288\n"
        "v_mfma_f32_16x16x32_bf16 v[136:139], v[168:171], v[180:183], v[136:139]\n"
        "ds_read_b128 v[72:75], v241 offset:12352\n"
        "s_waitcnt lgkmcnt(6)\n"
        "v_mfma_f32_16x16x32_bf16 v[136:139], v[144:147], v[184:187], v[136:139]\n"
        "ds_read_b128 v[76:79], v241 offset:12416\n"
        "v_mfma_f32_16x16x32_bf16 v[140:143], v[132:135], v[64:67], 0\n"
        "ds_read_b64_tr_b16 v[112:113], v242 offset:8\n"
        "ds_read_b64_tr_b16 v[114:115], v243 offset:8\n"
        "v_mfma_f32_16x16x32_bf16 v[140:143], v[100:103], v[180:183], v[140:143]\n"
        "ds_read_b64_tr_b16 v[120:121], v242 offset:6152\n"
        "ds_read_b64_tr_b16 v[122:123], v243 offset:6152\n"
        "v_mfma_f32_16x16x32_bf16 v[140:143], v[108:111], v[184:187], v[140:143]\n"
        "ds_read_b64_tr_b16 v[124:125], v242 offset:12296\n"
        "ds_read_b64_tr_b16 v[126:127], v243 offset:12296\n"
        "v_add_f32 v156, v128, v136\n"
        "v_add_f32 v157, v129, v137\n"
        "v_add_f32 v158, v130, v138\n"
        "v_add_f32 v159, v131, v139\n"
        "v_mfma_f32_16x16x32_bf16 v[152:155], v[88:91], v[64:67], 0\n"
        "v_mfma_f32_16x16x32_bf16 v[152:155], v[92:95], v[180:183], v[152:155]\n"
        "s_waitcnt lgkmcnt(6)\n"
        "v_add_f32 v156, v156, v140\n"
        "v_add_f32 v157, v157, v141\n"
        "v_add_f32 v158, v158, v142\n"
        "v_add_f32 v159, v159, v143\n"
        "v_mfma_f32_16x16x32_bf16 v[152:155], v[84:87], v[184:187], v[152:155]\n"
        "v_mfma_f32_16x16x32_bf16 v[160:163], v[96:99], v[64:67], 0\n"
        "v_mfma_f32_16x16x32_bf16 v[160:163], v[104:107], v[180:183], v[160:163]\n"
        "v_mfma_f32_16x16x32_bf16 v[160:163], v[80:83], v[184:187], v[160:163]\n"
        "v_mfma_f32_16x16x32_bf16 v[164:167], v[68:71], v[64:67], 0\n"
        "v_mfma_f32_16x16x32_bf16 v[164:167], v[72:75], v[180:183], v[164:167]\n"
        "v_mfma_f32_16x16x32_bf16 v[164:167], v[76:79], v[184:187], v[164:167]\n"
        "s_waitcnt lgkmcnt(0)\n"
        "v_mov_b32 v187, s35\n"
        "v_add_f32 v156, v156, v152\n"
        "v_add_f32 v157, v157, v153\n"
        "v_add_f32 v158, v158, v154\n"
        "v_add_f32 v159, v159, v155\n"
        "v_add_f32 v156, v156, v160\n"
        "v_add_f32 v157, v157, v161\n"
        "v_add_f32 v158, v158, v162\n"
        "v_add_f32 v159, v159, v163\n"
        "v_add_f32 v156, v156, v164\n"
        "v_add_f32 v157, v157, v165\n"
        "v_add_f32 v158, v158, v166\n"
        "v_mfma_f32_16x16x32_bf16 v[64:67], v[176:179], v[112:115], 0\n"
        "s_waitcnt vmcnt(6)\n"
        "s_mov_b64 exec, 1\n"
        "ds_inc_u32 v187, v240 offset:0\n"
        "s_mov_b64 exec, -1\n"
        "v_add_f32 v159, v159, v167\n"
        "v_add_f32 v156, v156, v157\n"
        "v_add_f32 v158, v158, v159\n"
        "v_add_f32 v156, v156, v158\n"
        "v_mov_b32 v157, v156\n"
        "v_mfma_f32_16x16x32_bf16 v[64:67], v[116:119], v[120:123], v[64:67]\n"
        "v_mfma_f32_16x16x32_bf16 v[64:67], v[172:175], v[124:127], v[64:67]\n"
        "v_permlane32_swap_b32 v156, v157\n"
        "v_add_f32 v156, v156, v157\n"
        "v_mov_b32 v157, v156\n"
        "v_mfma_f32_16x16x32_bf16 v[116:119], v[148:151], v[112:115], 0\n"
        "v_mfma_f32_16x16x32_bf16 v[116:119], v[168:171], v[120:123], v[116:119]\n"
        "global_load_dwordx4 v[40:43], v17, s[22:23] offset:-2048\n"
        "v_permlane16_swap_b32 v156, v157\n"
        "v_add_f32 v156, v156, v157\n"
        "v_mul_f32 v156, 0xbc2aaaab, v156\n"
        "v_mul_f32 v156, s34, v156\n"
        "v_fma_f32 v128, v128, s34, v156\n"
        "v_fma_f32 v129, v129, s34, v156\n"
        "v_fma_f32 v130, v130, s34, v156\n"
        "v_fma_f32 v131, v131, s34, v156\n"
        "v_fma_f32 v136, v136, s34, v156\n"
        "v_fma_f32 v137, v137, s34, v156\n"
        "v_fma_f32 v138, v138, s34, v156\n"
        "v_mfma_f32_16x16x32_bf16 v[116:119], v[144:147], v[124:127], v[116:119]\n"
        "v_fma_f32 v139, v139, s34, v156\n"
        "v_fma_f32 v140, v140, s34, v156\n"
        "v_fma_f32 v141, v141, s34, v156\n"
        "v_fma_f32 v142, v142, s34, v156\n"
        "v_fma_f32 v143, v143, s34, v156\n"
        "v_fma_f32 v152, v152, s34, v156\n"
        "v_fma_f32 v153, v153, s34, v156\n"
        "v_fma_f32 v154, v154, s34, v156\n"
        "v_add_f32 v144, v64, v116\n"
        "v_add_f32 v145, v65, v117\n"
        "v_add_f32 v146, v66, v118\n"
        "v_add_f32 v147, v67, v119\n"
        "v_fma_f32 v155, v155, s34, v156\n"
        "v_fma_f32 v160, v160, s34, v156\n"
        "v_fma_f32 v161, v161, s34, v156\n"
        "v_fma_f32 v162, v162, s34, v156\n"
        "v_mfma_f32_16x16x32_bf16 v[148:151], v[132:135], v[112:115], 0\n"
        "global_load_dwordx4 v[44:47], v17, s[22:23] offset:-1024\n"
        "v_fma_f32 v163, v163, s34, v156\n"
        "v_fma_f32 v164, v164, s34, v156\n"
        "v_fma_f32 v165, v165, s34, v156\n"
        "v_fma_f32 v166, v166, s34, v156\n"
        "v_fma_f32 v167, v167, s34, v156\n"
        "v_cvt_pk_bf16_f32 v132, v128, v129\n"
        "v_cvt_pk_bf16_f32 v133, v130, v131\n"
        "v_cvt_pk_bf16_f32 v134, v136, v137\n"
        "v_cvt_pk_bf16_f32 v135, v138, v139\n"
        "v_cvt_pk_bf16_f32 v136, v140, v141\n"
        "v_cvt_pk_bf16_f32 v137, v142, v143\n"
        "v_cvt_pk_bf16_f32 v138, v152, v153\n"
        "v_cvt_pk_bf16_f32 v139, v154, v155\n"
        "v_cvt_pk_bf16_f32 v140, v160, v161\n"
        "v_cvt_pk_bf16_f32 v141, v162, v163\n"
        "v_cvt_pk_bf16_f32 v142, v164, v165\n"
        "v_cvt_pk_bf16_f32 v143, v166, v167\n"
        "v_mfma_f32_16x16x32_bf16 v[148:151], v[100:103], v[120:123], v[148:151]\n"
        "v_mfma_f32_16x16x32_bf16 v[148:151], v[108:111], v[124:127], v[148:151]\n"
        "global_load_dwordx4 v[0:3], v17, s[22:23] offset:0\n"
        "v_mfma_f32_16x16x32_bf16 v[108:111], v[88:91], v[112:115], 0\n"
        "v_mfma_f32_16x16x32_bf16 v[108:111], v[92:95], v[120:123], v[108:111]\n"
        "global_load_dwordx4 v[4:7], v17, s[22:23] offset:1024\n"
        "v_mfma_f32_16x16x32_bf16 v[108:111], v[84:87], v[124:127], v[108:111]\n"
        "v_mfma_f32_16x16x32_bf16 v[92:95], v[96:99], v[112:115], 0\n"
        "global_load_dwordx4 v[8:11], v17, s[22:23] offset:2048\n"
        "v_mfma_f32_16x16x32_bf16 v[92:95], v[104:107], v[120:123], v[92:95]\n"
        "v_add_f32 v144, v144, v148\n"
        "v_add_f32 v145, v145, v149\n"
        "v_add_f32 v146, v146, v150\n"
        "v_add_f32 v147, v147, v151\n"
        "v_mfma_f32_16x16x32_bf16 v[92:95], v[80:83], v[124:127], v[92:95]\n"
        "global_load_dwordx4 v[12:15], v17, s[22:23] offset:3072\n"
        "v_add_f32 v144, v144, v108\n"
        "v_add_f32 v145, v145, v109\n"
        "v_add_f32 v146, v146, v110\n"
        "v_add_f32 v147, v147, v111\n"
        "v_mfma_f32_16x16x32_bf16 v[80:83], v[68:71], v[112:115], 0\n"
        "v_mfma_f32_16x16x32_bf16 v[80:83], v[72:75], v[120:123], v[80:83]\n"
        "v_mfma_f32_16x16x32_bf16 v[80:83], v[76:79], v[124:127], v[80:83]\n"
        "v_add_f32 v144, v144, v92\n"
        "v_add_f32 v145, v145, v93\n"
        "v_add_f32 v146, v146, v94\n"
        "v_add_f32 v147, v147, v95\n"
        "v_mov_b32 v181, s37\n"
        "s_nop 2\n"
        "v_add_f32 v144, v144, v80\n"
        "v_add_f32 v145, v145, v81\n"
        "v_add_f32 v146, v146, v82\n"
        "v_add_f32 v147, v147, v83\n"
        "v_add_f32 v144, v144, v145\n"
        "v_add_f32 v146, v146, v147\n"
        "v_add_f32 v144, v144, v146\n"
        "v_mov_b32 v145, v144\n"
        "s_nop 1\n"
        "v_permlane32_swap_b32 v144, v145\n"
        "v_add_f32 v144, v144, v145\n"
        "v_mov_b32 v145, v144\n"
        "s_nop 1\n"
        "v_permlane16_swap_b32 v144, v145\n"
        "v_add_f32 v144, v144, v145\n"
        "v_mul_f32 v144, 0xbc2aaaab, v144\n"
        "v_mul_f32 v144, s34, v144\n"
        "v_fma_f32 v64, v64, s34, v144\n"
        "v_fma_f32 v65, v65, s34, v144\n"
        "v_fma_f32 v66, v66, s34, v144\n"
        "v_fma_f32 v67, v67, s34, v144\n"
        "v_fma_f32 v116, v116, s34, v144\n"
        "v_fma_f32 v117, v117, s34, v144\n"
        "v_fma_f32 v118, v118, s34, v144\n"
        "v_fma_f32 v119, v119, s34, v144\n"
        "v_fma_f32 v148, v148, s34, v144\n"
        "v_fma_f32 v149, v149, s34, v144\n"
        "v_fma_f32 v150, v150, s34, v144\n"
        "v_fma_f32 v151, v151, s34, v144\n"
        "v_fma_f32 v108, v108, s34, v144\n"
        "v_fma_f32 v109, v109, s34, v144\n"
        "v_fma_f32 v110, v110, s34, v144\n"
        "v_fma_f32 v111, v111, s34, v144\n"
        "v_fma_f32 v92, v92, s34, v144\n"
        "v_fma_f32 v93, v93, s34, v144\n"
        "v_fma_f32 v94, v94, s34, v144\n"
        "v_fma_f32 v95, v95, s34, v144\n"
        "v_fma_f32 v80, v80, s34, v144\n"
        "v_fma_f32 v81, v81, s34, v144\n"
        "v_fma_f32 v82, v82, s34, v144\n"
        "v_fma_f32 v83, v83, s34, v144\n"
        "v_cvt_pk_bf16_f32 v176, v64, v65\n"
        "v_cvt_pk_bf16_f32 v177, v66, v67\n"
        "v_cvt_pk_bf16_f32 v178, v116, v117\n"
        "v_cvt_pk_bf16_f32 v179, v118, v119\n"
        "v_cvt_pk_bf16_f32 v64, v148, v149\n"
        "v_cvt_pk_bf16_f32 v65, v150, v151\n"
        "v_cvt_pk_bf16_f32 v66, v108, v109\n"
        "v_cvt_pk_bf16_f32 v67, v110, v111\n"
        "v_cvt_pk_bf16_f32 v116, v92, v93\n"
        "v_cvt_pk_bf16_f32 v117, v94, v95\n"
        "v_cvt_pk_bf16_f32 v118, v80, v81\n"
        "v_cvt_pk_bf16_f32 v119, v82, v83\n"
        "s_nop 1\n"
        "v_mov_b32 v151, s36\n"
        "L29_%=:\n"
        "ds_read_b32 v150, v151 offset:0\n"
        "s_waitcnt lgkmcnt(0)\n"
        "v_readfirstlane_b32 s40, v150\n"
        "s_nop 3\n"
        "s_cmp_ge_i32 s40, 4\n"
        "s_cbranch_scc1 L30_%=\n"
        "s_nop 0\n"
        "s_sub_u32 s41, s41, 1\n"
        "s_cbranch_scc0 L29_%=\n"
        "s_endpgm\n"
        "L30_%=:\n"
        "ds_write_b128 v244, v[132:135]\n"
        "ds_write_b128 v245, v[176:179]\n"
        "ds_write_b128 v244, v[136:139] offset:1024\n"
        "ds_write_b128 v245, v[64:67] offset:1024\n"
        "ds_write_b128 v244, v[140:143] offset:2048\n"
        "ds_write_b128 v245, v[116:119] offset:2048\n"
        "s_mov_b64 exec, 1\n"
        "ds_inc_u32 v181, v240 offset:0\n"
        "s_mov_b64 exec, -1\n"
        "s_setpc_b64 s[30:31]\n"
        "FOLDU_%=:\n"
        "ds_read_b128 v[84:87], v242\n"
        "ds_read_b128 v[76:79], v242 offset:64\n"
        "ds_read_b128 v[100:103], v242 offset:128\n"
        "ds_read_b128 v[96:99], v243\n"
        "ds_read_b128 v[68:71], v243 offset:64\n"
        "ds_read_b128 v[72:75], v243 offset:128\n"
        "v_mov_b32 v64, 0x3f803f80\n"
        "v_mov_b32 v65, 0x3f803f80\n"
        "v_mov_b32 v66, 0x3f803f80\n"
        "v_mov_b32 v67, 0x3f803f80\n"
        "ds_read_b128 v[88:91], v242 offset:6144\n"
        "s_waitcnt lgkmcnt(6)\n"
        "v_mfma_f32_16x16x32_bf16 v[108:111], v[84:87], v[64:67], 0\n"
        "s_waitcnt lgkmcnt(5)\n"
        "v_mfma_f32_16x16x32_bf16 v[108:111], v[76:79], v[64:67], v[108:111]\n"
        "ds_read_b128 v[84:87], v242 offset:6208\n"
        "s_waitcnt lgkmcnt(3)\n"
        "v_mfma_f32_16x16x32_bf16 v[108:111], v[100:103], v[64:67], v[108:111]\n"
        "ds_read_b128 v[76:79], v242 offset:6272\n"
        "v_mfma_f32_16x16x32_bf16 v[104:107], v[96:99], v[64:67], 0\n"
        "ds_read_b128 v[100:103], v243 offset:6144\n"
        "v_mfma_f32_16x16x32_bf16 v[104:107], v[68:71], v[64:67], v[104:107]\n"
        "ds_read_b128 v[96:99], v243 offset:6208\n"
        "s_waitcnt lgkmcnt(4)\n"
        "v_mfma_f32_16x16x32_bf16 v[104:107], v[72:75], v[64:67], v[104:107]\n"
        "ds_read_b128 v[68:71], v243 offset:6272\n"
        "v_cvt_pk_bf16_f32 v80, v108, v109\n"
        "v_cvt_pk_bf16_f32 v81, v110, v111\n"
        "v_mfma_f32_16x16x32_bf16 v[108:111], v[88:91], v[64:67], 0\n"
        "ds_read_b128 v[72:75], v242 offset:12288\n"
        "s_waitcnt lgkmcnt(5)\n"
        "v_mfma_f32_16x16x32_bf16 v[108:111], v[84:87], v[64:67], v[108:111]\n"
        "ds_read_b128 v[88:91], v242 offset:12352\n"
        "v_cvt_pk_bf16_f32 v82, v104, v105\n"
        "v_cvt_pk_bf16_f32 v83, v106, v107\n"
        "s_waitcnt lgkmcnt(5)\n"
        "v_mfma_f32_16x16x32_bf16 v[108:111], v[76:79], v[64:67], v[108:111]\n"
        "ds_read_b128 v[84:87], v242 offset:12416\n"
        "s_waitcnt lgkmcnt(5)\n"
        "v_mfma_f32_16x16x32_bf16 v[104:107], v[100:103], v[64:67], 0\n"
        "ds_read_b128 v[76:79], v243 offset:12288\n"
        "s_waitcnt lgkmcnt(5)\n"
        "v_mfma_f32_16x16x32_bf16 v[104:107], v[96:99], v[64:67], v[104:107]\n"
        "ds_read_b128 v[100:103], v243 offset:12352\n"
        "s_waitcnt lgkmcnt(5)\n"
        "v_mfma_f32_16x16x32_bf16 v[104:107], v[68:71], v[64:67], v[104:107]\n"
        "ds_read_b128 v[96:99], v243 offset:12416\n"
        "v_cvt_pk_bf16_f32 v92, v108, v109\n"
        "v_cvt_pk_bf16_f32 v93, v110, v111\n"
        "s_waitcnt lgkmcnt(5)\n"
        "v_mfma_f32_16x16x32_bf16 v[108:111], v[72:75], v[64:67], 0\n"
        "ds_read_b128 v[68:71], v240\n"
        "s_waitcnt lgkmcnt(5)\n"
        "v_mfma_f32_16x16x32_bf16 v[108:111], v[88:91], v[64:67], v[108:111]\n"
        "ds_read_b128 v[72:75], v241\n"
        "v_cvt_pk_bf16_f32 v94, v104, v105\n"
        "v_cvt_pk_bf16_f32 v95, v106, v107\n"
        "s_waitcnt lgkmcnt(5)\n"
        "v_mfma_f32_16x16x32_bf16 v[108:111], v[84:87], v[64:67], v[108:111]\n"
        "ds_read_b128 v[88:91], v240 offset:6144\n"
        "s_waitcnt lgkmcnt(5)\n"
        "v_mfma_f32_16x16x32_bf16 v[116:119], v[76:79], v[64:67], 0\n"
        "ds_read_b128 v[84:87], v241 offset:6144\n"
        "s_waitcnt lgkmcnt(5)\n"
        "v_mfma_f32_16x16x32_bf16 v[116:119], v[100:103], v[64:67], v[116:119]\n"
        "ds_read_b128 v[104:107], v240 offset:12288\n"
        "s_waitcnt lgkmcnt(5)\n"
        "v_mfma_f32_16x16x32_bf16 v[116:119], v[96:99], v[64:67], v[116:119]\n"
        "ds_read_b128 v[100:103], v241 offset:12288\n"
        "v_cvt_pk_bf16_f32 v96, v108, v109\n"
        "v_cvt_pk_bf16_f32 v97, v110, v111\n"
        "s_waitcnt lgkmcnt(5)\n"
        "v_mfma_f32_16x16x32_bf16 v[64:67], v[80:83], v[68:71], 0\n"
        "ds_read_b128 v[108:111], v240 offset:64\n"
        "s_waitcnt lgkmcnt(5)\n"
        "v_mfma_f32_16x16x32_bf16 v[68:71], v[80:83], v[72:75], 0\n"
        "ds_read_b128 v[112:115], v241 offset:64\n"
        "v_cvt_pk_bf16_f32 v98, v116, v117\n"
        "v_cvt_pk_bf16_f32 v99, v118, v119\n"
        "s_waitcnt lgkmcnt(5)\n"
        "v_mfma_f32_16x16x32_bf16 v[72:75], v[80:83], v[88:91], 0\n"
        "ds_read_b128 v[116:119], v240 offset:6208\n"
        "s_waitcnt lgkmcnt(5)\n"
        "v_mfma_f32_16x16x32_bf16 v[76:79], v[80:83], v[84:87], 0\n"
        "ds_read_b128 v[120:123], v241 offset:6208\n"
        "s_waitcnt lgkmcnt(5)\n"
        "v_mfma_f32_16x16x32_bf16 v[84:87], v[80:83], v[104:107], 0\n"
        "ds_read_b128 v[124:127], v240 offset:12352\n"
        "s_waitcnt lgkmcnt(5)\n"
        "v_mfma_f32_16x16x32_bf16 v[88:91], v[80:83], v[100:103], 0\n"
        "ds_read_b128 v[104:107], v241 offset:12352\n"
        "s_waitcnt lgkmcnt(5)\n"
        "v_mfma_f32_16x16x32_bf16 v[64:67], v[92:95], v[108:111], v[64:67]\n"
        "ds_read_b128 v[80:83], v240 offset:128\n"
        "s_waitcnt lgkmcnt(5)\n"
        "v_mfma_f32_16x16x32_bf16 v[68:71], v[92:95], v[112:115], v[68:71]\n"
        "ds_read_b128 v[100:103], v241 offset:128\n"
        "s_waitcnt lgkmcnt(5)\n"
        "v_mfma_f32_16x16x32_bf16 v[72:75], v[92:95], v[116:119], v[72:75]\n"
        "ds_read_b128 v[108:111], v240 offset:6272\n"
        "s_waitcnt lgkmcnt(5)\n"
        "v_mfma_f32_16x16x32_bf16 v[76:79], v[92:95], v[120:123], v[76:79]\n"
        "ds_read_b128 v[112:115], v241 offset:6272\n"
        "s_waitcnt lgkmcnt(5)\n"
        "v_mfma_f32_16x16x32_bf16 v[84:87], v[92:95], v[124:127], v[84:87]\n"
        "ds_read_b128 v[116:119], v240 offset:12416\n"
        "s_waitcnt lgkmcnt(5)\n"
        "v_mfma_f32_16x16x32_bf16 v[88:91], v[92:95], v[104:107], v[88:91]\n"
        "ds_read_b128 v[120:123], v241 offset:12416\n"
        "s_waitcnt lgkmcnt(5)\n"
        "v_mfma_f32_16x16x32_bf16 v[64:67], v[96:99], v[80:83], v[64:67]\n"
        "s_waitcnt lgkmcnt(4)\n"
        "v_mfma_f32_16x16x32_bf16 v[68:71], v[96:99], v[100:103], v[68:71]\n"
        "v_mov_b32 v83, s35\n"
        "s_waitcnt lgkmcnt(3)\n"
        "v_mfma_f32_16x16x32_bf16 v[72:75], v[96:99], v[108:111], v[72:75]\n"
        "s_waitcnt vmcnt(6)\n"
        "s_mov_b64 exec, 1\n"
        "ds_inc_u32 v83, v240 offset:0\n"
        "s_mov_b64 exec, -1\n"
        "s_waitcnt lgkmcnt(2)\n"
        "v_mfma_f32_16x16x32_bf16 v[76:79], v[96:99], v[112:115], v[76:79]\n"
        "v_add_f32 v103, v64, v68\n"
        "v_mfma_f32_16x16x32_bf16 v[84:87], v[96:99], v[116:119], v[84:87]\n"
        "s_waitcnt lgkmcnt(1)\n"
        "v_add_f32 v103, v103, v72\n"
        "v_mfma_f32_16x16x32_bf16 v[88:91], v[96:99], v[120:123], v[88:91]\n"
        "v_mov_b32 v71, s37\n"
        "s_nop 1\n"
        "v_add_f32 v103, v103, v76\n"
        "s_nop 0\n"
        "v_add_f32 v103, v103, v84\n"
        "s_nop 1\n"
        "v_add_f32 v103, v103, v88\n"
        "s_nop 1\n"
        "v_add_f32_dpp v103, v103, v103 row_ror:8 row_mask:0xf bank_mask:0xf\n"
        "s_nop 1\n"
        "v_add_f32_dpp v103, v103, v103 row_ror:4 row_mask:0xf bank_mask:0xf\n"
        "s_nop 1\n"
        "v_add_f32_dpp v103, v103, v103 row_ror:2 row_mask:0xf bank_mask:0xf\n"
        "s_nop 1\n"
        "v_add_f32_dpp v103, v103, v103 row_ror:1 row_mask:0xf bank_mask:0xf\n"
        "v_mul_f32 v103, 0xbc2aaaab, v103\n"
        "v_mul_f32 v103, s34, v103\n"
        "v_fma_f32 v64, v64, s34, v103\n"
        "v_fma_f32 v68, v68, s34, v103\n"
        "v_fma_f32 v72, v72, s34, v103\n"
        "v_fma_f32 v76, v76, s34, v103\n"
        "v_fma_f32 v84, v84, s34, v103\n"
        "v_fma_f32 v88, v88, s34, v103\n"
        "v_cvt_pk_bf16_f32 v64, v64, v64\n"
        "v_cvt_pk_bf16_f32 v68, v68, v68\n"
        "v_cvt_pk_bf16_f32 v72, v72, v72\n"
        "v_cvt_pk_bf16_f32 v76, v76, v76\n"
        "v_cvt_pk_bf16_f32 v84, v84, v84\n"
        "v_cvt_pk_bf16_f32 v88, v88, v88\n"
        "ds_write_b16 v244, v64\n"
        "ds_write_b16 v244, v68 offset:8\n"
        "ds_write_b16 v244, v72 offset:64\n"
        "ds_write_b16 v244, v76 offset:72\n"
        "ds_write_b16 v244, v84 offset:128\n"
        "ds_write_b16 v244, v88 offset:136\n"
        "s_mov_b64 exec, 1\n"
        "ds_inc_u32 v71, v240 offset:0\n"
        "s_mov_b64 exec, -1\n"
        "s_setpc_b64 s[30:31]\n"
        "PPRO_%=:\n"
        "s_cmp_lg_u32 s6, 0\n"
        "s_cbranch_scc1 L44_%=\n"
        "s_mov_b64 exec, 0xffff\n"
        "v_lshlrev_b32 v27, 2, v16\n"
        "v_add_u32 v27, 116224, v27\n"
        "v_mov_b32 v28, 0\n"
        "ds_write_b32 v27, v28\n"
        "s_mov_b64 exec, 0x7ffff\n"
        "s_bfe_u32 s7, s2, 0x50003\n"
        "s_mul_i32 s7, s7, 2400\n"
        "v_lshlrev_b32 v27, 7, v16\n"
        "v_add_u32 v27, s7, v27\n"
        "global_load_dword v252, v27, s[4:5]\n"
        "s_mov_b64 exec, -1\n"
        "L44_%=:\n"
        "s_cmp_lt_u32 s6, 2\n"
        "s_cselect_b32 s65, 3, 1\n"
        "s_cselect_b32 s61, 384, 0\n"
        "s_cselect_b32 s62, 9, 1\n"
        "s_cselect_b32 s63, 3, 4\n"
        "s_cselect_b32 s26, 3, 1\n"
        "s_cselect_b32 s34, 9, 1\n"
        "s_cselect_b32 s70, 72, 1\n"
        "s_cselect_b32 s71, 36, 4\n"
        "s_cselect_b32 s72, 6, 8\n"
        "s_mul_i32 s70, s70, 192\n"
        "s_mul_i32 s71, s71, 192\n"
        "s_mul_i32 s72, s72, 192\n"
        "s_mul_i32 s65, s65, 192\n"
        "s_mul_i32 s62, s62, 3072\n"
        "s_mul_i32 s63, s63, 1536\n"
        "s_mul_i32 s26, s26, 18432\n"
        "s_mul_i32 s34, s34, 1536\n"
        "s_add_u32 s7, s62, s63\n"
        "s_lshl_b32 s29, s63, 1\n"
        "s_add_u32 s60, s7, s63\n"
        "s_bitcmp1_b32 s6, 0\n"
        "s_cselect_b32 s28, s7, 0\n"
        "s_cselect_b32 s29, s29, s62\n"
        "s_cselect_b32 s60, s60, s63\n"
        "s_add_u32 s28, s28, s61\n"
        "s_add_u32 s29, s29, s61\n"
        "s_add_u32 s60, s60, s61\n"
        "v_add_u32 v24, 0, v16\n"
        "v_mul_u32_u24 v21, 171, v24\n"
        "v_lshrrev_b32 v21, 11, v21\n"
        "v_mad_i32_i24 v24, v21, -12, v24\n"
        "v_bfe_u32 v18, v21, 2, 2\n"
        "v_xor_b32 v18, v24, v18\n"
        "v_and_b32 v27, 3, v21\n"
        "v_mul_u32_u24 v27, s70, v27\n"
        "v_lshl_add_u32 v32, v18, 4, v27\n"
        "v_bfe_u32 v27, v21, 2, 1\n"
        "v_mad_u32_u24 v32, v27, s71, v32\n"
        "v_lshrrev_b32 v27, 3, v21\n"
        "v_mad_u32_u24 v32, v27, s72, v32\n"
        "v_and_b32 v27, 7, v21\n"
        "v_mul_u32_u24 v27, s65, v27\n"
        "v_lshl_add_u32 v18, v18, 4, v27\n"
        "v_add_u32 v25, 64, v16\n"
        "v_mul_u32_u24 v22, 171, v25\n"
        "v_lshrrev_b32 v22, 11, v22\n"
        "v_mad_i32_i24 v25, v22, -12, v25\n"
        "v_bfe_u32 v19, v22, 2, 2\n"
        "v_xor_b32 v19, v25, v19\n"
        "v_and_b32 v27, 3, v22\n"
        "v_mul_u32_u24 v27, s70, v27\n"
        "v_lshl_add_u32 v33, v19, 4, v27\n"
        "v_bfe_u32 v27, v22, 2, 1\n"
        "v_mad_u32_u24 v33, v27, s71, v33\n"
        "v_lshrrev_b32 v27, 3, v22\n"
        "v_mad_u32_u24 v33, v27, s72, v33\n"
        "v_and_b32 v27, 7, v22\n"
        "v_mul_u32_u24 v27, s65, v27\n"
        "v_lshl_add_u32 v19, v19, 4, v27\n"
        "v_lshrrev_b32 v27, 3, v22\n"
        "v_mad_u32_u24 v19, v27, s34, v19\n"
        "v_add_u32 v26, 128, v16\n"
        "v_mul_u32_u24 v23, 171, v26\n"
        "v_lshrrev_b32 v23, 11, v23\n"
        "v_mad_i32_i24 v26, v23, -12, v26\n"
        "v_bfe_u32 v20, v23, 2, 2\n"
        "v_xor_b32 v20, v26, v20\n"
        "v_and_b32 v27, 3, v23\n"
        "v_mul_u32_u24 v27, s70, v27\n"
        "v_lshl_add_u32 v34, v20, 4, v27\n"
        "v_bfe_u32 v27, v23, 2, 1\n"
        "v_mad_u32_u24 v34, v27, s71, v34\n"
        "v_lshrrev_b32 v27, 3, v23\n"
        "v_mad_u32_u24 v34, v27, s72, v34\n"
        "v_and_b32 v27, 7, v23\n"
        "v_mul_u32_u24 v27, s65, v27\n"
        "v_lshl_add_u32 v20, v20, 4, v27\n"
        "v_add_u32 v20, s34, v20\n"
        "s_and_b32 s7, s2, 7\n"
        "s_lshl_b32 s7, s7, 5\n"
        "s_lshr_b32 s14, s2, 3\n"
        "s_add_u32 s7, s7, s14\n"
        "s_mul_i32 s7, s7, 5329\n"
        "s_add_u32 s14, s7, 5329\n"
        "s_lshr_b32 s7, s7, 8\n"
        "s_lshr_b32 s14, s14, 8\n"
        "s_sub_u32 s14, s14, s7\n"
        "s_mul_i32 s15, s7, 57457\n"
        "s_lshr_b32 s15, s15, 22\n"
        "s_mul_i32 s16, s15, 73\n"
        "s_sub_u32 s16, s7, s16\n"
        "s_mul_i32 s15, s15, 686784\n"
        "s_mul_i32 s7, s16, 1344\n"
        "s_add_u32 s15, s15, s7\n"
        "s_sub_u32 s7, 6, s6\n"
        "v_mov_b32 v28, 96768\n"
        "s_mov_b32 s35, 50135232\n"
        "v_mov_b32 v29, 2048\n"
        "v_and_b32 v21, 7, v21\n"
        "v_cmp_lt_u32 vcc, s7, v21\n"
        "v_and_b32 v27, 3, v24\n"
        "v_mad_u32_u24 v29, v27, 48, v29\n"
        "v_lshrrev_b32 v27, 2, v24\n"
        "v_lshl_add_u32 v29, v27, 4, v29\n"
        "v_mul_u32_u24 v21, 192, v21\n"
        "v_cndmask_b32 v27, 0, v28, vcc\n"
        "v_add3_u32 v29, v29, v21, v27\n"
        "v_lshrrev_b32 v27, 3, v22\n"
        "v_xor_b32 v25, v25, v27\n"
        "v_mul_lo_u32 v30, v27, s35\n"
        "v_add_u32 v30, 1024, v30\n"
        "v_and_b32 v22, 7, v22\n"
        "v_cmp_lt_u32 vcc, s7, v22\n"
        "v_and_b32 v27, 3, v25\n"
        "v_mad_u32_u24 v30, v27, 48, v30\n"
        "v_lshrrev_b32 v27, 2, v25\n"
        "v_lshl_add_u32 v30, v27, 4, v30\n"
        "v_mul_u32_u24 v22, 192, v22\n"
        "v_cndmask_b32 v27, 0, v28, vcc\n"
        "v_add3_u32 v30, v30, v22, v27\n"
        "v_mov_b32 v31, 50135232\n"
        "v_xor_b32 v26, 1, v26\n"
        "v_and_b32 v23, 7, v23\n"
        "v_cmp_lt_u32 vcc, s7, v23\n"
        "v_and_b32 v27, 3, v26\n"
        "v_mad_u32_u24 v31, v27, 48, v31\n"
        "v_lshrrev_b32 v27, 2, v26\n"
        "v_lshl_add_u32 v31, v27, 4, v31\n"
        "v_mul_u32_u24 v23, 192, v23\n"
        "v_cndmask_b32 v27, 0, v28, vcc\n"
        "v_add3_u32 v31, v31, v23, v27\n"
        "s_mul_i32 s17, s6, 6208\n"
        "s_add_u32 s17, s17, 116288\n"
        "s_mov_b32 s18, s6\n"
        "s_mul_i32 s35, s6, 98304\n"
        "s_add_u32 s35, s35, s15\n"
        "s_add_u32 s36, s35, 100270464\n"
        "v_mov_b32 v250, 116224\n"
        "s_mov_b32 s41, 0x400000\n"
        "s_waitcnt lgkmcnt(0)\n"
        "s_cmp_lg_u32 s6, 0\n"
        "s_cbranch_scc1 L45_%=\n"
        "s_bitcmp1_b32 s2, 7\n"
        "s_cselect_b64 s[8:9], s[50:51], s[46:47]\n"
        "s_cselect_b64 s[10:11], s[52:53], s[48:49]\n"
        "s_bfe_u32 s7, s2, 0x40003\n"
        "s_cmp_ge_u32 s7, 12\n"
        "s_cselect_b64 s[8:9], s[10:11], s[8:9]\n"
        "s_cselect_b32 s10, 12, 0\n"
        "s_sub_u32 s7, s7, s10\n"
        "s_mul_i32 s7, s7, 4608\n"
        "v_lshlrev_b32 v27, 7, v16\n"
        "v_add_u32 v27, s7, v27\n"
        "s_mov_b32 exec_hi, 0xf\n"
        "global_load_dword v252, v27, s[8:9]\n"
        "s_mov_b32 exec_hi, -1\n"
        "L45_%=:\n"
        "s_cmp_lt_u32 s6, 2\n"
        "s_cselect_b64 s[24:25], s[46:47], s[48:49]\n"
        "s_and_b32 s25, s25, 0xffff\n"
        "s_sub_u32 s8, s44, 2048\n"
        "s_subb_u32 s9, s45, 0\n"
        "s_and_b32 s9, s9, 0xffff\n"
        "s_mov_b32 s10, 200542976\n"
        "s_mov_b32 s11, 0x20000\n"
        "s_mul_i32 s7, s6, 6144\n"
        "s_add_u32 s7, s7, 5120\n"
        "s_add_u32 s22, s4, s7\n"
        "s_addc_u32 s23, s5, 0\n"
        "s_sub_u32 s7, s6, 2\n"
        "s_and_b32 s7, s7, 3\n"
        "s_min_u32 s7, s7, 2\n"
        "s_lshl_b32 s7, s7, 10\n"
        "s_add_u32 m0, s7, 24704\n"
        "v_add_u32 v27, s7, v17\n"
        "s_barrier\n"
        "global_load_lds_dwordx4 v27, s[4:5]\n"
        "global_load_dwordx4 v[240:243], v17, s[22:23] offset:-2048\n"
        "global_load_dwordx4 v[244:247], v17, s[22:23] offset:-1024\n"
        "s_add_u32 m0, s64, 0\n"
        "s_nop 0\n"
        "buffer_load_dwordx4 v18, s[24:27], s28 offen lds\n"
        "s_add_u32 m0, s64, 1024\n"
        "s_nop 0\n"
        "buffer_load_dwordx4 v19, s[24:27], s28 offen lds\n"
        "s_add_u32 m0, s64, 2048\n"
        "s_nop 0\n"
        "buffer_load_dwordx4 v20, s[24:27], s28 offen lds\n"
        "s_add_u32 m0, s64, 3072\n"
        "s_nop 0\n"
        "buffer_load_dwordx4 v18, s[24:27], s29 offen lds\n"
        "s_add_u32 m0, s64, 4096\n"
        "s_nop 0\n"
        "buffer_load_dwordx4 v19, s[24:27], s29 offen lds\n"
        "s_add_u32 m0, s64, 5120\n"
        "s_nop 0\n"
        "buffer_load_dwordx4 v20, s[24:27], s29 offen lds\n"
        "s_add_u32 m0, s64, 6144\n"
        "s_nop 0\n"
        "buffer_load_dwordx4 v18, s[24:27], s60 offen lds\n"
        "s_add_u32 m0, s64, 7168\n"
        "s_nop 0\n"
        "buffer_load_dwordx4 v19, s[24:27], s60 offen lds\n"
        "s_add_u32 m0, s64, 8192\n"
        "s_nop 0\n"
        "buffer_load_dwordx4 v20, s[24:27], s60 offen lds\n"
        "s_cmp_lt_u32 s6, 2\n"
        "s_cselect_b32 s62, 3, 16\n"
        "s_cselect_b32 s63, 12, 32\n"
        "s_cselect_b64 s[24:25], s[50:51], s[52:53]\n"
        "s_cselect_b32 s64, 0, 117024\n"
        "s_mul_i32 s62, s62, 192\n"
        "s_mul_i32 s63, s63, 192\n"
        "s_and_b32 s25, s25, 0xffff\n"
        "s_mul_i32 s7, s6, 9216\n"
        "s_add_u32 s64, s64, s7\n"
        "s_add_u32 s64, s64, 6176\n"
        "s_add_u32 s7, s62, s63\n"
        "s_lshl_b32 s29, s63, 1\n"
        "s_add_u32 s60, s7, s63\n"
        "s_bitcmp1_b32 s6, 0\n"
        "s_cselect_b32 s28, s7, 0\n"
        "s_cselect_b32 s29, s29, s62\n"
        "s_cselect_b32 s60, s60, s63\n"
        "s_add_u32 s28, s28, s61\n"
        "s_add_u32 s29, s29, s61\n"
        "s_add_u32 s60, s60, s61\n"
        "s_add_u32 m0, s64, 0\n"
        "s_nop 0\n"
        "buffer_load_dwordx4 v32, s[24:27], s28 offen lds\n"
        "s_add_u32 m0, s64, 1024\n"
        "s_nop 0\n"
        "buffer_load_dwordx4 v33, s[24:27], s28 offen lds\n"
        "s_add_u32 m0, s64, 2048\n"
        "s_nop 0\n"
        "buffer_load_dwordx4 v34, s[24:27], s28 offen lds\n"
        "s_add_u32 m0, s64, 3072\n"
        "s_nop 0\n"
        "buffer_load_dwordx4 v32, s[24:27], s29 offen lds\n"
        "s_add_u32 m0, s64, 4096\n"
        "s_nop 0\n"
        "buffer_load_dwordx4 v33, s[24:27], s29 offen lds\n"
        "s_add_u32 m0, s64, 5120\n"
        "s_nop 0\n"
        "buffer_load_dwordx4 v34, s[24:27], s29 offen lds\n"
        "s_add_u32 m0, s64, 6144\n"
        "s_nop 0\n"
        "buffer_load_dwordx4 v32, s[24:27], s60 offen lds\n"
        "s_add_u32 m0, s64, 7168\n"
        "s_nop 0\n"
        "buffer_load_dwordx4 v33, s[24:27], s60 offen lds\n"
        "s_add_u32 m0, s64, 8192\n"
        "s_nop 0\n"
        "buffer_load_dwordx4 v34, s[24:27], s60 offen lds\n"
        "s_setprio 2\n"
        "s_waitcnt vmcnt(9)\n"
        "s_mov_b64 exec, 1\n"
        "ds_inc_u32 v250, v250 offset:20\n"
        "s_mov_b64 exec, -1\n"
        "L46_%=:\n"
        "ds_read_b32 v251, v250 offset:20\n"
        "s_waitcnt lgkmcnt(0)\n"
        "v_readfirstlane_b32 s40, v251\n"
        "s_nop 3\n"
        "s_cmp_ge_i32 s40, 4\n"
        "s_cbranch_scc1 L47_%=\n"
        "s_nop 0\n"
        "s_sub_u32 s41, s41, 1\n"
        "s_cbranch_scc0 L46_%=\n"
        "s_endpgm\n"
        "L47_%=:\n"
        "s_add_u32 m0, s17, 0\n"
        "s_nop 0\n"
        "buffer_load_dwordx4 v29, s[8:11], s35 offen lds\n"
        "buffer_load_dwordx4 v30, s[8:11], s35 offen offset:1024 lds\n"
        "buffer_load_dwordx4 v31, s[8:11], s35 offen offset:2048 lds\n"
        "s_add_u32 m0, s17, 3104\n"
        "s_nop 0\n"
        "buffer_load_dwordx4 v29, s[8:11], s36 offen lds\n"
        "buffer_load_dwordx4 v30, s[8:11], s36 offen offset:1024 lds\n"
        "buffer_load_dwordx4 v31, s[8:11], s36 offen offset:2048 lds\n"
        "s_cmp_eq_u32 s6, 2\n"
        "s_cbranch_scc1 L48_%=\n"
        "s_mov_b32 s34, 0x411cc471\n"
        "s_mov_b32 s35, 116256\n"
        "s_mov_b32 s36, 116248\n"
        "s_mov_b32 s37, 116252\n"
        "s_call_b64 s[30:31], FOLDW_%=\n"
        "s_branch L49_%=\n"
        "L48_%=:\n"
        "s_mov_b32 s34, 0x411cc471\n"
        "s_mov_b32 s35, 116256\n"
        "s_mov_b32 s36, 116280\n"
        "s_mov_b32 s37, 116252\n"
        "s_call_b64 s[30:31], FOLDU_%=\n"
        "global_load_dwordx4 v[40:43], v17, s[22:23] offset:-2048\n"
        "global_load_dwordx4 v[44:47], v17, s[22:23] offset:-1024\n"
        "global_load_dwordx4 v[0:3], v17, s[22:23] offset:0\n"
        "global_load_dwordx4 v[4:7], v17, s[22:23] offset:1024\n"
        "global_load_dwordx4 v[8:11], v17, s[22:23] offset:2048\n"
        "global_load_dwordx4 v[12:15], v17, s[22:23] offset:3072\n"
        "L49_%=:\n"
        "s_mov_b32 s44, 0xf0000f\n"
        "s_mov_b32 s45, 0xf0000f00\n"
        "s_mov_b32 s46, 0xff000ff\n"
        "s_mov_b32 s47, 0xf00fff00\n"
        "s_mov_b32 s48, 0x22222222\n"
        "s_mov_b32 s49, 0x22222222\n"
        "s_mov_b32 s50, 0x44444444\n"
        "s_mov_b32 s51, 0x44444444\n"
        "s_mov_b32 s52, 0x88888888\n"
        "s_mov_b32 s53, 0x88888888\n"
        "s_mov_b64 vcc, s[44:45]\n"
        "s_mov_b32 s28, 0x3c2aaaab\n"
        "s_mov_b32 s29, 0x3a7ba882\n"
        "s_mov_b32 s30, 0\n"
        "s_mov_b32 s34, 0\n"
        "s_mov_b32 s31, s18\n"
        "s_mov_b32 s36, s18\n"
        "s_mov_b32 s37, s18\n"
        "s_add_u32 s71, s15, 588672\n"
        "s_sub_u32 s72, 73, s16\n"
        "s_add_u32 s73, s17, 3104\n"
        "s_mov_b32 s68, 4\n"
        "s_mov_b32 s74, -1\n"
        "s_mov_b32 s41, 0x400000\n"
        "L12_%=:\n"
        "ds_read_b32 v251, v250 offset:28\n"
        "s_waitcnt lgkmcnt(0)\n"
        "v_readfirstlane_b32 s40, v251\n"
        "s_nop 3\n"
        "s_cmp_ge_i32 s40, 7\n"
        "s_cbranch_scc1 L13_%=\n"
        "s_nop 0\n"
        "s_sub_u32 s41, s41, 1\n"
        "s_cbranch_scc0 L12_%=\n"
        "s_endpgm\n"
        "L13_%=:\n"
        "s_waitcnt vmcnt(0)\n"
        "v_mov_b32 v255, 116224\n"
        "v_add_u32 v42, 27776, v0\n"
        "ds_read_b128 v[102:105], v42 offset:0\n"
        "ds_read_b128 v[106:109], v42 offset:1024\n"
        "ds_read_b128 v[110:113], v42 offset:2048\n"
        "ds_read_b128 v[114:117], v42 offset:3072\n"
        "ds_read_b128 v[118:121], v42 offset:4096\n"
        "ds_read_b128 v[122:125], v42 offset:5120\n"
        "ds_read_b128 v[126:129], v42 offset:6144\n"
        "ds_read_b128 v[130:133], v42 offset:7168\n"
        "ds_read_b128 v[134:137], v42 offset:8192\n"
        "ds_read_b128 v[138:141], v42 offset:9216\n"
        "ds_read_b128 v[142:145], v42 offset:10240\n"
        "ds_read_b128 v[146:149], v42 offset:11264\n"
        "ds_read_b128 v[150:153], v42 offset:12288\n"
        "ds_read_b128 v[154:157], v42 offset:13312\n"
        "ds_read_b128 v[158:161], v42 offset:14336\n"
        "ds_read_b128 v[162:165], v42 offset:15360\n"
        "ds_read_b128 v[166:169], v42 offset:16384\n"
        "ds_read_b128 v[170:173], v42 offset:17408\n"
        "ds_read_b128 v[174:177], v42 offset:18432\n"
        "ds_read_b128 v[178:181], v42 offset:19456\n"
        "ds_read_b128 v[182:185], v42 offset:20480\n"
        "ds_read_b128 v[186:189], v42 offset:21520\n"
        "ds_read_b128 v[190:193], v42 offset:22544\n"
        "ds_read_b128 v[194:197], v42 offset:23568\n"
        "ds_read_b128 v[198:201], v42 offset:24608\n"
        "ds_read_b128 v[202:205], v42 offset:25632\n"
        "ds_read_b128 v[206:209], v42 offset:26656\n"
        "ds_read_b128 v[210:213], v42 offset:27696\n"
        "ds_read_b128 v[214:217], v42 offset:28720\n"
        "ds_read_b128 v[218:221], v42 offset:29744\n"
        "s_waitcnt vmcnt(0)\n"
        "ds_read_b128 v[60:63], v1 offset:0\n"
        "ds_read_b128 v[66:69], v1 offset:64\n"
        "ds_read_b128 v[72:75], v1 offset:128\n"
        "ds_read_b128 v[20:23], v1 offset:768\n"
        "ds_read_b128 v[26:29], v1 offset:832\n"
        "ds_read_b128 v[32:35], v1 offset:896\n"
        "ds_read_b64 v[92:93], v4\n"
        "ds_read_b64 v[80:81], v4\n"
        "ds_read_b64 v[250:251], v4\n"
        "ds_read_b64 v[40:41], v4\n"
        "s_add_u32 s37, s37, 4\n"
        "s_cmp_ge_u32 s37, 49\n"
        "s_cselect_b32 s69, 49, 0\n"
        "s_sub_u32 s37, s37, s69\n"
        "s_sub_u32 s69, s37, 5\n"
        "s_cmp_lt_u32 s69, 4\n"
        "s_subb_u32 s36, s36, -4\n"
        "s_cmp_ge_u32 s36, 6\n"
        "s_cselect_b32 s69, 6, 0\n"
        "s_addc_u32 s34, s34, 0\n"
        "s_sub_u32 s36, s36, s69\n"
        "s_mul_i32 s38, s34, 1344\n"
        "s_cmp_ge_u32 s34, s72\n"
        "s_cselect_b32 s70, s71, s15\n"
        "s_add_u32 s38, s38, s70\n"
        "s_mul_i32 s69, s36, 98304\n"
        "s_add_u32 s38, s38, s69\n"
        "s_mov_b32 s35, s36\n"
        "s_mul_i32 s69, s36, -12\n"
        "s_add_u32 s69, s69, 60\n"
        "s_lshl_b64 s[54:55], -1, s69\n"
        "s_lshl_b64 s[56:57], s[54:55], 24\n"
        "s_ashr_i64 s[60:61], s[54:55], 8\n"
        "s_mov_b32 s58, s61\n"
        "s_mov_b32 s59, s56\n"
        "v_cndmask_b32_e64 v246, v7, v10, s[56:57]\n"
        "v_cndmask_b32_e64 v254, v8, v11, s[58:59]\n"
        "v_cndmask_b32_e64 v247, v9, v12, s[60:61]\n"
        "s_cmp_eq_u32 s37, 4\n"
        "s_cbranch_scc0 L14_%=\n"
        "s_nop 1\n"
        "ds_read_b32 v246, v0 offset:25728\n"
        "ds_read_b32 v254, v0 offset:25732\n"
        "ds_read_b32 v247, v0 offset:25736\n"
        "s_mov_b32 s35, 6\n"
        "s_mov_b32 s38, s15\n"
        "s_mov_b32 s69, 1344\n"
        "v_mov_b32 v45, 588672\n"
        "s_waitcnt lgkmcnt(0)\n"
        "v_lshrrev_b32 v44, 28, v246\n"
        "v_add_u32 v44, s34, v44\n"
        "v_cmp_le_u32 vcc, s72, v44\n"
        "v_and_b32 v246, 0x0fffffff, v246\n"
        "v_mad_u32_u24 v246, v44, s69, v246\n"
        "v_cndmask_b32 v44, 0, v45, vcc\n"
        "v_add_u32 v246, v246, v44\n"
        "v_lshrrev_b32 v44, 28, v254\n"
        "v_add_u32 v44, s34, v44\n"
        "v_cmp_le_u32 vcc, s72, v44\n"
        "v_and_b32 v254, 0x0fffffff, v254\n"
        "v_mad_u32_u24 v254, v44, s69, v254\n"
        "v_cndmask_b32 v44, 0, v45, vcc\n"
        "v_add_u32 v254, v254, v44\n"
        "v_lshrrev_b32 v44, 28, v247\n"
        "v_add_u32 v44, s34, v44\n"
        "v_cmp_le_u32 vcc, s72, v44\n"
        "v_and_b32 v247, 0x0fffffff, v247\n"
        "v_mad_u32_u24 v247, v44, s69, v247\n"
        "v_cndmask_b32 v44, 0, v45, vcc\n"
        "v_add_u32 v247, v247, v44\n"
        "s_mov_b64 vcc, s[44:45]\n"
        "L14_%=:\n"
        "s_cmp_lt_u32 s34, s14\n"
        "s_cselect_b32 s38, s38, 0x80000000\n"
        "s_add_u32 s39, s38, 100270464\n"
        "s_waitcnt lgkmcnt(0)\n"
        "s_mov_b64 exec, 1\n"
        "ds_inc_u32 v255, v13 offset:8\n"
        "s_mov_b64 exec, -1\n"
        "s_setprio 1\n"
        "PTOP_%=:\n"
        "s_waitcnt lgkmcnt(5)\n"
        "v_mfma_f32_16x16x32_bf16 v[50:53], v[60:63], v[60:63], 0\n"
        "s_setprio 1 \n"
        "v_cndmask_b32_e64 v95, v226, v227, s[48:49]\n"
        "s_nop 1\n"
        "v_cndmask_b32_e64 v244, v42, v43, s[48:49]\n"
        "s_waitcnt lgkmcnt(3)\n"
        "v_mfma_f32_16x16x32_bf16 v[50:53], v[66:69], v[66:69], v[50:53]\n"
        "ds_read_b32 v237, v255 offset:8\n"
        "v_cndmask_b32_e64 v95, v95, v228, s[50:51]\n"
        "s_waitcnt lgkmcnt(2)\n"
        "v_mfma_f32_16x16x32_bf16 v[50:53], v[72:75], v[72:75], v[50:53]\n"
        "s_mov_b32 m0, s17\n"
        "v_cndmask_b32_e64 v244, v244, v44, s[50:51]\n"
        "v_cndmask_b32_e64 v95, v95, v229, s[52:53]\n"
        "v_cndmask_b32_e64 v244, v244, v45, s[52:53]\n"
        "v_mfma_f32_16x16x32_bf16 v[226:229], v[20:23], v[20:23], 0\n"
        "v_mul_f32 v45, s28, v225\n"
        "v_cndmask_b32_dpp v95, v244, v95, vcc row_ror:4 row_mask:0xf bank_mask:0xf\n"
        "v_mfma_f32_16x16x32_bf16 v[226:229], v[26:29], v[26:29], v[226:229]\n"
        "v_fma_f32 v45, -v45, v225, v95\n"
        "v_add_f32_e64 v45, s29, |v45|\n"
        "s_waitcnt lgkmcnt(1)\n"
        "v_mfma_f32_16x16x32_bf16 v[226:229], v[32:35], v[32:35], v[226:229]\n"
        "buffer_load_dwordx4 v246, s[8:11], s38 offen lds\n"
        "v_rsq_f32 v95, v45\n"
        "v_mfma_f32_16x16x32_bf16 v[42:45], v[102:105], v[60:63], 0\n"
        "s_waitcnt lgkmcnt(0)\n"
        "v_readfirstlane_b32 s40, v237\n"
        "v_cndmask_b32_e64 v91, v50, v51, s[48:49]\n"
        "v_mfma_f32_16x16x32_bf16 v[42:45], v[106:109], v[66:69], v[42:45]\n"
        "s_cmp_lt_i32 s40, s68\n"
        "v_cndmask_b32_e64 v225, v226, v227, s[48:49]\n"
        "v_cndmask_b32_e64 v91, v91, v52, s[50:51]\n"
        "v_mfma_f32_16x16x32_bf16 v[42:45], v[110:113], v[72:75], v[42:45]\n"
        "buffer_load_dwordx4 v254, s[8:11], s38 offen offset:1024 lds\n"
        "s_cbranch_scc1 L3_%=\n"
        "L4_%=:\n"
        "v_cndmask_b32_e64 v225, v225, v228, s[50:51]\n"
        "v_cndmask_b32_e64 v91, v91, v53, s[52:53]\n"
        "v_cndmask_b32_e64 v225, v225, v229, s[52:53]\n"
        "v_mfma_f32_16x16x32_bf16 v[50:53], v[110:113], v[32:35], 0\n"
        "v_mfma_f32_16x16x32_bf16 v[50:53], v[106:109], v[26:29], v[50:53]\n"
        "v_cndmask_b32_dpp v91, v225, v91, vcc row_ror:4 row_mask:0xf bank_mask:0xf\n"
        "v_mfma_f32_16x16x32_bf16 v[50:53], v[102:105], v[20:23], v[50:53]\n"
        "buffer_load_dwordx4 v247, s[8:11], s38 offen offset:2048 lds\n"
        "ds_write_b128 v14, v[46:49]\n"
        "ds_write_b128 v14, v[54:57] offset:832\n"
        "s_cmp_eq_u32 s74, s30\n"
        "s_mov_b32 m0, s73\n"
        "v_mfma_f32_16x16x32_bf16 v[54:57], v[114:117], v[20:23], 0\n"
        "s_setprio 3\n"
        "s_cbranch_scc1 L5_%=\n"
        "s_setprio 1\n"
        "L5_%=:\n"
        "v_mfma_f32_16x16x32_bf16 v[54:57], v[118:121], v[26:29], v[54:57]\n"
        "v_mfma_f32_16x16x32_bf16 v[54:57], v[122:125], v[32:35], v[54:57]\n"
        "buffer_load_dwordx4 v246, s[8:11], s39 offen lds\n"
        "ds_write_b128 v14, v[96:99] offset:64\n"
        "v_mfma_f32_16x16x32_bf16 v[46:49], v[122:125], v[72:75], 0\n"
        "v_mfma_f32_16x16x32_bf16 v[46:49], v[118:121], v[66:69], v[46:49]\n"
        "v_mfma_f32_16x16x32_bf16 v[46:49], v[114:117], v[60:63], v[46:49]\n"
        "buffer_load_dwordx4 v254, s[8:11], s39 offen offset:1024 lds\n"
        "s_mov_b64 exec, s[46:47]\n"
        "ds_write_b32 v15, v95\n"
        "s_mov_b64 exec, -1\n"
        "v_mfma_f32_16x16x32_bf16 v[94:97], v[126:129], v[60:63], 0\n"
        "v_mfma_f32_16x16x32_bf16 v[94:97], v[130:133], v[66:69], v[94:97]\n"
        "v_mfma_f32_16x16x32_bf16 v[94:97], v[134:137], v[72:75], v[94:97]\n"
        "buffer_load_dwordx4 v247, s[8:11], s39 offen offset:2048 lds\n"
        "ds_write_b128 v14, v[230:233] offset:896\n"
        "ds_write_b128 v14, v[76:79] offset:128\n"
        "v_mfma_f32_16x16x32_bf16 v[82:85], v[134:137], v[32:35], 0\n"
        "v_mfma_f32_16x16x32_bf16 v[82:85], v[130:133], v[26:29], v[82:85]\n"
        "v_mfma_f32_16x16x32_bf16 v[82:85], v[126:129], v[20:23], v[82:85]\n"
        "ds_write_b128 v14, v[86:89] offset:960\n"
        "v_mfma_f32_16x16x32_bf16 v[86:89], v[138:141], v[20:23], 0\n"
        "v_mfma_f32_16x16x32_bf16 v[86:89], v[142:145], v[26:29], v[86:89]\n"
        "v_mfma_f32_16x16x32_bf16 v[86:89], v[146:149], v[32:35], v[86:89]\n"
        "s_mov_b64 exec, 1\n"
        "ds_inc_u32 v255, v13 offset:0\n"
        "s_mov_b64 exec, -1\n"
        "v_mfma_f32_16x16x32_bf16 v[98:101], v[146:149], v[72:75], 0\n"
        "v_mfma_f32_16x16x32_bf16 v[98:101], v[142:145], v[66:69], v[98:101]\n"
        "v_mfma_f32_16x16x32_bf16 v[98:101], v[138:141], v[60:63], v[98:101]\n"
        "v_mfma_f32_16x16x32_bf16 v[230:233], v[150:153], v[60:63], 0\n"
        "v_mfma_f32_16x16x32_bf16 v[230:233], v[154:157], v[66:69], v[230:233]\n"
        "v_mfma_f32_16x16x32_bf16 v[230:233], v[158:161], v[72:75], v[230:233]\n"
        "v_mfma_f32_16x16x32_bf16 v[238:241], v[158:161], v[32:35], 0\n"
        "v_mfma_f32_16x16x32_bf16 v[238:241], v[154:157], v[26:29], v[238:241]\n"
        "ds_read_b128 v[246:249], v2\n"
        "v_mfma_f32_16x16x32_bf16 v[238:241], v[150:153], v[20:23], v[238:241]\n"
        "ds_read_b128 v[76:79], v2 offset:64\n"
        "ds_read_b128 v[252:255], v2 offset:128\n"
        "v_mfma_f32_16x16x32_bf16 v[242:245], v[162:165], v[20:23], 0\n"
        "v_mfma_f32_16x16x32_bf16 v[242:245], v[166:169], v[26:29], v[242:245]\n"
        "v_mfma_f32_16x16x32_bf16 v[242:245], v[170:173], v[32:35], v[242:245]\n"
        "v_mfma_f32_16x16x32_bf16 v[234:237], v[170:173], v[72:75], 0\n"
        "v_mfma_f32_16x16x32_bf16 v[234:237], v[166:169], v[66:69], v[234:237]\n"
        "ds_read_b128 v[222:225], v4 offset:128\n"
        "v_mfma_f32_16x16x32_bf16 v[234:237], v[162:165], v[60:63], v[234:237]\n"
        "s_waitcnt lgkmcnt(1)\n"
        "v_mfma_f32_16x16x32_bf16 v[36:39], v[246:249], v[60:63], 0\n"
        "v_mfma_f32_16x16x32_bf16 v[36:39], v[76:79], v[66:69], v[36:39]\n"
        "ds_read_b128 v[226:229], v4 offset:128\n"
        "v_mfma_f32_16x16x32_bf16 v[36:39], v[252:255], v[72:75], v[36:39]\n"
        "s_and_b32 s69, s30, 1\n"
        "v_mfma_f32_16x16x32_bf16 v[16:19], v[252:255], v[32:35], 0\n"
        "s_lshl2_add_u32 s70, s69, 116224\n"
        "v_mfma_f32_16x16x32_bf16 v[16:19], v[76:79], v[26:29], v[16:19]\n"
        "v_mov_b32 v255, s70\n"
        "v_mfma_f32_16x16x32_bf16 v[16:19], v[246:249], v[20:23], v[16:19]\n"
        "s_waitcnt lgkmcnt(1)\n"
        "s_mul_i32 s69, s69, 40064\n"
        "v_smfmac_f32_16x16x64_bf16 v[222:225], v[60:63], v[42:49], v13\n"
        "s_mul_i32 s70, s31, 1664\n"
        "v_smfmac_f32_16x16x64_bf16 v[222:225], v[66:69], v[94:101], v13\n"
        "s_add_u32 s69, s69, s70\n"
        "v_smfmac_f32_16x16x64_bf16 v[222:225], v[72:75], v[230:237], v13\n"
        "s_waitcnt lgkmcnt(0)\n"
        "v_add_u32 v14, s69, v6\n"
        "v_cndmask_b32_dpp v237, v16, v36, vcc row_ror:4 row_mask:0xf bank_mask:0xf\n"
        "v_smfmac_f32_16x16x64_bf16 v[226:229], v[20:23], v[50:57], v13\n"
        "v_add_u32 v15, s69, v5\n"
        "v_smfmac_f32_16x16x64_bf16 v[226:229], v[26:29], v[82:89], v13\n"
        "s_lshr_b32 s68, s30, 1\n"
        "v_mul_f32 v89, s28, v237\n"
        "v_smfmac_f32_16x16x64_bf16 v[226:229], v[32:35], v[238:245], v13\n"
        "s_lshl2_add_u32 s68, s68, 4\n"
        "v_fma_f32 v89, -v89, v237, v91\n"
        "v_mfma_f32_16x16x32_bf16 v[82:85], v[20:23], v[174:177], 0\n"
        "s_cmp_eq_u32 s31, 6\n"
        "v_add_f32_e64 v89, s29, |v89|\n"
        "v_cndmask_b32_dpp v244, v17, v37, vcc row_ror:4 row_mask:0xf bank_mask:0xf\n"
        "v_mfma_f32_16x16x32_bf16 v[82:85], v[26:29], v[178:181], v[82:85]\n"
        "s_cbranch_scc1 L6_%=\n"
        "L7_%=:\n"
        "v_rsq_f32 v37, v89\n"
        "v_mfma_f32_16x16x32_bf16 v[82:85], v[32:35], v[182:185], v[82:85]\n"
        "s_mov_b32 s74, s30\n"
        "v_cndmask_b32_dpp v242, v226, v222, vcc row_ror:4 row_mask:0xf bank_mask:0xf\n"
        "v_cndmask_b32_dpp v243, v227, v223, vcc row_ror:4 row_mask:0xf bank_mask:0xf\n"
        "v_cndmask_b32_dpp v245, v228, v224, vcc row_ror:4 row_mask:0xf bank_mask:0xf\n"
        "v_mfma_f32_16x16x32_bf16 v[86:89], v[72:75], v[182:185], 0\n"
        "s_mov_b32 s30, s34\n"
        "v_mul_f32_dpp v242, v37, v242 quad_perm:[0,0,0,0] row_mask:0xf bank_mask:0xf\n"
        "v_mul_f32_dpp v243, v37, v243 quad_perm:[1,1,1,1] row_mask:0xf bank_mask:0xf\n"
        "v_mfma_f32_16x16x32_bf16 v[86:89], v[66:69], v[178:181], v[86:89]\n"
        "s_mov_b32 s31, s35\n"
        "v_mul_f32_dpp v245, v37, v245 quad_perm:[2,2,2,2] row_mask:0xf bank_mask:0xf\n"
        "v_cndmask_b32_dpp v17, v229, v225, vcc row_ror:4 row_mask:0xf bank_mask:0xf\n"
        "v_mfma_f32_16x16x32_bf16 v[86:89], v[60:63], v[174:177], v[86:89]\n"
        "s_add_u32 s37, s37, 4\n"
        "v_mul_f32_dpp v17, v37, v17 quad_perm:[3,3,3,3] row_mask:0xf bank_mask:0xf\n"
        "v_max3_f32 v229, v242, v243, v245\n"
        "v_max_f32 v229, v229, v17\n"
        "v_mfma_f32_16x16x32_bf16 v[222:225], v[60:63], v[210:213], 0\n"
        "s_cmp_ge_u32 s37, 49\n"
        "v_mul_f32 v229, v229, v37\n"
        "v_fma_f32 v242, v242, v37, -v229\n"
        "v_mfma_f32_16x16x32_bf16 v[222:225], v[66:69], v[214:217], v[222:225]\n"
        "s_cselect_b32 s69, 49, 0\n"
        "v_fma_f32 v243, v243, v37, -v229\n"
        "v_fma_f32 v245, v245, v37, -v229\n"
        "v_mfma_f32_16x16x32_bf16 v[222:225], v[72:75], v[218:221], v[222:225]\n"
        "s_sub_u32 s37, s37, s69\n"
        "v_fma_f32 v17, v17, v37, -v229\n"
        "v_cvt_pk_bf16_f32 v90, v86, v87\n"
        "v_mfma_f32_16x16x32_bf16 v[226:229], v[32:35], v[218:221], 0\n"
        "s_sub_u32 s69, s37, 5\n"
        "v_exp_f32 v242, v242\n"
        "v_mfma_f32_16x16x32_bf16 v[226:229], v[26:29], v[214:217], v[226:229]\n"
        "ds_read_b128 v[94:97], v0\n"
        "v_cvt_pk_bf16_f32 v91, v88, v89\n"
        "v_mfma_f32_16x16x32_bf16 v[226:229], v[20:23], v[210:213], v[226:229]\n"
        "ds_read_b128 v[50:53], v0 offset:1024\n"
        "ds_read_b128 v[86:89], v0 offset:2048\n"
        "v_exp_f32 v243, v243\n"
        "v_mfma_f32_16x16x32_bf16 v[246:249], v[20:23], v[186:189], 0\n"
        "s_cmp_lt_u32 s69, 4\n"
        "v_exp_f32 v245, v245\n"
        "v_mfma_f32_16x16x32_bf16 v[246:249], v[26:29], v[190:193], v[246:249]\n"
        "s_subb_u32 s36, s36, -4\n"
        "v_exp_f32 v17, v17\n"
        "v_mfma_f32_16x16x32_bf16 v[246:249], v[32:35], v[194:197], v[246:249]\n"
        "s_cmp_ge_u32 s36, 6\n"
        "v_add_f32 v57, v242, v243\n"
        "v_add_f32 v45, v245, v17\n"
        "v_add_f32 v57, v57, v45\n"
        "v_mfma_f32_16x16x32_bf16 v[42:45], v[72:75], v[194:197], 0\n"
        "s_cselect_b32 s69, 6, 0\n"
        "v_rcp_f32 v57, v57\n"
        "v_mfma_f32_16x16x32_bf16 v[42:45], v[66:69], v[190:193], v[42:45]\n"
        "s_addc_u32 s34, s34, 0\n"
        "v_mul_f32_dpp v242, v37, v242 quad_perm:[0,0,0,0] row_mask:0xf bank_mask:0xf\n"
        "v_mul_f32_dpp v243, v37, v243 quad_perm:[1,1,1,1] row_mask:0xf bank_mask:0xf\n"
        "v_mfma_f32_16x16x32_bf16 v[42:45], v[60:63], v[186:189], v[42:45]\n"
        "s_waitcnt lgkmcnt(0)\n"
        "s_sub_u32 s36, s36, s69\n"
        "v_mul_f32_dpp v245, v37, v245 quad_perm:[2,2,2,2] row_mask:0xf bank_mask:0xf\n"
        "v_mul_f32_dpp v17, v37, v17 quad_perm:[3,3,3,3] row_mask:0xf bank_mask:0xf\n"
        "v_mfma_f32_16x16x32_bf16 v[36:39], v[60:63], v[94:97], 0\n"
        "s_mul_i32 s38, s34, 1344\n"
        "v_mul_f32 v242, v242, v57\n"
        "v_mul_f32 v243, v243, v57\n"
        "v_mfma_f32_16x16x32_bf16 v[36:39], v[66:69], v[50:53], v[36:39]\n"
        "s_cmp_ge_u32 s34, s72\n"
        "v_mul_f32 v245, v245, v57\n"
        "v_mul_f32 v17, v17, v57\n"
        "v_mfma_f32_16x16x32_bf16 v[36:39], v[72:75], v[86:89], v[36:39]\n"
        "s_cselect_b32 s70, s71, s15\n"
        "v_cvt_pk_bf16_f32 v19, v242, v243\n"
        "v_cvt_pk_bf16_f32 v71, v245, v17\n"
        "v_mfma_f32_16x16x32_bf16 v[54:57], v[32:35], v[86:89], 0\n"
        "s_add_u32 s38, s38, s70\n"
        "v_and_b32 v58, v3, v19\n"
        "v_and_b32 v59, v3, v71\n"
        "v_mfma_f32_16x16x32_bf16 v[54:57], v[26:29], v[50:53], v[54:57]\n"
        "s_mul_i32 s69, s36, 98304\n"
        "v_and_b32_dpp v18, v19, v3 row_ror:12 row_mask:0xf bank_mask:0xf\n"
        "v_and_b32_dpp v19, v71, v3 row_ror:12 row_mask:0xf bank_mask:0xf\n"
        "v_mfma_f32_16x16x32_bf16 v[54:57], v[20:23], v[94:97], v[54:57]\n"
        "s_add_u32 s38, s38, s69\n"
        "v_cvt_pk_bf16_f32 v78, v82, v83\n"
        "v_cvt_pk_bf16_f32 v79, v84, v85\n"
        "v_mov_b64 v[70:71], v[58:59]\n"
        "v_cvt_pk_bf16_f32 v94, v222, v223\n"
        "v_mfma_f32_16x16x32_bf16 v[84:87], v[90:93], v[58:61], 0\n"
        "s_mov_b32 s35, s36\n"
        "v_cvt_pk_bf16_f32 v95, v224, v225\n"
        "v_mov_b64 v[30:31], v[18:19]\n"
        "v_mfma_f32_16x16x32_bf16 v[50:53], v[78:81], v[18:21], 0\n"
        "s_mul_i32 s69, s36, -12\n"
        "v_cvt_pk_bf16_f32 v82, v226, v227\n"
        "v_cvt_pk_bf16_f32 v83, v228, v229\n"
        "v_mov_b64 v[64:65], v[58:59]\n"
        "v_mfma_f32_16x16x32_bf16 v[230:233], v[92:95], v[68:71], 0\n"
        "ds_read_b128 v[222:225], v0 offset:3088\n"
        "ds_read_b128 v[98:101], v0 offset:4112\n"
        "ds_read_b128 v[238:241], v0 offset:5136\n"
        "v_mfma_f32_16x16x32_bf16 v[226:229], v[80:83], v[28:31], 0\n"
        "s_add_u32 s69, s69, 60\n"
        "s_lshl_b64 s[54:55], -1, s69\n"
        "v_cvt_pk_bf16_f32 v252, v42, v43\n"
        "v_cvt_pk_bf16_f32 v253, v44, v45\n"
        "v_mfma_f32_16x16x32_bf16 v[44:47], v[20:23], v[198:201], 0\n"
        "s_lshl_b64 s[56:57], s[54:55], 24\n"
        "v_mov_b64 v[24:25], v[18:19]\n"
        "v_cvt_pk_bf16_f32 v42, v246, v247\n"
        "v_mfma_f32_16x16x32_bf16 v[44:47], v[26:29], v[202:205], v[44:47]\n"
        "s_ashr_i64 s[60:61], s[54:55], 8\n"
        "v_cvt_pk_bf16_f32 v43, v248, v249\n"
        "v_cvt_pk_bf16_f32 v248, v36, v37\n"
        "v_mfma_f32_16x16x32_bf16 v[44:47], v[32:35], v[206:209], v[44:47]\n"
        "s_mov_b32 s58, s61\n"
        "v_cvt_pk_bf16_f32 v249, v38, v39\n"
        "v_cvt_pk_bf16_f32 v38, v54, v55\n"
        "v_mfma_f32_16x16x32_bf16 v[94:97], v[72:75], v[206:209], 0\n"
        "s_mov_b32 s59, s56\n"
        "v_cndmask_b32_e64 v246, v7, v10, s[56:57]\n"
        "v_cndmask_b32_e64 v254, v8, v11, s[58:59]\n"
        "v_mfma_f32_16x16x32_bf16 v[94:97], v[66:69], v[202:205], v[94:97]\n"
        "v_cndmask_b32_e64 v247, v9, v12, s[60:61]\n"
        "v_cvt_pk_bf16_f32 v39, v56, v57\n"
        "v_mfma_f32_16x16x32_bf16 v[94:97], v[60:63], v[198:201], v[94:97]\n"
        "s_waitcnt lgkmcnt(0)\n"
        "v_cvt_pk_bf16_f32 v78, v44, v45\n"
        "v_cvt_pk_bf16_f32 v79, v46, v47\n"
        "v_mov_b64 v[76:77], v[58:59]\n"
        "v_mfma_f32_16x16x32_bf16 v[56:59], v[60:63], v[222:225], 0\n"
        "s_cmp_eq_u32 s37, 4\n"
        "v_mov_b64 v[36:37], v[18:19]\n"
        "v_cvt_pk_bf16_f32 v46, v84, v85\n"
        "v_mfma_f32_16x16x32_bf16 v[56:59], v[66:69], v[98:101], v[56:59]\n"
        "s_cbranch_scc1 L8_%=\n"
        "L9_%=:\n"
        "v_cvt_pk_bf16_f32 v90, v94, v95\n"
        "v_cvt_pk_bf16_f32 v91, v96, v97\n"
        "v_mfma_f32_16x16x32_bf16 v[56:59], v[72:75], v[238:241], v[56:59]\n"
        "s_cmp_lt_u32 s34, s14\n"
        "v_cvt_pk_bf16_f32 v47, v86, v87\n"
        "v_cvt_pk_bf16_f32 v48, v230, v231\n"
        "v_cvt_pk_bf16_f32 v49, v232, v233\n"
        "v_mfma_f32_16x16x32_bf16 v[230:233], v[32:35], v[238:241], 0\n"
        "s_cselect_b32 s38, s38, 0x80000000\n"
        "v_cvt_pk_bf16_f32 v54, v50, v51\n"
        "v_cvt_pk_bf16_f32 v55, v52, v53\n"
        "v_mfma_f32_16x16x32_bf16 v[230:233], v[26:29], v[98:101], v[230:233]\n"
        "s_add_u32 s39, s38, 100270464\n"
        "v_cvt_pk_bf16_f32 v94, v56, v57\n"
        "v_cvt_pk_bf16_f32 v95, v58, v59\n"
        "v_mfma_f32_16x16x32_bf16 v[230:233], v[20:23], v[222:225], v[230:233]\n"
        "v_cvt_pk_bf16_f32 v56, v226, v227\n"
        "v_cvt_pk_bf16_f32 v57, v228, v229\n"
        "v_mul_f32_dpp v225, v244, v242 quad_perm:[0,0,0,0] row_mask:0xf bank_mask:0xf\n"
        "v_mfma_f32_16x16x32_bf16 v[98:101], v[250:253], v[62:65], 0\n"
        "v_mfma_f32_16x16x32_bf16 v[226:229], v[40:43], v[22:25], 0\n"
        "v_fmac_f32_dpp v225, v244, v243 quad_perm:[1,1,1,1] row_mask:0xf bank_mask:0xf\n"
        "v_mfma_f32_16x16x32_bf16 v[42:45], v[248:251], v[70:73], 0\n"
        "v_mfma_f32_16x16x32_bf16 v[86:89], v[38:41], v[30:33], 0\n"
        "v_cvt_pk_bf16_f32 v82, v230, v231\n"
        "v_cvt_pk_bf16_f32 v83, v232, v233\n"
        "v_fmac_f32_dpp v225, v244, v245 quad_perm:[2,2,2,2] row_mask:0xf bank_mask:0xf\n"
        "v_mfma_f32_16x16x32_bf16 v[230:233], v[90:93], v[64:67], 0\n"
        "s_waitcnt vmcnt(0)\n"
        "ds_read_b128 v[60:63], v1\n"
        "ds_read_b128 v[20:23], v1 offset:768\n"
        "v_cvt_pk_bf16_f32 v96, v98, v99\n"
        "v_mfma_f32_16x16x32_bf16 v[50:53], v[78:81], v[24:27], 0\n"
        "ds_read_b128 v[66:69], v1 offset:64\n"
        "ds_read_b128 v[26:29], v1 offset:832\n"
        "v_cvt_pk_bf16_f32 v97, v100, v101\n"
        "v_cvt_pk_bf16_f32 v98, v42, v43\n"
        "v_cvt_pk_bf16_f32 v99, v44, v45\n"
        "v_mfma_f32_16x16x32_bf16 v[238:241], v[92:95], v[74:77], 0\n"
        "ds_read_b128 v[72:75], v1 offset:128\n"
        "v_cvt_pk_bf16_f32 v76, v230, v231\n"
        "v_cvt_pk_bf16_f32 v77, v232, v233\n"
        "v_cvt_pk_bf16_f32 v230, v226, v227\n"
        "v_cvt_pk_bf16_f32 v231, v228, v229\n"
        "v_mfma_f32_16x16x32_bf16 v[42:45], v[80:83], v[34:37], 0\n"
        "ds_read_b128 v[32:35], v1 offset:896\n"
        "v_cvt_pk_bf16_f32 v232, v86, v87\n"
        "v_cvt_pk_bf16_f32 v78, v238, v239\n"
        "v_cvt_pk_bf16_f32 v79, v240, v241\n"
        "v_mfma_f32_16x16x32_bf16 v[226:229], v[46:49], v[46:49], 0\n"
        "v_cvt_pk_bf16_f32 v233, v88, v89\n"
        "v_cvt_pk_bf16_f32 v86, v50, v51\n"
        "v_mfma_f32_16x16x32_bf16 v[226:229], v[96:99], v[96:99], v[226:229]\n"
        "v_cvt_pk_bf16_f32 v87, v52, v53\n"
        "v_cvt_pk_bf16_f32 v88, v42, v43\n"
        "v_mfma_f32_16x16x32_bf16 v[226:229], v[76:79], v[76:79], v[226:229]\n"
        "v_cvt_pk_bf16_f32 v89, v44, v45\n"
        "v_fmac_f32_dpp v225, v244, v17 quad_perm:[3,3,3,3] row_mask:0xf bank_mask:0xf\n"
        "v_add_f32 v225, v225, v237\n"
        "v_mfma_f32_16x16x32_bf16 v[42:45], v[54:57], v[54:57], 0\n"
        "v_mfma_f32_16x16x32_bf16 v[42:45], v[230:233], v[230:233], v[42:45]\n"
        "v_mfma_f32_16x16x32_bf16 v[42:45], v[86:89], v[86:89], v[42:45]\n"
        "s_cmp_lt_u32 s30, s14\n"
        "s_cbranch_scc1 PTOP_%=\n"
        "s_waitcnt lgkmcnt(0)\n"
        "s_nop 15\n"
        "L10_%=:\n"
        "ds_read_b32 v254, v255 offset:8\n"
        "s_waitcnt lgkmcnt(0)\n"
        "v_readfirstlane_b32 s40, v254\n"
        "s_nop 3\n"
        "s_cmp_ge_i32 s40, s68\n"
        "s_cbranch_scc1 L11_%=\n"
        "s_sleep 1\n"
        "s_sub_u32 s41, s41, 1\n"
        "s_cbranch_scc0 L10_%=\n"
        "s_endpgm\n"
        "L11_%=:\n"
        "v_cndmask_b32_e64 v254, v226, v227, s[48:49]\n"
        "v_cndmask_b32_e64 v227, v42, v43, s[48:49]\n"
        "v_cndmask_b32_e64 v254, v254, v228, s[50:51]\n"
        "v_cndmask_b32_e64 v227, v227, v44, s[50:51]\n"
        "v_cndmask_b32_e64 v254, v254, v229, s[52:53]\n"
        "v_cndmask_b32_e64 v227, v227, v45, s[52:53]\n"
        "v_mul_f32 v45, s28, v225\n"
        "ds_write_b128 v14, v[46:49]\n"
        "v_cndmask_b32_dpp v254, v227, v254, vcc row_ror:4 row_mask:0xf bank_mask:0xf\n"
        "v_fma_f32 v45, -v45, v225, v254\n"
        "v_add_f32_e64 v45, s29, |v45|\n"
        "v_rsq_f32 v225, v45\n"
        "ds_write_b128 v14, v[54:57] offset:832\n"
        "ds_write_b128 v14, v[96:99] offset:64\n"
        "ds_write_b128 v14, v[230:233] offset:896\n"
        "ds_write_b128 v14, v[76:79] offset:128\n"
        "ds_write_b128 v14, v[86:89] offset:960\n"
        "s_mov_b64 exec, s[46:47]\n"
        "ds_write_b32 v15, v225\n"
        "s_mov_b64 exec, -1\n"
        "s_mov_b64 exec, 1\n"
        "ds_inc_u32 v255, v13 offset:0\n"
        "s_mov_b64 exec, -1\n"
        "s_endpgm\n"
        "L3_%=:\n"
        "s_nop 7\n"
        "s_nop 7\n"
        "L1_%=:\n"
        "ds_read_b32 v237, v255 offset:8\n"
        "s_waitcnt lgkmcnt(0)\n"
        "v_readfirstlane_b32 s40, v237\n"
        "s_nop 3\n"
        "s_cmp_ge_i32 s40, s68\n"
        "s_cbranch_scc1 L2_%=\n"
        "s_sleep 1\n"
        "s_sub_u32 s41, s41, 1\n"
        "s_cbranch_scc0 L1_%=\n"
        "s_endpgm\n"
        "L2_%=:\n"
        "s_nop 1\n"
        "s_branch L4_%=\n"
        "L6_%=:\n"
        "s_nop 7\n"
        "s_nop 7\n"
        "ds_read_u16 v14, v0 offset:25740\n"
        "ds_read_u16 v15, v0 offset:25742\n"
        "s_cmp_ge_u32 s30, 10\n"
        "s_cselect_b32 s69, 10, 0\n"
        "s_cmp_ge_u32 s30, 20\n"
        "s_cselect_b32 s69, 20, s69\n"
        "s_sub_u32 s69, s30, s69\n"
        "s_mul_i32 s69, s69, 832\n"
        "s_waitcnt lgkmcnt(0)\n"
        "v_add_u32 v14, s69, v14\n"
        "v_subrev_u32 v47, 8320, v14\n"
        "v_min_u32 v14, v14, v47\n"
        "v_add_u32 v14, 107904, v14\n"
        "v_add_u32 v15, s69, v15\n"
        "v_subrev_u32 v47, 8320, v15\n"
        "v_min_u32 v15, v15, v47\n"
        "v_add_u32 v15, 107904, v15\n"
        "s_nop 1\n"
        "s_branch L7_%=\n"
        "L8_%=:\n"
        "s_nop 7\n"
        "s_nop 7\n"
        "ds_read_b32 v246, v0 offset:25728\n"
        "ds_read_b32 v254, v0 offset:25732\n"
        "ds_read_b32 v247, v0 offset:25736\n"
        "s_mov_b32 s35, 6\n"
        "s_mov_b32 s38, s15\n"
        "s_mov_b32 s69, 1344\n"
        "v_mov_b32 v90, 588672\n"
        "s_waitcnt lgkmcnt(0)\n"
        "v_lshrrev_b32 v47, 28, v246\n"
        "v_add_u32 v47, s34, v47\n"
        "v_cmp_le_u32 vcc, s72, v47\n"
        "v_and_b32 v246, 0x0fffffff, v246\n"
        "v_mad_u32_u24 v246, v47, s69, v246\n"
        "v_cndmask_b32 v47, 0, v90, vcc\n"
        "v_add_u32 v246, v246, v47\n"
        "v_lshrrev_b32 v47, 28, v254\n"
        "v_add_u32 v47, s34, v47\n"
        "v_cmp_le_u32 vcc, s72, v47\n"
        "v_and_b32 v254, 0x0fffffff, v254\n"
        "v_mad_u32_u24 v254, v47, s69, v254\n"
        "v_cndmask_b32 v47, 0, v90, vcc\n"
        "v_add_u32 v254, v254, v47\n"
        "v_lshrrev_b32 v47, 28, v247\n"
        "v_add_u32 v47, s34, v47\n"
        "v_cmp_le_u32 vcc, s72, v47\n"
        "v_and_b32 v247, 0x0fffffff, v247\n"
        "v_mad_u32_u24 v247, v47, s69, v247\n"
        "v_cndmask_b32 v47, 0, v90, vcc\n"
        "v_add_u32 v247, v247, v47\n"
        "s_mov_b64 vcc, s[44:45]\n"
        "s_nop 1\n"
        "s_branch L9_%=\n"
        "CONS_%=:\n"
        "s_mov_b32 s41, 0x400000\n"
        "s_mov_b32 s48, 0xffffffff\n"
        "s_mov_b32 s49, 0xffff0000\n"
        "s_mov_b32 s50, 0xffff0000\n"
        "s_mov_b32 s51, 0x0\n"
        "s_mov_b32 s52, 0x0\n"
        "s_mov_b32 s53, 0xffff\n"
        "s_mov_b32 s54, 0x0\n"
        "s_mov_b32 s55, 0xffff0000\n"
        "s_mov_b32 s56, 0x0\n"
        "s_mov_b32 s57, 0xfeff0000\n"
        "s_mov_b32 s62, 0x11111111\n"
        "s_mov_b32 s63, 0x11111111\n"
        "s_mov_b64 s[44:45], s[12:13]\n"
        "s_and_b32 s45, s45, 0xffff\n"
        "s_mov_b32 s47, 0x20000\n"
        "s_mov_b32 s75, 832\n"
        "s_mov_b32 s74, 116224\n"
        "s_sub_u32 s73, s18, 7488\n"
        "s_mov_b32 s28, 0xffff0000\n"
        "s_mov_b32 s31, 0\n"
        "s_add_u32 s72, s15, s17\n"
        "s_add_u32 s70, s72, 588672\n"
        "s_sub_u32 s71, 73, s16\n"
        "s_mov_b32 s35, s72\n"
        "s_mov_b32 s36, 0x20000000\n"
        "s_mov_b32 s69, 40064\n"
        "v_mov_b32 v29, 116224\n"
        "L23_%=:\n"
        "ds_read_b32 v28, v29 offset:52\n"
        "s_waitcnt lgkmcnt(0)\n"
        "v_readfirstlane_b32 s40, v28\n"
        "s_nop 3\n"
        "s_cmp_ge_i32 s40, 6\n"
        "s_cbranch_scc1 L24_%=\n"
        "s_nop 0\n"
        "s_sub_u32 s41, s41, 1\n"
        "s_cbranch_scc0 L23_%=\n"
        "s_endpgm\n"
        "L24_%=:\n"
        "s_setprio 0\n"
        "v_add_u32 v28, 2, v1\n"
        "v_mov_b32 v29, 0x3f80\n"
        "s_mov_b32 exec_lo, 0\n"
        "s_mov_b32 exec_hi, 0x1000000\n"
        "ds_write_b16 v28, v29\n"
        "s_mov_b64 exec, -1\n"
        "v_add_u32 v28, 67840, v0\n"
        "ds_read_b128 v[92:95], v28 offset:0\n"
        "ds_read_b128 v[96:99], v28 offset:1024\n"
        "ds_read_b128 v[100:103], v28 offset:2048\n"
        "ds_read_b128 v[104:107], v28 offset:3072\n"
        "ds_read_b128 v[108:111], v28 offset:4096\n"
        "ds_read_b128 v[112:115], v28 offset:5120\n"
        "ds_read_b128 v[116:119], v28 offset:6144\n"
        "ds_read_b128 v[120:123], v28 offset:7168\n"
        "ds_read_b128 v[124:127], v28 offset:8192\n"
        "ds_read_b128 v[128:131], v28 offset:9216\n"
        "ds_read_b128 v[132:135], v28 offset:10240\n"
        "ds_read_b128 v[136:139], v28 offset:11264\n"
        "ds_read_b128 v[140:143], v28 offset:12288\n"
        "ds_read_b128 v[144:147], v28 offset:13312\n"
        "ds_read_b128 v[148:151], v28 offset:14336\n"
        "ds_read_b128 v[152:155], v28 offset:15360\n"
        "ds_read_b128 v[156:159], v28 offset:16384\n"
        "ds_read_b128 v[160:163], v28 offset:17408\n"
        "ds_read_b128 v[164:167], v0 offset:6176\n"
        "ds_read_b128 v[168:171], v0 offset:7200\n"
        "ds_read_b128 v[172:175], v0 offset:8224\n"
        "ds_read_b128 v[176:179], v0 offset:9264\n"
        "v_mov_b32 v29, 116224\n"
        "s_waitcnt lgkmcnt(0)\n"
        "s_mov_b64 exec, 1\n"
        "ds_inc_u32 v29, v1 offset:12\n"
        "s_mov_b64 exec, -1\n"
        "L25_%=:\n"
        "ds_read_b32 v28, v29 offset:0\n"
        "s_waitcnt lgkmcnt(0)\n"
        "v_readfirstlane_b32 s40, v28\n"
        "s_nop 3\n"
        "s_cmp_ge_i32 s40, 11\n"
        "s_cbranch_scc1 L26_%=\n"
        "s_sleep 1\n"
        "s_sub_u32 s41, s41, 1\n"
        "s_cbranch_scc0 L25_%=\n"
        "s_endpgm\n"
        "L26_%=:\n"
        "ds_read_b128 v[220:223], v3 offset:0\n"
        "ds_read_b128 v[184:187], v3 offset:3328\n"
        "ds_read_b128 v[52:55], v3 offset:64\n"
        "ds_read_b128 v[76:79], v3 offset:3392\n"
        "ds_read_b128 v[48:51], v3 offset:128\n"
        "ds_read_b128 v[240:243], v3 offset:3456\n"
        "s_waitcnt lgkmcnt(0)\n"
        "CFULL_%=:\n"
        "s_call_b64 s[64:65], CGRP_%=\n"
        "CTOP_%=:\n"
        "v_mfma_f32_16x16x32_bf16 v[204:207], v[92:95], v[220:223], 0\n"
        "v_cvt_pk_bf16_f32 v226, v80, v216\n"
        "v_cvt_pk_bf16_f32 v227, v84, v20\n"
        "v_mfma_f32_16x16x32_bf16 v[204:207], v[96:99], v[52:55], v[204:207]\n"
        "v_cvt_pk_bf16_f32 v228, v32, v36\n"
        "v_cvt_pk_bf16_f32 v230, v81, v217\n"
        "s_waitcnt lgkmcnt(2)\n"
        "v_mfma_f32_16x16x32_bf16 v[204:207], v[100:103], v[48:51], v[204:207]\n"
        "v_cvt_pk_bf16_f32 v231, v85, v21\n"
        "v_cvt_pk_bf16_f32 v232, v33, v37\n"
        "v_cvt_pk_bf16_f32 v84, v82, v218\n"
        "s_waitcnt lgkmcnt(1)\n"
        "v_mfma_f32_16x16x32_bf16 v[208:211], v[100:103], v[240:243], 0\n"
        "v_cvt_pk_bf16_f32 v85, v86, v22\n"
        "v_cvt_pk_bf16_f32 v86, v34, v38\n"
        "v_mfma_f32_16x16x32_bf16 v[208:211], v[96:99], v[76:79], v[208:211]\n"
        "v_cvt_pk_bf16_f32 v32, v83, v219\n"
        "v_cvt_pk_bf16_f32 v224, v204, v205\n"
        "v_mfma_f32_16x16x32_bf16 v[208:211], v[92:95], v[184:187], v[208:211]\n"
        "s_add_u32 s37, s36, 196224\n"
        "v_cvt_pk_bf16_f32 v225, v206, v207\n"
        "v_cvt_pk_bf16_f32 v33, v87, v23\n"
        "v_cvt_pk_bf16_f32 v34, v35, v39\n"
        "v_mfma_f32_16x16x32_bf16 v[204:207], v[104:107], v[184:187], 0\n"
        "v_cvt_pk_bf16_f32 v20, v40, v60\n"
        "v_cvt_pk_bf16_f32 v21, v72, v16\n"
        "v_mfma_f32_16x16x32_bf16 v[204:207], v[108:111], v[76:79], v[204:207]\n"
        "v_cvt_pk_bf16_f32 v38, v208, v209\n"
        "v_cvt_pk_bf16_f32 v39, v210, v211\n"
        "v_mfma_f32_16x16x32_bf16 v[204:207], v[112:115], v[240:243], v[204:207]\n"
        "buffer_store_dwordx3 v[226:228], v8, s[8:11], s37 offen offset:384 sc0 sc1 nt\n"
        "v_mfma_f32_16x16x32_bf16 v[208:211], v[112:115], v[48:51], 0\n"
        "v_cvt_pk_bf16_f32 v22, v24, v244\n"
        "v_cvt_pk_bf16_f32 v58, v41, v61\n"
        "v_mfma_f32_16x16x32_bf16 v[208:211], v[108:111], v[52:55], v[208:211]\n"
        "s_add_u32 s37, s36, 196224\n"
        "v_cvt_pk_bf16_f32 v59, v73, v17\n"
        "v_cvt_pk_bf16_f32 v60, v25, v245\n"
        "v_mfma_f32_16x16x32_bf16 v[208:211], v[104:107], v[220:223], v[208:211]\n"
        "v_cvt_pk_bf16_f32 v40, v204, v205\n"
        "v_cvt_pk_bf16_f32 v41, v206, v207\n"
        "v_cvt_pk_bf16_f32 v72, v42, v62\n"
        "v_mfma_f32_16x16x32_bf16 v[204:207], v[116:119], v[220:223], 0\n"
        "v_cvt_pk_bf16_f32 v73, v74, v18\n"
        "v_cvt_pk_bf16_f32 v74, v26, v246\n"
        "v_mfma_f32_16x16x32_bf16 v[204:207], v[120:123], v[52:55], v[204:207]\n"
        "v_cvt_pk_bf16_f32 v16, v43, v63\n"
        "v_cvt_pk_bf16_f32 v226, v208, v209\n"
        "v_mfma_f32_16x16x32_bf16 v[204:207], v[124:127], v[48:51], v[204:207]\n"
        "buffer_store_dwordx3 v[230:232], v9, s[8:11], s37 offen offset:1152 sc0 sc1 nt\n"
        "v_cvt_pk_bf16_f32 v227, v210, v211\n"
        "v_cvt_pk_bf16_f32 v17, v75, v19\n"
        "v_cvt_pk_bf16_f32 v18, v27, v247\n"
        "v_mfma_f32_16x16x32_bf16 v[208:211], v[124:127], v[240:243], 0\n"
        "s_add_u32 s37, s36, 294336\n"
        "v_mfma_f32_16x16x32_bf16 v[208:211], v[120:123], v[76:79], v[208:211]\n"
        "v_mfma_f32_16x16x32_bf16 v[208:211], v[116:119], v[184:187], v[208:211]\n"
        "v_cvt_pk_bf16_f32 v236, v204, v205\n"
        "v_cvt_pk_bf16_f32 v237, v206, v207\n"
        "v_mfma_f32_16x16x32_bf16 v[204:207], v[128:131], v[184:187], 0\n"
        "v_mfma_f32_16x16x32_bf16 v[204:207], v[132:135], v[76:79], v[204:207]\n"
        "v_mfma_f32_16x16x32_bf16 v[204:207], v[136:139], v[240:243], v[204:207]\n"
        "s_setprio 2\n"
        "buffer_store_dwordx3 v[84:86], v8, s[8:11], s37 offen offset:576 sc0 sc1 nt\n"
        "s_add_u32 s37, s36, 392448\n"
        "v_cvt_pk_bf16_f32 v244, v208, v209\n"
        "v_cvt_pk_bf16_f32 v245, v210, v211\n"
        "v_mfma_f32_16x16x32_bf16 v[208:211], v[136:139], v[48:51], 0\n"
        "ds_read_b32 v182, v6\n"
        "v_mfma_f32_16x16x32_bf16 v[208:211], v[132:135], v[52:55], v[208:211]\n"
        "v_cvt_pk_bf16_f32 v246, v204, v205\n"
        "v_cvt_pk_bf16_f32 v247, v206, v207\n"
        "v_mfma_f32_16x16x32_bf16 v[208:211], v[128:131], v[220:223], v[208:211]\n"
        "buffer_store_dwordx3 v[32:34], v8, s[8:11], s37 offen sc0 sc1 nt\n"
        "v_mfma_f32_16x16x32_bf16 v[204:207], v[140:143], v[220:223], 0\n"
        "v_mfma_f32_16x16x32_bf16 v[204:207], v[144:147], v[52:55], v[204:207]\n"
        "s_add_u32 s37, s36, 392448\n"
        "s_waitcnt lgkmcnt(0)\n"
        "v_and_b32 v254, s28, v182\n"
        "v_mfma_f32_16x16x32_bf16 v[204:207], v[148:151], v[48:51], v[204:207]\n"
        "v_sub_f32 v34, v182, v254\n"
        "v_cvt_pk_bf16_f32 v238, v208, v209\n"
        "v_cvt_pk_bf16_f32 v239, v210, v211\n"
        "v_mfma_f32_16x16x32_bf16 v[208:211], v[148:151], v[240:243], 0\n"
        "v_and_b32 v82, s28, v182\n"
        "v_cvt_pk_bf16_f32 v255, v34, v254\n"
        "v_mfma_f32_16x16x32_bf16 v[208:211], v[144:147], v[76:79], v[208:211]\n"
        "v_cvt_pk_bf16_f32 v83, v82, v34\n"
        "v_cvt_pk_bf16_f32 v44, v204, v205\n"
        "v_mfma_f32_16x16x32_bf16 v[208:211], v[140:143], v[184:187], v[208:211]\n"
        "buffer_store_dwordx3 v[20:22], v11, s[8:11], s37 offen offset:768 sc0 sc1 nt\n"
        "v_cvt_pk_bf16_f32 v45, v206, v207\n"
        "v_readlane_b32 s58, v182, 56\n"
        "v_mfma_f32_16x16x32_bf16 v[204:207], v[152:155], v[184:187], 0\n"
        "s_add_u32 s37, s36, 490560\n"
        "v_mfma_f32_16x16x32_bf16 v[204:207], v[156:159], v[76:79], v[204:207]\n"
        "v_mfma_f32_16x16x32_bf16 v[204:207], v[160:163], v[240:243], v[204:207]\n"
        "v_mfma_f32_16x16x32_bf16 v[188:191], v[160:163], v[48:51], 0\n"
        "v_cvt_pk_bf16_f32 v56, v208, v209\n"
        "v_cvt_pk_bf16_f32 v57, v210, v211\n"
        "v_mfma_f32_16x16x32_bf16 v[188:191], v[156:159], v[52:55], v[188:191]\n"
        "v_mfma_f32_16x16x32_bf16 v[188:191], v[152:155], v[220:223], v[188:191]\n"
        "buffer_store_dwordx3 v[58:60], v8, s[8:11], s37 offen offset:192 sc0 sc1 nt\n"
        "v_mfma_f32_16x16x4_4b_bf16 v[20:35], v[254:255], v[82:83], 0 blgp:4\n"
        "s_add_u32 s37, s36, 490560\n"
        "v_cvt_pk_bf16_f32 v58, v204, v205\n"
        "v_cvt_pk_bf16_f32 v59, v206, v207\n"
        "ds_read_b128 v[228:231], v3 offset:6656\n"
        "ds_read_b128 v[196:199], v3 offset:6720\n"
        "ds_read_b128 v[60:63], v3 offset:6784\n"
        "v_cvt_pk_bf16_f32 v47, v190, v191\n"
        "v_cvt_pk_bf16_f32 v46, v188, v189\n"
        "v_mfma_f32_16x16x32_bf16 v[68:71], v[220:223], v[224:227], 0\n"
        "v_mfma_f32_16x16x32_bf16 v[68:71], v[52:55], v[236:239], v[68:71]\n"
        "v_mfma_f32_16x16x32_bf16 v[68:71], v[48:51], v[44:47], v[68:71]\n"
        "buffer_store_dwordx3 v[72:74], v10, s[8:11], s37 offen offset:960 sc0 sc1 nt\n"
        "v_mfma_f32_16x16x32_bf16 v[250:253], v[240:243], v[44:47], 0\n"
        "ds_read_b128 v[64:67], v4\n"
        "v_mfma_f32_16x16x32_bf16 v[250:253], v[76:79], v[236:239], v[250:253]\n"
        "ds_read_b128 v[34:37], v4 offset:64\n"
        "s_add_u32 s37, s36, 588672\n"
        "v_mfma_f32_16x16x32_bf16 v[250:253], v[184:187], v[224:227], v[250:253]\n"
        "ds_read_b128 v[72:75], v4 offset:128\n"
        "v_mul_f32 v68, v68, v20\n"
        "v_mul_f32 v69, v69, v21\n"
        "v_exp_f32 v68, v68\n"
        "s_waitcnt lgkmcnt(3)\n"
        "v_mfma_f32_16x16x32_bf16 v[188:191], v[228:231], v[224:227], 0\n"
        "v_exp_f32 v69, v69\n"
        "v_mfma_f32_16x16x32_bf16 v[188:191], v[196:199], v[236:239], v[188:191]\n"
        "v_mul_f32 v70, v70, v22\n"
        "v_mul_f32 v71, v71, v23\n"
        "v_mfma_f32_16x16x32_bf16 v[188:191], v[60:63], v[44:47], v[188:191]\n"
        "buffer_store_dwordx3 v[16:18], v8, s[8:11], s37 offen offset:384 sc0 sc1 nt\n"
        "v_exp_f32 v70, v70\n"
        "s_waitcnt lgkmcnt(0)\n"
        "v_mfma_f32_16x16x32_bf16 v[16:19], v[72:75], v[44:47], 0\n"
        "v_add_f32 v183, v68, v69\n"
        "v_mul_f32 v250, v250, v24\n"
        "v_mfma_f32_16x16x32_bf16 v[16:19], v[34:37], v[236:239], v[16:19]\n"
        "v_exp_f32 v71, v71\n"
        "v_mfma_f32_16x16x32_bf16 v[16:19], v[64:67], v[224:227], v[16:19]\n"
        "v_add_f32 v183, v183, v70\n"
        "v_exp_f32 v250, v250\n"
        "v_mfma_f32_16x16x32_bf16 v[20:23], v[220:223], v[38:41], 0\n"
        "v_mul_f32 v251, v251, v25\n"
        "v_add_f32 v183, v183, v71\n"
        "v_mfma_f32_16x16x32_bf16 v[20:23], v[52:55], v[244:247], v[20:23]\n"
        "v_exp_f32 v251, v251\n"
        "v_mfma_f32_16x16x32_bf16 v[20:23], v[48:51], v[56:59], v[20:23]\n"
        "v_mul_f32 v252, v252, v26\n"
        "v_add_f32 v183, v183, v250\n"
        "v_exp_f32 v252, v252\n"
        "v_mfma_f32_16x16x32_bf16 v[224:227], v[240:243], v[56:59], 0\n"
        "v_mul_f32 v253, v253, v27\n"
        "v_add_f32 v183, v183, v251\n"
        "v_mfma_f32_16x16x32_bf16 v[224:227], v[76:79], v[244:247], v[224:227]\n"
        "v_exp_f32 v253, v253\n"
        "v_mfma_f32_16x16x32_bf16 v[224:227], v[184:187], v[38:41], v[224:227]\n"
        "v_mul_f32 v188, v188, v28\n"
        "v_add_f32 v183, v183, v252\n"
        "v_mul_f32 v189, v189, v29\n"
        "v_mfma_f32_16x16x32_bf16 v[236:239], v[228:231], v[38:41], 0\n"
        "v_exp_f32 v188, v188\n"
        "v_mfma_f32_16x16x32_bf16 v[236:239], v[196:199], v[244:247], v[236:239]\n"
        "v_add_f32 v183, v183, v253\n"
        "v_mul_f32 v190, v190, v30\n"
        "v_mfma_f32_16x16x32_bf16 v[236:239], v[60:63], v[56:59], v[236:239]\n"
        "v_exp_f32 v189, v189\n"
        "v_mfma_f32_16x16x4_4b_bf16 v[204:219], v[254:255], v[82:83], 0 blgp:5\n"
        "v_add_f32 v183, v183, v188\n"
        "v_exp_f32 v190, v190\n"
        "v_mul_f32 v191, v191, v31\n"
        "v_add_f32 v183, v183, v189\n"
        "v_exp_f32 v191, v191\n"
        "v_mul_f32 v16, v16, v32\n"
        "v_add_f32 v183, v183, v190\n"
        "v_exp_f32 v16, v16\n"
        "v_mfma_f32_16x16x32_bf16 v[84:87], v[72:75], v[56:59], 0\n"
        "v_add_f32 v183, v183, v191\n"
        "v_mfma_f32_16x16x32_bf16 v[84:87], v[34:37], v[244:247], v[84:87]\n"
        "v_mfma_f32_16x16x32_bf16 v[84:87], v[64:67], v[38:41], v[84:87]\n"
        "v_mul_f32 v20, v20, v204\n"
        "v_mul_f32 v21, v21, v205\n"
        "v_mul_f32 v22, v22, v206\n"
        "v_mfma_f32_16x16x32_bf16 v[244:247], v[92:95], v[228:231], 0\n"
        "v_exp_f32 v20, v20\n"
        "v_mfma_f32_16x16x32_bf16 v[244:247], v[96:99], v[196:199], v[244:247]\n"
        "v_exp_f32 v21, v21\n"
        "v_mfma_f32_16x16x32_bf16 v[244:247], v[100:103], v[60:63], v[244:247]\n"
        "v_exp_f32 v22, v22\n"
        "v_mul_f32 v23, v23, v207\n"
        "v_add_f32 v219, v20, v21\n"
        "v_mfma_f32_16x16x32_bf16 v[204:207], v[112:115], v[60:63], 0\n"
        "v_exp_f32 v23, v23\n"
        "v_mfma_f32_16x16x32_bf16 v[204:207], v[108:111], v[196:199], v[204:207]\n"
        "v_mul_f32 v224, v224, v208\n"
        "v_add_f32 v219, v219, v22\n"
        "v_mfma_f32_16x16x32_bf16 v[204:207], v[104:107], v[228:231], v[204:207]\n"
        "v_cvt_pk_bf16_f32 v40, v244, v245\n"
        "v_cvt_pk_bf16_f32 v41, v246, v247\n"
        "v_mul_f32 v225, v225, v209\n"
        "v_mfma_f32_16x16x32_bf16 v[244:247], v[116:119], v[228:231], 0\n"
        "v_exp_f32 v224, v224\n"
        "v_mfma_f32_16x16x32_bf16 v[244:247], v[120:123], v[196:199], v[244:247]\n"
        "v_add_f32 v219, v219, v23\n"
        "v_mul_f32 v226, v226, v210\n"
        "v_mfma_f32_16x16x32_bf16 v[244:247], v[124:127], v[60:63], v[244:247]\n"
        "v_cvt_pk_bf16_f32 v42, v204, v205\n"
        "v_cvt_pk_bf16_f32 v43, v206, v207\n"
        "v_exp_f32 v225, v225\n"
        "v_mfma_f32_16x16x32_bf16 v[204:207], v[136:139], v[60:63], 0\n"
        "v_add_f32 v219, v219, v224\n"
        "v_mul_f32 v227, v227, v211\n"
        "v_mfma_f32_16x16x32_bf16 v[204:207], v[132:135], v[196:199], v[204:207]\n"
        "v_exp_f32 v226, v226\n"
        "v_mfma_f32_16x16x32_bf16 v[204:207], v[128:131], v[228:231], v[204:207]\n"
        "v_cvt_pk_bf16_f32 v24, v244, v245\n"
        "v_cvt_pk_bf16_f32 v25, v246, v247\n"
        "v_add_f32 v219, v219, v225\n"
        "v_mfma_f32_16x16x32_bf16 v[208:211], v[140:143], v[228:231], 0\n"
        "v_exp_f32 v227, v227\n"
        "v_mfma_f32_16x16x32_bf16 v[208:211], v[144:147], v[196:199], v[208:211]\n"
        "v_mul_f32 v236, v236, v212\n"
        "v_add_f32 v219, v219, v226\n"
        "v_mfma_f32_16x16x32_bf16 v[208:211], v[148:151], v[60:63], v[208:211]\n"
        "v_cvt_pk_bf16_f32 v26, v204, v205\n"
        "v_cvt_pk_bf16_f32 v27, v206, v207\n"
        "v_exp_f32 v236, v236\n"
        "v_mfma_f32_16x16x32_bf16 v[232:235], v[160:163], v[60:63], 0\n"
        "v_mul_f32 v237, v237, v213\n"
        "v_add_f32 v219, v219, v227\n"
        "v_mfma_f32_16x16x32_bf16 v[232:235], v[156:159], v[196:199], v[232:235]\n"
        "v_exp_f32 v237, v237\n"
        "v_mfma_f32_16x16x32_bf16 v[232:235], v[152:155], v[228:231], v[232:235]\n"
        "v_cvt_pk_bf16_f32 v244, v208, v209\n"
        "v_cvt_pk_bf16_f32 v245, v210, v211\n"
        "v_mul_f32 v238, v238, v214\n"
        "v_add_f32 v219, v219, v236\n"
        "v_exp_f32 v238, v238\n"
        "v_mul_f32 v239, v239, v215\n"
        "v_add_f32 v219, v219, v237\n"
        "v_mfma_f32_16x16x4_4b_bf16 v[200:215], v[254:255], v[82:83], 0 blgp:6\n"
        "v_cvt_pk_bf16_f32 v246, v232, v233\n"
        "v_cvt_pk_bf16_f32 v247, v234, v235\n"
        "v_exp_f32 v239, v239\n"
        "v_mul_f32 v84, v84, v216\n"
        "v_add_f32 v219, v219, v238\n"
        "v_exp_f32 v84, v84\n"
        "v_add_f32 v219, v219, v239\n"
        "v_add_f32_dpp v183, v16, v183 quad_perm:[0,1,2,3] row_mask:0x4 bank_mask:0xf\n"
        "v_mfma_f32_16x16x32_bf16 v[232:235], v[220:223], v[40:43], 0\n"
        "v_add_f32_dpp v219, v84, v219 quad_perm:[0,1,2,3] row_mask:0x4 bank_mask:0xf\n"
        "v_mfma_f32_16x16x32_bf16 v[232:235], v[52:55], v[24:27], v[232:235]\n"
        "v_mfma_f32_16x16x32_bf16 v[232:235], v[48:51], v[244:247], v[232:235]\n"
        "v_permlane32_swap_b32 v183, v219\n"
        "v_add_f32 v183, v183, v219\n"
        "v_mfma_f32_16x16x32_bf16 v[80:83], v[240:243], v[244:247], 0\n"
        "v_mov_b32 v219, v183\n"
        "v_mfma_f32_16x16x32_bf16 v[80:83], v[76:79], v[24:27], v[80:83]\n"
        "v_mfma_f32_16x16x32_bf16 v[80:83], v[184:187], v[40:43], v[80:83]\n"
        "v_permlane16_swap_b32 v183, v219\n"
        "v_add_f32 v183, v183, v219\n"
        "v_mul_f32 v232, v232, v200\n"
        "v_mfma_f32_16x16x32_bf16 v[216:219], v[228:231], v[40:43], 0\n"
        "ds_read_b128 v[28:31], v12\n"
        "v_mul_f32 v233, v233, v201\n"
        "v_mfma_f32_16x16x32_bf16 v[216:219], v[196:199], v[24:27], v[216:219]\n"
        "ds_read_b128 v[44:47], v12 offset:64\n"
        "v_mul_f32 v234, v234, v202\n"
        "v_mfma_f32_16x16x32_bf16 v[216:219], v[60:63], v[244:247], v[216:219]\n"
        "ds_read_b128 v[56:59], v12 offset:128\n"
        "v_rcp_f32 v17, v183\n"
        "v_exp_f32 v232, v232\n"
        "v_and_b32 v180, s28, v17\n"
        "v_mfma_f32_16x16x32_bf16 v[86:89], v[72:75], v[244:247], 0\n"
        "v_sub_f32 v202, v17, v180\n"
        "v_cvt_pk_bf16_f32 v181, v180, v202\n"
        "v_mfma_f32_16x16x32_bf16 v[86:89], v[34:37], v[24:27], v[86:89]\n"
        "v_exp_f32 v233, v233\n"
        "v_mfma_f32_16x16x32_bf16 v[86:89], v[64:67], v[40:43], v[86:89]\n"
        "v_exp_f32 v234, v234\n"
        "s_waitcnt lgkmcnt(0)\n"
        "v_mfma_f32_16x16x32_bf16 v[40:43], v[28:31], v[220:223], 0\n"
        "v_mul_f32 v235, v235, v203\n"
        "v_add_f32 v27, v232, v233\n"
        "v_mfma_f32_16x16x32_bf16 v[40:43], v[44:47], v[52:55], v[40:43]\n"
        "ds_read_b32 v67, v7 offset:192\n"
        "v_mul_f32 v80, v80, v204\n"
        "v_mfma_f32_16x16x32_bf16 v[40:43], v[56:59], v[48:51], v[40:43]\n"
        "v_exp_f32 v235, v235\n"
        "v_add_f32 v27, v27, v234\n"
        "v_mul_f32 v81, v81, v205\n"
        "v_mfma_f32_16x16x32_bf16 v[200:203], v[56:59], v[240:243], 0\n"
        "v_exp_f32 v80, v80\n"
        "v_mfma_f32_16x16x32_bf16 v[200:203], v[44:47], v[76:79], v[200:203]\n"
        "v_add_f32 v27, v27, v235\n"
        "v_mul_f32 v82, v82, v206\n"
        "v_mfma_f32_16x16x32_bf16 v[200:203], v[28:31], v[184:187], v[200:203]\n"
        "v_exp_f32 v81, v81\n"
        "v_add_f32 v27, v27, v80\n"
        "v_mfma_f32_16x16x32_bf16 v[88:91], v[28:31], v[228:231], 0\n"
        "v_exp_f32 v82, v82\n"
        "v_mfma_f32_16x16x32_bf16 v[88:91], v[44:47], v[196:199], v[88:91]\n"
        "v_mul_f32 v83, v83, v207\n"
        "v_add_f32 v27, v27, v81\n"
        "v_mfma_f32_16x16x32_bf16 v[88:91], v[56:59], v[60:63], v[88:91]\n"
        "v_cndmask_b32_e64 v207, v40, v200, s[50:51]\n"
        "v_exp_f32 v83, v83\n"
        "v_mfma_f32_16x16x32_bf16 v[44:47], v[220:223], v[164:167], 0\n"
        "ds_read_b128 v[228:231], v13\n"
        "v_mul_f32 v216, v216, v208\n"
        "v_mfma_f32_16x16x32_bf16 v[44:47], v[52:55], v[168:171], v[44:47]\n"
        "ds_read_b128 v[196:199], v13 offset:64\n"
        "v_add_f32 v27, v27, v82\n"
        "v_mfma_f32_16x16x32_bf16 v[44:47], v[48:51], v[172:175], v[44:47]\n"
        "ds_read_b128 v[200:203], v13 offset:128\n"
        "v_cndmask_b32_e64 v207, v207, v88, s[52:53]\n"
        "s_waitcnt lgkmcnt(3)\n"
        "v_cndmask_b32_e64 v207, v207, v67, s[54:55]\n"
        "v_mul_f32 v207, v207, v182\n"
        "v_mul_f32 v217, v217, v209\n"
        "v_mfma_f32_16x16x32_bf16 v[40:43], v[240:243], v[172:175], 0\n"
        "ds_read_b128 v[72:75], v0 offset:11312\n"
        "v_cvt_pk_bf16_f32 v60, v44, v45\n"
        "v_mfma_f32_16x16x32_bf16 v[40:43], v[76:79], v[168:171], v[40:43]\n"
        "ds_read_b128 v[64:67], v0 offset:10288\n"
        "v_cvt_pk_bf16_f32 v61, v46, v47\n"
        "v_mfma_f32_16x16x32_bf16 v[40:43], v[184:187], v[164:167], v[40:43]\n"
        "s_mov_b64 exec, s[56:57]\n"
        "v_mov_b32 v207, 0xff800000\n"
        "s_mov_b64 exec, -1\n"
        "v_mul_f32 v207, s58, v207\n"
        "s_waitcnt lgkmcnt(2)\n"
        "v_mfma_f32_16x16x32_bf16 v[88:91], v[228:231], v[164:167], 0\n"
        "v_exp_f32 v216, v216\n"
        "v_mfma_f32_16x16x32_bf16 v[88:91], v[196:199], v[168:171], v[88:91]\n"
        "v_cvt_pk_bf16_f32 v62, v40, v41\n"
        "v_cvt_pk_bf16_f32 v63, v42, v43\n"
        "v_mfma_f32_16x16x32_bf16 v[88:91], v[200:203], v[172:175], v[88:91]\n"
        "s_waitcnt lgkmcnt(0)\n"
        "v_exp_f32 v207, v207\n"
        "v_add_f32 v27, v27, v83\n"
        "v_mul_f32 v218, v218, v210\n"
        "v_mfma_f32_16x16x4_4b_bf16 v[32:47], v[254:255], v[180:181], 0 blgp:4\n"
        "v_exp_f32 v217, v217\n"
        "v_add_f32 v27, v27, v216\n"
        "v_exp_f32 v218, v218\n"
        "v_cvt_pk_bf16_f32 v24, v88, v89\n"
        "v_cvt_pk_bf16_f32 v28, v90, v91\n"
        "v_mul_f32 v219, v219, v211\n"
        "v_add_f32_dpp v18, v207, v207 row_ror:8 row_mask:0xf bank_mask:0xf\n"
        "v_add_f32 v27, v27, v217\n"
        "v_mul_f32 v86, v86, v212\n"
        "v_mfma_f32_16x16x32_bf16 v[212:215], v[200:203], v[72:75], 0\n"
        "v_exp_f32 v219, v219\n"
        "v_mfma_f32_16x16x32_bf16 v[212:215], v[196:199], v[64:67], v[212:215]\n"
        "v_mul_f32 v68, v68, v32\n"
        "v_mul_f32 v69, v69, v33\n"
        "v_mfma_f32_16x16x32_bf16 v[212:215], v[228:231], v[176:179], v[212:215]\n"
        "v_mul_f32 v70, v70, v34\n"
        "v_mul_f32 v71, v71, v35\n"
        "v_mul_f32 v250, v250, v36\n"
        "v_mfma_f32_16x16x32_bf16 v[32:35], v[184:187], v[176:179], 0\n"
        "ds_read_b128 v[192:195], v0 offset:12352\n"
        "v_mul_f32 v251, v251, v37\n"
        "v_mfma_f32_16x16x32_bf16 v[32:35], v[76:79], v[64:67], v[32:35]\n"
        "ds_read_b128 v[208:211], v0 offset:13376\n"
        "v_cvt_pk_bf16_f32 v244, v212, v213\n"
        "v_mfma_f32_16x16x32_bf16 v[32:35], v[240:243], v[72:75], v[32:35]\n"
        "ds_read_b128 v[88:91], v0 offset:14400\n"
        "v_cvt_pk_bf16_f32 v248, v214, v215\n"
        "v_mul_f32 v252, v252, v38\n"
        "v_mul_f32 v253, v253, v39\n"
        "v_mul_f32 v188, v188, v40\n"
        "v_mul_f32 v189, v189, v41\n"
        "v_mfma_f32_16x16x32_bf16 v[56:59], v[48:51], v[72:75], 0\n"
        "v_mul_f32 v190, v190, v42\n"
        "v_cvt_pk_bf16_f32 v74, v32, v33\n"
        "v_mfma_f32_16x16x32_bf16 v[56:59], v[52:55], v[64:67], v[56:59]\n"
        "v_cvt_pk_bf16_f32 v75, v34, v35\n"
        "v_mul_f32 v191, v191, v43\n"
        "v_mfma_f32_16x16x32_bf16 v[56:59], v[220:223], v[176:179], v[56:59]\n"
        "v_fma_f32 v16, v16, v44, v15\n"
        "v_cvt_pk_bf16_f32 v64, v68, v69\n"
        "s_waitcnt lgkmcnt(0)\n"
        "v_mfma_f32_16x16x4_4b_bf16 v[32:47], v[254:255], v[180:181], 0 blgp:6\n"
        "v_cvt_pk_bf16_f32 v65, v70, v71\n"
        "v_cvt_pk_bf16_f32 v66, v250, v251\n"
        "v_cvt_pk_bf16_f32 v67, v252, v253\n"
        "v_cvt_pk_bf16_f32 v212, v188, v189\n"
        "v_cvt_pk_bf16_f32 v72, v56, v57\n"
        "v_cvt_pk_bf16_f32 v73, v58, v59\n"
        "v_cvt_pk_bf16_f32 v214, v190, v191\n"
        "v_cvt_pk_bf16_f32 v213, v16, 0\n"
        "v_add_f32_dpp v18, v18, v18 row_ror:4 row_mask:0xf bank_mask:0xf\n"
        "v_add_f32 v27, v27, v218\n"
        "v_mfma_f32_16x16x32_bf16 v[68:71], v[220:223], v[192:195], 0\n"
        "v_exp_f32 v86, v86\n"
        "v_mfma_f32_16x16x32_bf16 v[68:71], v[52:55], v[208:211], v[68:71]\n"
        "v_add_f32_dpp v18, v18, v18 row_ror:2 row_mask:0xf bank_mask:0xf\n"
        "v_add_f32 v27, v27, v219\n"
        "v_mfma_f32_16x16x32_bf16 v[68:71], v[48:51], v[88:91], v[68:71]\n"
        "v_add_f32_dpp v18, v18, v18 row_ror:1 row_mask:0xf bank_mask:0xf\n"
        "v_add_f32_dpp v27, v86, v27 quad_perm:[0,1,2,3] row_mask:0x4 bank_mask:0xf\n"
        "v_mul_f32 v207, v207, v182\n"
        "v_mfma_f32_16x16x32_bf16 v[250:253], v[240:243], v[88:91], 0\n"
        "ds_read_b128 v[188:191], v0 offset:17488\n"
        "v_min3_f32 v47, v183, v17, v182\n"
        "v_mfma_f32_16x16x32_bf16 v[250:253], v[76:79], v[208:211], v[250:253]\n"
        "ds_read_b128 v[180:183], v0 offset:16464\n"
        "v_cvt_pk_bf16_f32 v16, v68, v69\n"
        "v_mfma_f32_16x16x32_bf16 v[250:253], v[184:187], v[192:195], v[250:253]\n"
        "ds_read_b128 v[56:59], v0 offset:15440\n"
        "v_cvt_pk_bf16_f32 v17, v70, v71\n"
        "v_permlane32_swap_b32 v27, v18\n"
        "v_add_f32 v27, v27, v18\n"
        "v_mov_b32 v204, v27\n"
        "v_mfma_f32_16x16x32_bf16 v[68:71], v[228:231], v[192:195], 0\n"
        "v_mul_f32 v20, v20, v32\n"
        "v_mul_f32 v21, v21, v33\n"
        "v_mfma_f32_16x16x32_bf16 v[68:71], v[196:199], v[208:211], v[68:71]\n"
        "v_cvt_pk_bf16_f32 v18, v250, v251\n"
        "v_cvt_pk_bf16_f32 v19, v252, v253\n"
        "v_mfma_f32_16x16x32_bf16 v[68:71], v[200:203], v[88:91], v[68:71]\n"
        "v_permlane16_swap_b32 v27, v204\n"
        "s_waitcnt lgkmcnt(0)\n"
        "v_mfma_f32_16x16x32_bf16 v[250:253], v[200:203], v[188:191], 0\n"
        "v_add_f32 v27, v27, v204\n"
        "v_mul_f32 v22, v22, v34\n"
        "v_mfma_f32_16x16x32_bf16 v[250:253], v[196:199], v[180:183], v[250:253]\n"
        "v_rcp_f32 v87, v27\n"
        "v_mfma_f32_16x16x32_bf16 v[250:253], v[228:231], v[56:59], v[250:253]\n"
        "v_cvt_pk_bf16_f32 v204, v68, v69\n"
        "v_cvt_pk_bf16_f32 v208, v70, v71\n"
        "v_readlane_b32 s59, v87, 32\n"
        "v_mfma_f32_16x16x32_bf16 v[192:195], v[184:187], v[56:59], 0\n"
        "ds_read_b128 v[68:71], v0 offset:18528\n"
        "v_mul_f32 v207, s59, v207\n"
        "v_mfma_f32_16x16x32_bf16 v[192:195], v[76:79], v[180:183], v[192:195]\n"
        "ds_read_b128 v[30:33], v0 offset:19552\n"
        "v_cvt_pk_bf16_f32 v25, v250, v251\n"
        "v_mfma_f32_16x16x32_bf16 v[192:195], v[240:243], v[188:191], v[192:195]\n"
        "ds_read_b128 v[88:91], v0 offset:20576\n"
        "v_cvt_pk_bf16_f32 v29, v252, v253\n"
        "v_min3_f32 v47, v47, v27, v87\n"
        "v_cvt_pk_bf16_f32 v207, v207, 0\n"
        "v_cmp_nle_f32 vcc, 0x2b800000, v47\n"
        "v_mul_f32 v23, v23, v35\n"
        "v_mfma_f32_16x16x32_bf16 v[250:253], v[48:51], v[188:191], 0\n"
        "s_andn2_b64 vcc, vcc, s[56:57]\n"
        "v_cvt_pk_bf16_f32 v190, v192, v193\n"
        "v_cvt_pk_bf16_f32 v191, v194, v195\n"
        "v_mfma_f32_16x16x32_bf16 v[250:253], v[52:55], v[180:183], v[250:253]\n"
        "s_cbranch_scc1 CREDO_%=\n"
        "v_mul_f32 v224, v224, v36\n"
        "v_mul_f32 v225, v225, v37\n"
        "v_mfma_f32_16x16x32_bf16 v[250:253], v[220:223], v[56:59], v[250:253]\n"
        "v_mul_f32 v226, v226, v38\n"
        "s_waitcnt lgkmcnt(0)\n"
        "v_mfma_f32_16x16x32_bf16 v[56:59], v[220:223], v[68:71], 0\n"
        "s_mov_b32 exec_hi, 0xffff\n"
        "v_add_u32 v6, s69, v6\n"
        "s_mov_b32 exec_hi, -1\n"
        "v_mfma_f32_16x16x32_bf16 v[56:59], v[52:55], v[30:33], v[56:59]\n"
        "v_mul_f32 v227, v227, v39\n"
        "v_cvt_pk_bf16_f32 v188, v250, v251\n"
        "v_mfma_f32_16x16x32_bf16 v[56:59], v[48:51], v[88:91], v[56:59]\n"
        "v_cvt_pk_bf16_f32 v189, v252, v253\n"
        "v_mul_f32 v236, v236, v40\n"
        "v_mul_f32 v237, v237, v41\n"
        "v_mfma_f32_16x16x32_bf16 v[250:253], v[240:243], v[88:91], 0\n"
        "ds_read_b128 v[38:41], v0 offset:23664\n"
        "v_mul_f32 v238, v238, v42\n"
        "v_mfma_f32_16x16x32_bf16 v[250:253], v[76:79], v[30:33], v[250:253]\n"
        "ds_read_b128 v[180:183], v0 offset:22640\n"
        "v_cvt_pk_bf16_f32 v192, v56, v57\n"
        "v_mfma_f32_16x16x32_bf16 v[250:253], v[184:187], v[68:71], v[250:253]\n"
        "ds_read_b128 v[34:37], v0 offset:21616\n"
        "v_cvt_pk_bf16_f32 v193, v58, v59\n"
        "ds_write_b16 v1, v207\n"
        "v_mul_f32 v239, v239, v43\n"
        "v_mfma_f32_16x16x32_bf16 v[56:59], v[228:231], v[68:71], 0\n"
        "v_fma_f32 v84, v84, v44, v15\n"
        "v_cvt_pk_bf16_f32 v68, v20, v21\n"
        "v_mfma_f32_16x16x32_bf16 v[56:59], v[196:199], v[30:33], v[56:59]\n"
        "v_cvt_pk_bf16_f32 v194, v250, v251\n"
        "v_cvt_pk_bf16_f32 v195, v252, v253\n"
        "v_mfma_f32_16x16x32_bf16 v[56:59], v[200:203], v[88:91], v[56:59]\n"
        "ds_read2_b32 v[30:31], v7 offset0:0 offset1:3\n"
        "v_cvt_pk_bf16_f32 v69, v22, v23\n"
        "s_waitcnt lgkmcnt(2)\n"
        "v_mfma_f32_16x16x32_bf16 v[88:91], v[200:203], v[38:41], 0\n"
        "ds_read_b64_tr_b16 v[26:27], v5 offset:0\n"
        "v_cvt_pk_bf16_f32 v70, v224, v225\n"
        "v_mfma_f32_16x16x32_bf16 v[88:91], v[196:199], v[180:183], v[88:91]\n"
        "ds_read2_b32 v[250:251], v7 offset0:1 offset1:4\n"
        "v_cvt_pk_bf16_f32 v245, v56, v57\n"
        "v_mfma_f32_16x16x32_bf16 v[88:91], v[228:231], v[34:37], v[88:91]\n"
        "ds_read_b64_tr_b16 v[246:247], v5 offset:16\n"
        "v_cvt_pk_bf16_f32 v249, v58, v59\n"
        "v_mov_b32_dpp v215, v15 row_shr:4 row_mask:0xf bank_mask:0xf bound_ctrl:0\n"
        "v_mfma_f32_16x16x32_bf16 v[228:231], v[184:187], v[34:37], 0\n"
        "ds_read2_b32 v[210:211], v7 offset0:2 offset1:5\n"
        "v_cvt_pk_bf16_f32 v71, v226, v227\n"
        "v_mfma_f32_16x16x32_bf16 v[228:231], v[76:79], v[180:183], v[228:231]\n"
        "ds_read_b64_tr_b16 v[206:207], v5 offset:32\n"
        "v_cvt_pk_bf16_f32 v205, v88, v89\n"
        "v_mfma_f32_16x16x32_bf16 v[228:231], v[240:243], v[38:41], v[228:231]\n"
        "v_cvt_pk_bf16_f32 v209, v90, v91\n"
        "v_cvt_pk_bf16_f32 v240, v236, v237\n"
        "v_cvt_pk_bf16_f32 v242, v238, v239\n"
        "v_mfma_f32_16x16x32_bf16 v[44:47], v[48:51], v[38:41], 0\n"
        "v_cvt_pk_bf16_f32 v241, v84, 0\n"
        "v_and_b32 v38, s28, v87\n"
        "v_mfma_f32_16x16x32_bf16 v[44:47], v[52:55], v[180:183], v[44:47]\n"
        "v_sub_f32 v51, v87, v38\n"
        "v_cvt_pk_bf16_f32 v182, v228, v229\n"
        "v_mfma_f32_16x16x32_bf16 v[44:47], v[220:223], v[34:37], v[44:47]\n"
        "s_waitcnt lgkmcnt(1)\n"
        "v_cvt_pk_bf16_f32 v183, v230, v231\n"
        "v_cvt_pk_bf16_f32 v39, v38, v51\n"
        "v_add_u32 v3, s69, v3\n"
        "v_mfma_f32_16x16x32_bf16 v[220:223], v[64:67], v[60:63], 0\n"
        "v_add_u32 v13, s69, v13\n"
        "v_smfmac_f32_16x16x64_bf16 v[220:223], v[212:215], v[24:31], v14\n"
        "ds_read_b64_tr_b16 v[26:27], v5 offset:48\n"
        "v_cvt_pk_bf16_f32 v180, v44, v45\n"
        "v_cvt_pk_bf16_f32 v181, v46, v47\n"
        "v_mfma_f32_16x16x4_4b_bf16 v[40:55], v[254:255], v[38:39], 0 blgp:4\n"
        "v_mfma_f32_16x16x32_bf16 v[228:231], v[64:67], v[72:75], 0\n"
        "v_smfmac_f32_16x16x64_bf16 v[228:231], v[212:215], v[244:251], v14\n"
        "ds_read_b64_tr_b16 v[246:247], v5 offset:64\n"
        "s_waitcnt lgkmcnt(1)\n"
        "v_mfma_f32_16x16x32_bf16 v[252:255], v[64:67], v[16:19], 0\n"
        "s_add_u32 s31, s31, 1\n"
        "v_smfmac_f32_16x16x64_bf16 v[252:255], v[212:215], v[204:211], v14\n"
        "ds_read_b64_tr_b16 v[206:207], v5 offset:80\n"
        "s_mul_i32 s68, s31, 3\n"
        "v_mfma_f32_16x16x32_bf16 v[32:35], v[64:67], v[188:191], 0\n"
        "ds_read_b128 v[36:39], v2\n"
        "v_cvt_pk_bf16_f32 v236, v220, v228\n"
        "v_smfmac_f32_16x16x64_bf16 v[32:35], v[212:215], v[24:31], v14 abid:1\n"
        "ds_read_b128 v[20:23], v2 offset:64\n"
        "v_cvt_pk_bf16_f32 v200, v221, v229\n"
        "v_cvt_pk_bf16_f32 v196, v222, v230\n"
        "s_waitcnt lgkmcnt(3)\n"
        "v_mfma_f32_16x16x32_bf16 v[54:57], v[64:67], v[192:195], 0\n"
        "s_lshr_b32 s38, s31, 3\n"
        "v_cvt_pk_bf16_f32 v184, v223, v231\n"
        "v_mul_f32 v232, v232, v40\n"
        "v_smfmac_f32_16x16x64_bf16 v[54:57], v[212:215], v[244:251], v14 abid:1\n"
        "s_waitcnt lgkmcnt(2)\n"
        "s_add_u32 s38, s38, 11\n"
        "v_cvt_pk_bf16_f32 v237, v252, v32\n"
        "v_cvt_pk_bf16_f32 v201, v253, v33\n"
        "v_mfma_f32_16x16x32_bf16 v[220:223], v[64:67], v[180:183], 0\n"
        "s_bitcmp1_b32 s31, 0\n"
        "v_cvt_pk_bf16_f32 v197, v254, v34\n"
        "v_cvt_pk_bf16_f32 v185, v255, v35\n"
        "v_smfmac_f32_16x16x64_bf16 v[220:223], v[212:215], v[204:211], v14 abid:1\n"
        "s_waitcnt lgkmcnt(0)\n"
        "s_cselect_b32 s38, 3, s38\n"
        "v_mul_f32 v233, v233, v41\n"
        "v_mul_f32 v234, v234, v42\n"
        "v_mul_f32 v235, v235, v43\n"
        "v_mfma_f32_16x16x32_bf16 v[212:215], v[36:39], v[60:63], 0\n"
        "s_add_u32 s68, s68, s38\n"
        "v_mul_f32 v80, v80, v44\n"
        "v_cvt_pk_bf16_f32 v238, v54, v220\n"
        "v_smfmac_f32_16x16x64_bf16 v[212:215], v[20:23], v[24:31], v14\n"
        "s_cmp_lt_u32 s31, s14\n"
        "s_cselect_b32 s68, s68, 0\n"
        "v_cvt_pk_bf16_f32 v202, v55, v221\n"
        "v_cvt_pk_bf16_f32 v198, v56, v222\n"
        "v_mfma_f32_16x16x32_bf16 v[40:43], v[36:39], v[72:75], 0\n"
        "s_and_b32 s38, s31, 7\n"
        "v_cvt_pk_bf16_f32 v186, v57, v223\n"
        "v_mul_f32 v81, v81, v45\n"
        "v_smfmac_f32_16x16x64_bf16 v[40:43], v[20:23], v[244:251], v14\n"
        "s_cselect_b32 s30, 1, -7\n"
        "s_mul_i32 s29, s30, 192\n"
        "v_mul_f32 v82, v82, v46\n"
        "v_mul_f32 v83, v83, v47\n"
        "v_mul_f32 v216, v216, v48\n"
        "v_mfma_f32_16x16x32_bf16 v[44:47], v[36:39], v[16:19], 0\n"
        "s_mul_i32 s30, s30, 392\n"
        "v_mov_b32_dpp v243, v15 row_shr:4 row_mask:0xf bank_mask:0xf bound_ctrl:0\n"
        "v_cvt_pk_bf16_f32 v48, v212, v40\n"
        "v_smfmac_f32_16x16x64_bf16 v[44:47], v[20:23], v[204:211], v14\n"
        "s_add_u32 s37, s35, 0\n"
        "s_cmp_eq_u32 s73, s18\n"
        "v_mul_f32 v217, v217, v49\n"
        "v_mul_f32 v218, v218, v50\n"
        "v_mfma_f32_16x16x32_bf16 v[40:43], v[36:39], v[188:191], 0\n"
        "s_cselect_b32 s34, -7488, s75\n"
        "v_mul_f32 v219, v219, v51\n"
        "v_fma_f32 v86, v86, v52, v15\n"
        "v_smfmac_f32_16x16x64_bf16 v[40:43], v[20:23], v[24:31], v14 abid:1\n"
        "ds_read_b64_tr_b16 v[26:27], v5 offset:3328\n"
        "v_cvt_pk_bf16_f32 v230, v232, v233\n"
        "v_cvt_pk_bf16_f32 v231, v234, v235\n"
        "v_cvt_pk_bf16_f32 v232, v80, v81\n"
        "v_mfma_f32_16x16x32_bf16 v[78:81], v[36:39], v[192:195], 0\n"
        "s_add_u32 s73, s73, s34\n"
        "v_cvt_pk_bf16_f32 v233, v82, v83\n"
        "v_cvt_pk_bf16_f32 v224, v216, v217\n"
        "v_smfmac_f32_16x16x64_bf16 v[78:81], v[20:23], v[244:251], v14 abid:1\n"
        "ds_read_b64_tr_b16 v[246:247], v5 offset:3344\n"
        "buffer_store_dwordx3 v[236:238], v8, s[8:11], s37 offen sc0 sc1\n"
        "v_cvt_pk_bf16_f32 v49, v44, v40\n"
        "v_cvt_pk_bf16_f32 v226, v218, v219\n"
        "v_cvt_pk_bf16_f32 v225, v86, 0\n"
        "v_mfma_f32_16x16x32_bf16 v[216:219], v[36:39], v[180:183], 0\n"
        "s_xor_b32 s74, s74, 4\n"
        "s_mul_i32 s38, s31, 1344\n"
        "s_cmp_ge_u32 s31, s71\n"
        "v_smfmac_f32_16x16x64_bf16 v[216:219], v[20:23], v[204:211], v14 abid:1\n"
        "ds_read_b64_tr_b16 v[206:207], v5 offset:3360\n"
        "s_cselect_b32 s39, s70, s72\n"
        "s_waitcnt lgkmcnt(2)\n"
        "v_mfma_f32_16x16x32_bf16 v[80:83], v[68:71], v[60:63], 0\n"
        "s_add_u32 s37, s35, 0\n"
        "s_add_u32 s39, s39, s38\n"
        "v_writelane_b32 v6, s73, 56\n"
        "v_smfmac_f32_16x16x64_bf16 v[80:83], v[240:243], v[24:31], v14\n"
        "ds_read_b64_tr_b16 v[26:27], v5 offset:3376\n"
        "v_cvt_pk_bf16_f32 v50, v78, v216\n"
        "s_waitcnt lgkmcnt(2)\n"
        "v_mfma_f32_16x16x32_bf16 v[216:219], v[68:71], v[72:75], 0\n"
        "v_add_u32 v7, s30, v7\n"
        "v_smfmac_f32_16x16x64_bf16 v[216:219], v[240:243], v[244:251], v14\n"
        "ds_read_b64_tr_b16 v[246:247], v5 offset:3392\n"
        "buffer_store_dwordx3 v[200:202], v11, s[8:11], s37 offen offset:768 sc0 sc1\n"
        "s_waitcnt lgkmcnt(2)\n"
        "v_mfma_f32_16x16x32_bf16 v[84:87], v[68:71], v[16:19], 0\n"
        "v_mov_b32_dpp v227, v15 row_shr:4 row_mask:0xf bank_mask:0xf bound_ctrl:0\n"
        "v_smfmac_f32_16x16x64_bf16 v[84:87], v[240:243], v[204:211], v14\n"
        "ds_read_b64_tr_b16 v[206:207], v5 offset:3408\n"
        "s_add_u32 s37, s35, 98112\n"
        "s_mov_b64 exec, s[62:63]\n"
        "v_add_u32 v12, s29, v12\n"
        "v_add_u32 v4, s34, v4\n"
        "s_mov_b64 exec, -1\n"
        "s_waitcnt lgkmcnt(1)\n"
        "v_mfma_f32_16x16x32_bf16 v[20:23], v[68:71], v[188:191], 0\n"
        "v_smfmac_f32_16x16x64_bf16 v[20:23], v[240:243], v[24:31], v14 abid:1\n"
        "ds_read_b64_tr_b16 v[26:27], v5 offset:6656\n"
        "v_mov_b32 v78, s74\n"
        "v_mfma_f32_16x16x32_bf16 v[32:35], v[68:71], v[192:195], 0\n"
        "v_smfmac_f32_16x16x64_bf16 v[32:35], v[240:243], v[244:251], v14 abid:1\n"
        "ds_read_b64_tr_b16 v[246:247], v5 offset:6672\n"
        "buffer_store_dwordx3 v[196:198], v8, s[8:11], s37 offen offset:192 sc0 sc1 nt\n"
        "s_waitcnt lgkmcnt(2)\n"
        "v_mfma_f32_16x16x32_bf16 v[36:39], v[68:71], v[180:183], 0\n"
        "ds_read_b32 v223, v78\n"
        "v_smfmac_f32_16x16x64_bf16 v[36:39], v[240:243], v[204:211], v14 abid:1\n"
        "ds_read_b64_tr_b16 v[206:207], v5 offset:6688\n"
        "s_add_u32 s37, s35, 98112\n"
        "s_waitcnt lgkmcnt(3)\n"
        "v_mfma_f32_16x16x32_bf16 v[40:43], v[230:233], v[60:63], 0\n"
        "v_smfmac_f32_16x16x64_bf16 v[40:43], v[224:227], v[24:31], v14\n"
        "ds_read_b64_tr_b16 v[26:27], v5 offset:6704\n"
        "s_waitcnt lgkmcnt(3)\n"
        "v_mfma_f32_16x16x32_bf16 v[60:63], v[230:233], v[72:75], 0\n"
        "v_smfmac_f32_16x16x64_bf16 v[60:63], v[224:227], v[244:251], v14\n"
        "ds_read_b64_tr_b16 v[246:247], v5 offset:6720\n"
        "buffer_store_dwordx3 v[184:186], v10, s[8:11], s37 offen offset:960 sc0 sc1 nt\n"
        "s_add_u32 s37, s35, 588672\n"
        "s_add_u32 s46, s37, 1344\n"
        "buffer_store_dwordx3 v[48:50], v8, s[44:47], s37 offen offset:1152 sc0 sc1 nt\n"
        "s_waitcnt lgkmcnt(1)\n"
        "v_readfirstlane_b32 s40, v223\n"
        "v_mfma_f32_16x16x32_bf16 v[72:75], v[230:233], v[16:19], 0\n"
        "s_cmp_lt_i32 s40, s68\n"
        "v_xor_b32 v244, 4, v78\n"
        "v_smfmac_f32_16x16x64_bf16 v[72:75], v[224:227], v[204:211], v14\n"
        "ds_read_b64_tr_b16 v[206:207], v5 offset:6736\n"
        "s_cbranch_scc1 L17_%=\n"
        "L18_%=:\n"
        "ds_read_b128 v[220:223], v3\n"
        "ds_read_b128 v[184:187], v3 offset:3328\n"
        "ds_read_b128 v[52:55], v3 offset:64\n"
        "ds_read_b128 v[76:79], v3 offset:3392\n"
        "v_add_u32 v5, s69, v5\n"
        "v_mfma_f32_16x16x32_bf16 v[16:19], v[230:233], v[188:191], 0\n"
        "ds_read_b128 v[48:51], v3 offset:128\n"
        "v_smfmac_f32_16x16x64_bf16 v[16:19], v[224:227], v[24:31], v14 abid:1\n"
        "s_waitcnt lgkmcnt(6)\n"
        "ds_read_b128 v[240:243], v3 offset:3456\n"
        "v_mfma_f32_16x16x32_bf16 v[24:27], v[230:233], v[192:195], 0\n"
        "s_sub_u32 s69, 0, s69\n"
        "v_smfmac_f32_16x16x64_bf16 v[24:27], v[224:227], v[244:251], v14 abid:1\n"
        "s_waitcnt lgkmcnt(3)\n"
        "s_mov_b64 exec, 1\n"
        "ds_inc_u32 v244, v1 offset:8\n"
        "s_mov_b64 exec, -1\n"
        "s_mov_b32 s36, s35\n"
        "v_mfma_f32_16x16x32_bf16 v[244:247], v[230:233], v[180:183], 0\n"
        "s_mov_b32 s35, s39\n"
        "v_smfmac_f32_16x16x64_bf16 v[244:247], v[224:227], v[204:211], v14 abid:1\n"
        "s_setprio 0\n"
        "s_cmp_ge_u32 s31, s14\n"
        "s_cbranch_scc1 CEND_%=\n"
        "s_cmp_gt_i32 s29, 0\n"
        "s_cbranch_scc1 CTOP_%=\n"
        "s_branch CFULL_%=\n"
        "CEND_%=:\n"
        "s_waitcnt lgkmcnt(0)\n"
        "s_nop 15\n"
        "v_cvt_pk_bf16_f32 v214, v80, v216\n"
        "v_cvt_pk_bf16_f32 v215, v84, v20\n"
        "v_cvt_pk_bf16_f32 v216, v32, v36\n"
        "s_add_u32 s37, s36, 196224\n"
        "buffer_store_dwordx3 v[214:216], v8, s[8:11], s37 offen offset:384 sc0 sc1 nt\n"
        "v_cvt_pk_bf16_f32 v212, v81, v217\n"
        "v_cvt_pk_bf16_f32 v213, v85, v21\n"
        "v_cvt_pk_bf16_f32 v214, v33, v37\n"
        "s_add_u32 s37, s36, 196224\n"
        "buffer_store_dwordx3 v[212:214], v9, s[8:11], s37 offen offset:1152 sc0 sc1 nt\n"
        "v_cvt_pk_bf16_f32 v216, v82, v218\n"
        "v_cvt_pk_bf16_f32 v217, v86, v22\n"
        "v_cvt_pk_bf16_f32 v218, v34, v38\n"
        "s_add_u32 s37, s36, 294336\n"
        "buffer_store_dwordx3 v[216:218], v8, s[8:11], s37 offen offset:576 sc0 sc1 nt\n"
        "v_cvt_pk_bf16_f32 v214, v83, v219\n"
        "v_cvt_pk_bf16_f32 v215, v87, v23\n"
        "v_cvt_pk_bf16_f32 v216, v35, v39\n"
        "s_add_u32 s37, s36, 392448\n"
        "buffer_store_dwordx3 v[214:216], v8, s[8:11], s37 offen sc0 sc1 nt\n"
        "v_cvt_pk_bf16_f32 v38, v40, v60\n"
        "v_cvt_pk_bf16_f32 v39, v72, v16\n"
        "v_cvt_pk_bf16_f32 v40, v24, v244\n"
        "s_add_u32 s37, s36, 392448\n"
        "buffer_store_dwordx3 v[38:40], v11, s[8:11], s37 offen offset:768 sc0 sc1 nt\n"
        "v_cvt_pk_bf16_f32 v22, v41, v61\n"
        "v_cvt_pk_bf16_f32 v23, v73, v17\n"
        "v_cvt_pk_bf16_f32 v24, v25, v245\n"
        "s_add_u32 s37, s36, 490560\n"
        "buffer_store_dwordx3 v[22:24], v8, s[8:11], s37 offen offset:192 sc0 sc1 nt\n"
        "v_cvt_pk_bf16_f32 v40, v42, v62\n"
        "v_cvt_pk_bf16_f32 v41, v74, v18\n"
        "v_cvt_pk_bf16_f32 v42, v26, v246\n"
        "s_add_u32 s37, s36, 490560\n"
        "buffer_store_dwordx3 v[40:42], v10, s[8:11], s37 offen offset:960 sc0 sc1 nt\n"
        "s_add_u32 s37, s36, 588672\n"
        "v_cvt_pk_bf16_f32 v24, v43, v63\n"
        "v_cvt_pk_bf16_f32 v25, v75, v19\n"
        "v_cvt_pk_bf16_f32 v26, v27, v247\n"
        "buffer_store_dwordx3 v[24:26], v8, s[8:11], s37 offen offset:384 sc0 sc1 nt\n"
        "s_nop 1\n"
        "s_endpgm\n"
        "L17_%=:\n"
        "s_nop 7\n"
        "s_nop 7\n"
        "L15_%=:\n"
        "ds_read_b32 v223, v78 offset:0\n"
        "s_waitcnt lgkmcnt(0)\n"
        "v_readfirstlane_b32 s40, v223\n"
        "s_nop 3\n"
        "s_cmp_ge_i32 s40, s68\n"
        "s_cbranch_scc1 L16_%=\n"
        "s_sleep 1\n"
        "s_sub_u32 s41, s41, 1\n"
        "s_cbranch_scc0 L15_%=\n"
        "s_endpgm\n"
        "L16_%=:\n"
        "s_nop 1\n"
        "s_branch L18_%=\n"
        "CGRP_%=:\n"
        "s_setprio 3\n"
        "s_waitcnt lgkmcnt(0)\n"
        "s_nop 6\n"
        "ds_read_u16 v215, v0 offset:26752\n"
        "s_sub_u32 s60, s73, 108096\n"
        "ds_read_u16 v253, v0 offset:26754\n"
        "ds_read_u16 v71, v0 offset:26756\n"
        "ds_read_u16 v231, v0 offset:26758\n"
        "ds_read_u16 v67, v0 offset:26764\n"
        "ds_read_u16 v233, v0 offset:26766\n"
        "s_waitcnt lgkmcnt(5)\n"
        "v_add_u32 v215, s60, v215\n"
        "v_subrev_u32 v214, 8320, v215\n"
        "v_min_u32 v215, v215, v214\n"
        "v_add_u32 v215, 107904, v215\n"
        "ds_read_b128 v[44:47], v215 offset:64\n"
        "ds_read_b128 v[28:31], v215 offset:128\n"
        "ds_read_b128 v[56:59], v215\n"
        "s_waitcnt lgkmcnt(1)\n"
        "s_waitcnt lgkmcnt(0)\n"
        "v_mfma_f32_16x16x32_bf16 v[180:183], v[92:95], v[56:59], 0\n"
        "v_add_u32 v253, s60, v253\n"
        "v_subrev_u32 v214, 8320, v253\n"
        "v_mfma_f32_16x16x32_bf16 v[180:183], v[96:99], v[44:47], v[180:183]\n"
        "v_min_u32 v253, v253, v214\n"
        "v_add_u32 v253, 107904, v253\n"
        "v_mfma_f32_16x16x32_bf16 v[180:183], v[100:103], v[28:31], v[180:183]\n"
        "v_add_u32 v71, s19, v71\n"
        "v_add_u32 v233, s19, v233\n"
        "v_add_u32 v231, s19, v231\n"
        "v_mfma_f32_16x16x32_bf16 v[188:191], v[104:107], v[56:59], 0\n"
        "v_mfma_f32_16x16x32_bf16 v[188:191], v[108:111], v[44:47], v[188:191]\n"
        "v_mfma_f32_16x16x32_bf16 v[188:191], v[112:115], v[28:31], v[188:191]\n"
        "v_mfma_f32_16x16x32_bf16 v[208:211], v[116:119], v[56:59], 0\n"
        "v_mfma_f32_16x16x32_bf16 v[208:211], v[120:123], v[44:47], v[208:211]\n"
        "v_cvt_pk_bf16_f32 v88, v180, v181\n"
        "v_cvt_pk_bf16_f32 v89, v182, v183\n"
        "v_mfma_f32_16x16x32_bf16 v[208:211], v[124:127], v[28:31], v[208:211]\n"
        "v_mfma_f32_16x16x32_bf16 v[212:215], v[128:131], v[56:59], 0\n"
        "v_mfma_f32_16x16x32_bf16 v[212:215], v[132:135], v[44:47], v[212:215]\n"
        "v_mfma_f32_16x16x32_bf16 v[212:215], v[136:139], v[28:31], v[212:215]\n"
        "v_cvt_pk_bf16_f32 v90, v188, v189\n"
        "v_cvt_pk_bf16_f32 v91, v190, v191\n"
        "v_mfma_f32_16x16x32_bf16 v[188:191], v[140:143], v[56:59], 0\n"
        "v_mfma_f32_16x16x32_bf16 v[188:191], v[144:147], v[44:47], v[188:191]\n"
        "v_mfma_f32_16x16x32_bf16 v[188:191], v[148:151], v[28:31], v[188:191]\n"
        "v_cvt_pk_bf16_f32 v180, v208, v209\n"
        "v_cvt_pk_bf16_f32 v181, v210, v211\n"
        "v_mfma_f32_16x16x32_bf16 v[208:211], v[152:155], v[56:59], 0\n"
        "v_cvt_pk_bf16_f32 v182, v212, v213\n"
        "v_cvt_pk_bf16_f32 v183, v214, v215\n"
        "v_mfma_f32_16x16x32_bf16 v[208:211], v[156:159], v[44:47], v[208:211]\n"
        "ds_read_b64 v[254:255], v67\n"
        "v_mfma_f32_16x16x32_bf16 v[208:211], v[160:163], v[28:31], v[208:211]\n"
        "ds_read_b64 v[64:65], v253\n"
        "ds_read_b64 v[66:67], v253 offset:16\n"
        "v_cvt_pk_bf16_f32 v212, v188, v189\n"
        "v_cvt_pk_bf16_f32 v213, v190, v191\n"
        "ds_write_b128 v71, v[88:91]\n"
        "ds_read_b64 v[194:195], v253 offset:32\n"
        "ds_write_b128 v71, v[180:183] offset:64\n"
        "s_nop 0\n"
        "v_cvt_pk_bf16_f32 v215, v210, v211\n"
        "v_cvt_pk_bf16_f32 v214, v208, v209\n"
        "v_mfma_f32_16x16x32_bf16 v[188:191], v[56:59], v[88:91], 0\n"
        "v_mfma_f32_16x16x32_bf16 v[188:191], v[44:47], v[180:183], v[188:191]\n"
        "v_mfma_f32_16x16x32_bf16 v[188:191], v[28:31], v[212:215], v[188:191]\n"
        "s_waitcnt lgkmcnt(0)\n"
        "ds_write_b128 v71, v[212:215] offset:128\n"
        "v_mfma_f32_4x4x4_16b_bf16 v[180:183], v[64:65], v[254:255], 0\n"
        "v_mfma_f32_4x4x4_16b_bf16 v[88:91], v[66:67], v[254:255], 0\n"
        "ds_read_b128 v[212:215], v0 offset:10288\n"
        "ds_read_b128 v[208:211], v0 offset:11312\n"
        "v_mfma_f32_4x4x4_16b_bf16 v[64:67], v[194:195], v[254:255], 0\n"
        "v_mfma_f32_16x16x32_bf16 v[204:207], v[56:59], v[164:167], 0\n"
        "s_mov_b32 vcc_lo, 0x22222222\n"
        "s_mov_b32 vcc_hi, 0\n"
        "v_cndmask_b32 v230, v188, v189, vcc\n"
        "v_mfma_f32_16x16x32_bf16 v[204:207], v[44:47], v[168:171], v[204:207]\n"
        "ds_read_b128 v[192:195], v0 offset:12352\n"
        "v_mfma_f32_16x16x32_bf16 v[204:207], v[28:31], v[172:175], v[204:207]\n"
        "ds_read_b128 v[200:203], v0 offset:13376\n"
        "ds_read_b128 v[196:199], v0 offset:14400\n"
        "s_mov_b32 vcc_lo, 0x44444444\n"
        "s_mov_b32 vcc_hi, 0\n"
        "v_cndmask_b32 v230, v230, v190, vcc\n"
        "s_waitcnt lgkmcnt(3)\n"
        "v_mfma_f32_16x16x32_bf16 v[68:71], v[56:59], v[176:179], 0\n"
        "s_mov_b32 vcc_lo, 0x88888888\n"
        "s_mov_b32 vcc_hi, 0\n"
        "v_cndmask_b32 v230, v230, v191, vcc\n"
        "v_mfma_f32_16x16x32_bf16 v[68:71], v[44:47], v[212:215], v[68:71]\n"
        "ds_read_b64 v[236:237], v253 offset:48\n"
        "v_cvt_pk_bf16_f32 v238, v204, v180\n"
        "v_mfma_f32_16x16x32_bf16 v[68:71], v[28:31], v[208:211], v[68:71]\n"
        "ds_read_b64 v[214:215], v253 offset:64\n"
        "v_cvt_pk_bf16_f32 v248, v205, v181\n"
        "v_cvt_pk_bf16_f32 v250, v206, v182\n"
        "v_cvt_pk_bf16_f32 v252, v207, v183\n"
        "s_waitcnt lgkmcnt(2)\n"
        "v_mfma_f32_16x16x32_bf16 v[188:191], v[56:59], v[192:195], 0\n"
        "ds_read_b64 v[234:235], v253 offset:80\n"
        "v_mfma_f32_16x16x32_bf16 v[188:191], v[44:47], v[200:203], v[188:191]\n"
        "ds_read_b128 v[192:195], v0 offset:15440\n"
        "v_cvt_pk_bf16_f32 v239, v68, v88\n"
        "v_mfma_f32_16x16x32_bf16 v[188:191], v[28:31], v[196:199], v[188:191]\n"
        "ds_read_b128 v[200:203], v0 offset:16464\n"
        "ds_read_b128 v[196:199], v0 offset:17488\n"
        "v_cvt_pk_bf16_f32 v249, v69, v89\n"
        "v_cvt_pk_bf16_f32 v251, v70, v90\n"
        "v_cvt_pk_bf16_f32 v253, v71, v91\n"
        "s_waitcnt lgkmcnt(4)\n"
        "v_mfma_f32_4x4x4_16b_bf16 v[180:183], v[236:237], v[254:255], 0\n"
        "v_mfma_f32_4x4x4_16b_bf16 v[88:91], v[214:215], v[254:255], 0\n"
        "ds_read_b128 v[204:207], v0 offset:18528\n"
        "ds_read_b128 v[208:211], v0 offset:19552\n"
        "ds_read_b128 v[212:215], v0 offset:20576\n"
        "s_waitcnt lgkmcnt(5)\n"
        "v_mfma_f32_4x4x4_16b_bf16 v[68:71], v[234:235], v[254:255], 0\n"
        "v_cvt_pk_bf16_f32 v236, v188, v64\n"
        "v_cvt_pk_bf16_f32 v254, v189, v65\n"
        "v_cvt_pk_bf16_f32 v234, v190, v66\n"
        "v_cvt_pk_bf16_f32 v232, v191, v67\n"
        "s_waitcnt lgkmcnt(3)\n"
        "v_mfma_f32_16x16x32_bf16 v[64:67], v[56:59], v[192:195], 0\n"
        "v_mfma_f32_16x16x32_bf16 v[64:67], v[44:47], v[200:203], v[64:67]\n"
        "ds_read_b128 v[188:191], v0 offset:21616\n"
        "v_mfma_f32_16x16x32_bf16 v[64:67], v[28:31], v[196:199], v[64:67]\n"
        "ds_read_b128 v[192:195], v0 offset:22640\n"
        "ds_read_b128 v[196:199], v0 offset:23664\n"
        "ds_write_b64 v231, v[238:239]\n"
        "s_waitcnt lgkmcnt(4)\n"
        "v_mfma_f32_16x16x32_bf16 v[200:203], v[56:59], v[204:207], 0\n"
        "v_mfma_f32_16x16x32_bf16 v[200:203], v[44:47], v[208:211], v[200:203]\n"
        "v_mfma_f32_16x16x32_bf16 v[200:203], v[28:31], v[212:215], v[200:203]\n"
        "s_mov_b64 exec, 0x00f0000f\n"
        "ds_write_b32 v233, v230\n"
        "s_mov_b64 exec, -1\n"
        "v_cvt_pk_bf16_f32 v237, v64, v180\n"
        "v_cvt_pk_bf16_f32 v255, v65, v181\n"
        "v_cvt_pk_bf16_f32 v235, v66, v182\n"
        "s_waitcnt lgkmcnt(1)\n"
        "v_mfma_f32_16x16x32_bf16 v[212:215], v[56:59], v[188:191], 0\n"
        "v_cvt_pk_bf16_f32 v233, v67, v183\n"
        "v_cvt_pk_bf16_f32 v58, v200, v88\n"
        "v_mfma_f32_16x16x32_bf16 v[212:215], v[44:47], v[192:195], v[212:215]\n"
        "v_cvt_pk_bf16_f32 v56, v201, v89\n"
        "v_cvt_pk_bf16_f32 v46, v202, v90\n"
        "v_mfma_f32_16x16x32_bf16 v[212:215], v[28:31], v[196:199], v[212:215]\n"
        "v_cvt_pk_bf16_f32 v44, v203, v91\n"
        "ds_write_b64 v231, v[248:249] offset:392\n"
        "ds_write_b64 v231, v[250:251] offset:784\n"
        "ds_write_b64 v231, v[252:253] offset:1176\n"
        "ds_write_b64 v231, v[236:237] offset:8\n"
        "ds_write_b64 v231, v[254:255] offset:400\n"
        "ds_write_b64 v231, v[234:235] offset:792\n"
        "ds_write_b64 v231, v[232:233] offset:1184\n"
        "v_cvt_pk_bf16_f32 v59, v212, v68\n"
        "v_cvt_pk_bf16_f32 v57, v213, v69\n"
        "v_cvt_pk_bf16_f32 v47, v214, v70\n"
        "v_cvt_pk_bf16_f32 v45, v215, v71\n"
        "ds_write_b64 v231, v[58:59] offset:16\n"
        "ds_write_b64 v231, v[56:57] offset:408\n"
        "ds_write_b64 v231, v[46:47] offset:800\n"
        "ds_write_b64 v231, v[44:45] offset:1192\n"
        "s_waitcnt lgkmcnt(0)\n"
        "s_nop 6\n"
        "s_setprio 0\n"
        "s_setpc_b64 s[64:65]\n"
        "L21_%=:\n"
        "s_nop 7\n"
        "s_nop 7\n"
        "L19_%=:\n"
        "ds_read_b32 v55, v19 offset:0\n"
        "s_waitcnt lgkmcnt(0)\n"
        "v_readfirstlane_b32 s40, v55\n"
        "s_nop 3\n"
        "s_cmp_ge_i32 s40, s68\n"
        "s_cbranch_scc1 L20_%=\n"
        "s_sleep 1\n"
        "s_sub_u32 s41, s41, 1\n"
        "s_cbranch_scc0 L19_%=\n"
        "s_endpgm\n"
        "L20_%=:\n"
        "s_nop 1\n"
        "s_branch L22_%=\n"
        "CREDO_%=:\n"
        "s_waitcnt lgkmcnt(0)\n"
        "s_nop 15\n"
        "s_mov_b32 s36, 0x20000000\n"
        "ds_read_b128 v[220:223], v3 offset:0\n"
        "ds_read_b128 v[184:187], v3 offset:3328\n"
        "ds_read_b128 v[52:55], v3 offset:64\n"
        "ds_read_b128 v[76:79], v3 offset:3392\n"
        "ds_read_b128 v[48:51], v3 offset:128\n"
        "ds_read_b128 v[240:243], v3 offset:3456\n"
        "s_waitcnt lgkmcnt(0)\n"
        "s_branch MTOP_%=\n"
        "MFULL_%=:\n"
        "s_call_b64 s[64:65], CGRP_%=\n"
        "MTOP_%=:\n"
        "s_waitcnt lgkmcnt(6)\n"
        "v_mfma_f32_16x16x32_bf16 v[196:199], v[92:95], v[220:223], 0\n"
        "v_cvt_pk_bf16_f32 v202, v80, v216\n"
        "v_cvt_pk_bf16_f32 v203, v84, v20\n"
        "v_cvt_pk_bf16_f32 v204, v32, v36\n"
        "v_cvt_pk_bf16_f32 v44, v81, v217\n"
        "v_cvt_pk_bf16_f32 v45, v85, v21\n"
        "s_waitcnt lgkmcnt(4)\n"
        "v_mfma_f32_16x16x32_bf16 v[196:199], v[96:99], v[52:55], v[196:199]\n"
        "v_cvt_pk_bf16_f32 v46, v33, v37\n"
        "v_cvt_pk_bf16_f32 v84, v82, v218\n"
        "v_cvt_pk_bf16_f32 v85, v86, v22\n"
        "v_cvt_pk_bf16_f32 v86, v34, v38\n"
        "v_cvt_pk_bf16_f32 v20, v83, v219\n"
        "s_waitcnt lgkmcnt(2)\n"
        "v_mfma_f32_16x16x32_bf16 v[196:199], v[100:103], v[48:51], v[196:199]\n"
        "v_cvt_pk_bf16_f32 v21, v87, v23\n"
        "v_cvt_pk_bf16_f32 v22, v35, v39\n"
        "v_cvt_pk_bf16_f32 v212, v40, v60\n"
        "v_cvt_pk_bf16_f32 v213, v72, v16\n"
        "v_cvt_pk_bf16_f32 v214, v24, v244\n"
        "s_waitcnt lgkmcnt(1)\n"
        "v_mfma_f32_16x16x32_bf16 v[36:39], v[100:103], v[240:243], 0\n"
        "v_cvt_pk_bf16_f32 v58, v41, v61\n"
        "v_cvt_pk_bf16_f32 v59, v73, v17\n"
        "v_cvt_pk_bf16_f32 v80, v196, v197\n"
        "v_cvt_pk_bf16_f32 v81, v198, v199\n"
        "v_cvt_pk_bf16_f32 v60, v25, v245\n"
        "v_mfma_f32_16x16x32_bf16 v[36:39], v[96:99], v[76:79], v[36:39]\n"
        "v_cvt_pk_bf16_f32 v16, v42, v62\n"
        "v_cvt_pk_bf16_f32 v17, v74, v18\n"
        "v_cvt_pk_bf16_f32 v18, v26, v246\n"
        "v_cvt_pk_bf16_f32 v230, v43, v63\n"
        "v_cvt_pk_bf16_f32 v231, v75, v19\n"
        "v_mfma_f32_16x16x32_bf16 v[36:39], v[92:95], v[184:187], v[36:39]\n"
        "s_add_u32 s37, s36, 196224\n"
        "v_cvt_pk_bf16_f32 v232, v27, v247\n"
        "v_mfma_f32_16x16x32_bf16 v[224:227], v[104:107], v[184:187], 0\n"
        "v_mfma_f32_16x16x32_bf16 v[224:227], v[108:111], v[76:79], v[224:227]\n"
        "v_mfma_f32_16x16x32_bf16 v[224:227], v[112:115], v[240:243], v[224:227]\n"
        "buffer_store_dwordx3 v[202:204], v8, s[8:11], s37 offen offset:384 sc0 sc1 nt\n"
        "v_mfma_f32_16x16x32_bf16 v[196:199], v[112:115], v[48:51], 0\n"
        "v_mfma_f32_16x16x32_bf16 v[196:199], v[108:111], v[52:55], v[196:199]\n"
        "s_add_u32 s37, s36, 196224\n"
        "v_cvt_pk_bf16_f32 v72, v36, v37\n"
        "v_cvt_pk_bf16_f32 v73, v38, v39\n"
        "v_mfma_f32_16x16x32_bf16 v[196:199], v[104:107], v[220:223], v[196:199]\n"
        "v_mfma_f32_16x16x32_bf16 v[36:39], v[116:119], v[220:223], 0\n"
        "v_cvt_pk_bf16_f32 v74, v224, v225\n"
        "v_cvt_pk_bf16_f32 v75, v226, v227\n"
        "v_mfma_f32_16x16x32_bf16 v[36:39], v[120:123], v[52:55], v[36:39]\n"
        "buffer_store_dwordx3 v[44:46], v9, s[8:11], s37 offen offset:1152 sc0 sc1 nt\n"
        "v_mfma_f32_16x16x32_bf16 v[36:39], v[124:127], v[48:51], v[36:39]\n"
        "v_mfma_f32_16x16x32_bf16 v[224:227], v[124:127], v[240:243], 0\n"
        "s_add_u32 s37, s36, 294336\n"
        "v_cvt_pk_bf16_f32 v82, v196, v197\n"
        "v_cvt_pk_bf16_f32 v83, v198, v199\n"
        "v_mfma_f32_16x16x32_bf16 v[224:227], v[120:123], v[76:79], v[224:227]\n"
        "v_mfma_f32_16x16x32_bf16 v[224:227], v[116:119], v[184:187], v[224:227]\n"
        "v_mfma_f32_16x16x32_bf16 v[44:47], v[128:131], v[184:187], 0\n"
        "buffer_store_dwordx3 v[84:86], v8, s[8:11], s37 offen offset:576 sc0 sc1 nt\n"
        "v_cvt_pk_bf16_f32 v196, v36, v37\n"
        "v_mfma_f32_16x16x32_bf16 v[44:47], v[132:135], v[76:79], v[44:47]\n"
        "v_cvt_pk_bf16_f32 v197, v38, v39\n"
        "v_mfma_f32_16x16x32_bf16 v[44:47], v[136:139], v[240:243], v[44:47]\n"
        "s_setprio 2\n"
        "s_add_u32 s37, s36, 392448\n"
        "v_cvt_pk_bf16_f32 v36, v224, v225\n"
        "v_cvt_pk_bf16_f32 v37, v226, v227\n"
        "v_mfma_f32_16x16x32_bf16 v[224:227], v[136:139], v[48:51], 0\n"
        "ds_read_b32 v229, v6\n"
        "s_mov_b32 exec_hi, 0xffff\n"
        "v_add_u32 v6, s69, v6\n"
        "s_mov_b32 exec_hi, -1\n"
        "v_cvt_pk_bf16_f32 v38, v44, v45\n"
        "v_mfma_f32_16x16x32_bf16 v[224:227], v[132:135], v[52:55], v[224:227]\n"
        "v_cvt_pk_bf16_f32 v39, v46, v47\n"
        "v_mfma_f32_16x16x32_bf16 v[224:227], v[128:131], v[220:223], v[224:227]\n"
        "buffer_store_dwordx3 v[20:22], v8, s[8:11], s37 offen sc0 sc1 nt\n"
        "v_mfma_f32_16x16x32_bf16 v[44:47], v[140:143], v[220:223], 0\n"
        "v_mfma_f32_16x16x32_bf16 v[44:47], v[144:147], v[52:55], v[44:47]\n"
        "s_add_u32 s37, s36, 392448\n"
        "s_waitcnt lgkmcnt(0)\n"
        "v_readlane_b32 s58, v229, 56\n"
        "v_mfma_f32_16x16x32_bf16 v[44:47], v[148:151], v[48:51], v[44:47]\n"
        "v_mfma_f32_16x16x32_bf16 v[20:23], v[148:151], v[240:243], 0\n"
        "v_cvt_pk_bf16_f32 v198, v224, v225\n"
        "v_cvt_pk_bf16_f32 v199, v226, v227\n"
        "v_mfma_f32_16x16x32_bf16 v[20:23], v[144:147], v[76:79], v[20:23]\n"
        "buffer_store_dwordx3 v[212:214], v11, s[8:11], s37 offen offset:768 sc0 sc1 nt\n"
        "v_mfma_f32_16x16x32_bf16 v[20:23], v[140:143], v[184:187], v[20:23]\n"
        "v_mfma_f32_16x16x32_bf16 v[224:227], v[152:155], v[184:187], 0\n"
        "s_add_u32 s37, s36, 490560\n"
        "v_cvt_pk_bf16_f32 v56, v44, v45\n"
        "v_cvt_pk_bf16_f32 v57, v46, v47\n"
        "ds_read_i16 v71, v0 offset:26760\n"
        "v_mfma_f32_16x16x32_bf16 v[224:227], v[156:159], v[76:79], v[224:227]\n"
        "ds_read_i16 v255, v0 offset:26762\n"
        "v_mfma_f32_16x16x32_bf16 v[224:227], v[160:163], v[240:243], v[224:227]\n"
        "v_cvt_pk_bf16_f32 v44, v20, v21\n"
        "v_cvt_pk_bf16_f32 v45, v22, v23\n"
        "v_mfma_f32_16x16x32_bf16 v[212:215], v[160:163], v[48:51], 0\n"
        "buffer_store_dwordx3 v[58:60], v8, s[8:11], s37 offen offset:192 sc0 sc1 nt\n"
        "v_mfma_f32_16x16x32_bf16 v[212:215], v[156:159], v[52:55], v[212:215]\n"
        "v_mfma_f32_16x16x32_bf16 v[212:215], v[152:155], v[220:223], v[212:215]\n"
        "s_add_u32 s37, s36, 490560\n"
        "s_waitcnt lgkmcnt(0)\n"
        "v_add_u32 v71, v5, v71\n"
        "v_cvt_pk_bf16_f32 v46, v224, v225\n"
        "v_cvt_pk_bf16_f32 v47, v226, v227\n"
        "v_add_u32 v255, v5, v255\n"
        "v_mfma_f32_16x16x32_bf16 v[224:227], v[220:223], v[80:83], 0\n"
        "ds_read_b128 v[20:23], v3 offset:6656\n"
        "v_cvt_pk_bf16_f32 v58, v212, v213\n"
        "v_cvt_pk_bf16_f32 v59, v214, v215\n"
        "v_mfma_f32_16x16x32_bf16 v[224:227], v[52:55], v[196:199], v[224:227]\n"
        "ds_read_b128 v[212:215], v3 offset:6720\n"
        "v_mfma_f32_16x16x32_bf16 v[224:227], v[48:51], v[56:59], v[224:227]\n"
        "ds_read_b128 v[206:209], v3 offset:6784\n"
        "v_add_u32 v3, s69, v3\n"
        "v_mfma_f32_16x16x32_bf16 v[244:247], v[240:243], v[56:59], 0\n"
        "ds_read_b128 v[60:63], v4 offset:128\n"
        "v_mfma_f32_16x16x32_bf16 v[244:247], v[76:79], v[196:199], v[244:247]\n"
        "ds_read_b128 v[40:43], v4 offset:64\n"
        "ds_read_b32 v188, v71\n"
        "v_mfma_f32_16x16x32_bf16 v[244:247], v[184:187], v[80:83], v[244:247]\n"
        "ds_read_b128 v[24:27], v4\n"
        "ds_read_b32 v189, v71 offset:832\n"
        "s_waitcnt lgkmcnt(6)\n"
        "v_mfma_f32_16x16x32_bf16 v[30:33], v[20:23], v[80:83], 0\n"
        "ds_read_b32 v190, v71 offset:1664\n"
        "ds_read_b32 v191, v71 offset:2496\n"
        "v_mfma_f32_16x16x32_bf16 v[30:33], v[212:215], v[196:199], v[30:33]\n"
        "s_waitcnt lgkmcnt(6)\n"
        "buffer_store_dwordx3 v[16:18], v10, s[8:11], s37 offen offset:960 sc0 sc1 nt\n"
        "s_add_u32 s37, s36, 588672\n"
        "v_mfma_f32_16x16x32_bf16 v[30:33], v[206:209], v[56:59], v[30:33]\n"
        "ds_read_b32 v192, v71 offset:3328\n"
        "s_waitcnt lgkmcnt(3)\n"
        "v_mul_f32 v224, v224, v188\n"
        "v_mul_f32 v225, v225, v189\n"
        "v_mfma_f32_16x16x32_bf16 v[16:19], v[60:63], v[56:59], 0\n"
        "ds_read_b32 v193, v71 offset:4160\n"
        "s_waitcnt lgkmcnt(2)\n"
        "v_mul_f32 v226, v226, v190\n"
        "v_mul_f32 v227, v227, v191\n"
        "v_max3_f32 v250, v224, v225, v226\n"
        "v_mfma_f32_16x16x32_bf16 v[16:19], v[40:43], v[196:199], v[16:19]\n"
        "ds_read_b32 v194, v71 offset:4992\n"
        "ds_read_b32 v195, v71 offset:5824\n"
        "v_mfma_f32_16x16x32_bf16 v[16:19], v[24:27], v[80:83], v[16:19]\n"
        "ds_read_b32 v196, v71 offset:6656\n"
        "s_waitcnt lgkmcnt(4)\n"
        "v_mul_f32 v244, v244, v192\n"
        "v_max3_f32 v250, v250, v227, v244\n"
        "v_mfma_f32_16x16x32_bf16 v[80:83], v[220:223], v[72:75], 0\n"
        "ds_read_b32 v197, v71 offset:7488\n"
        "s_waitcnt lgkmcnt(4)\n"
        "v_mul_f32 v245, v245, v193\n"
        "v_mul_f32 v16, s58, v16\n"
        "v_mfma_f32_16x16x32_bf16 v[80:83], v[52:55], v[36:39], v[80:83]\n"
        "ds_read_b32 v198, v71 offset:8320\n"
        "s_waitcnt lgkmcnt(4)\n"
        "v_mul_f32 v246, v246, v194\n"
        "s_waitcnt lgkmcnt(3)\n"
        "v_mul_f32 v247, v247, v195\n"
        "v_mfma_f32_16x16x32_bf16 v[80:83], v[48:51], v[44:47], v[80:83]\n"
        "ds_read_b32 v199, v71 offset:9152\n"
        "s_waitcnt lgkmcnt(3)\n"
        "v_mul_f32 v30, v30, v196\n"
        "v_max3_f32 v250, v250, v245, v246\n"
        "v_max3_f32 v250, v250, v247, v30\n"
        "v_mfma_f32_16x16x32_bf16 v[68:71], v[240:243], v[44:47], 0\n"
        "ds_read_b32 v18, v255\n"
        "s_waitcnt lgkmcnt(3)\n"
        "v_mul_f32 v31, v31, v197\n"
        "v_mul_f32 v80, v80, v188\n"
        "v_mul_f32 v81, v81, v189\n"
        "v_mfma_f32_16x16x32_bf16 v[68:71], v[76:79], v[36:39], v[68:71]\n"
        "ds_read_b32 v17, v255 offset:3328\n"
        "v_mul_f32 v82, v82, v190\n"
        "v_mul_f32 v83, v83, v191\n"
        "v_max3_f32 v249, v80, v81, v82\n"
        "v_mfma_f32_16x16x32_bf16 v[68:71], v[184:187], v[72:75], v[68:71]\n"
        "ds_read_b32 v90, v255 offset:6656\n"
        "s_waitcnt lgkmcnt(3)\n"
        "v_mul_f32 v32, v32, v198\n"
        "v_mul_f32 v33, v33, v199\n"
        "v_max3_f32 v250, v250, v31, v32\n"
        "v_mfma_f32_16x16x32_bf16 v[252:255], v[20:23], v[72:75], 0\n"
        "s_mov_b64 exec, s[48:49]\n"
        "v_mov_b32 v16, 0xff800000\n"
        "s_mov_b64 exec, -1\n"
        "v_mul_f32 v68, v68, v192\n"
        "v_mul_f32 v69, v69, v193\n"
        "v_mul_f32 v70, v70, v194\n"
        "v_mfma_f32_16x16x32_bf16 v[252:255], v[212:215], v[36:39], v[252:255]\n"
        "buffer_store_dwordx3 v[230:232], v8, s[8:11], s37 offen offset:384 sc0 sc1 nt\n"
        "v_max3_f32 v249, v249, v83, v68\n"
        "v_mfma_f32_16x16x32_bf16 v[252:255], v[206:209], v[44:47], v[252:255]\n"
        "v_mul_f32 v71, v71, v195\n"
        "v_max3_f32 v249, v249, v69, v70\n"
        "v_max3_f32 v250, v250, v33, v16\n"
        "v_mfma_f32_16x16x32_bf16 v[230:233], v[60:63], v[44:47], 0\n"
        "v_mfma_f32_16x16x32_bf16 v[230:233], v[40:43], v[36:39], v[230:233]\n"
        "v_mfma_f32_16x16x32_bf16 v[230:233], v[24:27], v[72:75], v[230:233]\n"
        "v_mfma_f32_16x16x32_bf16 v[36:39], v[92:95], v[20:23], 0\n"
        "v_mfma_f32_16x16x32_bf16 v[36:39], v[96:99], v[212:215], v[36:39]\n"
        "v_mul_f32 v252, v252, v196\n"
        "v_mul_f32 v253, v253, v197\n"
        "v_mul_f32 v254, v254, v198\n"
        "v_max3_f32 v249, v249, v71, v252\n"
        "v_mul_f32 v255, v255, v199\n"
        "v_mfma_f32_16x16x32_bf16 v[36:39], v[100:103], v[206:209], v[36:39]\n"
        "v_mul_f32 v230, s58, v230\n"
        "v_max3_f32 v249, v249, v253, v254\n"
        "s_mov_b64 exec, s[48:49]\n"
        "v_mov_b32 v230, 0xff800000\n"
        "s_mov_b64 exec, -1\n"
        "v_mfma_f32_16x16x32_bf16 v[72:75], v[112:115], v[206:209], 0\n"
        "v_max3_f32 v249, v249, v255, v230\n"
        "v_mfma_f32_16x16x32_bf16 v[72:75], v[108:111], v[212:215], v[72:75]\n"
        "v_cvt_pk_bf16_f32 v232, v36, v37\n"
        "v_cvt_pk_bf16_f32 v233, v38, v39\n"
        "v_permlane32_swap_b32 v250, v249\n"
        "v_max_f32 v250, v250, v249\n"
        "v_mfma_f32_16x16x32_bf16 v[72:75], v[104:107], v[20:23], v[72:75]\n"
        "v_mov_b32 v249, v250\n"
        "v_mfma_f32_16x16x32_bf16 v[36:39], v[116:119], v[20:23], 0\n"
        "v_mfma_f32_16x16x32_bf16 v[36:39], v[120:123], v[212:215], v[36:39]\n"
        "v_permlane16_swap_b32 v250, v249\n"
        "v_max_f32 v250, v250, v249\n"
        "v_mov_b32 v249, v250\n"
        "v_mfma_f32_16x16x32_bf16 v[36:39], v[124:127], v[206:209], v[36:39]\n"
        "v_mfma_f32_16x16x32_bf16 v[84:87], v[136:139], v[206:209], 0\n"
        "v_cvt_pk_bf16_f32 v234, v72, v73\n"
        "v_cvt_pk_bf16_f32 v235, v74, v75\n"
        "v_permlane32_swap_b32 v250, v249\n"
        "v_mfma_f32_16x16x32_bf16 v[84:87], v[132:135], v[212:215], v[84:87]\n"
        "s_waitcnt lgkmcnt(0)\n"
        "v_mul_f32 v250, v250, v18\n"
        "v_mul_f32 v249, v249, v17\n"
        "v_cvt_pk_bf16_f32 v64, v36, v37\n"
        "v_cvt_pk_bf16_f32 v65, v38, v39\n"
        "v_fma_f32 v224, v224, v18, -v250\n"
        "v_mfma_f32_16x16x32_bf16 v[84:87], v[128:131], v[20:23], v[84:87]\n"
        "v_fma_f32 v225, v225, v18, -v250\n"
        "v_fma_f32 v80, v80, v17, -v249\n"
        "v_fma_f32 v81, v81, v17, -v249\n"
        "v_exp_f32 v224, v224\n"
        "v_mfma_f32_16x16x32_bf16 v[56:59], v[140:143], v[20:23], 0\n"
        "v_exp_f32 v225, v225\n"
        "v_fma_f32 v226, v226, v18, -v250\n"
        "v_exp_f32 v80, v80\n"
        "v_mfma_f32_16x16x32_bf16 v[56:59], v[144:147], v[212:215], v[56:59]\n"
        "v_cvt_pk_bf16_f32 v66, v84, v85\n"
        "v_cvt_pk_bf16_f32 v67, v86, v87\n"
        "v_exp_f32 v81, v81\n"
        "v_fma_f32 v82, v82, v17, -v249\n"
        "v_mfma_f32_16x16x32_bf16 v[56:59], v[148:151], v[206:209], v[56:59]\n"
        "v_exp_f32 v226, v226\n"
        "v_fma_f32 v227, v227, v18, -v250\n"
        "v_exp_f32 v82, v82\n"
        "v_mfma_f32_16x16x32_bf16 v[216:219], v[160:163], v[206:209], 0\n"
        "v_fma_f32 v83, v83, v17, -v249\n"
        "v_add_f32 v19, v224, v225\n"
        "v_exp_f32 v227, v227\n"
        "v_fma_f32 v244, v244, v18, -v250\n"
        "v_mfma_f32_16x16x32_bf16 v[216:219], v[156:159], v[212:215], v[216:219]\n"
        "v_cvt_pk_bf16_f32 v84, v56, v57\n"
        "v_cvt_pk_bf16_f32 v85, v58, v59\n"
        "v_add_f32 v251, v80, v81\n"
        "v_exp_f32 v83, v83\n"
        "v_mfma_f32_16x16x32_bf16 v[216:219], v[152:155], v[20:23], v[216:219]\n"
        "v_fma_f32 v68, v68, v17, -v249\n"
        "v_add_f32 v19, v19, v226\n"
        "v_exp_f32 v244, v244\n"
        "v_fma_f32 v245, v245, v18, -v250\n"
        "v_mfma_f32_16x16x32_bf16 v[56:59], v[220:223], v[232:235], 0\n"
        "v_add_f32 v251, v251, v82\n"
        "v_exp_f32 v68, v68\n"
        "v_fma_f32 v69, v69, v17, -v249\n"
        "v_cvt_pk_bf16_f32 v86, v216, v217\n"
        "v_mfma_f32_16x16x32_bf16 v[56:59], v[52:55], v[64:67], v[56:59]\n"
        "v_cvt_pk_bf16_f32 v87, v218, v219\n"
        "v_add_f32 v19, v19, v227\n"
        "v_exp_f32 v245, v245\n"
        "v_fma_f32 v246, v246, v18, -v250\n"
        "v_mfma_f32_16x16x32_bf16 v[56:59], v[48:51], v[84:87], v[56:59]\n"
        "v_add_f32 v251, v251, v83\n"
        "v_exp_f32 v69, v69\n"
        "v_fma_f32 v70, v70, v17, -v249\n"
        "v_add_f32 v19, v19, v244\n"
        "v_mfma_f32_16x16x32_bf16 v[216:219], v[240:243], v[84:87], 0\n"
        "v_exp_f32 v246, v246\n"
        "v_fma_f32 v247, v247, v18, -v250\n"
        "v_add_f32 v251, v251, v68\n"
        "v_mul_f32 v56, v56, v188\n"
        "v_mfma_f32_16x16x32_bf16 v[216:219], v[76:79], v[64:67], v[216:219]\n"
        "v_mul_f32 v57, v57, v189\n"
        "v_mul_f32 v58, v58, v190\n"
        "v_mul_f32 v59, v59, v191\n"
        "v_max3_f32 v91, v56, v57, v58\n"
        "v_fma_f32 v71, v71, v17, -v249\n"
        "v_mfma_f32_16x16x32_bf16 v[216:219], v[184:187], v[232:235], v[216:219]\n"
        "v_exp_f32 v70, v70\n"
        "v_add_f32 v19, v19, v245\n"
        "v_exp_f32 v247, v247\n"
        "v_mfma_f32_16x16x32_bf16 v[44:47], v[20:23], v[232:235], 0\n"
        "ds_read_b128 v[72:75], v12\n"
        "v_fma_f32 v30, v30, v18, -v250\n"
        "v_add_f32 v251, v251, v69\n"
        "v_exp_f32 v71, v71\n"
        "v_mfma_f32_16x16x32_bf16 v[44:47], v[212:215], v[64:67], v[44:47]\n"
        "ds_read_b128 v[236:239], v12 offset:64\n"
        "v_mul_f32 v216, v216, v192\n"
        "v_mul_f32 v217, v217, v193\n"
        "v_mul_f32 v218, v218, v194\n"
        "v_max3_f32 v91, v91, v59, v216\n"
        "v_mfma_f32_16x16x32_bf16 v[44:47], v[206:209], v[84:87], v[44:47]\n"
        "ds_read_b128 v[180:183], v12 offset:128\n"
        "v_mul_f32 v219, v219, v195\n"
        "v_max3_f32 v91, v91, v217, v218\n"
        "v_fma_f32 v252, v252, v17, -v249\n"
        "v_add_f32 v19, v19, v246\n"
        "v_mfma_f32_16x16x32_bf16 v[34:37], v[60:63], v[84:87], 0\n"
        "v_exp_f32 v30, v30\n"
        "v_fma_f32 v31, v31, v18, -v250\n"
        "v_mul_f32 v44, v44, v196\n"
        "v_mul_f32 v45, v45, v197\n"
        "v_mfma_f32_16x16x32_bf16 v[34:37], v[40:43], v[64:67], v[34:37]\n"
        "v_mul_f32 v46, v46, v198\n"
        "v_max3_f32 v91, v91, v219, v44\n"
        "v_mul_f32 v47, v47, v199\n"
        "v_max3_f32 v91, v91, v45, v46\n"
        "v_add_f32 v251, v251, v70\n"
        "v_mfma_f32_16x16x32_bf16 v[34:37], v[24:27], v[232:235], v[34:37]\n"
        "s_waitcnt lgkmcnt(0)\n"
        "v_exp_f32 v252, v252\n"
        "v_fma_f32 v253, v253, v17, -v249\n"
        "v_add_f32 v19, v19, v247\n"
        "v_fma_f32 v32, v32, v18, -v250\n"
        "v_mfma_f32_16x16x32_bf16 v[24:27], v[72:75], v[220:223], 0\n"
        "v_exp_f32 v31, v31\n"
        "v_add_f32 v251, v251, v71\n"
        "v_mul_f32 v34, s58, v34\n"
        "v_fma_f32 v254, v254, v17, -v249\n"
        "v_mfma_f32_16x16x32_bf16 v[24:27], v[236:239], v[52:55], v[24:27]\n"
        "ds_read_b32 v35, v7 offset:192\n"
        "v_exp_f32 v253, v253\n"
        "v_add_f32 v19, v19, v30\n"
        "v_fma_f32 v33, v33, v18, -v250\n"
        "v_mfma_f32_16x16x32_bf16 v[24:27], v[180:183], v[48:51], v[24:27]\n"
        "s_mov_b64 exec, s[48:49]\n"
        "v_mov_b32 v34, 0xff800000\n"
        "s_mov_b64 exec, -1\n"
        "v_max3_f32 v91, v91, v47, v34\n"
        "v_exp_f32 v32, v32\n"
        "v_mfma_f32_16x16x32_bf16 v[36:39], v[180:183], v[240:243], 0\n"
        "v_add_f32 v251, v251, v252\n"
        "v_exp_f32 v254, v254\n"
        "v_fma_f32 v255, v255, v17, -v249\n"
        "v_add_f32 v19, v19, v31\n"
        "v_mfma_f32_16x16x32_bf16 v[36:39], v[236:239], v[76:79], v[36:39]\n"
        "v_exp_f32 v33, v33\n"
        "v_fma_f32 v16, v16, v18, -v250\n"
        "v_add_f32 v251, v251, v253\n"
        "v_fma_f32 v230, v230, v17, -v249\n"
        "v_mfma_f32_16x16x32_bf16 v[36:39], v[72:75], v[184:187], v[36:39]\n"
        "v_exp_f32 v255, v255\n"
        "v_add_f32 v19, v19, v32\n"
        "v_exp_f32 v16, v16\n"
        "v_mfma_f32_16x16x32_bf16 v[232:235], v[72:75], v[20:23], 0\n"
        "v_add_f32 v251, v251, v254\n"
        "v_exp_f32 v230, v230\n"
        "v_add_f32 v19, v19, v33\n"
        "v_add_f32 v251, v251, v255\n"
        "v_mfma_f32_16x16x32_bf16 v[232:235], v[236:239], v[212:215], v[232:235]\n"
        "v_cndmask_b32_e64 v250, v24, v36, s[50:51]\n"
        "v_add_f32 v19, v19, v16\n"
        "v_add_f32 v251, v251, v230\n"
        "v_mul_f32 v224, v224, v188\n"
        "v_mul_f32 v225, v225, v189\n"
        "v_mfma_f32_16x16x32_bf16 v[232:235], v[180:183], v[206:209], v[232:235]\n"
        "v_permlane32_swap_b32 v19, v251\n"
        "v_add_f32 v19, v19, v251\n"
        "v_mov_b32 v215, v19\n"
        "v_mul_f32 v226, v226, v190\n"
        "v_mfma_f32_16x16x32_bf16 v[22:25], v[220:223], v[164:167], 0\n"
        "ds_read_b128 v[206:209], v13\n"
        "v_permlane16_swap_b32 v19, v215\n"
        "v_add_f32 v19, v19, v215\n"
        "v_cndmask_b32_e64 v250, v250, v232, s[52:53]\n"
        "v_mfma_f32_16x16x32_bf16 v[22:25], v[52:55], v[168:171], v[22:25]\n"
        "ds_read_b128 v[212:215], v13 offset:64\n"
        "s_waitcnt lgkmcnt(2)\n"
        "v_cndmask_b32_e64 v250, v250, v35, s[54:55]\n"
        "v_mul_f32 v250, v250, v229\n"
        "v_mul_f32 v227, v227, v191\n"
        "v_mfma_f32_16x16x32_bf16 v[22:25], v[48:51], v[172:175], v[22:25]\n"
        "ds_read_b128 v[236:239], v13 offset:128\n"
        "v_rcp_f32 v19, v19\n"
        "v_mul_f32 v244, v244, v192\n"
        "v_mov_b32 v251, v19\n"
        "v_mfma_f32_16x16x32_bf16 v[36:39], v[240:243], v[172:175], 0\n"
        "ds_read_b128 v[232:235], v0 offset:11312\n"
        "v_permlane32_swap_b32 v19, v251\n"
        "v_mul_f32 v245, v245, v193\n"
        "v_cvt_pk_bf16_f32 v180, v22, v23\n"
        "v_mfma_f32_16x16x32_bf16 v[36:39], v[76:79], v[168:171], v[36:39]\n"
        "ds_read_b128 v[20:23], v0 offset:10288\n"
        "v_cvt_pk_bf16_f32 v181, v24, v25\n"
        "v_mul_f32 v246, v246, v194\n"
        "v_mul_f32 v247, v247, v195\n"
        "v_mul_f32 v224, v224, v19\n"
        "v_mfma_f32_16x16x32_bf16 v[36:39], v[184:187], v[164:167], v[36:39]\n"
        "s_waitcnt lgkmcnt(2)\n"
        "v_mul_f32 v225, v225, v19\n"
        "v_mul_f32 v226, v226, v19\n"
        "v_mul_f32 v227, v227, v19\n"
        "v_mul_f32 v244, v244, v19\n"
        "v_mul_f32 v245, v245, v19\n"
        "v_mfma_f32_16x16x32_bf16 v[24:27], v[206:209], v[164:167], 0\n"
        "s_mov_b64 exec, s[56:57]\n"
        "v_mov_b32 v250, 0xff800000\n"
        "s_mov_b64 exec, -1\n"
        "v_cvt_pk_bf16_f32 v182, v36, v37\n"
        "v_cvt_pk_bf16_f32 v183, v38, v39\n"
        "v_max_f32_dpp v67, v250, v250 row_ror:8 row_mask:0xf bank_mask:0xf\n"
        "v_mfma_f32_16x16x32_bf16 v[24:27], v[212:215], v[168:171], v[24:27]\n"
        "v_mul_f32 v246, v246, v19\n"
        "v_max_f32_dpp v67, v67, v67 row_ror:4 row_mask:0xf bank_mask:0xf\n"
        "v_mul_f32 v247, v247, v19\n"
        "v_cvt_pk_bf16_f32 v36, v224, v225\n"
        "v_max_f32_dpp v67, v67, v67 row_ror:2 row_mask:0xf bank_mask:0xf\n"
        "v_mfma_f32_16x16x32_bf16 v[24:27], v[236:239], v[172:175], v[24:27]\n"
        "s_waitcnt lgkmcnt(0)\n"
        "v_max_f32_dpp v67, v67, v67 row_ror:1 row_mask:0xf bank_mask:0xf\n"
        "v_cvt_pk_bf16_f32 v37, v226, v227\n"
        "v_cvt_pk_bf16_f32 v38, v244, v245\n"
        "v_permlane32_swap_b32 v91, v67\n"
        "v_mfma_f32_16x16x32_bf16 v[40:43], v[236:239], v[232:235], 0\n"
        "v_max_f32 v91, v91, v67\n"
        "v_mov_b32 v67, v91\n"
        "v_cvt_pk_bf16_f32 v224, v24, v25\n"
        "v_cvt_pk_bf16_f32 v228, v26, v27\n"
        "v_cvt_pk_bf16_f32 v39, v246, v247\n"
        "v_mfma_f32_16x16x32_bf16 v[40:43], v[212:215], v[20:23], v[40:43]\n"
        "v_permlane16_swap_b32 v91, v67\n"
        "v_max_f32 v91, v91, v67\n"
        "v_mov_b32 v67, v91\n"
        "v_mul_f32 v30, v30, v196\n"
        "v_mfma_f32_16x16x32_bf16 v[40:43], v[206:209], v[176:179], v[40:43]\n"
        "v_permlane32_swap_b32 v91, v67\n"
        "v_mul_f32 v91, v91, v90\n"
        "v_fma_f32 v56, v56, v90, -v91\n"
        "v_fma_f32 v57, v57, v90, -v91\n"
        "v_mfma_f32_16x16x32_bf16 v[60:63], v[184:187], v[176:179], 0\n"
        "ds_read_b128 v[24:27], v0 offset:12352\n"
        "v_exp_f32 v56, v56\n"
        "v_exp_f32 v57, v57\n"
        "v_mfma_f32_16x16x32_bf16 v[60:63], v[76:79], v[20:23], v[60:63]\n"
        "ds_read_b128 v[200:203], v0 offset:13376\n"
        "v_cvt_pk_bf16_f32 v244, v40, v41\n"
        "v_cvt_pk_bf16_f32 v248, v42, v43\n"
        "v_fma_f32 v58, v58, v90, -v91\n"
        "v_fma_f32 v59, v59, v90, -v91\n"
        "v_mfma_f32_16x16x32_bf16 v[60:63], v[240:243], v[232:235], v[60:63]\n"
        "ds_read_b128 v[40:43], v0 offset:14400\n"
        "v_exp_f32 v58, v58\n"
        "v_add_f32 v211, v56, v57\n"
        "v_fma_f32 v216, v216, v90, -v91\n"
        "v_mfma_f32_16x16x32_bf16 v[84:87], v[48:51], v[232:235], 0\n"
        "v_exp_f32 v59, v59\n"
        "v_add_f32 v211, v211, v58\n"
        "v_exp_f32 v216, v216\n"
        "v_mfma_f32_16x16x32_bf16 v[84:87], v[52:55], v[20:23], v[84:87]\n"
        "v_cvt_pk_bf16_f32 v234, v60, v61\n"
        "v_cvt_pk_bf16_f32 v235, v62, v63\n"
        "v_fma_f32 v217, v217, v90, -v91\n"
        "v_add_f32 v211, v211, v59\n"
        "v_fma_f32 v218, v218, v90, -v91\n"
        "v_mfma_f32_16x16x32_bf16 v[84:87], v[220:223], v[176:179], v[84:87]\n"
        "s_waitcnt lgkmcnt(0)\n"
        "v_exp_f32 v217, v217\n"
        "v_add_f32 v211, v211, v216\n"
        "v_exp_f32 v218, v218\n"
        "v_mfma_f32_16x16x32_bf16 v[60:63], v[220:223], v[24:27], 0\n"
        "v_fma_f32 v219, v219, v90, -v91\n"
        "v_sub_f32 v250, v250, v67\n"
        "v_add_f32 v211, v211, v217\n"
        "v_cvt_pk_bf16_f32 v232, v84, v85\n"
        "v_cvt_pk_bf16_f32 v233, v86, v87\n"
        "v_mfma_f32_16x16x32_bf16 v[60:63], v[52:55], v[200:203], v[60:63]\n"
        "v_exp_f32 v219, v219\n"
        "v_fma_f32 v44, v44, v90, -v91\n"
        "v_mul_f32 v250, s58, v250\n"
        "v_add_f32 v211, v211, v218\n"
        "v_mfma_f32_16x16x32_bf16 v[60:63], v[48:51], v[40:43], v[60:63]\n"
        "v_exp_f32 v44, v44\n"
        "v_fma_f32 v45, v45, v90, -v91\n"
        "v_exp_f32 v250, v250\n"
        "v_mfma_f32_16x16x32_bf16 v[84:87], v[240:243], v[40:43], 0\n"
        "ds_read_b128 v[64:67], v0 offset:17488\n"
        "v_add_f32 v211, v211, v219\n"
        "v_exp_f32 v45, v45\n"
        "v_fma_f32 v46, v46, v90, -v91\n"
        "v_mfma_f32_16x16x32_bf16 v[84:87], v[76:79], v[200:203], v[84:87]\n"
        "ds_read_b128 v[20:23], v0 offset:16464\n"
        "v_cvt_pk_bf16_f32 v88, v60, v61\n"
        "v_cvt_pk_bf16_f32 v89, v62, v63\n"
        "v_add_f32 v211, v211, v44\n"
        "v_fma_f32 v47, v47, v90, -v91\n"
        "v_mfma_f32_16x16x32_bf16 v[84:87], v[184:187], v[24:27], v[84:87]\n"
        "ds_read_b128 v[60:63], v0 offset:15440\n"
        "v_exp_f32 v46, v46\n"
        "v_add_f32_dpp v249, v250, v250 row_ror:8 row_mask:0xf bank_mask:0xf\n"
        "v_add_f32 v211, v211, v45\n"
        "v_mfma_f32_16x16x32_bf16 v[72:75], v[206:209], v[24:27], 0\n"
        "v_exp_f32 v47, v47\n"
        "v_fma_f32 v34, v34, v90, -v91\n"
        "v_add_f32_dpp v249, v249, v249 row_ror:4 row_mask:0xf bank_mask:0xf\n"
        "v_cvt_pk_bf16_f32 v90, v84, v85\n"
        "v_mfma_f32_16x16x32_bf16 v[72:75], v[212:215], v[200:203], v[72:75]\n"
        "v_cvt_pk_bf16_f32 v91, v86, v87\n"
        "v_add_f32 v211, v211, v46\n"
        "v_exp_f32 v34, v34\n"
        "v_add_f32_dpp v249, v249, v249 row_ror:2 row_mask:0xf bank_mask:0xf\n"
        "v_mfma_f32_16x16x32_bf16 v[72:75], v[236:239], v[40:43], v[72:75]\n"
        "s_waitcnt lgkmcnt(0)\n"
        "v_add_f32 v211, v211, v47\n"
        "v_mul_f32 v31, v31, v197\n"
        "v_mul_f32 v32, v32, v198\n"
        "v_mul_f32 v33, v33, v199\n"
        "v_mul_f32 v16, s58, v16\n"
        "v_mfma_f32_16x16x32_bf16 v[84:87], v[236:239], v[64:67], 0\n"
        "v_add_f32_dpp v249, v249, v249 row_ror:1 row_mask:0xf bank_mask:0xf\n"
        "v_cvt_pk_bf16_f32 v200, v72, v73\n"
        "v_cvt_pk_bf16_f32 v204, v74, v75\n"
        "v_add_f32 v211, v211, v34\n"
        "v_mul_f32 v30, v30, v19\n"
        "v_mfma_f32_16x16x32_bf16 v[84:87], v[212:215], v[20:23], v[84:87]\n"
        "v_mul_f32 v31, v31, v19\n"
        "v_mul_f32 v32, v32, v19\n"
        "v_mul_f32 v33, v33, v19\n"
        "v_fma_f32 v16, v16, v19, v15\n"
        "v_cvt_pk_bf16_f32 v40, v30, v31\n"
        "v_mfma_f32_16x16x32_bf16 v[84:87], v[206:209], v[60:63], v[84:87]\n"
        "v_permlane32_swap_b32 v211, v249\n"
        "v_cvt_pk_bf16_f32 v42, v32, v33\n"
        "v_cvt_pk_bf16_f32 v41, v16, 0\n"
        "v_add_f32 v211, v211, v249\n"
        "v_mfma_f32_16x16x32_bf16 v[24:27], v[184:187], v[60:63], 0\n"
        "ds_read_b128 v[30:33], v0 offset:18528\n"
        "v_mov_b32 v249, v211\n"
        "v_mul_f32 v250, v250, v229\n"
        "v_cvt_pk_bf16_f32 v225, v84, v85\n"
        "v_cvt_pk_bf16_f32 v229, v86, v87\n"
        "v_mfma_f32_16x16x32_bf16 v[24:27], v[76:79], v[20:23], v[24:27]\n"
        "ds_read_b128 v[16:19], v0 offset:19552\n"
        "v_permlane16_swap_b32 v211, v249\n"
        "v_add_f32 v211, v211, v249\n"
        "v_mul_f32 v80, v80, v188\n"
        "v_mfma_f32_16x16x32_bf16 v[24:27], v[240:243], v[64:67], v[24:27]\n"
        "ds_read_b128 v[84:87], v0 offset:20576\n"
        "v_rcp_f32 v211, v211\n"
        "v_mul_f32 v81, v81, v189\n"
        "v_mov_b32 v249, v211\n"
        "v_mfma_f32_16x16x32_bf16 v[72:75], v[48:51], v[64:67], 0\n"
        "v_mul_f32 v82, v82, v190\n"
        "v_permlane32_swap_b32 v211, v249\n"
        "v_mul_f32 v250, v250, v249\n"
        "v_cvt_pk_bf16_f32 v66, v24, v25\n"
        "v_mfma_f32_16x16x32_bf16 v[72:75], v[52:55], v[20:23], v[72:75]\n"
        "v_cvt_pk_bf16_f32 v67, v26, v27\n"
        "v_cvt_pk_bf16_f32 v250, v250, 0\n"
        "v_mul_f32 v83, v83, v191\n"
        "v_mul_f32 v68, v68, v192\n"
        "v_mul_f32 v69, v69, v193\n"
        "v_mfma_f32_16x16x32_bf16 v[72:75], v[220:223], v[60:63], v[72:75]\n"
        "s_waitcnt lgkmcnt(0)\n"
        "ds_write_b16 v1, v250\n"
        "v_mul_f32 v70, v70, v194\n"
        "v_mul_f32 v71, v71, v195\n"
        "v_mfma_f32_16x16x32_bf16 v[20:23], v[220:223], v[30:33], 0\n"
        "v_mul_f32 v80, v80, v251\n"
        "v_mul_f32 v81, v81, v251\n"
        "v_mul_f32 v82, v82, v251\n"
        "v_cvt_pk_bf16_f32 v64, v72, v73\n"
        "v_cvt_pk_bf16_f32 v65, v74, v75\n"
        "v_mfma_f32_16x16x32_bf16 v[20:23], v[52:55], v[16:19], v[20:23]\n"
        "v_mul_f32 v83, v83, v251\n"
        "v_mul_f32 v68, v68, v251\n"
        "v_mul_f32 v69, v69, v251\n"
        "v_mul_f32 v70, v70, v251\n"
        "v_mul_f32 v71, v71, v251\n"
        "v_mfma_f32_16x16x32_bf16 v[20:23], v[48:51], v[84:87], v[20:23]\n"
        "v_cvt_pk_bf16_f32 v60, v80, v81\n"
        "v_cvt_pk_bf16_f32 v61, v82, v83\n"
        "v_cvt_pk_bf16_f32 v62, v68, v69\n"
        "v_cvt_pk_bf16_f32 v63, v70, v71\n"
        "v_mul_f32 v252, v252, v196\n"
        "v_mfma_f32_16x16x32_bf16 v[80:83], v[240:243], v[84:87], 0\n"
        "ds_read_b128 v[68:71], v0 offset:23664\n"
        "v_mul_f32 v253, v253, v197\n"
        "v_cvt_pk_bf16_f32 v28, v20, v21\n"
        "v_cvt_pk_bf16_f32 v29, v22, v23\n"
        "v_mul_f32 v254, v254, v198\n"
        "v_mfma_f32_16x16x32_bf16 v[80:83], v[76:79], v[16:19], v[80:83]\n"
        "ds_read_b128 v[20:23], v0 offset:22640\n"
        "v_mul_f32 v255, v255, v199\n"
        "v_mul_f32 v230, s58, v230\n"
        "v_mul_f32 v252, v252, v251\n"
        "v_mul_f32 v253, v253, v251\n"
        "v_mfma_f32_16x16x32_bf16 v[80:83], v[184:187], v[30:33], v[80:83]\n"
        "ds_read_b128 v[24:27], v0 offset:21616\n"
        "v_mul_f32 v254, v254, v251\n"
        "v_mul_f32 v255, v255, v251\n"
        "v_fma_f32 v230, v230, v251, v15\n"
        "v_cvt_pk_bf16_f32 v72, v252, v253\n"
        "v_mfma_f32_16x16x32_bf16 v[250:253], v[206:209], v[30:33], 0\n"
        "v_cvt_pk_bf16_f32 v74, v254, v255\n"
        "v_cvt_pk_bf16_f32 v73, v230, 0\n"
        "v_cvt_pk_bf16_f32 v30, v80, v81\n"
        "v_cvt_pk_bf16_f32 v31, v82, v83\n"
        "v_mul_f32 v56, v56, v188\n"
        "v_mfma_f32_16x16x32_bf16 v[250:253], v[212:215], v[16:19], v[250:253]\n"
        "v_mul_f32 v57, v57, v189\n"
        "v_mul_f32 v58, v58, v190\n"
        "v_mul_f32 v59, v59, v191\n"
        "v_mul_f32 v216, v216, v192\n"
        "v_mul_f32 v217, v217, v193\n"
        "v_mfma_f32_16x16x32_bf16 v[250:253], v[236:239], v[84:87], v[250:253]\n"
        "s_waitcnt lgkmcnt(0)\n"
        "v_mul_f32 v218, v218, v194\n"
        "v_mul_f32 v219, v219, v195\n"
        "v_mul_f32 v56, v56, v211\n"
        "v_mul_f32 v57, v57, v211\n"
        "v_mul_f32 v58, v58, v211\n"
        "v_mfma_f32_16x16x32_bf16 v[84:87], v[236:239], v[68:71], 0\n"
        "ds_read2_b32 v[230:231], v7 offset0:0 offset1:3\n"
        "v_cvt_pk_bf16_f32 v245, v250, v251\n"
        "v_cvt_pk_bf16_f32 v249, v252, v253\n"
        "v_mul_f32 v59, v59, v211\n"
        "v_mul_f32 v216, v216, v211\n"
        "v_mfma_f32_16x16x32_bf16 v[84:87], v[212:215], v[20:23], v[84:87]\n"
        "ds_read_b64_tr_b16 v[226:227], v5 offset:0\n"
        "v_mul_f32 v217, v217, v211\n"
        "v_mul_f32 v218, v218, v211\n"
        "v_mul_f32 v219, v219, v211\n"
        "v_cvt_pk_bf16_f32 v212, v56, v57\n"
        "v_mfma_f32_16x16x32_bf16 v[84:87], v[206:209], v[24:27], v[84:87]\n"
        "ds_read2_b32 v[250:251], v7 offset0:1 offset1:4\n"
        "v_mov_b32_dpp v43, v15 row_shr:4 row_mask:0xf bank_mask:0xf bound_ctrl:0\n"
        "v_cvt_pk_bf16_f32 v213, v58, v59\n"
        "v_cvt_pk_bf16_f32 v214, v216, v217\n"
        "v_cvt_pk_bf16_f32 v215, v218, v219\n"
        "v_mfma_f32_16x16x32_bf16 v[216:219], v[184:187], v[24:27], 0\n"
        "ds_read_b64_tr_b16 v[246:247], v5 offset:16\n"
        "v_mul_f32 v44, v44, v196\n"
        "v_cvt_pk_bf16_f32 v201, v84, v85\n"
        "v_cvt_pk_bf16_f32 v205, v86, v87\n"
        "v_mul_f32 v45, v45, v197\n"
        "v_mfma_f32_16x16x32_bf16 v[216:219], v[76:79], v[20:23], v[216:219]\n"
        "ds_read2_b32 v[206:207], v7 offset0:2 offset1:5\n"
        "v_mul_f32 v46, v46, v198\n"
        "v_mul_f32 v47, v47, v199\n"
        "v_mul_f32 v34, s58, v34\n"
        "v_mul_f32 v44, v44, v211\n"
        "v_mfma_f32_16x16x32_bf16 v[216:219], v[240:243], v[68:71], v[216:219]\n"
        "ds_read_b64_tr_b16 v[202:203], v5 offset:32\n"
        "v_mul_f32 v45, v45, v211\n"
        "v_mul_f32 v46, v46, v211\n"
        "v_mul_f32 v47, v47, v211\n"
        "v_fma_f32 v34, v34, v211, v15\n"
        "v_mfma_f32_16x16x32_bf16 v[240:243], v[48:51], v[68:71], 0\n"
        "v_cvt_pk_bf16_f32 v196, v44, v45\n"
        "v_cvt_pk_bf16_f32 v198, v46, v47\n"
        "v_cvt_pk_bf16_f32 v46, v216, v217\n"
        "v_cvt_pk_bf16_f32 v47, v218, v219\n"
        "v_cvt_pk_bf16_f32 v197, v34, 0\n"
        "v_mfma_f32_16x16x32_bf16 v[240:243], v[52:55], v[20:23], v[240:243]\n"
        "v_add_u32 v13, s69, v13\n"
        "v_mfma_f32_16x16x32_bf16 v[240:243], v[220:223], v[24:27], v[240:243]\n"
        "v_mfma_f32_16x16x32_bf16 v[216:219], v[36:39], v[180:183], 0\n"
        "s_waitcnt lgkmcnt(0)\n"
        "v_smfmac_f32_16x16x64_bf16 v[216:219], v[40:43], v[224:231], v14\n"
        "ds_read_b64_tr_b16 v[226:227], v5 offset:48\n"
        "v_mfma_f32_16x16x32_bf16 v[20:23], v[36:39], v[232:235], 0\n"
        "v_smfmac_f32_16x16x64_bf16 v[20:23], v[40:43], v[244:251], v14\n"
        "ds_read_b64_tr_b16 v[246:247], v5 offset:64\n"
        "v_mfma_f32_16x16x32_bf16 v[24:27], v[36:39], v[88:91], 0\n"
        "s_add_u32 s31, s31, 1\n"
        "v_cvt_pk_bf16_f32 v44, v240, v241\n"
        "v_cvt_pk_bf16_f32 v45, v242, v243\n"
        "v_smfmac_f32_16x16x64_bf16 v[24:27], v[40:43], v[200:207], v14\n"
        "ds_read_b64_tr_b16 v[202:203], v5 offset:80\n"
        "s_mul_i32 s68, s31, 3\n"
        "v_cvt_pk_bf16_f32 v186, v216, v20\n"
        "v_cvt_pk_bf16_f32 v34, v217, v21\n"
        "v_cvt_pk_bf16_f32 v54, v218, v22\n"
        "v_mfma_f32_16x16x32_bf16 v[240:243], v[36:39], v[64:67], 0\n"
        "ds_read_b128 v[80:83], v2\n"
        "v_cvt_pk_bf16_f32 v220, v219, v23\n"
        "s_lshr_b32 s38, s31, 3\n"
        "s_waitcnt lgkmcnt(2)\n"
        "v_smfmac_f32_16x16x64_bf16 v[240:243], v[40:43], v[224:231], v14 abid:1\n"
        "ds_read_b128 v[216:219], v2 offset:64\n"
        "s_add_u32 s38, s38, 11\n"
        "v_mfma_f32_16x16x32_bf16 v[16:19], v[36:39], v[28:31], 0\n"
        "s_bitcmp1_b32 s31, 0\n"
        "v_smfmac_f32_16x16x64_bf16 v[16:19], v[40:43], v[244:251], v14 abid:1\n"
        "s_cselect_b32 s38, 3, s38\n"
        "v_mfma_f32_16x16x32_bf16 v[20:23], v[36:39], v[44:47], 0\n"
        "s_waitcnt lgkmcnt(1)\n"
        "v_cvt_pk_bf16_f32 v187, v24, v240\n"
        "v_cvt_pk_bf16_f32 v35, v25, v241\n"
        "v_cvt_pk_bf16_f32 v55, v26, v242\n"
        "v_cvt_pk_bf16_f32 v221, v27, v243\n"
        "s_add_u32 s68, s68, s38\n"
        "v_smfmac_f32_16x16x64_bf16 v[20:23], v[40:43], v[200:207], v14 abid:1\n"
        "s_cmp_lt_u32 s31, s14\n"
        "v_mfma_f32_16x16x32_bf16 v[40:43], v[80:83], v[180:183], 0\n"
        "s_waitcnt lgkmcnt(0)\n"
        "s_cselect_b32 s68, s68, 0\n"
        "v_smfmac_f32_16x16x64_bf16 v[40:43], v[216:219], v[224:231], v14\n"
        "s_and_b32 s38, s31, 7\n"
        "v_mfma_f32_16x16x32_bf16 v[240:243], v[80:83], v[232:235], 0\n"
        "s_cselect_b32 s30, 1, -7\n"
        "v_cvt_pk_bf16_f32 v188, v16, v20\n"
        "v_cvt_pk_bf16_f32 v36, v17, v21\n"
        "v_cvt_pk_bf16_f32 v56, v18, v22\n"
        "v_cvt_pk_bf16_f32 v222, v19, v23\n"
        "v_smfmac_f32_16x16x64_bf16 v[240:243], v[216:219], v[244:251], v14\n"
        "s_mul_i32 s29, s30, 192\n"
        "v_mfma_f32_16x16x32_bf16 v[16:19], v[80:83], v[88:91], 0\n"
        "s_mul_i32 s30, s30, 392\n"
        "v_smfmac_f32_16x16x64_bf16 v[16:19], v[216:219], v[200:207], v14\n"
        "s_add_u32 s37, s35, 0\n"
        "s_cmp_eq_u32 s73, s18\n"
        "v_mov_b32_dpp v75, v15 row_shr:4 row_mask:0xf bank_mask:0xf bound_ctrl:0\n"
        "v_mfma_f32_16x16x32_bf16 v[84:87], v[80:83], v[64:67], 0\n"
        "s_cselect_b32 s34, -7488, s75\n"
        "v_cvt_pk_bf16_f32 v184, v40, v240\n"
        "v_smfmac_f32_16x16x64_bf16 v[84:87], v[216:219], v[224:231], v14 abid:1\n"
        "ds_read_b64_tr_b16 v[226:227], v5 offset:3328\n"
        "s_add_u32 s73, s73, s34\n"
        "v_mfma_f32_16x16x32_bf16 v[20:23], v[80:83], v[28:31], 0\n"
        "s_xor_b32 s74, s74, 4\n"
        "buffer_store_dwordx3 v[186:188], v8, s[8:11], s37 offen sc0 sc1\n"
        "v_smfmac_f32_16x16x64_bf16 v[20:23], v[216:219], v[244:251], v14 abid:1\n"
        "ds_read_b64_tr_b16 v[246:247], v5 offset:3344\n"
        "s_mul_i32 s38, s31, 1344\n"
        "v_cvt_pk_bf16_f32 v185, v16, v84\n"
        "v_mfma_f32_16x16x32_bf16 v[16:19], v[80:83], v[44:47], 0\n"
        "s_cmp_ge_u32 s31, s71\n"
        "v_smfmac_f32_16x16x64_bf16 v[16:19], v[216:219], v[200:207], v14 abid:1\n"
        "ds_read_b64_tr_b16 v[202:203], v5 offset:3360\n"
        "s_cselect_b32 s39, s70, s72\n"
        "s_add_u32 s37, s35, 0\n"
        "v_mfma_f32_16x16x32_bf16 v[80:83], v[60:63], v[180:183], 0\n"
        "s_waitcnt lgkmcnt(2)\n"
        "s_add_u32 s39, s39, s38\n"
        "v_writelane_b32 v6, s73, 56\n"
        "v_add_u32 v7, s30, v7\n"
        "v_cvt_pk_bf16_f32 v186, v20, v16\n"
        "v_smfmac_f32_16x16x64_bf16 v[80:83], v[72:75], v[224:231], v14\n"
        "ds_read_b64_tr_b16 v[226:227], v5 offset:3376\n"
        "s_mov_b64 exec, s[62:63]\n"
        "v_add_u32 v12, s29, v12\n"
        "v_add_u32 v4, s34, v4\n"
        "s_mov_b64 exec, -1\n"
        "v_mfma_f32_16x16x32_bf16 v[216:219], v[60:63], v[232:235], 0\n"
        "s_waitcnt lgkmcnt(1)\n"
        "buffer_store_dwordx3 v[34:36], v11, s[8:11], s37 offen offset:768 sc0 sc1\n"
        "v_smfmac_f32_16x16x64_bf16 v[216:219], v[72:75], v[244:251], v14\n"
        "ds_read_b64_tr_b16 v[246:247], v5 offset:3392\n"
        "v_mfma_f32_16x16x32_bf16 v[84:87], v[60:63], v[88:91], 0\n"
        "v_smfmac_f32_16x16x64_bf16 v[84:87], v[72:75], v[200:207], v14\n"
        "ds_read_b64_tr_b16 v[202:203], v5 offset:3408\n"
        "s_add_u32 s37, s35, 98112\n"
        "v_mov_b32_dpp v199, v15 row_shr:4 row_mask:0xf bank_mask:0xf bound_ctrl:0\n"
        "v_mfma_f32_16x16x32_bf16 v[20:23], v[60:63], v[64:67], 0\n"
        "s_waitcnt lgkmcnt(2)\n"
        "v_smfmac_f32_16x16x64_bf16 v[20:23], v[72:75], v[224:231], v14 abid:1\n"
        "ds_read_b64_tr_b16 v[226:227], v5 offset:6656\n"
        "v_mov_b32 v19, s74\n"
        "v_mfma_f32_16x16x32_bf16 v[32:35], v[60:63], v[28:31], 0\n"
        "s_waitcnt lgkmcnt(2)\n"
        "buffer_store_dwordx3 v[54:56], v8, s[8:11], s37 offen offset:192 sc0 sc1 nt\n"
        "v_smfmac_f32_16x16x64_bf16 v[32:35], v[72:75], v[244:251], v14 abid:1\n"
        "ds_read_b64_tr_b16 v[246:247], v5 offset:6672\n"
        "v_mfma_f32_16x16x32_bf16 v[36:39], v[60:63], v[44:47], 0\n"
        "s_waitcnt lgkmcnt(2)\n"
        "ds_read_b32 v55, v19\n"
        "v_smfmac_f32_16x16x64_bf16 v[36:39], v[72:75], v[200:207], v14 abid:1\n"
        "ds_read_b64_tr_b16 v[202:203], v5 offset:6688\n"
        "s_add_u32 s37, s35, 98112\n"
        "v_mfma_f32_16x16x32_bf16 v[40:43], v[212:215], v[180:183], 0\n"
        "s_waitcnt lgkmcnt(3)\n"
        "v_smfmac_f32_16x16x64_bf16 v[40:43], v[196:199], v[224:231], v14\n"
        "ds_read_b64_tr_b16 v[226:227], v5 offset:6704\n"
        "v_mfma_f32_16x16x32_bf16 v[60:63], v[212:215], v[232:235], 0\n"
        "s_waitcnt lgkmcnt(3)\n"
        "buffer_store_dwordx3 v[220:222], v10, s[8:11], s37 offen offset:960 sc0 sc1 nt\n"
        "s_add_u32 s37, s35, 588672\n"
        "s_add_u32 s46, s37, 1344\n"
        "v_smfmac_f32_16x16x64_bf16 v[60:63], v[196:199], v[244:251], v14\n"
        "ds_read_b64_tr_b16 v[246:247], v5 offset:6720\n"
        "buffer_store_dwordx3 v[184:186], v8, s[44:47], s37 offen offset:1152 sc0 sc1 nt\n"
        "s_waitcnt lgkmcnt(1)\n"
        "v_readfirstlane_b32 s40, v55\n"
        "v_mfma_f32_16x16x32_bf16 v[72:75], v[212:215], v[88:91], 0\n"
        "s_cmp_lt_i32 s40, s68\n"
        "v_xor_b32 v244, 4, v19\n"
        "v_smfmac_f32_16x16x64_bf16 v[72:75], v[196:199], v[200:207], v14\n"
        "ds_read_b64_tr_b16 v[202:203], v5 offset:6736\n"
        "s_cbranch_scc1 L21_%=\n"
        "L22_%=:\n"
        "ds_read_b128 v[220:223], v3\n"
        "ds_read_b128 v[184:187], v3 offset:3328\n"
        "v_add_u32 v5, s69, v5\n"
        "v_mfma_f32_16x16x32_bf16 v[16:19], v[212:215], v[64:67], 0\n"
        "ds_read_b128 v[52:55], v3 offset:64\n"
        "ds_read_b128 v[76:79], v3 offset:3392\n"
        "ds_read_b128 v[48:51], v3 offset:128\n"
        "v_smfmac_f32_16x16x64_bf16 v[16:19], v[196:199], v[224:231], v14 abid:1\n"
        "ds_read_b128 v[240:243], v3 offset:3456\n"
        "v_mfma_f32_16x16x32_bf16 v[24:27], v[212:215], v[28:31], 0\n"
        "s_waitcnt lgkmcnt(6)\n"
        "s_mov_b64 exec, 1\n"
        "ds_inc_u32 v244, v1 offset:8\n"
        "s_mov_b64 exec, -1\n"
        "v_smfmac_f32_16x16x64_bf16 v[24:27], v[196:199], v[244:251], v14 abid:1\n"
        "s_sub_u32 s69, 0, s69\n"
        "s_mov_b32 s36, s35\n"
        "v_mfma_f32_16x16x32_bf16 v[244:247], v[212:215], v[44:47], 0\n"
        "s_mov_b32 s35, s39\n"
        "v_smfmac_f32_16x16x64_bf16 v[244:247], v[196:199], v[200:207], v14 abid:1\n"
        "s_setprio 0\n"
        "s_cmp_ge_u32 s31, s14\n"
        "s_cbranch_scc1 CEND_%=\n"
        "s_cmp_gt_i32 s29, 0\n"
        "s_cbranch_scc1 MTOP_%=\n"
        "s_branch MFULL_%=\n"
        :: "{v0}"(tid), "{s[0:1]}"(kap), "{s2}"(bid), "{s[4:5]}"(tab) : CLOBBERS);
}

extern "C" void kernel_launch(void* const* d_in, const int* in_sizes, int n_in, void* d_out, int out_size, void* d_ws, size_t ws_size, hipStream_t stream) {
    (void)in_sizes; (void)n_in; (void)out_size; (void)ws_size;
    (void)hipFuncSetAttribute((const void*)swin_kernel, hipFuncAttributeMaxDynamicSharedMemorySize, LDS_BYTES);
    swin_kernel<<<256, 512, LDS_BYTES, stream>>>((const char*)d_in[0], (const char*)d_in[1], (const char*)d_in[2], (const char*)d_in[3], (const char*)d_in[4], (char*)d_out, (const char*)d_ws);
}
```

```cpp
#define LTOK_ 208
#define FPAD 32
#define NPR 36
#define NWC 22
#define LK0 3
#define LK1 2
#define LK2 1
#define LK3 0
#include <hip/hip_runtime.h>
#include <hip/hip_bf16.h>

constexpr int TOKB = 192, ROWB = 511 * TOKB;
constexpr unsigned FRAMEB = 511u * ROWB;
constexpr int LTOK = LTOK_;
constexpr int LFRAME = 48 * LTOK + FPAD;
constexpr int LBUF = 4 * LFRAME;
constexpr int LSTASH = 10 * 4 * LTOK;
constexpr int LSTG = 6208;
constexpr int VST = 392, LEXT = 1536 + 8 * VST;
constexpr int WT = 3088;
constexpr int LDS_AUX = (54 - NPR) / 3 * WT, LDS_TAB = LDS_AUX + 1024, LDS_BUF = LDS_TAB + 2048, LDS_STASH = LDS_BUF + 2 * LBUF, LDS_CNT = LDS_STASH + LSTASH,
              LDS_STG = LDS_CNT + 128, LDS_SCRAP = LDS_STG + 4 * LSTG, LDS_EXT = LDS_SCRAP + 512, LDS_BYTES = 163840;
static_assert(NWC >= 18 && NPR % 3 == 0 && LDS_EXT + 4 * LEXT <= LDS_BYTES && NPR * 1024 <= LBUF, "LDS");
constexpr unsigned RAWM = 18432, RAW_T = LDS_BUF, RAW_TV = RAW_T + 2 * RAWM + 128, RAW_SV = (36 - NPR) / 3 * WT, RAW_SO = LDS_EXT, RAW_SQ = LDS_BUF + LBUF, RAW_SK = RAW_SQ + RAWM;
static_assert(RAW_SV + RAWM <= LDS_AUX && RAW_SO + RAWM <= LDS_BYTES && RAW_SK + RAWM <= LDS_STASH && RAW_TV + 2 * RAWM <= LDS_STASH && RAWM + 6 * WT <= 2 * RAWM + 128, "fold LDS");

constexpr int phi(int v) { return v ^ (v >> 1); }
struct Tab {
    unsigned img[3][64][4];
    unsigned p[4][7][64][4];
    unsigned c[4][6][64][4];
    unsigned d[4][7][64][4];
    static constexpr void job(unsigned (*q)[64][4], int lane, unsigned L, unsigned R, int mode, int tau, unsigned st0, unsigned st1) {
        const int i = lane & 15, g = lane >> 4;
        for (int a = 0; a < 2; ++a) {
            const unsigned ra = (8 * (i >> 2) + 4 * a + (i & 3)) * 192 + ((g ^ ((2 * (i >> 2) + a) & 3)) * 16);
            q[0][lane][a] = L + ra;
            q[0][lane][2 + a] = mode == 0 ? R + tau * 6144 + ra : mode == 2 ? R + tau * 6144 + (16 * a + i) * 192 + ((g ^ (i >> 2)) * 16)
                                          : R + tau * 64 + (8 * g + 4 * a + (i >> 2)) * 192 + (((i & 3) ^ ((2 * g + a) & 3)) * 16);
        }
        q[1][lane][0] = st0; q[1][lane][1] = st1; q[1][lane][2] = LDS_SCRAP + g * 32;
    }
    static constexpr unsigned scat(bool win, int lane, int tau, int tb) {
        const int i = lane & 15, c = 32 * tau + 8 * (i >> 2) + 4 * tb + (i & 3), r = c / 24, o = c % 24, q = win ? o / 6 : o & 3, t = win ? o % 6 : o >> 2;
        return (win ? RAW_SV : 3 * t < NPR - 18 ? LDS_BUF + 18 * 1024 : -(NPR - 18) / 3 * WT) + t * WT + ((lane & 48) + 4 * q + r) * 16;
    }
    constexpr Tab() : img{}, p{}, c{}, d{} {
        for (int e = 0; e < 48; ++e) img[0][e >> 2][e & 3] = 0x3f803f80u;
        for (int e = 0; e < 8; ++e) img[0][36 + 10 + e][e >> 1] = e & 1 ? 0x3f800000u : 0x3f80u;
        for (int k = 0; k < 4; ++k) for (int lane = 0; lane < 64; ++lane) {
            const int i = lane & 15, g = lane >> 4, rowv = 8 * (i >> 2) + (i & 3);
            unsigned vi[20] = {};
            {
                const int pp = i >> 2, t = i & 3;
                const int t1 = (k + 3 - LK1) & 3;
                if (t1 < 3) job(p[k], lane, RAW_TV, RAW_TV + RAWM, 1, t1, scat(false, lane, t1, 0), scat(false, lane, t1, 1));
                else job(p[k], lane, RAW_TV, RAW_TV + RAWM, 0, 0, LDS_AUX + 192 + rowv * 2, 0);
                const unsigned wl = lane * 16, lm = pp == g ? ~0u : 0u, idp = LDS_AUX + 576 + (t == g ? 10 + pp : 0) * 16, sidx = 0xdddd;
                unsigned vA[3] = {};
                for (int m = 0; m < 3; ++m) {
                    const int n = 64 * m + lane, tf = n / 96, q = n % 96 / 12, s = (n % 12) ^ tf;
                    const unsigned cc = tf * FRAMEB + (3 * (s & 3) + (s >> 2)) * 16 + 2048 - 1024 * m;
                    vA[m] = cc + q * TOKB;
                    img[1][lane][m] = (cc + 6 * ROWB + 6 * TOKB) | ((unsigned)(2 * (q & 3) + (q >> 2)) << 28);
                }
                const unsigned vA0 = vA[0], vA1 = vA[1], vA2 = vA[2], vB0 = vA0 + (ROWB - 7 * TOKB), vB1 = vA1 + (ROWB - 7 * TOKB), vB2 = vA2 + (ROWB - 7 * TOKB);
                const unsigned dA = LDS_BUF + t * LFRAME + pp * LTOK + g * 16, rA = LDS_BUF + t * LFRAME + (pp == g ? pp : 4 + g) * LTOK + 192;
                img[1][lane][3] = (2 * pp * (4 * LTOK) + t * LTOK + g * 16) | (((pp == g ? 2 * pp : 2 * g + 1) * (4 * LTOK) + t * LTOK + 192) << 16);
                const unsigned cd0 = dA + k * 8 * LTOK, cr0 = rA + k * 8 * LTOK;
                const unsigned xr = LDS_STG + k * LSTG + (t >> 1) * 3104 + ((t & 1) * 8 + pp) * TOKB + (g ^ (t & 1)) * 16;
                const unsigned axp = LDS_AUX + (t < 2 ? t : 2) * 192 + g * 16;
                vi[0] = xr; vi[1] = axp; vi[2] = lm; vi[3] = rA; vi[4] = dA; vi[5] = vA0; vi[6] = vA1; vi[7] = vA2; vi[8] = vB0; vi[9] = vB1; vi[10] = vB2; vi[11] = sidx; vi[12] = cd0; vi[13] = cr0; vi[14] = wl; vi[15] = idp;
                for (int e = 0; e < 16; ++e) p[k][2 + (e >> 2)][lane][e & 3] = vi[e];
            }
            {
                const int t0 = (k + 3 - LK0) & 3, t2 = (k + 3 - LK2) & 3, t3 = (k + 3 - LK3) & 3;
                const unsigned s0 = LDS_BUF + t0 * 6144 + lane * 16, s3 = LDS_BUF + LBUF + t3 * 6144 + lane * 16;
                if (t0 < 3) job(c[k], lane, RAW_T, RAW_T + RAWM, 2, t0, s0, s0 + 3072); else job(c[k], lane, RAW_T, RAW_T + RAWM, 1, 0, LDS_SCRAP + rowv * 4, 0);
                if (t2 < 3) job(c[k] + 4, lane, RAW_SV, RAW_SO, 1, t2, scat(true, lane, t2, 0), scat(true, lane, t2, 1));
                if (t3 < 3) job(d[k], lane, RAW_SQ, RAW_SK, 0, t3, s3, s3 + 3072); else job(d[k], lane, RAW_SQ, RAW_SK, 1, 0, LDS_SCRAP + rowv * 4, 0);
                for (int m = 0; m < 3; ++m) {
                    const int n = 64 * m + lane, row = n / 12, pc = ((n % 12) ^ ((row >> 2) & 3)) * 16;
                    c[k][3][lane][m] = (24 * (row & 3) + 12 * ((row >> 2) & 1) + 2 * (row >> 3)) * 576 + pc;
                    c[k][2][lane][m] = row * 192 + pc;
                }
                const int dg = 16 * (2 * (g & 1) + (g >> 1)), pix = 4 * (i & 3) + phi(i >> 2);
                const unsigned wl = lane * 16, fr = LDS_BUF + k * LFRAME, ext = LDS_EXT + k * LEXT;
                const unsigned ta0 = fr + pix * LTOK + dg, zr = LDS_AUX + 384 + g * 16, t30 = i & 3 ? zr : LDS_STASH + k * LTOK + dg;
                const unsigned xa0 = fr + (4 * (i >> 2) + phi(g)) * LTOK + 96 * (i & 1) + 8 * ((i >> 1) & 1);
                const int sg = 8 * ((i >> 1) & 1) + 4 * (i >> 3) + 2 * ((i >> 2) & 1) + (i & 1);
                const unsigned tv0 = fr + (32 + 4 * (sg & 3) + phi(sg >> 2)) * LTOK + dg, c1f = (i >> 2) == 2 * g ? 0x3f800000u : 0u;
                const unsigned sidx = g < 2 ? 0x0e0e0404u | ((i >> 2) == 2 * g ? (i & 3) * 0x00100010u : (i >> 2) == 2 * g + 1 ? (i & 3) * 0x40004000u : 0u) : 0xeeee4444u;
                const unsigned ga0 = phi(g) * TOKB + 48 * (i & 3) + 12 * (i >> 2), wr = ROWB - 7 * TOKB;
                const unsigned ga1 = ga0 + (phi(g) >= 1 ? wr : 0), ga2 = ga0 + (phi(g) >= 2 ? wr : 0), ga3 = ga0 + (phi(g) >= 3 ? wr : 0);
                const unsigned pb = LDS_SCRAP + k * 128, pr0 = i < 4 && (i >> 1) == (g >> 1) ? pb + (i & 1) * 32 + (g & 1) * 16 : zr, pr1 = i == 0 || i == 2 ? pb + 64 + g * 16 : zr;
                const unsigned pw = g < 2 ? pb + ((i >> 1) & 1) * 32 + (i >> 3) * 16 + ((i >> 2) & 1) * 8 + g * 4 + (i & 1) * 2
                                          : pb + 64 + (i >> 2) * 16 + (g & 1) * 4 + ((i >> 1) & 1) * 8 + (i & 1) * 2 + (lane == 57 ? 6 : 0);
                const unsigned six2 = (i & 1 ? 0xeeeeu : 0x4444u) | ((i & 2 ? 0xeeee0000u : 0x44440000u) & (g < 2 ? 0x0f0f0000u : 0xffff0000u));
                const unsigned exs0 = i & 2 ? zr : ext + (i & 1) * 64 + g * 16, t3s0 = i & 2 ? zr : LDS_STASH + k * LTOK + 64 * (g >> 1) + 16 * (g & 1) + 4 * (i & 1);
                const unsigned ex0 = i & 3 ? zr : ext + g * 16, exv0 = ext + 1536 + i * 24;
                const unsigned rla0 = lane < 48 ? fr + (16 * g + pix) * LTOK + 192 : lane == 56 ? LDS_STASH + k * LTOK + 192 : LDS_AUX + 384;
                img[2][lane][0] = ((i & 7) * (4 * LTOK) + dg) | ((((4 * g + (i & 3)) & 7) * (4 * LTOK) + 96 * ((i >> 2) & 1) + 8 * (i >> 3)) << 16);
                img[2][lane][1] = ((i & 7) * 192u + g * 16) | ((1536u + i * 24 + (g & 1) * 4 * VST) << 16);
                img[2][lane][2] = ((fr + phi(g) * LTOK + 192 - xa0) & 0xffffu) | ((fr + pix * LTOK + 192 - xa0) << 16);
                img[2][lane][3] = (LDS_AUX + 576 + (10 + (i & 3)) * 16) | ((1536u + (i & 7) * VST + 384) << 16);
                vi[0] = wl; vi[1] = pw; vi[2] = pr0; vi[3] = pr1; vi[4] = six2; vi[5] = exs0; vi[6] = t3s0; vi[7] = ta0; vi[8] = t30; vi[9] = xa0; vi[10] = rla0; vi[11] = exv0; vi[12] = ga0; vi[13] = ga1; vi[14] = ga2; vi[15] = ga3; vi[16] = ex0; vi[17] = tv0; vi[18] = sidx; vi[19] = c1f;
                for (int e = 0; e < 20; ++e) d[k][2 + (e >> 2)][lane][e & 3] = vi[e];
            }
        }
    }
};
static __device__ const Tab TAB __attribute__((aligned(128))) = Tab();

#define CLOBBERS "v1", "v2", "v3", "v4", "v5", "v6", "v7", "v8", "v9", "v10", "v11", "v12", "v13", "v14", "v15", "v16", "v17", "v18", "v19", "v20", "v21", "v22", "v23", "v24", "v25", "v26", "v27", "v28", "v29", "v30", "v31", "v32", "v33", "v34", "v35", "v36", "v37", "v38", "v39", "v40", "v41", "v42", "v43", "v44", "v45", "v46", "v47", "v48", "v49", "v50", "v51", "v52", "v53", "v54", "v55", "v56", "v57", "v58", "v59", "v60", "v61", "v62", "v63", "v64", "v65", "v66", "v67", "v68", "v69", "v70", "v71", "v72", "v73", "v74", "v75", "v76", "v77", "v78", "v79", "v80", "v81", "v82", "v83", "v84", "v85", "v86", "v87", "v88", "v89", "v90", "v91", "v92", "v93", "v94", "v95", "v96", "v97", "v98", "v99", "v100", "v101", "v102", "v103", "v104", "v105", "v106", "v107", "v108", "v109", "v110", "v111", "v112", "v113", "v114", "v115", "v116", "v117", "v118", "v119", "v120", "v121", "v122", "v123", "v124", "v125", "v126", "v127", "v128", "v129", "v130", "v131", "v132", "v133", "v134", "v135", "v136", "v137", "v138", "v139", "v140", "v141", "v142", "v143", "v144", "v145", "v146", "v147", "v148", "v149", "v150", "v151", "v152", "v153", "v154", "v155", "v156", "v157", "v158", "v159", "v160", "v161", "v162", "v163", "v164", "v165", "v166", "v167", "v168", "v169", "v170", "v171", "v172", "v173", "v174", "v175", "v176", "v177", "v178", "v179", "v180", "v181", "v182", "v183", "v184", "v185", "v186", "v187", "v188", "v189", "v190", "v191", "v192", "v193", "v194", "v195", "v196", "v197", "v198", "v199", "v200", "v201", "v202", "v203", "v204", "v205", "v206", "v207", "v208", "v209", "v210", "v211", "v212", "v213", "v214", "v215", "v216", "v217", "v218", "v219", "v220", "v221", "v222", "v223", "v224", "v225", "v226", "v227", "v228", "v229", "v230", "v231", "v232", "v233", "v234", "v235", "v236", "v237", "v238", "v239", "v240", "v241", "v242", "v243", "v244", "v245", "v246", "v247", "v248", "v249", "v250", "v251", "v252", "v253", "v254", "v255", "s3", "s6", "s7", "s8", "s9", "s10", "s11", "s12", "s13", "s14", "s15", "s16", "s17", "s18", "s19", "s20", "s21", "s22", "s23", "s24", "s25", "s26", "s27", "s28", "s29", "s30", "s31", "s34", "s35", "s36", "s37", "s38", "s39", "s40", "s41", "s42", "s43", "s44", "s45", "s46", "s47", "s48", "s49", "s50", "s51", "s52", "s53", "s54", "s55", "s56", "s57", "s58", "s59", "s60", "s61", "s62", "s63", "s64", "s65", "s66", "s67", "s68", "s69", "s70", "s71", "s72", "s73", "s74", "s75", "s76", "s77", "vcc", "scc", "memory"
__global__ __launch_bounds__(512, 2) void swin_kernel(const char* x, const char* wqkv_t, const char* wo_t, const char* wqkv_s, const char* wo_s, char* out, const char* ws) {
    const unsigned long long kap = (unsigned long long)__builtin_amdgcn_kernarg_segment_ptr(), tab = (unsigned long long)&TAB;
    const unsigned tid = threadIdx.x, bid = blockIdx.x;
    asm volatile(
        "s_load_dwordx8 s[44:51], s[0:1], 0x0\n"
        "s_load_dwordx4 s[52:55], s[0:1], 0x20\n"
        "s_load_dwordx2 s[56:57], s[0:1], 0x30\n"
        "v_readfirstlane_b32 s3, v0\n"
        "v_and_b32 v56, 63, v0\n"
        "v_lshlrev_b32 v57, 4, v56\n"
        "s_bfe_u32 s3, s3, 0x30006\n"
        "s_and_b32 s6, s3, 3\n"
        "s_mul_i32 s64, s3, 9216\n"
        "s_add_u32 s64, s64, 21600\n"
        "s_mov_b32 s27, 0x20000\n"
        "s_cmp_lt_u32 s3, 4\n"
        "s_cbranch_scc0 PPRO_%=\n"
        "s_mul_i32 s7, s6, 6144\n"
        "s_add_u32 s7, s7, 33792\n"
        "s_add_u32 s22, s4, s7\n"
        "s_addc_u32 s23, s5, 0\n"
        "global_load_dwordx4 v[240:243], v57, s[22:23] offset:-2048\n"
        "global_load_dwordx4 v[244:247], v57, s[22:23] offset:-1024\n"
        "global_load_dwordx4 v[36:39], v57, s[22:23] offset:1024\n"
        "global_load_dwordx4 v[48:51], v57, s[22:23] offset:2048\n"
        "global_load_dwordx4 v[52:55], v57, s[22:23] offset:3072\n"
        "s_mov_b32 s65, 13824\n"
        "v_add_u32 v24, 0, v56\n"
        "v_mul_u32_u24 v21, 171, v24\n"
        "v_lshrrev_b32 v21, 11, v21\n"
        "v_mad_i32_i24 v24, v21, -12, v24\n"
        "v_bfe_u32 v58, v21, 2, 2\n"
        "v_xor_b32 v58, v24, v58\n"
        "v_and_b32 v27, 7, v21\n"
        "v_mul_u32_u24 v27, 576, v27\n"
        "v_lshl_add_u32 v58, v58, 4, v27\n"
        "v_add_u32 v25, 64, v56\n"
        "v_mul_u32_u24 v22, 171, v25\n"
        "v_lshrrev_b32 v22, 11, v22\n"
        "v_mad_i32_i24 v25, v22, -12, v25\n"
        "v_bfe_u32 v59, v22, 2, 2\n"
        "v_xor_b32 v59, v25, v59\n"
        "v_and_b32 v27, 7, v22\n"
        "v_mul_u32_u24 v27, 576, v27\n"
        "v_lshl_add_u32 v59, v59, 4, v27\n"
        "v_lshrrev_b32 v27, 3, v22\n"
        "v_mad_u32_u24 v59, v27, s65, v59\n"
        "v_add_u32 v26, 128, v56\n"
        "v_mul_u32_u24 v23, 171, v26\n"
        "v_lshrrev_b32 v23, 11, v23\n"
        "v_mad_i32_i24 v26, v23, -12, v26\n"
        "v_bfe_u32 v60, v23, 2, 2\n"
        "v_xor_b32 v60, v26, v60\n"
        "v_and_b32 v27, 7, v23\n"
        "v_mul_u32_u24 v27, 576, v27\n"
        "v_lshl_add_u32 v60, v60, 4, v27\n"
        "v_add_u32 v60, s65, v60\n"
        "s_lshr_b32 s61, s6, 1\n"
        "s_mul_i32 s61, s61, 192\n"
        "s_mov_b32 s62, 27648\n"
        "s_mov_b32 s63, 4608\n"
        "s_add_u32 s7, s62, s63\n"
        "s_lshl_b32 s29, s63, 1\n"
        "s_add_u32 s60, s7, s63\n"
        "s_bitcmp1_b32 s6, 0\n"
        "s_cselect_b32 s28, s7, 0\n"
        "s_cselect_b32 s29, s29, s62\n"
        "s_cselect_b32 s60, s60, s63\n"
        "s_add_u32 s28, s28, s61\n"
        "s_add_u32 s29, s29, s61\n"
        "s_add_u32 s60, s60, s61\n"
        "s_mov_b32 s26, 55296\n"
        "s_waitcnt lgkmcnt(0)\n"
        "s_mov_b32 s24, s46\n"
        "s_and_b32 s25, s47, 0xffff\n"
        "s_add_u32 m0, s64, 0\n"
        "s_nop 0\n"
        "buffer_load_dwordx4 v58, s[24:27], s28 offen lds\n"
        "s_add_u32 m0, s64, 1024\n"
        "s_nop 0\n"
        "buffer_load_dwordx4 v59, s[24:27], s28 offen lds\n"
        "s_add_u32 m0, s64, 2048\n"
        "s_nop 0\n"
        "buffer_load_dwordx4 v60, s[24:27], s28 offen lds\n"
        "s_add_u32 m0, s64, 3072\n"
        "s_nop 0\n"
        "buffer_load_dwordx4 v58, s[24:27], s29 offen lds\n"
        "s_add_u32 m0, s64, 4096\n"
        "s_nop 0\n"
        "buffer_load_dwordx4 v59, s[24:27], s29 offen lds\n"
        "s_add_u32 m0, s64, 5120\n"
        "s_nop 0\n"
        "buffer_load_dwordx4 v60, s[24:27], s29 offen lds\n"
        "s_add_u32 m0, s64, 6144\n"
        "s_nop 0\n"
        "buffer_load_dwordx4 v58, s[24:27], s60 offen lds\n"
        "s_add_u32 m0, s64, 7168\n"
        "s_nop 0\n"
        "buffer_load_dwordx4 v59, s[24:27], s60 offen lds\n"
        "s_add_u32 m0, s64, 8192\n"
        "s_nop 0\n"
        "buffer_load_dwordx4 v60, s[24:27], s60 offen lds\n"
        "s_barrier\n"
        "s_setprio 1\n"
        "v_mov_b32 v250, 110048\n"
        "s_mov_b32 s41, 0x400000\n"
        "s_and_b32 s7, s2, 7\n"
        "s_lshl_b32 s7, s7, 5\n"
        "s_lshr_b32 s14, s2, 3\n"
        "s_add_u32 s7, s7, s14\n"
        "s_mul_i32 s7, s7, 20\n"
        "s_mov_b32 s14, 22\n"
        "s_mul_i32 s15, s7, 57457\n"
        "s_lshr_b32 s15, s15, 22\n"
        "s_mul_i32 s16, s15, 73\n"
        "s_sub_u32 s16, s7, s16\n"
        "s_mul_i32 s15, s15, 686784\n"
        "s_mul_i32 s7, s16, 1344\n"
        "s_add_u32 s15, s15, s7\n"
        "s_mov_b32 s8, s54\n"
        "s_and_b32 s9, s55, 0xffff\n"
        "s_mov_b32 s10, 200540928\n"
        "s_mov_b32 s11, 0x20000\n"
        "s_mov_b64 s[12:13], s[54:55]\n"
        "s_mul_i32 s17, s6, 50135232\n"
        "s_mul_i32 s18, s6, 208\n"
        "s_add_u32 s18, s18, 109408\n"
        "s_mul_i32 s19, s6, 4672\n"
        "s_add_u32 s19, s19, 135520\n"
        "s_mul_i32 s7, s6, 7168\n"
        "s_add_u32 s7, s7, 59392\n"
        "s_add_u32 s22, s4, s7\n"
        "s_addc_u32 s23, s5, 0\n"
        "s_waitcnt vmcnt(0)\n"
        "s_mov_b64 exec, 1\n"
        "ds_inc_u32 v250, v250 offset:16\n"
        "s_mov_b64 exec, -1\n"
        "L65_%=:\n"
        "ds_read_b32 v251, v250 offset:16\n"
        "s_waitcnt lgkmcnt(0)\n"
        "v_readfirstlane_b32 s40, v251\n"
        "s_nop 3\n"
        "s_cmp_ge_i32 s40, 4\n"
        "s_cbranch_scc1 L66_%=\n"
        "s_nop 0\n"
        "s_sub_u32 s41, s41, 1\n"
        "s_cbranch_scc0 L65_%=\n"
        "s_endpgm\n"
        "L66_%=:\n"
        "s_cmp_eq_u32 s6, 3\n"
        "s_cbranch_scc1 L67_%=\n"
        "s_mov_b32 s34, 0x4162ccfd\n"
        "s_mov_b32 s35, 110072\n"
        "s_mov_b32 s36, 110072\n"
        "s_mov_b32 s37, 110076\n"
        "s_call_b64 s[30:31], FOLDM_%=\n"
        "s_branch L68_%=\n"
        "L67_%=:\n"
        "s_mov_b32 s34, 0xbe173353\n"
        "s_mov_b32 s35, 110104\n"
        "s_mov_b32 s36, 110104\n"
        "s_mov_b32 s37, 110072\n"
        "s_call_b64 s[30:31], FOLDR_%=\n"
        "s_cmp_lg_u32 s2, 0\n"
        "s_cbranch_scc1 L64_%=\n"
        "s_mov_b64 s[24:25], s[56:57]\n"
        "s_mov_b64 exec, 1\n"
        "v_mov_b32 v27, 0\n"
        "v_mov_b32 v28, 0\n"
        "global_atomic_swap v252, v27, v28, s[24:25] offset:0 sc0\n"
        "s_mov_b64 exec, -1\n"
        "s_waitcnt vmcnt(0)\n"
        "s_mov_b64 exec, 1\n"
        "v_mov_b32 v27, 0\n"
        "v_mov_b32 v28, 1\n"
        "global_atomic_add v252, v27, v28, s[24:25] offset:256 sc0\n"
        "s_mov_b64 exec, -1\n"
        "L64_%=:\n"
        "L68_%=:\n"
        "s_setprio 3\n"
        "v_mov_b64 v[240:241], v[48:49]\n"
        "v_mov_b64 v[242:243], v[50:51]\n"
        "v_mov_b64 v[244:245], v[52:53]\n"
        "v_mov_b64 v[246:247], v[54:55]\n"
        "L69_%=:\n"
        "ds_read_b32 v251, v250 offset:32\n"
        "s_waitcnt lgkmcnt(0)\n"
        "v_readfirstlane_b32 s40, v251\n"
        "s_nop 3\n"
        "s_cmp_ge_i32 s40, 4\n"
        "s_cbranch_scc1 L70_%=\n"
        "s_nop 0\n"
        "s_sub_u32 s41, s41, 1\n"
        "s_cbranch_scc0 L69_%=\n"
        "s_endpgm\n"
        "L70_%=:\n"
        "s_cmp_eq_u32 s6, 1\n"
        "s_cbranch_scc1 L71_%=\n"
        "s_mov_b32 s34, 0x411cc471\n"
        "s_mov_b32 s35, 110088\n"
        "s_mov_b32 s36, 110088\n"
        "s_mov_b32 s37, 110100\n"
        "s_call_b64 s[30:31], FOLDW_%=\n"
        "s_branch L72_%=\n"
        "L71_%=:\n"
        "s_mov_b64 exec, 1\n"
        "ds_inc_u32 v250, v250 offset:40\n"
        "s_mov_b64 exec, -1\n"
        "s_mov_b32 s24, s50\n"
        "s_and_b32 s25, s51, 0xffff\n"
        "s_mov_b32 s26, 55296\n"
        "s_mov_b32 m0, 61664\n"
        "s_mov_b32 s28, 0\n"
        "buffer_load_dwordx4 v36, s[24:27], s28 offen lds\n"
        "s_mov_b32 m0, 62688\n"
        "s_nop 0\n"
        "buffer_load_dwordx4 v37, s[24:27], s28 offen lds\n"
        "s_mov_b32 m0, 63712\n"
        "s_nop 0\n"
        "buffer_load_dwordx4 v38, s[24:27], s28 offen lds\n"
        "s_mov_b32 m0, 64736\n"
        "s_mov_b32 s28, 576\n"
        "buffer_load_dwordx4 v36, s[24:27], s28 offen lds\n"
        "s_mov_b32 m0, 65760\n"
        "s_nop 0\n"
        "buffer_load_dwordx4 v37, s[24:27], s28 offen lds\n"
        "s_mov_b32 m0, 66784\n"
        "s_nop 0\n"
        "buffer_load_dwordx4 v38, s[24:27], s28 offen lds\n"
        "s_mov_b32 m0, 67808\n"
        "s_mov_b32 s28, 2304\n"
        "buffer_load_dwordx4 v36, s[24:27], s28 offen lds\n"
        "s_mov_b32 m0, 68832\n"
        "s_nop 0\n"
        "buffer_load_dwordx4 v37, s[24:27], s28 offen lds\n"
        "s_mov_b32 m0, 69856\n"
        "s_nop 0\n"
        "buffer_load_dwordx4 v38, s[24:27], s28 offen lds\n"
        "s_mov_b32 m0, 70880\n"
        "s_mov_b32 s28, 2880\n"
        "buffer_load_dwordx4 v36, s[24:27], s28 offen lds\n"
        "s_mov_b32 m0, 71904\n"
        "s_nop 0\n"
        "buffer_load_dwordx4 v37, s[24:27], s28 offen lds\n"
        "s_mov_b32 m0, 72928\n"
        "s_nop 0\n"
        "buffer_load_dwordx4 v38, s[24:27], s28 offen lds\n"
        "s_mov_b32 m0, 73952\n"
        "s_mov_b32 s28, 4608\n"
        "buffer_load_dwordx4 v36, s[24:27], s28 offen lds\n"
        "s_mov_b32 m0, 74976\n"
        "s_nop 0\n"
        "buffer_load_dwordx4 v37, s[24:27], s28 offen lds\n"
        "s_mov_b32 m0, 76000\n"
        "s_nop 0\n"
        "buffer_load_dwordx4 v38, s[24:27], s28 offen lds\n"
        "s_mov_b32 m0, 77024\n"
        "s_mov_b32 s28, 5184\n"
        "buffer_load_dwordx4 v36, s[24:27], s28 offen lds\n"
        "s_mov_b32 m0, 78048\n"
        "s_nop 0\n"
        "buffer_load_dwordx4 v37, s[24:27], s28 offen lds\n"
        "s_mov_b32 m0, 79072\n"
        "s_nop 0\n"
        "buffer_load_dwordx4 v38, s[24:27], s28 offen lds\n"
        "s_mov_b32 s24, s50\n"
        "s_and_b32 s25, s51, 0xffff\n"
        "s_mov_b32 s26, 55296\n"
        "s_mov_b32 m0, 80096\n"
        "s_mov_b32 s28, 192\n"
        "buffer_load_dwordx4 v36, s[24:27], s28 offen lds\n"
        "s_mov_b32 m0, 81120\n"
        "s_nop 0\n"
        "buffer_load_dwordx4 v37, s[24:27], s28 offen lds\n"
        "s_mov_b32 m0, 82144\n"
        "s_nop 0\n"
        "buffer_load_dwordx4 v38, s[24:27], s28 offen lds\n"
        "s_mov_b32 m0, 83168\n"
        "s_mov_b32 s28, 768\n"
        "buffer_load_dwordx4 v36, s[24:27], s28 offen lds\n"
        "s_mov_b32 m0, 84192\n"
        "s_nop 0\n"
        "buffer_load_dwordx4 v37, s[24:27], s28 offen lds\n"
        "s_mov_b32 m0, 85216\n"
        "s_nop 0\n"
        "buffer_load_dwordx4 v38, s[24:27], s28 offen lds\n"
        "s_mov_b32 m0, 86240\n"
        "s_mov_b32 s28, 2496\n"
        "buffer_load_dwordx4 v36, s[24:27], s28 offen lds\n"
        "s_mov_b32 m0, 87264\n"
        "s_nop 0\n"
        "buffer_load_dwordx4 v37, s[24:27], s28 offen lds\n"
        "s_mov_b32 m0, 88288\n"
        "s_nop 0\n"
        "buffer_load_dwordx4 v38, s[24:27], s28 offen lds\n"
        "s_mov_b32 m0, 89312\n"
        "s_mov_b32 s28, 3072\n"
        "buffer_load_dwordx4 v36, s[24:27], s28 offen lds\n"
        "s_mov_b32 m0, 90336\n"
        "s_nop 0\n"
        "buffer_load_dwordx4 v37, s[24:27], s28 offen lds\n"
        "s_mov_b32 m0, 91360\n"
        "s_nop 0\n"
        "buffer_load_dwordx4 v38, s[24:27], s28 offen lds\n"
        "s_mov_b32 m0, 92384\n"
        "s_mov_b32 s28, 4800\n"
        "buffer_load_dwordx4 v36, s[24:27], s28 offen lds\n"
        "s_mov_b32 m0, 93408\n"
        "s_nop 0\n"
        "buffer_load_dwordx4 v37, s[24:27], s28 offen lds\n"
        "s_mov_b32 m0, 94432\n"
        "s_nop 0\n"
        "buffer_load_dwordx4 v38, s[24:27], s28 offen lds\n"
        "s_mov_b32 m0, 95456\n"
        "s_mov_b32 s28, 5376\n"
        "buffer_load_dwordx4 v36, s[24:27], s28 offen lds\n"
        "s_mov_b32 m0, 96480\n"
        "s_nop 0\n"
        "buffer_load_dwordx4 v37, s[24:27], s28 offen lds\n"
        "s_mov_b32 m0, 97504\n"
        "s_nop 0\n"
        "buffer_load_dwordx4 v38, s[24:27], s28 offen lds\n"
        "global_load_dwordx4 v[40:43], v57, s[22:23] offset:-3072\n"
        "global_load_dwordx4 v[44:47], v57, s[22:23] offset:-2048\n"
        "global_load_dwordx4 v[0:3], v57, s[22:23] offset:-1024\n"
        "global_load_dwordx4 v[4:7], v57, s[22:23] offset:0\n"
        "global_load_dwordx4 v[8:11], v57, s[22:23] offset:1024\n"
        "global_load_dwordx4 v[12:15], v57, s[22:23] offset:2048\n"
        "global_load_dwordx4 v[16:19], v57, s[22:23] offset:3072\n"
        "L72_%=:\n"
        "s_waitcnt vmcnt(0)\n"
        "s_mov_b64 exec, 1\n"
        "ds_inc_u32 v250, v250 offset:44\n"
        "s_mov_b64 exec, -1\n"
        "v_mov_b64 v[240:241], v[40:41]\n"
        "v_mov_b64 v[242:243], v[42:43]\n"
        "v_mov_b64 v[244:245], v[44:45]\n"
        "v_mov_b64 v[246:247], v[46:47]\n"
        "L73_%=:\n"
        "ds_read_b32 v251, v250 offset:44\n"
        "s_waitcnt lgkmcnt(0)\n"
        "v_readfirstlane_b32 s40, v251\n"
        "s_nop 3\n"
        "s_cmp_ge_i32 s40, 4\n"
        "s_cbranch_scc1 L74_%=\n"
        "s_nop 0\n"
        "s_sub_u32 s41, s41, 1\n"
        "s_cbranch_scc0 L73_%=\n"
        "s_endpgm\n"
        "L74_%=:\n"
        "s_cmp_eq_u32 s6, 0\n"
        "s_cbranch_scc1 L75_%=\n"
        "s_mov_b32 s34, 0x41622adf\n"
        "s_mov_b32 s35, 110096\n"
        "s_mov_b32 s36, 110096\n"
        "s_mov_b32 s37, 110100\n"
        "s_call_b64 s[30:31], FOLDM_%=\n"
        "s_branch L76_%=\n"
        "L75_%=:\n"
        "s_mov_b32 s34, 0xbe16c740\n"
        "s_mov_b32 s35, 110104\n"
        "s_mov_b32 s36, 110104\n"
        "s_mov_b32 s37, 110096\n"
        "s_call_b64 s[30:31], FOLDR_%=\n"
        "L76_%=:\n"
        "s_branch CONS_%=\n"
        "FOLDM_%=:\n"
        "ds_read_b128 v[88:91], v242\n"
        "ds_read_b128 v[92:95], v240\n"
        "ds_read_b128 v[180:183], v242 offset:64\n"
        "ds_read_b128 v[172:175], v240 offset:64\n"
        "ds_read_b128 v[184:187], v242 offset:128\n"
        "ds_read_b128 v[164:167], v240 offset:128\n"
        "ds_read_b128 v[168:171], v241\n"
        "ds_read_b128 v[100:103], v241 offset:64\n"
        "ds_read_b128 v[104:107], v241 offset:128\n"
        "ds_read_b128 v[140:143], v240 offset:6144\n"
        "ds_read_b128 v[132:135], v240 offset:6208\n"
        "ds_read_b128 v[96:99], v240 offset:6272\n"
        "ds_read_b128 v[136:139], v241 offset:6144\n"
        "ds_read_b128 v[128:131], v241 offset:6208\n"
        "ds_read_b128 v[108:111], v241 offset:6272\n"
        "s_waitcnt lgkmcnt(13)\n"
        "v_mfma_f32_16x16x32_bf16 v[64:67], v[92:95], v[88:91], 0\n"
        "s_waitcnt lgkmcnt(11)\n"
        "ds_read_b128 v[152:155], v240 offset:12288\n"
        "v_mfma_f32_16x16x32_bf16 v[64:67], v[172:175], v[180:183], v[64:67]\n"
        "s_waitcnt lgkmcnt(8)\n"
        "ds_read_b128 v[160:163], v240 offset:12352\n"
        "v_mfma_f32_16x16x32_bf16 v[64:67], v[164:167], v[184:187], v[64:67]\n"
        "ds_read_b128 v[112:115], v240 offset:12416\n"
        "v_mfma_f32_16x16x32_bf16 v[68:71], v[168:171], v[88:91], 0\n"
        "ds_read_b128 v[116:119], v241 offset:12288\n"
        "v_mfma_f32_16x16x32_bf16 v[68:71], v[100:103], v[180:183], v[68:71]\n"
        "s_waitcnt lgkmcnt(4)\n"
        "ds_read_b128 v[120:123], v241 offset:12352\n"
        "v_mfma_f32_16x16x32_bf16 v[68:71], v[104:107], v[184:187], v[68:71]\n"
        "ds_read_b128 v[124:127], v241 offset:12416\n"
        "v_mfma_f32_16x16x32_bf16 v[72:75], v[140:143], v[88:91], 0\n"
        "ds_read_b128 v[144:147], v243\n"
        "v_mfma_f32_16x16x32_bf16 v[72:75], v[132:135], v[180:183], v[72:75]\n"
        "ds_read_b128 v[148:151], v243 offset:64\n"
        "v_mfma_f32_16x16x32_bf16 v[72:75], v[96:99], v[184:187], v[72:75]\n"
        "ds_read_b128 v[156:159], v243 offset:128\n"
        "v_mfma_f32_16x16x32_bf16 v[76:79], v[136:139], v[88:91], 0\n"
        "v_add_f32 v176, v64, v68\n"
        "v_add_f32 v177, v65, v69\n"
        "v_add_f32 v178, v66, v70\n"
        "v_add_f32 v179, v67, v71\n"
        "v_mfma_f32_16x16x32_bf16 v[76:79], v[128:131], v[180:183], v[76:79]\n"
        "v_mfma_f32_16x16x32_bf16 v[76:79], v[108:111], v[184:187], v[76:79]\n"
        "s_waitcnt lgkmcnt(3)\n"
        "v_add_f32 v176, v176, v72\n"
        "v_add_f32 v177, v177, v73\n"
        "v_add_f32 v178, v178, v74\n"
        "v_add_f32 v179, v179, v75\n"
        "v_mfma_f32_16x16x32_bf16 v[80:83], v[152:155], v[88:91], 0\n"
        "v_mfma_f32_16x16x32_bf16 v[80:83], v[160:163], v[180:183], v[80:83]\n"
        "v_mfma_f32_16x16x32_bf16 v[80:83], v[112:115], v[184:187], v[80:83]\n"
        "v_add_f32 v176, v176, v76\n"
        "v_add_f32 v177, v177, v77\n"
        "v_add_f32 v178, v178, v78\n"
        "v_add_f32 v179, v179, v79\n"
        "v_mfma_f32_16x16x32_bf16 v[84:87], v[116:119], v[88:91], 0\n"
        "v_mfma_f32_16x16x32_bf16 v[84:87], v[120:123], v[180:183], v[84:87]\n"
        "v_mfma_f32_16x16x32_bf16 v[84:87], v[124:127], v[184:187], v[84:87]\n"
        "s_waitcnt lgkmcnt(0)\n"
        "v_add_f32 v176, v176, v80\n"
        "v_add_f32 v177, v177, v81\n"
        "v_add_f32 v178, v178, v82\n"
        "v_add_f32 v179, v179, v83\n"
        "v_mov_b32 v239, s35\n"
        "s_waitcnt vmcnt(6)\n"
        "s_mov_b64 exec, 1\n"
        "ds_inc_u32 v239, v239 offset:0\n"
        "s_mov_b64 exec, -1\n"
        "v_add_f32 v176, v176, v84\n"
        "v_add_f32 v177, v177, v85\n"
        "v_add_f32 v178, v178, v86\n"
        "v_add_f32 v179, v179, v87\n"
        "v_add_f32 v176, v176, v177\n"
        "v_add_f32 v178, v178, v179\n"
        "v_add_f32 v176, v176, v178\n"
        "v_mov_b32 v177, v176\n"
        "v_mfma_f32_16x16x32_bf16 v[88:91], v[92:95], v[144:147], 0\n"
        "v_mfma_f32_16x16x32_bf16 v[88:91], v[172:175], v[148:151], v[88:91]\n"
        "v_permlane32_swap_b32 v176, v177\n"
        "v_add_f32 v176, v176, v177\n"
        "v_mov_b32 v177, v176\n"
        "v_mfma_f32_16x16x32_bf16 v[88:91], v[164:167], v[156:159], v[88:91]\n"
        "v_mfma_f32_16x16x32_bf16 v[92:95], v[168:171], v[144:147], 0\n"
        "v_permlane16_swap_b32 v176, v177\n"
        "v_add_f32 v176, v176, v177\n"
        "v_mul_f32 v176, 0xbc2aaaab, v176\n"
        "v_mul_f32 v176, s34, v176\n"
        "v_fma_f32 v64, v64, s34, v176\n"
        "v_fma_f32 v65, v65, s34, v176\n"
        "v_fma_f32 v66, v66, s34, v176\n"
        "v_fma_f32 v67, v67, s34, v176\n"
        "v_fma_f32 v68, v68, s34, v176\n"
        "v_fma_f32 v69, v69, s34, v176\n"
        "v_fma_f32 v70, v70, s34, v176\n"
        "v_fma_f32 v71, v71, s34, v176\n"
        "v_fma_f32 v72, v72, s34, v176\n"
        "v_fma_f32 v73, v73, s34, v176\n"
        "v_fma_f32 v74, v74, s34, v176\n"
        "v_fma_f32 v75, v75, s34, v176\n"
        "v_fma_f32 v76, v76, s34, v176\n"
        "v_fma_f32 v77, v77, s34, v176\n"
        "v_fma_f32 v78, v78, s34, v176\n"
        "v_fma_f32 v79, v79, s34, v176\n"
        "v_fma_f32 v80, v80, s34, v176\n"
        "v_fma_f32 v81, v81, s34, v176\n"
        "v_fma_f32 v82, v82, s34, v176\n"
        "v_fma_f32 v83, v83, s34, v176\n"
        "v_fma_f32 v84, v84, s34, v176\n"
        "v_fma_f32 v85, v85, s34, v176\n"
        "v_fma_f32 v86, v86, s34, v176\n"
        "v_fma_f32 v87, v87, s34, v176\n"
        "v_mfma_f32_16x16x32_bf16 v[92:95], v[100:103], v[148:151], v[92:95]\n"
        "v_mfma_f32_16x16x32_bf16 v[92:95], v[104:107], v[156:159], v[92:95]\n"
        "v_mfma_f32_16x16x32_bf16 v[100:103], v[140:143], v[144:147], 0\n"
        "v_mfma_f32_16x16x32_bf16 v[100:103], v[132:135], v[148:151], v[100:103]\n"
        "v_mfma_f32_16x16x32_bf16 v[100:103], v[96:99], v[156:159], v[100:103]\n"
        "v_mfma_f32_16x16x32_bf16 v[104:107], v[136:139], v[144:147], 0\n"
        "v_mfma_f32_16x16x32_bf16 v[104:107], v[128:131], v[148:151], v[104:107]\n"
        "v_mfma_f32_16x16x32_bf16 v[104:107], v[108:111], v[156:159], v[104:107]\n"
        "v_mfma_f32_16x16x32_bf16 v[96:99], v[152:155], v[144:147], 0\n"
        "v_mfma_f32_16x16x32_bf16 v[96:99], v[160:163], v[148:151], v[96:99]\n"
        "v_add_f32 v128, v88, v92\n"
        "v_add_f32 v129, v89, v93\n"
        "v_add_f32 v130, v90, v94\n"
        "v_add_f32 v131, v91, v95\n"
        "v_add_f32 v128, v128, v100\n"
        "v_add_f32 v129, v129, v101\n"
        "v_add_f32 v130, v130, v102\n"
        "v_add_f32 v131, v131, v103\n"
        "v_add_f32 v128, v128, v104\n"
        "v_add_f32 v129, v129, v105\n"
        "v_add_f32 v130, v130, v106\n"
        "v_add_f32 v131, v131, v107\n"
        "v_mfma_f32_16x16x32_bf16 v[96:99], v[112:115], v[156:159], v[96:99]\n"
        "v_mfma_f32_16x16x32_bf16 v[108:111], v[116:119], v[144:147], 0\n"
        "v_mfma_f32_16x16x32_bf16 v[108:111], v[120:123], v[148:151], v[108:111]\n"
        "v_mfma_f32_16x16x32_bf16 v[108:111], v[124:127], v[156:159], v[108:111]\n"
        "v_mov_b32 v239, s37\n"
        "s_nop 3\n"
        "v_add_f32 v128, v128, v96\n"
        "v_add_f32 v129, v129, v97\n"
        "v_add_f32 v130, v130, v98\n"
        "v_add_f32 v131, v131, v99\n"
        "v_add_f32 v128, v128, v108\n"
        "v_add_f32 v129, v129, v109\n"
        "v_add_f32 v130, v130, v110\n"
        "v_add_f32 v131, v131, v111\n"
        "v_add_f32 v128, v128, v129\n"
        "v_add_f32 v130, v130, v131\n"
        "v_add_f32 v128, v128, v130\n"
        "v_mov_b32 v129, v128\n"
        "s_nop 1\n"
        "v_permlane32_swap_b32 v128, v129\n"
        "v_add_f32 v128, v128, v129\n"
        "v_mov_b32 v129, v128\n"
        "s_nop 1\n"
        "v_permlane16_swap_b32 v128, v129\n"
        "v_add_f32 v128, v128, v129\n"
        "v_mul_f32 v128, 0xbc2aaaab, v128\n"
        "v_mul_f32 v128, s34, v128\n"
        "v_fma_f32 v88, v88, s34, v128\n"
        "v_fma_f32 v89, v89, s34, v128\n"
        "v_fma_f32 v90, v90, s34, v128\n"
        "v_fma_f32 v91, v91, s34, v128\n"
        "v_fma_f32 v92, v92, s34, v128\n"
        "v_fma_f32 v93, v93, s34, v128\n"
        "v_fma_f32 v94, v94, s34, v128\n"
        "v_fma_f32 v95, v95, s34, v128\n"
        "v_fma_f32 v100, v100, s34, v128\n"
        "v_fma_f32 v101, v101, s34, v128\n"
        "v_fma_f32 v102, v102, s34, v128\n"
        "v_fma_f32 v103, v103, s34, v128\n"
        "v_fma_f32 v104, v104, s34, v128\n"
        "v_fma_f32 v105, v105, s34, v128\n"
        "v_fma_f32 v106, v106, s34, v128\n"
        "v_fma_f32 v107, v107, s34, v128\n"
        "v_fma_f32 v96, v96, s34, v128\n"
        "v_fma_f32 v97, v97, s34, v128\n"
        "v_fma_f32 v98, v98, s34, v128\n"
        "v_fma_f32 v99, v99, s34, v128\n"
        "v_fma_f32 v108, v108, s34, v128\n"
        "v_fma_f32 v109, v109, s34, v128\n"
        "v_fma_f32 v110, v110, s34, v128\n"
        "v_fma_f32 v111, v111, s34, v128\n"
        "v_mov_b32 v237, s36\n"
        "L60_%=:\n"
        "ds_read_b32 v238, v237 offset:0\n"
        "s_waitcnt lgkmcnt(0)\n"
        "v_readfirstlane_b32 s40, v238\n"
        "s_nop 3\n"
        "s_cmp_ge_i32 s40, 4\n"
        "s_cbranch_scc1 L61_%=\n"
        "s_nop 0\n"
        "s_sub_u32 s41, s41, 1\n"
        "s_cbranch_scc0 L60_%=\n"
        "s_endpgm\n"
        "L61_%=:\n"
        "ds_read_b128 v[132:135], v246\n"
        "ds_read_b128 v[128:131], v246 offset:16\n"
        "ds_read_b128 v[124:127], v246 offset:128\n"
        "ds_read_b128 v[120:123], v246 offset:144\n"
        "ds_read_b128 v[116:119], v246 offset:256\n"
        "ds_read_b128 v[112:115], v246 offset:272\n"
        "s_waitcnt lgkmcnt(5)\n"
        "v_add_f32 v64, v64, v132\n"
        "v_add_f32 v65, v65, v133\n"
        "v_add_f32 v66, v66, v134\n"
        "v_add_f32 v67, v67, v135\n"
        "v_add_f32 v88, v88, v132\n"
        "v_add_f32 v89, v89, v133\n"
        "v_add_f32 v90, v90, v134\n"
        "v_add_f32 v91, v91, v135\n"
        "s_waitcnt lgkmcnt(0)\n"
        "v_add_f32 v68, v68, v128\n"
        "v_add_f32 v69, v69, v129\n"
        "v_add_f32 v70, v70, v130\n"
        "v_add_f32 v71, v71, v131\n"
        "v_add_f32 v92, v92, v128\n"
        "v_add_f32 v93, v93, v129\n"
        "v_add_f32 v94, v94, v130\n"
        "v_add_f32 v95, v95, v131\n"
        "v_add_f32 v72, v72, v124\n"
        "v_add_f32 v73, v73, v125\n"
        "v_add_f32 v74, v74, v126\n"
        "v_add_f32 v75, v75, v127\n"
        "v_add_f32 v100, v100, v124\n"
        "v_add_f32 v101, v101, v125\n"
        "v_add_f32 v102, v102, v126\n"
        "v_add_f32 v103, v103, v127\n"
        "v_add_f32 v76, v76, v120\n"
        "v_add_f32 v77, v77, v121\n"
        "v_add_f32 v78, v78, v122\n"
        "v_add_f32 v79, v79, v123\n"
        "v_add_f32 v104, v104, v120\n"
        "v_add_f32 v105, v105, v121\n"
        "v_add_f32 v106, v106, v122\n"
        "v_add_f32 v107, v107, v123\n"
        "v_add_f32 v80, v80, v116\n"
        "v_add_f32 v81, v81, v117\n"
        "v_add_f32 v82, v82, v118\n"
        "v_add_f32 v83, v83, v119\n"
        "v_add_f32 v96, v96, v116\n"
        "v_add_f32 v97, v97, v117\n"
        "v_add_f32 v98, v98, v118\n"
        "v_add_f32 v99, v99, v119\n"
        "v_add_f32 v84, v84, v112\n"
        "v_add_f32 v85, v85, v113\n"
        "v_add_f32 v86, v86, v114\n"
        "v_add_f32 v87, v87, v115\n"
        "v_add_f32 v108, v108, v112\n"
        "v_add_f32 v109, v109, v113\n"
        "v_add_f32 v110, v110, v114\n"
        "v_add_f32 v111, v111, v115\n"
        "v_cvt_pk_bf16_f32 v112, v64, v65\n"
        "v_cvt_pk_bf16_f32 v113, v66, v67\n"
        "v_cvt_pk_bf16_f32 v114, v68, v69\n"
        "v_cvt_pk_bf16_f32 v115, v70, v71\n"
        "v_cvt_pk_bf16_f32 v64, v88, v89\n"
        "v_cvt_pk_bf16_f32 v65, v90, v91\n"
        "v_cvt_pk_bf16_f32 v66, v92, v93\n"
        "v_cvt_pk_bf16_f32 v67, v94, v95\n"
        "v_cvt_pk_bf16_f32 v68, v72, v73\n"
        "v_cvt_pk_bf16_f32 v69, v74, v75\n"
        "v_cvt_pk_bf16_f32 v70, v76, v77\n"
        "v_cvt_pk_bf16_f32 v71, v78, v79\n"
        "v_cvt_pk_bf16_f32 v72, v100, v101\n"
        "v_cvt_pk_bf16_f32 v73, v102, v103\n"
        "v_cvt_pk_bf16_f32 v74, v104, v105\n"
        "v_cvt_pk_bf16_f32 v75, v106, v107\n"
        "v_cvt_pk_bf16_f32 v76, v80, v81\n"
        "v_cvt_pk_bf16_f32 v77, v82, v83\n"
        "v_cvt_pk_bf16_f32 v78, v84, v85\n"
        "v_cvt_pk_bf16_f32 v79, v86, v87\n"
        "v_cvt_pk_bf16_f32 v80, v96, v97\n"
        "v_cvt_pk_bf16_f32 v81, v98, v99\n"
        "v_cvt_pk_bf16_f32 v82, v108, v109\n"
        "v_cvt_pk_bf16_f32 v83, v110, v111\n"
        "ds_write_b128 v244, v[112:115]\n"
        "ds_write_b128 v245, v[64:67]\n"
        "ds_write_b128 v244, v[68:71] offset:1024\n"
        "ds_write_b128 v245, v[72:75] offset:1024\n"
        "ds_write_b128 v244, v[76:79] offset:2048\n"
        "ds_write_b128 v245, v[80:83] offset:2048\n"
        "s_mov_b64 exec, 1\n"
        "ds_inc_u32 v239, v239 offset:0\n"
        "s_mov_b64 exec, -1\n"
        "s_setpc_b64 s[30:31]\n"
        "FOLDR_%=:\n"
        "ds_read_b64_tr_b16 v[84:85], v242 offset:0\n"
        "ds_read_b64_tr_b16 v[86:87], v243 offset:0\n"
        "ds_read_b64_tr_b16 v[92:93], v242 offset:6144\n"
        "ds_read_b64_tr_b16 v[94:95], v243 offset:6144\n"
        "ds_read_b64_tr_b16 v[80:81], v242 offset:12288\n"
        "ds_read_b64_tr_b16 v[82:83], v243 offset:12288\n"
        "ds_read_b64_tr_b16 v[76:77], v242 offset:8\n"
        "ds_read_b64_tr_b16 v[78:79], v243 offset:8\n"
        "ds_read_b64_tr_b16 v[68:69], v242 offset:6152\n"
        "ds_read_b64_tr_b16 v[70:71], v243 offset:6152\n"
        "ds_read_b64_tr_b16 v[72:73], v242 offset:12296\n"
        "ds_read_b64_tr_b16 v[74:75], v243 offset:12296\n"
        "v_mov_b32 v64, 0x3f803f80\n"
        "v_mov_b32 v65, 0x3f803f80\n"
        "v_mov_b32 v66, 0x3f803f80\n"
        "v_mov_b32 v67, 0x3f803f80\n"
        "ds_read_b64_tr_b16 v[88:89], v242 offset:64\n"
        "ds_read_b64_tr_b16 v[90:91], v243 offset:64\n"
        "s_waitcnt lgkmcnt(12)\n"
        "v_mfma_f32_16x16x32_bf16 v[104:107], v[84:87], v[64:67], 0\n"
        "s_waitcnt lgkmcnt(10)\n"
        "v_mfma_f32_16x16x32_bf16 v[104:107], v[92:95], v[64:67], v[104:107]\n"
        "s_waitcnt lgkmcnt(6)\n"
        "ds_read_b64_tr_b16 v[96:97], v242 offset:6208\n"
        "ds_read_b64_tr_b16 v[98:99], v243 offset:6208\n"
        "v_mfma_f32_16x16x32_bf16 v[104:107], v[80:83], v[64:67], v[104:107]\n"
        "ds_read_b64_tr_b16 v[92:93], v242 offset:12352\n"
        "ds_read_b64_tr_b16 v[94:95], v243 offset:12352\n"
        "v_mfma_f32_16x16x32_bf16 v[100:103], v[76:79], v[64:67], 0\n"
        "s_waitcnt lgkmcnt(6)\n"
        "ds_read_b64_tr_b16 v[80:81], v242 offset:72\n"
        "ds_read_b64_tr_b16 v[82:83], v243 offset:72\n"
        "v_mfma_f32_16x16x32_bf16 v[100:103], v[68:71], v[64:67], v[100:103]\n"
        "ds_read_b64_tr_b16 v[76:77], v242 offset:6216\n"
        "ds_read_b64_tr_b16 v[78:79], v243 offset:6216\n"
        "v_cvt_pk_bf16_f32 v68, v104, v105\n"
        "v_cvt_pk_bf16_f32 v69, v106, v107\n"
        "v_mfma_f32_16x16x32_bf16 v[100:103], v[72:75], v[64:67], v[100:103]\n"
        "s_waitcnt lgkmcnt(8)\n"
        "ds_read_b64_tr_b16 v[84:85], v242 offset:12360\n"
        "ds_read_b64_tr_b16 v[86:87], v243 offset:12360\n"
        "v_mfma_f32_16x16x32_bf16 v[104:107], v[88:91], v[64:67], 0\n"
        "ds_read_b64_tr_b16 v[72:73], v242 offset:128\n"
        "ds_read_b64_tr_b16 v[74:75], v243 offset:128\n"
        "s_waitcnt lgkmcnt(10)\n"
        "v_mfma_f32_16x16x32_bf16 v[104:107], v[96:99], v[64:67], v[104:107]\n"
        "ds_read_b64_tr_b16 v[88:89], v242 offset:6272\n"
        "ds_read_b64_tr_b16 v[90:91], v243 offset:6272\n"
        "v_cvt_pk_bf16_f32 v70, v100, v101\n"
        "v_cvt_pk_bf16_f32 v71, v102, v103\n"
        "s_waitcnt lgkmcnt(9)\n"
        "v_mfma_f32_16x16x32_bf16 v[104:107], v[92:95], v[64:67], v[104:107]\n"
        "s_waitcnt lgkmcnt(8)\n"
        "ds_read_b64_tr_b16 v[100:101], v242 offset:12416\n"
        "ds_read_b64_tr_b16 v[102:103], v243 offset:12416\n"
        "v_mfma_f32_16x16x32_bf16 v[108:111], v[80:83], v[64:67], 0\n"
        "s_waitcnt lgkmcnt(8)\n"
        "ds_read_b64_tr_b16 v[92:93], v242 offset:136\n"
        "ds_read_b64_tr_b16 v[94:95], v243 offset:136\n"
        "v_mfma_f32_16x16x32_bf16 v[108:111], v[76:79], v[64:67], v[108:111]\n"
        "ds_read_b64_tr_b16 v[96:97], v242 offset:6280\n"
        "ds_read_b64_tr_b16 v[98:99], v243 offset:6280\n"
        "v_cvt_pk_bf16_f32 v80, v104, v105\n"
        "v_cvt_pk_bf16_f32 v81, v106, v107\n"
        "s_waitcnt lgkmcnt(8)\n"
        "v_mfma_f32_16x16x32_bf16 v[108:111], v[84:87], v[64:67], v[108:111]\n"
        "ds_read_b64_tr_b16 v[76:77], v242 offset:12424\n"
        "ds_read_b64_tr_b16 v[78:79], v243 offset:12424\n"
        "v_mfma_f32_16x16x32_bf16 v[116:119], v[72:75], v[64:67], 0\n"
        "s_waitcnt lgkmcnt(8)\n"
        "ds_read_b128 v[72:75], v240\n"
        "v_mfma_f32_16x16x32_bf16 v[116:119], v[88:91], v[64:67], v[116:119]\n"
        "ds_read_b128 v[104:107], v241\n"
        "s_waitcnt lgkmcnt(8)\n"
        "v_mfma_f32_16x16x32_bf16 v[116:119], v[100:103], v[64:67], v[116:119]\n"
        "ds_read_b128 v[84:87], v240 offset:6144\n"
        "v_cvt_pk_bf16_f32 v82, v108, v109\n"
        "v_cvt_pk_bf16_f32 v83, v110, v111\n"
        "s_waitcnt lgkmcnt(7)\n"
        "v_mfma_f32_16x16x32_bf16 v[112:115], v[92:95], v[64:67], 0\n"
        "s_waitcnt lgkmcnt(5)\n"
        "ds_read_b128 v[92:95], v241 offset:6144\n"
        "v_mfma_f32_16x16x32_bf16 v[112:115], v[96:99], v[64:67], v[112:115]\n"
        "ds_read_b128 v[100:103], v240 offset:12288\n"
        "v_cvt_pk_bf16_f32 v88, v116, v117\n"
        "v_cvt_pk_bf16_f32 v89, v118, v119\n"
        "s_waitcnt lgkmcnt(5)\n"
        "v_mfma_f32_16x16x32_bf16 v[112:115], v[76:79], v[64:67], v[112:115]\n"
        "s_waitcnt lgkmcnt(4)\n"
        "ds_read_b128 v[116:119], v241 offset:12288\n"
        "v_mfma_f32_16x16x32_bf16 v[64:67], v[68:71], v[72:75], 0\n"
        "s_waitcnt lgkmcnt(4)\n"
        "ds_read_b128 v[120:123], v240 offset:64\n"
        "v_mfma_f32_16x16x32_bf16 v[72:75], v[68:71], v[104:107], 0\n"
        "ds_read_b128 v[108:111], v241 offset:64\n"
        "s_waitcnt lgkmcnt(5)\n"
        "v_mfma_f32_16x16x32_bf16 v[76:79], v[68:71], v[84:87], 0\n"
        "ds_read_b128 v[104:107], v240 offset:6208\n"
        "v_cvt_pk_bf16_f32 v90, v112, v113\n"
        "v_cvt_pk_bf16_f32 v91, v114, v115\n"
        "s_waitcnt lgkmcnt(5)\n"
        "v_mfma_f32_16x16x32_bf16 v[84:87], v[68:71], v[92:95], 0\n"
        "ds_read_b128 v[124:127], v241 offset:6208\n"
        "s_waitcnt lgkmcnt(5)\n"
        "v_mfma_f32_16x16x32_bf16 v[92:95], v[68:71], v[100:103], 0\n"
        "ds_read_b128 v[112:115], v240 offset:12352\n"
        "s_waitcnt lgkmcnt(5)\n"
        "v_mfma_f32_16x16x32_bf16 v[96:99], v[68:71], v[116:119], 0\n"
        "ds_read_b128 v[100:103], v241 offset:12352\n"
        "s_waitcnt lgkmcnt(5)\n"
        "v_mfma_f32_16x16x32_bf16 v[64:67], v[80:83], v[120:123], v[64:67]\n"
        "ds_read_b128 v[68:71], v240 offset:128\n"
        "s_waitcnt lgkmcnt(5)\n"
        "v_mfma_f32_16x16x32_bf16 v[72:75], v[80:83], v[108:111], v[72:75]\n"
        "ds_read_b128 v[116:119], v241 offset:128\n"
        "s_waitcnt lgkmcnt(5)\n"
        "v_mfma_f32_16x16x32_bf16 v[76:79], v[80:83], v[104:107], v[76:79]\n"
        "ds_read_b128 v[108:111], v240 offset:6272\n"
        "s_waitcnt lgkmcnt(5)\n"
        "v_mfma_f32_16x16x32_bf16 v[84:87], v[80:83], v[124:127], v[84:87]\n"
        "ds_read_b128 v[104:107], v241 offset:6272\n"
        "s_waitcnt lgkmcnt(5)\n"
        "v_mfma_f32_16x16x32_bf16 v[92:95], v[80:83], v[112:115], v[92:95]\n"
        "ds_read_b128 v[120:123], v240 offset:12416\n"
        "s_waitcnt lgkmcnt(5)\n"
        "v_mfma_f32_16x16x32_bf16 v[96:99], v[80:83], v[100:103], v[96:99]\n"
        "ds_read_b128 v[112:115], v241 offset:12416\n"
        "s_waitcnt lgkmcnt(5)\n"
        "v_mfma_f32_16x16x32_bf16 v[64:67], v[88:91], v[68:71], v[64:67]\n"
        "s_waitcnt lgkmcnt(4)\n"
        "v_mfma_f32_16x16x32_bf16 v[72:75], v[88:91], v[116:119], v[72:75]\n"
        "v_mov_b32 v239, s35\n"
        "s_waitcnt lgkmcnt(3)\n"
        "v_mfma_f32_16x16x32_bf16 v[76:79], v[88:91], v[108:111], v[76:79]\n"
        "s_waitcnt vmcnt(6)\n"
        "s_mov_b64 exec, 1\n"
        "ds_inc_u32 v239, v239 offset:0\n"
        "s_mov_b64 exec, -1\n"
        "s_waitcnt lgkmcnt(3)\n"
        "v_mfma_f32_16x16x32_bf16 v[84:87], v[88:91], v[104:107], v[84:87]\n"
        "v_add_f32 v238, v64, v72\n"
        "s_waitcnt lgkmcnt(2)\n"
        "v_mfma_f32_16x16x32_bf16 v[92:95], v[88:91], v[120:123], v[92:95]\n"
        "v_add_f32 v238, v238, v76\n"
        "s_waitcnt lgkmcnt(1)\n"
        "v_mfma_f32_16x16x32_bf16 v[96:99], v[88:91], v[112:115], v[96:99]\n"
        "v_mov_b32 v239, s37\n"
        "s_nop 0\n"
        "v_add_f32 v238, v238, v84\n"
        "s_nop 1\n"
        "v_add_f32 v238, v238, v92\n"
        "s_nop 1\n"
        "v_add_f32 v238, v238, v96\n"
        "s_nop 1\n"
        "v_add_f32_dpp v238, v238, v238 row_ror:8 row_mask:0xf bank_mask:0xf\n"
        "s_nop 1\n"
        "v_add_f32_dpp v238, v238, v238 row_ror:4 row_mask:0xf bank_mask:0xf\n"
        "s_nop 1\n"
        "v_add_f32_dpp v238, v238, v238 row_ror:2 row_mask:0xf bank_mask:0xf\n"
        "s_nop 1\n"
        "v_add_f32_dpp v238, v238, v238 row_ror:1 row_mask:0xf bank_mask:0xf\n"
        "v_mul_f32 v238, 0xbc2aaaab, v238\n"
        "v_mul_f32 v238, s34, v238\n"
        "v_fma_f32 v64, v64, s34, v238\n"
        "v_fma_f32 v72, v72, s34, v238\n"
        "v_fma_f32 v76, v76, s34, v238\n"
        "v_fma_f32 v84, v84, s34, v238\n"
        "v_fma_f32 v92, v92, s34, v238\n"
        "v_fma_f32 v96, v96, s34, v238\n"
        "ds_write_b32 v244, v64\n"
        "ds_write_b32 v244, v72 offset:16\n"
        "ds_write_b32 v244, v76 offset:128\n"
        "ds_write_b32 v244, v84 offset:144\n"
        "ds_write_b32 v244, v92 offset:256\n"
        "ds_write_b32 v244, v96 offset:272\n"
        "s_mov_b64 exec, 1\n"
        "ds_inc_u32 v239, v239 offset:0\n"
        "s_mov_b64 exec, -1\n"
        "s_setpc_b64 s[30:31]\n"
        "FOLDW_%=:\n"
        "ds_read_b64_tr_b16 v[64:65], v242 offset:0\n"
        "ds_read_b64_tr_b16 v[66:67], v243 offset:0\n"
        "ds_read_b128 v[172:175], v240\n"
        "ds_read_b64_tr_b16 v[180:181], v242 offset:6144\n"
        "ds_read_b64_tr_b16 v[182:183], v243 offset:6144\n"
        "ds_read_b128 v[120:123], v240 offset:64\n"
        "ds_read_b64_tr_b16 v[184:185], v242 offset:12288\n"
        "ds_read_b64_tr_b16 v[186:187], v243 offset:12288\n"
        "ds_read_b128 v[168:171], v240 offset:128\n"
        "ds_read_b128 v[160:163], v241\n"
        "ds_read_b128 v[148:151], v241 offset:64\n"
        "ds_read_b128 v[132:135], v241 offset:128\n"
        "ds_read_b128 v[124:127], v240 offset:6144\n"
        "ds_read_b128 v[104:107], v240 offset:6208\n"
        "ds_read_b128 v[92:95], v240 offset:6272\n"
        "ds_read_b128 v[96:99], v241 offset:6144\n"
        "ds_read_b128 v[88:91], v241 offset:6208\n"
        "ds_read_b128 v[84:87], v241 offset:6272\n"
        "s_waitcnt lgkmcnt(15)\n"
        "v_mfma_f32_16x16x32_bf16 v[136:139], v[172:175], v[64:67], 0\n"
        "ds_read_b128 v[100:103], v240 offset:12288\n"
        "s_waitcnt lgkmcnt(12)\n"
        "v_mfma_f32_16x16x32_bf16 v[136:139], v[120:123], v[180:183], v[136:139]\n"
        "s_waitcnt lgkmcnt(10)\n"
        "ds_read_b128 v[108:111], v240 offset:12352\n"
        "v_mfma_f32_16x16x32_bf16 v[136:139], v[168:171], v[184:187], v[136:139]\n"
        "s_waitcnt lgkmcnt(7)\n"
        "ds_read_b128 v[68:71], v240 offset:12416\n"
        "v_mfma_f32_16x16x32_bf16 v[140:143], v[160:163], v[64:67], 0\n"
        "ds_read_b128 v[72:75], v241 offset:12288\n"
        "v_mfma_f32_16x16x32_bf16 v[140:143], v[148:151], v[180:183], v[140:143]\n"
        "ds_read_b128 v[76:79], v241 offset:12352\n"
        "v_mfma_f32_16x16x32_bf16 v[140:143], v[132:135], v[184:187], v[140:143]\n"
        "ds_read_b128 v[80:83], v241 offset:12416\n"
        "v_mfma_f32_16x16x32_bf16 v[144:147], v[124:127], v[64:67], 0\n"
        "s_waitcnt lgkmcnt(6)\n"
        "ds_read_b64_tr_b16 v[112:113], v242 offset:8\n"
        "ds_read_b64_tr_b16 v[114:115], v243 offset:8\n"
        "v_mfma_f32_16x16x32_bf16 v[144:147], v[104:107], v[180:183], v[144:147]\n"
        "ds_read_b64_tr_b16 v[116:117], v242 offset:6152\n"
        "ds_read_b64_tr_b16 v[118:119], v243 offset:6152\n"
        "v_add_f32 v164, v136, v140\n"
        "v_add_f32 v165, v137, v141\n"
        "v_add_f32 v166, v138, v142\n"
        "v_add_f32 v167, v139, v143\n"
        "v_mfma_f32_16x16x32_bf16 v[144:147], v[92:95], v[184:187], v[144:147]\n"
        "ds_read_b64_tr_b16 v[128:129], v242 offset:12296\n"
        "ds_read_b64_tr_b16 v[130:131], v243 offset:12296\n"
        "v_mfma_f32_16x16x32_bf16 v[152:155], v[96:99], v[64:67], 0\n"
        "v_mfma_f32_16x16x32_bf16 v[152:155], v[88:91], v[180:183], v[152:155]\n"
        "v_mfma_f32_16x16x32_bf16 v[152:155], v[84:87], v[184:187], v[152:155]\n"
        "s_waitcnt lgkmcnt(6)\n"
        "v_mfma_f32_16x16x32_bf16 v[156:159], v[100:103], v[64:67], 0\n"
        "v_mfma_f32_16x16x32_bf16 v[156:159], v[108:111], v[180:183], v[156:159]\n"
        "v_add_f32 v164, v164, v144\n"
        "v_add_f32 v165, v165, v145\n"
        "v_add_f32 v166, v166, v146\n"
        "v_add_f32 v167, v167, v147\n"
        "v_mfma_f32_16x16x32_bf16 v[156:159], v[68:71], v[184:187], v[156:159]\n"
        "v_add_f32 v164, v164, v152\n"
        "v_add_f32 v165, v165, v153\n"
        "v_add_f32 v166, v166, v154\n"
        "v_add_f32 v167, v167, v155\n"
        "v_mfma_f32_16x16x32_bf16 v[176:179], v[72:75], v[64:67], 0\n"
        "v_mfma_f32_16x16x32_bf16 v[176:179], v[76:79], v[180:183], v[176:179]\n"
        "v_mfma_f32_16x16x32_bf16 v[176:179], v[80:83], v[184:187], v[176:179]\n"
        "s_waitcnt lgkmcnt(0)\n"
        "v_mov_b32 v239, s35\n"
        "v_add_f32 v164, v164, v156\n"
        "v_add_f32 v165, v165, v157\n"
        "v_add_f32 v166, v166, v158\n"
        "v_add_f32 v167, v167, v159\n"
        "s_waitcnt vmcnt(6)\n"
        "s_mov_b64 exec, 1\n"
        "ds_inc_u32 v239, v239 offset:0\n"
        "s_mov_b64 exec, -1\n"
        "v_add_f32 v164, v164, v176\n"
        "v_add_f32 v165, v165, v177\n"
        "v_mfma_f32_16x16x32_bf16 v[64:67], v[172:175], v[112:115], 0\n"
        "v_add_f32 v166, v166, v178\n"
        "v_add_f32 v167, v167, v179\n"
        "v_add_f32 v164, v164, v165\n"
        "v_add_f32 v166, v166, v167\n"
        "v_add_f32 v164, v164, v166\n"
        "v_mov_b32 v165, v164\n"
        "v_mfma_f32_16x16x32_bf16 v[64:67], v[120:123], v[116:119], v[64:67]\n"
        "v_mfma_f32_16x16x32_bf16 v[64:67], v[168:171], v[128:131], v[64:67]\n"
        "v_permlane32_swap_b32 v164, v165\n"
        "v_add_f32 v164, v164, v165\n"
        "v_mov_b32 v165, v164\n"
        "v_mfma_f32_16x16x32_bf16 v[120:123], v[160:163], v[112:115], 0\n"
        "global_load_dwordx4 v[40:43], v57, s[22:23] offset:-3072\n"
        "v_permlane16_swap_b32 v164, v165\n"
        "v_add_f32 v164, v164, v165\n"
        "v_mul_f32 v164, 0xbc2aaaab, v164\n"
        "v_mul_f32 v164, s34, v164\n"
        "v_fma_f32 v136, v136, s34, v164\n"
        "v_fma_f32 v137, v137, s34, v164\n"
        "v_fma_f32 v138, v138, s34, v164\n"
        "v_fma_f32 v139, v139, s34, v164\n"
        "v_fma_f32 v140, v140, s34, v164\n"
        "v_fma_f32 v141, v141, s34, v164\n"
        "v_mfma_f32_16x16x32_bf16 v[120:123], v[148:151], v[116:119], v[120:123]\n"
        "v_fma_f32 v142, v142, s34, v164\n"
        "v_fma_f32 v143, v143, s34, v164\n"
        "v_fma_f32 v144, v144, s34, v164\n"
        "v_fma_f32 v145, v145, s34, v164\n"
        "v_fma_f32 v146, v146, s34, v164\n"
        "v_fma_f32 v147, v147, s34, v164\n"
        "v_fma_f32 v152, v152, s34, v164\n"
        "v_fma_f32 v153, v153, s34, v164\n"
        "v_fma_f32 v154, v154, s34, v164\n"
        "v_fma_f32 v155, v155, s34, v164\n"
        "v_fma_f32 v156, v156, s34, v164\n"
        "v_fma_f32 v157, v157, s34, v164\n"
        "v_fma_f32 v158, v158, s34, v164\n"
        "v_fma_f32 v159, v159, s34, v164\n"
        "v_mfma_f32_16x16x32_bf16 v[120:123], v[132:135], v[128:131], v[120:123]\n"
        "global_load_dwordx4 v[44:47], v57, s[22:23] offset:-2048\n"
        "v_fma_f32 v176, v176, s34, v164\n"
        "v_fma_f32 v177, v177, s34, v164\n"
        "v_fma_f32 v178, v178, s34, v164\n"
        "v_fma_f32 v179, v179, s34, v164\n"
        "v_cvt_pk_bf16_f32 v132, v136, v137\n"
        "v_cvt_pk_bf16_f32 v133, v138, v139\n"
        "v_cvt_pk_bf16_f32 v134, v140, v141\n"
        "v_add_f32 v148, v64, v120\n"
        "v_add_f32 v149, v65, v121\n"
        "v_add_f32 v150, v66, v122\n"
        "v_add_f32 v151, v67, v123\n"
        "v_cvt_pk_bf16_f32 v135, v142, v143\n"
        "v_mfma_f32_16x16x32_bf16 v[140:143], v[124:127], v[112:115], 0\n"
        "v_cvt_pk_bf16_f32 v136, v144, v145\n"
        "v_cvt_pk_bf16_f32 v137, v146, v147\n"
        "v_cvt_pk_bf16_f32 v138, v152, v153\n"
        "v_cvt_pk_bf16_f32 v139, v154, v155\n"
        "v_cvt_pk_bf16_f32 v124, v156, v157\n"
        "v_cvt_pk_bf16_f32 v125, v158, v159\n"
        "v_cvt_pk_bf16_f32 v126, v176, v177\n"
        "v_cvt_pk_bf16_f32 v127, v178, v179\n"
        "v_mfma_f32_16x16x32_bf16 v[140:143], v[104:107], v[116:119], v[140:143]\n"
        "global_load_dwordx4 v[0:3], v57, s[22:23] offset:-1024\n"
        "v_mfma_f32_16x16x32_bf16 v[140:143], v[92:95], v[128:131], v[140:143]\n"
        "v_mfma_f32_16x16x32_bf16 v[104:107], v[96:99], v[112:115], 0\n"
        "global_load_dwordx4 v[4:7], v57, s[22:23] offset:0\n"
        "v_mfma_f32_16x16x32_bf16 v[104:107], v[88:91], v[116:119], v[104:107]\n"
        "v_mfma_f32_16x16x32_bf16 v[104:107], v[84:87], v[128:131], v[104:107]\n"
        "global_load_dwordx4 v[8:11], v57, s[22:23] offset:1024\n"
        "v_mfma_f32_16x16x32_bf16 v[84:87], v[100:103], v[112:115], 0\n"
        "v_mfma_f32_16x16x32_bf16 v[84:87], v[108:111], v[116:119], v[84:87]\n"
        "global_load_dwordx4 v[12:15], v57, s[22:23] offset:2048\n"
        "v_add_f32 v148, v148, v140\n"
        "v_add_f32 v149, v149, v141\n"
        "v_add_f32 v150, v150, v142\n"
        "v_add_f32 v151, v151, v143\n"
        "v_add_f32 v148, v148, v104\n"
        "v_add_f32 v149, v149, v105\n"
        "v_add_f32 v150, v150, v106\n"
        "v_add_f32 v151, v151, v107\n"
        "v_mfma_f32_16x16x32_bf16 v[84:87], v[68:71], v[128:131], v[84:87]\n"
        "v_mfma_f32_16x16x32_bf16 v[88:91], v[72:75], v[112:115], 0\n"
        "global_load_dwordx4 v[16:19], v57, s[22:23] offset:3072\n"
        "v_mfma_f32_16x16x32_bf16 v[88:91], v[76:79], v[116:119], v[88:91]\n"
        "v_mfma_f32_16x16x32_bf16 v[88:91], v[80:83], v[128:131], v[88:91]\n"
        "v_mov_b32 v239, s37\n"
        "s_nop 2\n"
        "v_add_f32 v148, v148, v84\n"
        "v_add_f32 v149, v149, v85\n"
        "v_add_f32 v150, v150, v86\n"
        "v_add_f32 v151, v151, v87\n"
        "v_add_f32 v148, v148, v88\n"
        "v_add_f32 v149, v149, v89\n"
        "v_add_f32 v150, v150, v90\n"
        "v_add_f32 v151, v151, v91\n"
        "v_add_f32 v148, v148, v149\n"
        "v_add_f32 v150, v150, v151\n"
        "v_add_f32 v148, v148, v150\n"
        "v_mov_b32 v149, v148\n"
        "s_nop 1\n"
        "v_permlane32_swap_b32 v148, v149\n"
        "v_add_f32 v148, v148, v149\n"
        "v_mov_b32 v149, v148\n"
        "s_nop 1\n"
        "v_permlane16_swap_b32 v148, v149\n"
        "v_add_f32 v148, v148, v149\n"
        "v_mul_f32 v148, 0xbc2aaaab, v148\n"
        "v_mul_f32 v148, s34, v148\n"
        "v_fma_f32 v64, v64, s34, v148\n"
        "v_fma_f32 v65, v65, s34, v148\n"
        "v_fma_f32 v66, v66, s34, v148\n"
        "v_fma_f32 v67, v67, s34, v148\n"
        "v_fma_f32 v120, v120, s34, v148\n"
        "v_fma_f32 v121, v121, s34, v148\n"
        "v_fma_f32 v122, v122, s34, v148\n"
        "v_fma_f32 v123, v123, s34, v148\n"
        "v_fma_f32 v140, v140, s34, v148\n"
        "v_fma_f32 v141, v141, s34, v148\n"
        "v_fma_f32 v142, v142, s34, v148\n"
        "v_fma_f32 v143, v143, s34, v148\n"
        "v_fma_f32 v104, v104, s34, v148\n"
        "v_fma_f32 v105, v105, s34, v148\n"
        "v_fma_f32 v106, v106, s34, v148\n"
        "v_fma_f32 v107, v107, s34, v148\n"
        "v_fma_f32 v84, v84, s34, v148\n"
        "v_fma_f32 v85, v85, s34, v148\n"
        "v_fma_f32 v86, v86, s34, v148\n"
        "v_fma_f32 v87, v87, s34, v148\n"
        "v_fma_f32 v88, v88, s34, v148\n"
        "v_fma_f32 v89, v89, s34, v148\n"
        "v_fma_f32 v90, v90, s34, v148\n"
        "v_fma_f32 v91, v91, s34, v148\n"
        "v_cvt_pk_bf16_f32 v68, v64, v65\n"
        "v_cvt_pk_bf16_f32 v69, v66, v67\n"
        "v_cvt_pk_bf16_f32 v70, v120, v121\n"
        "v_cvt_pk_bf16_f32 v71, v122, v123\n"
        "v_cvt_pk_bf16_f32 v64, v140, v141\n"
        "v_cvt_pk_bf16_f32 v65, v142, v143\n"
        "v_cvt_pk_bf16_f32 v66, v104, v105\n"
        "v_cvt_pk_bf16_f32 v67, v106, v107\n"
        "v_cvt_pk_bf16_f32 v72, v84, v85\n"
        "v_cvt_pk_bf16_f32 v73, v86, v87\n"
        "v_cvt_pk_bf16_f32 v74, v88, v89\n"
        "v_cvt_pk_bf16_f32 v75, v90, v91\n"
        "s_nop 1\n"
        "v_mov_b32 v238, s36\n"
        "L62_%=:\n"
        "ds_read_b32 v237, v238 offset:0\n"
        "s_waitcnt lgkmcnt(0)\n"
        "v_readfirstlane_b32 s40, v237\n"
        "s_nop 3\n"
        "s_cmp_ge_i32 s40, 4\n"
        "s_cbranch_scc1 L63_%=\n"
        "s_nop 0\n"
        "s_sub_u32 s41, s41, 1\n"
        "s_cbranch_scc0 L62_%=\n"
        "s_endpgm\n"
        "L63_%=:\n"
        "ds_write_b128 v244, v[132:135]\n"
        "ds_write_b128 v245, v[68:71]\n"
        "ds_write_b128 v244, v[136:139] offset:1024\n"
        "ds_write_b128 v245, v[64:67] offset:1024\n"
        "ds_write_b128 v244, v[124:127] offset:2048\n"
        "ds_write_b128 v245, v[72:75] offset:2048\n"
        "s_mov_b64 exec, 1\n"
        "ds_inc_u32 v239, v239 offset:0\n"
        "s_mov_b64 exec, -1\n"
        "s_setpc_b64 s[30:31]\n"
        "FOLDU_%=:\n"
        "ds_read_b128 v[92:95], v242\n"
        "ds_read_b128 v[88:91], v242 offset:64\n"
        "ds_read_b128 v[84:87], v242 offset:128\n"
        "ds_read_b128 v[80:83], v243\n"
        "ds_read_b128 v[68:71], v243 offset:64\n"
        "ds_read_b128 v[72:75], v243 offset:128\n"
        "v_mov_b32 v64, 0x3f803f80\n"
        "v_mov_b32 v65, 0x3f803f80\n"
        "v_mov_b32 v66, 0x3f803f80\n"
        "v_mov_b32 v67, 0x3f803f80\n"
        "ds_read_b128 v[76:79], v242 offset:6144\n"
        "s_waitcnt lgkmcnt(6)\n"
        "v_mfma_f32_16x16x32_bf16 v[100:103], v[92:95], v[64:67], 0\n"
        "s_waitcnt lgkmcnt(4)\n"
        "v_mfma_f32_16x16x32_bf16 v[100:103], v[88:91], v[64:67], v[100:103]\n"
        "ds_read_b128 v[96:99], v242 offset:6208\n"
        "v_mfma_f32_16x16x32_bf16 v[100:103], v[84:87], v[64:67], v[100:103]\n"
        "s_waitcnt lgkmcnt(1)\n"
        "ds_read_b128 v[92:95], v242 offset:6272\n"
        "v_mfma_f32_16x16x32_bf16 v[104:107], v[80:83], v[64:67], 0\n"
        "ds_read_b128 v[88:91], v243 offset:6144\n"
        "v_mfma_f32_16x16x32_bf16 v[104:107], v[68:71], v[64:67], v[104:107]\n"
        "ds_read_b128 v[80:83], v243 offset:6208\n"
        "v_mfma_f32_16x16x32_bf16 v[104:107], v[72:75], v[64:67], v[104:107]\n"
        "ds_read_b128 v[68:71], v243 offset:6272\n"
        "v_cvt_pk_bf16_f32 v84, v100, v101\n"
        "v_cvt_pk_bf16_f32 v85, v102, v103\n"
        "v_mfma_f32_16x16x32_bf16 v[100:103], v[76:79], v[64:67], 0\n"
        "ds_read_b128 v[72:75], v242 offset:12288\n"
        "s_waitcnt lgkmcnt(5)\n"
        "v_mfma_f32_16x16x32_bf16 v[100:103], v[96:99], v[64:67], v[100:103]\n"
        "ds_read_b128 v[76:79], v242 offset:12352\n"
        "v_cvt_pk_bf16_f32 v86, v104, v105\n"
        "v_cvt_pk_bf16_f32 v87, v106, v107\n"
        "s_waitcnt lgkmcnt(5)\n"
        "v_mfma_f32_16x16x32_bf16 v[100:103], v[92:95], v[64:67], v[100:103]\n"
        "s_waitcnt lgkmcnt(4)\n"
        "ds_read_b128 v[104:107], v242 offset:12416\n"
        "v_mfma_f32_16x16x32_bf16 v[96:99], v[88:91], v[64:67], 0\n"
        "s_waitcnt lgkmcnt(4)\n"
        "ds_read_b128 v[88:91], v243 offset:12288\n"
        "v_mfma_f32_16x16x32_bf16 v[96:99], v[80:83], v[64:67], v[96:99]\n"
        "s_waitcnt lgkmcnt(4)\n"
        "ds_read_b128 v[80:83], v243 offset:12352\n"
        "v_cvt_pk_bf16_f32 v92, v100, v101\n"
        "v_cvt_pk_bf16_f32 v93, v102, v103\n"
        "v_mfma_f32_16x16x32_bf16 v[96:99], v[68:71], v[64:67], v[96:99]\n"
        "s_waitcnt lgkmcnt(4)\n"
        "ds_read_b128 v[100:103], v243 offset:12416\n"
        "v_mfma_f32_16x16x32_bf16 v[112:115], v[72:75], v[64:67], 0\n"
        "ds_read_b128 v[68:71], v240\n"
        "s_waitcnt lgkmcnt(5)\n"
        "v_mfma_f32_16x16x32_bf16 v[112:115], v[76:79], v[64:67], v[112:115]\n"
        "ds_read_b128 v[72:75], v241\n"
        "s_waitcnt lgkmcnt(5)\n"
        "v_mfma_f32_16x16x32_bf16 v[112:115], v[104:107], v[64:67], v[112:115]\n"
        "ds_read_b128 v[76:79], v240 offset:6144\n"
        "v_cvt_pk_bf16_f32 v94, v96, v97\n"
        "v_cvt_pk_bf16_f32 v95, v98, v99\n"
        "s_waitcnt lgkmcnt(5)\n"
        "v_mfma_f32_16x16x32_bf16 v[108:111], v[88:91], v[64:67], 0\n"
        "s_waitcnt lgkmcnt(4)\n"
        "ds_read_b128 v[104:107], v241 offset:6144\n"
        "v_mfma_f32_16x16x32_bf16 v[108:111], v[80:83], v[64:67], v[108:111]\n"
        "ds_read_b128 v[88:91], v240 offset:12288\n"
        "v_cvt_pk_bf16_f32 v96, v112, v113\n"
        "v_cvt_pk_bf16_f32 v97, v114, v115\n"
        "s_waitcnt lgkmcnt(5)\n"
        "v_mfma_f32_16x16x32_bf16 v[108:111], v[100:103], v[64:67], v[108:111]\n"
        "s_waitcnt lgkmcnt(4)\n"
        "ds_read_b128 v[116:119], v241 offset:12288\n"
        "v_mfma_f32_16x16x32_bf16 v[64:67], v[84:87], v[68:71], 0\n"
        "s_waitcnt lgkmcnt(4)\n"
        "ds_read_b128 v[120:123], v240 offset:64\n"
        "v_mfma_f32_16x16x32_bf16 v[68:71], v[84:87], v[72:75], 0\n"
        "ds_read_b128 v[100:103], v241 offset:64\n"
        "s_waitcnt lgkmcnt(5)\n"
        "v_mfma_f32_16x16x32_bf16 v[72:75], v[84:87], v[76:79], 0\n"
        "ds_read_b128 v[112:115], v240 offset:6208\n"
        "v_cvt_pk_bf16_f32 v98, v108, v109\n"
        "v_cvt_pk_bf16_f32 v99, v110, v111\n"
        "s_waitcnt lgkmcnt(5)\n"
        "v_mfma_f32_16x16x32_bf16 v[76:79], v[84:87], v[104:107], 0\n"
        "ds_read_b128 v[108:111], v241 offset:6208\n"
        "s_waitcnt lgkmcnt(5)\n"
        "v_mfma_f32_16x16x32_bf16 v[80:83], v[84:87], v[88:91], 0\n"
        "ds_read_b128 v[104:107], v240 offset:12352\n"
        "s_waitcnt lgkmcnt(5)\n"
        "v_mfma_f32_16x16x32_bf16 v[88:91], v[84:87], v[116:119], 0\n"
        "ds_read_b128 v[124:127], v241 offset:12352\n"
        "s_waitcnt lgkmcnt(5)\n"
        "v_mfma_f32_16x16x32_bf16 v[64:67], v[92:95], v[120:123], v[64:67]\n"
        "ds_read_b128 v[84:87], v240 offset:128\n"
        "s_waitcnt lgkmcnt(5)\n"
        "v_mfma_f32_16x16x32_bf16 v[68:71], v[92:95], v[100:103], v[68:71]\n"
        "ds_read_b128 v[116:119], v241 offset:128\n"
        "s_waitcnt lgkmcnt(5)\n"
        "v_mfma_f32_16x16x32_bf16 v[72:75], v[92:95], v[112:115], v[72:75]\n"
        "ds_read_b128 v[100:103], v240 offset:6272\n"
        "s_waitcnt lgkmcnt(5)\n"
        "v_mfma_f32_16x16x32_bf16 v[76:79], v[92:95], v[108:111], v[76:79]\n"
        "ds_read_b128 v[112:115], v241 offset:6272\n"
        "s_waitcnt lgkmcnt(5)\n"
        "v_mfma_f32_16x16x32_bf16 v[80:83], v[92:95], v[104:107], v[80:83]\n"
        "ds_read_b128 v[108:111], v240 offset:12416\n"
        "s_waitcnt lgkmcnt(5)\n"
        "v_mfma_f32_16x16x32_bf16 v[88:91], v[92:95], v[124:127], v[88:91]\n"
        "ds_read_b128 v[104:107], v241 offset:12416\n"
        "s_waitcnt lgkmcnt(5)\n"
        "v_mfma_f32_16x16x32_bf16 v[64:67], v[96:99], v[84:87], v[64:67]\n"
        "s_waitcnt lgkmcnt(4)\n"
        "v_mfma_f32_16x16x32_bf16 v[68:71], v[96:99], v[116:119], v[68:71]\n"
        "v_mov_b32 v239, s35\n"
        "s_waitcnt lgkmcnt(3)\n"
        "v_mfma_f32_16x16x32_bf16 v[72:75], v[96:99], v[100:103], v[72:75]\n"
        "s_waitcnt vmcnt(6)\n"
        "s_mov_b64 exec, 1\n"
        "ds_inc_u32 v239, v239 offset:0\n"
        "s_mov_b64 exec, -1\n"
        "s_waitcnt lgkmcnt(3)\n"
        "v_mfma_f32_16x16x32_bf16 v[76:79], v[96:99], v[112:115], v[76:79]\n"
        "v_add_f32 v238, v64, v68\n"
        "s_waitcnt lgkmcnt(2)\n"
        "v_mfma_f32_16x16x32_bf16 v[80:83], v[96:99], v[108:111], v[80:83]\n"
        "v_add_f32 v238, v238, v72\n"
        "s_waitcnt lgkmcnt(1)\n"
        "v_mfma_f32_16x16x32_bf16 v[88:91], v[96:99], v[104:107], v[88:91]\n"
        "v_mov_b32 v239, s37\n"
        "s_nop 0\n"
        "v_add_f32 v238, v238, v76\n"
        "s_nop 1\n"
        "v_add_f32 v238, v238, v80\n"
        "s_nop 1\n"
        "v_add_f32 v238, v238, v88\n"
        "s_nop 1\n"
        "v_add_f32_dpp v238, v238, v238 row_ror:8 row_mask:0xf bank_mask:0xf\n"
        "s_nop 1\n"
        "v_add_f32_dpp v238, v238, v238 row_ror:4 row_mask:0xf bank_mask:0xf\n"
        "s_nop 1\n"
        "v_add_f32_dpp v238, v238, v238 row_ror:2 row_mask:0xf bank_mask:0xf\n"
        "s_nop 1\n"
        "v_add_f32_dpp v238, v238, v238 row_ror:1 row_mask:0xf bank_mask:0xf\n"
        "v_mul_f32 v238, 0xbc2aaaab, v238\n"
        "v_mul_f32 v238, s34, v238\n"
        "v_fma_f32 v64, v64, s34, v238\n"
        "v_fma_f32 v68, v68, s34, v238\n"
        "v_fma_f32 v72, v72, s34, v238\n"
        "v_fma_f32 v76, v76, s34, v238\n"
        "v_fma_f32 v80, v80, s34, v238\n"
        "v_fma_f32 v88, v88, s34, v238\n"
        "v_cvt_pk_bf16_f32 v64, v64, v64\n"
        "v_cvt_pk_bf16_f32 v68, v68, v68\n"
        "v_cvt_pk_bf16_f32 v72, v72, v72\n"
        "v_cvt_pk_bf16_f32 v76, v76, v76\n"
        "v_cvt_pk_bf16_f32 v80, v80, v80\n"
        "v_cvt_pk_bf16_f32 v88, v88, v88\n"
        "ds_write_b16 v244, v64\n"
        "ds_write_b16 v244, v68 offset:8\n"
        "ds_write_b16 v244, v72 offset:64\n"
        "ds_write_b16 v244, v76 offset:72\n"
        "ds_write_b16 v244, v80 offset:128\n"
        "ds_write_b16 v244, v88 offset:136\n"
        "s_mov_b64 exec, 1\n"
        "ds_inc_u32 v239, v239 offset:0\n"
        "s_mov_b64 exec, -1\n"
        "s_setpc_b64 s[30:31]\n"
        "PPRO_%=:\n"
        "s_add_u32 s64, s64, 128\n"
        "s_cmp_lg_u32 s6, 0\n"
        "s_cbranch_scc1 L78_%=\n"
        "s_mov_b64 exec, 0xffffffff\n"
        "v_lshlrev_b32 v27, 2, v56\n"
        "v_add_u32 v27, 110048, v27\n"
        "v_mov_b32 v28, 0\n"
        "ds_write_b32 v27, v28\n"
        "s_mov_b64 exec, 0x1fffff\n"
        "s_bfe_u32 s7, s2, 0x50003\n"
        "s_mul_i32 s7, s7, 2656\n"
        "v_lshlrev_b32 v27, 7, v56\n"
        "v_add_u32 v27, s7, v27\n"
        "global_load_dword v252, v27, s[4:5]\n"
        "s_mov_b64 exec, -1\n"
        "L78_%=:\n"
        "s_cmp_lt_u32 s6, 2\n"
        "s_cselect_b32 s65, 3, 1\n"
        "s_cselect_b32 s61, 384, 0\n"
        "s_cselect_b32 s62, 9, 1\n"
        "s_cselect_b32 s63, 3, 4\n"
        "s_cselect_b32 s26, 3, 1\n"
        "s_cselect_b32 s34, 9, 1\n"
        "s_cselect_b32 s70, 72, 1\n"
        "s_cselect_b32 s71, 36, 4\n"
        "s_cselect_b32 s72, 6, 8\n"
        "s_mul_i32 s70, s70, 192\n"
        "s_mul_i32 s71, s71, 192\n"
        "s_mul_i32 s72, s72, 192\n"
        "s_mul_i32 s65, s65, 192\n"
        "s_mul_i32 s62, s62, 3072\n"
        "s_mul_i32 s63, s63, 1536\n"
        "s_mul_i32 s26, s26, 18432\n"
        "s_mul_i32 s34, s34, 1536\n"
        "s_add_u32 s7, s62, s63\n"
        "s_lshl_b32 s29, s63, 1\n"
        "s_add_u32 s60, s7, s63\n"
        "s_bitcmp1_b32 s6, 0\n"
        "s_cselect_b32 s28, s7, 0\n"
        "s_cselect_b32 s29, s29, s62\n"
        "s_cselect_b32 s60, s60, s63\n"
        "s_add_u32 s28, s28, s61\n"
        "s_add_u32 s29, s29, s61\n"
        "s_add_u32 s60, s60, s61\n"
        "v_add_u32 v24, 0, v56\n"
        "v_mul_u32_u24 v21, 171, v24\n"
        "v_lshrrev_b32 v21, 11, v21\n"
        "v_mad_i32_i24 v24, v21, -12, v24\n"
        "v_bfe_u32 v58, v21, 2, 2\n"
        "v_xor_b32 v58, v24, v58\n"
        "v_and_b32 v27, 3, v21\n"
        "v_mul_u32_u24 v27, s70, v27\n"
        "v_lshl_add_u32 v32, v58, 4, v27\n"
        "v_bfe_u32 v27, v21, 2, 1\n"
        "v_mad_u32_u24 v32, v27, s71, v32\n"
        "v_lshrrev_b32 v27, 3, v21\n"
        "v_mad_u32_u24 v32, v27, s72, v32\n"
        "v_and_b32 v27, 7, v21\n"
        "v_mul_u32_u24 v27, s65, v27\n"
        "v_lshl_add_u32 v58, v58, 4, v27\n"
        "v_add_u32 v25, 64, v56\n"
        "v_mul_u32_u24 v22, 171, v25\n"
        "v_lshrrev_b32 v22, 11, v22\n"
        "v_mad_i32_i24 v25, v22, -12, v25\n"
        "v_bfe_u32 v59, v22, 2, 2\n"
        "v_xor_b32 v59, v25, v59\n"
        "v_and_b32 v27, 3, v22\n"
        "v_mul_u32_u24 v27, s70, v27\n"
        "v_lshl_add_u32 v33, v59, 4, v27\n"
        "v_bfe_u32 v27, v22, 2, 1\n"
        "v_mad_u32_u24 v33, v27, s71, v33\n"
        "v_lshrrev_b32 v27, 3, v22\n"
        "v_mad_u32_u24 v33, v27, s72, v33\n"
        "v_and_b32 v27, 7, v22\n"
        "v_mul_u32_u24 v27, s65, v27\n"
        "v_lshl_add_u32 v59, v59, 4, v27\n"
        "v_lshrrev_b32 v27, 3, v22\n"
        "v_mad_u32_u24 v59, v27, s34, v59\n"
        "v_add_u32 v26, 128, v56\n"
        "v_mul_u32_u24 v23, 171, v26\n"
        "v_lshrrev_b32 v23, 11, v23\n"
        "v_mad_i32_i24 v26, v23, -12, v26\n"
        "v_bfe_u32 v60, v23, 2, 2\n"
        "v_xor_b32 v60, v26, v60\n"
        "v_and_b32 v27, 3, v23\n"
        "v_mul_u32_u24 v27, s70, v27\n"
        "v_lshl_add_u32 v34, v60, 4, v27\n"
        "v_bfe_u32 v27, v23, 2, 1\n"
        "v_mad_u32_u24 v34, v27, s71, v34\n"
        "v_lshrrev_b32 v27, 3, v23\n"
        "v_mad_u32_u24 v34, v27, s72, v34\n"
        "v_and_b32 v27, 7, v23\n"
        "v_mul_u32_u24 v27, s65, v27\n"
        "v_lshl_add_u32 v60, v60, 4, v27\n"
        "v_add_u32 v60, s34, v60\n"
        "s_and_b32 s7, s2, 7\n"
        "s_lshl_b32 s7, s7, 5\n"
        "s_lshr_b32 s14, s2, 3\n"
        "s_add_u32 s7, s7, s14\n"
        "s_mul_i32 s7, s7, 20\n"
        "s_mov_b32 s14, 22\n"
        "s_mul_i32 s15, s7, 57457\n"
        "s_lshr_b32 s15, s15, 22\n"
        "s_mul_i32 s16, s15, 73\n"
        "s_sub_u32 s16, s7, s16\n"
        "s_mul_i32 s15, s15, 686784\n"
        "s_mul_i32 s7, s16, 1344\n"
        "s_add_u32 s15, s15, s7\n"
        "s_sub_u32 s7, 6, s6\n"
        "v_mov_b32 v28, 96768\n"
        "s_mov_b32 s35, 50135232\n"
        "v_mov_b32 v29, 2048\n"
        "v_and_b32 v21, 7, v21\n"
        "v_cmp_lt_u32 vcc, s7, v21\n"
        "v_and_b32 v27, 3, v24\n"
        "v_mad_u32_u24 v29, v27, 48, v29\n"
        "v_lshrrev_b32 v27, 2, v24\n"
        "v_lshl_add_u32 v29, v27, 4, v29\n"
        "v_mul_u32_u24 v21, 192, v21\n"
        "v_cndmask_b32 v27, 0, v28, vcc\n"
        "v_add3_u32 v29, v29, v21, v27\n"
        "v_lshrrev_b32 v27, 3, v22\n"
        "v_xor_b32 v25, v25, v27\n"
        "v_mul_lo_u32 v30, v27, s35\n"
        "v_add_u32 v30, 1024, v30\n"
        "v_and_b32 v22, 7, v22\n"
        "v_cmp_lt_u32 vcc, s7, v22\n"
        "v_and_b32 v27, 3, v25\n"
        "v_mad_u32_u24 v30, v27, 48, v30\n"
        "v_lshrrev_b32 v27, 2, v25\n"
        "v_lshl_add_u32 v30, v27, 4, v30\n"
        "v_mul_u32_u24 v22, 192, v22\n"
        "v_cndmask_b32 v27, 0, v28, vcc\n"
        "v_add3_u32 v30, v30, v22, v27\n"
        "v_mov_b32 v31, 50135232\n"
        "v_xor_b32 v26, 1, v26\n"
        "v_and_b32 v23, 7, v23\n"
        "v_cmp_lt_u32 vcc, s7, v23\n"
        "v_and_b32 v27, 3, v26\n"
        "v_mad_u32_u24 v31, v27, 48, v31\n"
        "v_lshrrev_b32 v27, 2, v26\n"
        "v_lshl_add_u32 v31, v27, 4, v31\n"
        "v_mul_u32_u24 v23, 192, v23\n"
        "v_cndmask_b32 v27, 0, v28, vcc\n"
        "v_add3_u32 v31, v31, v23, v27\n"
        "s_mul_i32 s17, s6, 6208\n"
        "s_add_u32 s17, s17, 110176\n"
        "s_mov_b32 s18, s6\n"
        "s_mul_i32 s35, s6, 98304\n"
        "s_add_u32 s35, s35, s15\n"
        "s_add_u32 s36, s35, 100270464\n"
        "v_mov_b32 v250, 110048\n"
        "s_mov_b32 s41, 0x400000\n"
        "s_waitcnt lgkmcnt(0)\n"
        "s_cmp_lg_u32 s6, 0\n"
        "s_cbranch_scc1 L79_%=\n"
        "s_bitcmp1_b32 s2, 7\n"
        "s_cselect_b64 s[8:9], s[50:51], s[46:47]\n"
        "s_cselect_b64 s[10:11], s[52:53], s[48:49]\n"
        "s_bfe_u32 s7, s2, 0x40003\n"
        "s_cmp_ge_u32 s7, 12\n"
        "s_cselect_b64 s[8:9], s[10:11], s[8:9]\n"
        "s_cselect_b32 s10, 12, 0\n"
        "s_sub_u32 s7, s7, s10\n"
        "s_mul_i32 s7, s7, 4608\n"
        "v_lshlrev_b32 v27, 7, v56\n"
        "v_add_u32 v27, s7, v27\n"
        "s_mov_b32 exec_hi, 0xf\n"
        "global_load_dword v252, v27, s[8:9]\n"
        "s_mov_b32 exec_hi, -1\n"
        "s_mov_b64 s[24:25], s[56:57]\n"
        "s_mov_b64 exec, 1\n"
        "v_mov_b32 v27, 0\n"
        "global_load_dword v253, v27, s[24:25] offset:256 sc1\n"
        "s_mov_b64 exec, -1\n"
        "L79_%=:\n"
        "s_cmp_lt_u32 s6, 2\n"
        "s_cselect_b64 s[24:25], s[46:47], s[48:49]\n"
        "s_and_b32 s25, s25, 0xffff\n"
        "s_sub_u32 s8, s44, 2048\n"
        "s_subb_u32 s9, s45, 0\n"
        "s_and_b32 s9, s9, 0xffff\n"
        "s_mov_b32 s10, 200542976\n"
        "s_mov_b32 s11, 0x20000\n"
        "s_mul_i32 s7, s6, 7168\n"
        "s_add_u32 s7, s7, 6144\n"
        "s_add_u32 s22, s4, s7\n"
        "s_addc_u32 s23, s5, 0\n"
        "s_sub_u32 s7, s6, 2\n"
        "s_and_b32 s7, s7, 3\n"
        "s_min_u32 s7, s7, 2\n"
        "s_lshl_b32 s7, s7, 10\n"
        "s_add_u32 m0, s7, 18528\n"
        "v_add_u32 v27, s7, v57\n"
        "s_barrier\n"
        "global_load_lds_dwordx4 v27, s[4:5]\n"
        "global_load_dwordx4 v[240:243], v57, s[22:23] offset:-3072\n"
        "global_load_dwordx4 v[244:247], v57, s[22:23] offset:-2048\n"
        "s_add_u32 m0, s64, 0\n"
        "s_nop 0\n"
        "buffer_load_dwordx4 v58, s[24:27], s28 offen lds\n"
        "s_add_u32 m0, s64, 1024\n"
        "s_nop 0\n"
        "buffer_load_dwordx4 v59, s[24:27], s28 offen lds\n"
        "s_add_u32 m0, s64, 2048\n"
        "s_nop 0\n"
        "buffer_load_dwordx4 v60, s[24:27], s28 offen lds\n"
        "s_add_u32 m0, s64, 3072\n"
        "s_nop 0\n"
        "buffer_load_dwordx4 v58, s[24:27], s29 offen lds\n"
        "s_add_u32 m0, s64, 4096\n"
        "s_nop 0\n"
        "buffer_load_dwordx4 v59, s[24:27], s29 offen lds\n"
        "s_add_u32 m0, s64, 5120\n"
        "s_nop 0\n"
        "buffer_load_dwordx4 v60, s[24:27], s29 offen lds\n"
        "s_add_u32 m0, s64, 6144\n"
        "s_nop 0\n"
        "buffer_load_dwordx4 v58, s[24:27], s60 offen lds\n"
        "s_add_u32 m0, s64, 7168\n"
        "s_nop 0\n"
        "buffer_load_dwordx4 v59, s[24:27], s60 offen lds\n"
        "s_add_u32 m0, s64, 8192\n"
        "s_nop 0\n"
        "buffer_load_dwordx4 v60, s[24:27], s60 offen lds\n"
        "s_cmp_lt_u32 s6, 2\n"
        "s_cselect_b32 s62, 3, 16\n"
        "s_cselect_b32 s63, 12, 32\n"
        "s_cselect_b64 s[24:25], s[50:51], s[52:53]\n"
        "s_cselect_b32 s64, 0, 117088\n"
        "s_mul_i32 s62, s62, 192\n"
        "s_mul_i32 s63, s63, 192\n"
        "s_and_b32 s25, s25, 0xffff\n"
        "s_mul_i32 s7, s6, 9216\n"
        "s_add_u32 s64, s64, s7\n"
        "s_add_u32 s64, s64, 0\n"
        "s_add_u32 s7, s62, s63\n"
        "s_lshl_b32 s29, s63, 1\n"
        "s_add_u32 s60, s7, s63\n"
        "s_bitcmp1_b32 s6, 0\n"
        "s_cselect_b32 s28, s7, 0\n"
        "s_cselect_b32 s29, s29, s62\n"
        "s_cselect_b32 s60, s60, s63\n"
        "s_add_u32 s28, s28, s61\n"
        "s_add_u32 s29, s29, s61\n"
        "s_add_u32 s60, s60, s61\n"
        "s_add_u32 m0, s64, 0\n"
        "s_nop 0\n"
        "buffer_load_dwordx4 v32, s[24:27], s28 offen lds\n"
        "s_add_u32 m0, s64, 1024\n"
        "s_nop 0\n"
        "buffer_load_dwordx4 v33, s[24:27], s28 offen lds\n"
        "s_add_u32 m0, s64, 2048\n"
        "s_nop 0\n"
        "buffer_load_dwordx4 v34, s[24:27], s28 offen lds\n"
        "s_add_u32 m0, s64, 3072\n"
        "s_nop 0\n"
        "buffer_load_dwordx4 v32, s[24:27], s29 offen lds\n"
        "s_add_u32 m0, s64, 4096\n"
        "s_nop 0\n"
        "buffer_load_dwordx4 v33, s[24:27], s29 offen lds\n"
        "s_add_u32 m0, s64, 5120\n"
        "s_nop 0\n"
        "buffer_load_dwordx4 v34, s[24:27], s29 offen lds\n"
        "s_add_u32 m0, s64, 6144\n"
        "s_nop 0\n"
        "buffer_load_dwordx4 v32, s[24:27], s60 offen lds\n"
        "s_add_u32 m0, s64, 7168\n"
        "s_nop 0\n"
        "buffer_load_dwordx4 v33, s[24:27], s60 offen lds\n"
        "s_add_u32 m0, s64, 8192\n"
        "s_nop 0\n"
        "buffer_load_dwordx4 v34, s[24:27], s60 offen lds\n"
        "s_setprio 2\n"
        "s_waitcnt vmcnt(9)\n"
        "s_mov_b64 exec, 1\n"
        "ds_inc_u32 v250, v250 offset:20\n"
        "s_mov_b64 exec, -1\n"
        "L80_%=:\n"
        "ds_read_b32 v251, v250 offset:20\n"
        "s_waitcnt lgkmcnt(0)\n"
        "v_readfirstlane_b32 s40, v251\n"
        "s_nop 3\n"
        "s_cmp_ge_i32 s40, 4\n"
        "s_cbranch_scc1 L81_%=\n"
        "s_nop 0\n"
        "s_sub_u32 s41, s41, 1\n"
        "s_cbranch_scc0 L80_%=\n"
        "s_endpgm\n"
        "L81_%=:\n"
        "s_add_u32 m0, s17, 0\n"
        "s_nop 0\n"
        "buffer_load_dwordx4 v29, s[8:11], s35 offen lds\n"
        "buffer_load_dwordx4 v30, s[8:11], s35 offen offset:1024 lds\n"
        "buffer_load_dwordx4 v31, s[8:11], s35 offen offset:2048 lds\n"
        "s_add_u32 m0, s17, 3104\n"
        "s_nop 0\n"
        "buffer_load_dwordx4 v29, s[8:11], s36 offen lds\n"
        "buffer_load_dwordx4 v30, s[8:11], s36 offen offset:1024 lds\n"
        "buffer_load_dwordx4 v31, s[8:11], s36 offen offset:2048 lds\n"
        "s_cmp_eq_u32 s6, 2\n"
        "s_cbranch_scc1 L82_%=\n"
        "s_mov_b32 s34, 0x411cc471\n"
        "s_mov_b32 s35, 110080\n"
        "s_mov_b32 s36, 110072\n"
        "s_mov_b32 s37, 110076\n"
        "s_call_b64 s[30:31], FOLDW_%=\n"
        "s_branch L83_%=\n"
        "L82_%=:\n"
        "s_mov_b32 s34, 0x411cc471\n"
        "s_mov_b32 s35, 110080\n"
        "s_mov_b32 s36, 110104\n"
        "s_mov_b32 s37, 110076\n"
        "s_call_b64 s[30:31], FOLDU_%=\n"
        "global_load_dwordx4 v[40:43], v57, s[22:23] offset:-3072\n"
        "global_load_dwordx4 v[44:47], v57, s[22:23] offset:-2048\n"
        "global_load_dwordx4 v[0:3], v57, s[22:23] offset:-1024\n"
        "global_load_dwordx4 v[4:7], v57, s[22:23] offset:0\n"
        "global_load_dwordx4 v[8:11], v57, s[22:23] offset:1024\n"
        "global_load_dwordx4 v[12:15], v57, s[22:23] offset:2048\n"
        "global_load_dwordx4 v[16:19], v57, s[22:23] offset:3072\n"
        "L83_%=:\n"
        "s_mov_b64 s[24:25], s[56:57]\n"
        "s_mov_b32 s44, 0xf0000f\n"
        "s_mov_b32 s45, 0xf0000f00\n"
        "s_mov_b32 s46, 0xff000ff\n"
        "s_mov_b32 s47, 0xf00fff00\n"
        "s_mov_b32 s48, 0x22222222\n"
        "s_mov_b32 s49, 0x22222222\n"
        "s_mov_b32 s50, 0x44444444\n"
        "s_mov_b32 s51, 0x44444444\n"
        "s_mov_b32 s52, 0x88888888\n"
        "s_mov_b32 s53, 0x88888888\n"
        "s_mov_b64 vcc, s[44:45]\n"
        "s_mov_b32 s28, 0x3c2aaaab\n"
        "s_mov_b32 s29, 0x3a7ba882\n"
        "s_mov_b32 s30, 0\n"
        "s_mov_b32 s34, 0\n"
        "s_mov_b32 s31, s18\n"
        "s_mov_b32 s36, s18\n"
        "s_mov_b32 s37, s18\n"
        "s_add_u32 s71, s15, 588672\n"
        "s_sub_u32 s72, 73, s16\n"
        "s_add_u32 s73, s17, 3104\n"
        "s_mov_b32 s68, 4\n"
        "s_mov_b32 s76, 16\n"
        "s_mov_b32 s74, -1\n"
        "s_mov_b32 s41, 0x400000\n"
        "L23_%=:\n"
        "ds_read_b32 v251, v250 offset:28\n"
        "s_waitcnt lgkmcnt(0)\n"
        "v_readfirstlane_b32 s40, v251\n"
        "s_nop 3\n"
        "s_cmp_ge_i32 s40, 7\n"
        "s_cbranch_scc1 L24_%=\n"
        "s_nop 0\n"
        "s_sub_u32 s41, s41, 1\n"
        "s_cbranch_scc0 L23_%=\n"
        "s_endpgm\n"
        "L24_%=:\n"
        "s_waitcnt vmcnt(0)\n"
        "v_readfirstlane_b32 s26, v253\n"
        "ds_read_b64 v[80:81], v15\n"
        "ds_read_b64 v[68:69], v15\n"
        "ds_read_b64 v[74:75], v15\n"
        "ds_read_b64 v[84:85], v15\n"
        "v_mov_b32 v91, 110048\n"
        "v_add_u32 v58, 21600, v14\n"
        "ds_read_b128 v[112:115], v58 offset:0\n"
        "ds_read_b128 v[116:119], v58 offset:1024\n"
        "ds_read_b128 v[120:123], v58 offset:2048\n"
        "ds_read_b128 v[124:127], v58 offset:3072\n"
        "ds_read_b128 v[128:131], v58 offset:4096\n"
        "ds_read_b128 v[132:135], v58 offset:5120\n"
        "ds_read_b128 v[136:139], v58 offset:6144\n"
        "ds_read_b128 v[140:143], v58 offset:7168\n"
        "ds_read_b128 v[144:147], v58 offset:8192\n"
        "ds_read_b128 v[148:151], v58 offset:9216\n"
        "ds_read_b128 v[152:155], v58 offset:10240\n"
        "ds_read_b128 v[156:159], v58 offset:11264\n"
        "ds_read_b128 v[160:163], v58 offset:12288\n"
        "ds_read_b128 v[164:167], v58 offset:13312\n"
        "ds_read_b128 v[168:171], v58 offset:14336\n"
        "ds_read_b128 v[172:175], v58 offset:15360\n"
        "ds_read_b128 v[176:179], v58 offset:16384\n"
        "ds_read_b128 v[180:183], v58 offset:17408\n"
        "ds_read_b128 v[184:187], v58 offset:18432\n"
        "ds_read_b128 v[188:191], v58 offset:19456\n"
        "ds_read_b128 v[192:195], v58 offset:20480\n"
        "ds_read_b128 v[196:199], v58 offset:21520\n"
        "ds_read_b128 v[200:203], v58 offset:22544\n"
        "ds_read_b128 v[204:207], v58 offset:23568\n"
        "ds_read_b128 v[208:211], v58 offset:24608\n"
        "ds_read_b128 v[212:215], v58 offset:25632\n"
        "ds_read_b128 v[216:219], v58 offset:26656\n"
        "ds_read_b128 v[220:223], v58 offset:27696\n"
        "ds_read_b128 v[224:227], v58 offset:28720\n"
        "ds_read_b128 v[228:231], v58 offset:29744\n"
        "ds_read_b128 v[232:235], v58 offset:30784\n"
        "ds_read_b128 v[236:239], v58 offset:31808\n"
        "ds_read_b128 v[240:243], v58 offset:32832\n"
        "ds_read_b128 v[244:247], v58 offset:33872\n"
        "ds_read_b128 v[248:251], v58 offset:34896\n"
        "ds_read_b128 v[252:255], v58 offset:35920\n"
        "ds_read_b128 v[20:23], v0 offset:0\n"
        "ds_read_b128 v[26:29], v0 offset:64\n"
        "ds_read_b128 v[32:35], v0 offset:128\n"
        "ds_read_b128 v[40:43], v0 offset:768\n"
        "ds_read_b128 v[46:49], v0 offset:832\n"
        "ds_read_b128 v[52:55], v0 offset:896\n"
        "s_add_u32 s37, s37, 4\n"
        "s_cmp_ge_u32 s37, 49\n"
        "s_cselect_b32 s69, 49, 0\n"
        "s_sub_u32 s37, s37, s69\n"
        "s_sub_u32 s69, s37, 5\n"
        "s_cmp_lt_u32 s69, 4\n"
        "s_subb_u32 s36, s36, -4\n"
        "s_cmp_ge_u32 s36, 6\n"
        "s_cselect_b32 s69, 6, 0\n"
        "s_addc_u32 s34, s34, 0\n"
        "s_sub_u32 s36, s36, s69\n"
        "s_mul_i32 s38, s34, 1344\n"
        "s_cmp_ge_u32 s34, s76\n"
        "s_cbranch_scc0 L27_%=\n"
        "s_nop 1\n"
        "v_mov_b32 v60, 110048\n"
        "L25_%=:\n"
        "ds_read_b32 v61, v60 offset:64\n"
        "s_waitcnt lgkmcnt(0)\n"
        "v_readfirstlane_b32 s40, v61\n"
        "s_nop 3\n"
        "s_cmp_ge_i32 s40, 1\n"
        "s_cbranch_scc1 L26_%=\n"
        "s_sleep 1\n"
        "s_sub_u32 s41, s41, 1\n"
        "s_cbranch_scc0 L25_%=\n"
        "s_endpgm\n"
        "L26_%=:\n"
        "v_mbcnt_lo_u32_b32 v61, -1, 0\n"
        "v_lshl_add_u32 v60, v61, 2, v60\n"
        "ds_read_b32 v61, v60 offset:68\n"
        "s_mov_b32 s14, s40\n"
        "s_mov_b32 s76, -1\n"
        "s_waitcnt lgkmcnt(0)\n"
        "v_readlane_b32 s22, v61, 0\n"
        "v_readlane_b32 s23, v61, 1\n"
        "s_nop 3\n"
        "L27_%=:\n"
        "s_cmp_ge_u32 s34, s72\n"
        "s_cselect_b32 s70, s71, s15\n"
        "s_cmp_ge_u32 s34, 20\n"
        "s_cselect_b32 s70, s22, s70\n"
        "s_cmp_ge_u32 s34, 21\n"
        "s_cselect_b32 s70, s23, s70\n"
        "s_add_u32 s38, s38, s70\n"
        "s_mul_i32 s69, s36, 98304\n"
        "s_add_u32 s38, s38, s69\n"
        "s_mov_b32 s35, s36\n"
        "s_mul_i32 s69, s36, -12\n"
        "s_add_u32 s69, s69, 60\n"
        "s_lshl_b64 s[54:55], -1, s69\n"
        "s_lshl_b64 s[56:57], s[54:55], 24\n"
        "s_ashr_i64 s[60:61], s[54:55], 8\n"
        "s_mov_b32 s58, s61\n"
        "s_mov_b32 s59, s56\n"
        "v_cndmask_b32_e64 v30, v5, v8, s[56:57]\n"
        "v_cndmask_b32_e64 v50, v6, v9, s[58:59]\n"
        "v_cndmask_b32_e64 v39, v7, v10, s[60:61]\n"
        "s_cmp_eq_u32 s37, 4\n"
        "s_cbranch_scc0 L28_%=\n"
        "s_nop 1\n"
        "ds_read_b32 v30, v14 offset:19552\n"
        "ds_read_b32 v50, v14 offset:19556\n"
        "ds_read_b32 v39, v14 offset:19560\n"
        "s_mov_b32 s35, 6\n"
        "s_mov_b32 s38, 0\n"
        "s_mov_b32 s69, 1344\n"
        "s_waitcnt lgkmcnt(0)\n"
        "v_lshrrev_b32 v60, 28, v30\n"
        "v_add_u32 v60, s34, v60\n"
        "v_and_b32 v30, 0x0fffffff, v30\n"
        "v_mad_u32_u24 v30, v60, s69, v30\n"
        "v_mov_b32 v61, s15\n"
        "v_cmpx_le_u32 vcc, s72, v60\n"
        "v_mov_b32 v61, s71\n"
        "s_mov_b64 exec, -1\n"
        "v_cmpx_le_u32 vcc, 20, v60\n"
        "v_mov_b32 v61, s22\n"
        "s_mov_b64 exec, -1\n"
        "v_cmpx_le_u32 vcc, 21, v60\n"
        "v_mov_b32 v61, s23\n"
        "s_mov_b64 exec, -1\n"
        "v_add_u32 v30, v30, v61\n"
        "v_lshrrev_b32 v60, 28, v50\n"
        "v_add_u32 v60, s34, v60\n"
        "v_and_b32 v50, 0x0fffffff, v50\n"
        "v_mad_u32_u24 v50, v60, s69, v50\n"
        "v_mov_b32 v61, s15\n"
        "v_cmpx_le_u32 vcc, s72, v60\n"
        "v_mov_b32 v61, s71\n"
        "s_mov_b64 exec, -1\n"
        "v_cmpx_le_u32 vcc, 20, v60\n"
        "v_mov_b32 v61, s22\n"
        "s_mov_b64 exec, -1\n"
        "v_cmpx_le_u32 vcc, 21, v60\n"
        "v_mov_b32 v61, s23\n"
        "s_mov_b64 exec, -1\n"
        "v_add_u32 v50, v50, v61\n"
        "v_lshrrev_b32 v60, 28, v39\n"
        "v_add_u32 v60, s34, v60\n"
        "v_and_b32 v39, 0x0fffffff, v39\n"
        "v_mad_u32_u24 v39, v60, s69, v39\n"
        "v_mov_b32 v61, s15\n"
        "v_cmpx_le_u32 vcc, s72, v60\n"
        "v_mov_b32 v61, s71\n"
        "s_mov_b64 exec, -1\n"
        "v_cmpx_le_u32 vcc, 20, v60\n"
        "v_mov_b32 v61, s22\n"
        "s_mov_b64 exec, -1\n"
        "v_cmpx_le_u32 vcc, 21, v60\n"
        "v_mov_b32 v61, s23\n"
        "s_mov_b64 exec, -1\n"
        "v_add_u32 v39, v39, v61\n"
        "s_nop 4\n"
        "s_mov_b64 vcc, s[44:45]\n"
        "L28_%=:\n"
        "s_cmp_lt_u32 s34, s14\n"
        "s_cselect_b32 s38, s38, 0x80000000\n"
        "s_add_u32 s39, s38, 100270464\n"
        "s_waitcnt lgkmcnt(0)\n"
        "s_mov_b64 exec, 1\n"
        "ds_inc_u32 v91, v11 offset:8\n"
        "s_mov_b64 exec, -1\n"
        "s_cmp_eq_u32 s18, 0\n"
        "s_cselect_b32 s75, -20, 8\n"
        "s_setprio 1\n"
        "PTOP_%=:\n"
        "s_waitcnt lgkmcnt(5)\n"
        "v_mfma_f32_16x16x32_bf16 v[104:107], v[112:115], v[20:23], 0\n"
        "s_mov_b32 m0, s17\n"
        "v_cndmask_b32_e64 v101, v92, v93, s[48:49]\n"
        "s_nop 1\n"
        "v_cndmask_b32_e64 v102, v96, v97, s[48:49]\n"
        "s_waitcnt lgkmcnt(3)\n"
        "v_mfma_f32_16x16x32_bf16 v[104:107], v[116:119], v[26:29], v[104:107]\n"
        "ds_read_b32 v103, v91 offset:8\n"
        "v_cndmask_b32_e64 v101, v101, v94, s[50:51]\n"
        "s_waitcnt lgkmcnt(2)\n"
        "v_mfma_f32_16x16x32_bf16 v[104:107], v[120:123], v[32:35], v[104:107]\n"
        "s_waitcnt lgkmcnt(1)\n"
        "buffer_load_dwordx4 v30, s[8:11], s38 offen lds\n"
        "v_mfma_f32_16x16x32_bf16 v[108:111], v[132:135], v[32:35], 0\n"
        "v_cndmask_b32_e64 v102, v102, v98, s[50:51]\n"
        "v_cndmask_b32_e64 v101, v101, v95, s[52:53]\n"
        "v_mfma_f32_16x16x32_bf16 v[108:111], v[128:131], v[26:29], v[108:111]\n"
        "s_setprio 3 \n"
        "v_cndmask_b32_e64 v102, v102, v99, s[52:53]\n"
        "v_mul_f32 v90, s28, v38\n"
        "v_mfma_f32_16x16x32_bf16 v[108:111], v[124:127], v[20:23], v[108:111]\n"
        "buffer_load_dwordx4 v50, s[8:11], s38 offen offset:1024 lds\n"
        "v_mfma_f32_16x16x32_bf16 v[96:99], v[136:139], v[20:23], 0\n"
        "s_waitcnt lgkmcnt(0)\n"
        "v_readfirstlane_b32 s40, v103\n"
        "v_cndmask_b32_dpp v101, v102, v101, vcc row_ror:4 row_mask:0xf bank_mask:0xf\n"
        "v_mfma_f32_16x16x32_bf16 v[96:99], v[140:143], v[26:29], v[96:99]\n"
        "s_cmp_lt_i32 s40, s68\n"
        "v_fma_f32 v90, -v90, v38, v101\n"
        "v_add_f32_e64 v90, s29, |v90|\n"
        "v_mfma_f32_16x16x32_bf16 v[96:99], v[144:147], v[32:35], v[96:99]\n"
        "buffer_load_dwordx4 v39, s[8:11], s38 offen offset:2048 lds\n"
        "s_cbranch_scc1 L3_%=\n"
        "L4_%=:\n"
        "v_mfma_f32_16x16x32_bf16 v[100:103], v[156:159], v[32:35], 0\n"
        "s_mov_b32 m0, s73\n"
        "v_rsq_f32 v83, v90\n"
        "v_mfma_f32_16x16x32_bf16 v[100:103], v[152:155], v[26:29], v[100:103]\n"
        "v_mfma_f32_16x16x32_bf16 v[100:103], v[148:151], v[20:23], v[100:103]\n"
        "buffer_load_dwordx4 v30, s[8:11], s39 offen lds\n"
        "ds_write_b128 v12, v[58:61]\n"
        "ds_write_b128 v12, v[62:65] offset:832\n"
        "s_cmp_eq_u32 s74, s30\n"
        "v_mfma_f32_16x16x32_bf16 v[60:63], v[160:163], v[20:23], 0\n"
        "s_setprio 3\n"
        "s_cbranch_scc1 L5_%=\n"
        "s_setprio 1\n"
        "L5_%=:\n"
        "v_mfma_f32_16x16x32_bf16 v[60:63], v[164:167], v[26:29], v[60:63]\n"
        "v_mfma_f32_16x16x32_bf16 v[60:63], v[168:171], v[32:35], v[60:63]\n"
        "buffer_load_dwordx4 v50, s[8:11], s39 offen offset:1024 lds\n"
        "v_mfma_f32_16x16x32_bf16 v[64:67], v[180:183], v[32:35], 0\n"
        "v_mfma_f32_16x16x32_bf16 v[64:67], v[176:179], v[26:29], v[64:67]\n"
        "v_mfma_f32_16x16x32_bf16 v[64:67], v[172:175], v[20:23], v[64:67]\n"
        "buffer_load_dwordx4 v39, s[8:11], s39 offen offset:2048 lds\n"
        "ds_write_b128 v12, v[16:19] offset:64\n"
        "ds_write_b128 v12, v[86:89] offset:896\n"
        "v_mfma_f32_16x16x32_bf16 v[36:39], v[20:23], v[20:23], 0\n"
        "v_mfma_f32_16x16x32_bf16 v[36:39], v[26:29], v[26:29], v[36:39]\n"
        "v_mfma_f32_16x16x32_bf16 v[36:39], v[32:35], v[32:35], v[36:39]\n"
        "ds_write_b128 v12, v[76:79] offset:128\n"
        "v_mfma_f32_16x16x32_bf16 v[16:19], v[40:43], v[40:43], 0\n"
        "v_mov_b64 v[86:87], 0\n"
        "v_mov_b64 v[88:89], 0\n"
        "v_mfma_f32_16x16x32_bf16 v[16:19], v[46:49], v[46:49], v[16:19]\n"
        "v_mfma_f32_16x16x32_bf16 v[16:19], v[52:55], v[52:55], v[16:19]\n"
        "ds_write_b128 v12, v[70:73] offset:960\n"
        "v_smfmac_f32_16x16x64_bf16 v[86:89], v[20:23], v[104:111], v11\n"
        "v_cndmask_b32_e64 v82, v36, v37, s[48:49]\n"
        "v_smfmac_f32_16x16x64_bf16 v[86:89], v[26:29], v[96:103], v11\n"
        "v_cndmask_b32_e64 v82, v82, v38, s[50:51]\n"
        "v_smfmac_f32_16x16x64_bf16 v[86:89], v[32:35], v[60:67], v11\n"
        "s_mov_b64 exec, s[46:47]\n"
        "ds_write_b32 v13, v83\n"
        "s_mov_b64 exec, -1\n"
        "v_mfma_f32_16x16x32_bf16 v[104:107], v[112:115], v[40:43], 0\n"
        "v_cndmask_b32_e64 v82, v82, v39, s[52:53]\n"
        "v_cndmask_b32_e64 v103, v16, v17, s[48:49]\n"
        "v_mfma_f32_16x16x32_bf16 v[104:107], v[116:119], v[46:49], v[104:107]\n"
        "v_cndmask_b32_e64 v103, v103, v18, s[50:51]\n"
        "v_cndmask_b32_e64 v103, v103, v19, s[52:53]\n"
        "v_mfma_f32_16x16x32_bf16 v[104:107], v[120:123], v[52:55], v[104:107]\n"
        "s_mov_b64 exec, 1\n"
        "ds_inc_u32 v91, v11 offset:0\n"
        "s_mov_b64 exec, -1\n"
        "v_mfma_f32_16x16x32_bf16 v[108:111], v[132:135], v[52:55], 0\n"
        "v_cndmask_b32_dpp v82, v103, v82, vcc row_ror:4 row_mask:0xf bank_mask:0xf\n"
        "v_mfma_f32_16x16x32_bf16 v[108:111], v[128:131], v[46:49], v[108:111]\n"
        "v_mfma_f32_16x16x32_bf16 v[108:111], v[124:127], v[40:43], v[108:111]\n"
        "v_mfma_f32_16x16x32_bf16 v[92:95], v[136:139], v[40:43], 0\n"
        "v_mfma_f32_16x16x32_bf16 v[92:95], v[140:143], v[46:49], v[92:95]\n"
        "v_mfma_f32_16x16x32_bf16 v[92:95], v[144:147], v[52:55], v[92:95]\n"
        "v_mfma_f32_16x16x32_bf16 v[96:99], v[156:159], v[52:55], 0\n"
        "v_mfma_f32_16x16x32_bf16 v[96:99], v[152:155], v[46:49], v[96:99]\n"
        "ds_read_b128 v[76:79], v1\n"
        "v_mfma_f32_16x16x32_bf16 v[96:99], v[148:151], v[40:43], v[96:99]\n"
        "ds_read_b128 v[70:73], v1 offset:64\n"
        "ds_read_b128 v[100:103], v1 offset:128\n"
        "v_mfma_f32_16x16x32_bf16 v[60:63], v[160:163], v[40:43], 0\n"
        "v_mfma_f32_16x16x32_bf16 v[60:63], v[164:167], v[46:49], v[60:63]\n"
        "s_and_b32 s69, s30, 1\n"
        "v_mfma_f32_16x16x32_bf16 v[60:63], v[168:171], v[52:55], v[60:63]\n"
        "s_lshl2_add_u32 s70, s69, 110048\n"
        "v_mfma_f32_16x16x32_bf16 v[64:67], v[180:183], v[52:55], 0\n"
        "v_mov_b32 v91, s70\n"
        "v_mfma_f32_16x16x32_bf16 v[64:67], v[176:179], v[46:49], v[64:67]\n"
        "s_mul_i32 s69, s69, 40064\n"
        "v_mfma_f32_16x16x32_bf16 v[64:67], v[172:175], v[40:43], v[64:67]\n"
        "s_waitcnt lgkmcnt(0)\n"
        "s_mul_i32 s70, s31, 1664\n"
        "v_mfma_f32_16x16x32_bf16 v[36:39], v[76:79], v[20:23], 0\n"
        "s_add_u32 s69, s69, s70\n"
        "v_mfma_f32_16x16x32_bf16 v[36:39], v[70:73], v[26:29], v[36:39]\n"
        "v_add_u32 v12, s69, v4\n"
        "v_mfma_f32_16x16x32_bf16 v[36:39], v[100:103], v[32:35], v[36:39]\n"
        "v_mov_b64 v[56:57], 0\n"
        "v_mov_b64 v[58:59], 0\n"
        "v_mfma_f32_16x16x32_bf16 v[16:19], v[76:79], v[40:43], 0\n"
        "v_add_u32 v13, s69, v3\n"
        "s_lshr_b32 s68, s30, 1\n"
        "v_mfma_f32_16x16x32_bf16 v[16:19], v[70:73], v[46:49], v[16:19]\n"
        "s_lshl2_add_u32 s68, s68, 4\n"
        "v_mfma_f32_16x16x32_bf16 v[16:19], v[100:103], v[52:55], v[16:19]\n"
        "s_cmp_eq_u32 s31, 6\n"
        "v_smfmac_f32_16x16x64_bf16 v[56:59], v[40:43], v[104:111], v11\n"
        "s_cbranch_scc1 L8_%=\n"
        "L9_%=:\n"
        "v_smfmac_f32_16x16x64_bf16 v[56:59], v[46:49], v[92:99], v11\n"
        "s_mov_b32 s74, s30\n"
        "v_smfmac_f32_16x16x64_bf16 v[56:59], v[52:55], v[60:67], v11\n"
        "s_mov_b32 s30, s34\n"
        "v_mfma_f32_16x16x32_bf16 v[70:73], v[52:55], v[192:195], 0\n"
        "s_mov_b32 s31, s35\n"
        "v_cndmask_b32_dpp v90, v16, v36, vcc row_ror:4 row_mask:0xf bank_mask:0xf\n"
        "v_mul_f32 v111, s28, v90\n"
        "v_mfma_f32_16x16x32_bf16 v[70:73], v[46:49], v[188:191], v[70:73]\n"
        "s_add_u32 s37, s37, 4\n"
        "v_fma_f32 v111, -v111, v90, v82\n"
        "v_add_f32_e64 v111, s29, |v111|\n"
        "v_mfma_f32_16x16x32_bf16 v[70:73], v[40:43], v[184:187], v[70:73]\n"
        "s_cmp_ge_u32 s37, 49\n"
        "v_rsq_f32 v102, v111\n"
        "v_cndmask_b32_dpp v108, v56, v86, vcc row_ror:4 row_mask:0xf bank_mask:0xf\n"
        "v_mfma_f32_16x16x32_bf16 v[62:65], v[32:35], v[192:195], 0\n"
        "s_cselect_b32 s69, 49, 0\n"
        "v_cndmask_b32_dpp v110, v57, v87, vcc row_ror:4 row_mask:0xf bank_mask:0xf\n"
        "v_cndmask_b32_dpp v111, v58, v88, vcc row_ror:4 row_mask:0xf bank_mask:0xf\n"
        "v_mfma_f32_16x16x32_bf16 v[62:65], v[26:29], v[188:191], v[62:65]\n"
        "s_sub_u32 s37, s37, s69\n"
        "v_cndmask_b32_dpp v109, v59, v89, vcc row_ror:4 row_mask:0xf bank_mask:0xf\n"
        "v_mul_f32_dpp v108, v102, v108 quad_perm:[0,0,0,0] row_mask:0xf bank_mask:0xf\n"
        "v_mfma_f32_16x16x32_bf16 v[62:65], v[20:23], v[184:187], v[62:65]\n"
        "s_sub_u32 s69, s37, 5\n"
        "v_mul_f32_dpp v110, v102, v110 quad_perm:[1,1,1,1] row_mask:0xf bank_mask:0xf\n"
        "v_mul_f32_dpp v111, v102, v111 quad_perm:[2,2,2,2] row_mask:0xf bank_mask:0xf\n"
        "v_mfma_f32_16x16x32_bf16 v[104:107], v[20:23], v[220:223], 0\n"
        "s_cmp_lt_u32 s69, 4\n"
        "v_mul_f32_dpp v109, v102, v109 quad_perm:[3,3,3,3] row_mask:0xf bank_mask:0xf\n"
        "v_max3_f32 v99, v108, v110, v111\n"
        "v_mfma_f32_16x16x32_bf16 v[104:107], v[26:29], v[224:227], v[104:107]\n"
        "s_subb_u32 s36, s36, -4\n"
        "v_max_f32 v99, v99, v109\n"
        "v_mul_f32 v99, v99, v102\n"
        "v_mfma_f32_16x16x32_bf16 v[104:107], v[32:35], v[228:231], v[104:107]\n"
        "s_cmp_ge_u32 s36, 6\n"
        "v_fma_f32 v108, v108, v102, -v99\n"
        "v_fma_f32 v110, v110, v102, -v99\n"
        "v_fma_f32 v111, v111, v102, -v99\n"
        "v_mfma_f32_16x16x32_bf16 v[92:95], v[40:43], v[220:223], 0\n"
        "s_cselect_b32 s69, 6, 0\n"
        "v_fma_f32 v109, v109, v102, -v99\n"
        "v_cvt_pk_bf16_f32 v78, v62, v63\n"
        "v_mfma_f32_16x16x32_bf16 v[92:95], v[46:49], v[224:227], v[92:95]\n"
        "s_addc_u32 s34, s34, 0\n"
        "v_exp_f32 v108, v108\n"
        "v_mfma_f32_16x16x32_bf16 v[92:95], v[52:55], v[228:231], v[92:95]\n"
        "s_sub_u32 s36, s36, s69\n"
        "v_exp_f32 v110, v110\n"
        "v_mfma_f32_16x16x32_bf16 v[96:99], v[52:55], v[204:207], 0\n"
        "s_mul_i32 s38, s34, 1344\n"
        "v_exp_f32 v111, v111\n"
        "v_mfma_f32_16x16x32_bf16 v[96:99], v[46:49], v[200:203], v[96:99]\n"
        "s_cmp_ge_u32 s34, s76\n"
        "v_exp_f32 v109, v109\n"
        "v_mfma_f32_16x16x32_bf16 v[96:99], v[40:43], v[196:199], v[96:99]\n"
        "s_cbranch_scc1 L10_%=\n"
        "L11_%=:\n"
        "v_add_f32 v83, v108, v110\n"
        "v_add_f32 v103, v111, v109\n"
        "v_add_f32 v83, v83, v103\n"
        "v_mfma_f32_16x16x32_bf16 v[86:89], v[32:35], v[204:207], 0\n"
        "s_cmp_ge_u32 s34, s72\n"
        "v_rcp_f32 v83, v83\n"
        "v_mfma_f32_16x16x32_bf16 v[86:89], v[26:29], v[200:203], v[86:89]\n"
        "s_cselect_b32 s70, s71, s15\n"
        "v_mul_f32_dpp v108, v102, v108 quad_perm:[0,0,0,0] row_mask:0xf bank_mask:0xf\n"
        "v_mul_f32_dpp v110, v102, v110 quad_perm:[1,1,1,1] row_mask:0xf bank_mask:0xf\n"
        "v_mfma_f32_16x16x32_bf16 v[86:89], v[20:23], v[196:199], v[86:89]\n"
        "s_cmp_ge_u32 s34, 20\n"
        "v_mul_f32_dpp v111, v102, v111 quad_perm:[2,2,2,2] row_mask:0xf bank_mask:0xf\n"
        "v_mul_f32_dpp v109, v102, v109 quad_perm:[3,3,3,3] row_mask:0xf bank_mask:0xf\n"
        "v_mul_f32 v108, v108, v83\n"
        "v_mfma_f32_16x16x32_bf16 v[100:103], v[20:23], v[232:235], 0\n"
        "s_cselect_b32 s70, s22, s70\n"
        "v_mul_f32 v110, v110, v83\n"
        "v_mul_f32 v111, v111, v83\n"
        "v_mfma_f32_16x16x32_bf16 v[100:103], v[26:29], v[236:239], v[100:103]\n"
        "s_cmp_ge_u32 s34, 21\n"
        "v_mul_f32 v109, v109, v83\n"
        "v_cvt_pk_bf16_f32 v82, v108, v110\n"
        "v_mfma_f32_16x16x32_bf16 v[100:103], v[32:35], v[240:243], v[100:103]\n"
        "s_cselect_b32 s70, s23, s70\n"
        "v_cvt_pk_bf16_f32 v83, v111, v109\n"
        "v_and_b32 v18, v2, v82\n"
        "v_and_b32 v19, v2, v83\n"
        "v_mfma_f32_16x16x32_bf16 v[56:59], v[40:43], v[232:235], 0\n"
        "s_add_u32 s38, s38, s70\n"
        "v_cvt_pk_bf16_f32 v79, v64, v65\n"
        "v_cvt_pk_bf16_f32 v66, v70, v71\n"
        "v_mfma_f32_16x16x32_bf16 v[56:59], v[46:49], v[236:239], v[56:59]\n"
        "s_mul_i32 s69, s36, 98304\n"
        "v_cvt_pk_bf16_f32 v67, v72, v73\n"
        "v_and_b32_dpp v38, v82, v2 row_ror:12 row_mask:0xf bank_mask:0xf\n"
        "v_mfma_f32_16x16x32_bf16 v[56:59], v[52:55], v[240:243], v[56:59]\n"
        "s_add_u32 s38, s38, s69\n"
        "v_and_b32_dpp v39, v83, v2 row_ror:12 row_mask:0xf bank_mask:0xf\n"
        "v_mov_b64 v[30:31], v[18:19]\n"
        "v_mfma_f32_16x16x32_bf16 v[60:63], v[78:81], v[18:21], 0\n"
        "s_mov_b32 s35, s36\n"
        "v_cvt_pk_bf16_f32 v82, v104, v105\n"
        "v_cvt_pk_bf16_f32 v83, v106, v107\n"
        "v_mov_b64 v[50:51], v[38:39]\n"
        "v_mfma_f32_16x16x32_bf16 v[104:107], v[66:69], v[38:41], 0\n"
        "s_mul_i32 s69, s36, -12\n"
        "v_cvt_pk_bf16_f32 v70, v92, v93\n"
        "v_cvt_pk_bf16_f32 v71, v94, v95\n"
        "v_mfma_f32_16x16x32_bf16 v[64:67], v[80:83], v[28:31], 0\n"
        "s_add_u32 s69, s69, 60\n"
        "v_mov_b64 v[24:25], v[18:19]\n"
        "v_cvt_pk_bf16_f32 v76, v86, v87\n"
        "v_cvt_pk_bf16_f32 v77, v88, v89\n"
        "v_mfma_f32_16x16x32_bf16 v[92:95], v[68:71], v[48:51], 0\n"
        "s_lshl_b64 s[54:55], -1, s69\n"
        "v_mov_b64 v[44:45], v[38:39]\n"
        "v_cvt_pk_bf16_f32 v86, v96, v97\n"
        "v_cvt_pk_bf16_f32 v87, v98, v99\n"
        "v_mfma_f32_16x16x32_bf16 v[96:99], v[74:77], v[22:25], 0\n"
        "s_lshl_b64 s[56:57], s[54:55], 24\n"
        "v_cvt_pk_bf16_f32 v72, v100, v101\n"
        "v_cvt_pk_bf16_f32 v73, v102, v103\n"
        "v_mfma_f32_16x16x32_bf16 v[100:103], v[84:87], v[42:45], 0\n"
        "s_ashr_i64 s[60:61], s[54:55], 8\n"
        "v_cvt_pk_bf16_f32 v82, v56, v57\n"
        "v_cvt_pk_bf16_f32 v83, v58, v59\n"
        "v_mfma_f32_16x16x32_bf16 v[86:89], v[72:75], v[30:33], 0\n"
        "s_mov_b32 s58, s61\n"
        "v_cndmask_b32_dpp v31, v17, v37, vcc row_ror:4 row_mask:0xf bank_mask:0xf\n"
        "v_mov_b64 v[36:37], v[18:19]\n"
        "v_mfma_f32_16x16x32_bf16 v[70:73], v[82:85], v[50:53], 0\n"
        "s_mov_b32 s59, s56\n"
        "v_mov_b64 v[56:57], v[38:39]\n"
        "v_cvt_pk_bf16_f32 v58, v60, v61\n"
        "v_mfma_f32_16x16x32_bf16 v[76:79], v[52:55], v[216:219], 0\n"
        "v_cndmask_b32_e64 v30, v5, v8, s[56:57]\n"
        "v_cvt_pk_bf16_f32 v59, v62, v63\n"
        "v_mfma_f32_16x16x32_bf16 v[76:79], v[46:49], v[212:215], v[76:79]\n"
        "v_cndmask_b32_e64 v50, v6, v9, s[58:59]\n"
        "v_cvt_pk_bf16_f32 v62, v104, v105\n"
        "v_mfma_f32_16x16x32_bf16 v[76:79], v[40:43], v[208:211], v[76:79]\n"
        "v_cndmask_b32_e64 v39, v7, v10, s[60:61]\n"
        "v_cvt_pk_bf16_f32 v63, v106, v107\n"
        "v_mfma_f32_16x16x32_bf16 v[104:107], v[32:35], v[216:219], 0\n"
        "s_cmp_eq_u32 s37, 4\n"
        "v_cvt_pk_bf16_f32 v60, v64, v65\n"
        "v_cvt_pk_bf16_f32 v61, v66, v67\n"
        "v_mfma_f32_16x16x32_bf16 v[104:107], v[26:29], v[212:215], v[104:107]\n"
        "s_cbranch_scc1 L12_%=\n"
        "L13_%=:\n"
        "v_cvt_pk_bf16_f32 v16, v96, v97\n"
        "v_cvt_pk_bf16_f32 v17, v98, v99\n"
        "v_mfma_f32_16x16x32_bf16 v[104:107], v[20:23], v[208:211], v[104:107]\n"
        "s_cmp_lt_u32 s34, s14\n"
        "v_cvt_pk_bf16_f32 v18, v86, v87\n"
        "v_cvt_pk_bf16_f32 v66, v76, v77\n"
        "v_cvt_pk_bf16_f32 v67, v78, v79\n"
        "v_mfma_f32_16x16x32_bf16 v[96:99], v[20:23], v[244:247], 0\n"
        "s_cselect_b32 s38, s38, 0x80000000\n"
        "v_cvt_pk_bf16_f32 v19, v88, v89\n"
        "v_cvt_pk_bf16_f32 v64, v92, v93\n"
        "v_mfma_f32_16x16x32_bf16 v[96:99], v[26:29], v[248:251], v[96:99]\n"
        "s_add_u32 s39, s38, 100270464\n"
        "v_cvt_pk_bf16_f32 v78, v104, v105\n"
        "v_cvt_pk_bf16_f32 v79, v106, v107\n"
        "v_mfma_f32_16x16x32_bf16 v[96:99], v[32:35], v[252:255], v[96:99]\n"
        "v_cvt_pk_bf16_f32 v65, v94, v95\n"
        "v_cvt_pk_bf16_f32 v86, v100, v101\n"
        "v_mfma_f32_16x16x32_bf16 v[104:107], v[40:43], v[244:247], 0\n"
        "v_cvt_pk_bf16_f32 v87, v102, v103\n"
        "v_cvt_pk_bf16_f32 v88, v70, v71\n"
        "v_mfma_f32_16x16x32_bf16 v[104:107], v[46:49], v[248:251], v[104:107]\n"
        "v_cvt_pk_bf16_f32 v89, v72, v73\n"
        "v_mul_f32_dpp v38, v31, v108 quad_perm:[0,0,0,0] row_mask:0xf bank_mask:0xf\n"
        "v_mfma_f32_16x16x32_bf16 v[104:107], v[52:55], v[252:255], v[104:107]\n"
        "v_cvt_pk_bf16_f32 v82, v96, v97\n"
        "v_cvt_pk_bf16_f32 v83, v98, v99\n"
        "v_mfma_f32_16x16x32_bf16 v[96:99], v[78:81], v[24:27], 0\n"
        "s_waitcnt vmcnt(0)\n"
        "ds_read_b128 v[20:23], v0\n"
        "ds_read_b128 v[40:43], v0 offset:768\n"
        "ds_read_b128 v[26:29], v0 offset:64\n"
        "v_mfma_f32_16x16x32_bf16 v[100:103], v[66:69], v[44:47], 0\n"
        "s_add_u32 s75, s75, 1\n"
        "ds_read_b128 v[46:49], v0 offset:832\n"
        "v_cvt_pk_bf16_f32 v70, v104, v105\n"
        "v_cvt_pk_bf16_f32 v71, v106, v107\n"
        "v_cvt_pk_bf16_f32 v76, v96, v97\n"
        "v_mfma_f32_16x16x32_bf16 v[104:107], v[80:83], v[34:37], 0\n"
        "s_cmp_lt_u32 s75, 4\n"
        "ds_read_b128 v[32:35], v0 offset:128\n"
        "v_cvt_pk_bf16_f32 v77, v98, v99\n"
        "v_mfma_f32_16x16x32_bf16 v[96:99], v[68:71], v[54:57], 0\n"
        "s_cbranch_scc1 L19_%=\n"
        "L20_%=:\n"
        "ds_read_b128 v[52:55], v0 offset:896\n"
        "v_cvt_pk_bf16_f32 v70, v100, v101\n"
        "v_cvt_pk_bf16_f32 v71, v102, v103\n"
        "v_cvt_pk_bf16_f32 v78, v104, v105\n"
        "v_cvt_pk_bf16_f32 v79, v106, v107\n"
        "v_mfma_f32_16x16x32_bf16 v[92:95], v[58:61], v[58:61], 0\n"
        "v_fmac_f32_dpp v38, v31, v110 quad_perm:[1,1,1,1] row_mask:0xf bank_mask:0xf\n"
        "v_cvt_pk_bf16_f32 v72, v96, v97\n"
        "v_mfma_f32_16x16x32_bf16 v[92:95], v[16:19], v[16:19], v[92:95]\n"
        "v_cvt_pk_bf16_f32 v73, v98, v99\n"
        "v_fmac_f32_dpp v38, v31, v111 quad_perm:[2,2,2,2] row_mask:0xf bank_mask:0xf\n"
        "v_mfma_f32_16x16x32_bf16 v[92:95], v[76:79], v[76:79], v[92:95]\n"
        "v_mfma_f32_16x16x32_bf16 v[96:99], v[62:65], v[62:65], 0\n"
        "v_fmac_f32_dpp v38, v31, v109 quad_perm:[3,3,3,3] row_mask:0xf bank_mask:0xf\n"
        "v_add_f32 v38, v38, v90\n"
        "v_mfma_f32_16x16x32_bf16 v[96:99], v[86:89], v[86:89], v[96:99]\n"
        "v_mfma_f32_16x16x32_bf16 v[96:99], v[70:73], v[70:73], v[96:99]\n"
        "s_cmp_lt_u32 s30, s14\n"
        "s_cbranch_scc1 PTOP_%=\n"
        "s_waitcnt lgkmcnt(0)\n"
        "s_nop 15\n"
        "L21_%=:\n"
        "ds_read_b32 v255, v91 offset:8\n"
        "s_waitcnt lgkmcnt(0)\n"
        "v_readfirstlane_b32 s40, v255\n"
        "s_nop 3\n"
        "s_cmp_ge_i32 s40, s68\n"
        "s_cbranch_scc1 L22_%=\n"
        "s_sleep 1\n"
        "s_sub_u32 s41, s41, 1\n"
        "s_cbranch_scc0 L21_%=\n"
        "s_endpgm\n"
        "L22_%=:\n"
        "v_cndmask_b32_e64 v255, v92, v93, s[48:49]\n"
        "v_cndmask_b32_e64 v254, v96, v97, s[48:49]\n"
        "v_cndmask_b32_e64 v255, v255, v94, s[50:51]\n"
        "v_cndmask_b32_e64 v254, v254, v98, s[50:51]\n"
        "v_cndmask_b32_e64 v255, v255, v95, s[52:53]\n"
        "v_cndmask_b32_e64 v254, v254, v99, s[52:53]\n"
        "v_mul_f32 v253, s28, v38\n"
        "ds_write_b128 v12, v[58:61]\n"
        "v_cndmask_b32_dpp v255, v254, v255, vcc row_ror:4 row_mask:0xf bank_mask:0xf\n"
        "v_fma_f32 v253, -v253, v38, v255\n"
        "v_add_f32_e64 v253, s29, |v253|\n"
        "v_rsq_f32 v255, v253\n"
        "ds_write_b128 v12, v[62:65] offset:832\n"
        "ds_write_b128 v12, v[16:19] offset:64\n"
        "ds_write_b128 v12, v[86:89] offset:896\n"
        "ds_write_b128 v12, v[76:79] offset:128\n"
        "ds_write_b128 v12, v[70:73] offset:960\n"
        "s_mov_b64 exec, s[46:47]\n"
        "ds_write_b32 v13, v255\n"
        "s_mov_b64 exec, -1\n"
        "s_mov_b64 exec, 1\n"
        "ds_inc_u32 v91, v11 offset:0\n"
        "s_mov_b64 exec, -1\n"
        "s_or_b32 s40, s18, s2\n"
        "s_cbranch_scc1 L31_%=\n"
        "L30_%=:\n"
        "s_mov_b64 exec, 1\n"
        "v_mov_b32 v58, 0\n"
        "v_mov_b32 v59, 1\n"
        "global_atomic_add v60, v58, v59, s[24:25] offset:256 sc0\n"
        "s_mov_b64 exec, -1\n"
        "s_mov_b64 exec, 1\n"
        "v_mov_b32 v58, 0\n"
        "v_mov_b32 v59, 0\n"
        "global_atomic_add v60, v58, v59, s[24:25] offset:0 sc0\n"
        "s_mov_b64 exec, -1\n"
        "s_waitcnt vmcnt(0)\n"
        "v_readfirstlane_b32 s40, v60\n"
        "s_nop 3\n"
        "s_cmp_ge_u32 s40, 512\n"
        "s_cbranch_scc1 L31_%=\n"
        "s_sleep 8\n"
        "s_sub_u32 s41, s41, 64\n"
        "s_cbranch_scc0 L30_%=\n"
        "L31_%=:\n"
        "s_endpgm\n"
        "L3_%=:\n"
        "s_nop 7\n"
        "L1_%=:\n"
        "ds_read_b32 v103, v91 offset:8\n"
        "s_waitcnt lgkmcnt(0)\n"
        "v_readfirstlane_b32 s40, v103\n"
        "s_nop 3\n"
        "s_cmp_ge_i32 s40, s68\n"
        "s_cbranch_scc1 L2_%=\n"
        "s_sleep 1\n"
        "s_sub_u32 s41, s41, 1\n"
        "s_cbranch_scc0 L1_%=\n"
        "s_endpgm\n"
        "L2_%=:\n"
        "s_nop 1\n"
        "s_branch L4_%=\n"
        "L8_%=:\n"
        "s_nop 7\n"
        "ds_read_u16 v12, v14 offset:19564\n"
        "ds_read_u16 v13, v14 offset:19566\n"
        "s_cmp_ge_u32 s30, 10\n"
        "s_cselect_b32 s69, 10, 0\n"
        "s_cmp_ge_u32 s30, 20\n"
        "s_cselect_b32 s69, 20, s69\n"
        "s_sub_u32 s69, s30, s69\n"
        "s_mul_i32 s69, s69, 832\n"
        "s_waitcnt lgkmcnt(0)\n"
        "v_add_u32 v12, s69, v12\n"
        "v_subrev_u32 v51, 8320, v12\n"
        "v_min_u32 v12, v12, v51\n"
        "v_add_u32 v12, 101728, v12\n"
        "v_add_u32 v13, s69, v13\n"
        "v_subrev_u32 v51, 8320, v13\n"
        "v_min_u32 v13, v13, v51\n"
        "v_add_u32 v13, 101728, v13\n"
        "s_nop 1\n"
        "s_branch L9_%=\n"
        "L10_%=:\n"
        "s_nop 7\n"
        "v_mov_b32 v51, 110048\n"
        "L6_%=:\n"
        "ds_read_b32 v83, v51 offset:64\n"
        "s_waitcnt lgkmcnt(0)\n"
        "v_readfirstlane_b32 s40, v83\n"
        "s_nop 3\n"
        "s_cmp_ge_i32 s40, 1\n"
        "s_cbranch_scc1 L7_%=\n"
        "s_sleep 1\n"
        "s_sub_u32 s41, s41, 1\n"
        "s_cbranch_scc0 L6_%=\n"
        "s_endpgm\n"
        "L7_%=:\n"
        "v_mbcnt_lo_u32_b32 v83, -1, 0\n"
        "v_lshl_add_u32 v51, v83, 2, v51\n"
        "ds_read_b32 v83, v51 offset:68\n"
        "s_mov_b32 s14, s40\n"
        "s_mov_b32 s76, -1\n"
        "s_waitcnt lgkmcnt(0)\n"
        "v_readlane_b32 s22, v83, 0\n"
        "v_readlane_b32 s23, v83, 1\n"
        "s_nop 3\n"
        "s_nop 1\n"
        "s_branch L11_%=\n"
        "L12_%=:\n"
        "s_nop 7\n"
        "ds_read_b32 v30, v14 offset:19552\n"
        "ds_read_b32 v50, v14 offset:19556\n"
        "ds_read_b32 v39, v14 offset:19560\n"
        "s_mov_b32 s35, 6\n"
        "s_mov_b32 s38, 0\n"
        "s_mov_b32 s69, 1344\n"
        "s_waitcnt lgkmcnt(0)\n"
        "v_lshrrev_b32 v51, 28, v30\n"
        "v_add_u32 v51, s34, v51\n"
        "v_and_b32 v30, 0x0fffffff, v30\n"
        "v_mad_u32_u24 v30, v51, s69, v30\n"
        "v_mov_b32 v83, s15\n"
        "v_cmpx_le_u32 vcc, s72, v51\n"
        "v_mov_b32 v83, s71\n"
        "s_mov_b64 exec, -1\n"
        "v_cmpx_le_u32 vcc, 20, v51\n"
        "v_mov_b32 v83, s22\n"
        "s_mov_b64 exec, -1\n"
        "v_cmpx_le_u32 vcc, 21, v51\n"
        "v_mov_b32 v83, s23\n"
        "s_mov_b64 exec, -1\n"
        "v_add_u32 v30, v30, v83\n"
        "v_lshrrev_b32 v51, 28, v50\n"
        "v_add_u32 v51, s34, v51\n"
        "v_and_b32 v50, 0x0fffffff, v50\n"
        "v_mad_u32_u24 v50, v51, s69, v50\n"
        "v_mov_b32 v83, s15\n"
        "v_cmpx_le_u32 vcc, s72, v51\n"
        "v_mov_b32 v83, s71\n"
        "s_mov_b64 exec, -1\n"
        "v_cmpx_le_u32 vcc, 20, v51\n"
        "v_mov_b32 v83, s22\n"
        "s_mov_b64 exec, -1\n"
        "v_cmpx_le_u32 vcc, 21, v51\n"
        "v_mov_b32 v83, s23\n"
        "s_mov_b64 exec, -1\n"
        "v_add_u32 v50, v50, v83\n"
        "v_lshrrev_b32 v51, 28, v39\n"
        "v_add_u32 v51, s34, v51\n"
        "v_and_b32 v39, 0x0fffffff, v39\n"
        "v_mad_u32_u24 v39, v51, s69, v39\n"
        "v_mov_b32 v83, s15\n"
        "v_cmpx_le_u32 vcc, s72, v51\n"
        "v_mov_b32 v83, s71\n"
        "s_mov_b64 exec, -1\n"
        "v_cmpx_le_u32 vcc, 20, v51\n"
        "v_mov_b32 v83, s22\n"
        "s_mov_b64 exec, -1\n"
        "v_cmpx_le_u32 vcc, 21, v51\n"
        "v_mov_b32 v83, s23\n"
        "s_mov_b64 exec, -1\n"
        "v_add_u32 v39, v39, v83\n"
        "s_nop 4\n"
        "s_mov_b64 vcc, s[44:45]\n"
        "s_nop 1\n"
        "s_branch L13_%=\n"
        "L19_%=:\n"
        "s_nop 7\n"
        "v_readfirstlane_b32 s62, v15\n"
        "s_cmp_eq_u32 s75, 1\n"
        "s_cbranch_scc1 L14_%=\n"
        "s_cmp_ge_u32 s75, 2\n"
        "s_cbranch_scc1 L15_%=\n"
        "s_mov_b32 s27, 20\n"
        "s_cmp_lg_u32 s2, 0\n"
        "s_cbranch_scc1 L18_%=\n"
        "s_not_b32 s26, s26\n"
        "s_mov_b64 exec, 1\n"
        "v_mov_b32 v51, 0\n"
        "v_mov_b32 v83, 1\n"
        "global_atomic_add v15, v51, v83, s[24:25] offset:256 sc0\n"
        "s_mov_b64 exec, -1\n"
        "s_branch L16_%=\n"
        "L18_%=:\n"
        "s_mov_b64 exec, 1\n"
        "v_mov_b32 v51, 0\n"
        "global_load_dword v15, v51, s[24:25] offset:256 sc1\n"
        "s_mov_b64 exec, -1\n"
        "s_branch L16_%=\n"
        "L14_%=:\n"
        "s_cmp_lg_u32 s62, s26\n"
        "s_cbranch_scc1 L17_%=\n"
        "s_sleep 8\n"
        "s_mov_b64 exec, 1\n"
        "v_mov_b32 v51, 0\n"
        "global_load_dword v15, v51, s[24:25] offset:256 sc1\n"
        "s_mov_b64 exec, -1\n"
        "s_waitcnt vmcnt(0)\n"
        "v_readfirstlane_b32 s62, v15\n"
        "s_sub_u32 s41, s41, 64\n"
        "s_cbranch_scc0 L14_%=\n"
        "s_endpgm\n"
        "L15_%=:\n"
        "s_cmp_lt_u32 s62, 209\n"
        "s_addc_u32 s27, s27, 0\n"
        "s_min_u32 s62, s62, 208\n"
        "s_add_u32 s62, s62, 5120\n"
        "s_mul_i32 s64, s62, 57457\n"
        "s_lshr_b32 s64, s64, 22\n"
        "s_sub_u32 s62, s62, 18\n"
        "s_sub_u32 s62, s62, s75\n"
        "s_mul_i32 s62, s62, 1344\n"
        "s_mul_i32 s64, s64, 588672\n"
        "s_add_u32 s62, s62, s64\n"
        "s_lshl2_add_u32 s64, s75, 110048\n"
        "v_mov_b32 v51, s64\n"
        "v_mov_b32 v83, s62\n"
        "ds_write_b32 v51, v83 offset:60\n"
        "s_waitcnt lgkmcnt(0)\n"
        "s_cmp_lt_u32 s75, 3\n"
        "s_cbranch_scc1 L17_%=\n"
        "v_mov_b32 v83, s27\n"
        "ds_write_b32 v51, v83 offset:52\n"
        "s_waitcnt lgkmcnt(0)\n"
        "s_branch L16_%=\n"
        "L17_%=:\n"
        "s_mov_b64 exec, 1\n"
        "v_mov_b32 v51, 0\n"
        "v_mov_b32 v83, 1\n"
        "global_atomic_add v15, v51, v83, s[24:25] offset:0 sc0\n"
        "s_mov_b64 exec, -1\n"
        "L16_%=:\n"
        "s_nop 1\n"
        "s_branch L20_%=\n"
        "CONS_%=:\n"
        "s_mov_b32 s41, 0x400000\n"
        "s_mov_b32 s48, 0xffffffff\n"
        "s_mov_b32 s49, 0xffff0000\n"
        "s_mov_b32 s50, 0x33333333\n"
        "s_mov_b32 s51, 0x33333333\n"
        "s_mov_b32 s56, 0x0\n"
        "s_mov_b32 s57, 0xfeff0000\n"
        "s_mov_b32 s62, 0x11111111\n"
        "s_mov_b32 s63, 0x11111111\n"
        "s_mov_b64 s[44:45], s[12:13]\n"
        "s_and_b32 s45, s45, 0xffff\n"
        "s_mov_b32 s47, 0x20000\n"
        "s_mov_b32 s75, 832\n"
        "s_mov_b32 s74, 110048\n"
        "s_mov_b32 s76, 17\n"
        "s_sub_u32 s73, s18, 7488\n"
        "s_mov_b32 s28, 0xffff0000\n"
        "s_mov_b32 s31, 0\n"
        "s_add_u32 s72, s15, s17\n"
        "s_add_u32 s70, s72, 588672\n"
        "s_sub_u32 s71, 73, s16\n"
        "s_mov_b32 s35, s72\n"
        "s_mov_b32 s36, 0x20000000\n"
        "s_mov_b32 s69, 40064\n"
        "v_mov_b32 v21, 110048\n"
        "L56_%=:\n"
        "ds_read_b32 v20, v21 offset:52\n"
        "s_waitcnt lgkmcnt(0)\n"
        "v_readfirstlane_b32 s40, v20\n"
        "s_nop 3\n"
        "s_cmp_ge_i32 s40, 6\n"
        "s_cbranch_scc1 L57_%=\n"
        "s_nop 0\n"
        "s_sub_u32 s41, s41, 1\n"
        "s_cbranch_scc0 L56_%=\n"
        "s_endpgm\n"
        "L57_%=:\n"
        "s_setprio 0\n"
        "v_add_u32 v20, 2, v1\n"
        "v_mov_b32 v21, 0x3f80\n"
        "s_mov_b32 exec_lo, 0\n"
        "s_mov_b32 exec_hi, 0x1000000\n"
        "ds_write_b16 v20, v21\n"
        "s_mov_b64 exec, -1\n"
        "v_add_u32 v20, 61664, v0\n"
        "ds_read_b128 v[168:171], v20 offset:0\n"
        "ds_read_b128 v[172:175], v20 offset:1024\n"
        "ds_read_b128 v[176:179], v20 offset:2048\n"
        "ds_read_b128 v[180:183], v20 offset:3072\n"
        "ds_read_b128 v[184:187], v20 offset:4096\n"
        "ds_read_b128 v[188:191], v20 offset:5120\n"
        "ds_read_b128 v[192:195], v20 offset:6144\n"
        "ds_read_b128 v[196:199], v20 offset:7168\n"
        "ds_read_b128 v[200:203], v20 offset:8192\n"
        "ds_read_b128 v[204:207], v20 offset:9216\n"
        "ds_read_b128 v[208:211], v20 offset:10240\n"
        "ds_read_b128 v[212:215], v20 offset:11264\n"
        "ds_read_b128 v[216:219], v20 offset:12288\n"
        "ds_read_b128 v[220:223], v20 offset:13312\n"
        "ds_read_b128 v[224:227], v20 offset:14336\n"
        "ds_read_b128 v[228:231], v20 offset:15360\n"
        "ds_read_b128 v[232:235], v20 offset:16384\n"
        "ds_read_b128 v[236:239], v20 offset:17408\n"
        "ds_read_b128 v[240:243], v0 offset:0\n"
        "ds_read_b128 v[244:247], v0 offset:1024\n"
        "ds_read_b128 v[248:251], v0 offset:2048\n"
        "ds_read_b128 v[252:255], v0 offset:3088\n"
        "v_mov_b32 v21, 110048\n"
        "s_waitcnt lgkmcnt(0)\n"
        "s_mov_b64 exec, 1\n"
        "ds_inc_u32 v21, v1 offset:12\n"
        "s_mov_b64 exec, -1\n"
        "L58_%=:\n"
        "ds_read_b32 v20, v21 offset:0\n"
        "s_waitcnt lgkmcnt(0)\n"
        "v_readfirstlane_b32 s40, v20\n"
        "s_nop 3\n"
        "s_cmp_ge_i32 s40, 11\n"
        "s_cbranch_scc1 L59_%=\n"
        "s_sleep 1\n"
        "s_sub_u32 s41, s41, 1\n"
        "s_cbranch_scc0 L58_%=\n"
        "s_endpgm\n"
        "L59_%=:\n"
        "ds_read_b128 v[104:107], v7 offset:0\n"
        "ds_read_b128 v[56:59], v7 offset:3328\n"
        "ds_read_b128 v[108:111], v7 offset:64\n"
        "ds_read_b128 v[60:63], v7 offset:3392\n"
        "ds_read_b128 v[152:155], v7 offset:128\n"
        "ds_read_b128 v[132:135], v7 offset:3456\n"
        "s_waitcnt lgkmcnt(0)\n"
        "CFULL_%=:\n"
        "s_call_b64 s[64:65], CGRP_%=\n"
        "CTOP_%=:\n"
        "s_waitcnt lgkmcnt(6)\n"
        "v_mfma_f32_16x16x32_bf16 v[32:35], v[168:171], v[104:107], 0\n"
        "v_cvt_pk_bf16_f32 v160, v100, v148\n"
        "v_cvt_pk_bf16_f32 v161, v28, v84\n"
        "s_waitcnt lgkmcnt(4)\n"
        "v_mfma_f32_16x16x32_bf16 v[32:35], v[172:175], v[108:111], v[32:35]\n"
        "v_cvt_pk_bf16_f32 v162, v44, v156\n"
        "v_cvt_pk_bf16_f32 v164, v101, v149\n"
        "s_waitcnt lgkmcnt(2)\n"
        "v_mfma_f32_16x16x32_bf16 v[32:35], v[176:179], v[152:155], v[32:35]\n"
        "ds_read_b32 v143, v10\n"
        "v_cvt_pk_bf16_f32 v165, v29, v85\n"
        "v_mfma_f32_16x16x32_bf16 v[52:55], v[188:191], v[152:155], 0\n"
        "v_cvt_pk_bf16_f32 v166, v45, v157\n"
        "v_cvt_pk_bf16_f32 v124, v102, v150\n"
        "v_mfma_f32_16x16x32_bf16 v[52:55], v[184:187], v[108:111], v[52:55]\n"
        "v_cvt_pk_bf16_f32 v125, v30, v86\n"
        "v_cvt_pk_bf16_f32 v126, v46, v158\n"
        "v_mfma_f32_16x16x32_bf16 v[52:55], v[180:183], v[104:107], v[52:55]\n"
        "s_add_u32 s37, s36, 196224\n"
        "v_cvt_pk_bf16_f32 v20, v32, v33\n"
        "v_cvt_pk_bf16_f32 v21, v34, v35\n"
        "v_cvt_pk_bf16_f32 v144, v103, v151\n"
        "v_mfma_f32_16x16x32_bf16 v[32:35], v[192:195], v[104:107], 0\n"
        "v_cvt_pk_bf16_f32 v145, v31, v87\n"
        "v_cvt_pk_bf16_f32 v146, v47, v159\n"
        "v_mfma_f32_16x16x32_bf16 v[32:35], v[196:199], v[108:111], v[32:35]\n"
        "v_cvt_pk_bf16_f32 v22, v52, v53\n"
        "v_cvt_pk_bf16_f32 v23, v54, v55\n"
        "v_mfma_f32_16x16x32_bf16 v[32:35], v[200:203], v[152:155], v[32:35]\n"
        "buffer_store_dwordx3 v[160:162], v12, s[8:11], s37 offen offset:384 sc0 sc1 nt\n"
        "v_cvt_pk_bf16_f32 v138, v48, v40\n"
        "v_cvt_pk_bf16_f32 v139, v88, v36\n"
        "v_cvt_pk_bf16_f32 v140, v72, v76\n"
        "v_cvt_pk_bf16_f32 v100, v49, v41\n"
        "v_cvt_pk_bf16_f32 v101, v89, v37\n"
        "v_mfma_f32_16x16x32_bf16 v[28:31], v[212:215], v[152:155], 0\n"
        "v_cvt_pk_bf16_f32 v102, v73, v77\n"
        "v_cvt_pk_bf16_f32 v24, v32, v33\n"
        "v_mfma_f32_16x16x32_bf16 v[28:31], v[208:211], v[108:111], v[28:31]\n"
        "s_add_u32 s37, s36, 196224\n"
        "v_cvt_pk_bf16_f32 v25, v34, v35\n"
        "v_cvt_pk_bf16_f32 v156, v50, v42\n"
        "v_mfma_f32_16x16x32_bf16 v[28:31], v[204:207], v[104:107], v[28:31]\n"
        "v_cvt_pk_bf16_f32 v157, v90, v38\n"
        "v_cvt_pk_bf16_f32 v158, v74, v78\n"
        "v_cvt_pk_bf16_f32 v120, v51, v43\n"
        "v_mfma_f32_16x16x32_bf16 v[52:55], v[216:219], v[104:107], 0\n"
        "s_setprio 2\n"
        "ds_read_b128 v[44:47], v16 offset:128\n"
        "v_cvt_pk_bf16_f32 v121, v91, v39\n"
        "v_mfma_f32_16x16x32_bf16 v[52:55], v[220:223], v[108:111], v[52:55]\n"
        "ds_read_b128 v[32:35], v5\n"
        "v_cvt_pk_bf16_f32 v26, v28, v29\n"
        "v_mfma_f32_16x16x32_bf16 v[52:55], v[224:227], v[152:155], v[52:55]\n"
        "buffer_store_dwordx3 v[164:166], v13, s[8:11], s37 offen offset:1152 sc0 sc1 nt\n"
        "v_cvt_pk_bf16_f32 v27, v30, v31\n"
        "v_cvt_pk_bf16_f32 v122, v75, v79\n"
        "s_waitcnt lgkmcnt(2)\n"
        "v_and_b32 v150, s28, v143\n"
        "v_and_b32 v160, s28, v143\n"
        "v_sub_f32 v167, v143, v150\n"
        "v_cvt_pk_bf16_f32 v151, v167, v150\n"
        "v_mfma_f32_16x16x32_bf16 v[48:51], v[236:239], v[152:155], 0\n"
        "s_add_u32 s37, s36, 294336\n"
        "v_cvt_pk_bf16_f32 v28, v52, v53\n"
        "v_cvt_pk_bf16_f32 v29, v54, v55\n"
        "v_mfma_f32_16x16x32_bf16 v[48:51], v[232:235], v[108:111], v[48:51]\n"
        "ds_read_b128 v[112:115], v7 offset:6656\n"
        "v_cvt_pk_bf16_f32 v161, v160, v167\n"
        "v_mfma_f32_16x16x32_bf16 v[48:51], v[228:231], v[104:107], v[48:51]\n"
        "s_waitcnt lgkmcnt(1)\n"
        "ds_read_b128 v[116:119], v7 offset:6720\n"
        "ds_read_b128 v[80:83], v7 offset:6784\n"
        "v_mfma_f32_16x16x32_bf16 v[52:55], v[44:47], v[152:155], 0\n"
        "ds_read_b32 v167, v11 offset:192\n"
        "v_readlane_b32 s58, v143, 56\n"
        "v_smfmac_f32_16x16x64_bf16 v[52:55], v[32:35], v[104:111], v4\n"
        "buffer_store_dwordx3 v[124:126], v12, s[8:11], s37 offen offset:576 sc0 sc1 nt\n"
        "v_cvt_pk_bf16_f32 v30, v48, v49\n"
        "v_cvt_pk_bf16_f32 v31, v50, v51\n"
        "v_mfma_f32_16x16x32_bf16 v[76:79], v[44:47], v[132:135], 0\n"
        "s_add_u32 s37, s36, 392448\n"
        "v_smfmac_f32_16x16x64_bf16 v[76:79], v[32:35], v[56:63], v4\n"
        "s_waitcnt lgkmcnt(1)\n"
        "v_mfma_f32_16x16x32_bf16 v[36:39], v[44:47], v[80:83], 0\n"
        "s_waitcnt lgkmcnt(0)\n"
        "v_add_f32_dpp v167, v52, v53 quad_perm:[0,1,2,3] row_mask:0x1 bank_mask:0xf\n"
        "v_smfmac_f32_16x16x64_bf16 v[36:39], v[32:35], v[112:119], v4\n"
        "buffer_store_dwordx3 v[144:146], v12, s[8:11], s37 offen sc0 sc1 nt\n"
        "v_mfma_f32_16x16x4_4b_bf16 v[40:55], v[150:151], v[160:161], 0 blgp:4\n"
        "v_mfma_f32_16x16x32_bf16 v[124:127], v[104:107], v[20:23], 0\n"
        "v_add_f32_dpp v167, v76, v77 quad_perm:[0,1,2,3] row_mask:0x2 bank_mask:0xf\n"
        "v_mfma_f32_16x16x32_bf16 v[124:127], v[108:111], v[24:27], v[124:127]\n"
        "s_add_u32 s37, s36, 392448\n"
        "v_mfma_f32_16x16x32_bf16 v[124:127], v[152:155], v[28:31], v[124:127]\n"
        "v_mfma_f32_16x16x32_bf16 v[72:75], v[132:135], v[28:31], 0\n"
        "ds_read_b128 v[64:67], v8 offset:128\n"
        "v_add_f32_dpp v167, v36, v37 quad_perm:[0,1,2,3] row_mask:0x4 bank_mask:0xf\n"
        "v_mfma_f32_16x16x32_bf16 v[72:75], v[60:63], v[24:27], v[72:75]\n"
        "ds_read2_b32 v[92:93], v6 offset0:0 offset1:2\n"
        "v_mul_f32 v167, v167, v143\n"
        "v_mfma_f32_16x16x32_bf16 v[72:75], v[56:59], v[20:23], v[72:75]\n"
        "buffer_store_dwordx3 v[138:140], v15, s[8:11], s37 offen offset:768 sc0 sc1 nt\n"
        "ds_read2_b32 v[94:95], v6 offset0:8 offset1:10\n"
        "v_mul_f32 v124, v124, v40\n"
        "v_mul_f32 v125, v125, v41\n"
        "v_mul_f32 v126, v126, v42\n"
        "v_mul_f32 v127, v127, v43\n"
        "v_mfma_f32_16x16x32_bf16 v[128:131], v[112:115], v[20:23], 0\n"
        "s_add_u32 s37, s36, 490560\n"
        "v_exp_f32 v124, v124\n"
        "v_mfma_f32_16x16x32_bf16 v[128:131], v[116:119], v[24:27], v[128:131]\n"
        "s_mov_b64 exec, s[56:57]\n"
        "v_mov_b32 v167, 0xff800000\n"
        "s_mov_b64 exec, -1\n"
        "v_mfma_f32_16x16x32_bf16 v[128:131], v[80:83], v[28:31], v[128:131]\n"
        "v_mul_f32 v167, s58, v167\n"
        "v_mul_f32 v72, v72, v44\n"
        "s_waitcnt lgkmcnt(0)\n"
        "v_mfma_f32_16x16x32_bf16 v[96:99], v[64:67], v[28:31], 0\n"
        "v_exp_f32 v167, v167\n"
        "v_smfmac_f32_16x16x64_bf16 v[96:99], v[92:95], v[20:27], v4\n"
        "buffer_store_dwordx3 v[100:102], v12, s[8:11], s37 offen offset:192 sc0 sc1 nt\n"
        "v_add_f32_dpp v166, v167, v167 row_ror:8 row_mask:0xf bank_mask:0xf\n"
        "v_exp_f32 v125, v125\n"
        "v_exp_f32 v126, v126\n"
        "v_mfma_f32_16x16x32_bf16 v[20:23], v[168:171], v[56:59], 0\n"
        "s_add_u32 s37, s36, 490560\n"
        "v_exp_f32 v127, v127\n"
        "v_mfma_f32_16x16x32_bf16 v[20:23], v[172:175], v[60:63], v[20:23]\n"
        "v_add_f32 v165, v124, v125\n"
        "v_add_f32_dpp v166, v166, v166 row_ror:4 row_mask:0xf bank_mask:0xf\n"
        "v_mfma_f32_16x16x32_bf16 v[20:23], v[176:179], v[132:135], v[20:23]\n"
        "v_add_f32 v165, v165, v126\n"
        "v_add_f32 v165, v165, v127\n"
        "v_add_f32_dpp v166, v166, v166 row_ror:2 row_mask:0xf bank_mask:0xf\n"
        "v_mfma_f32_16x16x32_bf16 v[24:27], v[188:191], v[132:135], 0\n"
        "v_mul_f32 v73, v73, v45\n"
        "v_mul_f32 v74, v74, v46\n"
        "v_mfma_f32_16x16x32_bf16 v[24:27], v[184:187], v[60:63], v[24:27]\n"
        "v_mul_f32 v75, v75, v47\n"
        "v_cvt_pk_bf16_f32 v44, v20, v21\n"
        "v_mfma_f32_16x16x32_bf16 v[24:27], v[180:183], v[56:59], v[24:27]\n"
        "buffer_store_dwordx3 v[156:158], v14, s[8:11], s37 offen offset:960 sc0 sc1 nt\n"
        "v_cvt_pk_bf16_f32 v45, v22, v23\n"
        "v_exp_f32 v72, v72\n"
        "v_exp_f32 v73, v73\n"
        "v_exp_f32 v74, v74\n"
        "v_mfma_f32_16x16x32_bf16 v[20:23], v[192:195], v[56:59], 0\n"
        "s_add_u32 s37, s36, 588672\n"
        "v_exp_f32 v75, v75\n"
        "v_mfma_f32_16x16x32_bf16 v[20:23], v[196:199], v[60:63], v[20:23]\n"
        "v_cvt_pk_bf16_f32 v46, v24, v25\n"
        "v_cvt_pk_bf16_f32 v47, v26, v27\n"
        "v_mfma_f32_16x16x32_bf16 v[20:23], v[200:203], v[132:135], v[20:23]\n"
        "buffer_store_dwordx3 v[120:122], v12, s[8:11], s37 offen offset:384 sc0 sc1 nt\n"
        "v_add_f32 v165, v165, v72\n"
        "v_add_f32 v165, v165, v73\n"
        "v_add_f32 v165, v165, v74\n"
        "v_add_f32 v165, v165, v75\n"
        "v_mul_f32 v128, v128, v48\n"
        "v_mul_f32 v129, v129, v49\n"
        "v_mfma_f32_16x16x32_bf16 v[24:27], v[212:215], v[132:135], 0\n"
        "v_cvt_pk_bf16_f32 v48, v20, v21\n"
        "v_cvt_pk_bf16_f32 v49, v22, v23\n"
        "v_mfma_f32_16x16x32_bf16 v[24:27], v[208:211], v[60:63], v[24:27]\n"
        "v_mul_f32 v130, v130, v50\n"
        "v_mul_f32 v131, v131, v51\n"
        "v_mfma_f32_16x16x32_bf16 v[24:27], v[204:207], v[56:59], v[24:27]\n"
        "v_exp_f32 v128, v128\n"
        "v_add_f32 v96, v96, v97\n"
        "v_mfma_f32_16x16x32_bf16 v[20:23], v[216:219], v[56:59], 0\n"
        "v_exp_f32 v129, v129\n"
        "v_mfma_f32_16x16x32_bf16 v[20:23], v[220:223], v[60:63], v[20:23]\n"
        "v_exp_f32 v130, v130\n"
        "v_mfma_f32_16x16x32_bf16 v[20:23], v[224:227], v[132:135], v[20:23]\n"
        "v_exp_f32 v131, v131\n"
        "v_cvt_pk_bf16_f32 v50, v24, v25\n"
        "v_mfma_f32_16x16x32_bf16 v[76:79], v[236:239], v[132:135], 0\n"
        "v_cvt_pk_bf16_f32 v51, v26, v27\n"
        "v_add_f32 v165, v165, v128\n"
        "v_mfma_f32_16x16x32_bf16 v[76:79], v[232:235], v[60:63], v[76:79]\n"
        "v_add_f32 v165, v165, v129\n"
        "v_add_f32 v165, v165, v130\n"
        "v_mfma_f32_16x16x32_bf16 v[76:79], v[228:231], v[56:59], v[76:79]\n"
        "v_cvt_pk_bf16_f32 v40, v20, v21\n"
        "v_cvt_pk_bf16_f32 v41, v22, v23\n"
        "v_mfma_f32_16x16x4_4b_bf16 v[20:35], v[150:151], v[160:161], 0 blgp:5\n"
        "v_add_f32 v165, v165, v131\n"
        "v_mul_f32 v96, v96, v52\n"
        "v_exp_f32 v96, v96\n"
        "v_add_f32_dpp v166, v166, v166 row_ror:1 row_mask:0xf bank_mask:0xf\n"
        "v_mul_f32 v167, v167, v143\n"
        "v_cvt_pk_bf16_f32 v42, v76, v77\n"
        "v_cvt_pk_bf16_f32 v43, v78, v79\n"
        "v_add_f32_dpp v165, v96, v165 quad_perm:[0,1,2,3] row_mask:0x4 bank_mask:0xf\n"
        "v_mfma_f32_16x16x32_bf16 v[156:159], v[104:107], v[44:47], 0\n"
        "v_mfma_f32_16x16x32_bf16 v[156:159], v[108:111], v[48:51], v[156:159]\n"
        "v_permlane32_swap_b32 v165, v166\n"
        "v_mfma_f32_16x16x32_bf16 v[156:159], v[152:155], v[40:43], v[156:159]\n"
        "v_add_f32 v165, v165, v166\n"
        "v_mov_b32 v166, v165\n"
        "v_mfma_f32_16x16x32_bf16 v[76:79], v[132:135], v[40:43], 0\n"
        "v_mfma_f32_16x16x32_bf16 v[76:79], v[60:63], v[48:51], v[76:79]\n"
        "v_permlane16_swap_b32 v165, v166\n"
        "v_mfma_f32_16x16x32_bf16 v[76:79], v[56:59], v[44:47], v[76:79]\n"
        "v_add_f32 v165, v165, v166\n"
        "v_rcp_f32 v164, v165\n"
        "v_mfma_f32_16x16x32_bf16 v[88:91], v[112:115], v[44:47], 0\n"
        "v_and_b32 v162, s28, v164\n"
        "v_sub_f32 v166, v164, v162\n"
        "v_mfma_f32_16x16x32_bf16 v[88:91], v[116:119], v[48:51], v[88:91]\n"
        "v_cvt_pk_bf16_f32 v163, v162, v166\n"
        "v_min3_f32 v141, v165, v164, v143\n"
        "v_mfma_f32_16x16x32_bf16 v[88:91], v[80:83], v[40:43], v[88:91]\n"
        "v_readlane_b32 s59, v164, 32\n"
        "v_mul_f32 v156, v156, v20\n"
        "v_mul_f32 v157, v157, v21\n"
        "v_mfma_f32_16x16x32_bf16 v[120:123], v[64:67], v[40:43], 0\n"
        "v_mul_f32 v167, s59, v167\n"
        "v_cvt_pk_bf16_f32 v167, v167, 0\n"
        "v_smfmac_f32_16x16x64_bf16 v[120:123], v[92:95], v[44:51], v4\n"
        "v_mul_f32 v158, v158, v22\n"
        "v_mul_f32 v159, v159, v23\n"
        "v_mfma_f32_16x16x32_bf16 v[44:47], v[168:171], v[112:115], 0\n"
        "v_exp_f32 v156, v156\n"
        "v_mfma_f32_16x16x32_bf16 v[44:47], v[172:175], v[116:119], v[44:47]\n"
        "v_exp_f32 v157, v157\n"
        "v_mfma_f32_16x16x32_bf16 v[44:47], v[176:179], v[80:83], v[44:47]\n"
        "ds_write_b16 v1, v167\n"
        "v_add_f32 v167, v156, v157\n"
        "v_mfma_f32_16x16x32_bf16 v[20:23], v[188:191], v[80:83], 0\n"
        "v_exp_f32 v158, v158\n"
        "v_mfma_f32_16x16x32_bf16 v[20:23], v[184:187], v[116:119], v[20:23]\n"
        "v_exp_f32 v159, v159\n"
        "v_mfma_f32_16x16x32_bf16 v[20:23], v[180:183], v[112:115], v[20:23]\n"
        "v_add_f32 v167, v167, v158\n"
        "v_cvt_pk_bf16_f32 v142, v44, v45\n"
        "v_cvt_pk_bf16_f32 v143, v46, v47\n"
        "v_add_f32 v167, v167, v159\n"
        "v_mfma_f32_16x16x32_bf16 v[44:47], v[192:195], v[112:115], 0\n"
        "v_mul_f32 v76, v76, v24\n"
        "v_mul_f32 v77, v77, v25\n"
        "v_mfma_f32_16x16x32_bf16 v[44:47], v[196:199], v[116:119], v[44:47]\n"
        "v_cvt_pk_bf16_f32 v144, v20, v21\n"
        "v_cvt_pk_bf16_f32 v145, v22, v23\n"
        "v_mfma_f32_16x16x32_bf16 v[44:47], v[200:203], v[80:83], v[44:47]\n"
        "v_mul_f32 v78, v78, v26\n"
        "v_mul_f32 v79, v79, v27\n"
        "v_mul_f32 v88, v88, v28\n"
        "v_mfma_f32_16x16x32_bf16 v[20:23], v[212:215], v[80:83], 0\n"
        "v_exp_f32 v76, v76\n"
        "v_mfma_f32_16x16x32_bf16 v[20:23], v[208:211], v[116:119], v[20:23]\n"
        "v_exp_f32 v77, v77\n"
        "v_mfma_f32_16x16x32_bf16 v[20:23], v[204:207], v[112:115], v[20:23]\n"
        "v_cvt_pk_bf16_f32 v146, v44, v45\n"
        "v_cvt_pk_bf16_f32 v147, v46, v47\n"
        "v_add_f32 v167, v167, v76\n"
        "v_mfma_f32_16x16x32_bf16 v[24:27], v[216:219], v[112:115], 0\n"
        "v_exp_f32 v78, v78\n"
        "v_mfma_f32_16x16x32_bf16 v[24:27], v[220:223], v[116:119], v[24:27]\n"
        "v_exp_f32 v79, v79\n"
        "v_mfma_f32_16x16x32_bf16 v[24:27], v[224:227], v[80:83], v[24:27]\n"
        "v_cvt_pk_bf16_f32 v148, v20, v21\n"
        "v_cvt_pk_bf16_f32 v149, v22, v23\n"
        "v_add_f32 v167, v167, v77\n"
        "v_mfma_f32_16x16x32_bf16 v[20:23], v[236:239], v[80:83], 0\n"
        "v_add_f32 v167, v167, v78\n"
        "v_add_f32 v167, v167, v79\n"
        "v_mfma_f32_16x16x32_bf16 v[20:23], v[232:235], v[116:119], v[20:23]\n"
        "v_mul_f32 v89, v89, v29\n"
        "v_cvt_pk_bf16_f32 v68, v24, v25\n"
        "v_mfma_f32_16x16x32_bf16 v[20:23], v[228:231], v[112:115], v[20:23]\n"
        "v_cvt_pk_bf16_f32 v69, v26, v27\n"
        "v_mul_f32 v90, v90, v30\n"
        "v_mfma_f32_16x16x4_4b_bf16 v[38:53], v[150:151], v[162:163], 0 blgp:4\n"
        "v_exp_f32 v88, v88\n"
        "v_mul_f32 v91, v91, v31\n"
        "v_exp_f32 v89, v89\n"
        "v_add_f32 v167, v167, v88\n"
        "v_exp_f32 v90, v90\n"
        "v_cvt_pk_bf16_f32 v70, v20, v21\n"
        "v_cvt_pk_bf16_f32 v71, v22, v23\n"
        "v_mfma_f32_16x16x32_bf16 v[100:103], v[104:107], v[142:145], 0\n"
        "v_exp_f32 v91, v91\n"
        "v_mfma_f32_16x16x32_bf16 v[100:103], v[108:111], v[146:149], v[100:103]\n"
        "v_add_f32 v167, v167, v89\n"
        "v_add_f32 v167, v167, v90\n"
        "v_mfma_f32_16x16x32_bf16 v[100:103], v[152:155], v[68:71], v[100:103]\n"
        "v_mul_f32 v124, v124, v38\n"
        "v_mul_f32 v125, v125, v39\n"
        "v_add_f32 v167, v167, v91\n"
        "v_mfma_f32_16x16x32_bf16 v[84:87], v[132:135], v[68:71], 0\n"
        "v_add_f32 v120, v120, v121\n"
        "v_mul_f32 v120, v120, v32\n"
        "v_mfma_f32_16x16x32_bf16 v[84:87], v[60:63], v[146:149], v[84:87]\n"
        "v_exp_f32 v120, v120\n"
        "v_mfma_f32_16x16x32_bf16 v[84:87], v[56:59], v[142:145], v[84:87]\n"
        "v_mul_f32 v126, v126, v40\n"
        "v_add_f32_dpp v167, v120, v167 quad_perm:[0,1,2,3] row_mask:0x4 bank_mask:0xf\n"
        "v_mul_f32 v127, v127, v41\n"
        "v_mfma_f32_16x16x32_bf16 v[136:139], v[112:115], v[142:145], 0\n"
        "v_mul_f32 v72, v72, v42\n"
        "v_mul_f32 v73, v73, v43\n"
        "v_mfma_f32_16x16x32_bf16 v[136:139], v[116:119], v[146:149], v[136:139]\n"
        "v_mul_f32 v74, v74, v44\n"
        "v_mul_f32 v75, v75, v45\n"
        "v_mfma_f32_16x16x32_bf16 v[136:139], v[80:83], v[68:71], v[136:139]\n"
        "v_mul_f32 v128, v128, v46\n"
        "v_mul_f32 v129, v129, v47\n"
        "v_mul_f32 v130, v130, v48\n"
        "v_mfma_f32_16x16x4_4b_bf16 v[22:37], v[150:151], v[160:161], 0 blgp:6\n"
        "v_mul_f32 v131, v131, v49\n"
        "v_cvt_pk_bf16_f32 v160, v124, v125\n"
        "v_cvt_pk_bf16_f32 v161, v126, v127\n"
        "v_cvt_pk_bf16_f32 v162, v72, v73\n"
        "v_cvt_pk_bf16_f32 v163, v74, v75\n"
        "v_cvt_pk_bf16_f32 v164, v128, v129\n"
        "v_fma_f32 v96, v96, v50, v19\n"
        "v_cvt_pk_bf16_f32 v166, v130, v131\n"
        "v_cvt_pk_bf16_f32 v165, v96, 0\n"
        "v_mfma_f32_16x16x32_bf16 v[122:125], v[64:67], v[68:71], 0\n"
        "v_smfmac_f32_16x16x64_bf16 v[122:125], v[92:95], v[142:149], v4\n"
        "v_mfma_f32_16x16x32_bf16 v[68:71], v[104:107], v[240:243], 0\n"
        "ds_read_b128 v[40:43], v17\n"
        "v_mul_f32 v100, v100, v22\n"
        "v_mfma_f32_16x16x32_bf16 v[68:71], v[108:111], v[244:247], v[68:71]\n"
        "ds_read_b128 v[48:51], v17 offset:64\n"
        "v_mul_f32 v101, v101, v23\n"
        "v_mfma_f32_16x16x32_bf16 v[68:71], v[152:155], v[248:251], v[68:71]\n"
        "ds_read_b128 v[52:55], v17 offset:128\n"
        "v_mul_f32 v102, v102, v24\n"
        "v_mfma_f32_16x16x32_bf16 v[72:75], v[132:135], v[248:251], 0\n"
        "ds_read_b128 v[80:83], v0 offset:5136\n"
        "v_mul_f32 v103, v103, v25\n"
        "v_mfma_f32_16x16x32_bf16 v[72:75], v[60:63], v[244:247], v[72:75]\n"
        "ds_read_b128 v[96:99], v0 offset:4112\n"
        "v_mul_f32 v84, v84, v26\n"
        "v_mfma_f32_16x16x32_bf16 v[72:75], v[56:59], v[240:243], v[72:75]\n"
        "v_cvt_pk_bf16_f32 v64, v68, v69\n"
        "v_cvt_pk_bf16_f32 v65, v70, v71\n"
        "v_mul_f32 v85, v85, v27\n"
        "s_waitcnt lgkmcnt(2)\n"
        "v_mfma_f32_16x16x32_bf16 v[24:27], v[40:43], v[240:243], 0\n"
        "v_exp_f32 v100, v100\n"
        "v_mfma_f32_16x16x32_bf16 v[24:27], v[48:51], v[244:247], v[24:27]\n"
        "v_exp_f32 v101, v101\n"
        "v_mfma_f32_16x16x32_bf16 v[24:27], v[52:55], v[248:251], v[24:27]\n"
        "s_waitcnt lgkmcnt(0)\n"
        "v_cvt_pk_bf16_f32 v66, v72, v73\n"
        "v_cvt_pk_bf16_f32 v67, v74, v75\n"
        "v_exp_f32 v102, v102\n"
        "v_mfma_f32_16x16x32_bf16 v[44:47], v[52:55], v[80:83], 0\n"
        "v_exp_f32 v103, v103\n"
        "v_mfma_f32_16x16x32_bf16 v[44:47], v[48:51], v[96:99], v[44:47]\n"
        "v_add_f32 v131, v100, v101\n"
        "v_cvt_pk_bf16_f32 v124, v24, v25\n"
        "v_mfma_f32_16x16x32_bf16 v[44:47], v[40:43], v[252:255], v[44:47]\n"
        "v_cvt_pk_bf16_f32 v128, v26, v27\n"
        "v_add_f32 v131, v131, v102\n"
        "v_add_f32 v131, v131, v103\n"
        "v_mfma_f32_16x16x32_bf16 v[72:75], v[56:59], v[252:255], 0\n"
        "ds_read_b128 v[36:39], v0 offset:6176\n"
        "v_mul_f32 v86, v86, v28\n"
        "v_mfma_f32_16x16x32_bf16 v[72:75], v[60:63], v[96:99], v[72:75]\n"
        "ds_read_b128 v[92:95], v0 offset:7200\n"
        "v_mul_f32 v87, v87, v29\n"
        "v_mfma_f32_16x16x32_bf16 v[72:75], v[132:135], v[80:83], v[72:75]\n"
        "ds_read_b128 v[142:145], v0 offset:8224\n"
        "v_cvt_pk_bf16_f32 v112, v44, v45\n"
        "v_mfma_f32_16x16x32_bf16 v[24:27], v[152:155], v[80:83], 0\n"
        "v_exp_f32 v84, v84\n"
        "v_mfma_f32_16x16x32_bf16 v[24:27], v[108:111], v[96:99], v[24:27]\n"
        "v_exp_f32 v85, v85\n"
        "v_mfma_f32_16x16x32_bf16 v[24:27], v[104:107], v[252:255], v[24:27]\n"
        "v_cvt_pk_bf16_f32 v116, v46, v47\n"
        "v_cvt_pk_bf16_f32 v22, v72, v73\n"
        "s_waitcnt lgkmcnt(0)\n"
        "v_mfma_f32_16x16x32_bf16 v[44:47], v[104:107], v[36:39], 0\n"
        "v_cvt_pk_bf16_f32 v23, v74, v75\n"
        "v_add_f32 v131, v131, v84\n"
        "v_mfma_f32_16x16x32_bf16 v[44:47], v[108:111], v[92:95], v[44:47]\n"
        "v_exp_f32 v86, v86\n"
        "v_mfma_f32_16x16x32_bf16 v[44:47], v[152:155], v[142:145], v[44:47]\n"
        "v_cvt_pk_bf16_f32 v20, v24, v25\n"
        "v_cvt_pk_bf16_f32 v21, v26, v27\n"
        "v_exp_f32 v87, v87\n"
        "v_mfma_f32_16x16x32_bf16 v[96:99], v[132:135], v[142:145], 0\n"
        "ds_read_b128 v[68:71], v0 offset:11312\n"
        "v_add_f32 v131, v131, v85\n"
        "v_mfma_f32_16x16x32_bf16 v[96:99], v[60:63], v[92:95], v[96:99]\n"
        "ds_read_b128 v[146:149], v0 offset:10288\n"
        "v_add_f32 v131, v131, v86\n"
        "v_mfma_f32_16x16x32_bf16 v[96:99], v[56:59], v[36:39], v[96:99]\n"
        "ds_read_b128 v[80:83], v0 offset:9264\n"
        "v_add_f32 v131, v131, v87\n"
        "v_mfma_f32_16x16x32_bf16 v[72:75], v[40:43], v[36:39], 0\n"
        "v_mul_f32 v136, v136, v30\n"
        "v_mul_f32 v137, v137, v31\n"
        "v_mfma_f32_16x16x32_bf16 v[72:75], v[48:51], v[92:95], v[72:75]\n"
        "v_mul_f32 v138, v138, v32\n"
        "v_mul_f32 v139, v139, v33\n"
        "v_mfma_f32_16x16x32_bf16 v[72:75], v[52:55], v[142:145], v[72:75]\n"
        "v_exp_f32 v136, v136\n"
        "s_waitcnt lgkmcnt(0)\n"
        "v_mfma_f32_16x16x32_bf16 v[30:33], v[52:55], v[68:71], 0\n"
        "v_exp_f32 v137, v137\n"
        "v_mfma_f32_16x16x32_bf16 v[30:33], v[48:51], v[146:149], v[30:33]\n"
        "v_exp_f32 v138, v138\n"
        "v_mfma_f32_16x16x32_bf16 v[30:33], v[40:43], v[80:83], v[30:33]\n"
        "v_cvt_pk_bf16_f32 v92, v44, v45\n"
        "v_cvt_pk_bf16_f32 v93, v46, v47\n"
        "v_cvt_pk_bf16_f32 v94, v96, v97\n"
        "v_cvt_pk_bf16_f32 v95, v98, v99\n"
        "v_mfma_f32_16x16x32_bf16 v[96:99], v[56:59], v[80:83], 0\n"
        "ds_read_b128 v[24:27], v0 offset:12352\n"
        "v_cvt_pk_bf16_f32 v140, v72, v73\n"
        "v_mfma_f32_16x16x32_bf16 v[96:99], v[60:63], v[146:149], v[96:99]\n"
        "ds_read_b128 v[36:39], v0 offset:13376\n"
        "v_cvt_pk_bf16_f32 v144, v74, v75\n"
        "v_mfma_f32_16x16x32_bf16 v[96:99], v[132:135], v[68:71], v[96:99]\n"
        "ds_read_b128 v[44:47], v0 offset:14400\n"
        "v_exp_f32 v139, v139\n"
        "v_add_f32 v131, v131, v136\n"
        "v_add_f32 v131, v131, v137\n"
        "v_mfma_f32_16x16x32_bf16 v[72:75], v[152:155], v[68:71], 0\n"
        "v_add_f32 v131, v131, v138\n"
        "v_add_f32 v131, v131, v139\n"
        "v_mfma_f32_16x16x32_bf16 v[72:75], v[108:111], v[146:149], v[72:75]\n"
        "v_add_f32 v122, v122, v123\n"
        "v_mul_f32 v122, v122, v34\n"
        "v_mfma_f32_16x16x32_bf16 v[72:75], v[104:107], v[80:83], v[72:75]\n"
        "v_exp_f32 v122, v122\n"
        "s_waitcnt lgkmcnt(0)\n"
        "v_mfma_f32_16x16x32_bf16 v[80:83], v[104:107], v[24:27], 0\n"
        "v_add_f32_dpp v131, v122, v131 quad_perm:[0,1,2,3] row_mask:0x4 bank_mask:0xf\n"
        "v_cvt_pk_bf16_f32 v125, v30, v31\n"
        "v_mfma_f32_16x16x32_bf16 v[80:83], v[108:111], v[36:39], v[80:83]\n"
        "v_permlane32_swap_b32 v167, v131\n"
        "v_mfma_f32_16x16x32_bf16 v[80:83], v[152:155], v[44:47], v[80:83]\n"
        "v_add_f32 v167, v167, v131\n"
        "v_mov_b32 v145, v167\n"
        "v_cvt_pk_bf16_f32 v129, v32, v33\n"
        "v_cvt_pk_bf16_f32 v70, v96, v97\n"
        "v_mfma_f32_16x16x32_bf16 v[146:149], v[132:135], v[44:47], 0\n"
        "ds_read_b128 v[32:35], v0 offset:17488\n"
        "v_cvt_pk_bf16_f32 v71, v98, v99\n"
        "v_mfma_f32_16x16x32_bf16 v[146:149], v[60:63], v[36:39], v[146:149]\n"
        "ds_read_b128 v[96:99], v0 offset:16464\n"
        "v_cvt_pk_bf16_f32 v68, v72, v73\n"
        "v_mfma_f32_16x16x32_bf16 v[146:149], v[56:59], v[24:27], v[146:149]\n"
        "ds_read_b128 v[28:31], v0 offset:15440\n"
        "v_cvt_pk_bf16_f32 v69, v74, v75\n"
        "v_permlane16_swap_b32 v167, v145\n"
        "v_mfma_f32_16x16x32_bf16 v[72:75], v[40:43], v[24:27], 0\n"
        "v_add_f32 v167, v167, v145\n"
        "v_cvt_pk_bf16_f32 v24, v80, v81\n"
        "v_mfma_f32_16x16x32_bf16 v[72:75], v[48:51], v[36:39], v[72:75]\n"
        "v_rcp_f32 v145, v167\n"
        "v_mfma_f32_16x16x32_bf16 v[72:75], v[52:55], v[44:47], v[72:75]\n"
        "ds_read2_b32 v[130:131], v11 offset0:0 offset1:3\n"
        "v_and_b32 v80, s28, v145\n"
        "s_waitcnt lgkmcnt(1)\n"
        "v_mfma_f32_16x16x32_bf16 v[36:39], v[52:55], v[32:35], 0\n"
        "ds_read_b64_tr_b16 v[126:127], v9 offset:0\n"
        "v_sub_f32 v143, v145, v80\n"
        "v_mfma_f32_16x16x32_bf16 v[36:39], v[48:51], v[96:99], v[36:39]\n"
        "ds_read2_b32 v[118:119], v11 offset0:1 offset1:4\n"
        "v_cvt_pk_bf16_f32 v81, v80, v143\n"
        "v_mfma_f32_16x16x32_bf16 v[36:39], v[40:43], v[28:31], v[36:39]\n"
        "ds_read_b64_tr_b16 v[114:115], v9 offset:16\n"
        "v_mfma_f32_16x16x4_4b_bf16 v[40:55], v[150:151], v[80:81], 0 blgp:4\n"
        "v_min3_f32 v141, v141, v167, v145\n"
        "v_cvt_pk_bf16_f32 v25, v82, v83\n"
        "v_cvt_pk_bf16_f32 v26, v146, v147\n"
        "v_cvt_pk_bf16_f32 v27, v148, v149\n"
        "v_cvt_pk_bf16_f32 v113, v72, v73\n"
        "v_cvt_pk_bf16_f32 v117, v74, v75\n"
        "v_cmp_nle_f32 vcc, 0x2b800000, v141\n"
        "v_mov_b32_dpp v167, v19 row_shr:4 row_mask:0xf bank_mask:0xf bound_ctrl:0\n"
        "v_cvt_pk_bf16_f32 v141, v36, v37\n"
        "v_cvt_pk_bf16_f32 v145, v38, v39\n"
        "s_andn2_b64 vcc, vcc, s[56:57]\n"
        "v_mfma_f32_16x16x32_bf16 v[72:75], v[56:59], v[28:31], 0\n"
        "s_cbranch_scc1 CREDO_%=\n"
        "ds_read2_b32 v[146:147], v11 offset0:2 offset1:5\n"
        "v_mul_f32 v77, v77, v45\n"
        "v_mfma_f32_16x16x32_bf16 v[72:75], v[60:63], v[96:99], v[72:75]\n"
        "ds_read_b64_tr_b16 v[142:143], v9 offset:32\n"
        "v_mul_f32 v78, v78, v46\n"
        "v_mfma_f32_16x16x32_bf16 v[72:75], v[132:135], v[32:35], v[72:75]\n"
        "s_mov_b32 exec_hi, 0xffff\n"
        "v_add_u32 v10, s69, v10\n"
        "s_mov_b32 exec_hi, -1\n"
        "v_mul_f32 v156, v156, v40\n"
        "v_mul_f32 v157, v157, v41\n"
        "v_mfma_f32_16x16x32_bf16 v[56:59], v[152:155], v[32:35], 0\n"
        "v_mul_f32 v158, v158, v42\n"
        "v_mul_f32 v159, v159, v43\n"
        "v_mfma_f32_16x16x32_bf16 v[56:59], v[108:111], v[96:99], v[56:59]\n"
        "s_add_u32 s31, s31, 1\n"
        "v_mul_f32 v76, v76, v44\n"
        "v_mul_f32 v79, v79, v47\n"
        "v_mfma_f32_16x16x32_bf16 v[56:59], v[104:107], v[28:31], v[56:59]\n"
        "s_mul_i32 s38, s31, 1344\n"
        "v_mul_f32 v88, v88, v48\n"
        "v_mul_f32 v89, v89, v49\n"
        "v_mul_f32 v90, v90, v50\n"
        "v_mfma_f32_16x16x4_4b_bf16 v[30:45], v[150:151], v[80:81], 0 blgp:6\n"
        "v_mul_f32 v91, v91, v51\n"
        "v_fma_f32 v120, v120, v52, v19\n"
        "v_cvt_pk_bf16_f32 v98, v72, v73\n"
        "v_cvt_pk_bf16_f32 v99, v74, v75\n"
        "v_cvt_pk_bf16_f32 v96, v56, v57\n"
        "v_cvt_pk_bf16_f32 v97, v58, v59\n"
        "v_cvt_pk_bf16_f32 v48, v156, v157\n"
        "v_cvt_pk_bf16_f32 v49, v158, v159\n"
        "v_cvt_pk_bf16_f32 v50, v76, v77\n"
        "v_cvt_pk_bf16_f32 v51, v78, v79\n"
        "v_cvt_pk_bf16_f32 v72, v88, v89\n"
        "v_mfma_f32_16x16x32_bf16 v[76:79], v[160:163], v[64:67], 0\n"
        "s_waitcnt lgkmcnt(0)\n"
        "v_mul_f32 v100, v100, v30\n"
        "v_mul_f32 v101, v101, v31\n"
        "v_smfmac_f32_16x16x64_bf16 v[76:79], v[164:167], v[124:131], v18\n"
        "s_cmp_ge_u32 s31, s76\n"
        "s_cbranch_scc1 L34_%=\n"
        "L35_%=:\n"
        "ds_read_b64_tr_b16 v[126:127], v9 offset:48\n"
        "v_mul_f32 v102, v102, v32\n"
        "v_mfma_f32_16x16x32_bf16 v[28:31], v[160:163], v[20:23], 0\n"
        "s_cmp_ge_u32 s31, s71\n"
        "v_mul_f32 v103, v103, v33\n"
        "v_mul_f32 v84, v84, v34\n"
        "v_smfmac_f32_16x16x64_bf16 v[28:31], v[164:167], v[112:119], v18\n"
        "s_cselect_b32 s39, s70, s72\n"
        "ds_read_b64_tr_b16 v[114:115], v9 offset:64\n"
        "v_mfma_f32_16x16x32_bf16 v[52:55], v[160:163], v[92:95], 0\n"
        "s_cmp_ge_u32 s31, 20\n"
        "v_mul_f32 v85, v85, v35\n"
        "v_mul_f32 v86, v86, v36\n"
        "v_smfmac_f32_16x16x64_bf16 v[52:55], v[164:167], v[140:147], v18\n"
        "s_cselect_b32 s39, s22, s39\n"
        "ds_read_b64_tr_b16 v[142:143], v9 offset:80\n"
        "v_mfma_f32_16x16x32_bf16 v[156:159], v[160:163], v[68:71], 0\n"
        "s_waitcnt lgkmcnt(2)\n"
        "ds_read_b128 v[32:35], v2\n"
        "v_mul_f32 v87, v87, v37\n"
        "v_smfmac_f32_16x16x64_bf16 v[156:159], v[164:167], v[124:131], v18 abid:1\n"
        "s_cmp_ge_u32 s31, 21\n"
        "s_cselect_b32 s39, s23, s39\n"
        "ds_read_b128 v[56:59], v3\n"
        "v_mul_f32 v136, v136, v38\n"
        "v_mfma_f32_16x16x32_bf16 v[44:47], v[160:163], v[24:27], 0\n"
        "s_waitcnt lgkmcnt(3)\n"
        "v_mul_f32 v137, v137, v39\n"
        "v_mov_b64 v[104:105], 0\n"
        "v_smfmac_f32_16x16x64_bf16 v[44:47], v[164:167], v[112:119], v18 abid:1\n"
        "s_add_u32 s39, s39, s38\n"
        "s_mul_i32 s68, s31, 3\n"
        "v_mov_b64 v[106:107], 0\n"
        "v_cvt_pk_bf16_f32 v74, v90, v91\n"
        "v_cvt_pk_bf16_f32 v73, v120, 0\n"
        "v_mfma_f32_16x16x32_bf16 v[80:83], v[160:163], v[96:99], 0\n"
        "s_waitcnt lgkmcnt(1)\n"
        "v_mov_b64 v[60:61], 0\n"
        "v_mov_b64 v[62:63], 0\n"
        "v_smfmac_f32_16x16x64_bf16 v[80:83], v[164:167], v[140:147], v18 abid:1\n"
        "s_lshr_b32 s38, s31, 3\n"
        "s_waitcnt lgkmcnt(0)\n"
        "v_mov_b32_dpp v75, v19 row_shr:4 row_mask:0xf bank_mask:0xf bound_ctrl:0\n"
        "v_mul_f32 v138, v138, v40\n"
        "v_smfmac_f32_16x16x64_bf16 v[104:107], v[32:35], v[64:71], v4\n"
        "s_add_u32 s38, s38, 11\n"
        "s_bitcmp1_b32 s31, 0\n"
        "v_mov_b64 v[36:37], 0\n"
        "v_smfmac_f32_16x16x64_bf16 v[104:107], v[56:59], v[124:131], v4 abid:1\n"
        "s_cselect_b32 s38, 3, s38\n"
        "ds_read_b64_tr_b16 v[126:127], v9 offset:3328\n"
        "v_mov_b64 v[38:39], 0\n"
        "v_smfmac_f32_16x16x64_bf16 v[60:63], v[32:35], v[20:27], v4\n"
        "s_add_u32 s68, s68, s38\n"
        "v_mul_f32 v139, v139, v41\n"
        "v_smfmac_f32_16x16x64_bf16 v[60:63], v[56:59], v[112:119], v4 abid:1\n"
        "s_cmp_lt_u32 s31, s14\n"
        "ds_read_b64_tr_b16 v[114:115], v9 offset:3344\n"
        "v_smfmac_f32_16x16x64_bf16 v[36:39], v[32:35], v[92:99], v4\n"
        "s_cselect_b32 s68, s68, 0\n"
        "v_fma_f32 v122, v122, v42, v19\n"
        "v_smfmac_f32_16x16x64_bf16 v[36:39], v[56:59], v[140:147], v4 abid:1\n"
        "s_add_u32 s37, s35, 0\n"
        "s_and_b32 s38, s31, 7\n"
        "ds_read_b64_tr_b16 v[142:143], v9 offset:3360\n"
        "v_cvt_pk_bf16_f32 v32, v100, v101\n"
        "v_cvt_pk_bf16_f32 v33, v102, v103\n"
        "v_mfma_f32_16x16x32_bf16 v[100:103], v[48:51], v[64:67], 0\n"
        "s_waitcnt lgkmcnt(2)\n"
        "v_cvt_pk_bf16_f32 v34, v84, v85\n"
        "v_cvt_pk_bf16_f32 v154, v76, v28\n"
        "v_smfmac_f32_16x16x64_bf16 v[100:103], v[72:75], v[124:131], v18\n"
        "s_cselect_b32 s30, 1, -7\n"
        "s_mul_i32 s29, s30, 192\n"
        "ds_read_b64_tr_b16 v[126:127], v9 offset:3376\n"
        "v_cvt_pk_bf16_f32 v155, v52, v156\n"
        "v_mfma_f32_16x16x32_bf16 v[148:151], v[48:51], v[20:23], 0\n"
        "s_waitcnt lgkmcnt(2)\n"
        "v_cvt_pk_bf16_f32 v156, v44, v80\n"
        "v_cvt_pk_bf16_f32 v35, v86, v87\n"
        "v_smfmac_f32_16x16x64_bf16 v[148:151], v[72:75], v[112:119], v18\n"
        "buffer_store_dwordx3 v[154:156], v12, s[8:11], s37 offen sc0 sc1\n"
        "s_mul_i32 s30, s30, 392\n"
        "ds_read_b64_tr_b16 v[114:115], v9 offset:3392\n"
        "v_cvt_pk_bf16_f32 v52, v136, v137\n"
        "v_cvt_pk_bf16_f32 v154, v77, v29\n"
        "v_cvt_pk_bf16_f32 v164, v78, v30\n"
        "v_cvt_pk_bf16_f32 v88, v79, v31\n"
        "v_mfma_f32_16x16x32_bf16 v[28:31], v[48:51], v[92:95], 0\n"
        "s_waitcnt lgkmcnt(2)\n"
        "v_cvt_pk_bf16_f32 v155, v53, v157\n"
        "v_cvt_pk_bf16_f32 v165, v54, v158\n"
        "v_smfmac_f32_16x16x64_bf16 v[28:31], v[72:75], v[140:147], v18\n"
        "s_add_u32 s37, s35, 0\n"
        "s_cmp_eq_u32 s73, s18\n"
        "ds_read_b64_tr_b16 v[142:143], v9 offset:3408\n"
        "v_cvt_pk_bf16_f32 v156, v45, v81\n"
        "v_cvt_pk_bf16_f32 v89, v55, v159\n"
        "v_mfma_f32_16x16x32_bf16 v[84:87], v[48:51], v[68:71], 0\n"
        "s_waitcnt lgkmcnt(2)\n"
        "v_cvt_pk_bf16_f32 v166, v46, v82\n"
        "v_cvt_pk_bf16_f32 v90, v47, v83\n"
        "v_smfmac_f32_16x16x64_bf16 v[84:87], v[72:75], v[124:131], v18 abid:1\n"
        "buffer_store_dwordx3 v[154:156], v15, s[8:11], s37 offen offset:768 sc0 sc1\n"
        "s_cselect_b32 s34, -7488, s75\n"
        "ds_read_b64_tr_b16 v[126:127], v9 offset:6656\n"
        "s_add_u32 s37, s35, 98112\n"
        "v_add_f32 v104, v104, v105\n"
        "v_add_f32 v106, v106, v107\n"
        "v_mfma_f32_16x16x32_bf16 v[44:47], v[48:51], v[24:27], 0\n"
        "s_waitcnt lgkmcnt(2)\n"
        "v_add_f32 v60, v60, v61\n"
        "v_add_f32 v62, v62, v63\n"
        "v_smfmac_f32_16x16x64_bf16 v[44:47], v[72:75], v[112:119], v18 abid:1\n"
        "s_add_u32 s73, s73, s34\n"
        "s_xor_b32 s74, s74, 4\n"
        "ds_read_b64_tr_b16 v[114:115], v9 offset:6672\n"
        "v_cvt_pk_bf16_f32 v152, v104, v60\n"
        "v_mfma_f32_16x16x32_bf16 v[156:159], v[48:51], v[96:99], 0\n"
        "s_waitcnt lgkmcnt(2)\n"
        "v_add_f32 v36, v36, v37\n"
        "v_add_f32 v38, v38, v39\n"
        "v_smfmac_f32_16x16x64_bf16 v[156:159], v[72:75], v[140:147], v18 abid:1\n"
        "buffer_store_dwordx3 v[164:166], v12, s[8:11], s37 offen offset:192 sc0 sc1 nt\n"
        "s_add_u32 s37, s35, 98112\n"
        "ds_read_b64_tr_b16 v[142:143], v9 offset:6688\n"
        "v_cvt_pk_bf16_f32 v153, v36, v106\n"
        "v_cvt_pk_bf16_f32 v154, v62, v38\n"
        "v_mov_b32 v161, s74\n"
        "v_cvt_pk_bf16_f32 v54, v138, v139\n"
        "v_mov_b32_dpp v55, v19 row_shr:4 row_mask:0xf bank_mask:0xf bound_ctrl:0\n"
        "v_cvt_pk_bf16_f32 v53, v122, 0\n"
        "v_mfma_f32_16x16x32_bf16 v[48:51], v[32:35], v[64:67], 0\n"
        "s_waitcnt lgkmcnt(2)\n"
        "ds_read_b32 v166, v161\n"
        "v_add_u32 v7, s69, v7\n"
        "v_smfmac_f32_16x16x64_bf16 v[48:51], v[52:55], v[124:131], v18\n"
        "ds_read_b64_tr_b16 v[126:127], v9 offset:6704\n"
        "v_mfma_f32_16x16x32_bf16 v[40:43], v[32:35], v[20:23], 0\n"
        "s_waitcnt lgkmcnt(3)\n"
        "v_add_u32 v17, s69, v17\n"
        "v_writelane_b32 v10, s73, 56\n"
        "v_smfmac_f32_16x16x64_bf16 v[40:43], v[52:55], v[112:119], v18\n"
        "buffer_store_dwordx3 v[88:90], v14, s[8:11], s37 offen offset:960 sc0 sc1 nt\n"
        "s_add_u32 s37, s35, 588672\n"
        "ds_read_b64_tr_b16 v[114:115], v9 offset:6720\n"
        "s_add_u32 s46, s37, 1344\n"
        "v_add_u32 v11, s30, v11\n"
        "v_mfma_f32_16x16x32_bf16 v[88:91], v[32:35], v[92:95], 0\n"
        "s_waitcnt lgkmcnt(2)\n"
        "v_readfirstlane_b32 s40, v166\n"
        "s_cmp_lt_i32 s40, s68\n"
        "v_smfmac_f32_16x16x64_bf16 v[88:91], v[52:55], v[140:147], v18\n"
        "buffer_store_dwordx3 v[152:154], v12, s[44:47], s37 offen offset:1152 sc0 sc1 nt\n"
        "ds_read_b64_tr_b16 v[142:143], v9 offset:6736\n"
        "v_add_u32 v9, s69, v9\n"
        "s_mov_b64 exec, s[62:63]\n"
        "v_add_u32 v16, s29, v16\n"
        "v_add_u32 v8, s34, v8\n"
        "s_mov_b64 exec, -1\n"
        "v_mfma_f32_16x16x32_bf16 v[36:39], v[32:35], v[68:71], 0\n"
        "s_waitcnt lgkmcnt(1)\n"
        "v_xor_b32 v165, 4, v161\n"
        "s_cbranch_scc1 L38_%=\n"
        "L39_%=:\n"
        "v_smfmac_f32_16x16x64_bf16 v[36:39], v[52:55], v[124:131], v18 abid:1\n"
        "s_mov_b64 exec, s[50:51]\n"
        "v_add_u32 v5, s29, v5\n"
        "v_add_u32 v6, s34, v6\n"
        "s_mov_b64 exec, -1\n"
        "ds_read_b128 v[104:107], v7\n"
        "ds_read_b128 v[56:59], v7 offset:3328\n"
        "ds_read_b128 v[108:111], v7 offset:64\n"
        "ds_read_b128 v[60:63], v7 offset:3392\n"
        "ds_read_b128 v[152:155], v7 offset:128\n"
        "ds_read_b128 v[132:135], v7 offset:3456\n"
        "v_mfma_f32_16x16x32_bf16 v[72:75], v[32:35], v[24:27], 0\n"
        "s_sub_u32 s69, 0, s69\n"
        "v_smfmac_f32_16x16x64_bf16 v[72:75], v[52:55], v[112:119], v18 abid:1\n"
        "s_mov_b64 exec, 1\n"
        "ds_inc_u32 v165, v1 offset:8\n"
        "s_mov_b64 exec, -1\n"
        "v_mfma_f32_16x16x32_bf16 v[76:79], v[32:35], v[96:99], 0\n"
        "s_waitcnt lgkmcnt(7)\n"
        "s_mov_b32 s36, s35\n"
        "s_mov_b32 s35, s39\n"
        "v_smfmac_f32_16x16x64_bf16 v[76:79], v[52:55], v[140:147], v18 abid:1\n"
        "s_setprio 0\n"
        "CNEXT_%=:\n"
        "s_cmp_ge_u32 s31, s14\n"
        "s_cbranch_scc1 CEND_%=\n"
        "s_cmp_gt_i32 s29, 0\n"
        "s_cbranch_scc1 CTOP_%=\n"
        "s_branch CFULL_%=\n"
        "CEND_%=:\n"
        "s_waitcnt lgkmcnt(0)\n"
        "s_nop 15\n"
        "v_cvt_pk_bf16_f32 v252, v100, v148\n"
        "v_cvt_pk_bf16_f32 v253, v28, v84\n"
        "v_cvt_pk_bf16_f32 v254, v44, v156\n"
        "s_add_u32 s37, s36, 196224\n"
        "v_cvt_pk_bf16_f32 v248, v101, v149\n"
        "v_cvt_pk_bf16_f32 v249, v29, v85\n"
        "buffer_store_dwordx3 v[252:254], v12, s[8:11], s37 offen offset:384 sc0 sc1 nt\n"
        "v_cvt_pk_bf16_f32 v250, v45, v157\n"
        "s_add_u32 s37, s36, 196224\n"
        "v_cvt_pk_bf16_f32 v252, v102, v150\n"
        "v_cvt_pk_bf16_f32 v253, v30, v86\n"
        "buffer_store_dwordx3 v[248:250], v13, s[8:11], s37 offen offset:1152 sc0 sc1 nt\n"
        "v_cvt_pk_bf16_f32 v254, v46, v158\n"
        "s_add_u32 s37, s36, 294336\n"
        "v_cvt_pk_bf16_f32 v244, v103, v151\n"
        "v_cvt_pk_bf16_f32 v245, v31, v87\n"
        "buffer_store_dwordx3 v[252:254], v12, s[8:11], s37 offen offset:576 sc0 sc1 nt\n"
        "v_cvt_pk_bf16_f32 v246, v47, v159\n"
        "s_add_u32 s37, s36, 392448\n"
        "buffer_store_dwordx3 v[244:246], v12, s[8:11], s37 offen sc0 sc1 nt\n"
        "v_cvt_pk_bf16_f32 v248, v48, v40\n"
        "v_cvt_pk_bf16_f32 v249, v88, v36\n"
        "v_cvt_pk_bf16_f32 v250, v72, v76\n"
        "s_add_u32 s37, s36, 392448\n"
        "v_cvt_pk_bf16_f32 v252, v49, v41\n"
        "v_cvt_pk_bf16_f32 v253, v89, v37\n"
        "buffer_store_dwordx3 v[248:250], v15, s[8:11], s37 offen offset:768 sc0 sc1 nt\n"
        "v_cvt_pk_bf16_f32 v254, v73, v77\n"
        "s_add_u32 s37, s36, 490560\n"
        "v_cvt_pk_bf16_f32 v248, v50, v42\n"
        "v_cvt_pk_bf16_f32 v249, v90, v38\n"
        "buffer_store_dwordx3 v[252:254], v12, s[8:11], s37 offen offset:192 sc0 sc1 nt\n"
        "v_cvt_pk_bf16_f32 v250, v74, v78\n"
        "s_add_u32 s37, s36, 490560\n"
        "buffer_store_dwordx3 v[248:250], v14, s[8:11], s37 offen offset:960 sc0 sc1 nt\n"
        "s_add_u32 s37, s36, 588672\n"
        "v_cvt_pk_bf16_f32 v252, v51, v43\n"
        "v_cvt_pk_bf16_f32 v253, v91, v39\n"
        "v_cvt_pk_bf16_f32 v254, v75, v79\n"
        "buffer_store_dwordx3 v[252:254], v12, s[8:11], s37 offen offset:384 sc0 sc1 nt\n"
        "s_nop 1\n"
        "s_endpgm\n"
        "L34_%=:\n"
        "s_nop 7\n"
        "v_mov_b32 v148, 110048\n"
        "L32_%=:\n"
        "ds_read_b32 v149, v148 offset:64\n"
        "s_waitcnt lgkmcnt(0)\n"
        "v_readfirstlane_b32 s40, v149\n"
        "s_nop 3\n"
        "s_cmp_ge_i32 s40, 1\n"
        "s_cbranch_scc1 L33_%=\n"
        "s_sleep 1\n"
        "s_sub_u32 s41, s41, 1\n"
        "s_cbranch_scc0 L32_%=\n"
        "s_endpgm\n"
        "L33_%=:\n"
        "v_mbcnt_lo_u32_b32 v149, -1, 0\n"
        "v_lshl_add_u32 v148, v149, 2, v148\n"
        "ds_read_b32 v149, v148 offset:68\n"
        "s_mov_b32 s14, s40\n"
        "s_mov_b32 s76, -1\n"
        "s_waitcnt lgkmcnt(0)\n"
        "v_readlane_b32 s22, v149, 0\n"
        "v_readlane_b32 s23, v149, 1\n"
        "s_nop 3\n"
        "s_add_u32 s22, s22, s17\n"
        "s_add_u32 s23, s23, s17\n"
        "s_nop 1\n"
        "s_branch L35_%=\n"
        "L38_%=:\n"
        "s_nop 7\n"
        "L36_%=:\n"
        "ds_read_b32 v166, v161 offset:0\n"
        "s_waitcnt lgkmcnt(0)\n"
        "v_readfirstlane_b32 s40, v166\n"
        "s_nop 3\n"
        "s_cmp_ge_i32 s40, s68\n"
        "s_cbranch_scc1 L37_%=\n"
        "s_sleep 1\n"
        "s_sub_u32 s41, s41, 1\n"
        "s_cbranch_scc0 L36_%=\n"
        "s_endpgm\n"
        "L37_%=:\n"
        "s_nop 1\n"
        "s_branch L39_%=\n"
        "CGRP_%=:\n"
        "s_setprio 3\n"
        "s_waitcnt lgkmcnt(0)\n"
        "s_nop 6\n"
        "ds_read_u16 v164, v0 offset:20576\n"
        "s_sub_u32 s60, s73, 101920\n"
        "ds_read_u16 v163, v0 offset:20578\n"
        "ds_read_u16 v162, v0 offset:20580\n"
        "ds_read_u16 v167, v0 offset:20582\n"
        "ds_read_u16 v161, v0 offset:20588\n"
        "ds_read_u16 v166, v0 offset:20590\n"
        "v_lshrrev_b32 v165, 8, v0\n"
        "s_waitcnt lgkmcnt(5)\n"
        "v_add_u32 v164, s60, v164\n"
        "v_subrev_u32 v160, 8320, v164\n"
        "v_min_u32 v164, v164, v160\n"
        "v_add_u32 v164, 101728, v164\n"
        "ds_read_b128 v[24:27], v164 offset:64\n"
        "ds_read_b128 v[20:23], v164 offset:128\n"
        "ds_read_b128 v[32:35], v164\n"
        "s_waitcnt lgkmcnt(1)\n"
        "s_waitcnt lgkmcnt(0)\n"
        "v_mfma_f32_16x16x32_bf16 v[64:67], v[168:171], v[32:35], 0\n"
        "v_add_u32 v163, s60, v163\n"
        "v_subrev_u32 v160, 8320, v163\n"
        "v_mfma_f32_16x16x32_bf16 v[64:67], v[172:175], v[24:27], v[64:67]\n"
        "v_min_u32 v163, v163, v160\n"
        "v_add_u32 v163, 101728, v163\n"
        "v_mfma_f32_16x16x32_bf16 v[64:67], v[176:179], v[20:23], v[64:67]\n"
        "v_add_u32 v162, s19, v162\n"
        "v_mad_i32_i24 v165, v165, -8, v162\n"
        "v_add_u32 v166, s19, v166\n"
        "v_add_u32 v167, s19, v167\n"
        "v_mfma_f32_16x16x32_bf16 v[68:71], v[180:183], v[32:35], 0\n"
        "v_mfma_f32_16x16x32_bf16 v[68:71], v[184:187], v[24:27], v[68:71]\n"
        "v_mfma_f32_16x16x32_bf16 v[68:71], v[188:191], v[20:23], v[68:71]\n"
        "v_mfma_f32_16x16x32_bf16 v[80:83], v[192:195], v[32:35], 0\n"
        "v_cvt_pk_bf16_f32 v52, v64, v65\n"
        "v_cvt_pk_bf16_f32 v53, v66, v67\n"
        "v_mfma_f32_16x16x32_bf16 v[80:83], v[196:199], v[24:27], v[80:83]\n"
        "v_mfma_f32_16x16x32_bf16 v[80:83], v[200:203], v[20:23], v[80:83]\n"
        "v_mfma_f32_16x16x32_bf16 v[92:95], v[204:207], v[32:35], 0\n"
        "v_mfma_f32_16x16x32_bf16 v[92:95], v[208:211], v[24:27], v[92:95]\n"
        "v_mfma_f32_16x16x32_bf16 v[92:95], v[212:215], v[20:23], v[92:95]\n"
        "v_cvt_pk_bf16_f32 v54, v68, v69\n"
        "v_cvt_pk_bf16_f32 v55, v70, v71\n"
        "ds_write2_b32 v165, v52, v54 offset0:0 offset1:1\n"
        "ds_write2_b32 v165, v53, v55 offset0:16 offset1:17\n"
        "v_mfma_f32_16x16x32_bf16 v[68:71], v[216:219], v[32:35], 0\n"
        "v_cvt_pk_bf16_f32 v64, v80, v81\n"
        "v_cvt_pk_bf16_f32 v65, v82, v83\n"
        "v_mfma_f32_16x16x32_bf16 v[68:71], v[220:223], v[24:27], v[68:71]\n"
        "v_cvt_pk_bf16_f32 v66, v92, v93\n"
        "v_cvt_pk_bf16_f32 v67, v94, v95\n"
        "v_mfma_f32_16x16x32_bf16 v[68:71], v[224:227], v[20:23], v[68:71]\n"
        "ds_write2_b32 v165, v64, v66 offset0:8 offset1:9\n"
        "ds_write2_b32 v165, v65, v67 offset0:24 offset1:25\n"
        "v_mfma_f32_16x16x32_bf16 v[92:95], v[228:231], v[32:35], 0\n"
        "v_mfma_f32_16x16x32_bf16 v[92:95], v[232:235], v[24:27], v[92:95]\n"
        "ds_read_b64 v[164:165], v161\n"
        "v_mfma_f32_16x16x32_bf16 v[92:95], v[236:239], v[20:23], v[92:95]\n"
        "ds_read_b64 v[144:145], v163\n"
        "ds_read_b64 v[146:147], v163 offset:16\n"
        "v_cvt_pk_bf16_f32 v96, v68, v69\n"
        "v_cvt_pk_bf16_f32 v97, v70, v71\n"
        "ds_read_b64 v[160:161], v163 offset:32\n"
        "s_nop 2\n"
        "v_cvt_pk_bf16_f32 v99, v94, v95\n"
        "v_cvt_pk_bf16_f32 v98, v92, v93\n"
        "v_mfma_f32_16x16x32_bf16 v[80:83], v[32:35], v[52:55], 0\n"
        "v_mfma_f32_16x16x32_bf16 v[80:83], v[24:27], v[64:67], v[80:83]\n"
        "v_mfma_f32_16x16x32_bf16 v[80:83], v[20:23], v[96:99], v[80:83]\n"
        "s_waitcnt lgkmcnt(1)\n"
        "ds_write_b128 v162, v[96:99] offset:128\n"
        "v_mfma_f32_4x4x4_16b_bf16 v[68:71], v[144:145], v[164:165], 0\n"
        "v_mfma_f32_4x4x4_16b_bf16 v[64:67], v[146:147], v[164:165], 0\n"
        "ds_read_b128 v[124:127], v0 offset:4112\n"
        "ds_read_b128 v[92:95], v0 offset:5136\n"
        "s_waitcnt lgkmcnt(3)\n"
        "v_mfma_f32_4x4x4_16b_bf16 v[52:55], v[160:161], v[164:165], 0\n"
        "s_mov_b32 vcc_lo, 0x22222222\n"
        "s_mov_b32 vcc_hi, 0\n"
        "v_cndmask_b32 v141, v80, v81, vcc\n"
        "v_mfma_f32_16x16x32_bf16 v[96:99], v[32:35], v[240:243], 0\n"
        "s_mov_b32 vcc_lo, 0x44444444\n"
        "s_mov_b32 vcc_hi, 0\n"
        "v_cndmask_b32 v141, v141, v82, vcc\n"
        "v_mfma_f32_16x16x32_bf16 v[96:99], v[24:27], v[244:247], v[96:99]\n"
        "ds_read_b128 v[116:119], v0 offset:6176\n"
        "v_mfma_f32_16x16x32_bf16 v[96:99], v[20:23], v[248:251], v[96:99]\n"
        "ds_read_b128 v[120:123], v0 offset:7200\n"
        "ds_read_b128 v[112:115], v0 offset:8224\n"
        "s_mov_b32 vcc_lo, 0x88888888\n"
        "s_mov_b32 vcc_hi, 0\n"
        "v_cndmask_b32 v141, v141, v83, vcc\n"
        "s_waitcnt lgkmcnt(3)\n"
        "v_mfma_f32_16x16x32_bf16 v[80:83], v[32:35], v[252:255], 0\n"
        "v_mfma_f32_16x16x32_bf16 v[80:83], v[24:27], v[124:127], v[80:83]\n"
        "ds_read_b64 v[144:145], v163 offset:48\n"
        "v_cvt_pk_bf16_f32 v136, v96, v68\n"
        "v_mfma_f32_16x16x32_bf16 v[80:83], v[20:23], v[92:95], v[80:83]\n"
        "ds_read_b64 v[142:143], v163 offset:64\n"
        "v_cvt_pk_bf16_f32 v160, v97, v69\n"
        "v_cvt_pk_bf16_f32 v146, v98, v70\n"
        "v_cvt_pk_bf16_f32 v162, v99, v71\n"
        "s_waitcnt lgkmcnt(2)\n"
        "v_mfma_f32_16x16x32_bf16 v[96:99], v[32:35], v[116:119], 0\n"
        "ds_read_b64 v[138:139], v163 offset:80\n"
        "v_mfma_f32_16x16x32_bf16 v[96:99], v[24:27], v[120:123], v[96:99]\n"
        "ds_read_b128 v[92:95], v0 offset:9264\n"
        "v_cvt_pk_bf16_f32 v137, v80, v64\n"
        "v_mfma_f32_16x16x32_bf16 v[96:99], v[20:23], v[112:115], v[96:99]\n"
        "s_waitcnt lgkmcnt(3)\n"
        "ds_read_b128 v[116:119], v0 offset:10288\n"
        "ds_read_b128 v[112:115], v0 offset:11312\n"
        "v_cvt_pk_bf16_f32 v161, v81, v65\n"
        "v_cvt_pk_bf16_f32 v147, v82, v66\n"
        "v_cvt_pk_bf16_f32 v163, v83, v67\n"
        "ds_write_b64 v167, v[136:137]\n"
        "v_mfma_f32_4x4x4_16b_bf16 v[80:83], v[144:145], v[164:165], 0\n"
        "s_waitcnt lgkmcnt(5)\n"
        "v_mfma_f32_4x4x4_16b_bf16 v[68:71], v[142:143], v[164:165], 0\n"
        "ds_read_b128 v[120:123], v0 offset:12352\n"
        "ds_read_b128 v[124:127], v0 offset:13376\n"
        "ds_read_b128 v[128:131], v0 offset:14400\n"
        "s_waitcnt lgkmcnt(7)\n"
        "v_mfma_f32_4x4x4_16b_bf16 v[64:67], v[138:139], v[164:165], 0\n"
        "v_cvt_pk_bf16_f32 v140, v96, v52\n"
        "v_cvt_pk_bf16_f32 v142, v97, v53\n"
        "v_cvt_pk_bf16_f32 v144, v98, v54\n"
        "v_cvt_pk_bf16_f32 v164, v99, v55\n"
        "s_waitcnt lgkmcnt(4)\n"
        "v_mfma_f32_16x16x32_bf16 v[136:139], v[32:35], v[92:95], 0\n"
        "v_mfma_f32_16x16x32_bf16 v[136:139], v[24:27], v[116:119], v[136:139]\n"
        "ds_read_b128 v[96:99], v0 offset:15440\n"
        "v_mfma_f32_16x16x32_bf16 v[136:139], v[20:23], v[112:115], v[136:139]\n"
        "ds_read_b128 v[92:95], v0 offset:16464\n"
        "ds_read_b128 v[52:55], v0 offset:17488\n"
        "ds_write_b64 v167, v[160:161] offset:392\n"
        "ds_write_b64 v167, v[146:147] offset:784\n"
        "s_waitcnt lgkmcnt(5)\n"
        "v_mfma_f32_16x16x32_bf16 v[112:115], v[32:35], v[120:123], 0\n"
        "v_mfma_f32_16x16x32_bf16 v[112:115], v[24:27], v[124:127], v[112:115]\n"
        "v_mfma_f32_16x16x32_bf16 v[112:115], v[20:23], v[128:131], v[112:115]\n"
        "s_mov_b64 exec, 0x00f0000f\n"
        "ds_write_b32 v166, v141\n"
        "s_mov_b64 exec, -1\n"
        "v_cvt_pk_bf16_f32 v141, v136, v80\n"
        "v_cvt_pk_bf16_f32 v143, v137, v81\n"
        "v_cvt_pk_bf16_f32 v145, v138, v82\n"
        "v_cvt_pk_bf16_f32 v165, v139, v83\n"
        "s_waitcnt lgkmcnt(1)\n"
        "v_mfma_f32_16x16x32_bf16 v[80:83], v[32:35], v[96:99], 0\n"
        "v_cvt_pk_bf16_f32 v160, v112, v68\n"
        "v_cvt_pk_bf16_f32 v146, v113, v69\n"
        "v_mfma_f32_16x16x32_bf16 v[80:83], v[24:27], v[92:95], v[80:83]\n"
        "v_cvt_pk_bf16_f32 v138, v114, v70\n"
        "v_cvt_pk_bf16_f32 v136, v115, v71\n"
        "v_mfma_f32_16x16x32_bf16 v[80:83], v[20:23], v[52:55], v[80:83]\n"
        "ds_write_b64 v167, v[162:163] offset:1176\n"
        "ds_write_b64 v167, v[140:141] offset:8\n"
        "ds_write_b64 v167, v[142:143] offset:400\n"
        "ds_write_b64 v167, v[144:145] offset:792\n"
        "ds_write_b64 v167, v[164:165] offset:1184\n"
        "s_nop 2\n"
        "v_cvt_pk_bf16_f32 v161, v80, v64\n"
        "v_cvt_pk_bf16_f32 v147, v81, v65\n"
        "v_cvt_pk_bf16_f32 v139, v82, v66\n"
        "v_cvt_pk_bf16_f32 v137, v83, v67\n"
        "ds_write_b64 v167, v[160:161] offset:16\n"
        "ds_write_b64 v167, v[146:147] offset:408\n"
        "ds_write_b64 v167, v[138:139] offset:800\n"
        "ds_write_b64 v167, v[136:137] offset:1192\n"
        "s_waitcnt lgkmcnt(0)\n"
        "s_nop 6\n"
        "s_setprio 0\n"
        "s_setpc_b64 s[64:65]\n"
        "L50_%=:\n"
        "s_nop 7\n"
        "v_mov_b32 v167, 110048\n"
        "L48_%=:\n"
        "ds_read_b32 v166, v167 offset:64\n"
        "s_waitcnt lgkmcnt(0)\n"
        "v_readfirstlane_b32 s40, v166\n"
        "s_nop 3\n"
        "s_cmp_ge_i32 s40, 1\n"
        "s_cbranch_scc1 L49_%=\n"
        "s_sleep 1\n"
        "s_sub_u32 s41, s41, 1\n"
        "s_cbranch_scc0 L48_%=\n"
        "s_endpgm\n"
        "L49_%=:\n"
        "v_mbcnt_lo_u32_b32 v166, -1, 0\n"
        "v_lshl_add_u32 v167, v166, 2, v167\n"
        "ds_read_b32 v166, v167 offset:68\n"
        "s_mov_b32 s14, s40\n"
        "s_mov_b32 s76, -1\n"
        "s_waitcnt lgkmcnt(0)\n"
        "v_readlane_b32 s22, v166, 0\n"
        "v_readlane_b32 s23, v166, 1\n"
        "s_nop 3\n"
        "s_add_u32 s22, s22, s17\n"
        "s_add_u32 s23, s23, s17\n"
        "s_nop 1\n"
        "s_branch L51_%=\n"
        "L54_%=:\n"
        "s_nop 7\n"
        "L52_%=:\n"
        "ds_read_b32 v163, v167 offset:0\n"
        "s_waitcnt lgkmcnt(0)\n"
        "v_readfirstlane_b32 s40, v163\n"
        "s_nop 3\n"
        "s_cmp_ge_i32 s40, s68\n"
        "s_cbranch_scc1 L53_%=\n"
        "s_sleep 1\n"
        "s_sub_u32 s41, s41, 1\n"
        "s_cbranch_scc0 L52_%=\n"
        "s_endpgm\n"
        "L53_%=:\n"
        "s_nop 1\n"
        "s_branch L55_%=\n"
        "CREDO_%=:\n"
        "s_waitcnt lgkmcnt(0)\n"
        "s_nop 15\n"
        "s_mov_b32 s36, 0x20000000\n"
        "ds_read_b128 v[104:107], v7 offset:0\n"
        "ds_read_b128 v[56:59], v7 offset:3328\n"
        "ds_read_b128 v[108:111], v7 offset:64\n"
        "ds_read_b128 v[60:63], v7 offset:3392\n"
        "ds_read_b128 v[152:155], v7 offset:128\n"
        "ds_read_b128 v[132:135], v7 offset:3456\n"
        "s_waitcnt lgkmcnt(0)\n"
        "s_branch MTOP_%=\n"
        "MTOP_%=:\n"
        "v_mfma_f32_16x16x32_bf16 v[24:27], v[168:171], v[104:107], 0\n"
        "v_cvt_pk_bf16_f32 v124, v100, v148\n"
        "v_cvt_pk_bf16_f32 v125, v28, v84\n"
        "v_cvt_pk_bf16_f32 v126, v44, v156\n"
        "v_cvt_pk_bf16_f32 v128, v101, v149\n"
        "v_cvt_pk_bf16_f32 v129, v29, v85\n"
        "v_cvt_pk_bf16_f32 v130, v45, v157\n"
        "v_cvt_pk_bf16_f32 v136, v102, v150\n"
        "v_cvt_pk_bf16_f32 v137, v30, v86\n"
        "v_mfma_f32_16x16x32_bf16 v[24:27], v[172:175], v[108:111], v[24:27]\n"
        "v_cvt_pk_bf16_f32 v138, v46, v158\n"
        "v_cvt_pk_bf16_f32 v140, v103, v151\n"
        "v_cvt_pk_bf16_f32 v141, v31, v87\n"
        "v_cvt_pk_bf16_f32 v142, v47, v159\n"
        "v_cvt_pk_bf16_f32 v144, v48, v40\n"
        "v_cvt_pk_bf16_f32 v145, v88, v36\n"
        "v_cvt_pk_bf16_f32 v146, v72, v76\n"
        "v_cvt_pk_bf16_f32 v148, v49, v41\n"
        "v_mfma_f32_16x16x32_bf16 v[24:27], v[176:179], v[152:155], v[24:27]\n"
        "ds_read_b32 v165, v10\n"
        "v_cvt_pk_bf16_f32 v149, v89, v37\n"
        "v_cvt_pk_bf16_f32 v150, v73, v77\n"
        "v_cvt_pk_bf16_f32 v156, v50, v42\n"
        "v_cvt_pk_bf16_f32 v157, v90, v38\n"
        "v_cvt_pk_bf16_f32 v158, v74, v78\n"
        "v_cvt_pk_bf16_f32 v160, v51, v43\n"
        "v_cvt_pk_bf16_f32 v161, v91, v39\n"
        "v_mfma_f32_16x16x32_bf16 v[28:31], v[188:191], v[152:155], 0\n"
        "s_mov_b32 exec_hi, 0xffff\n"
        "v_add_u32 v10, s69, v10\n"
        "s_mov_b32 exec_hi, -1\n"
        "v_cvt_pk_bf16_f32 v20, v24, v25\n"
        "v_cvt_pk_bf16_f32 v21, v26, v27\n"
        "v_cvt_pk_bf16_f32 v162, v75, v79\n"
        "v_mfma_f32_16x16x32_bf16 v[28:31], v[184:187], v[108:111], v[28:31]\n"
        "v_mfma_f32_16x16x32_bf16 v[28:31], v[180:183], v[104:107], v[28:31]\n"
        "s_add_u32 s37, s36, 196224\n"
        "s_waitcnt lgkmcnt(0)\n"
        "v_readlane_b32 s58, v165, 56\n"
        "v_mfma_f32_16x16x32_bf16 v[40:43], v[192:195], v[104:107], 0\n"
        "v_mfma_f32_16x16x32_bf16 v[40:43], v[196:199], v[108:111], v[40:43]\n"
        "v_mfma_f32_16x16x32_bf16 v[40:43], v[200:203], v[152:155], v[40:43]\n"
        "buffer_store_dwordx3 v[124:126], v12, s[8:11], s37 offen offset:384 sc0 sc1 nt\n"
        "v_mfma_f32_16x16x32_bf16 v[36:39], v[212:215], v[152:155], 0\n"
        "v_cvt_pk_bf16_f32 v22, v28, v29\n"
        "v_cvt_pk_bf16_f32 v23, v30, v31\n"
        "v_mfma_f32_16x16x32_bf16 v[36:39], v[208:211], v[108:111], v[36:39]\n"
        "s_add_u32 s37, s36, 196224\n"
        "v_mfma_f32_16x16x32_bf16 v[36:39], v[204:207], v[104:107], v[36:39]\n"
        "v_mfma_f32_16x16x32_bf16 v[32:35], v[216:219], v[104:107], 0\n"
        "s_setprio 2\n"
        "v_cvt_pk_bf16_f32 v24, v40, v41\n"
        "v_cvt_pk_bf16_f32 v25, v42, v43\n"
        "v_mfma_f32_16x16x32_bf16 v[32:35], v[220:223], v[108:111], v[32:35]\n"
        "buffer_store_dwordx3 v[128:130], v13, s[8:11], s37 offen offset:1152 sc0 sc1 nt\n"
        "v_mfma_f32_16x16x32_bf16 v[32:35], v[224:227], v[152:155], v[32:35]\n"
        "ds_read_b128 v[40:43], v16 offset:128\n"
        "v_cvt_pk_bf16_f32 v26, v36, v37\n"
        "v_cvt_pk_bf16_f32 v27, v38, v39\n"
        "v_mfma_f32_16x16x32_bf16 v[48:51], v[236:239], v[152:155], 0\n"
        "s_add_u32 s37, s36, 294336\n"
        "ds_read_b128 v[44:47], v5\n"
        "v_mfma_f32_16x16x32_bf16 v[48:51], v[232:235], v[108:111], v[48:51]\n"
        "ds_read_b32 v164, v11 offset:192\n"
        "v_cvt_pk_bf16_f32 v28, v32, v33\n"
        "v_cvt_pk_bf16_f32 v29, v34, v35\n"
        "v_mfma_f32_16x16x32_bf16 v[48:51], v[228:231], v[104:107], v[48:51]\n"
        "s_waitcnt lgkmcnt(2)\n"
        "v_mfma_f32_16x16x32_bf16 v[52:55], v[40:43], v[152:155], 0\n"
        "buffer_store_dwordx3 v[136:138], v12, s[8:11], s37 offen offset:576 sc0 sc1 nt\n"
        "ds_read_b128 v[64:67], v7 offset:6784\n"
        "s_waitcnt lgkmcnt(2)\n"
        "v_smfmac_f32_16x16x64_bf16 v[52:55], v[44:47], v[104:111], v4\n"
        "ds_read_b128 v[32:35], v7 offset:6656\n"
        "ds_read_b128 v[36:39], v7 offset:6720\n"
        "v_cvt_pk_bf16_f32 v30, v48, v49\n"
        "v_cvt_pk_bf16_f32 v31, v50, v51\n"
        "v_add_u32 v7, s69, v7\n"
        "v_mfma_f32_16x16x32_bf16 v[48:51], v[40:43], v[132:135], 0\n"
        "s_add_u32 s37, s36, 392448\n"
        "v_smfmac_f32_16x16x64_bf16 v[48:51], v[44:47], v[56:63], v4\n"
        "s_waitcnt lgkmcnt(2)\n"
        "v_add_f32_dpp v164, v52, v53 quad_perm:[0,1,2,3] row_mask:0x1 bank_mask:0xf\n"
        "v_mfma_f32_16x16x32_bf16 v[52:55], v[40:43], v[64:67], 0\n"
        "s_waitcnt lgkmcnt(0)\n"
        "v_smfmac_f32_16x16x64_bf16 v[52:55], v[44:47], v[32:39], v4\n"
        "buffer_store_dwordx3 v[140:142], v12, s[8:11], s37 offen sc0 sc1 nt\n"
        "v_mfma_f32_16x16x32_bf16 v[96:99], v[104:107], v[20:23], 0\n"
        "v_mfma_f32_16x16x32_bf16 v[96:99], v[108:111], v[24:27], v[96:99]\n"
        "s_add_u32 s37, s36, 392448\n"
        "v_add_f32_dpp v164, v48, v49 quad_perm:[0,1,2,3] row_mask:0x2 bank_mask:0xf\n"
        "v_mfma_f32_16x16x32_bf16 v[96:99], v[152:155], v[28:31], v[96:99]\n"
        "v_mfma_f32_16x16x32_bf16 v[100:103], v[132:135], v[28:31], 0\n"
        "v_mfma_f32_16x16x32_bf16 v[100:103], v[60:63], v[24:27], v[100:103]\n"
        "buffer_store_dwordx3 v[144:146], v15, s[8:11], s37 offen offset:768 sc0 sc1 nt\n"
        "v_add_f32_dpp v164, v52, v53 quad_perm:[0,1,2,3] row_mask:0x4 bank_mask:0xf\n"
        "v_mul_f32 v164, v164, v165\n"
        "s_mov_b64 exec, s[56:57]\n"
        "v_mov_b32 v164, 0xff800000\n"
        "s_mov_b64 exec, -1\n"
        "v_mfma_f32_16x16x32_bf16 v[100:103], v[56:59], v[20:23], v[100:103]\n"
        "ds_read_b128 v[72:75], v8 offset:128\n"
        "v_max_f32_dpp v163, v164, v164 row_ror:8 row_mask:0xf bank_mask:0xf\n"
        "v_mfma_f32_16x16x32_bf16 v[88:91], v[32:35], v[20:23], 0\n"
        "s_add_u32 s37, s36, 490560\n"
        "ds_read2_b32 v[76:77], v6 offset0:0 offset1:2\n"
        "ds_read2_b32 v[78:79], v6 offset0:8 offset1:10\n"
        "ds_read_i16 v167, v0 offset:20584\n"
        "ds_read_i16 v151, v0 offset:20586\n"
        "v_max_f32_dpp v163, v163, v163 row_ror:4 row_mask:0xf bank_mask:0xf\n"
        "v_mfma_f32_16x16x32_bf16 v[88:91], v[36:39], v[24:27], v[88:91]\n"
        "v_mfma_f32_16x16x32_bf16 v[88:91], v[64:67], v[28:31], v[88:91]\n"
        "v_max_f32_dpp v163, v163, v163 row_ror:2 row_mask:0xf bank_mask:0xf\n"
        "s_waitcnt lgkmcnt(4)\n"
        "v_mfma_f32_16x16x32_bf16 v[92:95], v[72:75], v[28:31], 0\n"
        "buffer_store_dwordx3 v[148:150], v12, s[8:11], s37 offen offset:192 sc0 sc1 nt\n"
        "v_max_f32_dpp v163, v163, v163 row_ror:1 row_mask:0xf bank_mask:0xf\n"
        "s_waitcnt lgkmcnt(2)\n"
        "v_smfmac_f32_16x16x64_bf16 v[92:95], v[76:79], v[20:27], v4\n"
        "s_waitcnt lgkmcnt(0)\n"
        "v_add_u32 v167, v9, v167\n"
        "v_add_u32 v151, v9, v151\n"
        "v_mfma_f32_16x16x32_bf16 v[20:23], v[168:171], v[56:59], 0\n"
        "s_add_u32 s37, s36, 490560\n"
        "v_mfma_f32_16x16x32_bf16 v[20:23], v[172:175], v[60:63], v[20:23]\n"
        "v_mfma_f32_16x16x32_bf16 v[20:23], v[176:179], v[132:135], v[20:23]\n"
        "v_mfma_f32_16x16x32_bf16 v[28:31], v[188:191], v[132:135], 0\n"
        "buffer_store_dwordx3 v[156:158], v14, s[8:11], s37 offen offset:960 sc0 sc1 nt\n"
        "v_add_f32 v92, v92, v93\n"
        "v_mul_f32 v92, s58, v92\n"
        "s_mov_b64 exec, s[48:49]\n"
        "v_mov_b32 v92, 0xff800000\n"
        "s_mov_b64 exec, -1\n"
        "v_mfma_f32_16x16x32_bf16 v[28:31], v[184:187], v[60:63], v[28:31]\n"
        "v_cvt_pk_bf16_f32 v44, v20, v21\n"
        "v_cvt_pk_bf16_f32 v45, v22, v23\n"
        "v_mfma_f32_16x16x32_bf16 v[28:31], v[180:183], v[56:59], v[28:31]\n"
        "s_add_u32 s37, s36, 588672\n"
        "ds_read_b32 v20, v167\n"
        "ds_read_b32 v21, v167 offset:832\n"
        "ds_read_b32 v22, v167 offset:1664\n"
        "ds_read_b32 v23, v167 offset:2496\n"
        "v_mfma_f32_16x16x32_bf16 v[40:43], v[192:195], v[56:59], 0\n"
        "ds_read_b32 v24, v167 offset:3328\n"
        "ds_read_b32 v25, v167 offset:4160\n"
        "v_cvt_pk_bf16_f32 v46, v28, v29\n"
        "v_cvt_pk_bf16_f32 v47, v30, v31\n"
        "ds_read_b32 v26, v167 offset:4992\n"
        "ds_read_b32 v27, v167 offset:5824\n"
        "v_mfma_f32_16x16x32_bf16 v[40:43], v[196:199], v[60:63], v[40:43]\n"
        "ds_read_b32 v28, v167 offset:6656\n"
        "ds_read_b32 v29, v167 offset:7488\n"
        "ds_read_b32 v30, v167 offset:8320\n"
        "ds_read_b32 v31, v167 offset:9152\n"
        "s_waitcnt lgkmcnt(11)\n"
        "v_mul_f32 v96, v96, v20\n"
        "s_waitcnt lgkmcnt(9)\n"
        "v_mul_f32 v97, v97, v21\n"
        "v_mfma_f32_16x16x32_bf16 v[40:43], v[200:203], v[132:135], v[40:43]\n"
        "buffer_store_dwordx3 v[160:162], v12, s[8:11], s37 offen offset:384 sc0 sc1 nt\n"
        "v_mul_f32 v98, v98, v22\n"
        "s_waitcnt lgkmcnt(6)\n"
        "v_mul_f32 v99, v99, v23\n"
        "v_mul_f32 v100, v100, v24\n"
        "v_max3_f32 v161, v96, v97, v98\n"
        "v_mul_f32 v101, v101, v25\n"
        "s_waitcnt lgkmcnt(4)\n"
        "v_mul_f32 v102, v102, v26\n"
        "v_cvt_pk_bf16_f32 v48, v40, v41\n"
        "v_cvt_pk_bf16_f32 v49, v42, v43\n"
        "v_max3_f32 v161, v161, v99, v100\n"
        "v_mfma_f32_16x16x32_bf16 v[40:43], v[212:215], v[132:135], 0\n"
        "ds_read_b32 v159, v151\n"
        "v_mul_f32 v103, v103, v27\n"
        "s_waitcnt lgkmcnt(1)\n"
        "v_mul_f32 v88, v88, v28\n"
        "v_max3_f32 v161, v161, v101, v102\n"
        "v_mul_f32 v89, v89, v29\n"
        "v_mul_f32 v90, v90, v30\n"
        "v_max3_f32 v161, v161, v103, v88\n"
        "v_mfma_f32_16x16x32_bf16 v[40:43], v[208:211], v[60:63], v[40:43]\n"
        "ds_read_b32 v166, v151 offset:3328\n"
        "v_mul_f32 v91, v91, v31\n"
        "v_max3_f32 v161, v161, v89, v90\n"
        "v_max3_f32 v161, v161, v91, v92\n"
        "v_mfma_f32_16x16x32_bf16 v[40:43], v[204:207], v[56:59], v[40:43]\n"
        "ds_read_b32 v167, v151 offset:6656\n"
        "v_permlane32_swap_b32 v161, v163\n"
        "v_max_f32 v161, v161, v163\n"
        "v_mov_b32 v163, v161\n"
        "v_mfma_f32_16x16x32_bf16 v[52:55], v[216:219], v[56:59], 0\n"
        "v_mfma_f32_16x16x32_bf16 v[52:55], v[220:223], v[60:63], v[52:55]\n"
        "v_permlane16_swap_b32 v161, v163\n"
        "v_max_f32 v161, v161, v163\n"
        "v_cvt_pk_bf16_f32 v50, v40, v41\n"
        "v_cvt_pk_bf16_f32 v51, v42, v43\n"
        "v_mov_b32 v163, v161\n"
        "v_mfma_f32_16x16x32_bf16 v[52:55], v[224:227], v[132:135], v[52:55]\n"
        "v_mfma_f32_16x16x32_bf16 v[68:71], v[236:239], v[132:135], 0\n"
        "v_permlane32_swap_b32 v161, v163\n"
        "s_waitcnt lgkmcnt(0)\n"
        "v_mul_f32 v161, v161, v159\n"
        "v_fma_f32 v96, v96, v159, -v161\n"
        "v_fma_f32 v97, v97, v159, -v161\n"
        "v_exp_f32 v96, v96\n"
        "v_mfma_f32_16x16x32_bf16 v[68:71], v[232:235], v[60:63], v[68:71]\n"
        "v_cvt_pk_bf16_f32 v40, v52, v53\n"
        "v_cvt_pk_bf16_f32 v41, v54, v55\n"
        "v_exp_f32 v97, v97\n"
        "v_fma_f32 v98, v98, v159, -v161\n"
        "v_exp_f32 v98, v98\n"
        "v_fma_f32 v99, v99, v159, -v161\n"
        "v_mfma_f32_16x16x32_bf16 v[68:71], v[228:231], v[56:59], v[68:71]\n"
        "v_add_f32 v162, v96, v97\n"
        "v_exp_f32 v99, v99\n"
        "v_fma_f32 v100, v100, v159, -v161\n"
        "v_add_f32 v162, v162, v98\n"
        "v_exp_f32 v100, v100\n"
        "v_fma_f32 v101, v101, v159, -v161\n"
        "v_mfma_f32_16x16x32_bf16 v[52:55], v[104:107], v[44:47], 0\n"
        "v_add_f32 v162, v162, v99\n"
        "v_cvt_pk_bf16_f32 v42, v68, v69\n"
        "v_cvt_pk_bf16_f32 v43, v70, v71\n"
        "v_exp_f32 v101, v101\n"
        "v_fma_f32 v102, v102, v159, -v161\n"
        "v_add_f32 v162, v162, v100\n"
        "v_fma_f32 v103, v103, v159, -v161\n"
        "v_mfma_f32_16x16x32_bf16 v[52:55], v[108:111], v[48:51], v[52:55]\n"
        "v_exp_f32 v102, v102\n"
        "v_sub_f32 v164, v164, v163\n"
        "v_add_f32 v162, v162, v101\n"
        "v_exp_f32 v103, v103\n"
        "v_fma_f32 v88, v88, v159, -v161\n"
        "v_mul_f32 v164, s58, v164\n"
        "v_mfma_f32_16x16x32_bf16 v[52:55], v[152:155], v[40:43], v[52:55]\n"
        "v_add_f32 v162, v162, v102\n"
        "v_exp_f32 v88, v88\n"
        "v_fma_f32 v89, v89, v159, -v161\n"
        "v_exp_f32 v164, v164\n"
        "v_add_f32 v162, v162, v103\n"
        "v_fma_f32 v90, v90, v159, -v161\n"
        "v_mfma_f32_16x16x32_bf16 v[68:71], v[132:135], v[40:43], 0\n"
        "v_exp_f32 v89, v89\n"
        "v_add_f32 v162, v162, v88\n"
        "v_exp_f32 v90, v90\n"
        "v_fma_f32 v91, v91, v159, -v161\n"
        "v_add_f32_dpp v160, v164, v164 row_ror:8 row_mask:0xf bank_mask:0xf\n"
        "v_add_f32 v162, v162, v89\n"
        "v_mfma_f32_16x16x32_bf16 v[68:71], v[60:63], v[48:51], v[68:71]\n"
        "v_exp_f32 v91, v91\n"
        "v_fma_f32 v92, v92, v159, -v161\n"
        "v_add_f32_dpp v160, v160, v160 row_ror:4 row_mask:0xf bank_mask:0xf\n"
        "v_add_f32 v162, v162, v90\n"
        "v_exp_f32 v92, v92\n"
        "v_add_f32_dpp v160, v160, v160 row_ror:2 row_mask:0xf bank_mask:0xf\n"
        "v_mfma_f32_16x16x32_bf16 v[68:71], v[56:59], v[44:47], v[68:71]\n"
        "v_add_f32 v162, v162, v91\n"
        "v_add_f32_dpp v160, v160, v160 row_ror:1 row_mask:0xf bank_mask:0xf\n"
        "v_add_f32 v162, v162, v92\n"
        "v_mul_f32 v96, v96, v20\n"
        "v_mul_f32 v97, v97, v21\n"
        "v_permlane32_swap_b32 v162, v160\n"
        "v_add_f32 v162, v162, v160\n"
        "v_mfma_f32_16x16x32_bf16 v[80:83], v[32:35], v[44:47], 0\n"
        "v_mov_b32 v163, v162\n"
        "v_mul_f32 v98, v98, v22\n"
        "v_mul_f32 v99, v99, v23\n"
        "v_permlane16_swap_b32 v162, v163\n"
        "v_add_f32 v162, v162, v163\n"
        "v_rcp_f32 v162, v162\n"
        "v_mfma_f32_16x16x32_bf16 v[80:83], v[36:39], v[48:51], v[80:83]\n"
        "v_mov_b32 v160, v162\n"
        "v_mul_f32 v100, v100, v24\n"
        "v_mul_f32 v101, v101, v25\n"
        "v_permlane32_swap_b32 v162, v160\n"
        "v_mul_f32 v102, v102, v26\n"
        "v_mul_f32 v103, v103, v27\n"
        "v_mul_f32 v96, v96, v162\n"
        "v_mfma_f32_16x16x32_bf16 v[80:83], v[64:67], v[40:43], v[80:83]\n"
        "v_mul_f32 v97, v97, v162\n"
        "v_mul_f32 v98, v98, v162\n"
        "v_mul_f32 v99, v99, v162\n"
        "v_mul_f32 v100, v100, v162\n"
        "v_mul_f32 v101, v101, v162\n"
        "v_mul_f32 v102, v102, v162\n"
        "v_mul_f32 v103, v103, v162\n"
        "v_mul_f32 v52, v52, v20\n"
        "v_mfma_f32_16x16x32_bf16 v[84:87], v[72:75], v[40:43], 0\n"
        "v_mul_f32 v53, v53, v21\n"
        "v_mul_f32 v54, v54, v22\n"
        "v_cvt_pk_bf16_f32 v40, v96, v97\n"
        "v_cvt_pk_bf16_f32 v41, v98, v99\n"
        "v_cvt_pk_bf16_f32 v42, v100, v101\n"
        "v_cvt_pk_bf16_f32 v43, v102, v103\n"
        "v_mul_f32 v55, v55, v23\n"
        "v_mul_f32 v68, v68, v24\n"
        "v_smfmac_f32_16x16x64_bf16 v[84:87], v[76:79], v[44:51], v4\n"
        "v_max3_f32 v163, v52, v53, v54\n"
        "v_mul_f32 v88, v88, v28\n"
        "v_mul_f32 v89, v89, v29\n"
        "v_mul_f32 v90, v90, v30\n"
        "v_mul_f32 v91, v91, v31\n"
        "v_mul_f32 v92, s58, v92\n"
        "v_mul_f32 v69, v69, v25\n"
        "v_mfma_f32_16x16x32_bf16 v[48:51], v[168:171], v[32:35], 0\n"
        "v_add_f32 v84, v84, v85\n"
        "v_mul_f32 v70, v70, v26\n"
        "v_max3_f32 v163, v163, v55, v68\n"
        "v_mul_f32 v88, v88, v162\n"
        "v_mul_f32 v89, v89, v162\n"
        "v_mul_f32 v90, v90, v162\n"
        "v_mul_f32 v91, v91, v162\n"
        "v_fma_f32 v92, v92, v162, v19\n"
        "v_mfma_f32_16x16x32_bf16 v[48:51], v[172:175], v[36:39], v[48:51]\n"
        "v_mul_f32 v71, v71, v27\n"
        "v_mul_f32 v80, v80, v28\n"
        "v_mul_f32 v84, s58, v84\n"
        "v_max3_f32 v163, v163, v69, v70\n"
        "v_cvt_pk_bf16_f32 v44, v88, v89\n"
        "v_cvt_pk_bf16_f32 v46, v90, v91\n"
        "v_cvt_pk_bf16_f32 v45, v92, 0\n"
        "v_mul_f32 v81, v81, v29\n"
        "v_mfma_f32_16x16x32_bf16 v[48:51], v[176:179], v[64:67], v[48:51]\n"
        "s_mov_b64 exec, s[48:49]\n"
        "v_mov_b32 v84, 0xff800000\n"
        "s_mov_b64 exec, -1\n"
        "v_mul_f32 v82, v82, v30\n"
        "v_max3_f32 v163, v163, v71, v80\n"
        "v_mul_f32 v83, v83, v31\n"
        "v_max3_f32 v163, v163, v81, v82\n"
        "v_max3_f32 v163, v163, v83, v84\n"
        "v_cvt_pk_bf16_f32 v92, v48, v49\n"
        "v_mfma_f32_16x16x32_bf16 v[88:91], v[188:191], v[64:67], 0\n"
        "v_cvt_pk_bf16_f32 v93, v50, v51\n"
        "v_mul_f32 v164, v164, v165\n"
        "v_mul_f32 v164, v164, v160\n"
        "v_cvt_pk_bf16_f32 v164, v164, 0\n"
        "ds_write_b16 v1, v164\n"
        "v_mfma_f32_16x16x32_bf16 v[88:91], v[184:187], v[36:39], v[88:91]\n"
        "v_mfma_f32_16x16x32_bf16 v[88:91], v[180:183], v[32:35], v[88:91]\n"
        "v_mfma_f32_16x16x32_bf16 v[48:51], v[192:195], v[32:35], 0\n"
        "v_mfma_f32_16x16x32_bf16 v[48:51], v[196:199], v[36:39], v[48:51]\n"
        "v_mfma_f32_16x16x32_bf16 v[48:51], v[200:203], v[64:67], v[48:51]\n"
        "v_mfma_f32_16x16x32_bf16 v[112:115], v[212:215], v[64:67], 0\n"
        "v_mfma_f32_16x16x32_bf16 v[112:115], v[208:211], v[36:39], v[112:115]\n"
        "v_mfma_f32_16x16x32_bf16 v[112:115], v[204:207], v[32:35], v[112:115]\n"
        "v_mfma_f32_16x16x32_bf16 v[100:103], v[216:219], v[32:35], 0\n"
        "v_mfma_f32_16x16x32_bf16 v[100:103], v[220:223], v[36:39], v[100:103]\n"
        "v_cvt_pk_bf16_f32 v94, v88, v89\n"
        "v_cvt_pk_bf16_f32 v95, v90, v91\n"
        "v_mfma_f32_16x16x32_bf16 v[100:103], v[224:227], v[64:67], v[100:103]\n"
        "v_cvt_pk_bf16_f32 v96, v48, v49\n"
        "v_cvt_pk_bf16_f32 v97, v50, v51\n"
        "v_mfma_f32_16x16x32_bf16 v[48:51], v[236:239], v[64:67], 0\n"
        "v_cvt_pk_bf16_f32 v98, v112, v113\n"
        "v_cvt_pk_bf16_f32 v99, v114, v115\n"
        "v_mfma_f32_16x16x32_bf16 v[48:51], v[232:235], v[36:39], v[48:51]\n"
        "v_mfma_f32_16x16x32_bf16 v[48:51], v[228:231], v[32:35], v[48:51]\n"
        "v_mfma_f32_16x16x32_bf16 v[116:119], v[104:107], v[92:95], 0\n"
        "v_cvt_pk_bf16_f32 v112, v100, v101\n"
        "v_cvt_pk_bf16_f32 v113, v102, v103\n"
        "v_mfma_f32_16x16x32_bf16 v[116:119], v[108:111], v[96:99], v[116:119]\n"
        "s_nop 3\n"
        "v_cvt_pk_bf16_f32 v114, v48, v49\n"
        "v_cvt_pk_bf16_f32 v115, v50, v51\n"
        "s_nop 1\n"
        "v_mfma_f32_16x16x32_bf16 v[116:119], v[152:155], v[112:115], v[116:119]\n"
        "v_mfma_f32_16x16x32_bf16 v[124:127], v[132:135], v[112:115], 0\n"
        "v_mfma_f32_16x16x32_bf16 v[124:127], v[60:63], v[96:99], v[124:127]\n"
        "v_mfma_f32_16x16x32_bf16 v[124:127], v[56:59], v[92:95], v[124:127]\n"
        "v_mfma_f32_16x16x32_bf16 v[88:91], v[32:35], v[92:95], 0\n"
        "v_mfma_f32_16x16x32_bf16 v[88:91], v[36:39], v[96:99], v[88:91]\n"
        "v_mfma_f32_16x16x32_bf16 v[88:91], v[64:67], v[112:115], v[88:91]\n"
        "v_mfma_f32_16x16x32_bf16 v[48:51], v[72:75], v[112:115], 0\n"
        "v_smfmac_f32_16x16x64_bf16 v[48:51], v[76:79], v[92:99], v4\n"
        "v_mul_f32 v116, v116, v20\n"
        "v_mul_f32 v117, v117, v21\n"
        "v_mul_f32 v118, v118, v22\n"
        "v_mul_f32 v119, v119, v23\n"
        "v_mul_f32 v124, v124, v24\n"
        "v_max3_f32 v162, v116, v117, v118\n"
        "v_mul_f32 v125, v125, v25\n"
        "v_mfma_f32_16x16x32_bf16 v[72:75], v[104:107], v[240:243], 0\n"
        "v_add_f32 v48, v48, v49\n"
        "v_mul_f32 v126, v126, v26\n"
        "v_max3_f32 v162, v162, v119, v124\n"
        "v_mul_f32 v127, v127, v27\n"
        "v_mul_f32 v88, v88, v28\n"
        "v_mul_f32 v48, s58, v48\n"
        "v_max3_f32 v162, v162, v125, v126\n"
        "v_mul_f32 v89, v89, v29\n"
        "v_mfma_f32_16x16x32_bf16 v[72:75], v[108:111], v[244:247], v[72:75]\n"
        "s_mov_b64 exec, s[48:49]\n"
        "v_mov_b32 v48, 0xff800000\n"
        "s_mov_b64 exec, -1\n"
        "v_mul_f32 v90, v90, v30\n"
        "v_max3_f32 v162, v162, v127, v88\n"
        "v_mul_f32 v91, v91, v31\n"
        "v_max3_f32 v162, v162, v89, v90\n"
        "v_max3_f32 v162, v162, v91, v48\n"
        "v_mfma_f32_16x16x32_bf16 v[72:75], v[152:155], v[248:251], v[72:75]\n"
        "ds_read_b128 v[32:35], v17\n"
        "v_permlane32_swap_b32 v163, v162\n"
        "v_max_f32 v163, v163, v162\n"
        "v_mov_b32 v165, v163\n"
        "v_mfma_f32_16x16x32_bf16 v[76:79], v[132:135], v[248:251], 0\n"
        "ds_read_b128 v[36:39], v17 offset:64\n"
        "v_permlane16_swap_b32 v163, v165\n"
        "v_max_f32 v163, v163, v165\n"
        "v_cvt_pk_bf16_f32 v64, v72, v73\n"
        "v_cvt_pk_bf16_f32 v65, v74, v75\n"
        "v_mov_b32 v162, v163\n"
        "v_mfma_f32_16x16x32_bf16 v[76:79], v[60:63], v[244:247], v[76:79]\n"
        "ds_read_b128 v[100:103], v17 offset:128\n"
        "v_permlane32_swap_b32 v163, v162\n"
        "v_mul_f32 v163, v163, v166\n"
        "v_mul_f32 v162, v162, v167\n"
        "v_fma_f32 v52, v52, v166, -v163\n"
        "v_fma_f32 v53, v53, v166, -v163\n"
        "v_fma_f32 v116, v116, v167, -v162\n"
        "v_mfma_f32_16x16x32_bf16 v[76:79], v[56:59], v[240:243], v[76:79]\n"
        "s_waitcnt lgkmcnt(1)\n"
        "ds_read_b128 v[112:115], v0 offset:5136\n"
        "v_fma_f32 v117, v117, v167, -v162\n"
        "v_exp_f32 v52, v52\n"
        "v_exp_f32 v53, v53\n"
        "v_fma_f32 v54, v54, v166, -v163\n"
        "v_fma_f32 v118, v118, v167, -v162\n"
        "v_mfma_f32_16x16x32_bf16 v[92:95], v[32:35], v[240:243], 0\n"
        "ds_read_b128 v[120:123], v0 offset:4112\n"
        "v_cvt_pk_bf16_f32 v66, v76, v77\n"
        "v_cvt_pk_bf16_f32 v67, v78, v79\n"
        "v_exp_f32 v116, v116\n"
        "v_exp_f32 v117, v117\n"
        "v_fma_f32 v55, v55, v166, -v163\n"
        "v_mfma_f32_16x16x32_bf16 v[92:95], v[36:39], v[244:247], v[92:95]\n"
        "s_waitcnt lgkmcnt(1)\n"
        "v_exp_f32 v54, v54\n"
        "v_exp_f32 v118, v118\n"
        "v_fma_f32 v119, v119, v167, -v162\n"
        "v_add_f32 v164, v52, v53\n"
        "v_exp_f32 v55, v55\n"
        "v_mfma_f32_16x16x32_bf16 v[92:95], v[100:103], v[248:251], v[92:95]\n"
        "v_fma_f32 v68, v68, v166, -v163\n"
        "v_add_f32 v165, v116, v117\n"
        "v_exp_f32 v119, v119\n"
        "v_fma_f32 v124, v124, v167, -v162\n"
        "v_add_f32 v164, v164, v54\n"
        "v_exp_f32 v68, v68\n"
        "v_mfma_f32_16x16x32_bf16 v[128:131], v[100:103], v[112:115], 0\n"
        "s_waitcnt lgkmcnt(0)\n"
        "v_cvt_pk_bf16_f32 v72, v92, v93\n"
        "v_cvt_pk_bf16_f32 v76, v94, v95\n"
        "v_fma_f32 v69, v69, v166, -v163\n"
        "v_add_f32 v165, v165, v118\n"
        "v_exp_f32 v124, v124\n"
        "v_fma_f32 v125, v125, v167, -v162\n"
        "v_add_f32 v164, v164, v55\n"
        "v_mfma_f32_16x16x32_bf16 v[128:131], v[36:39], v[120:123], v[128:131]\n"
        "v_exp_f32 v69, v69\n"
        "v_fma_f32 v70, v70, v166, -v163\n"
        "v_add_f32 v165, v165, v119\n"
        "v_exp_f32 v125, v125\n"
        "v_fma_f32 v126, v126, v167, -v162\n"
        "v_add_f32 v164, v164, v68\n"
        "v_mfma_f32_16x16x32_bf16 v[128:131], v[32:35], v[252:255], v[128:131]\n"
        "v_exp_f32 v70, v70\n"
        "v_fma_f32 v71, v71, v166, -v163\n"
        "v_add_f32 v165, v165, v124\n"
        "v_exp_f32 v126, v126\n"
        "v_fma_f32 v127, v127, v167, -v162\n"
        "v_add_f32 v164, v164, v69\n"
        "v_mfma_f32_16x16x32_bf16 v[140:143], v[56:59], v[252:255], 0\n"
        "v_exp_f32 v71, v71\n"
        "v_cvt_pk_bf16_f32 v92, v128, v129\n"
        "v_cvt_pk_bf16_f32 v96, v130, v131\n"
        "v_fma_f32 v80, v80, v166, -v163\n"
        "v_add_f32 v165, v165, v125\n"
        "v_exp_f32 v127, v127\n"
        "v_mfma_f32_16x16x32_bf16 v[140:143], v[60:63], v[120:123], v[140:143]\n"
        "v_fma_f32 v88, v88, v167, -v162\n"
        "v_add_f32 v164, v164, v70\n"
        "v_exp_f32 v80, v80\n"
        "v_fma_f32 v81, v81, v166, -v163\n"
        "v_add_f32 v165, v165, v126\n"
        "v_exp_f32 v88, v88\n"
        "v_mfma_f32_16x16x32_bf16 v[140:143], v[132:135], v[112:115], v[140:143]\n"
        "ds_read_b128 v[144:147], v0 offset:6176\n"
        "v_fma_f32 v89, v89, v167, -v162\n"
        "v_add_f32 v164, v164, v71\n"
        "v_exp_f32 v81, v81\n"
        "v_fma_f32 v82, v82, v166, -v163\n"
        "v_add_f32 v165, v165, v127\n"
        "v_fma_f32 v90, v90, v167, -v162\n"
        "v_mfma_f32_16x16x32_bf16 v[148:151], v[152:155], v[112:115], 0\n"
        "ds_read_b128 v[136:139], v0 offset:7200\n"
        "v_cvt_pk_bf16_f32 v114, v140, v141\n"
        "v_cvt_pk_bf16_f32 v115, v142, v143\n"
        "v_exp_f32 v89, v89\n"
        "v_add_f32 v164, v164, v80\n"
        "v_exp_f32 v82, v82\n"
        "v_mfma_f32_16x16x32_bf16 v[148:151], v[108:111], v[120:123], v[148:151]\n"
        "ds_read_b128 v[140:143], v0 offset:8224\n"
        "v_fma_f32 v83, v83, v166, -v163\n"
        "v_add_f32 v165, v165, v88\n"
        "v_exp_f32 v90, v90\n"
        "v_fma_f32 v91, v91, v167, -v162\n"
        "v_add_f32 v164, v164, v81\n"
        "v_fma_f32 v84, v84, v166, -v163\n"
        "v_mfma_f32_16x16x32_bf16 v[148:151], v[104:107], v[252:255], v[148:151]\n"
        "s_waitcnt lgkmcnt(1)\n"
        "v_exp_f32 v83, v83\n"
        "v_add_f32 v165, v165, v89\n"
        "v_exp_f32 v91, v91\n"
        "v_fma_f32 v48, v48, v167, -v162\n"
        "v_add_f32 v164, v164, v82\n"
        "v_add_f32 v165, v165, v90\n"
        "v_mfma_f32_16x16x32_bf16 v[128:131], v[104:107], v[144:147], 0\n"
        "v_cvt_pk_bf16_f32 v112, v148, v149\n"
        "v_cvt_pk_bf16_f32 v113, v150, v151\n"
        "v_exp_f32 v84, v84\n"
        "v_exp_f32 v48, v48\n"
        "v_add_f32 v164, v164, v83\n"
        "v_add_f32 v165, v165, v91\n"
        "v_mfma_f32_16x16x32_bf16 v[128:131], v[108:111], v[136:139], v[128:131]\n"
        "s_waitcnt lgkmcnt(0)\n"
        "v_add_f32 v164, v164, v84\n"
        "v_add_f32 v165, v165, v48\n"
        "v_mul_f32 v52, v52, v20\n"
        "v_mul_f32 v53, v53, v21\n"
        "v_permlane32_swap_b32 v164, v165\n"
        "v_add_f32 v164, v164, v165\n"
        "v_mov_b32 v167, v164\n"
        "v_mfma_f32_16x16x32_bf16 v[128:131], v[152:155], v[140:143], v[128:131]\n"
        "v_mul_f32 v54, v54, v22\n"
        "v_permlane16_swap_b32 v164, v167\n"
        "v_add_f32 v164, v164, v167\n"
        "v_rcp_f32 v164, v164\n"
        "v_mul_f32 v55, v55, v23\n"
        "v_mov_b32 v165, v164\n"
        "v_mfma_f32_16x16x32_bf16 v[156:159], v[132:135], v[140:143], 0\n"
        "v_mul_f32 v68, v68, v24\n"
        "v_cvt_pk_bf16_f32 v120, v128, v129\n"
        "v_cvt_pk_bf16_f32 v121, v130, v131\n"
        "v_permlane32_swap_b32 v164, v165\n"
        "v_mul_f32 v69, v69, v25\n"
        "v_mul_f32 v70, v70, v26\n"
        "v_mul_f32 v71, v71, v27\n"
        "v_mfma_f32_16x16x32_bf16 v[156:159], v[60:63], v[136:139], v[156:159]\n"
        "v_mul_f32 v52, v52, v164\n"
        "v_mul_f32 v53, v53, v164\n"
        "v_mul_f32 v54, v54, v164\n"
        "v_mul_f32 v55, v55, v164\n"
        "v_mul_f32 v68, v68, v164\n"
        "v_mul_f32 v69, v69, v164\n"
        "v_mul_f32 v70, v70, v164\n"
        "v_mul_f32 v71, v71, v164\n"
        "v_mfma_f32_16x16x32_bf16 v[156:159], v[56:59], v[144:147], v[156:159]\n"
        "ds_read_b128 v[148:151], v0 offset:11312\n"
        "v_cvt_pk_bf16_f32 v128, v52, v53\n"
        "v_cvt_pk_bf16_f32 v129, v54, v55\n"
        "v_cvt_pk_bf16_f32 v130, v68, v69\n"
        "v_cvt_pk_bf16_f32 v131, v70, v71\n"
        "v_mul_f32 v80, v80, v28\n"
        "v_mul_f32 v81, v81, v29\n"
        "v_mul_f32 v82, v82, v30\n"
        "v_mfma_f32_16x16x32_bf16 v[52:55], v[32:35], v[144:147], 0\n"
        "ds_read_b128 v[68:71], v0 offset:10288\n"
        "v_cvt_pk_bf16_f32 v122, v156, v157\n"
        "v_cvt_pk_bf16_f32 v123, v158, v159\n"
        "v_mul_f32 v83, v83, v31\n"
        "v_mul_f32 v84, s58, v84\n"
        "v_mul_f32 v80, v80, v164\n"
        "v_mul_f32 v81, v81, v164\n"
        "v_mul_f32 v82, v82, v164\n"
        "v_mfma_f32_16x16x32_bf16 v[52:55], v[36:39], v[136:139], v[52:55]\n"
        "ds_read_b128 v[144:147], v0 offset:9264\n"
        "v_mul_f32 v83, v83, v164\n"
        "v_fma_f32 v84, v84, v164, v19\n"
        "v_cvt_pk_bf16_f32 v136, v80, v81\n"
        "v_cvt_pk_bf16_f32 v138, v82, v83\n"
        "v_cvt_pk_bf16_f32 v137, v84, 0\n"
        "v_mul_f32 v116, v116, v20\n"
        "v_mul_f32 v117, v117, v21\n"
        "v_mfma_f32_16x16x32_bf16 v[52:55], v[100:103], v[140:143], v[52:55]\n"
        "s_waitcnt lgkmcnt(1)\n"
        "v_mul_f32 v118, v118, v22\n"
        "v_mul_f32 v119, v119, v23\n"
        "v_mul_f32 v124, v124, v24\n"
        "v_mul_f32 v125, v125, v25\n"
        "v_mul_f32 v126, v126, v26\n"
        "v_mul_f32 v127, v127, v27\n"
        "v_mul_f32 v116, v116, v165\n"
        "v_cvt_pk_bf16_f32 v20, v52, v53\n"
        "v_mfma_f32_16x16x32_bf16 v[84:87], v[100:103], v[148:151], 0\n"
        "v_cvt_pk_bf16_f32 v24, v54, v55\n"
        "v_mul_f32 v117, v117, v165\n"
        "v_mul_f32 v118, v118, v165\n"
        "v_mul_f32 v119, v119, v165\n"
        "v_mul_f32 v124, v124, v165\n"
        "v_mul_f32 v125, v125, v165\n"
        "v_mul_f32 v126, v126, v165\n"
        "v_mul_f32 v127, v127, v165\n"
        "v_mfma_f32_16x16x32_bf16 v[84:87], v[36:39], v[68:71], v[84:87]\n"
        "s_waitcnt lgkmcnt(0)\n"
        "v_cvt_pk_bf16_f32 v52, v116, v117\n"
        "v_cvt_pk_bf16_f32 v53, v118, v119\n"
        "v_cvt_pk_bf16_f32 v54, v124, v125\n"
        "v_cvt_pk_bf16_f32 v55, v126, v127\n"
        "v_mul_f32 v88, v88, v28\n"
        "v_mul_f32 v89, v89, v29\n"
        "v_mul_f32 v90, v90, v30\n"
        "v_mul_f32 v91, v91, v31\n"
        "v_mfma_f32_16x16x32_bf16 v[84:87], v[32:35], v[144:147], v[84:87]\n"
        "v_mul_f32 v48, s58, v48\n"
        "v_mul_f32 v88, v88, v165\n"
        "v_mul_f32 v89, v89, v165\n"
        "v_mul_f32 v90, v90, v165\n"
        "v_mul_f32 v91, v91, v165\n"
        "v_fma_f32 v48, v48, v165, v19\n"
        "v_cvt_pk_bf16_f32 v80, v88, v89\n"
        "v_cvt_pk_bf16_f32 v82, v90, v91\n"
        "v_mfma_f32_16x16x32_bf16 v[28:31], v[56:59], v[144:147], 0\n"
        "v_cvt_pk_bf16_f32 v73, v84, v85\n"
        "v_cvt_pk_bf16_f32 v77, v86, v87\n"
        "v_cvt_pk_bf16_f32 v81, v48, 0\n"
        "v_add_u32 v17, s69, v17\n"
        "v_mfma_f32_16x16x32_bf16 v[28:31], v[60:63], v[68:71], v[28:31]\n"
        "v_mfma_f32_16x16x32_bf16 v[28:31], v[132:135], v[148:151], v[28:31]\n"
        "ds_read_b128 v[84:87], v0 offset:12352\n"
        "v_mfma_f32_16x16x32_bf16 v[48:51], v[152:155], v[148:151], 0\n"
        "ds_read_b128 v[124:127], v0 offset:13376\n"
        "v_mfma_f32_16x16x32_bf16 v[48:51], v[108:111], v[68:71], v[48:51]\n"
        "ds_read_b128 v[88:91], v0 offset:14400\n"
        "v_mfma_f32_16x16x32_bf16 v[48:51], v[104:107], v[144:147], v[48:51]\n"
        "s_waitcnt lgkmcnt(2)\n"
        "v_mfma_f32_16x16x32_bf16 v[144:147], v[104:107], v[84:87], 0\n"
        "v_cvt_pk_bf16_f32 v70, v28, v29\n"
        "v_cvt_pk_bf16_f32 v71, v30, v31\n"
        "s_waitcnt lgkmcnt(1)\n"
        "v_mfma_f32_16x16x32_bf16 v[144:147], v[108:111], v[124:127], v[144:147]\n"
        "s_waitcnt lgkmcnt(0)\n"
        "v_mfma_f32_16x16x32_bf16 v[144:147], v[152:155], v[88:91], v[144:147]\n"
        "v_cvt_pk_bf16_f32 v68, v48, v49\n"
        "v_cvt_pk_bf16_f32 v69, v50, v51\n"
        "v_mfma_f32_16x16x32_bf16 v[148:151], v[132:135], v[88:91], 0\n"
        "v_mfma_f32_16x16x32_bf16 v[148:151], v[60:63], v[124:127], v[148:151]\n"
        "v_mfma_f32_16x16x32_bf16 v[148:151], v[56:59], v[84:87], v[148:151]\n"
        "ds_read_b128 v[48:51], v0 offset:17488\n"
        "v_mfma_f32_16x16x32_bf16 v[140:143], v[32:35], v[84:87], 0\n"
        "ds_read_b128 v[28:31], v0 offset:16464\n"
        "v_cvt_pk_bf16_f32 v116, v144, v145\n"
        "v_cvt_pk_bf16_f32 v117, v146, v147\n"
        "v_mfma_f32_16x16x32_bf16 v[140:143], v[36:39], v[124:127], v[140:143]\n"
        "ds_read_b128 v[84:87], v0 offset:15440\n"
        "v_mfma_f32_16x16x32_bf16 v[140:143], v[100:103], v[88:91], v[140:143]\n"
        "v_cvt_pk_bf16_f32 v118, v148, v149\n"
        "v_cvt_pk_bf16_f32 v119, v150, v151\n"
        "s_waitcnt lgkmcnt(2)\n"
        "v_mfma_f32_16x16x32_bf16 v[88:91], v[100:103], v[48:51], 0\n"
        "s_waitcnt lgkmcnt(1)\n"
        "v_mfma_f32_16x16x32_bf16 v[88:91], v[36:39], v[28:31], v[88:91]\n"
        "s_waitcnt lgkmcnt(0)\n"
        "v_mfma_f32_16x16x32_bf16 v[88:91], v[32:35], v[84:87], v[88:91]\n"
        "ds_read2_b32 v[78:79], v11 offset0:0 offset1:3\n"
        "v_cvt_pk_bf16_f32 v93, v140, v141\n"
        "v_cvt_pk_bf16_f32 v97, v142, v143\n"
        "v_mov_b32_dpp v47, v19 row_shr:4 row_mask:0xf bank_mask:0xf bound_ctrl:0\n"
        "v_mfma_f32_16x16x32_bf16 v[32:35], v[56:59], v[84:87], 0\n"
        "ds_read_b64_tr_b16 v[74:75], v9 offset:0\n"
        "v_mfma_f32_16x16x32_bf16 v[32:35], v[60:63], v[28:31], v[32:35]\n"
        "ds_read2_b32 v[98:99], v11 offset0:1 offset1:4\n"
        "v_cvt_pk_bf16_f32 v21, v88, v89\n"
        "v_cvt_pk_bf16_f32 v25, v90, v91\n"
        "v_mfma_f32_16x16x32_bf16 v[32:35], v[132:135], v[48:51], v[32:35]\n"
        "ds_read_b64_tr_b16 v[94:95], v9 offset:16\n"
        "v_mfma_f32_16x16x32_bf16 v[36:39], v[152:155], v[48:51], 0\n"
        "ds_read2_b32 v[26:27], v11 offset0:2 offset1:5\n"
        "v_mfma_f32_16x16x32_bf16 v[36:39], v[108:111], v[28:31], v[36:39]\n"
        "s_add_u32 s31, s31, 1\n"
        "ds_read_b64_tr_b16 v[22:23], v9 offset:32\n"
        "v_mfma_f32_16x16x32_bf16 v[36:39], v[104:107], v[84:87], v[36:39]\n"
        "s_mul_i32 s38, s31, 1344\n"
        "v_cvt_pk_bf16_f32 v126, v32, v33\n"
        "v_cvt_pk_bf16_f32 v127, v34, v35\n"
        "v_mfma_f32_16x16x32_bf16 v[28:31], v[40:43], v[64:67], 0\n"
        "s_waitcnt lgkmcnt(3)\n"
        "s_cmp_ge_u32 s31, s76\n"
        "v_smfmac_f32_16x16x64_bf16 v[28:31], v[44:47], v[72:79], v18\n"
        "s_cbranch_scc1 L50_%=\n"
        "L51_%=:\n"
        "v_cvt_pk_bf16_f32 v124, v36, v37\n"
        "v_cvt_pk_bf16_f32 v125, v38, v39\n"
        "v_mfma_f32_16x16x32_bf16 v[32:35], v[40:43], v[112:115], 0\n"
        "s_waitcnt lgkmcnt(1)\n"
        "s_cmp_ge_u32 s31, s71\n"
        "v_smfmac_f32_16x16x64_bf16 v[32:35], v[44:47], v[92:99], v18\n"
        "s_cselect_b32 s39, s70, s72\n"
        "ds_read_b64_tr_b16 v[74:75], v9 offset:48\n"
        "v_mfma_f32_16x16x32_bf16 v[36:39], v[40:43], v[120:123], 0\n"
        "s_waitcnt lgkmcnt(1)\n"
        "s_cmp_ge_u32 s31, 20\n"
        "v_smfmac_f32_16x16x64_bf16 v[36:39], v[44:47], v[20:27], v18\n"
        "s_cselect_b32 s39, s22, s39\n"
        "ds_read_b64_tr_b16 v[94:95], v9 offset:64\n"
        "v_cvt_pk_bf16_f32 v152, v28, v32\n"
        "v_cvt_pk_bf16_f32 v156, v29, v33\n"
        "v_cvt_pk_bf16_f32 v160, v30, v34\n"
        "v_cvt_pk_bf16_f32 v164, v31, v35\n"
        "v_mfma_f32_16x16x32_bf16 v[60:63], v[40:43], v[68:71], 0\n"
        "s_cmp_ge_u32 s31, 21\n"
        "s_waitcnt lgkmcnt(1)\n"
        "v_smfmac_f32_16x16x64_bf16 v[60:63], v[44:47], v[72:79], v18 abid:1\n"
        "s_cselect_b32 s39, s23, s39\n"
        "ds_read_b64_tr_b16 v[22:23], v9 offset:80\n"
        "v_mfma_f32_16x16x32_bf16 v[56:59], v[40:43], v[116:119], 0\n"
        "s_add_u32 s39, s39, s38\n"
        "ds_read_b128 v[32:35], v2\n"
        "v_mov_b64 v[28:29], 0\n"
        "v_mov_b64 v[30:31], 0\n"
        "s_waitcnt lgkmcnt(2)\n"
        "v_smfmac_f32_16x16x64_bf16 v[56:59], v[44:47], v[92:99], v18 abid:1\n"
        "s_mul_i32 s68, s31, 3\n"
        "ds_read_b128 v[48:51], v3\n"
        "v_cvt_pk_bf16_f32 v153, v36, v60\n"
        "v_cvt_pk_bf16_f32 v157, v37, v61\n"
        "v_cvt_pk_bf16_f32 v161, v38, v62\n"
        "v_cvt_pk_bf16_f32 v165, v39, v63\n"
        "v_mfma_f32_16x16x32_bf16 v[60:63], v[40:43], v[124:127], 0\n"
        "s_lshr_b32 s38, s31, 3\n"
        "v_mov_b64 v[36:37], 0\n"
        "v_mov_b64 v[38:39], 0\n"
        "s_waitcnt lgkmcnt(2)\n"
        "v_smfmac_f32_16x16x64_bf16 v[60:63], v[44:47], v[20:27], v18 abid:1\n"
        "s_waitcnt lgkmcnt(1)\n"
        "s_add_u32 s38, s38, 11\n"
        "v_mov_b32_dpp v139, v19 row_shr:4 row_mask:0xf bank_mask:0xf bound_ctrl:0\n"
        "v_smfmac_f32_16x16x64_bf16 v[28:31], v[32:35], v[64:71], v4\n"
        "s_bitcmp1_b32 s31, 0\n"
        "v_mov_b64 v[40:41], 0\n"
        "v_mov_b64 v[42:43], 0\n"
        "s_waitcnt lgkmcnt(0)\n"
        "v_smfmac_f32_16x16x64_bf16 v[28:31], v[48:51], v[72:79], v4 abid:1\n"
        "s_cselect_b32 s38, 3, s38\n"
        "v_cvt_pk_bf16_f32 v154, v56, v60\n"
        "v_cvt_pk_bf16_f32 v158, v57, v61\n"
        "v_cvt_pk_bf16_f32 v162, v58, v62\n"
        "v_cvt_pk_bf16_f32 v166, v59, v63\n"
        "v_smfmac_f32_16x16x64_bf16 v[36:39], v[32:35], v[112:119], v4\n"
        "s_add_u32 s68, s68, s38\n"
        "v_smfmac_f32_16x16x64_bf16 v[36:39], v[48:51], v[92:99], v4 abid:1\n"
        "s_cmp_lt_u32 s31, s14\n"
        "ds_read_b64_tr_b16 v[74:75], v9 offset:3328\n"
        "v_add_f32 v28, v28, v29\n"
        "v_add_f32 v30, v30, v31\n"
        "v_smfmac_f32_16x16x64_bf16 v[40:43], v[32:35], v[120:127], v4\n"
        "s_cselect_b32 s68, s68, 0\n"
        "v_smfmac_f32_16x16x64_bf16 v[40:43], v[48:51], v[20:27], v4 abid:1\n"
        "s_add_u32 s37, s35, 0\n"
        "s_and_b32 s38, s31, 7\n"
        "ds_read_b64_tr_b16 v[94:95], v9 offset:3344\n"
        "v_add_f32 v36, v36, v37\n"
        "v_add_f32 v38, v38, v39\n"
        "v_cvt_pk_bf16_f32 v144, v28, v36\n"
        "v_mfma_f32_16x16x32_bf16 v[100:103], v[128:131], v[64:67], 0\n"
        "s_cselect_b32 s30, 1, -7\n"
        "v_add_f32 v40, v40, v41\n"
        "v_add_f32 v42, v42, v43\n"
        "v_cvt_pk_bf16_f32 v145, v40, v30\n"
        "v_cvt_pk_bf16_f32 v146, v38, v42\n"
        "s_waitcnt lgkmcnt(1)\n"
        "v_smfmac_f32_16x16x64_bf16 v[100:103], v[136:139], v[72:79], v18\n"
        "s_mul_i32 s29, s30, 192\n"
        "ds_read_b64_tr_b16 v[22:23], v9 offset:3360\n"
        "v_mfma_f32_16x16x32_bf16 v[148:151], v[128:131], v[112:115], 0\n"
        "buffer_store_dwordx3 v[152:154], v12, s[8:11], s37 offen sc0 sc1\n"
        "s_mul_i32 s30, s30, 392\n"
        "s_waitcnt lgkmcnt(1)\n"
        "v_smfmac_f32_16x16x64_bf16 v[148:151], v[136:139], v[92:99], v18\n"
        "s_add_u32 s37, s35, 0\n"
        "s_cmp_eq_u32 s73, s18\n"
        "ds_read_b64_tr_b16 v[74:75], v9 offset:3376\n"
        "v_mfma_f32_16x16x32_bf16 v[28:31], v[128:131], v[120:123], 0\n"
        "s_cselect_b32 s34, -7488, s75\n"
        "s_waitcnt lgkmcnt(1)\n"
        "v_smfmac_f32_16x16x64_bf16 v[28:31], v[136:139], v[20:27], v18\n"
        "s_add_u32 s73, s73, s34\n"
        "ds_read_b64_tr_b16 v[94:95], v9 offset:3392\n"
        "v_mov_b32_dpp v83, v19 row_shr:4 row_mask:0xf bank_mask:0xf bound_ctrl:0\n"
        "v_mfma_f32_16x16x32_bf16 v[84:87], v[128:131], v[68:71], 0\n"
        "buffer_store_dwordx3 v[156:158], v15, s[8:11], s37 offen offset:768 sc0 sc1\n"
        "s_xor_b32 s74, s74, 4\n"
        "v_writelane_b32 v10, s73, 56\n"
        "v_add_u32 v11, s30, v11\n"
        "s_mov_b64 exec, s[62:63]\n"
        "v_add_u32 v16, s29, v16\n"
        "v_add_u32 v8, s34, v8\n"
        "s_mov_b64 exec, -1\n"
        "s_waitcnt lgkmcnt(1)\n"
        "v_smfmac_f32_16x16x64_bf16 v[84:87], v[136:139], v[72:79], v18 abid:1\n"
        "s_add_u32 s37, s35, 98112\n"
        "ds_read_b64_tr_b16 v[22:23], v9 offset:3408\n"
        "v_mov_b32 v167, s74\n"
        "s_mov_b64 exec, s[50:51]\n"
        "v_add_u32 v5, s29, v5\n"
        "v_add_u32 v6, s34, v6\n"
        "s_mov_b64 exec, -1\n"
        "v_mfma_f32_16x16x32_bf16 v[44:47], v[128:131], v[116:119], 0\n"
        "s_waitcnt lgkmcnt(1)\n"
        "v_smfmac_f32_16x16x64_bf16 v[44:47], v[136:139], v[92:99], v18 abid:1\n"
        "ds_read_b64_tr_b16 v[74:75], v9 offset:6656\n"
        "v_mfma_f32_16x16x32_bf16 v[156:159], v[128:131], v[124:127], 0\n"
        "buffer_store_dwordx3 v[160:162], v12, s[8:11], s37 offen offset:192 sc0 sc1 nt\n"
        "ds_read_b32 v163, v167\n"
        "s_waitcnt lgkmcnt(2)\n"
        "v_smfmac_f32_16x16x64_bf16 v[156:159], v[136:139], v[20:27], v18 abid:1\n"
        "s_add_u32 s37, s35, 98112\n"
        "ds_read_b64_tr_b16 v[94:95], v9 offset:6672\n"
        "v_mfma_f32_16x16x32_bf16 v[48:51], v[52:55], v[64:67], 0\n"
        "s_waitcnt lgkmcnt(2)\n"
        "v_smfmac_f32_16x16x64_bf16 v[48:51], v[80:83], v[72:79], v18\n"
        "ds_read_b64_tr_b16 v[22:23], v9 offset:6688\n"
        "v_mfma_f32_16x16x32_bf16 v[40:43], v[52:55], v[112:115], 0\n"
        "buffer_store_dwordx3 v[164:166], v14, s[8:11], s37 offen offset:960 sc0 sc1 nt\n"
        "s_add_u32 s37, s35, 588672\n"
        "s_add_u32 s46, s37, 1344\n"
        "s_waitcnt lgkmcnt(1)\n"
        "v_smfmac_f32_16x16x64_bf16 v[40:43], v[80:83], v[92:99], v18\n"
        "buffer_store_dwordx3 v[144:146], v12, s[44:47], s37 offen offset:1152 sc0 sc1 nt\n"
        "ds_read_b64_tr_b16 v[74:75], v9 offset:6704\n"
        "v_readfirstlane_b32 s40, v163\n"
        "v_mfma_f32_16x16x32_bf16 v[88:91], v[52:55], v[120:123], 0\n"
        "s_cmp_lt_i32 s40, s68\n"
        "s_waitcnt lgkmcnt(1)\n"
        "v_smfmac_f32_16x16x64_bf16 v[88:91], v[80:83], v[20:27], v18\n"
        "ds_read_b64_tr_b16 v[94:95], v9 offset:6720\n"
        "v_mfma_f32_16x16x32_bf16 v[36:39], v[52:55], v[68:71], 0\n"
        "v_xor_b32 v166, 4, v167\n"
        "s_cbranch_scc1 L54_%=\n"
        "L55_%=:\n"
        "ds_read_b128 v[104:107], v7\n"
        "ds_read_b128 v[56:59], v7 offset:3328\n"
        "ds_read_b128 v[108:111], v7 offset:64\n"
        "ds_read_b128 v[60:63], v7 offset:3392\n"
        "ds_read_b128 v[152:155], v7 offset:128\n"
        "ds_read_b128 v[132:135], v7 offset:3456\n"
        "s_waitcnt lgkmcnt(7)\n"
        "v_smfmac_f32_16x16x64_bf16 v[36:39], v[80:83], v[72:79], v18 abid:1\n"
        "ds_read_b64_tr_b16 v[22:23], v9 offset:6736\n"
        "v_add_u32 v9, s69, v9\n"
        "s_mov_b64 exec, 1\n"
        "ds_inc_u32 v166, v1 offset:8\n"
        "s_mov_b64 exec, -1\n"
        "v_mfma_f32_16x16x32_bf16 v[72:75], v[52:55], v[116:119], 0\n"
        "s_waitcnt lgkmcnt(8)\n"
        "s_sub_u32 s69, 0, s69\n"
        "v_smfmac_f32_16x16x64_bf16 v[72:75], v[80:83], v[92:99], v18 abid:1\n"
        "s_mov_b32 s36, s35\n"
        "v_mfma_f32_16x16x32_bf16 v[76:79], v[52:55], v[124:127], 0\n"
        "s_mov_b32 s35, s39\n"
        "s_waitcnt lgkmcnt(1)\n"
        "v_smfmac_f32_16x16x64_bf16 v[76:79], v[80:83], v[20:27], v18 abid:1\n"
        "s_setprio 0\n"
        "s_waitcnt lgkmcnt(0)\n"
        "s_nop 15\n"
        "s_branch CNEXT_%=\n"
        :: "{v0}"(tid), "{s[0:1]}"(kap), "{s2}"(bid), "{s[4:5]}"(tab) : CLOBBERS);
}

extern "C" void kernel_launch(void* const* d_in, const int* in_sizes, int n_in, void* d_out, int out_size, void* d_ws, size_t ws_size, hipStream_t stream) {
    (void)in_sizes; (void)n_in; (void)out_size; (void)ws_size;
    (void)hipFuncSetAttribute((const void*)swin_kernel, hipFuncAttributeMaxDynamicSharedMemorySize, LDS_BYTES);
    swin_kernel<<<256, 512, LDS_BYTES, stream>>>((const char*)d_in[0], (const char*)d_in[1], (const char*)d_in[2], (const char*)d_in[3], (const char*)d_in[4], (char*)d_out, (const char*)d_ws);
}
```

```cpp
#define LTOK_ 208
#define FPAD 32
#define NPR 36
#define NWC 22
#define LK0 3
#define LK1 2
#define LK2 1
#define LK3 0
#include <hip/hip_runtime.h>
#include <hip/hip_bf16.h>

constexpr int TOKB = 192, ROWB = 511 * TOKB;
constexpr unsigned FRAMEB = 511u * ROWB;
constexpr int LTOK = LTOK_;
constexpr int LFRAME = 48 * LTOK + FPAD;
constexpr int LBUF = 4 * LFRAME;
constexpr int LSTASH = 10 * 4 * LTOK;
constexpr int LSTG = 6208;
constexpr int VST = 392, LEXT = 1536 + 8 * VST;
constexpr int WT = 3088;
constexpr int LDS_AUX = (54 - NPR) / 3 * WT, LDS_TAB = LDS_AUX + 1024, LDS_BUF = LDS_TAB + 2048, LDS_STASH = LDS_BUF + 2 * LBUF, LDS_CNT = LDS_STASH + LSTASH,
              LDS_STG = LDS_CNT + 128, LDS_SCRAP = LDS_STG + 4 * LSTG, LDS_EXT = LDS_SCRAP + 512, LDS_BYTES = 163840;
static_assert(NWC >= 18 && NPR % 3 == 0 && LDS_EXT + 4 * LEXT <= LDS_BYTES && NPR * 1024 <= LBUF, "LDS");
constexpr unsigned RAWM = 18432, RAW_T = LDS_BUF, RAW_TV = RAW_T + 2 * RAWM + 128, RAW_SV = (36 - NPR) / 3 * WT, RAW_SO = LDS_EXT, RAW_SQ = LDS_BUF + LBUF, RAW_SK = RAW_SQ + RAWM;
static_assert(RAW_SV + RAWM <= LDS_AUX && RAW_SO + RAWM <= LDS_BYTES && RAW_SK + RAWM <= LDS_STASH && RAW_TV + 2 * RAWM <= LDS_STASH && RAWM + 6 * WT <= 2 * RAWM + 128, "fold LDS");

constexpr int phi(int v) { return v ^ (v >> 1); }
struct Tab {
    unsigned img[3][64][4];
    unsigned p[4][7][64][4];
    unsigned c[4][6][64][4];
    unsigned d[4][7][64][4];
    static constexpr void job(unsigned (*q)[64][4], int lane, unsigned L, unsigned R, int mode, int tau, unsigned st0, unsigned st1) {
        const int i = lane & 15, g = lane >> 4;
        for (int a = 0; a < 2; ++a) {
            const unsigned ra = (8 * (i >> 2) + 4 * a + (i & 3)) * 192 + ((g ^ ((2 * (i >> 2) + a) & 3)) * 16);
            q[0][lane][a] = L + ra;
            q[0][lane][2 + a] = mode == 0 ? R + tau * 6144 + ra : mode == 2 ? R + tau * 6144 + (16 * a + i) * 192 + ((g ^ (i >> 2)) * 16)
                                          : R + tau * 64 + (8 * g + 4 * a + (i >> 2)) * 192 + (((i & 3) ^ ((2 * g + a) & 3)) * 16);
        }
        q[1][lane][0] = st0; q[1][lane][1] = st1; q[1][lane][2] = LDS_SCRAP + g * 32;
    }
    static constexpr unsigned scat(bool win, int lane, int tau, int tb) {
        const int i = lane & 15, c = 32 * tau + 8 * (i >> 2) + 4 * tb + (i & 3), r = c / 24, o = c % 24, q = win ? o / 6 : o & 3, t = win ? o % 6 : o >> 2;
        return (win ? RAW_SV : 3 * t < NPR - 18 ? LDS_BUF + 18 * 1024 : -(NPR - 18) / 3 * WT) + t * WT + ((lane & 48) + 4 * q + r) * 16;
    }
    constexpr Tab() : img{}, p{}, c{}, d{} {
        for (int e = 0; e < 48; ++e) img[0][e >> 2][e & 3] = 0x3f803f80u;
        img[0][12][0] = 0x7f800000u;
        for (int e = 0; e < 8; ++e) img[0][36 + 10 + e][e >> 1] = e & 1 ? 0x3f800000u : 0x3f80u;
        for (int k = 0; k < 4; ++k) for (int lane = 0; lane < 64; ++lane) {
            const int i = lane & 15, g = lane >> 4, rowv = 8 * (i >> 2) + (i & 3);
            unsigned vi[20] = {};
            {
                const int pp = i >> 2, t = i & 3;
                const int t1 = (k + 3 - LK1) & 3;
                if (t1 < 3) job(p[k], lane, RAW_TV, RAW_TV + RAWM, 1, t1, scat(false, lane, t1, 0), scat(false, lane, t1, 1));
                const unsigned wl = lane * 16, lm = pp == g ? ~0u : 0u, idp = LDS_AUX + 576 + (t == g ? 10 + pp : 0) * 16, sidx = 0xdddd;
                unsigned vA[3] = {};
                for (int m = 0; m < 3; ++m) {
                    const int n = 64 * m + lane, tf = n / 96, q = n % 96 / 12, s = (n % 12) ^ tf;
                    const unsigned cc = tf * FRAMEB + (3 * (s & 3) + (s >> 2)) * 16 + 2048 - 1024 * m;
                    vA[m] = cc + q * TOKB;
                    img[1][lane][m] = (cc + 6 * ROWB + 6 * TOKB) | ((unsigned)(2 * (q & 3) + (q >> 2)) << 28);
                }
                const unsigned vA0 = vA[0], vA1 = vA[1], vA2 = vA[2], vB0 = vA0 + (ROWB - 7 * TOKB), vB1 = vA1 + (ROWB - 7 * TOKB), vB2 = vA2 + (ROWB - 7 * TOKB);
                const unsigned dA = LDS_BUF + t * LFRAME + pp * LTOK + g * 16, rA = LDS_BUF + t * LFRAME + (pp == g ? pp : 4 + g) * LTOK + 192;
                img[1][lane][3] = (2 * pp * (4 * LTOK) + t * LTOK + g * 16) | (((pp == g ? 2 * pp : 2 * g + 1) * (4 * LTOK) + t * LTOK + 192) << 16);
                const unsigned cd0 = dA + k * 8 * LTOK, cr0 = rA + k * 8 * LTOK;
                const unsigned xr = LDS_STG + k * LSTG + (t >> 1) * 3104 + ((t & 1) * 8 + pp) * TOKB + (g ^ (t & 1)) * 16;
                const unsigned axp = LDS_AUX + (t ? 384 : 0);
                vi[0] = xr; vi[1] = lm; vi[2] = rA; vi[3] = dA; vi[4] = vA0; vi[5] = vA1; vi[6] = vA2; vi[7] = vB0; vi[8] = vB1; vi[9] = vB2; vi[10] = sidx; vi[11] = cd0; vi[12] = cr0; vi[13] = wl; vi[14] = idp; vi[15] = axp;
                for (int e = 0; e < 16; ++e) p[k][2 + (e >> 2)][lane][e & 3] = vi[e];
            }
            {
                const int t0 = (k + 3 - LK0) & 3, t2 = (k + 3 - LK2) & 3, t3 = (k + 3 - LK3) & 3;
                const unsigned s0 = LDS_BUF + t0 * 6144 + lane * 16, s3 = LDS_BUF + LBUF + t3 * 6144 + lane * 16;
                if (t0 < 3) job(c[k], lane, RAW_T, RAW_T + RAWM, 2, t0, s0, s0 + 3072); else job(c[k], lane, RAW_T, RAW_T + RAWM, 1, 0, LDS_SCRAP + rowv * 4, 0);
                if (t2 < 3) job(c[k] + 4, lane, RAW_SV, RAW_SO, 1, t2, scat(true, lane, t2, 0), scat(true, lane, t2, 1));
                if (t3 < 3) job(d[k], lane, RAW_SQ, RAW_SK, 0, t3, s3, s3 + 3072); else job(d[k], lane, RAW_SQ, RAW_SK, 1, 0, LDS_SCRAP + rowv * 4, 0);
                for (int m = 0; m < 3; ++m) {
                    const int n = 64 * m + lane, row = n / 12, pc = ((n % 12) ^ ((row >> 2) & 3)) * 16;
                    c[k][3][lane][m] = (24 * (row & 3) + 12 * ((row >> 2) & 1) + 2 * (row >> 3)) * 576 + pc;
                    c[k][2][lane][m] = row * 192 + pc;
                }
                const int dg = 16 * (2 * (g & 1) + (g >> 1)), pix = 4 * (i & 3) + phi(i >> 2);
                const unsigned wl = lane * 16, fr = LDS_BUF + k * LFRAME, ext = LDS_EXT + k * LEXT;
                const unsigned ta0 = fr + pix * LTOK + dg, zr = LDS_AUX + 384 + g * 16, t30 = i & 3 ? zr : LDS_STASH + k * LTOK + dg;
                const unsigned xa0 = fr + (4 * (i >> 2) + phi(g)) * LTOK + 96 * (i & 1) + 8 * ((i >> 1) & 1);
                const int sg = 8 * ((i >> 1) & 1) + 4 * (i >> 3) + 2 * ((i >> 2) & 1) + (i & 1);
                const unsigned tv0 = fr + (32 + 4 * (sg & 3) + phi(sg >> 2)) * LTOK + dg, c1f = (i >> 2) == 2 * g ? 0x3f800000u : 0u;
                const unsigned sidx = g < 2 ? 0x0e0e0404u | ((i >> 2) == 2 * g ? (i & 3) * 0x00100010u : (i >> 2) == 2 * g + 1 ? (i & 3) * 0x40004000u : 0u) : 0xeeee4444u;
                const unsigned ga0 = phi(g) * TOKB + 48 * (i & 3) + 12 * (i >> 2), wr = ROWB - 7 * TOKB;
                const unsigned ga1 = ga0 + (phi(g) >= 1 ? wr : 0), ga2 = ga0 + (phi(g) >= 2 ? wr : 0), ga3 = ga0 + (phi(g) >= 3 ? wr : 0);
                const unsigned pb = LDS_SCRAP + k * 128, pr0 = i < 4 && (i >> 1) == (g >> 1) ? pb + (i & 1) * 32 + (g & 1) * 16 : zr, pr1 = i == 0 || i == 2 ? pb + 64 + g * 16 : zr;
                const unsigned pw = g < 2 ? pb + ((i >> 1) & 1) * 32 + (i >> 3) * 16 + ((i >> 2) & 1) * 8 + g * 4 + (i & 1) * 2
                                          : pb + 64 + (i >> 2) * 16 + (g & 1) * 4 + ((i >> 1) & 1) * 8 + (i & 1) * 2 + (lane == 57 ? 6 : 0);
                const unsigned six2 = (i & 1 ? 0xeeeeu : 0x4444u) | ((i & 2 ? 0xeeee0000u : 0x44440000u) & (g < 2 ? 0x0f0f0000u : 0xffff0000u));
                const unsigned exs0 = i & 2 ? LDS_AUX + g * 16 : ext + (i & 1) * 64 + g * 16, t3s0 = i & 2 ? zr : LDS_STASH + k * LTOK + 64 * (g >> 1) + 16 * (g & 1) + 4 * (i & 1);
                const unsigned ex0 = i & 1 ? zr : i & 2 ? LDS_AUX + g * 16 : ext + g * 16, exv0 = ext + 1536 + i * 24;
                const unsigned rla0 = lane < 48 ? fr + (16 * g + pix) * LTOK + 192 : lane == 56 ? LDS_STASH + k * LTOK + 192 : LDS_AUX + 192;
                img[2][lane][0] = ((i & 7) * (4 * LTOK) + dg) | ((((4 * g + (i & 3)) & 7) * (4 * LTOK) + 96 * ((i >> 2) & 1) + 8 * (i >> 3)) << 16);
                img[2][lane][1] = ((i & 7) * 192u + g * 16) | ((1536u + i * 24 + (g & 1) * 4 * VST) << 16);
                img[2][lane][2] = ((fr + phi(g) * LTOK + 192 - xa0) & 0xffffu) | ((fr + pix * LTOK + 192 - xa0) << 16);
                img[2][lane][3] = (LDS_AUX + 576 + (10 + (i & 3)) * 16) | ((1536u + (i & 7) * VST + 384) << 16);
                vi[0] = wl; vi[1] = pw; vi[2] = pr0; vi[3] = pr1; vi[4] = six2; vi[5] = exs0; vi[6] = t3s0; vi[7] = ta0; vi[8] = t30; vi[9] = xa0; vi[10] = rla0; vi[11] = exv0; vi[12] = ga0; vi[13] = ga1; vi[14] = ga2; vi[15] = ga3; vi[16] = ex0; vi[17] = tv0; vi[18] = sidx; vi[19] = c1f;
                for (int e = 0; e < 20; ++e) d[k][2 + (e >> 2)][lane][e & 3] = vi[e];
            }
        }
    }
};
static __device__ const Tab TAB __attribute__((aligned(128))) = Tab();

#define CLOBBERS "v1", "v2", "v3", "v4", "v5", "v6", "v7", "v8", "v9", "v10", "v11", "v12", "v13", "v14", "v15", "v16", "v17", "v18", "v19", "v20", "v21", "v22", "v23", "v24", "v25", "v26", "v27", "v28", "v29", "v30", "v31", "v32", "v33", "v34", "v35", "v36", "v37", "v38", "v39", "v40", "v41", "v42", "v43", "v44", "v45", "v46", "v47", "v48", "v49", "v50", "v51", "v52", "v53", "v54", "v55", "v56", "v57", "v58", "v59", "v60", "v61", "v62", "v63", "v64", "v65", "v66", "v67", "v68", "v69", "v70", "v71", "v72", "v73", "v74", "v75", "v76", "v77", "v78", "v79", "v80", "v81", "v82", "v83", "v84", "v85", "v86", "v87", "v88", "v89", "v90", "v91", "v92", "v93", "v94", "v95", "v96", "v97", "v98", "v99", "v100", "v101", "v102", "v103", "v104", "v105", "v106", "v107", "v108", "v109", "v110", "v111", "v112", "v113", "v114", "v115", "v116", "v117", "v118", "v119", "v120", "v121", "v122", "v123", "v124", "v125", "v126", "v127", "v128", "v129", "v130", "v131", "v132", "v133", "v134", "v135", "v136", "v137", "v138", "v139", "v140", "v141", "v142", "v143", "v144", "v145", "v146", "v147", "v148", "v149", "v150", "v151", "v152", "v153", "v154", "v155", "v156", "v157", "v158", "v159", "v160", "v161", "v162", "v163", "v164", "v165", "v166", "v167", "v168", "v169", "v170", "v171", "v172", "v173", "v174", "v175", "v176", "v177", "v178", "v179", "v180", "v181", "v182", "v183", "v184", "v185", "v186", "v187", "v188", "v189", "v190", "v191", "v192", "v193", "v194", "v195", "v196", "v197", "v198", "v199", "v200", "v201", "v202", "v203", "v204", "v205", "v206", "v207", "v208", "v209", "v210", "v211", "v212", "v213", "v214", "v215", "v216", "v217", "v218", "v219", "v220", "v221", "v222", "v223", "v224", "v225", "v226", "v227", "v228", "v229", "v230", "v231", "v232", "v233", "v234", "v235", "v236", "v237", "v238", "v239", "v240", "v241", "v242", "v243", "v244", "v245", "v246", "v247", "v248", "v249", "v250", "v251", "v252", "v253", "v254", "v255", "s3", "s6", "s7", "s8", "s9", "s10", "s11", "s12", "s13", "s14", "s15", "s16", "s17", "s18", "s19", "s20", "s21", "s22", "s23", "s24", "s25", "s26", "s27", "s28", "s29", "s30", "s31", "s34", "s35", "s36", "s37", "s38", "s39", "s40", "s41", "s42", "s43", "s44", "s45", "s46", "s47", "s48", "s49", "s50", "s51", "s52", "s53", "s54", "s55", "s56", "s57", "s58", "s59", "s60", "s61", "s62", "s63", "s64", "s65", "s66", "s67", "s68", "s69", "s70", "s71", "s72", "s73", "s74", "s75", "s76", "s77", "vcc", "scc", "memory"
__global__ __launch_bounds__(512, 2) void swin_kernel(const char* x, const char* wqkv_t, const char* wo_t, const char* wqkv_s, const char* wo_s, char* out, const char* ws) {
    const unsigned long long kap = (unsigned long long)__builtin_amdgcn_kernarg_segment_ptr(), tab = (unsigned long long)&TAB;
    const unsigned tid = threadIdx.x, bid = blockIdx.x;
    asm volatile(
        "s_load_dwordx8 s[44:51], s[0:1], 0x0\n"
        "s_load_dwordx4 s[52:55], s[0:1], 0x20\n"
        "s_load_dwordx2 s[56:57], s[0:1], 0x30\n"
        "v_readfirstlane_b32 s3, v0\n"
        "v_and_b32 v56, 63, v0\n"
        "v_lshlrev_b32 v57, 4, v56\n"
        "s_bfe_u32 s3, s3, 0x30006\n"
        "s_and_b32 s6, s3, 3\n"
        "s_mul_i32 s64, s3, 9216\n"
        "s_add_u32 s64, s64, 21600\n"
        "s_mov_b32 s27, 0x20000\n"
        "s_cmp_lt_u32 s3, 4\n"
        "s_cbranch_scc0 PPRO_%=\n"
        "s_mul_i32 s7, s6, 6144\n"
        "s_add_u32 s7, s7, 33792\n"
        "s_add_u32 s22, s4, s7\n"
        "s_addc_u32 s23, s5, 0\n"
        "global_load_dwordx4 v[240:243], v57, s[22:23] offset:-2048\n"
        "global_load_dwordx4 v[244:247], v57, s[22:23] offset:-1024\n"
        "global_load_dwordx4 v[36:39], v57, s[22:23] offset:1024\n"
        "global_load_dwordx4 v[48:51], v57, s[22:23] offset:2048\n"
        "global_load_dwordx4 v[52:55], v57, s[22:23] offset:3072\n"
        "s_mov_b32 s65, 13824\n"
        "v_add_u32 v24, 0, v56\n"
        "v_mul_u32_u24 v21, 171, v24\n"
        "v_lshrrev_b32 v21, 11, v21\n"
        "v_mad_i32_i24 v24, v21, -12, v24\n"
        "v_bfe_u32 v58, v21, 2, 2\n"
        "v_xor_b32 v58, v24, v58\n"
        "v_and_b32 v27, 7, v21\n"
        "v_mul_u32_u24 v27, 576, v27\n"
        "v_lshl_add_u32 v58, v58, 4, v27\n"
        "v_add_u32 v25, 64, v56\n"
        "v_mul_u32_u24 v22, 171, v25\n"
        "v_lshrrev_b32 v22, 11, v22\n"
        "v_mad_i32_i24 v25, v22, -12, v25\n"
        "v_bfe_u32 v59, v22, 2, 2\n"
        "v_xor_b32 v59, v25, v59\n"
        "v_and_b32 v27, 7, v22\n"
        "v_mul_u32_u24 v27, 576, v27\n"
        "v_lshl_add_u32 v59, v59, 4, v27\n"
        "v_lshrrev_b32 v27, 3, v22\n"
        "v_mad_u32_u24 v59, v27, s65, v59\n"
        "v_add_u32 v26, 128, v56\n"
        "v_mul_u32_u24 v23, 171, v26\n"
        "v_lshrrev_b32 v23, 11, v23\n"
        "v_mad_i32_i24 v26, v23, -12, v26\n"
        "v_bfe_u32 v60, v23, 2, 2\n"
        "v_xor_b32 v60, v26, v60\n"
        "v_and_b32 v27, 7, v23\n"
        "v_mul_u32_u24 v27, 576, v27\n"
        "v_lshl_add_u32 v60, v60, 4, v27\n"
        "v_add_u32 v60, s65, v60\n"
        "s_lshr_b32 s61, s6, 1\n"
        "s_mul_i32 s61, s61, 192\n"
        "s_mov_b32 s62, 27648\n"
        "s_mov_b32 s63, 4608\n"
        "s_add_u32 s7, s62, s63\n"
        "s_lshl_b32 s29, s63, 1\n"
        "s_add_u32 s60, s7, s63\n"
        "s_bitcmp1_b32 s6, 0\n"
        "s_cselect_b32 s28, s7, 0\n"
        "s_cselect_b32 s29, s29, s62\n"
        "s_cselect_b32 s60, s60, s63\n"
        "s_add_u32 s28, s28, s61\n"
        "s_add_u32 s29, s29, s61\n"
        "s_add_u32 s60, s60, s61\n"
        "s_mov_b32 s26, 55296\n"
        "s_waitcnt lgkmcnt(0)\n"
        "s_mov_b32 s24, s46\n"
        "s_and_b32 s25, s47, 0xffff\n"
        "s_add_u32 m0, s64, 0\n"
        "s_nop 0\n"
        "buffer_load_dwordx4 v58, s[24:27], s28 offen lds\n"
        "s_add_u32 m0, s64, 1024\n"
        "s_nop 0\n"
        "buffer_load_dwordx4 v59, s[24:27], s28 offen lds\n"
        "s_add_u32 m0, s64, 2048\n"
        "s_nop 0\n"
        "buffer_load_dwordx4 v60, s[24:27], s28 offen lds\n"
        "s_add_u32 m0, s64, 3072\n"
        "s_nop 0\n"
        "buffer_load_dwordx4 v58, s[24:27], s29 offen lds\n"
        "s_add_u32 m0, s64, 4096\n"
        "s_nop 0\n"
        "buffer_load_dwordx4 v59, s[24:27], s29 offen lds\n"
        "s_add_u32 m0, s64, 5120\n"
        "s_nop 0\n"
        "buffer_load_dwordx4 v60, s[24:27], s29 offen lds\n"
        "s_add_u32 m0, s64, 6144\n"
        "s_nop 0\n"
        "buffer_load_dwordx4 v58, s[24:27], s60 offen lds\n"
        "s_add_u32 m0, s64, 7168\n"
        "s_nop 0\n"
        "buffer_load_dwordx4 v59, s[24:27], s60 offen lds\n"
        "s_add_u32 m0, s64, 8192\n"
        "s_nop 0\n"
        "buffer_load_dwordx4 v60, s[24:27], s60 offen lds\n"
        "s_barrier\n"
        "s_setprio 1\n"
        "v_mov_b32 v250, 110048\n"
        "s_mov_b32 s41, 0x400000\n"
        "s_and_b32 s7, s2, 7\n"
        "s_lshl_b32 s7, s7, 5\n"
        "s_lshr_b32 s14, s2, 3\n"
        "s_add_u32 s7, s7, s14\n"
        "s_mul_i32 s7, s7, 20\n"
        "s_mov_b32 s14, 22\n"
        "s_mul_i32 s15, s7, 57457\n"
        "s_lshr_b32 s15, s15, 22\n"
        "s_mul_i32 s16, s15, 73\n"
        "s_sub_u32 s16, s7, s16\n"
        "s_mul_i32 s15, s15, 686784\n"
        "s_mul_i32 s7, s16, 1344\n"
        "s_add_u32 s15, s15, s7\n"
        "s_mov_b32 s8, s54\n"
        "s_and_b32 s9, s55, 0xffff\n"
        "s_mov_b32 s10, 200540928\n"
        "s_mov_b32 s11, 0x20000\n"
        "s_mov_b64 s[12:13], s[54:55]\n"
        "s_mul_i32 s17, s6, 50135232\n"
        "s_mul_i32 s18, s6, 208\n"
        "s_add_u32 s18, s18, 109408\n"
        "s_mul_i32 s19, s6, 4672\n"
        "s_add_u32 s19, s19, 135520\n"
        "s_mul_i32 s7, s6, 7168\n"
        "s_add_u32 s7, s7, 59392\n"
        "s_add_u32 s22, s4, s7\n"
        "s_addc_u32 s23, s5, 0\n"
        "s_waitcnt vmcnt(0)\n"
        "s_mov_b64 exec, 1\n"
        "ds_inc_u32 v250, v250 offset:16\n"
        "s_mov_b64 exec, -1\n"
        "L65_%=:\n"
        "ds_read_b32 v251, v250 offset:16\n"
        "s_waitcnt lgkmcnt(0)\n"
        "v_readfirstlane_b32 s40, v251\n"
        "s_nop 3\n"
        "s_cmp_ge_i32 s40, 4\n"
        "s_cbranch_scc1 L66_%=\n"
        "s_nop 0\n"
        "s_sub_u32 s41, s41, 1\n"
        "s_cbranch_scc0 L65_%=\n"
        "s_endpgm\n"
        "L66_%=:\n"
        "s_cmp_eq_u32 s6, 3\n"
        "s_cbranch_scc1 L67_%=\n"
        "s_mov_b32 s34, 0x4162ccfd\n"
        "s_mov_b32 s35, 110072\n"
        "s_mov_b32 s36, 110072\n"
        "s_mov_b32 s37, 110076\n"
        "s_call_b64 s[30:31], FOLDM_%=\n"
        "s_branch L68_%=\n"
        "L67_%=:\n"
        "s_mov_b32 s34, 0xbe173353\n"
        "s_mov_b32 s35, 110104\n"
        "s_mov_b32 s36, 110104\n"
        "s_mov_b32 s37, 110072\n"
        "s_call_b64 s[30:31], FOLDR_%=\n"
        "s_cmp_lg_u32 s2, 0\n"
        "s_cbranch_scc1 L64_%=\n"
        "s_mov_b64 s[24:25], s[56:57]\n"
        "s_mov_b64 exec, 1\n"
        "v_mov_b32 v27, 0\n"
        "v_mov_b32 v28, 0\n"
        "global_atomic_swap v252, v27, v28, s[24:25] offset:0 sc0\n"
        "s_mov_b64 exec, -1\n"
        "s_waitcnt vmcnt(0)\n"
        "s_mov_b64 exec, 1\n"
        "v_mov_b32 v27, 0\n"
        "v_mov_b32 v28, 1\n"
        "global_atomic_add v252, v27, v28, s[24:25] offset:256 sc0\n"
        "s_mov_b64 exec, -1\n"
        "L64_%=:\n"
        "L68_%=:\n"
        "s_setprio 3\n"
        "s_waitcnt vmcnt(0)\n"
        "v_mov_b64 v[240:241], v[48:49]\n"
        "v_mov_b64 v[242:243], v[50:51]\n"
        "v_mov_b64 v[244:245], v[52:53]\n"
        "v_mov_b64 v[246:247], v[54:55]\n"
        "L69_%=:\n"
        "ds_read_b32 v251, v250 offset:32\n"
        "s_waitcnt lgkmcnt(0)\n"
        "v_readfirstlane_b32 s40, v251\n"
        "s_nop 3\n"
        "s_cmp_ge_i32 s40, 4\n"
        "s_cbranch_scc1 L70_%=\n"
        "s_nop 0\n"
        "s_sub_u32 s41, s41, 1\n"
        "s_cbranch_scc0 L69_%=\n"
        "s_endpgm\n"
        "L70_%=:\n"
        "s_cmp_eq_u32 s6, 1\n"
        "s_cbranch_scc1 L71_%=\n"
        "s_mov_b32 s34, 0x411cc471\n"
        "s_mov_b32 s35, 110088\n"
        "s_mov_b32 s36, 110088\n"
        "s_mov_b32 s37, 110100\n"
        "s_call_b64 s[30:31], FOLDW_%=\n"
        "s_branch L72_%=\n"
        "L71_%=:\n"
        "s_mov_b64 exec, 1\n"
        "ds_inc_u32 v250, v250 offset:40\n"
        "s_mov_b64 exec, -1\n"
        "s_mov_b32 s24, s50\n"
        "s_and_b32 s25, s51, 0xffff\n"
        "s_mov_b32 s26, 55296\n"
        "s_mov_b32 m0, 61664\n"
        "s_mov_b32 s28, 0\n"
        "buffer_load_dwordx4 v36, s[24:27], s28 offen lds\n"
        "s_mov_b32 m0, 62688\n"
        "s_nop 0\n"
        "buffer_load_dwordx4 v37, s[24:27], s28 offen lds\n"
        "s_mov_b32 m0, 63712\n"
        "s_nop 0\n"
        "buffer_load_dwordx4 v38, s[24:27], s28 offen lds\n"
        "s_mov_b32 m0, 64736\n"
        "s_mov_b32 s28, 576\n"
        "buffer_load_dwordx4 v36, s[24:27], s28 offen lds\n"
        "s_mov_b32 m0, 65760\n"
        "s_nop 0\n"
        "buffer_load_dwordx4 v37, s[24:27], s28 offen lds\n"
        "s_mov_b32 m0, 66784\n"
        "s_nop 0\n"
        "buffer_load_dwordx4 v38, s[24:27], s28 offen lds\n"
        "s_mov_b32 m0, 67808\n"
        "s_mov_b32 s28, 2304\n"
        "buffer_load_dwordx4 v36, s[24:27], s28 offen lds\n"
        "s_mov_b32 m0, 68832\n"
        "s_nop 0\n"
        "buffer_load_dwordx4 v37, s[24:27], s28 offen lds\n"
        "s_mov_b32 m0, 69856\n"
        "s_nop 0\n"
        "buffer_load_dwordx4 v38, s[24:27], s28 offen lds\n"
        "s_mov_b32 m0, 70880\n"
        "s_mov_b32 s28, 2880\n"
        "buffer_load_dwordx4 v36, s[24:27], s28 offen lds\n"
        "s_mov_b32 m0, 71904\n"
        "s_nop 0\n"
        "buffer_load_dwordx4 v37, s[24:27], s28 offen lds\n"
        "s_mov_b32 m0, 72928\n"
        "s_nop 0\n"
        "buffer_load_dwordx4 v38, s[24:27], s28 offen lds\n"
        "s_mov_b32 m0, 73952\n"
        "s_mov_b32 s28, 4608\n"
        "buffer_load_dwordx4 v36, s[24:27], s28 offen lds\n"
        "s_mov_b32 m0, 74976\n"
        "s_nop 0\n"
        "buffer_load_dwordx4 v37, s[24:27], s28 offen lds\n"
        "s_mov_b32 m0, 76000\n"
        "s_nop 0\n"
        "buffer_load_dwordx4 v38, s[24:27], s28 offen lds\n"
        "s_mov_b32 m0, 77024\n"
        "s_mov_b32 s28, 5184\n"
        "buffer_load_dwordx4 v36, s[24:27], s28 offen lds\n"
        "s_mov_b32 m0, 78048\n"
        "s_nop 0\n"
        "buffer_load_dwordx4 v37, s[24:27], s28 offen lds\n"
        "s_mov_b32 m0, 79072\n"
        "s_nop 0\n"
        "buffer_load_dwordx4 v38, s[24:27], s28 offen lds\n"
        "s_mov_b32 s24, s50\n"
        "s_and_b32 s25, s51, 0xffff\n"
        "s_mov_b32 s26, 55296\n"
        "s_mov_b32 m0, 80096\n"
        "s_mov_b32 s28, 192\n"
        "buffer_load_dwordx4 v36, s[24:27], s28 offen lds\n"
        "s_mov_b32 m0, 81120\n"
        "s_nop 0\n"
        "buffer_load_dwordx4 v37, s[24:27], s28 offen lds\n"
        "s_mov_b32 m0, 82144\n"
        "s_nop 0\n"
        "buffer_load_dwordx4 v38, s[24:27], s28 offen lds\n"
        "s_mov_b32 m0, 83168\n"
        "s_mov_b32 s28, 768\n"
        "buffer_load_dwordx4 v36, s[24:27], s28 offen lds\n"
        "s_mov_b32 m0, 84192\n"
        "s_nop 0\n"
        "buffer_load_dwordx4 v37, s[24:27], s28 offen lds\n"
        "s_mov_b32 m0, 85216\n"
        "s_nop 0\n"
        "buffer_load_dwordx4 v38, s[24:27], s28 offen lds\n"
        "s_mov_b32 m0, 86240\n"
        "s_mov_b32 s28, 2496\n"
        "buffer_load_dwordx4 v36, s[24:27], s28 offen lds\n"
        "s_mov_b32 m0, 87264\n"
        "s_nop 0\n"
        "buffer_load_dwordx4 v37, s[24:27], s28 offen lds\n"
        "s_mov_b32 m0, 88288\n"
        "s_nop 0\n"
        "buffer_load_dwordx4 v38, s[24:27], s28 offen lds\n"
        "s_mov_b32 m0, 89312\n"
        "s_mov_b32 s28, 3072\n"
        "buffer_load_dwordx4 v36, s[24:27], s28 offen lds\n"
        "s_mov_b32 m0, 90336\n"
        "s_nop 0\n"
        "buffer_load_dwordx4 v37, s[24:27], s28 offen lds\n"
        "s_mov_b32 m0, 91360\n"
        "s_nop 0\n"
        "buffer_load_dwordx4 v38, s[24:27], s28 offen lds\n"
        "s_mov_b32 m0, 92384\n"
        "s_mov_b32 s28, 4800\n"
        "buffer_load_dwordx4 v36, s[24:27], s28 offen lds\n"
        "s_mov_b32 m0, 93408\n"
        "s_nop 0\n"
        "buffer_load_dwordx4 v37, s[24:27], s28 offen lds\n"
        "s_mov_b32 m0, 94432\n"
        "s_nop 0\n"
        "buffer_load_dwordx4 v38, s[24:27], s28 offen lds\n"
        "s_mov_b32 m0, 95456\n"
        "s_mov_b32 s28, 5376\n"
        "buffer_load_dwordx4 v36, s[24:27], s28 offen lds\n"
        "s_mov_b32 m0, 96480\n"
        "s_nop 0\n"
        "buffer_load_dwordx4 v37, s[24:27], s28 offen lds\n"
        "s_mov_b32 m0, 97504\n"
        "s_nop 0\n"
        "buffer_load_dwordx4 v38, s[24:27], s28 offen lds\n"
        "global_load_dwordx4 v[40:43], v57, s[22:23] offset:-3072\n"
        "global_load_dwordx4 v[44:47], v57, s[22:23] offset:-2048\n"
        "global_load_dwordx4 v[0:3], v57, s[22:23] offset:-1024\n"
        "global_load_dwordx4 v[4:7], v57, s[22:23] offset:0\n"
        "global_load_dwordx4 v[8:11], v57, s[22:23] offset:1024\n"
        "global_load_dwordx4 v[12:15], v57, s[22:23] offset:2048\n"
        "global_load_dwordx4 v[16:19], v57, s[22:23] offset:3072\n"
        "L72_%=:\n"
        "s_waitcnt vmcnt(0)\n"
        "s_mov_b64 exec, 1\n"
        "ds_inc_u32 v250, v250 offset:44\n"
        "s_mov_b64 exec, -1\n"
        "v_mov_b64 v[240:241], v[40:41]\n"
        "v_mov_b64 v[242:243], v[42:43]\n"
        "v_mov_b64 v[244:245], v[44:45]\n"
        "v_mov_b64 v[246:247], v[46:47]\n"
        "L73_%=:\n"
        "ds_read_b32 v251, v250 offset:44\n"
        "s_waitcnt lgkmcnt(0)\n"
        "v_readfirstlane_b32 s40, v251\n"
        "s_nop 3\n"
        "s_cmp_ge_i32 s40, 4\n"
        "s_cbranch_scc1 L74_%=\n"
        "s_nop 0\n"
        "s_sub_u32 s41, s41, 1\n"
        "s_cbranch_scc0 L73_%=\n"
        "s_endpgm\n"
        "L74_%=:\n"
        "s_cmp_eq_u32 s6, 0\n"
        "s_cbranch_scc1 L75_%=\n"
        "s_mov_b32 s34, 0x41622adf\n"
        "s_mov_b32 s35, 110096\n"
        "s_mov_b32 s36, 110096\n"
        "s_mov_b32 s37, 110100\n"
        "s_call_b64 s[30:31], FOLDM_%=\n"
        "s_branch L76_%=\n"
        "L75_%=:\n"
        "s_mov_b32 s34, 0xbe16c740\n"
        "s_mov_b32 s35, 110104\n"
        "s_mov_b32 s36, 110104\n"
        "s_mov_b32 s37, 110096\n"
        "s_call_b64 s[30:31], FOLDR_%=\n"
        "L76_%=:\n"
        "s_branch CONS_%=\n"
        "FOLDM_%=:\n"
        "ds_read_b128 v[88:91], v242\n"
        "ds_read_b128 v[92:95], v240\n"
        "ds_read_b128 v[180:183], v242 offset:64\n"
        "ds_read_b128 v[172:175], v240 offset:64\n"
        "ds_read_b128 v[184:187], v242 offset:128\n"
        "ds_read_b128 v[164:167], v240 offset:128\n"
        "ds_read_b128 v[168:171], v241\n"
        "ds_read_b128 v[100:103], v241 offset:64\n"
        "ds_read_b128 v[104:107], v241 offset:128\n"
        "ds_read_b128 v[140:143], v240 offset:6144\n"
        "ds_read_b128 v[132:135], v240 offset:6208\n"
        "ds_read_b128 v[96:99], v240 offset:6272\n"
        "ds_read_b128 v[136:139], v241 offset:6144\n"
        "ds_read_b128 v[128:131], v241 offset:6208\n"
        "ds_read_b128 v[108:111], v241 offset:6272\n"
        "s_waitcnt lgkmcnt(13)\n"
        "v_mfma_f32_16x16x32_bf16 v[64:67], v[92:95], v[88:91], 0\n"
        "s_waitcnt lgkmcnt(11)\n"
        "ds_read_b128 v[152:155], v240 offset:12288\n"
        "v_mfma_f32_16x16x32_bf16 v[64:67], v[172:175], v[180:183], v[64:67]\n"
        "s_waitcnt lgkmcnt(8)\n"
        "ds_read_b128 v[160:163], v240 offset:12352\n"
        "v_mfma_f32_16x16x32_bf16 v[64:67], v[164:167], v[184:187], v[64:67]\n"
        "ds_read_b128 v[112:115], v240 offset:12416\n"
        "v_mfma_f32_16x16x32_bf16 v[68:71], v[168:171], v[88:91], 0\n"
        "ds_read_b128 v[116:119], v241 offset:12288\n"
        "v_mfma_f32_16x16x32_bf16 v[68:71], v[100:103], v[180:183], v[68:71]\n"
        "s_waitcnt lgkmcnt(4)\n"
        "ds_read_b128 v[120:123], v241 offset:12352\n"
        "v_mfma_f32_16x16x32_bf16 v[68:71], v[104:107], v[184:187], v[68:71]\n"
        "ds_read_b128 v[124:127], v241 offset:12416\n"
        "v_mfma_f32_16x16x32_bf16 v[72:75], v[140:143], v[88:91], 0\n"
        "ds_read_b128 v[144:147], v243\n"
        "v_mfma_f32_16x16x32_bf16 v[72:75], v[132:135], v[180:183], v[72:75]\n"
        "ds_read_b128 v[148:151], v243 offset:64\n"
        "v_mfma_f32_16x16x32_bf16 v[72:75], v[96:99], v[184:187], v[72:75]\n"
        "ds_read_b128 v[156:159], v243 offset:128\n"
        "v_mfma_f32_16x16x32_bf16 v[76:79], v[136:139], v[88:91], 0\n"
        "v_add_f32 v176, v64, v68\n"
        "v_add_f32 v177, v65, v69\n"
        "v_add_f32 v178, v66, v70\n"
        "v_add_f32 v179, v67, v71\n"
        "v_mfma_f32_16x16x32_bf16 v[76:79], v[128:131], v[180:183], v[76:79]\n"
        "v_mfma_f32_16x16x32_bf16 v[76:79], v[108:111], v[184:187], v[76:79]\n"
        "s_waitcnt lgkmcnt(3)\n"
        "v_add_f32 v176, v176, v72\n"
        "v_add_f32 v177, v177, v73\n"
        "v_add_f32 v178, v178, v74\n"
        "v_add_f32 v179, v179, v75\n"
        "v_mfma_f32_16x16x32_bf16 v[80:83], v[152:155], v[88:91], 0\n"
        "v_mfma_f32_16x16x32_bf16 v[80:83], v[160:163], v[180:183], v[80:83]\n"
        "v_mfma_f32_16x16x32_bf16 v[80:83], v[112:115], v[184:187], v[80:83]\n"
        "v_add_f32 v176, v176, v76\n"
        "v_add_f32 v177, v177, v77\n"
        "v_add_f32 v178, v178, v78\n"
        "v_add_f32 v179, v179, v79\n"
        "v_mfma_f32_16x16x32_bf16 v[84:87], v[116:119], v[88:91], 0\n"
        "v_mfma_f32_16x16x32_bf16 v[84:87], v[120:123], v[180:183], v[84:87]\n"
        "v_mfma_f32_16x16x32_bf16 v[84:87], v[124:127], v[184:187], v[84:87]\n"
        "s_waitcnt lgkmcnt(0)\n"
        "v_add_f32 v176, v176, v80\n"
        "v_add_f32 v177, v177, v81\n"
        "v_add_f32 v178, v178, v82\n"
        "v_add_f32 v179, v179, v83\n"
        "v_mov_b32 v239, s35\n"
        "s_waitcnt vmcnt(6)\n"
        "s_mov_b64 exec, 1\n"
        "ds_inc_u32 v239, v239 offset:0\n"
        "s_mov_b64 exec, -1\n"
        "v_add_f32 v176, v176, v84\n"
        "v_add_f32 v177, v177, v85\n"
        "v_add_f32 v178, v178, v86\n"
        "v_add_f32 v179, v179, v87\n"
        "v_add_f32 v176, v176, v177\n"
        "v_add_f32 v178, v178, v179\n"
        "v_add_f32 v176, v176, v178\n"
        "v_mov_b32 v177, v176\n"
        "v_mfma_f32_16x16x32_bf16 v[88:91], v[92:95], v[144:147], 0\n"
        "v_mfma_f32_16x16x32_bf16 v[88:91], v[172:175], v[148:151], v[88:91]\n"
        "v_permlane32_swap_b32 v176, v177\n"
        "v_add_f32 v176, v176, v177\n"
        "v_mov_b32 v177, v176\n"
        "v_mfma_f32_16x16x32_bf16 v[88:91], v[164:167], v[156:159], v[88:91]\n"
        "v_mfma_f32_16x16x32_bf16 v[92:95], v[168:171], v[144:147], 0\n"
        "v_permlane16_swap_b32 v176, v177\n"
        "v_add_f32 v176, v176, v177\n"
        "v_mul_f32 v176, 0xbc2aaaab, v176\n"
        "v_mul_f32 v176, s34, v176\n"
        "v_fma_f32 v64, v64, s34, v176\n"
        "v_fma_f32 v65, v65, s34, v176\n"
        "v_fma_f32 v66, v66, s34, v176\n"
        "v_fma_f32 v67, v67, s34, v176\n"
        "v_fma_f32 v68, v68, s34, v176\n"
        "v_fma_f32 v69, v69, s34, v176\n"
        "v_fma_f32 v70, v70, s34, v176\n"
        "v_fma_f32 v71, v71, s34, v176\n"
        "v_fma_f32 v72, v72, s34, v176\n"
        "v_fma_f32 v73, v73, s34, v176\n"
        "v_fma_f32 v74, v74, s34, v176\n"
        "v_fma_f32 v75, v75, s34, v176\n"
        "v_fma_f32 v76, v76, s34, v176\n"
        "v_fma_f32 v77, v77, s34, v176\n"
        "v_fma_f32 v78, v78, s34, v176\n"
        "v_fma_f32 v79, v79, s34, v176\n"
        "v_fma_f32 v80, v80, s34, v176\n"
        "v_fma_f32 v81, v81, s34, v176\n"
        "v_fma_f32 v82, v82, s34, v176\n"
        "v_fma_f32 v83, v83, s34, v176\n"
        "v_fma_f32 v84, v84, s34, v176\n"
        "v_fma_f32 v85, v85, s34, v176\n"
        "v_fma_f32 v86, v86, s34, v176\n"
        "v_fma_f32 v87, v87, s34, v176\n"
        "v_mfma_f32_16x16x32_bf16 v[92:95], v[100:103], v[148:151], v[92:95]\n"
        "v_mfma_f32_16x16x32_bf16 v[92:95], v[104:107], v[156:159], v[92:95]\n"
        "v_mfma_f32_16x16x32_bf16 v[100:103], v[140:143], v[144:147], 0\n"
        "v_mfma_f32_16x16x32_bf16 v[100:103], v[132:135], v[148:151], v[100:103]\n"
        "v_mfma_f32_16x16x32_bf16 v[100:103], v[96:99], v[156:159], v[100:103]\n"
        "v_mfma_f32_16x16x32_bf16 v[104:107], v[136:139], v[144:147], 0\n"
        "v_mfma_f32_16x16x32_bf16 v[104:107], v[128:131], v[148:151], v[104:107]\n"
        "v_mfma_f32_16x16x32_bf16 v[104:107], v[108:111], v[156:159], v[104:107]\n"
        "v_mfma_f32_16x16x32_bf16 v[96:99], v[152:155], v[144:147], 0\n"
        "v_mfma_f32_16x16x32_bf16 v[96:99], v[160:163], v[148:151], v[96:99]\n"
        "v_add_f32 v128, v88, v92\n"
        "v_add_f32 v129, v89, v93\n"
        "v_add_f32 v130, v90, v94\n"
        "v_add_f32 v131, v91, v95\n"
        "v_add_f32 v128, v128, v100\n"
        "v_add_f32 v129, v129, v101\n"
        "v_add_f32 v130, v130, v102\n"
        "v_add_f32 v131, v131, v103\n"
        "v_add_f32 v128, v128, v104\n"
        "v_add_f32 v129, v129, v105\n"
        "v_add_f32 v130, v130, v106\n"
        "v_add_f32 v131, v131, v107\n"
        "v_mfma_f32_16x16x32_bf16 v[96:99], v[112:115], v[156:159], v[96:99]\n"
        "v_mfma_f32_16x16x32_bf16 v[108:111], v[116:119], v[144:147], 0\n"
        "v_mfma_f32_16x16x32_bf16 v[108:111], v[120:123], v[148:151], v[108:111]\n"
        "v_mfma_f32_16x16x32_bf16 v[108:111], v[124:127], v[156:159], v[108:111]\n"
        "v_mov_b32 v239, s37\n"
        "s_nop 3\n"
        "v_add_f32 v128, v128, v96\n"
        "v_add_f32 v129, v129, v97\n"
        "v_add_f32 v130, v130, v98\n"
        "v_add_f32 v131, v131, v99\n"
        "v_add_f32 v128, v128, v108\n"
        "v_add_f32 v129, v129, v109\n"
        "v_add_f32 v130, v130, v110\n"
        "v_add_f32 v131, v131, v111\n"
        "v_add_f32 v128, v128, v129\n"
        "v_add_f32 v130, v130, v131\n"
        "v_add_f32 v128, v128, v130\n"
        "v_mov_b32 v129, v128\n"
        "s_nop 1\n"
        "v_permlane32_swap_b32 v128, v129\n"
        "v_add_f32 v128, v128, v129\n"
        "v_mov_b32 v129, v128\n"
        "s_nop 1\n"
        "v_permlane16_swap_b32 v128, v129\n"
        "v_add_f32 v128, v128, v129\n"
        "v_mul_f32 v128, 0xbc2aaaab, v128\n"
        "v_mul_f32 v128, s34, v128\n"
        "v_fma_f32 v88, v88, s34, v128\n"
        "v_fma_f32 v89, v89, s34, v128\n"
        "v_fma_f32 v90, v90, s34, v128\n"
        "v_fma_f32 v91, v91, s34, v128\n"
        "v_fma_f32 v92, v92, s34, v128\n"
        "v_fma_f32 v93, v93, s34, v128\n"
        "v_fma_f32 v94, v94, s34, v128\n"
        "v_fma_f32 v95, v95, s34, v128\n"
        "v_fma_f32 v100, v100, s34, v128\n"
        "v_fma_f32 v101, v101, s34, v128\n"
        "v_fma_f32 v102, v102, s34, v128\n"
        "v_fma_f32 v103, v103, s34, v128\n"
        "v_fma_f32 v104, v104, s34, v128\n"
        "v_fma_f32 v105, v105, s34, v128\n"
        "v_fma_f32 v106, v106, s34, v128\n"
        "v_fma_f32 v107, v107, s34, v128\n"
        "v_fma_f32 v96, v96, s34, v128\n"
        "v_fma_f32 v97, v97, s34, v128\n"
        "v_fma_f32 v98, v98, s34, v128\n"
        "v_fma_f32 v99, v99, s34, v128\n"
        "v_fma_f32 v108, v108, s34, v128\n"
        "v_fma_f32 v109, v109, s34, v128\n"
        "v_fma_f32 v110, v110, s34, v128\n"
        "v_fma_f32 v111, v111, s34, v128\n"
        "v_mov_b32 v237, s36\n"
        "L60_%=:\n"
        "ds_read_b32 v238, v237 offset:0\n"
        "s_waitcnt lgkmcnt(0)\n"
        "v_readfirstlane_b32 s40, v238\n"
        "s_nop 3\n"
        "s_cmp_ge_i32 s40, 4\n"
        "s_cbranch_scc1 L61_%=\n"
        "s_nop 0\n"
        "s_sub_u32 s41, s41, 1\n"
        "s_cbranch_scc0 L60_%=\n"
        "s_endpgm\n"
        "L61_%=:\n"
        "ds_read_b128 v[132:135], v246\n"
        "ds_read_b128 v[128:131], v246 offset:16\n"
        "ds_read_b128 v[124:127], v246 offset:128\n"
        "ds_read_b128 v[120:123], v246 offset:144\n"
        "ds_read_b128 v[116:119], v246 offset:256\n"
        "ds_read_b128 v[112:115], v246 offset:272\n"
        "s_waitcnt lgkmcnt(5)\n"
        "v_add_f32 v64, v64, v132\n"
        "v_add_f32 v65, v65, v133\n"
        "v_add_f32 v66, v66, v134\n"
        "v_add_f32 v67, v67, v135\n"
        "s_waitcnt lgkmcnt(0)\n"
        "v_add_f32 v68, v68, v128\n"
        "v_add_f32 v69, v69, v129\n"
        "v_add_f32 v70, v70, v130\n"
        "v_add_f32 v71, v71, v131\n"
        "v_add_f32 v88, v88, v132\n"
        "v_add_f32 v89, v89, v133\n"
        "v_add_f32 v90, v90, v134\n"
        "v_add_f32 v91, v91, v135\n"
        "v_add_f32 v92, v92, v128\n"
        "v_add_f32 v93, v93, v129\n"
        "v_add_f32 v94, v94, v130\n"
        "v_add_f32 v95, v95, v131\n"
        "v_add_f32 v72, v72, v124\n"
        "v_add_f32 v73, v73, v125\n"
        "v_add_f32 v74, v74, v126\n"
        "v_add_f32 v75, v75, v127\n"
        "v_add_f32 v76, v76, v120\n"
        "v_add_f32 v77, v77, v121\n"
        "v_add_f32 v78, v78, v122\n"
        "v_add_f32 v79, v79, v123\n"
        "v_add_f32 v100, v100, v124\n"
        "v_add_f32 v101, v101, v125\n"
        "v_add_f32 v102, v102, v126\n"
        "v_add_f32 v103, v103, v127\n"
        "v_add_f32 v104, v104, v120\n"
        "v_add_f32 v105, v105, v121\n"
        "v_add_f32 v106, v106, v122\n"
        "v_add_f32 v107, v107, v123\n"
        "v_add_f32 v80, v80, v116\n"
        "v_add_f32 v81, v81, v117\n"
        "v_add_f32 v82, v82, v118\n"
        "v_add_f32 v83, v83, v119\n"
        "v_add_f32 v84, v84, v112\n"
        "v_add_f32 v85, v85, v113\n"
        "v_add_f32 v86, v86, v114\n"
        "v_add_f32 v87, v87, v115\n"
        "v_add_f32 v96, v96, v116\n"
        "v_add_f32 v97, v97, v117\n"
        "v_add_f32 v98, v98, v118\n"
        "v_add_f32 v99, v99, v119\n"
        "v_add_f32 v108, v108, v112\n"
        "v_add_f32 v109, v109, v113\n"
        "v_add_f32 v110, v110, v114\n"
        "v_add_f32 v111, v111, v115\n"
        "v_cvt_pk_bf16_f32 v112, v64, v65\n"
        "v_cvt_pk_bf16_f32 v113, v66, v67\n"
        "v_cvt_pk_bf16_f32 v114, v68, v69\n"
        "v_cvt_pk_bf16_f32 v115, v70, v71\n"
        "v_cvt_pk_bf16_f32 v64, v88, v89\n"
        "v_cvt_pk_bf16_f32 v65, v90, v91\n"
        "v_cvt_pk_bf16_f32 v66, v92, v93\n"
        "v_cvt_pk_bf16_f32 v67, v94, v95\n"
        "v_cvt_pk_bf16_f32 v68, v72, v73\n"
        "v_cvt_pk_bf16_f32 v69, v74, v75\n"
        "v_cvt_pk_bf16_f32 v70, v76, v77\n"
        "v_cvt_pk_bf16_f32 v71, v78, v79\n"
        "v_cvt_pk_bf16_f32 v72, v100, v101\n"
        "v_cvt_pk_bf16_f32 v73, v102, v103\n"
        "v_cvt_pk_bf16_f32 v74, v104, v105\n"
        "v_cvt_pk_bf16_f32 v75, v106, v107\n"
        "v_cvt_pk_bf16_f32 v76, v80, v81\n"
        "v_cvt_pk_bf16_f32 v77, v82, v83\n"
        "v_cvt_pk_bf16_f32 v78, v84, v85\n"
        "v_cvt_pk_bf16_f32 v79, v86, v87\n"
        "v_cvt_pk_bf16_f32 v80, v96, v97\n"
        "v_cvt_pk_bf16_f32 v81, v98, v99\n"
        "v_cvt_pk_bf16_f32 v82, v108, v109\n"
        "v_cvt_pk_bf16_f32 v83, v110, v111\n"
        "ds_write_b128 v244, v[112:115]\n"
        "ds_write_b128 v245, v[64:67]\n"
        "ds_write_b128 v244, v[68:71] offset:1024\n"
        "ds_write_b128 v245, v[72:75] offset:1024\n"
        "ds_write_b128 v244, v[76:79] offset:2048\n"
        "ds_write_b128 v245, v[80:83] offset:2048\n"
        "s_mov_b64 exec, 1\n"
        "ds_inc_u32 v239, v239 offset:0\n"
        "s_mov_b64 exec, -1\n"
        "s_setpc_b64 s[30:31]\n"
        "FOLDR_%=:\n"
        "ds_read_b64_tr_b16 v[84:85], v242 offset:0\n"
        "ds_read_b64_tr_b16 v[86:87], v243 offset:0\n"
        "ds_read_b64_tr_b16 v[92:93], v242 offset:6144\n"
        "ds_read_b64_tr_b16 v[94:95], v243 offset:6144\n"
        "ds_read_b64_tr_b16 v[80:81], v242 offset:12288\n"
        "ds_read_b64_tr_b16 v[82:83], v243 offset:12288\n"
        "ds_read_b64_tr_b16 v[76:77], v242 offset:8\n"
        "ds_read_b64_tr_b16 v[78:79], v243 offset:8\n"
        "ds_read_b64_tr_b16 v[68:69], v242 offset:6152\n"
        "ds_read_b64_tr_b16 v[70:71], v243 offset:6152\n"
        "ds_read_b64_tr_b16 v[72:73], v242 offset:12296\n"
        "ds_read_b64_tr_b16 v[74:75], v243 offset:12296\n"
        "v_mov_b32 v64, 0x3f803f80\n"
        "v_mov_b32 v65, 0x3f803f80\n"
        "v_mov_b32 v66, 0x3f803f80\n"
        "v_mov_b32 v67, 0x3f803f80\n"
        "ds_read_b64_tr_b16 v[88:89], v242 offset:64\n"
        "ds_read_b64_tr_b16 v[90:91], v243 offset:64\n"
        "s_waitcnt lgkmcnt(12)\n"
        "v_mfma_f32_16x16x32_bf16 v[104:107], v[84:87], v[64:67], 0\n"
        "s_waitcnt lgkmcnt(10)\n"
        "v_mfma_f32_16x16x32_bf16 v[104:107], v[92:95], v[64:67], v[104:107]\n"
        "s_waitcnt lgkmcnt(6)\n"
        "ds_read_b64_tr_b16 v[96:97], v242 offset:6208\n"
        "ds_read_b64_tr_b16 v[98:99], v243 offset:6208\n"
        "v_mfma_f32_16x16x32_bf16 v[104:107], v[80:83], v[64:67], v[104:107]\n"
        "ds_read_b64_tr_b16 v[92:93], v242 offset:12352\n"
        "ds_read_b64_tr_b16 v[94:95], v243 offset:12352\n"
        "v_mfma_f32_16x16x32_bf16 v[100:103], v[76:79], v[64:67], 0\n"
        "s_waitcnt lgkmcnt(6)\n"
        "ds_read_b64_tr_b16 v[80:81], v242 offset:72\n"
        "ds_read_b64_tr_b16 v[82:83], v243 offset:72\n"
        "v_mfma_f32_16x16x32_bf16 v[100:103], v[68:71], v[64:67], v[100:103]\n"
        "ds_read_b64_tr_b16 v[76:77], v242 offset:6216\n"
        "ds_read_b64_tr_b16 v[78:79], v243 offset:6216\n"
        "v_cvt_pk_bf16_f32 v68, v104, v105\n"
        "v_cvt_pk_bf16_f32 v69, v106, v107\n"
        "v_mfma_f32_16x16x32_bf16 v[100:103], v[72:75], v[64:67], v[100:103]\n"
        "s_waitcnt lgkmcnt(8)\n"
        "ds_read_b64_tr_b16 v[84:85], v242 offset:12360\n"
        "ds_read_b64_tr_b16 v[86:87], v243 offset:12360\n"
        "v_mfma_f32_16x16x32_bf16 v[104:107], v[88:91], v[64:67], 0\n"
        "ds_read_b64_tr_b16 v[72:73], v242 offset:128\n"
        "ds_read_b64_tr_b16 v[74:75], v243 offset:128\n"
        "s_waitcnt lgkmcnt(10)\n"
        "v_mfma_f32_16x16x32_bf16 v[104:107], v[96:99], v[64:67], v[104:107]\n"
        "ds_read_b64_tr_b16 v[88:89], v242 offset:6272\n"
        "ds_read_b64_tr_b16 v[90:91], v243 offset:6272\n"
        "v_cvt_pk_bf16_f32 v70, v100, v101\n"
        "v_cvt_pk_bf16_f32 v71, v102, v103\n"
        "s_waitcnt lgkmcnt(9)\n"
        "v_mfma_f32_16x16x32_bf16 v[104:107], v[92:95], v[64:67], v[104:107]\n"
        "s_waitcnt lgkmcnt(8)\n"
        "ds_read_b64_tr_b16 v[100:101], v242 offset:12416\n"
        "ds_read_b64_tr_b16 v[102:103], v243 offset:12416\n"
        "v_mfma_f32_16x16x32_bf16 v[108:111], v[80:83], v[64:67], 0\n"
        "s_waitcnt lgkmcnt(8)\n"
        "ds_read_b64_tr_b16 v[92:93], v242 offset:136\n"
        "ds_read_b64_tr_b16 v[94:95], v243 offset:136\n"
        "v_mfma_f32_16x16x32_bf16 v[108:111], v[76:79], v[64:67], v[108:111]\n"
        "ds_read_b64_tr_b16 v[96:97], v242 offset:6280\n"
        "ds_read_b64_tr_b16 v[98:99], v243 offset:6280\n"
        "v_cvt_pk_bf16_f32 v80, v104, v105\n"
        "v_cvt_pk_bf16_f32 v81, v106, v107\n"
        "s_waitcnt lgkmcnt(8)\n"
        "v_mfma_f32_16x16x32_bf16 v[108:111], v[84:87], v[64:67], v[108:111]\n"
        "ds_read_b64_tr_b16 v[76:77], v242 offset:12424\n"
        "ds_read_b64_tr_b16 v[78:79], v243 offset:12424\n"
        "v_mfma_f32_16x16x32_bf16 v[116:119], v[72:75], v[64:67], 0\n"
        "s_waitcnt lgkmcnt(8)\n"
        "ds_read_b128 v[72:75], v240\n"
        "v_mfma_f32_16x16x32_bf16 v[116:119], v[88:91], v[64:67], v[116:119]\n"
        "ds_read_b128 v[104:107], v241\n"
        "s_waitcnt lgkmcnt(8)\n"
        "v_mfma_f32_16x16x32_bf16 v[116:119], v[100:103], v[64:67], v[116:119]\n"
        "ds_read_b128 v[84:87], v240 offset:6144\n"
        "v_cvt_pk_bf16_f32 v82, v108, v109\n"
        "v_cvt_pk_bf16_f32 v83, v110, v111\n"
        "s_waitcnt lgkmcnt(7)\n"
        "v_mfma_f32_16x16x32_bf16 v[112:115], v[92:95], v[64:67], 0\n"
        "s_waitcnt lgkmcnt(5)\n"
        "ds_read_b128 v[92:95], v241 offset:6144\n"
        "v_mfma_f32_16x16x32_bf16 v[112:115], v[96:99], v[64:67], v[112:115]\n"
        "ds_read_b128 v[100:103], v240 offset:12288\n"
        "v_cvt_pk_bf16_f32 v88, v116, v117\n"
        "v_cvt_pk_bf16_f32 v89, v118, v119\n"
        "s_waitcnt lgkmcnt(5)\n"
        "v_mfma_f32_16x16x32_bf16 v[112:115], v[76:79], v[64:67], v[112:115]\n"
        "s_waitcnt lgkmcnt(4)\n"
        "ds_read_b128 v[116:119], v241 offset:12288\n"
        "v_mfma_f32_16x16x32_bf16 v[64:67], v[68:71], v[72:75], 0\n"
        "s_waitcnt lgkmcnt(4)\n"
        "ds_read_b128 v[120:123], v240 offset:64\n"
        "v_mfma_f32_16x16x32_bf16 v[72:75], v[68:71], v[104:107], 0\n"
        "ds_read_b128 v[108:111], v241 offset:64\n"
        "s_waitcnt lgkmcnt(5)\n"
        "v_mfma_f32_16x16x32_bf16 v[76:79], v[68:71], v[84:87], 0\n"
        "ds_read_b128 v[104:107], v240 offset:6208\n"
        "v_cvt_pk_bf16_f32 v90, v112, v113\n"
        "v_cvt_pk_bf16_f32 v91, v114, v115\n"
        "s_waitcnt lgkmcnt(5)\n"
        "v_mfma_f32_16x16x32_bf16 v[84:87], v[68:71], v[92:95], 0\n"
        "ds_read_b128 v[124:127], v241 offset:6208\n"
        "s_waitcnt lgkmcnt(5)\n"
        "v_mfma_f32_16x16x32_bf16 v[92:95], v[68:71], v[100:103], 0\n"
        "ds_read_b128 v[112:115], v240 offset:12352\n"
        "s_waitcnt lgkmcnt(5)\n"
        "v_mfma_f32_16x16x32_bf16 v[96:99], v[68:71], v[116:119], 0\n"
        "ds_read_b128 v[100:103], v241 offset:12352\n"
        "s_waitcnt lgkmcnt(5)\n"
        "v_mfma_f32_16x16x32_bf16 v[64:67], v[80:83], v[120:123], v[64:67]\n"
        "ds_read_b128 v[68:71], v240 offset:128\n"
        "s_waitcnt lgkmcnt(5)\n"
        "v_mfma_f32_16x16x32_bf16 v[72:75], v[80:83], v[108:111], v[72:75]\n"
        "ds_read_b128 v[116:119], v241 offset:128\n"
        "s_waitcnt lgkmcnt(5)\n"
        "v_mfma_f32_16x16x32_bf16 v[76:79], v[80:83], v[104:107], v[76:79]\n"
        "ds_read_b128 v[108:111], v240 offset:6272\n"
        "s_waitcnt lgkmcnt(5)\n"
        "v_mfma_f32_16x16x32_bf16 v[84:87], v[80:83], v[124:127], v[84:87]\n"
        "ds_read_b128 v[104:107], v241 offset:6272\n"
        "s_waitcnt lgkmcnt(5)\n"
        "v_mfma_f32_16x16x32_bf16 v[92:95], v[80:83], v[112:115], v[92:95]\n"
        "ds_read_b128 v[120:123], v240 offset:12416\n"
        "s_waitcnt lgkmcnt(5)\n"
        "v_mfma_f32_16x16x32_bf16 v[96:99], v[80:83], v[100:103], v[96:99]\n"
        "ds_read_b128 v[112:115], v241 offset:12416\n"
        "s_waitcnt lgkmcnt(5)\n"
        "v_mfma_f32_16x16x32_bf16 v[64:67], v[88:91], v[68:71], v[64:67]\n"
        "s_waitcnt lgkmcnt(4)\n"
        "v_mfma_f32_16x16x32_bf16 v[72:75], v[88:91], v[116:119], v[72:75]\n"
        "v_mov_b32 v239, s35\n"
        "s_waitcnt lgkmcnt(3)\n"
        "v_mfma_f32_16x16x32_bf16 v[76:79], v[88:91], v[108:111], v[76:79]\n"
        "s_waitcnt vmcnt(6)\n"
        "s_mov_b64 exec, 1\n"
        "ds_inc_u32 v239, v239 offset:0\n"
        "s_mov_b64 exec, -1\n"
        "s_waitcnt lgkmcnt(3)\n"
        "v_mfma_f32_16x16x32_bf16 v[84:87], v[88:91], v[104:107], v[84:87]\n"
        "v_add_f32 v238, v64, v72\n"
        "s_waitcnt lgkmcnt(2)\n"
        "v_mfma_f32_16x16x32_bf16 v[92:95], v[88:91], v[120:123], v[92:95]\n"
        "v_add_f32 v238, v238, v76\n"
        "s_waitcnt lgkmcnt(1)\n"
        "v_mfma_f32_16x16x32_bf16 v[96:99], v[88:91], v[112:115], v[96:99]\n"
        "v_mov_b32 v239, s37\n"
        "s_nop 0\n"
        "v_add_f32 v238, v238, v84\n"
        "s_nop 1\n"
        "v_add_f32 v238, v238, v92\n"
        "s_nop 1\n"
        "v_add_f32 v238, v238, v96\n"
        "s_nop 1\n"
        "v_add_f32_dpp v238, v238, v238 row_ror:8 row_mask:0xf bank_mask:0xf\n"
        "s_nop 1\n"
        "v_add_f32_dpp v238, v238, v238 row_ror:4 row_mask:0xf bank_mask:0xf\n"
        "s_nop 1\n"
        "v_add_f32_dpp v238, v238, v238 row_ror:2 row_mask:0xf bank_mask:0xf\n"
        "s_nop 1\n"
        "v_add_f32_dpp v238, v238, v238 row_ror:1 row_mask:0xf bank_mask:0xf\n"
        "v_mul_f32 v238, 0xbc2aaaab, v238\n"
        "v_mul_f32 v238, s34, v238\n"
        "v_fma_f32 v64, v64, s34, v238\n"
        "v_fma_f32 v72, v72, s34, v238\n"
        "v_fma_f32 v76, v76, s34, v238\n"
        "v_fma_f32 v84, v84, s34, v238\n"
        "v_fma_f32 v92, v92, s34, v238\n"
        "v_fma_f32 v96, v96, s34, v238\n"
        "ds_write_b32 v244, v64\n"
        "ds_write_b32 v244, v72 offset:16\n"
        "ds_write_b32 v244, v76 offset:128\n"
        "ds_write_b32 v244, v84 offset:144\n"
        "ds_write_b32 v244, v92 offset:256\n"
        "ds_write_b32 v244, v96 offset:272\n"
        "s_mov_b64 exec, 1\n"
        "ds_inc_u32 v239, v239 offset:0\n"
        "s_mov_b64 exec, -1\n"
        "s_setpc_b64 s[30:31]\n"
        "FOLDW_%=:\n"
        "ds_read_b64_tr_b16 v[64:65], v242 offset:0\n"
        "ds_read_b64_tr_b16 v[66:67], v243 offset:0\n"
        "ds_read_b128 v[172:175], v240\n"
        "ds_read_b64_tr_b16 v[180:181], v242 offset:6144\n"
        "ds_read_b64_tr_b16 v[182:183], v243 offset:6144\n"
        "ds_read_b128 v[120:123], v240 offset:64\n"
        "ds_read_b64_tr_b16 v[184:185], v242 offset:12288\n"
        "ds_read_b64_tr_b16 v[186:187], v243 offset:12288\n"
        "ds_read_b128 v[168:171], v240 offset:128\n"
        "ds_read_b128 v[160:163], v241\n"
        "ds_read_b128 v[148:151], v241 offset:64\n"
        "ds_read_b128 v[132:135], v241 offset:128\n"
        "ds_read_b128 v[124:127], v240 offset:6144\n"
        "ds_read_b128 v[104:107], v240 offset:6208\n"
        "ds_read_b128 v[92:95], v240 offset:6272\n"
        "ds_read_b128 v[96:99], v241 offset:6144\n"
        "ds_read_b128 v[88:91], v241 offset:6208\n"
        "ds_read_b128 v[84:87], v241 offset:6272\n"
        "s_waitcnt lgkmcnt(15)\n"
        "v_mfma_f32_16x16x32_bf16 v[136:139], v[172:175], v[64:67], 0\n"
        "ds_read_b128 v[100:103], v240 offset:12288\n"
        "s_waitcnt lgkmcnt(12)\n"
        "v_mfma_f32_16x16x32_bf16 v[136:139], v[120:123], v[180:183], v[136:139]\n"
        "s_waitcnt lgkmcnt(10)\n"
        "ds_read_b128 v[108:111], v240 offset:12352\n"
        "v_mfma_f32_16x16x32_bf16 v[136:139], v[168:171], v[184:187], v[136:139]\n"
        "s_waitcnt lgkmcnt(7)\n"
        "ds_read_b128 v[68:71], v240 offset:12416\n"
        "v_mfma_f32_16x16x32_bf16 v[140:143], v[160:163], v[64:67], 0\n"
        "ds_read_b128 v[72:75], v241 offset:12288\n"
        "v_mfma_f32_16x16x32_bf16 v[140:143], v[148:151], v[180:183], v[140:143]\n"
        "ds_read_b128 v[76:79], v241 offset:12352\n"
        "v_mfma_f32_16x16x32_bf16 v[140:143], v[132:135], v[184:187], v[140:143]\n"
        "ds_read_b128 v[80:83], v241 offset:12416\n"
        "v_mfma_f32_16x16x32_bf16 v[144:147], v[124:127], v[64:67], 0\n"
        "s_waitcnt lgkmcnt(6)\n"
        "ds_read_b64_tr_b16 v[112:113], v242 offset:8\n"
        "ds_read_b64_tr_b16 v[114:115], v243 offset:8\n"
        "v_mfma_f32_16x16x32_bf16 v[144:147], v[104:107], v[180:183], v[144:147]\n"
        "ds_read_b64_tr_b16 v[116:117], v242 offset:6152\n"
        "ds_read_b64_tr_b16 v[118:119], v243 offset:6152\n"
        "v_add_f32 v164, v136, v140\n"
        "v_add_f32 v165, v137, v141\n"
        "v_add_f32 v166, v138, v142\n"
        "v_add_f32 v167, v139, v143\n"
        "v_mfma_f32_16x16x32_bf16 v[144:147], v[92:95], v[184:187], v[144:147]\n"
        "ds_read_b64_tr_b16 v[128:129], v242 offset:12296\n"
        "ds_read_b64_tr_b16 v[130:131], v243 offset:12296\n"
        "v_mfma_f32_16x16x32_bf16 v[152:155], v[96:99], v[64:67], 0\n"
        "v_mfma_f32_16x16x32_bf16 v[152:155], v[88:91], v[180:183], v[152:155]\n"
        "v_mfma_f32_16x16x32_bf16 v[152:155], v[84:87], v[184:187], v[152:155]\n"
        "s_waitcnt lgkmcnt(6)\n"
        "v_mfma_f32_16x16x32_bf16 v[156:159], v[100:103], v[64:67], 0\n"
        "v_mfma_f32_16x16x32_bf16 v[156:159], v[108:111], v[180:183], v[156:159]\n"
        "v_add_f32 v164, v164, v144\n"
        "v_add_f32 v165, v165, v145\n"
        "v_add_f32 v166, v166, v146\n"
        "v_add_f32 v167, v167, v147\n"
        "v_mfma_f32_16x16x32_bf16 v[156:159], v[68:71], v[184:187], v[156:159]\n"
        "v_add_f32 v164, v164, v152\n"
        "v_add_f32 v165, v165, v153\n"
        "v_add_f32 v166, v166, v154\n"
        "v_add_f32 v167, v167, v155\n"
        "v_mfma_f32_16x16x32_bf16 v[176:179], v[72:75], v[64:67], 0\n"
        "v_mfma_f32_16x16x32_bf16 v[176:179], v[76:79], v[180:183], v[176:179]\n"
        "v_mfma_f32_16x16x32_bf16 v[176:179], v[80:83], v[184:187], v[176:179]\n"
        "s_waitcnt lgkmcnt(0)\n"
        "v_mov_b32 v239, s35\n"
        "v_add_f32 v164, v164, v156\n"
        "v_add_f32 v165, v165, v157\n"
        "v_add_f32 v166, v166, v158\n"
        "v_add_f32 v167, v167, v159\n"
        "s_waitcnt vmcnt(6)\n"
        "s_mov_b64 exec, 1\n"
        "ds_inc_u32 v239, v239 offset:0\n"
        "s_mov_b64 exec, -1\n"
        "v_add_f32 v164, v164, v176\n"
        "v_add_f32 v165, v165, v177\n"
        "v_mfma_f32_16x16x32_bf16 v[64:67], v[172:175], v[112:115], 0\n"
        "v_add_f32 v166, v166, v178\n"
        "v_add_f32 v167, v167, v179\n"
        "v_add_f32 v164, v164, v165\n"
        "v_add_f32 v166, v166, v167\n"
        "v_add_f32 v164, v164, v166\n"
        "v_mov_b32 v165, v164\n"
        "v_mfma_f32_16x16x32_bf16 v[64:67], v[120:123], v[116:119], v[64:67]\n"
        "v_mfma_f32_16x16x32_bf16 v[64:67], v[168:171], v[128:131], v[64:67]\n"
        "v_permlane32_swap_b32 v164, v165\n"
        "v_add_f32 v164, v164, v165\n"
        "v_mov_b32 v165, v164\n"
        "v_mfma_f32_16x16x32_bf16 v[120:123], v[160:163], v[112:115], 0\n"
        "global_load_dwordx4 v[40:43], v57, s[22:23] offset:-3072\n"
        "v_permlane16_swap_b32 v164, v165\n"
        "v_add_f32 v164, v164, v165\n"
        "v_mul_f32 v164, 0xbc2aaaab, v164\n"
        "v_mul_f32 v164, s34, v164\n"
        "v_fma_f32 v136, v136, s34, v164\n"
        "v_fma_f32 v137, v137, s34, v164\n"
        "v_fma_f32 v138, v138, s34, v164\n"
        "v_fma_f32 v139, v139, s34, v164\n"
        "v_fma_f32 v140, v140, s34, v164\n"
        "v_fma_f32 v141, v141, s34, v164\n"
        "v_mfma_f32_16x16x32_bf16 v[120:123], v[148:151], v[116:119], v[120:123]\n"
        "v_fma_f32 v142, v142, s34, v164\n"
        "v_fma_f32 v143, v143, s34, v164\n"
        "v_fma_f32 v144, v144, s34, v164\n"
        "v_fma_f32 v145, v145, s34, v164\n"
        "v_fma_f32 v146, v146, s34, v164\n"
        "v_fma_f32 v147, v147, s34, v164\n"
        "v_fma_f32 v152, v152, s34, v164\n"
        "v_fma_f32 v153, v153, s34, v164\n"
        "v_fma_f32 v154, v154, s34, v164\n"
        "v_fma_f32 v155, v155, s34, v164\n"
        "v_fma_f32 v156, v156, s34, v164\n"
        "v_fma_f32 v157, v157, s34, v164\n"
        "v_fma_f32 v158, v158, s34, v164\n"
        "v_fma_f32 v159, v159, s34, v164\n"
        "v_mfma_f32_16x16x32_bf16 v[120:123], v[132:135], v[128:131], v[120:123]\n"
        "global_load_dwordx4 v[44:47], v57, s[22:23] offset:-2048\n"
        "v_fma_f32 v176, v176, s34, v164\n"
        "v_fma_f32 v177, v177, s34, v164\n"
        "v_fma_f32 v178, v178, s34, v164\n"
        "v_fma_f32 v179, v179, s34, v164\n"
        "v_cvt_pk_bf16_f32 v132, v136, v137\n"
        "v_cvt_pk_bf16_f32 v133, v138, v139\n"
        "v_cvt_pk_bf16_f32 v134, v140, v141\n"
        "v_add_f32 v148, v64, v120\n"
        "v_add_f32 v149, v65, v121\n"
        "v_add_f32 v150, v66, v122\n"
        "v_add_f32 v151, v67, v123\n"
        "v_cvt_pk_bf16_f32 v135, v142, v143\n"
        "v_mfma_f32_16x16x32_bf16 v[140:143], v[124:127], v[112:115], 0\n"
        "v_cvt_pk_bf16_f32 v136, v144, v145\n"
        "v_cvt_pk_bf16_f32 v137, v146, v147\n"
        "v_cvt_pk_bf16_f32 v138, v152, v153\n"
        "v_cvt_pk_bf16_f32 v139, v154, v155\n"
        "v_cvt_pk_bf16_f32 v124, v156, v157\n"
        "v_cvt_pk_bf16_f32 v125, v158, v159\n"
        "v_cvt_pk_bf16_f32 v126, v176, v177\n"
        "v_cvt_pk_bf16_f32 v127, v178, v179\n"
        "v_mfma_f32_16x16x32_bf16 v[140:143], v[104:107], v[116:119], v[140:143]\n"
        "global_load_dwordx4 v[0:3], v57, s[22:23] offset:-1024\n"
        "v_mfma_f32_16x16x32_bf16 v[140:143], v[92:95], v[128:131], v[140:143]\n"
        "v_mfma_f32_16x16x32_bf16 v[104:107], v[96:99], v[112:115], 0\n"
        "global_load_dwordx4 v[4:7], v57, s[22:23] offset:0\n"
        "v_mfma_f32_16x16x32_bf16 v[104:107], v[88:91], v[116:119], v[104:107]\n"
        "v_mfma_f32_16x16x32_bf16 v[104:107], v[84:87], v[128:131], v[104:107]\n"
        "global_load_dwordx4 v[8:11], v57, s[22:23] offset:1024\n"
        "v_mfma_f32_16x16x32_bf16 v[84:87], v[100:103], v[112:115], 0\n"
        "v_mfma_f32_16x16x32_bf16 v[84:87], v[108:111], v[116:119], v[84:87]\n"
        "global_load_dwordx4 v[12:15], v57, s[22:23] offset:2048\n"
        "v_add_f32 v148, v148, v140\n"
        "v_add_f32 v149, v149, v141\n"
        "v_add_f32 v150, v150, v142\n"
        "v_add_f32 v151, v151, v143\n"
        "v_add_f32 v148, v148, v104\n"
        "v_add_f32 v149, v149, v105\n"
        "v_add_f32 v150, v150, v106\n"
        "v_add_f32 v151, v151, v107\n"
        "v_mfma_f32_16x16x32_bf16 v[84:87], v[68:71], v[128:131], v[84:87]\n"
        "v_mfma_f32_16x16x32_bf16 v[88:91], v[72:75], v[112:115], 0\n"
        "global_load_dwordx4 v[16:19], v57, s[22:23] offset:3072\n"
        "v_mfma_f32_16x16x32_bf16 v[88:91], v[76:79], v[116:119], v[88:91]\n"
        "v_mfma_f32_16x16x32_bf16 v[88:91], v[80:83], v[128:131], v[88:91]\n"
        "v_mov_b32 v239, s37\n"
        "s_nop 2\n"
        "v_add_f32 v148, v148, v84\n"
        "v_add_f32 v149, v149, v85\n"
        "v_add_f32 v150, v150, v86\n"
        "v_add_f32 v151, v151, v87\n"
        "v_add_f32 v148, v148, v88\n"
        "v_add_f32 v149, v149, v89\n"
        "v_add_f32 v150, v150, v90\n"
        "v_add_f32 v151, v151, v91\n"
        "v_add_f32 v148, v148, v149\n"
        "v_add_f32 v150, v150, v151\n"
        "v_add_f32 v148, v148, v150\n"
        "v_mov_b32 v149, v148\n"
        "s_nop 1\n"
        "v_permlane32_swap_b32 v148, v149\n"
        "v_add_f32 v148, v148, v149\n"
        "v_mov_b32 v149, v148\n"
        "s_nop 1\n"
        "v_permlane16_swap_b32 v148, v149\n"
        "v_add_f32 v148, v148, v149\n"
        "v_mul_f32 v148, 0xbc2aaaab, v148\n"
        "v_mul_f32 v148, s34, v148\n"
        "v_fma_f32 v64, v64, s34, v148\n"
        "v_fma_f32 v65, v65, s34, v148\n"
        "v_fma_f32 v66, v66, s34, v148\n"
        "v_fma_f32 v67, v67, s34, v148\n"
        "v_fma_f32 v120, v120, s34, v148\n"
        "v_fma_f32 v121, v121, s34, v148\n"
        "v_fma_f32 v122, v122, s34, v148\n"
        "v_fma_f32 v123, v123, s34, v148\n"
        "v_fma_f32 v140, v140, s34, v148\n"
        "v_fma_f32 v141, v141, s34, v148\n"
        "v_fma_f32 v142, v142, s34, v148\n"
        "v_fma_f32 v143, v143, s34, v148\n"
        "v_fma_f32 v104, v104, s34, v148\n"
        "v_fma_f32 v105, v105, s34, v148\n"
        "v_fma_f32 v106, v106, s34, v148\n"
        "v_fma_f32 v107, v107, s34, v148\n"
        "v_fma_f32 v84, v84, s34, v148\n"
        "v_fma_f32 v85, v85, s34, v148\n"
        "v_fma_f32 v86, v86, s34, v148\n"
        "v_fma_f32 v87, v87, s34, v148\n"
        "v_fma_f32 v88, v88, s34, v148\n"
        "v_fma_f32 v89, v89, s34, v148\n"
        "v_fma_f32 v90, v90, s34, v148\n"
        "v_fma_f32 v91, v91, s34, v148\n"
        "v_cvt_pk_bf16_f32 v68, v64, v65\n"
        "v_cvt_pk_bf16_f32 v69, v66, v67\n"
        "v_cvt_pk_bf16_f32 v70, v120, v121\n"
        "v_cvt_pk_bf16_f32 v71, v122, v123\n"
        "v_cvt_pk_bf16_f32 v64, v140, v141\n"
        "v_cvt_pk_bf16_f32 v65, v142, v143\n"
        "v_cvt_pk_bf16_f32 v66, v104, v105\n"
        "v_cvt_pk_bf16_f32 v67, v106, v107\n"
        "v_cvt_pk_bf16_f32 v72, v84, v85\n"
        "v_cvt_pk_bf16_f32 v73, v86, v87\n"
        "v_cvt_pk_bf16_f32 v74, v88, v89\n"
        "v_cvt_pk_bf16_f32 v75, v90, v91\n"
        "s_nop 1\n"
        "v_mov_b32 v238, s36\n"
        "L62_%=:\n"
        "ds_read_b32 v237, v238 offset:0\n"
        "s_waitcnt lgkmcnt(0)\n"
        "v_readfirstlane_b32 s40, v237\n"
        "s_nop 3\n"
        "s_cmp_ge_i32 s40, 4\n"
        "s_cbranch_scc1 L63_%=\n"
        "s_nop 0\n"
        "s_sub_u32 s41, s41, 1\n"
        "s_cbranch_scc0 L62_%=\n"
        "s_endpgm\n"
        "L63_%=:\n"
        "ds_write_b128 v244, v[132:135]\n"
        "ds_write_b128 v245, v[68:71]\n"
        "ds_write_b128 v244, v[136:139] offset:1024\n"
        "ds_write_b128 v245, v[64:67] offset:1024\n"
        "ds_write_b128 v244, v[124:127] offset:2048\n"
        "ds_write_b128 v245, v[72:75] offset:2048\n"
        "s_mov_b64 exec, 1\n"
        "ds_inc_u32 v239, v239 offset:0\n"
        "s_mov_b64 exec, -1\n"
        "s_setpc_b64 s[30:31]\n"
        "PPRO_%=:\n"
        "s_add_u32 s64, s64, 128\n"
        "s_cmp_lg_u32 s6, 0\n"
        "s_cbranch_scc1 L78_%=\n"
        "s_mov_b64 exec, 0xffffffff\n"
        "v_lshlrev_b32 v27, 2, v56\n"
        "v_add_u32 v27, 110048, v27\n"
        "v_mov_b32 v28, 0\n"
        "ds_write_b32 v27, v28\n"
        "s_mov_b64 exec, 0x1fffff\n"
        "s_bfe_u32 s7, s2, 0x50003\n"
        "s_mul_i32 s7, s7, 2656\n"
        "v_lshlrev_b32 v27, 7, v56\n"
        "v_add_u32 v27, s7, v27\n"
        "global_load_dword v252, v27, s[4:5]\n"
        "s_mov_b64 exec, -1\n"
        "L78_%=:\n"
        "s_cmp_lt_u32 s6, 2\n"
        "s_cselect_b32 s65, 3, 1\n"
        "s_cselect_b32 s61, 384, 0\n"
        "s_cselect_b32 s62, 9, 1\n"
        "s_cselect_b32 s63, 3, 4\n"
        "s_cselect_b32 s26, 3, 1\n"
        "s_cselect_b32 s34, 9, 1\n"
        "s_cselect_b32 s70, 72, 1\n"
        "s_cselect_b32 s71, 36, 4\n"
        "s_cselect_b32 s72, 6, 8\n"
        "s_mul_i32 s70, s70, 192\n"
        "s_mul_i32 s71, s71, 192\n"
        "s_mul_i32 s72, s72, 192\n"
        "s_mul_i32 s65, s65, 192\n"
        "s_mul_i32 s62, s62, 3072\n"
        "s_mul_i32 s63, s63, 1536\n"
        "s_mul_i32 s26, s26, 18432\n"
        "s_mul_i32 s34, s34, 1536\n"
        "s_add_u32 s7, s62, s63\n"
        "s_lshl_b32 s29, s63, 1\n"
        "s_add_u32 s60, s7, s63\n"
        "s_bitcmp1_b32 s6, 0\n"
        "s_cselect_b32 s28, s7, 0\n"
        "s_cselect_b32 s29, s29, s62\n"
        "s_cselect_b32 s60, s60, s63\n"
        "s_add_u32 s28, s28, s61\n"
        "s_add_u32 s29, s29, s61\n"
        "s_add_u32 s60, s60, s61\n"
        "v_add_u32 v24, 0, v56\n"
        "v_mul_u32_u24 v21, 171, v24\n"
        "v_lshrrev_b32 v21, 11, v21\n"
        "v_mad_i32_i24 v24, v21, -12, v24\n"
        "v_bfe_u32 v58, v21, 2, 2\n"
        "v_xor_b32 v58, v24, v58\n"
        "v_and_b32 v27, 3, v21\n"
        "v_mul_u32_u24 v27, s70, v27\n"
        "v_lshl_add_u32 v32, v58, 4, v27\n"
        "v_bfe_u32 v27, v21, 2, 1\n"
        "v_mad_u32_u24 v32, v27, s71, v32\n"
        "v_lshrrev_b32 v27, 3, v21\n"
        "v_mad_u32_u24 v32, v27, s72, v32\n"
        "v_and_b32 v27, 7, v21\n"
        "v_mul_u32_u24 v27, s65, v27\n"
        "v_lshl_add_u32 v58, v58, 4, v27\n"
        "v_add_u32 v25, 64, v56\n"
        "v_mul_u32_u24 v22, 171, v25\n"
        "v_lshrrev_b32 v22, 11, v22\n"
        "v_mad_i32_i24 v25, v22, -12, v25\n"
        "v_bfe_u32 v59, v22, 2, 2\n"
        "v_xor_b32 v59, v25, v59\n"
        "v_and_b32 v27, 3, v22\n"
        "v_mul_u32_u24 v27, s70, v27\n"
        "v_lshl_add_u32 v33, v59, 4, v27\n"
        "v_bfe_u32 v27, v22, 2, 1\n"
        "v_mad_u32_u24 v33, v27, s71, v33\n"
        "v_lshrrev_b32 v27, 3, v22\n"
        "v_mad_u32_u24 v33, v27, s72, v33\n"
        "v_and_b32 v27, 7, v22\n"
        "v_mul_u32_u24 v27, s65, v27\n"
        "v_lshl_add_u32 v59, v59, 4, v27\n"
        "v_lshrrev_b32 v27, 3, v22\n"
        "v_mad_u32_u24 v59, v27, s34, v59\n"
        "v_add_u32 v26, 128, v56\n"
        "v_mul_u32_u24 v23, 171, v26\n"
        "v_lshrrev_b32 v23, 11, v23\n"
        "v_mad_i32_i24 v26, v23, -12, v26\n"
        "v_bfe_u32 v60, v23, 2, 2\n"
        "v_xor_b32 v60, v26, v60\n"
        "v_and_b32 v27, 3, v23\n"
        "v_mul_u32_u24 v27, s70, v27\n"
        "v_lshl_add_u32 v34, v60, 4, v27\n"
        "v_bfe_u32 v27, v23, 2, 1\n"
        "v_mad_u32_u24 v34, v27, s71, v34\n"
        "v_lshrrev_b32 v27, 3, v23\n"
        "v_mad_u32_u24 v34, v27, s72, v34\n"
        "v_and_b32 v27, 7, v23\n"
        "v_mul_u32_u24 v27, s65, v27\n"
        "v_lshl_add_u32 v60, v60, 4, v27\n"
        "v_add_u32 v60, s34, v60\n"
        "s_and_b32 s7, s2, 7\n"
        "s_lshl_b32 s7, s7, 5\n"
        "s_lshr_b32 s14, s2, 3\n"
        "s_add_u32 s7, s7, s14\n"
        "s_mul_i32 s7, s7, 20\n"
        "s_mov_b32 s14, 22\n"
        "s_mul_i32 s15, s7, 57457\n"
        "s_lshr_b32 s15, s15, 22\n"
        "s_mul_i32 s16, s15, 73\n"
        "s_sub_u32 s16, s7, s16\n"
        "s_mul_i32 s15, s15, 686784\n"
        "s_mul_i32 s7, s16, 1344\n"
        "s_add_u32 s15, s15, s7\n"
        "s_sub_u32 s7, 6, s6\n"
        "v_mov_b32 v28, 96768\n"
        "s_mov_b32 s35, 50135232\n"
        "v_mov_b32 v29, 2048\n"
        "v_and_b32 v21, 7, v21\n"
        "v_cmp_lt_u32 vcc, s7, v21\n"
        "v_and_b32 v27, 3, v24\n"
        "v_mad_u32_u24 v29, v27, 48, v29\n"
        "v_lshrrev_b32 v27, 2, v24\n"
        "v_lshl_add_u32 v29, v27, 4, v29\n"
        "v_mul_u32_u24 v21, 192, v21\n"
        "v_cndmask_b32 v27, 0, v28, vcc\n"
        "v_add3_u32 v29, v29, v21, v27\n"
        "v_lshrrev_b32 v27, 3, v22\n"
        "v_xor_b32 v25, v25, v27\n"
        "v_mul_lo_u32 v30, v27, s35\n"
        "v_add_u32 v30, 1024, v30\n"
        "v_and_b32 v22, 7, v22\n"
        "v_cmp_lt_u32 vcc, s7, v22\n"
        "v_and_b32 v27, 3, v25\n"
        "v_mad_u32_u24 v30, v27, 48, v30\n"
        "v_lshrrev_b32 v27, 2, v25\n"
        "v_lshl_add_u32 v30, v27, 4, v30\n"
        "v_mul_u32_u24 v22, 192, v22\n"
        "v_cndmask_b32 v27, 0, v28, vcc\n"
        "v_add3_u32 v30, v30, v22, v27\n"
        "v_mov_b32 v31, 50135232\n"
        "v_xor_b32 v26, 1, v26\n"
        "v_and_b32 v23, 7, v23\n"
        "v_cmp_lt_u32 vcc, s7, v23\n"
        "v_and_b32 v27, 3, v26\n"
        "v_mad_u32_u24 v31, v27, 48, v31\n"
        "v_lshrrev_b32 v27, 2, v26\n"
        "v_lshl_add_u32 v31, v27, 4, v31\n"
        "v_mul_u32_u24 v23, 192, v23\n"
        "v_cndmask_b32 v27, 0, v28, vcc\n"
        "v_add3_u32 v31, v31, v23, v27\n"
        "s_mul_i32 s17, s6, 6208\n"
        "s_add_u32 s17, s17, 110176\n"
        "s_mov_b32 s18, s6\n"
        "s_mul_i32 s74, s6, 98304\n"
        "s_add_u32 s74, s74, s15\n"
        "s_add_u32 s75, s74, 100270464\n"
        "v_mov_b32 v250, 110048\n"
        "s_mov_b32 s41, 0x400000\n"
        "s_waitcnt lgkmcnt(0)\n"
        "s_cmp_lg_u32 s6, 0\n"
        "s_cbranch_scc1 L79_%=\n"
        "s_bitcmp1_b32 s2, 7\n"
        "s_cselect_b64 s[8:9], s[50:51], s[46:47]\n"
        "s_cselect_b64 s[10:11], s[52:53], s[48:49]\n"
        "s_bfe_u32 s7, s2, 0x40003\n"
        "s_cmp_ge_u32 s7, 12\n"
        "s_cselect_b64 s[8:9], s[10:11], s[8:9]\n"
        "s_cselect_b32 s10, 12, 0\n"
        "s_sub_u32 s7, s7, s10\n"
        "s_mul_i32 s7, s7, 4608\n"
        "v_lshlrev_b32 v27, 7, v56\n"
        "v_add_u32 v27, s7, v27\n"
        "s_mov_b32 exec_hi, 0xf\n"
        "global_load_dword v252, v27, s[8:9]\n"
        "s_mov_b32 exec_hi, -1\n"
        "s_mov_b64 s[24:25], s[56:57]\n"
        "s_mov_b64 exec, 1\n"
        "v_mov_b32 v27, 0\n"
        "global_load_dword v253, v27, s[24:25] offset:256 sc1\n"
        "s_mov_b64 exec, -1\n"
        "L79_%=:\n"
        "s_cmp_lt_u32 s6, 2\n"
        "s_cselect_b64 s[24:25], s[46:47], s[48:49]\n"
        "s_and_b32 s25, s25, 0xffff\n"
        "s_sub_u32 s8, s44, 2048\n"
        "s_subb_u32 s9, s45, 0\n"
        "s_and_b32 s9, s9, 0xffff\n"
        "s_mov_b32 s10, 200542976\n"
        "s_mov_b32 s11, 0x20000\n"
        "s_mul_i32 s7, s6, 7168\n"
        "s_add_u32 s7, s7, 6144\n"
        "s_add_u32 s22, s4, s7\n"
        "s_addc_u32 s23, s5, 0\n"
        "s_sub_u32 s7, s6, 2\n"
        "s_and_b32 s7, s7, 3\n"
        "s_min_u32 s7, s7, 2\n"
        "s_lshl_b32 s7, s7, 10\n"
        "s_add_u32 s73, s7, 18528\n"
        "v_add_u32 v27, s7, v57\n"
        "s_barrier\n"
        "s_mov_b32 m0, s73\n"
        "s_nop 0\n"
        "global_load_lds_dwordx4 v27, s[4:5]\n"
        "global_load_dwordx4 v[240:243], v57, s[22:23] offset:-3072\n"
        "global_load_dwordx4 v[244:247], v57, s[22:23] offset:-2048\n"
        "s_add_u32 m0, s64, 0\n"
        "s_nop 0\n"
        "buffer_load_dwordx4 v58, s[24:27], s28 offen lds\n"
        "s_add_u32 m0, s64, 1024\n"
        "s_nop 0\n"
        "buffer_load_dwordx4 v59, s[24:27], s28 offen lds\n"
        "s_add_u32 m0, s64, 2048\n"
        "s_nop 0\n"
        "buffer_load_dwordx4 v60, s[24:27], s28 offen lds\n"
        "s_add_u32 m0, s64, 3072\n"
        "s_nop 0\n"
        "buffer_load_dwordx4 v58, s[24:27], s29 offen lds\n"
        "s_add_u32 m0, s64, 4096\n"
        "s_nop 0\n"
        "buffer_load_dwordx4 v59, s[24:27], s29 offen lds\n"
        "s_add_u32 m0, s64, 5120\n"
        "s_nop 0\n"
        "buffer_load_dwordx4 v60, s[24:27], s29 offen lds\n"
        "s_add_u32 m0, s64, 6144\n"
        "s_nop 0\n"
        "buffer_load_dwordx4 v58, s[24:27], s60 offen lds\n"
        "s_add_u32 m0, s64, 7168\n"
        "s_nop 0\n"
        "buffer_load_dwordx4 v59, s[24:27], s60 offen lds\n"
        "s_add_u32 m0, s64, 8192\n"
        "s_nop 0\n"
        "buffer_load_dwordx4 v60, s[24:27], s60 offen lds\n"
        "s_cmp_lt_u32 s6, 2\n"
        "s_cselect_b32 s62, 3, 16\n"
        "s_cselect_b32 s63, 12, 32\n"
        "s_cselect_b64 s[24:25], s[50:51], s[52:53]\n"
        "s_cselect_b32 s64, 0, 117088\n"
        "s_mul_i32 s62, s62, 192\n"
        "s_mul_i32 s63, s63, 192\n"
        "s_and_b32 s25, s25, 0xffff\n"
        "s_mul_i32 s7, s6, 9216\n"
        "s_add_u32 s64, s64, s7\n"
        "s_add_u32 s64, s64, 0\n"
        "s_add_u32 s7, s62, s63\n"
        "s_lshl_b32 s29, s63, 1\n"
        "s_add_u32 s60, s7, s63\n"
        "s_bitcmp1_b32 s6, 0\n"
        "s_cselect_b32 s28, s7, 0\n"
        "s_cselect_b32 s29, s29, s62\n"
        "s_cselect_b32 s60, s60, s63\n"
        "s_add_u32 s28, s28, s61\n"
        "s_add_u32 s29, s29, s61\n"
        "s_add_u32 s60, s60, s61\n"
        "s_add_u32 m0, s64, 0\n"
        "s_nop 0\n"
        "buffer_load_dwordx4 v32, s[24:27], s28 offen lds\n"
        "s_add_u32 m0, s64, 1024\n"
        "s_nop 0\n"
        "buffer_load_dwordx4 v33, s[24:27], s28 offen lds\n"
        "s_add_u32 m0, s64, 2048\n"
        "s_nop 0\n"
        "buffer_load_dwordx4 v34, s[24:27], s28 offen lds\n"
        "s_add_u32 m0, s64, 3072\n"
        "s_nop 0\n"
        "buffer_load_dwordx4 v32, s[24:27], s29 offen lds\n"
        "s_add_u32 m0, s64, 4096\n"
        "s_nop 0\n"
        "buffer_load_dwordx4 v33, s[24:27], s29 offen lds\n"
        "s_add_u32 m0, s64, 5120\n"
        "s_nop 0\n"
        "buffer_load_dwordx4 v34, s[24:27], s29 offen lds\n"
        "s_add_u32 m0, s64, 6144\n"
        "s_nop 0\n"
        "buffer_load_dwordx4 v32, s[24:27], s60 offen lds\n"
        "s_add_u32 m0, s64, 7168\n"
        "s_nop 0\n"
        "buffer_load_dwordx4 v33, s[24:27], s60 offen lds\n"
        "s_add_u32 m0, s64, 8192\n"
        "s_nop 0\n"
        "buffer_load_dwordx4 v34, s[24:27], s60 offen lds\n"
        "s_setprio 2\n"
        "s_waitcnt vmcnt(9)\n"
        "s_mov_b64 exec, 1\n"
        "ds_inc_u32 v250, v250 offset:20\n"
        "s_mov_b64 exec, -1\n"
        "L80_%=:\n"
        "ds_read_b32 v251, v250 offset:20\n"
        "s_waitcnt lgkmcnt(0)\n"
        "v_readfirstlane_b32 s40, v251\n"
        "s_nop 3\n"
        "s_cmp_ge_i32 s40, 4\n"
        "s_cbranch_scc1 L81_%=\n"
        "s_nop 0\n"
        "s_sub_u32 s41, s41, 1\n"
        "s_cbranch_scc0 L80_%=\n"
        "s_endpgm\n"
        "L81_%=:\n"
        "s_add_u32 m0, s17, 0\n"
        "s_nop 0\n"
        "buffer_load_dwordx4 v29, s[8:11], s74 offen lds\n"
        "buffer_load_dwordx4 v30, s[8:11], s74 offen offset:1024 lds\n"
        "buffer_load_dwordx4 v31, s[8:11], s74 offen offset:2048 lds\n"
        "s_add_u32 m0, s17, 3104\n"
        "s_nop 0\n"
        "buffer_load_dwordx4 v29, s[8:11], s75 offen lds\n"
        "buffer_load_dwordx4 v30, s[8:11], s75 offen offset:1024 lds\n"
        "buffer_load_dwordx4 v31, s[8:11], s75 offen offset:2048 lds\n"
        "s_cmp_eq_u32 s6, 2\n"
        "s_cbranch_scc1 L82_%=\n"
        "s_mov_b32 s34, 0x411cc471\n"
        "s_mov_b32 s35, 110080\n"
        "s_mov_b32 s36, 110072\n"
        "s_mov_b32 s37, 110076\n"
        "s_call_b64 s[30:31], FOLDW_%=\n"
        "s_branch L83_%=\n"
        "L82_%=:\n"
        "s_waitcnt vmcnt(6)\n"
        "s_mov_b64 exec, 1\n"
        "ds_inc_u32 v250, v250 offset:32\n"
        "s_mov_b64 exec, -1\n"
        "s_mov_b64 exec, 1\n"
        "ds_inc_u32 v250, v250 offset:28\n"
        "s_mov_b64 exec, -1\n"
        "global_load_dwordx4 v[40:43], v57, s[22:23] offset:-3072\n"
        "global_load_dwordx4 v[44:47], v57, s[22:23] offset:-2048\n"
        "global_load_dwordx4 v[0:3], v57, s[22:23] offset:-1024\n"
        "global_load_dwordx4 v[4:7], v57, s[22:23] offset:0\n"
        "global_load_dwordx4 v[8:11], v57, s[22:23] offset:1024\n"
        "global_load_dwordx4 v[12:15], v57, s[22:23] offset:2048\n"
        "global_load_dwordx4 v[16:19], v57, s[22:23] offset:3072\n"
        "L83_%=:\n"
        "s_mov_b64 s[24:25], s[56:57]\n"
        "s_mov_b32 s44, 0xf0000f\n"
        "s_mov_b32 s45, 0xf0000f00\n"
        "s_mov_b32 s46, 0xff000ff\n"
        "s_mov_b32 s47, 0xf00fff00\n"
        "s_mov_b32 s48, 0x22222222\n"
        "s_mov_b32 s49, 0x22222222\n"
        "s_mov_b32 s50, 0x44444444\n"
        "s_mov_b32 s51, 0x44444444\n"
        "s_mov_b32 s52, 0x88888888\n"
        "s_mov_b32 s53, 0x88888888\n"
        "s_mov_b64 vcc, s[44:45]\n"
        "s_mov_b32 s28, 0x3c2aaaab\n"
        "s_mov_b32 s29, 0x3a7ba882\n"
        "s_mov_b32 s30, 0\n"
        "s_mov_b32 s34, 0\n"
        "s_mov_b32 s31, s18\n"
        "s_mov_b32 s36, s18\n"
        "s_mov_b32 s37, s18\n"
        "s_add_u32 s71, s15, 588672\n"
        "s_sub_u32 s72, 73, s16\n"
        "s_add_u32 s73, s17, 3104\n"
        "s_mov_b32 s68, 4\n"
        "s_mov_b32 s76, 16\n"
        "s_mov_b32 s74, -1\n"
        "s_mov_b32 s41, 0x400000\n"
        "L23_%=:\n"
        "ds_read_b32 v251, v250 offset:28\n"
        "s_waitcnt lgkmcnt(0)\n"
        "v_readfirstlane_b32 s40, v251\n"
        "s_nop 3\n"
        "s_cmp_ge_i32 s40, 7\n"
        "s_cbranch_scc1 L24_%=\n"
        "s_nop 0\n"
        "s_sub_u32 s41, s41, 1\n"
        "s_cbranch_scc0 L23_%=\n"
        "s_endpgm\n"
        "L24_%=:\n"
        "s_waitcnt vmcnt(0)\n"
        "v_readfirstlane_b32 s26, v253\n"
        "ds_read_b64 v[92:93], v14\n"
        "ds_read_b64 v[68:69], v14\n"
        "ds_read_b64 v[72:73], v14\n"
        "ds_read_b64 v[86:87], v14\n"
        "ds_read_b128 v[20:23], v15\n"
        "v_mov_b32 v15, 110048\n"
        "v_add_u32 v62, 21600, v13\n"
        "ds_read_b128 v[112:115], v62 offset:0\n"
        "ds_read_b128 v[116:119], v62 offset:1024\n"
        "ds_read_b128 v[120:123], v62 offset:2048\n"
        "ds_read_b128 v[124:127], v62 offset:3072\n"
        "ds_read_b128 v[128:131], v62 offset:4096\n"
        "ds_read_b128 v[132:135], v62 offset:5120\n"
        "ds_read_b128 v[136:139], v62 offset:6144\n"
        "ds_read_b128 v[140:143], v62 offset:7168\n"
        "ds_read_b128 v[144:147], v62 offset:8192\n"
        "ds_read_b128 v[148:151], v62 offset:9216\n"
        "ds_read_b128 v[152:155], v62 offset:10240\n"
        "ds_read_b128 v[156:159], v62 offset:11264\n"
        "ds_read_b128 v[160:163], v62 offset:12288\n"
        "ds_read_b128 v[164:167], v62 offset:13312\n"
        "ds_read_b128 v[168:171], v62 offset:14336\n"
        "ds_read_b128 v[172:175], v62 offset:15360\n"
        "ds_read_b128 v[176:179], v62 offset:16384\n"
        "ds_read_b128 v[180:183], v62 offset:17408\n"
        "ds_read_b128 v[184:187], v62 offset:18432\n"
        "ds_read_b128 v[188:191], v62 offset:19456\n"
        "ds_read_b128 v[192:195], v62 offset:20480\n"
        "ds_read_b128 v[196:199], v62 offset:21520\n"
        "ds_read_b128 v[200:203], v62 offset:22544\n"
        "ds_read_b128 v[204:207], v62 offset:23568\n"
        "ds_read_b128 v[208:211], v62 offset:24608\n"
        "ds_read_b128 v[212:215], v62 offset:25632\n"
        "ds_read_b128 v[216:219], v62 offset:26656\n"
        "ds_read_b128 v[220:223], v62 offset:27696\n"
        "ds_read_b128 v[224:227], v62 offset:28720\n"
        "ds_read_b128 v[228:231], v62 offset:29744\n"
        "ds_read_b128 v[232:235], v62 offset:30784\n"
        "ds_read_b128 v[236:239], v62 offset:31808\n"
        "ds_read_b128 v[240:243], v62 offset:32832\n"
        "ds_read_b128 v[244:247], v62 offset:33872\n"
        "ds_read_b128 v[248:251], v62 offset:34896\n"
        "ds_read_b128 v[252:255], v62 offset:35920\n"
        "ds_read_b128 v[44:47], v0 offset:0\n"
        "ds_read_b128 v[50:53], v0 offset:64\n"
        "ds_read_b128 v[56:59], v0 offset:128\n"
        "ds_read_b128 v[26:29], v0 offset:768\n"
        "ds_read_b128 v[32:35], v0 offset:832\n"
        "ds_read_b128 v[38:41], v0 offset:896\n"
        "s_add_u32 s37, s37, 4\n"
        "s_cmp_ge_u32 s37, 49\n"
        "s_cselect_b32 s69, 49, 0\n"
        "s_sub_u32 s37, s37, s69\n"
        "s_sub_u32 s69, s37, 5\n"
        "s_cmp_lt_u32 s69, 4\n"
        "s_subb_u32 s36, s36, -4\n"
        "s_cmp_ge_u32 s36, 6\n"
        "s_cselect_b32 s69, 6, 0\n"
        "s_addc_u32 s34, s34, 0\n"
        "s_sub_u32 s36, s36, s69\n"
        "s_mul_i32 s38, s34, 1344\n"
        "s_cmp_ge_u32 s34, s76\n"
        "s_cbranch_scc0 L27_%=\n"
        "s_nop 1\n"
        "v_mov_b32 v64, 110048\n"
        "L25_%=:\n"
        "ds_read_b32 v65, v64 offset:64\n"
        "s_waitcnt lgkmcnt(0)\n"
        "v_readfirstlane_b32 s40, v65\n"
        "s_nop 3\n"
        "s_cmp_ge_i32 s40, 1\n"
        "s_cbranch_scc1 L26_%=\n"
        "s_sleep 1\n"
        "s_sub_u32 s41, s41, 1\n"
        "s_cbranch_scc0 L25_%=\n"
        "s_endpgm\n"
        "L26_%=:\n"
        "v_mbcnt_lo_u32_b32 v65, -1, 0\n"
        "v_lshl_add_u32 v64, v65, 2, v64\n"
        "ds_read_b32 v65, v64 offset:68\n"
        "s_mov_b32 s14, s40\n"
        "s_mov_b32 s76, -1\n"
        "s_waitcnt lgkmcnt(0)\n"
        "v_readlane_b32 s22, v65, 0\n"
        "v_readlane_b32 s23, v65, 1\n"
        "s_nop 3\n"
        "L27_%=:\n"
        "s_cmp_ge_u32 s34, s72\n"
        "s_cselect_b32 s70, s71, s15\n"
        "s_cmp_ge_u32 s34, 20\n"
        "s_cselect_b32 s70, s22, s70\n"
        "s_cmp_ge_u32 s34, 21\n"
        "s_cselect_b32 s70, s23, s70\n"
        "s_add_u32 s38, s38, s70\n"
        "s_mul_i32 s69, s36, 98304\n"
        "s_add_u32 s38, s38, s69\n"
        "s_mov_b32 s35, s36\n"
        "s_mul_i32 s69, s36, -12\n"
        "s_add_u32 s69, s69, 60\n"
        "s_lshl_b64 s[54:55], -1, s69\n"
        "s_lshl_b64 s[56:57], s[54:55], 24\n"
        "s_ashr_i64 s[60:61], s[54:55], 8\n"
        "s_mov_b32 s58, s61\n"
        "s_mov_b32 s59, s56\n"
        "v_cndmask_b32_e64 v97, v4, v7, s[56:57]\n"
        "v_cndmask_b32_e64 v25, v5, v8, s[58:59]\n"
        "v_cndmask_b32_e64 v96, v6, v9, s[60:61]\n"
        "s_cmp_eq_u32 s37, 4\n"
        "s_cbranch_scc0 L28_%=\n"
        "s_nop 1\n"
        "ds_read_b32 v97, v13 offset:19552\n"
        "ds_read_b32 v25, v13 offset:19556\n"
        "ds_read_b32 v96, v13 offset:19560\n"
        "s_mov_b32 s35, 6\n"
        "s_mov_b32 s38, 0\n"
        "s_mov_b32 s69, 1344\n"
        "s_waitcnt lgkmcnt(0)\n"
        "v_lshrrev_b32 v64, 28, v97\n"
        "v_add_u32 v64, s34, v64\n"
        "v_and_b32 v97, 0x0fffffff, v97\n"
        "v_mad_u32_u24 v97, v64, s69, v97\n"
        "v_mov_b32 v65, s15\n"
        "v_cmpx_le_u32 vcc, s72, v64\n"
        "v_mov_b32 v65, s71\n"
        "s_mov_b64 exec, -1\n"
        "v_cmpx_le_u32 vcc, 20, v64\n"
        "v_mov_b32 v65, s22\n"
        "s_mov_b64 exec, -1\n"
        "v_cmpx_le_u32 vcc, 21, v64\n"
        "v_mov_b32 v65, s23\n"
        "s_mov_b64 exec, -1\n"
        "v_add_u32 v97, v97, v65\n"
        "v_lshrrev_b32 v64, 28, v25\n"
        "v_add_u32 v64, s34, v64\n"
        "v_and_b32 v25, 0x0fffffff, v25\n"
        "v_mad_u32_u24 v25, v64, s69, v25\n"
        "v_mov_b32 v65, s15\n"
        "v_cmpx_le_u32 vcc, s72, v64\n"
        "v_mov_b32 v65, s71\n"
        "s_mov_b64 exec, -1\n"
        "v_cmpx_le_u32 vcc, 20, v64\n"
        "v_mov_b32 v65, s22\n"
        "s_mov_b64 exec, -1\n"
        "v_cmpx_le_u32 vcc, 21, v64\n"
        "v_mov_b32 v65, s23\n"
        "s_mov_b64 exec, -1\n"
        "v_add_u32 v25, v25, v65\n"
        "v_lshrrev_b32 v64, 28, v96\n"
        "v_add_u32 v64, s34, v64\n"
        "v_and_b32 v96, 0x0fffffff, v96\n"
        "v_mad_u32_u24 v96, v64, s69, v96\n"
        "v_mov_b32 v65, s15\n"
        "v_cmpx_le_u32 vcc, s72, v64\n"
        "v_mov_b32 v65, s71\n"
        "s_mov_b64 exec, -1\n"
        "v_cmpx_le_u32 vcc, 20, v64\n"
        "v_mov_b32 v65, s22\n"
        "s_mov_b64 exec, -1\n"
        "v_cmpx_le_u32 vcc, 21, v64\n"
        "v_mov_b32 v65, s23\n"
        "s_mov_b64 exec, -1\n"
        "v_add_u32 v96, v96, v65\n"
        "s_nop 4\n"
        "s_mov_b64 vcc, s[44:45]\n"
        "L28_%=:\n"
        "s_cmp_lt_u32 s34, s14\n"
        "s_cselect_b32 s38, s38, 0x80000000\n"
        "s_add_u32 s39, s38, 100270464\n"
        "s_waitcnt lgkmcnt(0)\n"
        "s_mov_b64 exec, 1\n"
        "ds_inc_u32 v15, v10 offset:8\n"
        "s_mov_b64 exec, -1\n"
        "s_cmp_eq_u32 s18, 0\n"
        "s_cselect_b32 s75, -20, 8\n"
        "s_setprio 1\n"
        "PTOP_%=:\n"
        "s_waitcnt lgkmcnt(5)\n"
        "v_mfma_f32_16x16x32_bf16 v[60:63], v[112:115], v[44:47], 0\n"
        "s_mov_b32 m0, s17\n"
        "v_cndmask_b32_e64 v110, v74, v75, s[48:49]\n"
        "s_nop 1\n"
        "v_cndmask_b32_e64 v95, v78, v79, s[48:49]\n"
        "s_waitcnt lgkmcnt(3)\n"
        "v_mfma_f32_16x16x32_bf16 v[60:63], v[116:119], v[50:53], v[60:63]\n"
        "ds_read_b32 v111, v15 offset:8\n"
        "v_cndmask_b32_e64 v110, v110, v76, s[50:51]\n"
        "s_waitcnt lgkmcnt(2)\n"
        "v_mfma_f32_16x16x32_bf16 v[60:63], v[120:123], v[56:59], v[60:63]\n"
        "s_waitcnt lgkmcnt(1)\n"
        "buffer_load_dwordx4 v97, s[8:11], s38 offen lds\n"
        "v_mfma_f32_16x16x32_bf16 v[64:67], v[132:135], v[56:59], 0\n"
        "v_cndmask_b32_e64 v95, v95, v80, s[50:51]\n"
        "v_cndmask_b32_e64 v110, v110, v77, s[52:53]\n"
        "v_mfma_f32_16x16x32_bf16 v[64:67], v[128:131], v[50:53], v[64:67]\n"
        "s_setprio 3 \n"
        "v_cndmask_b32_e64 v95, v95, v81, s[52:53]\n"
        "v_mfma_f32_16x16x32_bf16 v[64:67], v[124:127], v[44:47], v[64:67]\n"
        "buffer_load_dwordx4 v25, s[8:11], s38 offen offset:1024 lds\n"
        "v_mfma_f32_16x16x32_bf16 v[74:77], v[136:139], v[44:47], 0\n"
        "s_waitcnt lgkmcnt(0)\n"
        "v_readfirstlane_b32 s40, v111\n"
        "v_cndmask_b32_dpp v110, v95, v110, vcc row_ror:4 row_mask:0xf bank_mask:0xf\n"
        "v_mfma_f32_16x16x32_bf16 v[74:77], v[140:143], v[50:53], v[74:77]\n"
        "s_cmp_lt_i32 s40, s68\n"
        "v_mfma_f32_16x16x32_bf16 v[74:77], v[144:147], v[56:59], v[74:77]\n"
        "s_cbranch_scc1 L3_%=\n"
        "L4_%=:\n"
        "ds_write_b128 v11, v[102:105]\n"
        "s_mov_b64 exec, s[46:47]\n"
        "ds_write_b32 v12, v110\n"
        "s_mov_b64 exec, -1\n"
        "v_mfma_f32_16x16x32_bf16 v[78:81], v[156:159], v[56:59], 0\n"
        "v_mfma_f32_16x16x32_bf16 v[78:81], v[152:155], v[50:53], v[78:81]\n"
        "v_mfma_f32_16x16x32_bf16 v[78:81], v[148:151], v[44:47], v[78:81]\n"
        "buffer_load_dwordx4 v96, s[8:11], s38 offen offset:2048 lds\n"
        "s_mov_b32 m0, s73\n"
        "ds_write_b128 v11, v[98:101] offset:832\n"
        "s_cmp_eq_u32 s74, s30\n"
        "v_mfma_f32_16x16x32_bf16 v[98:101], v[160:163], v[44:47], 0\n"
        "s_setprio 3\n"
        "s_cbranch_scc1 L5_%=\n"
        "s_setprio 1\n"
        "L5_%=:\n"
        "v_mfma_f32_16x16x32_bf16 v[98:101], v[164:167], v[50:53], v[98:101]\n"
        "v_mfma_f32_16x16x32_bf16 v[98:101], v[168:171], v[56:59], v[98:101]\n"
        "buffer_load_dwordx4 v97, s[8:11], s39 offen lds\n"
        "ds_write_b128 v11, v[88:91] offset:64\n"
        "v_mfma_f32_16x16x32_bf16 v[102:105], v[180:183], v[56:59], 0\n"
        "v_mfma_f32_16x16x32_bf16 v[102:105], v[176:179], v[50:53], v[102:105]\n"
        "v_mfma_f32_16x16x32_bf16 v[102:105], v[172:175], v[44:47], v[102:105]\n"
        "buffer_load_dwordx4 v25, s[8:11], s39 offen offset:1024 lds\n"
        "ds_write_b128 v11, v[82:85] offset:896\n"
        "v_mfma_f32_16x16x32_bf16 v[88:91], v[44:47], v[44:47], 0\n"
        "v_mfma_f32_16x16x32_bf16 v[88:91], v[50:53], v[50:53], v[88:91]\n"
        "v_mfma_f32_16x16x32_bf16 v[88:91], v[56:59], v[56:59], v[88:91]\n"
        "buffer_load_dwordx4 v96, s[8:11], s39 offen offset:2048 lds\n"
        "ds_write_b128 v11, v[16:19] offset:128\n"
        "v_mov_b64 v[16:17], 0\n"
        "v_mov_b64 v[18:19], 0\n"
        "v_mfma_f32_16x16x32_bf16 v[94:97], v[26:29], v[26:29], 0\n"
        "v_mfma_f32_16x16x32_bf16 v[94:97], v[32:35], v[32:35], v[94:97]\n"
        "v_mfma_f32_16x16x32_bf16 v[94:97], v[38:41], v[38:41], v[94:97]\n"
        "ds_write_b128 v11, v[106:109] offset:960\n"
        "v_smfmac_f32_16x16x64_bf16 v[16:19], v[44:47], v[60:67], v10\n"
        "v_cndmask_b32_e64 v85, v88, v89, s[48:49]\n"
        "v_smfmac_f32_16x16x64_bf16 v[16:19], v[50:53], v[74:81], v10\n"
        "v_cndmask_b32_e64 v85, v85, v90, s[50:51]\n"
        "v_smfmac_f32_16x16x64_bf16 v[16:19], v[56:59], v[98:105], v10\n"
        "s_mov_b64 exec, 1\n"
        "ds_inc_u32 v15, v10 offset:0\n"
        "s_mov_b64 exec, -1\n"
        "v_mfma_f32_16x16x32_bf16 v[76:79], v[112:115], v[26:29], 0\n"
        "v_cndmask_b32_e64 v111, v94, v95, s[48:49]\n"
        "v_cndmask_b32_e64 v111, v111, v96, s[50:51]\n"
        "v_mfma_f32_16x16x32_bf16 v[76:79], v[116:119], v[32:35], v[76:79]\n"
        "v_cndmask_b32_e64 v85, v85, v91, s[52:53]\n"
        "v_cndmask_b32_e64 v111, v111, v97, s[52:53]\n"
        "v_mfma_f32_16x16x32_bf16 v[76:79], v[120:123], v[38:41], v[76:79]\n"
        "v_mfma_f32_16x16x32_bf16 v[80:83], v[132:135], v[38:41], 0\n"
        "v_cndmask_b32_dpp v85, v111, v85, vcc row_ror:4 row_mask:0xf bank_mask:0xf\n"
        "v_mfma_f32_16x16x32_bf16 v[80:83], v[128:131], v[32:35], v[80:83]\n"
        "v_mfma_f32_16x16x32_bf16 v[80:83], v[124:127], v[26:29], v[80:83]\n"
        "v_mfma_f32_16x16x32_bf16 v[96:99], v[136:139], v[26:29], 0\n"
        "v_mfma_f32_16x16x32_bf16 v[96:99], v[140:143], v[32:35], v[96:99]\n"
        "v_mfma_f32_16x16x32_bf16 v[96:99], v[144:147], v[38:41], v[96:99]\n"
        "v_mfma_f32_16x16x32_bf16 v[100:103], v[156:159], v[38:41], 0\n"
        "v_mfma_f32_16x16x32_bf16 v[100:103], v[152:155], v[32:35], v[100:103]\n"
        "v_mfma_f32_16x16x32_bf16 v[100:103], v[148:151], v[26:29], v[100:103]\n"
        "v_mfma_f32_16x16x32_bf16 v[104:107], v[160:163], v[26:29], 0\n"
        "v_mfma_f32_16x16x32_bf16 v[104:107], v[164:167], v[32:35], v[104:107]\n"
        "s_and_b32 s69, s30, 1\n"
        "v_mfma_f32_16x16x32_bf16 v[104:107], v[168:171], v[38:41], v[104:107]\n"
        "s_lshl2_add_u32 s70, s69, 110048\n"
        "v_mfma_f32_16x16x32_bf16 v[108:111], v[180:183], v[38:41], 0\n"
        "v_mov_b32 v15, s70\n"
        "v_mfma_f32_16x16x32_bf16 v[108:111], v[176:179], v[32:35], v[108:111]\n"
        "s_mul_i32 s69, s69, 40064\n"
        "v_mfma_f32_16x16x32_bf16 v[108:111], v[172:175], v[26:29], v[108:111]\n"
        "s_mul_i32 s70, s31, 1664\n"
        "v_mfma_f32_16x16x32_bf16 v[64:67], v[20:23], v[44:47], 0\n"
        "s_add_u32 s69, s69, s70\n"
        "v_mfma_f32_16x16x32_bf16 v[64:67], v[20:23], v[50:53], v[64:67]\n"
        "v_add_u32 v11, s69, v3\n"
        "v_mfma_f32_16x16x32_bf16 v[64:67], v[20:23], v[56:59], v[64:67]\n"
        "v_add_u32 v12, s69, v2\n"
        "v_mov_b64 v[88:89], 0\n"
        "v_mfma_f32_16x16x32_bf16 v[60:63], v[20:23], v[26:29], 0\n"
        "s_lshr_b32 s68, s30, 1\n"
        "v_mov_b64 v[90:91], 0\n"
        "v_mfma_f32_16x16x32_bf16 v[60:63], v[20:23], v[32:35], v[60:63]\n"
        "s_lshl2_add_u32 s68, s68, 4\n"
        "v_mfma_f32_16x16x32_bf16 v[60:63], v[20:23], v[38:41], v[60:63]\n"
        "s_cmp_eq_u32 s31, 6\n"
        "v_smfmac_f32_16x16x64_bf16 v[88:91], v[26:29], v[76:83], v10\n"
        "s_cbranch_scc1 L8_%=\n"
        "L9_%=:\n"
        "v_smfmac_f32_16x16x64_bf16 v[88:91], v[32:35], v[96:103], v10\n"
        "s_mov_b32 s74, s30\n"
        "v_smfmac_f32_16x16x64_bf16 v[88:91], v[38:41], v[104:111], v10\n"
        "s_mov_b32 s30, s34\n"
        "v_mfma_f32_16x16x32_bf16 v[76:79], v[38:41], v[192:195], 0\n"
        "s_mov_b32 s31, s35\n"
        "v_cndmask_b32_dpp v110, v60, v64, vcc row_ror:4 row_mask:0xf bank_mask:0xf\n"
        "v_mul_f32 v111, s28, v110\n"
        "v_mfma_f32_16x16x32_bf16 v[76:79], v[32:35], v[188:191], v[76:79]\n"
        "s_add_u32 s37, s37, 4\n"
        "v_fma_f32 v111, -v111, v110, v85\n"
        "v_add_f32_e64 v111, s29, |v111|\n"
        "v_mfma_f32_16x16x32_bf16 v[76:79], v[26:29], v[184:187], v[76:79]\n"
        "s_cmp_ge_u32 s37, 49\n"
        "v_rsq_f32 v107, v111\n"
        "v_cndmask_b32_dpp v98, v88, v16, vcc row_ror:4 row_mask:0xf bank_mask:0xf\n"
        "v_mfma_f32_16x16x32_bf16 v[108:111], v[56:59], v[192:195], 0\n"
        "s_cselect_b32 s69, 49, 0\n"
        "v_cndmask_b32_dpp v96, v89, v17, vcc row_ror:4 row_mask:0xf bank_mask:0xf\n"
        "v_cndmask_b32_dpp v99, v90, v18, vcc row_ror:4 row_mask:0xf bank_mask:0xf\n"
        "v_mfma_f32_16x16x32_bf16 v[108:111], v[50:53], v[188:191], v[108:111]\n"
        "s_sub_u32 s37, s37, s69\n"
        "v_mul_f32_dpp v98, v107, v98 quad_perm:[0,0,0,0] row_mask:0xf bank_mask:0xf\n"
        "v_mul_f32_dpp v96, v107, v96 quad_perm:[1,1,1,1] row_mask:0xf bank_mask:0xf\n"
        "v_mfma_f32_16x16x32_bf16 v[108:111], v[44:47], v[184:187], v[108:111]\n"
        "s_sub_u32 s69, s37, 5\n"
        "v_mul_f32_dpp v99, v107, v99 quad_perm:[2,2,2,2] row_mask:0xf bank_mask:0xf\n"
        "v_cndmask_b32_dpp v97, v91, v19, vcc row_ror:4 row_mask:0xf bank_mask:0xf\n"
        "v_mfma_f32_16x16x32_bf16 v[60:63], v[44:47], v[220:223], 0\n"
        "s_cmp_lt_u32 s69, 4\n"
        "v_mul_f32_dpp v97, v107, v97 quad_perm:[3,3,3,3] row_mask:0xf bank_mask:0xf\n"
        "v_max3_f32 v106, v98, v96, v99\n"
        "v_mfma_f32_16x16x32_bf16 v[60:63], v[50:53], v[224:227], v[60:63]\n"
        "s_subb_u32 s36, s36, -4\n"
        "v_max_f32 v106, v106, v97\n"
        "v_mul_f32 v106, v106, v107\n"
        "v_mfma_f32_16x16x32_bf16 v[60:63], v[56:59], v[228:231], v[60:63]\n"
        "s_cmp_ge_u32 s36, 6\n"
        "v_fma_f32 v98, v98, v107, -v106\n"
        "v_fma_f32 v96, v96, v107, -v106\n"
        "v_fma_f32 v99, v99, v107, -v106\n"
        "v_mfma_f32_16x16x32_bf16 v[100:103], v[26:29], v[220:223], 0\n"
        "s_cselect_b32 s69, 6, 0\n"
        "v_fma_f32 v97, v97, v107, -v106\n"
        "v_cvt_pk_bf16_f32 v90, v108, v109\n"
        "v_mfma_f32_16x16x32_bf16 v[100:103], v[32:35], v[224:227], v[100:103]\n"
        "s_addc_u32 s34, s34, 0\n"
        "v_exp_f32 v98, v98\n"
        "v_mfma_f32_16x16x32_bf16 v[100:103], v[38:41], v[228:231], v[100:103]\n"
        "s_sub_u32 s36, s36, s69\n"
        "v_exp_f32 v96, v96\n"
        "v_mfma_f32_16x16x32_bf16 v[16:19], v[38:41], v[204:207], 0\n"
        "s_mul_i32 s38, s34, 1344\n"
        "v_exp_f32 v99, v99\n"
        "v_mfma_f32_16x16x32_bf16 v[16:19], v[32:35], v[200:203], v[16:19]\n"
        "s_cmp_ge_u32 s34, s76\n"
        "v_exp_f32 v97, v97\n"
        "v_mfma_f32_16x16x32_bf16 v[16:19], v[26:29], v[196:199], v[16:19]\n"
        "s_cbranch_scc1 L10_%=\n"
        "L11_%=:\n"
        "v_add_f32 v109, v98, v96\n"
        "v_add_f32 v108, v99, v97\n"
        "v_add_f32 v109, v109, v108\n"
        "v_mfma_f32_16x16x32_bf16 v[82:85], v[56:59], v[204:207], 0\n"
        "s_cmp_ge_u32 s34, s72\n"
        "v_rcp_f32 v109, v109\n"
        "v_mfma_f32_16x16x32_bf16 v[82:85], v[50:53], v[200:203], v[82:85]\n"
        "s_cselect_b32 s70, s71, s15\n"
        "v_mul_f32_dpp v98, v107, v98 quad_perm:[0,0,0,0] row_mask:0xf bank_mask:0xf\n"
        "v_mul_f32_dpp v96, v107, v96 quad_perm:[1,1,1,1] row_mask:0xf bank_mask:0xf\n"
        "v_mfma_f32_16x16x32_bf16 v[82:85], v[44:47], v[196:199], v[82:85]\n"
        "s_cmp_ge_u32 s34, 20\n"
        "v_mul_f32_dpp v99, v107, v99 quad_perm:[2,2,2,2] row_mask:0xf bank_mask:0xf\n"
        "v_mul_f32_dpp v97, v107, v97 quad_perm:[3,3,3,3] row_mask:0xf bank_mask:0xf\n"
        "v_mfma_f32_16x16x32_bf16 v[104:107], v[44:47], v[232:235], 0\n"
        "s_cselect_b32 s70, s22, s70\n"
        "v_mul_f32 v98, v98, v109\n"
        "v_mul_f32 v96, v96, v109\n"
        "v_mfma_f32_16x16x32_bf16 v[104:107], v[50:53], v[236:239], v[104:107]\n"
        "s_cmp_ge_u32 s34, 21\n"
        "v_mul_f32 v99, v99, v109\n"
        "v_mul_f32 v97, v97, v109\n"
        "v_mfma_f32_16x16x32_bf16 v[104:107], v[56:59], v[240:243], v[104:107]\n"
        "s_cselect_b32 s70, s23, s70\n"
        "v_cvt_pk_bf16_f32 v109, v98, v96\n"
        "v_cvt_pk_bf16_f32 v108, v99, v97\n"
        "v_and_b32 v42, v1, v109\n"
        "v_mfma_f32_16x16x32_bf16 v[96:99], v[26:29], v[232:235], 0\n"
        "s_add_u32 s38, s38, s70\n"
        "v_and_b32 v43, v1, v108\n"
        "v_cvt_pk_bf16_f32 v91, v110, v111\n"
        "v_mfma_f32_16x16x32_bf16 v[96:99], v[32:35], v[236:239], v[96:99]\n"
        "s_mul_i32 s69, s36, 98304\n"
        "v_and_b32_dpp v24, v109, v1 row_ror:12 row_mask:0xf bank_mask:0xf\n"
        "v_and_b32_dpp v25, v108, v1 row_ror:12 row_mask:0xf bank_mask:0xf\n"
        "v_mfma_f32_16x16x32_bf16 v[96:99], v[38:41], v[240:243], v[96:99]\n"
        "s_add_u32 s38, s38, s69\n"
        "v_cvt_pk_bf16_f32 v66, v76, v77\n"
        "v_cvt_pk_bf16_f32 v67, v78, v79\n"
        "v_mov_b64 v[54:55], v[42:43]\n"
        "v_mfma_f32_16x16x32_bf16 v[76:79], v[90:93], v[42:45], 0\n"
        "s_mov_b32 s35, s36\n"
        "v_cvt_pk_bf16_f32 v94, v60, v61\n"
        "v_cvt_pk_bf16_f32 v95, v62, v63\n"
        "v_mfma_f32_16x16x32_bf16 v[62:65], v[66:69], v[24:27], 0\n"
        "s_mul_i32 s69, s36, -12\n"
        "v_mov_b64 v[36:37], v[24:25]\n"
        "v_cvt_pk_bf16_f32 v70, v100, v101\n"
        "v_cvt_pk_bf16_f32 v71, v102, v103\n"
        "v_mfma_f32_16x16x32_bf16 v[108:111], v[92:95], v[52:55], 0\n"
        "s_add_u32 s69, s69, 60\n"
        "v_mov_b64 v[48:49], v[42:43]\n"
        "v_cvt_pk_bf16_f32 v74, v82, v83\n"
        "v_mfma_f32_16x16x32_bf16 v[80:83], v[68:71], v[34:37], 0\n"
        "s_lshl_b64 s[54:55], -1, s69\n"
        "v_cvt_pk_bf16_f32 v75, v84, v85\n"
        "v_mov_b64 v[30:31], v[24:25]\n"
        "v_mfma_f32_16x16x32_bf16 v[100:103], v[38:41], v[216:219], 0\n"
        "s_lshl_b64 s[56:57], s[54:55], 24\n"
        "v_cvt_pk_bf16_f32 v88, v16, v17\n"
        "v_cvt_pk_bf16_f32 v89, v18, v19\n"
        "v_mfma_f32_16x16x32_bf16 v[100:103], v[32:35], v[212:215], v[100:103]\n"
        "s_ashr_i64 s[60:61], s[54:55], 8\n"
        "v_cvt_pk_bf16_f32 v70, v104, v105\n"
        "v_cvt_pk_bf16_f32 v71, v106, v107\n"
        "v_mfma_f32_16x16x32_bf16 v[100:103], v[26:29], v[208:211], v[100:103]\n"
        "s_mov_b32 s58, s61\n"
        "v_cvt_pk_bf16_f32 v84, v96, v97\n"
        "v_cvt_pk_bf16_f32 v85, v98, v99\n"
        "v_mfma_f32_16x16x32_bf16 v[104:107], v[56:59], v[216:219], 0\n"
        "s_mov_b32 s59, s56\n"
        "v_mov_b64 v[60:61], v[42:43]\n"
        "v_mov_b64 v[42:43], v[24:25]\n"
        "v_mfma_f32_16x16x32_bf16 v[104:107], v[50:53], v[212:215], v[104:107]\n"
        "v_cndmask_b32_e64 v97, v4, v7, s[56:57]\n"
        "v_cvt_pk_bf16_f32 v66, v100, v101\n"
        "v_mfma_f32_16x16x32_bf16 v[104:107], v[44:47], v[208:211], v[104:107]\n"
        "v_cndmask_b32_e64 v25, v5, v8, s[58:59]\n"
        "v_cvt_pk_bf16_f32 v67, v102, v103\n"
        "v_cvt_pk_bf16_f32 v102, v76, v77\n"
        "v_mfma_f32_16x16x32_bf16 v[98:101], v[72:75], v[46:49], 0\n"
        "v_cndmask_b32_e64 v96, v6, v9, s[60:61]\n"
        "v_cvt_pk_bf16_f32 v103, v78, v79\n"
        "v_mfma_f32_16x16x32_bf16 v[16:19], v[86:89], v[28:31], 0\n"
        "s_cmp_eq_u32 s37, 4\n"
        "v_cvt_pk_bf16_f32 v90, v104, v105\n"
        "v_cvt_pk_bf16_f32 v91, v106, v107\n"
        "v_mfma_f32_16x16x32_bf16 v[76:79], v[44:47], v[244:247], 0\n"
        "s_cbranch_scc1 L12_%=\n"
        "L13_%=:\n"
        "v_cvt_pk_bf16_f32 v104, v108, v109\n"
        "v_cvt_pk_bf16_f32 v105, v110, v111\n"
        "v_mfma_f32_16x16x32_bf16 v[76:79], v[50:53], v[248:251], v[76:79]\n"
        "s_cmp_lt_u32 s34, s14\n"
        "v_cvt_pk_bf16_f32 v88, v98, v99\n"
        "v_cvt_pk_bf16_f32 v89, v100, v101\n"
        "v_mfma_f32_16x16x32_bf16 v[76:79], v[56:59], v[252:255], v[76:79]\n"
        "s_cselect_b32 s38, s38, 0x80000000\n"
        "v_cvt_pk_bf16_f32 v98, v62, v63\n"
        "v_cvt_pk_bf16_f32 v99, v64, v65\n"
        "v_cvt_pk_bf16_f32 v100, v80, v81\n"
        "v_mfma_f32_16x16x32_bf16 v[62:65], v[26:29], v[244:247], 0\n"
        "s_add_u32 s39, s38, 100270464\n"
        "v_cvt_pk_bf16_f32 v101, v82, v83\n"
        "v_cvt_pk_bf16_f32 v82, v16, v17\n"
        "v_mfma_f32_16x16x32_bf16 v[62:65], v[32:35], v[248:251], v[62:65]\n"
        "v_cvt_pk_bf16_f32 v94, v76, v77\n"
        "v_cvt_pk_bf16_f32 v95, v78, v79\n"
        "v_mfma_f32_16x16x32_bf16 v[62:65], v[38:41], v[252:255], v[62:65]\n"
        "v_cvt_pk_bf16_f32 v83, v18, v19\n"
        "v_mfma_f32_16x16x32_bf16 v[16:19], v[70:73], v[54:57], 0\n"
        "v_mfma_f32_16x16x32_bf16 v[74:77], v[84:87], v[36:39], 0\n"
        "v_mfma_f32_16x16x32_bf16 v[108:111], v[90:93], v[48:51], 0\n"
        "s_waitcnt vmcnt(0)\n"
        "ds_read_b128 v[44:47], v0\n"
        "ds_read_b128 v[26:29], v0 offset:768\n"
        "ds_read_b128 v[50:53], v0 offset:64\n"
        "v_cvt_pk_bf16_f32 v70, v62, v63\n"
        "v_mfma_f32_16x16x32_bf16 v[78:81], v[66:69], v[30:33], 0\n"
        "s_add_u32 s75, s75, 1\n"
        "ds_read_b128 v[32:35], v0 offset:832\n"
        "v_cvt_pk_bf16_f32 v71, v64, v65\n"
        "v_cvt_pk_bf16_f32 v90, v16, v17\n"
        "v_cvt_pk_bf16_f32 v91, v18, v19\n"
        "v_mfma_f32_16x16x32_bf16 v[64:67], v[92:95], v[58:61], 0\n"
        "s_cmp_lt_u32 s75, 4\n"
        "ds_read_b128 v[56:59], v0 offset:128\n"
        "v_cvt_pk_bf16_f32 v16, v108, v109\n"
        "v_cvt_pk_bf16_f32 v17, v110, v111\n"
        "v_cvt_pk_bf16_f32 v84, v74, v75\n"
        "v_mfma_f32_16x16x32_bf16 v[60:63], v[68:71], v[40:43], 0\n"
        "s_cbranch_scc1 L19_%=\n"
        "L20_%=:\n"
        "ds_read_b128 v[38:41], v0 offset:896\n"
        "v_cvt_pk_bf16_f32 v18, v64, v65\n"
        "v_cvt_pk_bf16_f32 v19, v66, v67\n"
        "v_cvt_pk_bf16_f32 v85, v76, v77\n"
        "v_mfma_f32_16x16x32_bf16 v[74:77], v[102:105], v[102:105], 0\n"
        "v_cvt_pk_bf16_f32 v106, v78, v79\n"
        "v_cvt_pk_bf16_f32 v107, v80, v81\n"
        "v_mfma_f32_16x16x32_bf16 v[74:77], v[88:91], v[88:91], v[74:77]\n"
        "v_cvt_pk_bf16_f32 v108, v60, v61\n"
        "v_cvt_pk_bf16_f32 v109, v62, v63\n"
        "v_mfma_f32_16x16x32_bf16 v[74:77], v[16:19], v[16:19], v[74:77]\n"
        "v_mfma_f32_16x16x32_bf16 v[78:81], v[98:101], v[98:101], 0\n"
        "v_mfma_f32_16x16x32_bf16 v[78:81], v[82:85], v[82:85], v[78:81]\n"
        "v_mfma_f32_16x16x32_bf16 v[78:81], v[106:109], v[106:109], v[78:81]\n"
        "s_cmp_lt_u32 s30, s14\n"
        "s_cbranch_scc1 PTOP_%=\n"
        "s_waitcnt lgkmcnt(0)\n"
        "s_nop 15\n"
        "L21_%=:\n"
        "ds_read_b32 v255, v15 offset:8\n"
        "s_waitcnt lgkmcnt(0)\n"
        "v_readfirstlane_b32 s40, v255\n"
        "s_nop 3\n"
        "s_cmp_ge_i32 s40, s68\n"
        "s_cbranch_scc1 L22_%=\n"
        "s_sleep 1\n"
        "s_sub_u32 s41, s41, 1\n"
        "s_cbranch_scc0 L21_%=\n"
        "s_endpgm\n"
        "L22_%=:\n"
        "v_cndmask_b32_e64 v255, v74, v75, s[48:49]\n"
        "v_cndmask_b32_e64 v254, v78, v79, s[48:49]\n"
        "v_cndmask_b32_e64 v255, v255, v76, s[50:51]\n"
        "v_cndmask_b32_e64 v254, v254, v80, s[50:51]\n"
        "v_cndmask_b32_e64 v255, v255, v77, s[52:53]\n"
        "v_cndmask_b32_e64 v254, v254, v81, s[52:53]\n"
        "ds_write_b128 v11, v[102:105]\n"
        "ds_write_b128 v11, v[98:101] offset:832\n"
        "v_cndmask_b32_dpp v255, v254, v255, vcc row_ror:4 row_mask:0xf bank_mask:0xf\n"
        "ds_write_b128 v11, v[88:91] offset:64\n"
        "ds_write_b128 v11, v[82:85] offset:896\n"
        "ds_write_b128 v11, v[16:19] offset:128\n"
        "ds_write_b128 v11, v[106:109] offset:960\n"
        "s_mov_b64 exec, s[46:47]\n"
        "ds_write_b32 v12, v255\n"
        "s_mov_b64 exec, -1\n"
        "s_mov_b64 exec, 1\n"
        "ds_inc_u32 v15, v10 offset:0\n"
        "s_mov_b64 exec, -1\n"
        "s_or_b32 s40, s18, s2\n"
        "s_cbranch_scc1 L31_%=\n"
        "L30_%=:\n"
        "s_mov_b64 exec, 1\n"
        "v_mov_b32 v62, 0\n"
        "v_mov_b32 v63, 1\n"
        "global_atomic_add v64, v62, v63, s[24:25] offset:256 sc0\n"
        "s_mov_b64 exec, -1\n"
        "s_mov_b64 exec, 1\n"
        "v_mov_b32 v62, 0\n"
        "v_mov_b32 v63, 0\n"
        "global_atomic_add v64, v62, v63, s[24:25] offset:0 sc0\n"
        "s_mov_b64 exec, -1\n"
        "s_waitcnt vmcnt(0)\n"
        "v_readfirstlane_b32 s40, v64\n"
        "s_nop 3\n"
        "s_cmp_ge_u32 s40, 512\n"
        "s_cbranch_scc1 L31_%=\n"
        "s_sleep 8\n"
        "s_sub_u32 s41, s41, 64\n"
        "s_cbranch_scc0 L30_%=\n"
        "L31_%=:\n"
        "s_endpgm\n"
        "L3_%=:\n"
        "s_nop 7\n"
        "L1_%=:\n"
        "ds_read_b32 v111, v15 offset:8\n"
        "s_waitcnt lgkmcnt(0)\n"
        "v_readfirstlane_b32 s40, v111\n"
        "s_nop 3\n"
        "s_cmp_ge_i32 s40, s68\n"
        "s_cbranch_scc1 L2_%=\n"
        "s_sleep 1\n"
        "s_sub_u32 s41, s41, 1\n"
        "s_cbranch_scc0 L1_%=\n"
        "s_endpgm\n"
        "L2_%=:\n"
        "s_nop 1\n"
        "s_branch L4_%=\n"
        "L8_%=:\n"
        "s_nop 7\n"
        "ds_read_u16 v11, v13 offset:19564\n"
        "ds_read_u16 v12, v13 offset:19566\n"
        "s_cmp_ge_u32 s30, 10\n"
        "s_cselect_b32 s69, 10, 0\n"
        "s_cmp_ge_u32 s30, 20\n"
        "s_cselect_b32 s69, 20, s69\n"
        "s_sub_u32 s69, s30, s69\n"
        "s_mul_i32 s69, s69, 832\n"
        "s_waitcnt lgkmcnt(0)\n"
        "v_add_u32 v11, s69, v11\n"
        "v_subrev_u32 v74, 8320, v11\n"
        "v_min_u32 v11, v11, v74\n"
        "v_add_u32 v11, 101728, v11\n"
        "v_add_u32 v12, s69, v12\n"
        "v_subrev_u32 v74, 8320, v12\n"
        "v_min_u32 v12, v12, v74\n"
        "v_add_u32 v12, 101728, v12\n"
        "s_nop 1\n"
        "s_branch L9_%=\n"
        "L10_%=:\n"
        "s_nop 7\n"
        "v_mov_b32 v74, 110048\n"
        "L6_%=:\n"
        "ds_read_b32 v106, v74 offset:64\n"
        "s_waitcnt lgkmcnt(0)\n"
        "v_readfirstlane_b32 s40, v106\n"
        "s_nop 3\n"
        "s_cmp_ge_i32 s40, 1\n"
        "s_cbranch_scc1 L7_%=\n"
        "s_sleep 1\n"
        "s_sub_u32 s41, s41, 1\n"
        "s_cbranch_scc0 L6_%=\n"
        "s_endpgm\n"
        "L7_%=:\n"
        "v_mbcnt_lo_u32_b32 v106, -1, 0\n"
        "v_lshl_add_u32 v74, v106, 2, v74\n"
        "ds_read_b32 v106, v74 offset:68\n"
        "s_mov_b32 s14, s40\n"
        "s_mov_b32 s76, -1\n"
        "s_waitcnt lgkmcnt(0)\n"
        "v_readlane_b32 s22, v106, 0\n"
        "v_readlane_b32 s23, v106, 1\n"
        "s_nop 3\n"
        "s_nop 1\n"
        "s_branch L11_%=\n"
        "L12_%=:\n"
        "s_nop 7\n"
        "ds_read_b32 v97, v13 offset:19552\n"
        "ds_read_b32 v25, v13 offset:19556\n"
        "ds_read_b32 v96, v13 offset:19560\n"
        "s_mov_b32 s35, 6\n"
        "s_mov_b32 s38, 0\n"
        "s_mov_b32 s69, 1344\n"
        "s_waitcnt lgkmcnt(0)\n"
        "v_lshrrev_b32 v74, 28, v97\n"
        "v_add_u32 v74, s34, v74\n"
        "v_and_b32 v97, 0x0fffffff, v97\n"
        "v_mad_u32_u24 v97, v74, s69, v97\n"
        "v_mov_b32 v106, s15\n"
        "v_cmpx_le_u32 vcc, s72, v74\n"
        "v_mov_b32 v106, s71\n"
        "s_mov_b64 exec, -1\n"
        "v_cmpx_le_u32 vcc, 20, v74\n"
        "v_mov_b32 v106, s22\n"
        "s_mov_b64 exec, -1\n"
        "v_cmpx_le_u32 vcc, 21, v74\n"
        "v_mov_b32 v106, s23\n"
        "s_mov_b64 exec, -1\n"
        "v_add_u32 v97, v97, v106\n"
        "v_lshrrev_b32 v74, 28, v25\n"
        "v_add_u32 v74, s34, v74\n"
        "v_and_b32 v25, 0x0fffffff, v25\n"
        "v_mad_u32_u24 v25, v74, s69, v25\n"
        "v_mov_b32 v106, s15\n"
        "v_cmpx_le_u32 vcc, s72, v74\n"
        "v_mov_b32 v106, s71\n"
        "s_mov_b64 exec, -1\n"
        "v_cmpx_le_u32 vcc, 20, v74\n"
        "v_mov_b32 v106, s22\n"
        "s_mov_b64 exec, -1\n"
        "v_cmpx_le_u32 vcc, 21, v74\n"
        "v_mov_b32 v106, s23\n"
        "s_mov_b64 exec, -1\n"
        "v_add_u32 v25, v25, v106\n"
        "v_lshrrev_b32 v74, 28, v96\n"
        "v_add_u32 v74, s34, v74\n"
        "v_and_b32 v96, 0x0fffffff, v96\n"
        "v_mad_u32_u24 v96, v74, s69, v96\n"
        "v_mov_b32 v106, s15\n"
        "v_cmpx_le_u32 vcc, s72, v74\n"
        "v_mov_b32 v106, s71\n"
        "s_mov_b64 exec, -1\n"
        "v_cmpx_le_u32 vcc, 20, v74\n"
        "v_mov_b32 v106, s22\n"
        "s_mov_b64 exec, -1\n"
        "v_cmpx_le_u32 vcc, 21, v74\n"
        "v_mov_b32 v106, s23\n"
        "s_mov_b64 exec, -1\n"
        "v_add_u32 v96, v96, v106\n"
        "s_nop 4\n"
        "s_mov_b64 vcc, s[44:45]\n"
        "s_nop 1\n"
        "s_branch L13_%=\n"
        "L19_%=:\n"
        "s_nop 7\n"
        "v_readfirstlane_b32 s62, v14\n"
        "s_cmp_eq_u32 s75, 1\n"
        "s_cbranch_scc1 L14_%=\n"
        "s_cmp_ge_u32 s75, 2\n"
        "s_cbranch_scc1 L15_%=\n"
        "s_mov_b32 s27, 20\n"
        "s_cmp_lg_u32 s2, 0\n"
        "s_cbranch_scc1 L18_%=\n"
        "s_not_b32 s26, s26\n"
        "s_mov_b64 exec, 1\n"
        "v_mov_b32 v74, 0\n"
        "v_mov_b32 v106, 1\n"
        "global_atomic_add v14, v74, v106, s[24:25] offset:256 sc0\n"
        "s_mov_b64 exec, -1\n"
        "s_branch L16_%=\n"
        "L18_%=:\n"
        "s_mov_b64 exec, 1\n"
        "v_mov_b32 v74, 0\n"
        "global_load_dword v14, v74, s[24:25] offset:256 sc1\n"
        "s_mov_b64 exec, -1\n"
        "s_branch L16_%=\n"
        "L14_%=:\n"
        "s_cmp_lg_u32 s62, s26\n"
        "s_cbranch_scc1 L17_%=\n"
        "s_sleep 8\n"
        "s_mov_b64 exec, 1\n"
        "v_mov_b32 v74, 0\n"
        "global_load_dword v14, v74, s[24:25] offset:256 sc1\n"
        "s_mov_b64 exec, -1\n"
        "s_waitcnt vmcnt(0)\n"
        "v_readfirstlane_b32 s62, v14\n"
        "s_sub_u32 s41, s41, 64\n"
        "s_cbranch_scc0 L14_%=\n"
        "s_endpgm\n"
        "L15_%=:\n"
        "s_cmp_lt_u32 s62, 209\n"
        "s_addc_u32 s27, s27, 0\n"
        "s_min_u32 s62, s62, 208\n"
        "s_add_u32 s62, s62, 5120\n"
        "s_mul_i32 s64, s62, 57457\n"
        "s_lshr_b32 s64, s64, 22\n"
        "s_sub_u32 s62, s62, 18\n"
        "s_sub_u32 s62, s62, s75\n"
        "s_mul_i32 s62, s62, 1344\n"
        "s_mul_i32 s64, s64, 588672\n"
        "s_add_u32 s62, s62, s64\n"
        "s_lshl2_add_u32 s64, s75, 110048\n"
        "v_mov_b32 v74, s64\n"
        "v_mov_b32 v106, s62\n"
        "ds_write_b32 v74, v106 offset:60\n"
        "s_waitcnt lgkmcnt(0)\n"
        "s_cmp_lt_u32 s75, 3\n"
        "s_cbranch_scc1 L17_%=\n"
        "v_mov_b32 v106, s27\n"
        "ds_write_b32 v74, v106 offset:52\n"
        "s_waitcnt lgkmcnt(0)\n"
        "s_branch L16_%=\n"
        "L17_%=:\n"
        "s_mov_b64 exec, 1\n"
        "v_mov_b32 v74, 0\n"
        "v_mov_b32 v106, 1\n"
        "global_atomic_add v14, v74, v106, s[24:25] offset:0 sc0\n"
        "s_mov_b64 exec, -1\n"
        "L16_%=:\n"
        "s_nop 1\n"
        "s_branch L20_%=\n"
        "CONS_%=:\n"
        "s_mov_b32 s41, 0x400000\n"
        "s_mov_b32 s48, 0xffffffff\n"
        "s_mov_b32 s49, 0xffff0000\n"
        "s_mov_b32 s50, 0x33333333\n"
        "s_mov_b32 s51, 0x33333333\n"
        "s_mov_b32 s52, 0x3c2aaaab\n"
        "s_mov_b32 s53, 0x3a7ba882\n"
        "s_mov_b32 s56, 0x0\n"
        "s_mov_b32 s57, 0xfeff0000\n"
        "s_mov_b32 s62, 0x11111111\n"
        "s_mov_b32 s63, 0x11111111\n"
        "s_mov_b64 s[44:45], s[12:13]\n"
        "s_and_b32 s45, s45, 0xffff\n"
        "s_mov_b32 s47, 0x20000\n"
        "s_mov_b32 s75, 832\n"
        "s_mov_b32 s74, 110048\n"
        "s_mov_b32 s76, 17\n"
        "s_sub_u32 s73, s18, 7488\n"
        "s_mov_b32 s28, 0xffff0000\n"
        "s_mov_b32 s31, 0\n"
        "s_add_u32 s72, s15, s17\n"
        "s_add_u32 s70, s72, 588672\n"
        "s_sub_u32 s71, 73, s16\n"
        "s_mov_b32 s35, s72\n"
        "s_mov_b32 s36, 0x20000000\n"
        "s_mov_b32 s69, 40064\n"
        "L56_%=:\n"
        "ds_read_b32 v251, v250 offset:52\n"
        "s_waitcnt lgkmcnt(0)\n"
        "v_readfirstlane_b32 s40, v251\n"
        "s_nop 3\n"
        "s_cmp_ge_i32 s40, 6\n"
        "s_cbranch_scc1 L57_%=\n"
        "s_nop 0\n"
        "s_sub_u32 s41, s41, 1\n"
        "s_cbranch_scc0 L56_%=\n"
        "s_endpgm\n"
        "L57_%=:\n"
        "s_waitcnt vmcnt(0)\n"
        "s_setprio 0\n"
        "v_add_u32 v24, 2, v1\n"
        "v_mov_b32 v25, 0x3f80\n"
        "s_mov_b32 exec_lo, 0\n"
        "s_mov_b32 exec_hi, 0x1000000\n"
        "ds_write_b16 v24, v25\n"
        "s_mov_b64 exec, -1\n"
        "v_add_u32 v24, 61664, v0\n"
        "ds_read_b128 v[168:171], v24 offset:0\n"
        "ds_read_b128 v[172:175], v24 offset:1024\n"
        "ds_read_b128 v[176:179], v24 offset:2048\n"
        "ds_read_b128 v[180:183], v24 offset:3072\n"
        "ds_read_b128 v[184:187], v24 offset:4096\n"
        "ds_read_b128 v[188:191], v24 offset:5120\n"
        "ds_read_b128 v[192:195], v24 offset:6144\n"
        "ds_read_b128 v[196:199], v24 offset:7168\n"
        "ds_read_b128 v[200:203], v24 offset:8192\n"
        "ds_read_b128 v[204:207], v24 offset:9216\n"
        "ds_read_b128 v[208:211], v24 offset:10240\n"
        "ds_read_b128 v[212:215], v24 offset:11264\n"
        "ds_read_b128 v[216:219], v24 offset:12288\n"
        "ds_read_b128 v[220:223], v24 offset:13312\n"
        "ds_read_b128 v[224:227], v24 offset:14336\n"
        "ds_read_b128 v[228:231], v24 offset:15360\n"
        "ds_read_b128 v[232:235], v24 offset:16384\n"
        "ds_read_b128 v[236:239], v24 offset:17408\n"
        "ds_read_b128 v[240:243], v0 offset:0\n"
        "ds_read_b128 v[244:247], v0 offset:1024\n"
        "ds_read_b128 v[248:251], v0 offset:2048\n"
        "ds_read_b128 v[252:255], v0 offset:3088\n"
        "v_mov_b32 v25, 110048\n"
        "s_waitcnt lgkmcnt(0)\n"
        "s_mov_b64 exec, 1\n"
        "ds_inc_u32 v25, v1 offset:12\n"
        "s_mov_b64 exec, -1\n"
        "L58_%=:\n"
        "ds_read_b32 v24, v25 offset:0\n"
        "s_waitcnt lgkmcnt(0)\n"
        "v_readfirstlane_b32 s40, v24\n"
        "s_nop 3\n"
        "s_cmp_ge_i32 s40, 11\n"
        "s_cbranch_scc1 L59_%=\n"
        "s_sleep 1\n"
        "s_sub_u32 s41, s41, 1\n"
        "s_cbranch_scc0 L58_%=\n"
        "s_endpgm\n"
        "L59_%=:\n"
        "ds_read_b128 v[104:107], v7 offset:0\n"
        "ds_read_b128 v[148:151], v7 offset:3328\n"
        "ds_read_b128 v[108:111], v7 offset:64\n"
        "ds_read_b128 v[152:155], v7 offset:3392\n"
        "ds_read_b128 v[100:103], v7 offset:128\n"
        "ds_read_b128 v[112:115], v7 offset:3456\n"
        "s_waitcnt lgkmcnt(0)\n"
        "CFULL_%=:\n"
        "s_call_b64 s[64:65], CGRP_%=\n"
        "CTOP_%=:\n"
        "s_waitcnt lgkmcnt(6)\n"
        "v_mfma_f32_16x16x32_bf16 v[32:35], v[168:171], v[104:107], 0\n"
        "v_cvt_pk_bf16_f32 v126, v136, v132\n"
        "v_cvt_pk_bf16_f32 v127, v20, v160\n"
        "s_waitcnt lgkmcnt(4)\n"
        "v_mfma_f32_16x16x32_bf16 v[32:35], v[172:175], v[108:111], v[32:35]\n"
        "v_cvt_pk_bf16_f32 v128, v116, v40\n"
        "v_cvt_pk_bf16_f32 v144, v137, v133\n"
        "s_waitcnt lgkmcnt(2)\n"
        "v_mfma_f32_16x16x32_bf16 v[32:35], v[176:179], v[100:103], v[32:35]\n"
        "v_cvt_pk_bf16_f32 v145, v21, v161\n"
        "v_cvt_pk_bf16_f32 v146, v117, v41\n"
        "v_mfma_f32_16x16x32_bf16 v[24:27], v[188:191], v[100:103], 0\n"
        "ds_read_b128 v[44:47], v16 offset:128\n"
        "v_cvt_pk_bf16_f32 v156, v138, v134\n"
        "v_mfma_f32_16x16x32_bf16 v[24:27], v[184:187], v[108:111], v[24:27]\n"
        "s_setprio 2\n"
        "ds_read_b128 v[64:67], v5\n"
        "v_cvt_pk_bf16_f32 v28, v32, v33\n"
        "v_mfma_f32_16x16x32_bf16 v[24:27], v[180:183], v[104:107], v[24:27]\n"
        "s_add_u32 s37, s36, 196224\n"
        "v_cvt_pk_bf16_f32 v29, v34, v35\n"
        "v_cvt_pk_bf16_f32 v157, v22, v162\n"
        "v_cvt_pk_bf16_f32 v158, v118, v42\n"
        "v_cvt_pk_bf16_f32 v122, v139, v135\n"
        "v_mfma_f32_16x16x32_bf16 v[36:39], v[192:195], v[104:107], 0\n"
        "v_cvt_pk_bf16_f32 v123, v23, v163\n"
        "v_cvt_pk_bf16_f32 v124, v119, v43\n"
        "v_mfma_f32_16x16x32_bf16 v[36:39], v[196:199], v[108:111], v[36:39]\n"
        "ds_read_b128 v[88:91], v7 offset:6656\n"
        "v_cvt_pk_bf16_f32 v30, v24, v25\n"
        "v_mfma_f32_16x16x32_bf16 v[36:39], v[200:203], v[100:103], v[36:39]\n"
        "buffer_store_dwordx3 v[126:128], v12, s[8:11], s37 offen offset:384 sc0 sc1 nt\n"
        "ds_read_b128 v[92:95], v7 offset:6720\n"
        "ds_read_b128 v[160:163], v7 offset:6784\n"
        "v_cvt_pk_bf16_f32 v31, v26, v27\n"
        "s_waitcnt lgkmcnt(3)\n"
        "v_mfma_f32_16x16x32_bf16 v[48:51], v[44:47], v[100:103], 0\n"
        "v_cvt_pk_bf16_f32 v130, v80, v72\n"
        "v_cvt_pk_bf16_f32 v131, v60, v164\n"
        "v_smfmac_f32_16x16x64_bf16 v[48:51], v[64:67], v[104:111], v4\n"
        "s_add_u32 s37, s36, 196224\n"
        "v_cvt_pk_bf16_f32 v32, v36, v37\n"
        "v_cvt_pk_bf16_f32 v33, v38, v39\n"
        "v_mfma_f32_16x16x32_bf16 v[52:55], v[44:47], v[112:115], 0\n"
        "v_cvt_pk_bf16_f32 v132, v140, v76\n"
        "v_cvt_pk_bf16_f32 v134, v81, v73\n"
        "v_smfmac_f32_16x16x64_bf16 v[52:55], v[64:67], v[148:155], v4\n"
        "v_cvt_pk_bf16_f32 v135, v61, v165\n"
        "s_waitcnt lgkmcnt(0)\n"
        "v_mfma_f32_16x16x32_bf16 v[56:59], v[44:47], v[160:163], 0\n"
        "v_cvt_pk_bf16_f32 v136, v141, v77\n"
        "v_cvt_pk_bf16_f32 v164, v82, v74\n"
        "v_smfmac_f32_16x16x64_bf16 v[56:59], v[64:67], v[88:95], v4\n"
        "buffer_store_dwordx3 v[144:146], v13, s[8:11], s37 offen offset:1152 sc0 sc1 nt\n"
        "v_cvt_pk_bf16_f32 v165, v62, v166\n"
        "v_cvt_pk_bf16_f32 v166, v142, v78\n"
        "v_cvt_pk_bf16_f32 v144, v83, v75\n"
        "v_cvt_pk_bf16_f32 v145, v63, v167\n"
        "v_cvt_pk_bf16_f32 v146, v143, v79\n"
        "v_mfma_f32_16x16x32_bf16 v[44:47], v[212:215], v[100:103], 0\n"
        "s_add_u32 s37, s36, 294336\n"
        "ds_read_b64 v[80:81], v11 offset:192\n"
        "v_mfma_f32_16x16x32_bf16 v[44:47], v[208:211], v[108:111], v[44:47]\n"
        "ds_read_b32 v137, v10\n"
        "v_mfma_f32_16x16x32_bf16 v[44:47], v[204:207], v[104:107], v[44:47]\n"
        "v_mfma_f32_16x16x32_bf16 v[20:23], v[216:219], v[104:107], 0\n"
        "v_mfma_f32_16x16x32_bf16 v[20:23], v[220:223], v[108:111], v[20:23]\n"
        "v_mfma_f32_16x16x32_bf16 v[20:23], v[224:227], v[100:103], v[20:23]\n"
        "buffer_store_dwordx3 v[156:158], v12, s[8:11], s37 offen offset:576 sc0 sc1 nt\n"
        "s_waitcnt lgkmcnt(1)\n"
        "v_add_f32_dpp v81, v50, v51 quad_perm:[0,1,2,3] row_mask:0x1 bank_mask:0xf\n"
        "v_add_f32_dpp v80, v48, v49 quad_perm:[0,1,2,3] row_mask:0x1 bank_mask:0xf\n"
        "s_add_u32 s37, s36, 392448\n"
        "v_cvt_pk_bf16_f32 v34, v44, v45\n"
        "v_mfma_f32_16x16x32_bf16 v[40:43], v[236:239], v[100:103], 0\n"
        "v_cvt_pk_bf16_f32 v35, v46, v47\n"
        "v_cvt_pk_bf16_f32 v44, v20, v21\n"
        "v_mfma_f32_16x16x32_bf16 v[40:43], v[232:235], v[108:111], v[40:43]\n"
        "v_cvt_pk_bf16_f32 v45, v22, v23\n"
        "v_add_f32_dpp v81, v54, v55 quad_perm:[0,1,2,3] row_mask:0x2 bank_mask:0xf\n"
        "v_mfma_f32_16x16x32_bf16 v[40:43], v[228:231], v[104:107], v[40:43]\n"
        "buffer_store_dwordx3 v[122:124], v12, s[8:11], s37 offen sc0 sc1 nt\n"
        "v_add_f32_dpp v80, v52, v53 quad_perm:[0,1,2,3] row_mask:0x2 bank_mask:0xf\n"
        "v_add_f32_dpp v81, v58, v59 quad_perm:[0,1,2,3] row_mask:0x4 bank_mask:0xf\n"
        "v_mul_f32 v159, s52, v81\n"
        "v_add_f32_dpp v80, v56, v57 quad_perm:[0,1,2,3] row_mask:0x4 bank_mask:0xf\n"
        "s_waitcnt lgkmcnt(0)\n"
        "v_fma_f32 v159, -v159, v81, v137\n"
        "v_add_f32_e64 v159, s53, |v159|\n"
        "v_cvt_pk_bf16_f32 v47, v42, v43\n"
        "v_cvt_pk_bf16_f32 v46, v40, v41\n"
        "v_mfma_f32_16x16x32_bf16 v[24:27], v[104:107], v[28:31], 0\n"
        "v_rsq_f32 v137, v159\n"
        "v_mfma_f32_16x16x32_bf16 v[24:27], v[108:111], v[32:35], v[24:27]\n"
        "s_add_u32 s37, s36, 392448\n"
        "v_and_b32 v74, s28, v137\n"
        "v_and_b32 v116, s28, v137\n"
        "v_mfma_f32_16x16x32_bf16 v[24:27], v[100:103], v[44:47], v[24:27]\n"
        "v_sub_f32 v159, v137, v74\n"
        "v_mul_f32 v80, v80, v137\n"
        "v_cvt_pk_bf16_f32 v75, v159, v74\n"
        "v_mfma_f32_16x16x32_bf16 v[20:23], v[112:115], v[44:47], 0\n"
        "ds_read_b128 v[96:99], v8 offset:128\n"
        "v_cvt_pk_bf16_f32 v117, v116, v159\n"
        "v_mfma_f32_16x16x32_bf16 v[20:23], v[152:155], v[32:35], v[20:23]\n"
        "ds_read2_b32 v[156:157], v6 offset0:0 offset1:2\n"
        "v_readlane_b32 s58, v137, 56\n"
        "v_mfma_f32_16x16x32_bf16 v[20:23], v[148:151], v[28:31], v[20:23]\n"
        "buffer_store_dwordx3 v[130:132], v15, s[8:11], s37 offen offset:768 sc0 sc1 nt\n"
        "ds_read2_b32 v[158:159], v6 offset0:8 offset1:10\n"
        "s_mov_b64 exec, s[56:57]\n"
        "v_mov_b32 v80, 0xff800000\n"
        "s_mov_b64 exec, -1\n"
        "v_mfma_f32_16x16x4_4b_bf16 v[118:133], v[74:75], v[116:117], 0 blgp:4\n"
        "v_mul_f32 v80, s58, v80\n"
        "v_exp_f32 v80, v80\n"
        "v_mfma_f32_16x16x32_bf16 v[64:67], v[88:91], v[28:31], 0\n"
        "s_add_u32 s37, s36, 490560\n"
        "v_add_f32_dpp v139, v80, v80 row_ror:8 row_mask:0xf bank_mask:0xf\n"
        "v_mul_f32 v80, v80, v137\n"
        "v_mfma_f32_16x16x32_bf16 v[64:67], v[92:95], v[32:35], v[64:67]\n"
        "v_add_f32_dpp v139, v139, v139 row_ror:4 row_mask:0xf bank_mask:0xf\n"
        "v_mfma_f32_16x16x32_bf16 v[64:67], v[160:163], v[44:47], v[64:67]\n"
        "s_waitcnt lgkmcnt(0)\n"
        "v_add_f32_dpp v139, v139, v139 row_ror:2 row_mask:0xf bank_mask:0xf\n"
        "v_mfma_f32_16x16x32_bf16 v[68:71], v[96:99], v[44:47], 0\n"
        "v_mul_f32 v24, v24, v118\n"
        "v_mul_f32 v25, v25, v119\n"
        "v_smfmac_f32_16x16x64_bf16 v[68:71], v[156:159], v[28:35], v4\n"
        "buffer_store_dwordx3 v[134:136], v12, s[8:11], s37 offen offset:192 sc0 sc1 nt\n"
        "v_mul_f32 v26, v26, v120\n"
        "v_mul_f32 v27, v27, v121\n"
        "v_exp_f32 v24, v24\n"
        "v_mul_f32 v20, v20, v122\n"
        "v_mfma_f32_16x16x32_bf16 v[28:31], v[168:171], v[148:151], 0\n"
        "s_add_u32 s37, s36, 490560\n"
        "v_exp_f32 v25, v25\n"
        "v_mfma_f32_16x16x32_bf16 v[28:31], v[172:175], v[152:155], v[28:31]\n"
        "v_exp_f32 v26, v26\n"
        "v_mfma_f32_16x16x32_bf16 v[28:31], v[176:179], v[112:115], v[28:31]\n"
        "v_exp_f32 v27, v27\n"
        "v_add_f32 v136, v24, v25\n"
        "v_mfma_f32_16x16x32_bf16 v[32:35], v[188:191], v[112:115], 0\n"
        "v_add_f32 v136, v136, v26\n"
        "v_add_f32 v136, v136, v27\n"
        "v_mfma_f32_16x16x32_bf16 v[32:35], v[184:187], v[152:155], v[32:35]\n"
        "v_mul_f32 v21, v21, v123\n"
        "v_mul_f32 v22, v22, v124\n"
        "v_mfma_f32_16x16x32_bf16 v[32:35], v[180:183], v[148:151], v[32:35]\n"
        "buffer_store_dwordx3 v[164:166], v14, s[8:11], s37 offen offset:960 sc0 sc1 nt\n"
        "v_cvt_pk_bf16_f32 v48, v28, v29\n"
        "v_cvt_pk_bf16_f32 v49, v30, v31\n"
        "v_mul_f32 v23, v23, v125\n"
        "v_exp_f32 v20, v20\n"
        "v_mfma_f32_16x16x32_bf16 v[28:31], v[192:195], v[148:151], 0\n"
        "s_add_u32 s37, s36, 588672\n"
        "v_exp_f32 v21, v21\n"
        "v_mfma_f32_16x16x32_bf16 v[28:31], v[196:199], v[152:155], v[28:31]\n"
        "v_cvt_pk_bf16_f32 v50, v32, v33\n"
        "v_cvt_pk_bf16_f32 v51, v34, v35\n"
        "v_mfma_f32_16x16x32_bf16 v[28:31], v[200:203], v[112:115], v[28:31]\n"
        "buffer_store_dwordx3 v[144:146], v12, s[8:11], s37 offen offset:384 sc0 sc1 nt\n"
        "v_exp_f32 v22, v22\n"
        "v_add_f32 v136, v136, v20\n"
        "v_exp_f32 v23, v23\n"
        "v_add_f32 v136, v136, v21\n"
        "v_mfma_f32_16x16x32_bf16 v[32:35], v[212:215], v[112:115], 0\n"
        "v_add_f32 v136, v136, v22\n"
        "v_add_f32 v136, v136, v23\n"
        "v_mfma_f32_16x16x32_bf16 v[32:35], v[208:211], v[152:155], v[32:35]\n"
        "v_cvt_pk_bf16_f32 v52, v28, v29\n"
        "v_cvt_pk_bf16_f32 v53, v30, v31\n"
        "v_mfma_f32_16x16x32_bf16 v[32:35], v[204:207], v[148:151], v[32:35]\n"
        "v_mul_f32 v64, v64, v126\n"
        "v_mul_f32 v65, v65, v127\n"
        "v_mul_f32 v66, v66, v128\n"
        "v_mfma_f32_16x16x32_bf16 v[28:31], v[216:219], v[148:151], 0\n"
        "v_mul_f32 v67, v67, v129\n"
        "v_add_f32 v68, v68, v69\n"
        "v_mfma_f32_16x16x32_bf16 v[28:31], v[220:223], v[152:155], v[28:31]\n"
        "v_exp_f32 v64, v64\n"
        "v_mfma_f32_16x16x32_bf16 v[28:31], v[224:227], v[112:115], v[28:31]\n"
        "v_cvt_pk_bf16_f32 v54, v32, v33\n"
        "v_cvt_pk_bf16_f32 v55, v34, v35\n"
        "v_add_f32 v136, v136, v64\n"
        "v_mfma_f32_16x16x32_bf16 v[56:59], v[236:239], v[112:115], 0\n"
        "v_exp_f32 v65, v65\n"
        "v_mfma_f32_16x16x32_bf16 v[56:59], v[232:235], v[152:155], v[56:59]\n"
        "v_exp_f32 v66, v66\n"
        "v_mfma_f32_16x16x32_bf16 v[56:59], v[228:231], v[148:151], v[56:59]\n"
        "v_cvt_pk_bf16_f32 v44, v28, v29\n"
        "v_cvt_pk_bf16_f32 v45, v30, v31\n"
        "v_mfma_f32_16x16x4_4b_bf16 v[28:43], v[74:75], v[116:117], 0 blgp:5\n"
        "v_exp_f32 v67, v67\n"
        "v_add_f32 v136, v136, v65\n"
        "v_add_f32 v136, v136, v66\n"
        "v_mul_f32 v68, v68, v130\n"
        "v_exp_f32 v68, v68\n"
        "v_cvt_pk_bf16_f32 v46, v56, v57\n"
        "v_cvt_pk_bf16_f32 v47, v58, v59\n"
        "v_mfma_f32_16x16x32_bf16 v[144:147], v[104:107], v[48:51], 0\n"
        "v_add_f32 v136, v136, v67\n"
        "v_add_f32_dpp v139, v139, v139 row_ror:1 row_mask:0xf bank_mask:0xf\n"
        "v_mfma_f32_16x16x32_bf16 v[144:147], v[108:111], v[52:55], v[144:147]\n"
        "v_add_f32_dpp v136, v68, v136 quad_perm:[0,1,2,3] row_mask:0x4 bank_mask:0xf\n"
        "v_mfma_f32_16x16x32_bf16 v[144:147], v[100:103], v[44:47], v[144:147]\n"
        "v_mfma_f32_16x16x32_bf16 v[60:63], v[112:115], v[44:47], 0\n"
        "v_permlane32_swap_b32 v136, v139\n"
        "v_mfma_f32_16x16x32_bf16 v[60:63], v[152:155], v[52:55], v[60:63]\n"
        "v_add_f32 v136, v136, v139\n"
        "v_mov_b32 v139, v136\n"
        "v_mfma_f32_16x16x32_bf16 v[60:63], v[148:151], v[48:51], v[60:63]\n"
        "v_mfma_f32_16x16x32_bf16 v[76:79], v[88:91], v[48:51], 0\n"
        "v_permlane16_swap_b32 v136, v139\n"
        "v_mfma_f32_16x16x32_bf16 v[76:79], v[92:95], v[52:55], v[76:79]\n"
        "v_add_f32 v136, v136, v139\n"
        "v_mul_f32 v144, v144, v28\n"
        "v_mfma_f32_16x16x32_bf16 v[76:79], v[160:163], v[44:47], v[76:79]\n"
        "v_rcp_f32 v138, v136\n"
        "v_mul_f32 v145, v145, v29\n"
        "v_and_b32 v134, s28, v138\n"
        "v_mfma_f32_16x16x32_bf16 v[82:85], v[96:99], v[44:47], 0\n"
        "v_sub_f32 v139, v138, v134\n"
        "v_cvt_pk_bf16_f32 v135, v134, v139\n"
        "v_smfmac_f32_16x16x64_bf16 v[82:85], v[156:159], v[48:55], v4\n"
        "v_mul_f32 v146, v146, v30\n"
        "v_mul_f32 v147, v147, v31\n"
        "v_mfma_f32_16x16x32_bf16 v[48:51], v[168:171], v[88:91], 0\n"
        "v_exp_f32 v144, v144\n"
        "v_mfma_f32_16x16x32_bf16 v[48:51], v[172:175], v[92:95], v[48:51]\n"
        "v_exp_f32 v145, v145\n"
        "v_mfma_f32_16x16x32_bf16 v[48:51], v[176:179], v[160:163], v[48:51]\n"
        "v_exp_f32 v146, v146\n"
        "v_add_f32 v69, v144, v145\n"
        "v_mfma_f32_16x16x32_bf16 v[28:31], v[188:191], v[160:163], 0\n"
        "v_exp_f32 v147, v147\n"
        "v_mfma_f32_16x16x32_bf16 v[28:31], v[184:187], v[92:95], v[28:31]\n"
        "v_add_f32 v69, v69, v146\n"
        "v_add_f32 v69, v69, v147\n"
        "v_mfma_f32_16x16x32_bf16 v[28:31], v[180:183], v[88:91], v[28:31]\n"
        "v_cvt_pk_bf16_f32 v52, v48, v49\n"
        "v_cvt_pk_bf16_f32 v53, v50, v51\n"
        "v_mul_f32 v60, v60, v32\n"
        "v_mul_f32 v61, v61, v33\n"
        "v_mfma_f32_16x16x32_bf16 v[44:47], v[192:195], v[88:91], 0\n"
        "v_mul_f32 v62, v62, v34\n"
        "v_mul_f32 v63, v63, v35\n"
        "v_mfma_f32_16x16x32_bf16 v[44:47], v[196:199], v[92:95], v[44:47]\n"
        "v_cvt_pk_bf16_f32 v54, v28, v29\n"
        "v_cvt_pk_bf16_f32 v55, v30, v31\n"
        "v_mfma_f32_16x16x32_bf16 v[44:47], v[200:203], v[160:163], v[44:47]\n"
        "v_exp_f32 v60, v60\n"
        "v_mul_f32 v76, v76, v36\n"
        "v_mfma_f32_16x16x32_bf16 v[32:35], v[212:215], v[160:163], 0\n"
        "v_exp_f32 v61, v61\n"
        "v_mfma_f32_16x16x32_bf16 v[32:35], v[208:211], v[92:95], v[32:35]\n"
        "v_exp_f32 v62, v62\n"
        "v_mfma_f32_16x16x32_bf16 v[32:35], v[204:207], v[88:91], v[32:35]\n"
        "v_exp_f32 v63, v63\n"
        "v_cvt_pk_bf16_f32 v56, v44, v45\n"
        "v_cvt_pk_bf16_f32 v57, v46, v47\n"
        "v_mfma_f32_16x16x32_bf16 v[28:31], v[216:219], v[88:91], 0\n"
        "v_add_f32 v69, v69, v60\n"
        "v_add_f32 v69, v69, v61\n"
        "v_mfma_f32_16x16x32_bf16 v[28:31], v[220:223], v[92:95], v[28:31]\n"
        "v_add_f32 v69, v69, v62\n"
        "v_cvt_pk_bf16_f32 v58, v32, v33\n"
        "v_mfma_f32_16x16x32_bf16 v[28:31], v[224:227], v[160:163], v[28:31]\n"
        "v_cvt_pk_bf16_f32 v59, v34, v35\n"
        "v_add_f32 v69, v69, v63\n"
        "v_mul_f32 v77, v77, v37\n"
        "v_mfma_f32_16x16x32_bf16 v[48:51], v[236:239], v[160:163], 0\n"
        "v_mul_f32 v78, v78, v38\n"
        "v_mul_f32 v79, v79, v39\n"
        "v_mfma_f32_16x16x32_bf16 v[48:51], v[232:235], v[92:95], v[48:51]\n"
        "v_exp_f32 v76, v76\n"
        "v_mfma_f32_16x16x32_bf16 v[48:51], v[228:231], v[88:91], v[48:51]\n"
        "v_cvt_pk_bf16_f32 v44, v28, v29\n"
        "v_cvt_pk_bf16_f32 v45, v30, v31\n"
        "v_mfma_f32_16x16x4_4b_bf16 v[118:133], v[74:75], v[134:135], 0 blgp:4\n"
        "v_exp_f32 v77, v77\n"
        "v_exp_f32 v78, v78\n"
        "v_add_f32 v69, v69, v76\n"
        "v_exp_f32 v79, v79\n"
        "v_add_f32 v69, v69, v77\n"
        "v_cvt_pk_bf16_f32 v46, v48, v49\n"
        "v_cvt_pk_bf16_f32 v47, v50, v51\n"
        "v_mfma_f32_16x16x32_bf16 v[140:143], v[104:107], v[52:55], 0\n"
        "v_add_f32 v69, v69, v78\n"
        "v_add_f32 v69, v69, v79\n"
        "v_mfma_f32_16x16x32_bf16 v[140:143], v[108:111], v[56:59], v[140:143]\n"
        "v_add_f32 v82, v82, v83\n"
        "v_mul_f32 v24, v24, v118\n"
        "v_mfma_f32_16x16x32_bf16 v[140:143], v[100:103], v[44:47], v[140:143]\n"
        "v_mul_f32 v25, v25, v119\n"
        "v_mul_f32 v82, v82, v40\n"
        "v_mul_f32 v26, v26, v120\n"
        "v_mfma_f32_16x16x32_bf16 v[164:167], v[112:115], v[44:47], 0\n"
        "v_exp_f32 v82, v82\n"
        "v_mfma_f32_16x16x32_bf16 v[164:167], v[152:155], v[56:59], v[164:167]\n"
        "v_mul_f32 v27, v27, v121\n"
        "v_add_f32_dpp v69, v82, v69 quad_perm:[0,1,2,3] row_mask:0x4 bank_mask:0xf\n"
        "v_mfma_f32_16x16x32_bf16 v[164:167], v[148:151], v[52:55], v[164:167]\n"
        "v_mul_f32 v20, v20, v122\n"
        "v_mul_f32 v21, v21, v123\n"
        "v_mul_f32 v22, v22, v124\n"
        "v_mul_f32 v23, v23, v125\n"
        "v_mfma_f32_16x16x32_bf16 v[48:51], v[88:91], v[52:55], 0\n"
        "v_mul_f32 v64, v64, v126\n"
        "v_mul_f32 v65, v65, v127\n"
        "v_mfma_f32_16x16x32_bf16 v[48:51], v[92:95], v[56:59], v[48:51]\n"
        "v_mul_f32 v66, v66, v128\n"
        "v_mul_f32 v67, v67, v129\n"
        "v_mfma_f32_16x16x32_bf16 v[48:51], v[160:163], v[44:47], v[48:51]\n"
        "v_fma_f32 v68, v68, v130, v19\n"
        "v_mfma_f32_16x16x4_4b_bf16 v[120:135], v[74:75], v[116:117], 0 blgp:6\n"
        "v_cvt_pk_bf16_f32 v40, v24, v25\n"
        "v_cvt_pk_bf16_f32 v41, v26, v27\n"
        "v_cvt_pk_bf16_f32 v42, v20, v21\n"
        "v_cvt_pk_bf16_f32 v43, v22, v23\n"
        "v_cvt_pk_bf16_f32 v160, v64, v65\n"
        "v_cvt_pk_bf16_f32 v162, v66, v67\n"
        "v_cvt_pk_bf16_f32 v161, v68, 0\n"
        "v_min3_f32 v83, v136, v138, v137\n"
        "v_readlane_b32 s59, v138, 32\n"
        "v_mfma_f32_16x16x32_bf16 v[24:27], v[96:99], v[44:47], 0\n"
        "v_smfmac_f32_16x16x64_bf16 v[24:27], v[156:159], v[52:59], v4\n"
        "v_mul_f32 v80, s59, v80\n"
        "v_mul_f32 v140, v140, v120\n"
        "v_mfma_f32_16x16x32_bf16 v[56:59], v[104:107], v[240:243], 0\n"
        "ds_read_b128 v[44:47], v17\n"
        "v_mul_f32 v141, v141, v121\n"
        "v_mfma_f32_16x16x32_bf16 v[56:59], v[108:111], v[244:247], v[56:59]\n"
        "ds_read_b128 v[70:73], v17 offset:64\n"
        "v_mul_f32 v142, v142, v122\n"
        "v_mfma_f32_16x16x32_bf16 v[56:59], v[100:103], v[248:251], v[56:59]\n"
        "ds_read_b128 v[156:159], v17 offset:128\n"
        "v_mul_f32 v143, v143, v123\n"
        "v_mfma_f32_16x16x32_bf16 v[20:23], v[112:115], v[248:251], 0\n"
        "ds_read_b128 v[52:55], v0 offset:5136\n"
        "v_mul_f32 v164, v164, v124\n"
        "v_mfma_f32_16x16x32_bf16 v[20:23], v[152:155], v[244:247], v[20:23]\n"
        "ds_read_b128 v[88:91], v0 offset:4112\n"
        "v_mul_f32 v165, v165, v125\n"
        "v_mfma_f32_16x16x32_bf16 v[20:23], v[148:151], v[240:243], v[20:23]\n"
        "v_cvt_pk_bf16_f32 v84, v56, v57\n"
        "v_cvt_pk_bf16_f32 v85, v58, v59\n"
        "v_mul_f32 v166, v166, v126\n"
        "s_waitcnt lgkmcnt(2)\n"
        "v_mfma_f32_16x16x32_bf16 v[56:59], v[44:47], v[240:243], 0\n"
        "v_exp_f32 v140, v140\n"
        "v_mfma_f32_16x16x32_bf16 v[56:59], v[70:73], v[244:247], v[56:59]\n"
        "v_exp_f32 v141, v141\n"
        "v_mfma_f32_16x16x32_bf16 v[56:59], v[156:159], v[248:251], v[56:59]\n"
        "s_waitcnt lgkmcnt(0)\n"
        "v_cvt_pk_bf16_f32 v86, v20, v21\n"
        "v_cvt_pk_bf16_f32 v87, v22, v23\n"
        "v_exp_f32 v142, v142\n"
        "v_mfma_f32_16x16x32_bf16 v[36:39], v[156:159], v[52:55], 0\n"
        "v_exp_f32 v143, v143\n"
        "v_mfma_f32_16x16x32_bf16 v[36:39], v[70:73], v[88:91], v[36:39]\n"
        "v_add_f32 v125, v140, v141\n"
        "v_cvt_pk_bf16_f32 v26, v56, v57\n"
        "v_mfma_f32_16x16x32_bf16 v[36:39], v[44:47], v[252:255], v[36:39]\n"
        "v_cvt_pk_bf16_f32 v30, v58, v59\n"
        "v_add_f32 v125, v125, v142\n"
        "v_add_f32 v125, v125, v143\n"
        "v_mfma_f32_16x16x32_bf16 v[20:23], v[148:151], v[252:255], 0\n"
        "ds_read_b128 v[32:35], v0 offset:6176\n"
        "v_mul_f32 v167, v167, v127\n"
        "v_mfma_f32_16x16x32_bf16 v[20:23], v[152:155], v[88:91], v[20:23]\n"
        "ds_read_b128 v[136:139], v0 offset:7200\n"
        "v_cvt_pk_bf16_f32 v122, v36, v37\n"
        "v_mfma_f32_16x16x32_bf16 v[20:23], v[112:115], v[52:55], v[20:23]\n"
        "ds_read_b128 v[64:67], v0 offset:8224\n"
        "v_cvt_pk_bf16_f32 v126, v38, v39\n"
        "v_mfma_f32_16x16x32_bf16 v[116:119], v[100:103], v[52:55], 0\n"
        "v_exp_f32 v164, v164\n"
        "v_mfma_f32_16x16x32_bf16 v[116:119], v[108:111], v[88:91], v[116:119]\n"
        "v_exp_f32 v165, v165\n"
        "v_mfma_f32_16x16x32_bf16 v[116:119], v[104:107], v[252:255], v[116:119]\n"
        "v_exp_f32 v166, v166\n"
        "v_cvt_pk_bf16_f32 v94, v20, v21\n"
        "s_waitcnt lgkmcnt(0)\n"
        "v_mfma_f32_16x16x32_bf16 v[36:39], v[104:107], v[32:35], 0\n"
        "v_cvt_pk_bf16_f32 v95, v22, v23\n"
        "v_add_f32 v125, v125, v164\n"
        "v_mfma_f32_16x16x32_bf16 v[36:39], v[108:111], v[136:139], v[36:39]\n"
        "v_exp_f32 v167, v167\n"
        "v_mfma_f32_16x16x32_bf16 v[36:39], v[100:103], v[64:67], v[36:39]\n"
        "v_cvt_pk_bf16_f32 v92, v116, v117\n"
        "v_cvt_pk_bf16_f32 v93, v118, v119\n"
        "v_add_f32 v125, v125, v165\n"
        "v_add_f32 v125, v125, v166\n"
        "v_mfma_f32_16x16x32_bf16 v[20:23], v[112:115], v[64:67], 0\n"
        "ds_read_b128 v[56:59], v0 offset:11312\n"
        "v_add_f32 v125, v125, v167\n"
        "v_mfma_f32_16x16x32_bf16 v[20:23], v[152:155], v[136:139], v[20:23]\n"
        "ds_read_b128 v[116:119], v0 offset:10288\n"
        "v_cvt_pk_bf16_f32 v52, v36, v37\n"
        "v_mfma_f32_16x16x32_bf16 v[20:23], v[148:151], v[32:35], v[20:23]\n"
        "ds_read_b128 v[88:91], v0 offset:9264\n"
        "v_cvt_pk_bf16_f32 v53, v38, v39\n"
        "v_mfma_f32_16x16x32_bf16 v[36:39], v[44:47], v[32:35], 0\n"
        "v_mul_f32 v48, v48, v128\n"
        "v_mul_f32 v49, v49, v129\n"
        "v_mfma_f32_16x16x32_bf16 v[36:39], v[70:73], v[136:139], v[36:39]\n"
        "v_mul_f32 v50, v50, v130\n"
        "v_mul_f32 v51, v51, v131\n"
        "v_mfma_f32_16x16x32_bf16 v[36:39], v[156:159], v[64:67], v[36:39]\n"
        "v_cvt_pk_bf16_f32 v54, v20, v21\n"
        "v_cvt_pk_bf16_f32 v55, v22, v23\n"
        "v_add_f32 v24, v24, v25\n"
        "s_waitcnt lgkmcnt(0)\n"
        "v_mfma_f32_16x16x32_bf16 v[20:23], v[156:159], v[56:59], 0\n"
        "v_exp_f32 v48, v48\n"
        "v_mfma_f32_16x16x32_bf16 v[20:23], v[70:73], v[116:119], v[20:23]\n"
        "v_exp_f32 v49, v49\n"
        "v_mfma_f32_16x16x32_bf16 v[20:23], v[44:47], v[88:91], v[20:23]\n"
        "v_cvt_pk_bf16_f32 v64, v36, v37\n"
        "v_cvt_pk_bf16_f32 v68, v38, v39\n"
        "v_add_f32 v125, v125, v48\n"
        "v_mfma_f32_16x16x32_bf16 v[36:39], v[148:151], v[88:91], 0\n"
        "ds_read_b128 v[32:35], v0 offset:12352\n"
        "v_add_f32 v125, v125, v49\n"
        "v_mfma_f32_16x16x32_bf16 v[36:39], v[152:155], v[116:119], v[36:39]\n"
        "ds_read_b128 v[136:139], v0 offset:13376\n"
        "v_cvt_pk_bf16_f32 v27, v20, v21\n"
        "v_mfma_f32_16x16x32_bf16 v[36:39], v[112:115], v[56:59], v[36:39]\n"
        "ds_read_b128 v[128:131], v0 offset:14400\n"
        "v_cvt_pk_bf16_f32 v31, v22, v23\n"
        "v_mfma_f32_16x16x32_bf16 v[20:23], v[100:103], v[56:59], 0\n"
        "v_exp_f32 v50, v50\n"
        "v_mfma_f32_16x16x32_bf16 v[20:23], v[108:111], v[116:119], v[20:23]\n"
        "v_exp_f32 v51, v51\n"
        "v_mfma_f32_16x16x32_bf16 v[20:23], v[104:107], v[88:91], v[20:23]\n"
        "v_add_f32 v125, v125, v50\n"
        "v_cvt_pk_bf16_f32 v90, v36, v37\n"
        "v_cvt_pk_bf16_f32 v91, v38, v39\n"
        "s_waitcnt lgkmcnt(0)\n"
        "v_mfma_f32_16x16x32_bf16 v[118:121], v[104:107], v[32:35], 0\n"
        "v_mul_f32 v24, v24, v132\n"
        "v_add_f32 v125, v125, v51\n"
        "v_mfma_f32_16x16x32_bf16 v[118:121], v[108:111], v[136:139], v[118:121]\n"
        "v_cvt_pk_bf16_f32 v88, v20, v21\n"
        "v_cvt_pk_bf16_f32 v89, v22, v23\n"
        "v_mfma_f32_16x16x32_bf16 v[118:121], v[100:103], v[128:131], v[118:121]\n"
        "v_exp_f32 v24, v24\n"
        "v_cvt_pk_bf16_f32 v80, v80, 0\n"
        "v_mfma_f32_16x16x32_bf16 v[20:23], v[112:115], v[128:131], 0\n"
        "ds_read_b128 v[132:135], v0 offset:17488\n"
        "v_add_f32_dpp v125, v24, v125 quad_perm:[0,1,2,3] row_mask:0x4 bank_mask:0xf\n"
        "v_mfma_f32_16x16x32_bf16 v[20:23], v[152:155], v[136:139], v[20:23]\n"
        "ds_read_b128 v[36:39], v0 offset:16464\n"
        "v_mfma_f32_16x16x32_bf16 v[20:23], v[148:151], v[32:35], v[20:23]\n"
        "ds_read_b128 v[56:59], v0 offset:15440\n"
        "v_cvt_pk_bf16_f32 v96, v118, v119\n"
        "v_mfma_f32_16x16x32_bf16 v[116:119], v[44:47], v[32:35], 0\n"
        "v_permlane32_swap_b32 v69, v125\n"
        "v_mfma_f32_16x16x32_bf16 v[116:119], v[70:73], v[136:139], v[116:119]\n"
        "v_add_f32 v69, v69, v125\n"
        "v_mov_b32 v139, v69\n"
        "v_mfma_f32_16x16x32_bf16 v[116:119], v[156:159], v[128:131], v[116:119]\n"
        "ds_read2_b32 v[32:33], v11 offset0:0 offset1:3\n"
        "v_permlane16_swap_b32 v69, v139\n"
        "v_add_f32 v69, v69, v139\n"
        "s_waitcnt lgkmcnt(1)\n"
        "v_mfma_f32_16x16x32_bf16 v[136:139], v[156:159], v[132:135], 0\n"
        "ds_read_b64_tr_b16 v[28:29], v9 offset:0\n"
        "v_cvt_pk_bf16_f32 v97, v120, v121\n"
        "v_mfma_f32_16x16x32_bf16 v[136:139], v[70:73], v[36:39], v[136:139]\n"
        "ds_read2_b32 v[128:129], v11 offset0:1 offset1:4\n"
        "v_cvt_pk_bf16_f32 v98, v20, v21\n"
        "v_mfma_f32_16x16x32_bf16 v[136:139], v[44:47], v[56:59], v[136:139]\n"
        "ds_read_b64_tr_b16 v[124:125], v9 offset:16\n"
        "v_cvt_pk_bf16_f32 v99, v22, v23\n"
        "v_cvt_pk_bf16_f32 v123, v116, v117\n"
        "v_cvt_pk_bf16_f32 v127, v118, v119\n"
        "v_mfma_f32_16x16x32_bf16 v[44:47], v[148:151], v[56:59], 0\n"
        "ds_read2_b32 v[70:71], v11 offset0:2 offset1:5\n"
        "v_mov_b32_dpp v163, v19 row_shr:4 row_mask:0xf bank_mask:0xf bound_ctrl:0\n"
        "v_mfma_f32_16x16x32_bf16 v[44:47], v[152:155], v[36:39], v[44:47]\n"
        "ds_read_b64_tr_b16 v[66:67], v9 offset:32\n"
        "v_cvt_pk_bf16_f32 v65, v136, v137\n"
        "v_mfma_f32_16x16x32_bf16 v[44:47], v[112:115], v[132:135], v[44:47]\n"
        "v_rcp_f32 v159, v69\n"
        "v_mfma_f32_16x16x32_bf16 v[20:23], v[100:103], v[132:135], 0\n"
        "v_and_b32 v156, s28, v159\n"
        "v_sub_f32 v151, v159, v156\n"
        "v_mfma_f32_16x16x32_bf16 v[20:23], v[108:111], v[36:39], v[20:23]\n"
        "v_cvt_pk_bf16_f32 v157, v156, v151\n"
        "v_min3_f32 v83, v83, v69, v159\n"
        "v_mfma_f32_16x16x32_bf16 v[20:23], v[104:107], v[56:59], v[20:23]\n"
        "v_cmp_nle_f32 vcc, 0x2b800000, v83\n"
        "v_mfma_f32_16x16x4_4b_bf16 v[106:121], v[74:75], v[156:157], 0 blgp:4\n"
        "ds_write_b16 v1, v80\n"
        "v_cvt_pk_bf16_f32 v69, v138, v139\n"
        "v_cvt_pk_bf16_f32 v58, v44, v45\n"
        "v_cvt_pk_bf16_f32 v59, v46, v47\n"
        "s_andn2_b64 vcc, vcc, s[56:57]\n"
        "s_cbranch_scc1 CREDO_%=\n"
        "v_cvt_pk_bf16_f32 v56, v20, v21\n"
        "v_cvt_pk_bf16_f32 v57, v22, v23\n"
        "s_add_u32 s31, s31, 1\n"
        "s_mul_i32 s38, s31, 1344\n"
        "v_mfma_f32_16x16x32_bf16 v[148:151], v[40:43], v[84:87], 0\n"
        "s_waitcnt lgkmcnt(1)\n"
        "v_mul_f32 v144, v144, v106\n"
        "v_mul_f32 v145, v145, v107\n"
        "v_smfmac_f32_16x16x64_bf16 v[148:151], v[160:163], v[26:33], v18\n"
        "s_cmp_ge_u32 s31, s76\n"
        "s_cbranch_scc1 L34_%=\n"
        "L35_%=:\n"
        "ds_read_b64_tr_b16 v[28:29], v9 offset:48\n"
        "v_mfma_f32_16x16x32_bf16 v[44:47], v[40:43], v[92:95], 0\n"
        "s_cmp_ge_u32 s31, s71\n"
        "v_mul_f32 v146, v146, v108\n"
        "v_mul_f32 v147, v147, v109\n"
        "v_smfmac_f32_16x16x64_bf16 v[44:47], v[160:163], v[122:129], v18\n"
        "s_cselect_b32 s39, s70, s72\n"
        "ds_read_b64_tr_b16 v[124:125], v9 offset:64\n"
        "v_mul_f32 v60, v60, v110\n"
        "v_mul_f32 v61, v61, v111\n"
        "v_mfma_f32_16x16x32_bf16 v[136:139], v[40:43], v[52:55], 0\n"
        "s_cmp_ge_u32 s31, 20\n"
        "v_mul_f32 v62, v62, v112\n"
        "v_mul_f32 v63, v63, v113\n"
        "v_smfmac_f32_16x16x64_bf16 v[136:139], v[160:163], v[64:71], v18\n"
        "s_cselect_b32 s39, s22, s39\n"
        "ds_read_b64_tr_b16 v[66:67], v9 offset:80\n"
        "v_mul_f32 v76, v76, v114\n"
        "v_mul_f32 v77, v77, v115\n"
        "v_mfma_f32_16x16x32_bf16 v[20:23], v[40:43], v[88:91], 0\n"
        "s_waitcnt lgkmcnt(2)\n"
        "ds_read_b128 v[36:39], v2\n"
        "v_mul_f32 v78, v78, v116\n"
        "v_smfmac_f32_16x16x64_bf16 v[20:23], v[160:163], v[26:33], v18 abid:1\n"
        "s_cmp_ge_u32 s31, 21\n"
        "s_cselect_b32 s39, s23, s39\n"
        "ds_read_b128 v[130:133], v3\n"
        "v_mfma_f32_16x16x4_4b_bf16 v[100:115], v[74:75], v[156:157], 0 blgp:6\n"
        "v_mul_f32 v79, v79, v117\n"
        "v_fma_f32 v82, v82, v118, v19\n"
        "v_cvt_pk_bf16_f32 v156, v144, v145\n"
        "v_cvt_pk_bf16_f32 v157, v146, v147\n"
        "v_cvt_pk_bf16_f32 v158, v60, v61\n"
        "v_cvt_pk_bf16_f32 v159, v62, v63\n"
        "v_cvt_pk_bf16_f32 v152, v76, v77\n"
        "v_cvt_pk_bf16_f32 v154, v78, v79\n"
        "v_cvt_pk_bf16_f32 v153, v82, 0\n"
        "v_cvt_pk_bf16_f32 v134, v148, v44\n"
        "v_cvt_pk_bf16_f32 v135, v136, v20\n"
        "v_mfma_f32_16x16x32_bf16 v[76:79], v[40:43], v[96:99], 0\n"
        "s_waitcnt lgkmcnt(2)\n"
        "v_mul_f32 v140, v140, v100\n"
        "v_mul_f32 v141, v141, v101\n"
        "v_smfmac_f32_16x16x64_bf16 v[76:79], v[160:163], v[122:129], v18 abid:1\n"
        "s_add_u32 s39, s39, s38\n"
        "s_mul_i32 s68, s31, 3\n"
        "v_mul_f32 v142, v142, v102\n"
        "v_mul_f32 v143, v143, v103\n"
        "v_mov_b64 v[100:101], 0\n"
        "v_mfma_f32_16x16x32_bf16 v[144:147], v[40:43], v[56:59], 0\n"
        "s_lshr_b32 s38, s31, 3\n"
        "v_mov_b64 v[102:103], 0\n"
        "v_mov_b64 v[60:61], 0\n"
        "v_smfmac_f32_16x16x64_bf16 v[144:147], v[160:163], v[64:71], v18 abid:1\n"
        "s_waitcnt lgkmcnt(0)\n"
        "s_add_u32 s38, s38, 11\n"
        "v_mov_b64 v[62:63], 0\n"
        "v_mov_b32_dpp v155, v19 row_shr:4 row_mask:0xf bank_mask:0xf bound_ctrl:0\n"
        "v_mul_f32 v164, v164, v104\n"
        "v_smfmac_f32_16x16x64_bf16 v[100:103], v[36:39], v[84:91], v4\n"
        "s_bitcmp1_b32 s31, 0\n"
        "v_mov_b64 v[40:41], 0\n"
        "v_smfmac_f32_16x16x64_bf16 v[100:103], v[130:133], v[26:33], v4 abid:1\n"
        "s_cselect_b32 s38, 3, s38\n"
        "s_add_u32 s37, s35, 0\n"
        "ds_read_b64_tr_b16 v[28:29], v9 offset:3328\n"
        "v_mov_b64 v[42:43], 0\n"
        "v_smfmac_f32_16x16x64_bf16 v[60:63], v[36:39], v[92:99], v4\n"
        "s_add_u32 s68, s68, s38\n"
        "v_cvt_pk_bf16_f32 v136, v76, v144\n"
        "v_smfmac_f32_16x16x64_bf16 v[60:63], v[130:133], v[122:129], v4 abid:1\n"
        "s_cmp_lt_u32 s31, s14\n"
        "ds_read_b64_tr_b16 v[124:125], v9 offset:3344\n"
        "v_mul_f32 v165, v165, v105\n"
        "v_cvt_pk_bf16_f32 v160, v149, v45\n"
        "v_smfmac_f32_16x16x64_bf16 v[40:43], v[36:39], v[52:59], v4\n"
        "s_cselect_b32 s68, s68, 0\n"
        "v_cvt_pk_bf16_f32 v116, v150, v46\n"
        "v_smfmac_f32_16x16x64_bf16 v[40:43], v[130:133], v[64:71], v4 abid:1\n"
        "buffer_store_dwordx3 v[134:136], v12, s[8:11], s37 offen sc0 sc1\n"
        "ds_read_b64_tr_b16 v[66:67], v9 offset:3360\n"
        "v_cvt_pk_bf16_f32 v74, v151, v47\n"
        "v_cvt_pk_bf16_f32 v161, v137, v21\n"
        "v_cvt_pk_bf16_f32 v117, v138, v22\n"
        "v_cvt_pk_bf16_f32 v75, v139, v23\n"
        "v_cvt_pk_bf16_f32 v162, v77, v145\n"
        "v_mfma_f32_16x16x32_bf16 v[136:139], v[156:159], v[84:87], 0\n"
        "s_waitcnt lgkmcnt(2)\n"
        "v_cvt_pk_bf16_f32 v118, v78, v146\n"
        "v_cvt_pk_bf16_f32 v76, v79, v147\n"
        "v_smfmac_f32_16x16x64_bf16 v[136:139], v[152:155], v[26:33], v18\n"
        "s_add_u32 s37, s35, 0\n"
        "s_and_b32 s38, s31, 7\n"
        "ds_read_b64_tr_b16 v[28:29], v9 offset:3376\n"
        "v_add_f32 v100, v100, v101\n"
        "v_mfma_f32_16x16x32_bf16 v[132:135], v[156:159], v[92:95], 0\n"
        "s_waitcnt lgkmcnt(2)\n"
        "v_add_f32 v102, v102, v103\n"
        "v_add_f32 v60, v60, v61\n"
        "v_smfmac_f32_16x16x64_bf16 v[132:135], v[152:155], v[122:129], v18\n"
        "buffer_store_dwordx3 v[160:162], v15, s[8:11], s37 offen offset:768 sc0 sc1\n"
        "ds_read_b64_tr_b16 v[124:125], v9 offset:3392\n"
        "s_cselect_b32 s30, 1, -7\n"
        "v_add_f32 v62, v62, v63\n"
        "v_cvt_pk_bf16_f32 v146, v100, v60\n"
        "v_add_f32 v40, v40, v41\n"
        "v_add_f32 v42, v42, v43\n"
        "v_mfma_f32_16x16x32_bf16 v[20:23], v[156:159], v[52:55], 0\n"
        "s_waitcnt lgkmcnt(2)\n"
        "s_add_u32 s37, s35, 98112\n"
        "s_mul_i32 s29, s30, 192\n"
        "v_smfmac_f32_16x16x64_bf16 v[20:23], v[152:155], v[64:71], v18\n"
        "s_mul_i32 s30, s30, 392\n"
        "s_cmp_eq_u32 s73, s18\n"
        "ds_read_b64_tr_b16 v[66:67], v9 offset:3408\n"
        "v_cvt_pk_bf16_f32 v147, v40, v102\n"
        "v_cvt_pk_bf16_f32 v148, v62, v42\n"
        "v_mul_f32 v166, v166, v106\n"
        "v_mfma_f32_16x16x32_bf16 v[160:163], v[156:159], v[88:91], 0\n"
        "s_waitcnt lgkmcnt(2)\n"
        "v_mul_f32 v167, v167, v107\n"
        "v_mul_f32 v48, v48, v108\n"
        "v_smfmac_f32_16x16x64_bf16 v[160:163], v[152:155], v[26:33], v18 abid:1\n"
        "buffer_store_dwordx3 v[116:118], v12, s[8:11], s37 offen offset:192 sc0 sc1 nt\n"
        "ds_read_b64_tr_b16 v[28:29], v9 offset:6656\n"
        "v_mul_f32 v49, v49, v109\n"
        "v_mul_f32 v50, v50, v110\n"
        "s_cselect_b32 s34, -7488, s75\n"
        "s_add_u32 s37, s35, 98112\n"
        "v_mul_f32 v51, v51, v111\n"
        "v_mfma_f32_16x16x32_bf16 v[116:119], v[156:159], v[96:99], 0\n"
        "s_waitcnt lgkmcnt(2)\n"
        "v_fma_f32 v24, v24, v112, v19\n"
        "v_cvt_pk_bf16_f32 v36, v140, v141\n"
        "v_smfmac_f32_16x16x64_bf16 v[116:119], v[152:155], v[122:129], v18 abid:1\n"
        "s_add_u32 s73, s73, s34\n"
        "s_xor_b32 s74, s74, 4\n"
        "ds_read_b64_tr_b16 v[124:125], v9 offset:6672\n"
        "v_cvt_pk_bf16_f32 v37, v142, v143\n"
        "v_cvt_pk_bf16_f32 v38, v164, v165\n"
        "v_cvt_pk_bf16_f32 v39, v166, v167\n"
        "v_mfma_f32_16x16x32_bf16 v[40:43], v[156:159], v[56:59], 0\n"
        "s_waitcnt lgkmcnt(2)\n"
        "v_cvt_pk_bf16_f32 v44, v48, v49\n"
        "v_cvt_pk_bf16_f32 v46, v50, v51\n"
        "v_smfmac_f32_16x16x64_bf16 v[40:43], v[152:155], v[64:71], v18 abid:1\n"
        "buffer_store_dwordx3 v[74:76], v14, s[8:11], s37 offen offset:960 sc0 sc1 nt\n"
        "ds_read_b64_tr_b16 v[66:67], v9 offset:6688\n"
        "s_add_u32 s37, s35, 588672\n"
        "s_add_u32 s46, s37, 1344\n"
        "v_mov_b32 v158, s74\n"
        "v_cvt_pk_bf16_f32 v45, v24, 0\n"
        "v_mov_b32_dpp v47, v19 row_shr:4 row_mask:0xf bank_mask:0xf bound_ctrl:0\n"
        "v_mfma_f32_16x16x32_bf16 v[80:83], v[36:39], v[84:87], 0\n"
        "s_waitcnt lgkmcnt(2)\n"
        "ds_read_b32 v157, v158\n"
        "v_add_u32 v7, s69, v7\n"
        "v_smfmac_f32_16x16x64_bf16 v[80:83], v[44:47], v[26:33], v18\n"
        "buffer_store_dwordx3 v[146:148], v12, s[44:47], s37 offen offset:1152 sc0 sc1 nt\n"
        "ds_read_b64_tr_b16 v[28:29], v9 offset:6704\n"
        "v_add_u32 v17, s69, v17\n"
        "v_add_u32 v11, s30, v11\n"
        "s_mov_b32 exec_hi, 0xffff\n"
        "v_add_u32 v10, s69, v10\n"
        "s_mov_b32 exec_hi, -1\n"
        "v_mfma_f32_16x16x32_bf16 v[72:75], v[36:39], v[92:95], 0\n"
        "s_waitcnt lgkmcnt(2)\n"
        "v_writelane_b32 v10, s73, 56\n"
        "v_smfmac_f32_16x16x64_bf16 v[72:75], v[44:47], v[122:129], v18\n"
        "ds_read_b64_tr_b16 v[124:125], v9 offset:6720\n"
        "v_mfma_f32_16x16x32_bf16 v[60:63], v[36:39], v[52:55], 0\n"
        "s_waitcnt lgkmcnt(2)\n"
        "v_readfirstlane_b32 s40, v157\n"
        "s_cmp_lt_i32 s40, s68\n"
        "v_smfmac_f32_16x16x64_bf16 v[60:63], v[44:47], v[64:71], v18\n"
        "ds_read_b64_tr_b16 v[66:67], v9 offset:6736\n"
        "v_mfma_f32_16x16x32_bf16 v[164:167], v[36:39], v[88:91], 0\n"
        "s_waitcnt lgkmcnt(2)\n"
        "v_xor_b32 v159, 4, v158\n"
        "s_cbranch_scc1 L38_%=\n"
        "L39_%=:\n"
        "v_smfmac_f32_16x16x64_bf16 v[164:167], v[44:47], v[26:33], v18 abid:1\n"
        "s_mov_b64 exec, s[62:63]\n"
        "v_add_u32 v16, s29, v16\n"
        "v_add_u32 v8, s34, v8\n"
        "s_mov_b64 exec, -1\n"
        "ds_read_b128 v[104:107], v7\n"
        "ds_read_b128 v[148:151], v7 offset:3328\n"
        "ds_read_b128 v[108:111], v7 offset:64\n"
        "ds_read_b128 v[152:155], v7 offset:3392\n"
        "ds_read_b128 v[100:103], v7 offset:128\n"
        "ds_read_b128 v[112:115], v7 offset:3456\n"
        "s_mov_b64 exec, s[50:51]\n"
        "v_add_u32 v5, s29, v5\n"
        "v_add_u32 v6, s34, v6\n"
        "s_mov_b64 exec, -1\n"
        "v_mfma_f32_16x16x32_bf16 v[140:143], v[36:39], v[96:99], 0\n"
        "s_waitcnt lgkmcnt(6)\n"
        "v_add_u32 v9, s69, v9\n"
        "s_sub_u32 s69, 0, s69\n"
        "v_smfmac_f32_16x16x64_bf16 v[140:143], v[44:47], v[122:129], v18 abid:1\n"
        "s_mov_b64 exec, 1\n"
        "ds_inc_u32 v159, v1 offset:8\n"
        "s_mov_b64 exec, -1\n"
        "v_mfma_f32_16x16x32_bf16 v[76:79], v[36:39], v[56:59], 0\n"
        "s_mov_b32 s36, s35\n"
        "s_mov_b32 s35, s39\n"
        "v_smfmac_f32_16x16x64_bf16 v[76:79], v[44:47], v[64:71], v18 abid:1\n"
        "s_setprio 0\n"
        "CNEXT_%=:\n"
        "s_cmp_ge_u32 s31, s14\n"
        "s_cbranch_scc1 CEND_%=\n"
        "s_cmp_gt_i32 s29, 0\n"
        "s_cbranch_scc1 CTOP_%=\n"
        "s_branch CFULL_%=\n"
        "CEND_%=:\n"
        "s_waitcnt lgkmcnt(0)\n"
        "s_nop 15\n"
        "v_cvt_pk_bf16_f32 v248, v136, v132\n"
        "v_cvt_pk_bf16_f32 v249, v20, v160\n"
        "v_cvt_pk_bf16_f32 v250, v116, v40\n"
        "s_add_u32 s37, s36, 196224\n"
        "buffer_store_dwordx3 v[248:250], v12, s[8:11], s37 offen offset:384 sc0 sc1 nt\n"
        "v_cvt_pk_bf16_f32 v252, v137, v133\n"
        "v_cvt_pk_bf16_f32 v253, v21, v161\n"
        "v_cvt_pk_bf16_f32 v254, v117, v41\n"
        "s_add_u32 s37, s36, 196224\n"
        "buffer_store_dwordx3 v[252:254], v13, s[8:11], s37 offen offset:1152 sc0 sc1 nt\n"
        "v_cvt_pk_bf16_f32 v248, v138, v134\n"
        "v_cvt_pk_bf16_f32 v249, v22, v162\n"
        "v_cvt_pk_bf16_f32 v250, v118, v42\n"
        "s_add_u32 s37, s36, 294336\n"
        "buffer_store_dwordx3 v[248:250], v12, s[8:11], s37 offen offset:576 sc0 sc1 nt\n"
        "v_cvt_pk_bf16_f32 v252, v139, v135\n"
        "v_cvt_pk_bf16_f32 v253, v23, v163\n"
        "v_cvt_pk_bf16_f32 v254, v119, v43\n"
        "s_add_u32 s37, s36, 392448\n"
        "buffer_store_dwordx3 v[252:254], v12, s[8:11], s37 offen sc0 sc1 nt\n"
        "v_cvt_pk_bf16_f32 v248, v80, v72\n"
        "v_cvt_pk_bf16_f32 v249, v60, v164\n"
        "v_cvt_pk_bf16_f32 v250, v140, v76\n"
        "s_add_u32 s37, s36, 392448\n"
        "v_cvt_pk_bf16_f32 v252, v81, v73\n"
        "v_cvt_pk_bf16_f32 v253, v61, v165\n"
        "v_cvt_pk_bf16_f32 v254, v141, v77\n"
        "buffer_store_dwordx3 v[248:250], v15, s[8:11], s37 offen offset:768 sc0 sc1 nt\n"
        "s_add_u32 s37, s36, 490560\n"
        "v_cvt_pk_bf16_f32 v246, v82, v74\n"
        "v_cvt_pk_bf16_f32 v247, v62, v166\n"
        "v_cvt_pk_bf16_f32 v248, v142, v78\n"
        "buffer_store_dwordx3 v[252:254], v12, s[8:11], s37 offen offset:192 sc0 sc1 nt\n"
        "s_add_u32 s37, s36, 490560\n"
        "buffer_store_dwordx3 v[246:248], v14, s[8:11], s37 offen offset:960 sc0 sc1 nt\n"
        "s_add_u32 s37, s36, 588672\n"
        "v_cvt_pk_bf16_f32 v252, v83, v75\n"
        "v_cvt_pk_bf16_f32 v253, v63, v167\n"
        "v_cvt_pk_bf16_f32 v254, v143, v79\n"
        "buffer_store_dwordx3 v[252:254], v12, s[8:11], s37 offen offset:384 sc0 sc1 nt\n"
        "s_nop 1\n"
        "s_endpgm\n"
        "L34_%=:\n"
        "s_nop 7\n"
        "v_mov_b32 v158, 110048\n"
        "L32_%=:\n"
        "ds_read_b32 v159, v158 offset:64\n"
        "s_waitcnt lgkmcnt(0)\n"
        "v_readfirstlane_b32 s40, v159\n"
        "s_nop 3\n"
        "s_cmp_ge_i32 s40, 1\n"
        "s_cbranch_scc1 L33_%=\n"
        "s_sleep 1\n"
        "s_sub_u32 s41, s41, 1\n"
        "s_cbranch_scc0 L32_%=\n"
        "s_endpgm\n"
        "L33_%=:\n"
        "v_mbcnt_lo_u32_b32 v159, -1, 0\n"
        "v_lshl_add_u32 v158, v159, 2, v158\n"
        "ds_read_b32 v159, v158 offset:68\n"
        "s_mov_b32 s14, s40\n"
        "s_mov_b32 s76, -1\n"
        "s_waitcnt lgkmcnt(0)\n"
        "v_readlane_b32 s22, v159, 0\n"
        "v_readlane_b32 s23, v159, 1\n"
        "s_nop 3\n"
        "s_add_u32 s22, s22, s17\n"
        "s_add_u32 s23, s23, s17\n"
        "s_nop 1\n"
        "s_branch L35_%=\n"
        "L38_%=:\n"
        "s_nop 7\n"
        "L36_%=:\n"
        "ds_read_b32 v157, v158 offset:0\n"
        "s_waitcnt lgkmcnt(0)\n"
        "v_readfirstlane_b32 s40, v157\n"
        "s_nop 3\n"
        "s_cmp_ge_i32 s40, s68\n"
        "s_cbranch_scc1 L37_%=\n"
        "s_sleep 1\n"
        "s_sub_u32 s41, s41, 1\n"
        "s_cbranch_scc0 L36_%=\n"
        "s_endpgm\n"
        "L37_%=:\n"
        "s_nop 1\n"
        "s_branch L39_%=\n"
        "CGRP_%=:\n"
        "s_setprio 3\n"
        "s_waitcnt lgkmcnt(0)\n"
        "s_nop 6\n"
        "ds_read_u16 v156, v0 offset:20576\n"
        "s_sub_u32 s60, s73, 101920\n"
        "ds_read_u16 v158, v0 offset:20578\n"
        "ds_read_u16 v146, v0 offset:20580\n"
        "ds_read_u16 v159, v0 offset:20582\n"
        "ds_read_u16 v145, v0 offset:20588\n"
        "ds_read_u16 v147, v0 offset:20590\n"
        "v_lshrrev_b32 v157, 8, v0\n"
        "s_waitcnt lgkmcnt(5)\n"
        "v_add_u32 v156, s60, v156\n"
        "v_subrev_u32 v144, 8320, v156\n"
        "v_min_u32 v156, v156, v144\n"
        "v_add_u32 v156, 101728, v156\n"
        "ds_read_b128 v[24:27], v156 offset:64\n"
        "ds_read_b128 v[28:31], v156 offset:128\n"
        "ds_read_b128 v[32:35], v156\n"
        "s_waitcnt lgkmcnt(1)\n"
        "s_waitcnt lgkmcnt(0)\n"
        "v_mfma_f32_16x16x32_bf16 v[44:47], v[168:171], v[32:35], 0\n"
        "v_add_u32 v158, s60, v158\n"
        "v_subrev_u32 v144, 8320, v158\n"
        "v_mfma_f32_16x16x32_bf16 v[44:47], v[172:175], v[24:27], v[44:47]\n"
        "v_min_u32 v158, v158, v144\n"
        "v_add_u32 v158, 101728, v158\n"
        "v_mfma_f32_16x16x32_bf16 v[44:47], v[176:179], v[28:31], v[44:47]\n"
        "v_add_u32 v146, s19, v146\n"
        "v_mad_i32_i24 v157, v157, -8, v146\n"
        "v_add_u32 v147, s19, v147\n"
        "v_add_u32 v159, s19, v159\n"
        "v_mfma_f32_16x16x32_bf16 v[52:55], v[180:183], v[32:35], 0\n"
        "v_mfma_f32_16x16x32_bf16 v[52:55], v[184:187], v[24:27], v[52:55]\n"
        "v_mfma_f32_16x16x32_bf16 v[52:55], v[188:191], v[28:31], v[52:55]\n"
        "v_mfma_f32_16x16x32_bf16 v[48:51], v[192:195], v[32:35], 0\n"
        "v_cvt_pk_bf16_f32 v36, v44, v45\n"
        "v_cvt_pk_bf16_f32 v37, v46, v47\n"
        "v_mfma_f32_16x16x32_bf16 v[48:51], v[196:199], v[24:27], v[48:51]\n"
        "v_mfma_f32_16x16x32_bf16 v[48:51], v[200:203], v[28:31], v[48:51]\n"
        "v_mfma_f32_16x16x32_bf16 v[44:47], v[204:207], v[32:35], 0\n"
        "v_mfma_f32_16x16x32_bf16 v[44:47], v[208:211], v[24:27], v[44:47]\n"
        "v_mfma_f32_16x16x32_bf16 v[44:47], v[212:215], v[28:31], v[44:47]\n"
        "v_cvt_pk_bf16_f32 v38, v52, v53\n"
        "v_cvt_pk_bf16_f32 v39, v54, v55\n"
        "ds_write2_b32 v157, v36, v38 offset0:0 offset1:1\n"
        "v_mfma_f32_16x16x32_bf16 v[56:59], v[216:219], v[32:35], 0\n"
        "v_mfma_f32_16x16x32_bf16 v[56:59], v[220:223], v[24:27], v[56:59]\n"
        "v_cvt_pk_bf16_f32 v52, v48, v49\n"
        "v_cvt_pk_bf16_f32 v53, v50, v51\n"
        "v_mfma_f32_16x16x32_bf16 v[56:59], v[224:227], v[28:31], v[56:59]\n"
        "v_cvt_pk_bf16_f32 v54, v44, v45\n"
        "v_cvt_pk_bf16_f32 v55, v46, v47\n"
        "v_mov_b32 v44, 0x3f803f80\n"
        "v_mov_b32 v45, 0x3f803f80\n"
        "v_mov_b32 v46, 0x3f803f80\n"
        "v_mov_b32 v47, 0x3f803f80\n"
        "v_mfma_f32_16x16x32_bf16 v[68:71], v[228:231], v[32:35], 0\n"
        "v_mfma_f32_16x16x32_bf16 v[68:71], v[232:235], v[24:27], v[68:71]\n"
        "v_cvt_pk_bf16_f32 v64, v56, v57\n"
        "v_cvt_pk_bf16_f32 v65, v58, v59\n"
        "v_mfma_f32_16x16x32_bf16 v[68:71], v[236:239], v[28:31], v[68:71]\n"
        "ds_write2_b32 v157, v37, v39 offset0:16 offset1:17\n"
        "ds_write2_b32 v157, v52, v54 offset0:8 offset1:9\n"
        "ds_write2_b32 v157, v53, v55 offset0:24 offset1:25\n"
        "s_nop 4\n"
        "v_cvt_pk_bf16_f32 v67, v70, v71\n"
        "v_cvt_pk_bf16_f32 v66, v68, v69\n"
        "v_mfma_f32_16x16x32_bf16 v[48:51], v[32:35], v[36:39], 0\n"
        "v_mfma_f32_16x16x32_bf16 v[48:51], v[24:27], v[52:55], v[48:51]\n"
        "ds_read_b64 v[156:157], v145\n"
        "v_mfma_f32_16x16x32_bf16 v[48:51], v[28:31], v[64:67], v[48:51]\n"
        "ds_read_b64 v[128:129], v158\n"
        "ds_read_b64 v[130:131], v158 offset:16\n"
        "ds_write_b128 v146, v[64:67] offset:128\n"
        "v_mfma_f32_16x16x32_bf16 v[56:59], v[44:47], v[32:35], 0\n"
        "ds_read_b64 v[144:145], v158 offset:32\n"
        "v_mfma_f32_16x16x32_bf16 v[56:59], v[44:47], v[24:27], v[56:59]\n"
        "v_mfma_f32_16x16x32_bf16 v[56:59], v[44:47], v[28:31], v[56:59]\n"
        "s_waitcnt lgkmcnt(3)\n"
        "v_mfma_f32_4x4x4_16b_bf16 v[52:55], v[128:129], v[156:157], 0\n"
        "s_waitcnt lgkmcnt(1)\n"
        "v_mfma_f32_4x4x4_16b_bf16 v[44:47], v[130:131], v[156:157], 0\n"
        "ds_read_b128 v[84:87], v0 offset:4112\n"
        "ds_read_b128 v[64:67], v0 offset:5136\n"
        "s_waitcnt lgkmcnt(2)\n"
        "v_mfma_f32_4x4x4_16b_bf16 v[36:39], v[144:145], v[156:157], 0\n"
        "s_mov_b32 vcc_lo, 0x22222222\n"
        "s_mov_b32 vcc_hi, 0\n"
        "v_cndmask_b32 v146, v48, v49, vcc\n"
        "v_mfma_f32_16x16x32_bf16 v[92:95], v[32:35], v[240:243], 0\n"
        "s_mov_b32 vcc_lo, 0x44444444\n"
        "s_mov_b32 vcc_hi, 0\n"
        "v_cndmask_b32 v146, v146, v50, vcc\n"
        "v_mfma_f32_16x16x32_bf16 v[92:95], v[24:27], v[244:247], v[92:95]\n"
        "ds_read_b128 v[88:91], v0 offset:6176\n"
        "v_mfma_f32_16x16x32_bf16 v[92:95], v[28:31], v[248:251], v[92:95]\n"
        "ds_read_b128 v[96:99], v0 offset:7200\n"
        "ds_read_b128 v[68:71], v0 offset:8224\n"
        "s_mov_b32 vcc_lo, 0x88888888\n"
        "s_mov_b32 vcc_hi, 0\n"
        "v_cndmask_b32 v146, v146, v51, vcc\n"
        "s_mov_b64 exec, 0x00f0000f\n"
        "ds_write2_b32 v147, v146, v56 offset0:0 offset1:1\n"
        "s_mov_b64 exec, -1\n"
        "s_waitcnt lgkmcnt(4)\n"
        "v_mfma_f32_16x16x32_bf16 v[48:51], v[32:35], v[252:255], 0\n"
        "v_cvt_pk_bf16_f32 v126, v92, v52\n"
        "v_cvt_pk_bf16_f32 v144, v93, v53\n"
        "v_mfma_f32_16x16x32_bf16 v[48:51], v[24:27], v[84:87], v[48:51]\n"
        "ds_read_b64 v[128:129], v158 offset:48\n"
        "v_cvt_pk_bf16_f32 v130, v94, v54\n"
        "v_mfma_f32_16x16x32_bf16 v[48:51], v[28:31], v[64:67], v[48:51]\n"
        "ds_read_b64 v[124:125], v158 offset:64\n"
        "v_cvt_pk_bf16_f32 v146, v95, v55\n"
        "s_waitcnt lgkmcnt(3)\n"
        "v_mfma_f32_16x16x32_bf16 v[64:67], v[32:35], v[88:91], 0\n"
        "ds_read_b64 v[122:123], v158 offset:80\n"
        "v_mfma_f32_16x16x32_bf16 v[64:67], v[24:27], v[96:99], v[64:67]\n"
        "ds_read_b128 v[56:59], v0 offset:9264\n"
        "v_mfma_f32_16x16x32_bf16 v[64:67], v[28:31], v[68:71], v[64:67]\n"
        "ds_read_b128 v[84:87], v0 offset:10288\n"
        "ds_read_b128 v[68:71], v0 offset:11312\n"
        "v_cvt_pk_bf16_f32 v127, v48, v44\n"
        "v_cvt_pk_bf16_f32 v145, v49, v45\n"
        "v_cvt_pk_bf16_f32 v131, v50, v46\n"
        "v_cvt_pk_bf16_f32 v147, v51, v47\n"
        "ds_write_b64 v159, v[126:127]\n"
        "s_waitcnt lgkmcnt(5)\n"
        "v_mfma_f32_4x4x4_16b_bf16 v[52:55], v[128:129], v[156:157], 0\n"
        "v_mfma_f32_4x4x4_16b_bf16 v[48:51], v[124:125], v[156:157], 0\n"
        "ds_read_b128 v[96:99], v0 offset:12352\n"
        "ds_read_b128 v[92:95], v0 offset:13376\n"
        "ds_read_b128 v[88:91], v0 offset:14400\n"
        "s_waitcnt lgkmcnt(6)\n"
        "v_mfma_f32_4x4x4_16b_bf16 v[44:47], v[122:123], v[156:157], 0\n"
        "v_cvt_pk_bf16_f32 v124, v64, v36\n"
        "v_cvt_pk_bf16_f32 v126, v65, v37\n"
        "v_cvt_pk_bf16_f32 v128, v66, v38\n"
        "v_cvt_pk_bf16_f32 v156, v67, v39\n"
        "s_waitcnt lgkmcnt(4)\n"
        "v_mfma_f32_16x16x32_bf16 v[120:123], v[32:35], v[56:59], 0\n"
        "v_mfma_f32_16x16x32_bf16 v[120:123], v[24:27], v[84:87], v[120:123]\n"
        "ds_read_b128 v[36:39], v0 offset:15440\n"
        "v_mfma_f32_16x16x32_bf16 v[120:123], v[28:31], v[68:71], v[120:123]\n"
        "ds_read_b128 v[56:59], v0 offset:16464\n"
        "ds_read_b128 v[64:67], v0 offset:17488\n"
        "ds_write_b64 v159, v[144:145] offset:392\n"
        "ds_write_b64 v159, v[130:131] offset:784\n"
        "s_waitcnt lgkmcnt(5)\n"
        "v_mfma_f32_16x16x32_bf16 v[68:71], v[32:35], v[96:99], 0\n"
        "v_mfma_f32_16x16x32_bf16 v[68:71], v[24:27], v[92:95], v[68:71]\n"
        "v_mfma_f32_16x16x32_bf16 v[68:71], v[28:31], v[88:91], v[68:71]\n"
        "v_cvt_pk_bf16_f32 v125, v120, v52\n"
        "v_cvt_pk_bf16_f32 v127, v121, v53\n"
        "v_cvt_pk_bf16_f32 v129, v122, v54\n"
        "v_cvt_pk_bf16_f32 v157, v123, v55\n"
        "s_waitcnt lgkmcnt(1)\n"
        "v_mfma_f32_16x16x32_bf16 v[52:55], v[32:35], v[36:39], 0\n"
        "v_mfma_f32_16x16x32_bf16 v[52:55], v[24:27], v[56:59], v[52:55]\n"
        "v_mfma_f32_16x16x32_bf16 v[52:55], v[28:31], v[64:67], v[52:55]\n"
        "v_cvt_pk_bf16_f32 v144, v68, v48\n"
        "v_cvt_pk_bf16_f32 v130, v69, v49\n"
        "v_cvt_pk_bf16_f32 v122, v70, v50\n"
        "v_cvt_pk_bf16_f32 v120, v71, v51\n"
        "ds_write_b64 v159, v[146:147] offset:1176\n"
        "ds_write_b64 v159, v[124:125] offset:8\n"
        "ds_write_b64 v159, v[126:127] offset:400\n"
        "ds_write_b64 v159, v[128:129] offset:792\n"
        "v_cvt_pk_bf16_f32 v145, v52, v44\n"
        "v_cvt_pk_bf16_f32 v131, v53, v45\n"
        "v_cvt_pk_bf16_f32 v123, v54, v46\n"
        "v_cvt_pk_bf16_f32 v121, v55, v47\n"
        "ds_write_b64 v159, v[156:157] offset:1184\n"
        "ds_write_b64 v159, v[144:145] offset:16\n"
        "ds_write_b64 v159, v[130:131] offset:408\n"
        "ds_write_b64 v159, v[122:123] offset:800\n"
        "ds_write_b64 v159, v[120:121] offset:1192\n"
        "s_waitcnt lgkmcnt(0)\n"
        "s_nop 6\n"
        "s_setprio 0\n"
        "s_setpc_b64 s[64:65]\n"
        "L50_%=:\n"
        "s_nop 7\n"
        "v_mov_b32 v166, 110048\n"
        "L48_%=:\n"
        "ds_read_b32 v167, v166 offset:64\n"
        "s_waitcnt lgkmcnt(0)\n"
        "v_readfirstlane_b32 s40, v167\n"
        "s_nop 3\n"
        "s_cmp_ge_i32 s40, 1\n"
        "s_cbranch_scc1 L49_%=\n"
        "s_sleep 1\n"
        "s_sub_u32 s41, s41, 1\n"
        "s_cbranch_scc0 L48_%=\n"
        "s_endpgm\n"
        "L49_%=:\n"
        "v_mbcnt_lo_u32_b32 v167, -1, 0\n"
        "v_lshl_add_u32 v166, v167, 2, v166\n"
        "ds_read_b32 v167, v166 offset:68\n"
        "s_mov_b32 s14, s40\n"
        "s_mov_b32 s76, -1\n"
        "s_waitcnt lgkmcnt(0)\n"
        "v_readlane_b32 s22, v167, 0\n"
        "v_readlane_b32 s23, v167, 1\n"
        "s_nop 3\n"
        "s_add_u32 s22, s22, s17\n"
        "s_add_u32 s23, s23, s17\n"
        "s_nop 1\n"
        "s_branch L51_%=\n"
        "L54_%=:\n"
        "s_nop 7\n"
        "L52_%=:\n"
        "ds_read_b32 v158, v159 offset:0\n"
        "s_waitcnt lgkmcnt(0)\n"
        "v_readfirstlane_b32 s40, v158\n"
        "s_nop 3\n"
        "s_cmp_ge_i32 s40, s68\n"
        "s_cbranch_scc1 L53_%=\n"
        "s_sleep 1\n"
        "s_sub_u32 s41, s41, 1\n"
        "s_cbranch_scc0 L52_%=\n"
        "s_endpgm\n"
        "L53_%=:\n"
        "s_nop 1\n"
        "s_branch L55_%=\n"
        "CREDO_%=:\n"
        "s_waitcnt lgkmcnt(0)\n"
        "s_nop 15\n"
        "s_mov_b32 s36, 0x20000000\n"
        "ds_read_b128 v[104:107], v7 offset:0\n"
        "ds_read_b128 v[148:151], v7 offset:3328\n"
        "ds_read_b128 v[108:111], v7 offset:64\n"
        "ds_read_b128 v[152:155], v7 offset:3392\n"
        "ds_read_b128 v[100:103], v7 offset:128\n"
        "ds_read_b128 v[112:115], v7 offset:3456\n"
        "s_waitcnt lgkmcnt(0)\n"
        "s_branch MTOP_%=\n"
        "MTOP_%=:\n"
        "v_mfma_f32_16x16x32_bf16 v[24:27], v[168:171], v[104:107], 0\n"
        "v_cvt_pk_bf16_f32 v120, v136, v132\n"
        "v_cvt_pk_bf16_f32 v121, v20, v160\n"
        "v_cvt_pk_bf16_f32 v122, v116, v40\n"
        "v_cvt_pk_bf16_f32 v124, v137, v133\n"
        "v_cvt_pk_bf16_f32 v125, v21, v161\n"
        "v_cvt_pk_bf16_f32 v126, v117, v41\n"
        "v_cvt_pk_bf16_f32 v128, v138, v134\n"
        "v_cvt_pk_bf16_f32 v129, v22, v162\n"
        "v_mfma_f32_16x16x32_bf16 v[24:27], v[172:175], v[108:111], v[24:27]\n"
        "v_cvt_pk_bf16_f32 v130, v118, v42\n"
        "v_cvt_pk_bf16_f32 v132, v139, v135\n"
        "v_cvt_pk_bf16_f32 v133, v23, v163\n"
        "v_cvt_pk_bf16_f32 v134, v119, v43\n"
        "v_cvt_pk_bf16_f32 v136, v80, v72\n"
        "v_cvt_pk_bf16_f32 v137, v60, v164\n"
        "v_cvt_pk_bf16_f32 v138, v140, v76\n"
        "v_cvt_pk_bf16_f32 v144, v81, v73\n"
        "v_mfma_f32_16x16x32_bf16 v[24:27], v[176:179], v[100:103], v[24:27]\n"
        "v_cvt_pk_bf16_f32 v145, v61, v165\n"
        "v_cvt_pk_bf16_f32 v146, v141, v77\n"
        "v_cvt_pk_bf16_f32 v156, v82, v74\n"
        "v_cvt_pk_bf16_f32 v157, v62, v166\n"
        "v_cvt_pk_bf16_f32 v158, v142, v78\n"
        "v_cvt_pk_bf16_f32 v160, v83, v75\n"
        "v_cvt_pk_bf16_f32 v161, v63, v167\n"
        "v_cvt_pk_bf16_f32 v162, v143, v79\n"
        "v_mfma_f32_16x16x32_bf16 v[32:35], v[188:191], v[100:103], 0\n"
        "v_cvt_pk_bf16_f32 v40, v24, v25\n"
        "v_cvt_pk_bf16_f32 v41, v26, v27\n"
        "v_mfma_f32_16x16x32_bf16 v[32:35], v[184:187], v[108:111], v[32:35]\n"
        "s_setprio 2\n"
        "v_mfma_f32_16x16x32_bf16 v[32:35], v[180:183], v[104:107], v[32:35]\n"
        "s_add_u32 s37, s36, 196224\n"
        "ds_read_b128 v[28:31], v16 offset:128\n"
        "ds_read_b32 v163, v10\n"
        "v_mfma_f32_16x16x32_bf16 v[24:27], v[192:195], v[104:107], 0\n"
        "ds_read_b128 v[20:23], v5\n"
        "v_mfma_f32_16x16x32_bf16 v[24:27], v[196:199], v[108:111], v[24:27]\n"
        "ds_read_b64 v[164:165], v11 offset:192\n"
        "v_mfma_f32_16x16x32_bf16 v[24:27], v[200:203], v[100:103], v[24:27]\n"
        "buffer_store_dwordx3 v[120:122], v12, s[8:11], s37 offen offset:384 sc0 sc1 nt\n"
        "v_cvt_pk_bf16_f32 v42, v32, v33\n"
        "v_cvt_pk_bf16_f32 v43, v34, v35\n"
        "s_waitcnt lgkmcnt(3)\n"
        "v_mfma_f32_16x16x32_bf16 v[48:51], v[28:31], v[100:103], 0\n"
        "ds_read_b128 v[52:55], v7 offset:6784\n"
        "ds_read_i16 v167, v0 offset:20584\n"
        "ds_read_i16 v159, v0 offset:20586\n"
        "v_cvt_pk_bf16_f32 v44, v24, v25\n"
        "v_cvt_pk_bf16_f32 v45, v26, v27\n"
        "s_waitcnt lgkmcnt(3)\n"
        "v_smfmac_f32_16x16x64_bf16 v[48:51], v[20:23], v[104:111], v4\n"
        "s_add_u32 s37, s36, 196224\n"
        "ds_read_b128 v[32:35], v7 offset:6656\n"
        "ds_read_b128 v[36:39], v7 offset:6720\n"
        "v_add_u32 v7, s69, v7\n"
        "v_mfma_f32_16x16x32_bf16 v[56:59], v[28:31], v[112:115], 0\n"
        "v_smfmac_f32_16x16x64_bf16 v[56:59], v[20:23], v[148:155], v4\n"
        "s_waitcnt lgkmcnt(4)\n"
        "v_mfma_f32_16x16x32_bf16 v[24:27], v[28:31], v[52:55], 0\n"
        "buffer_store_dwordx3 v[124:126], v13, s[8:11], s37 offen offset:1152 sc0 sc1 nt\n"
        "v_add_f32_dpp v165, v50, v51 quad_perm:[0,1,2,3] row_mask:0x1 bank_mask:0xf\n"
        "s_waitcnt lgkmcnt(2)\n"
        "v_add_u32 v167, v9, v167\n"
        "v_add_f32_dpp v164, v48, v49 quad_perm:[0,1,2,3] row_mask:0x1 bank_mask:0xf\n"
        "v_add_u32 v159, v9, v159\n"
        "v_add_f32_dpp v165, v58, v59 quad_perm:[0,1,2,3] row_mask:0x2 bank_mask:0xf\n"
        "v_add_f32_dpp v164, v56, v57 quad_perm:[0,1,2,3] row_mask:0x2 bank_mask:0xf\n"
        "s_waitcnt lgkmcnt(0)\n"
        "v_smfmac_f32_16x16x64_bf16 v[24:27], v[20:23], v[32:39], v4\n"
        "v_mfma_f32_16x16x32_bf16 v[28:31], v[212:215], v[100:103], 0\n"
        "s_add_u32 s37, s36, 294336\n"
        "v_mfma_f32_16x16x32_bf16 v[28:31], v[208:211], v[108:111], v[28:31]\n"
        "v_mfma_f32_16x16x32_bf16 v[28:31], v[204:207], v[104:107], v[28:31]\n"
        "v_mfma_f32_16x16x32_bf16 v[20:23], v[216:219], v[104:107], 0\n"
        "buffer_store_dwordx3 v[128:130], v12, s[8:11], s37 offen offset:576 sc0 sc1 nt\n"
        "v_mfma_f32_16x16x32_bf16 v[20:23], v[220:223], v[108:111], v[20:23]\n"
        "v_mfma_f32_16x16x32_bf16 v[20:23], v[224:227], v[100:103], v[20:23]\n"
        "s_add_u32 s37, s36, 392448\n"
        "v_add_f32_dpp v165, v26, v27 quad_perm:[0,1,2,3] row_mask:0x4 bank_mask:0xf\n"
        "v_mul_f32 v166, s52, v165\n"
        "v_fma_f32 v166, -v166, v165, v163\n"
        "v_cvt_pk_bf16_f32 v46, v28, v29\n"
        "v_cvt_pk_bf16_f32 v47, v30, v31\n"
        "v_add_f32_e64 v166, s53, |v166|\n"
        "v_rsq_f32 v163, v166\n"
        "v_mfma_f32_16x16x32_bf16 v[56:59], v[236:239], v[100:103], 0\n"
        "s_mov_b32 exec_hi, 0xffff\n"
        "ds_write_b32 v10, v163\n"
        "s_mov_b32 exec_hi, -1\n"
        "v_cvt_pk_bf16_f32 v48, v20, v21\n"
        "v_cvt_pk_bf16_f32 v49, v22, v23\n"
        "ds_read_b32 v20, v167\n"
        "v_add_f32_dpp v164, v24, v25 quad_perm:[0,1,2,3] row_mask:0x4 bank_mask:0xf\n"
        "v_mfma_f32_16x16x32_bf16 v[56:59], v[232:235], v[108:111], v[56:59]\n"
        "s_mov_b32 exec_hi, 0xffff\n"
        "v_add_u32 v10, s69, v10\n"
        "s_mov_b32 exec_hi, -1\n"
        "ds_read_b32 v21, v167 offset:832\n"
        "ds_read_b32 v22, v167 offset:1664\n"
        "ds_read_b32 v23, v167 offset:2496\n"
        "ds_read_b32 v24, v167 offset:3328\n"
        "v_mul_f32 v164, v164, v163\n"
        "v_mfma_f32_16x16x32_bf16 v[56:59], v[228:231], v[104:107], v[56:59]\n"
        "buffer_store_dwordx3 v[132:134], v12, s[8:11], s37 offen sc0 sc1 nt\n"
        "ds_read_b32 v25, v167 offset:4160\n"
        "ds_read_b32 v26, v167 offset:4992\n"
        "ds_read_b32 v27, v167 offset:5824\n"
        "ds_read_b32 v28, v167 offset:6656\n"
        "ds_read_b32 v29, v167 offset:7488\n"
        "ds_read_b32 v30, v167 offset:8320\n"
        "ds_read_b32 v31, v167 offset:9152\n"
        "v_cvt_pk_bf16_f32 v50, v56, v57\n"
        "v_cvt_pk_bf16_f32 v51, v58, v59\n"
        "v_readlane_b32 s58, v163, 56\n"
        "v_mfma_f32_16x16x32_bf16 v[68:71], v[104:107], v[40:43], 0\n"
        "s_mov_b64 exec, s[56:57]\n"
        "v_mov_b32 v164, 0xff800000\n"
        "s_mov_b64 exec, -1\n"
        "ds_read_b32 v165, v159\n"
        "ds_read_b32 v166, v159 offset:3328\n"
        "v_max_f32_dpp v147, v164, v164 row_ror:8 row_mask:0xf bank_mask:0xf\n"
        "v_mfma_f32_16x16x32_bf16 v[68:71], v[108:111], v[44:47], v[68:71]\n"
        "s_add_u32 s37, s36, 392448\n"
        "v_max_f32_dpp v147, v147, v147 row_ror:4 row_mask:0xf bank_mask:0xf\n"
        "ds_read_b32 v167, v159 offset:6656\n"
        "v_mfma_f32_16x16x32_bf16 v[68:71], v[100:103], v[48:51], v[68:71]\n"
        "v_max_f32_dpp v147, v147, v147 row_ror:2 row_mask:0xf bank_mask:0xf\n"
        "v_mfma_f32_16x16x32_bf16 v[76:79], v[112:115], v[48:51], 0\n"
        "v_mfma_f32_16x16x32_bf16 v[76:79], v[152:155], v[44:47], v[76:79]\n"
        "buffer_store_dwordx3 v[136:138], v15, s[8:11], s37 offen offset:768 sc0 sc1 nt\n"
        "v_max_f32_dpp v147, v147, v147 row_ror:1 row_mask:0xf bank_mask:0xf\n"
        "v_mfma_f32_16x16x32_bf16 v[76:79], v[148:151], v[40:43], v[76:79]\n"
        "ds_read_b128 v[56:59], v8 offset:128\n"
        "v_mfma_f32_16x16x32_bf16 v[72:75], v[32:35], v[40:43], 0\n"
        "s_add_u32 s37, s36, 490560\n"
        "ds_read2_b32 v[60:61], v6 offset0:0 offset1:2\n"
        "ds_read2_b32 v[62:63], v6 offset0:8 offset1:10\n"
        "s_waitcnt lgkmcnt(3)\n"
        "v_mul_f32 v68, v68, v20\n"
        "v_mul_f32 v69, v69, v21\n"
        "v_mul_f32 v70, v70, v22\n"
        "v_mul_f32 v71, v71, v23\n"
        "v_mul_f32 v76, v76, v24\n"
        "v_mfma_f32_16x16x32_bf16 v[72:75], v[36:39], v[44:47], v[72:75]\n"
        "v_max3_f32 v159, v68, v69, v70\n"
        "v_mul_f32 v77, v77, v25\n"
        "v_mul_f32 v78, v78, v26\n"
        "v_max3_f32 v159, v159, v71, v76\n"
        "v_mul_f32 v79, v79, v27\n"
        "v_max3_f32 v159, v159, v77, v78\n"
        "v_mfma_f32_16x16x32_bf16 v[72:75], v[52:55], v[48:51], v[72:75]\n"
        "s_waitcnt lgkmcnt(2)\n"
        "v_mfma_f32_16x16x32_bf16 v[80:83], v[56:59], v[48:51], 0\n"
        "buffer_store_dwordx3 v[144:146], v12, s[8:11], s37 offen offset:192 sc0 sc1 nt\n"
        "s_waitcnt lgkmcnt(0)\n"
        "v_smfmac_f32_16x16x64_bf16 v[80:83], v[60:63], v[40:47], v4\n"
        "v_mfma_f32_16x16x32_bf16 v[48:51], v[168:171], v[148:151], 0\n"
        "s_add_u32 s37, s36, 490560\n"
        "v_mfma_f32_16x16x32_bf16 v[48:51], v[172:175], v[152:155], v[48:51]\n"
        "v_mul_f32 v72, v72, v28\n"
        "v_mul_f32 v73, v73, v29\n"
        "v_mul_f32 v74, v74, v30\n"
        "v_max3_f32 v159, v159, v79, v72\n"
        "v_mul_f32 v75, v75, v31\n"
        "v_add_f32 v80, v80, v81\n"
        "v_mul_f32 v80, s58, v80\n"
        "v_max3_f32 v159, v159, v73, v74\n"
        "v_mfma_f32_16x16x32_bf16 v[48:51], v[176:179], v[112:115], v[48:51]\n"
        "s_mov_b64 exec, s[48:49]\n"
        "v_mov_b32 v80, 0xff800000\n"
        "s_mov_b64 exec, -1\n"
        "v_max3_f32 v159, v159, v75, v80\n"
        "v_mfma_f32_16x16x32_bf16 v[40:43], v[188:191], v[112:115], 0\n"
        "buffer_store_dwordx3 v[156:158], v14, s[8:11], s37 offen offset:960 sc0 sc1 nt\n"
        "v_permlane32_swap_b32 v159, v147\n"
        "v_max_f32 v159, v159, v147\n"
        "v_cvt_pk_bf16_f32 v44, v48, v49\n"
        "v_cvt_pk_bf16_f32 v45, v50, v51\n"
        "v_mov_b32 v158, v159\n"
        "v_mfma_f32_16x16x32_bf16 v[40:43], v[184:187], v[152:155], v[40:43]\n"
        "v_mfma_f32_16x16x32_bf16 v[40:43], v[180:183], v[148:151], v[40:43]\n"
        "s_add_u32 s37, s36, 588672\n"
        "v_permlane16_swap_b32 v159, v158\n"
        "v_max_f32 v159, v159, v158\n"
        "v_mov_b32 v147, v159\n"
        "v_mfma_f32_16x16x32_bf16 v[64:67], v[192:195], v[148:151], 0\n"
        "v_mfma_f32_16x16x32_bf16 v[64:67], v[196:199], v[152:155], v[64:67]\n"
        "v_permlane32_swap_b32 v159, v147\n"
        "v_mul_f32 v159, v159, v165\n"
        "v_cvt_pk_bf16_f32 v46, v40, v41\n"
        "v_cvt_pk_bf16_f32 v47, v42, v43\n"
        "v_fma_f32 v68, v68, v165, -v159\n"
        "v_fma_f32 v69, v69, v165, -v159\n"
        "v_fma_f32 v70, v70, v165, -v159\n"
        "v_mfma_f32_16x16x32_bf16 v[64:67], v[200:203], v[112:115], v[64:67]\n"
        "buffer_store_dwordx3 v[160:162], v12, s[8:11], s37 offen offset:384 sc0 sc1 nt\n"
        "v_exp_f32 v68, v68\n"
        "v_exp_f32 v69, v69\n"
        "v_exp_f32 v70, v70\n"
        "v_fma_f32 v71, v71, v165, -v159\n"
        "v_add_f32 v162, v68, v69\n"
        "v_exp_f32 v71, v71\n"
        "v_mfma_f32_16x16x32_bf16 v[40:43], v[212:215], v[112:115], 0\n"
        "v_cvt_pk_bf16_f32 v48, v64, v65\n"
        "v_cvt_pk_bf16_f32 v49, v66, v67\n"
        "v_fma_f32 v76, v76, v165, -v159\n"
        "v_add_f32 v162, v162, v70\n"
        "v_exp_f32 v76, v76\n"
        "v_fma_f32 v77, v77, v165, -v159\n"
        "v_add_f32 v162, v162, v71\n"
        "v_mfma_f32_16x16x32_bf16 v[40:43], v[208:211], v[152:155], v[40:43]\n"
        "v_exp_f32 v77, v77\n"
        "v_fma_f32 v78, v78, v165, -v159\n"
        "v_add_f32 v162, v162, v76\n"
        "v_exp_f32 v78, v78\n"
        "v_fma_f32 v79, v79, v165, -v159\n"
        "v_sub_f32 v164, v164, v147\n"
        "v_mfma_f32_16x16x32_bf16 v[40:43], v[204:207], v[148:151], v[40:43]\n"
        "v_add_f32 v162, v162, v77\n"
        "v_exp_f32 v79, v79\n"
        "v_fma_f32 v72, v72, v165, -v159\n"
        "v_mul_f32 v164, s58, v164\n"
        "v_add_f32 v162, v162, v78\n"
        "v_exp_f32 v72, v72\n"
        "v_mfma_f32_16x16x32_bf16 v[64:67], v[216:219], v[148:151], 0\n"
        "v_fma_f32 v73, v73, v165, -v159\n"
        "v_cvt_pk_bf16_f32 v50, v40, v41\n"
        "v_cvt_pk_bf16_f32 v51, v42, v43\n"
        "v_exp_f32 v164, v164\n"
        "v_add_f32 v162, v162, v79\n"
        "v_exp_f32 v73, v73\n"
        "v_mfma_f32_16x16x32_bf16 v[64:67], v[220:223], v[152:155], v[64:67]\n"
        "v_fma_f32 v74, v74, v165, -v159\n"
        "v_add_f32 v162, v162, v72\n"
        "v_exp_f32 v74, v74\n"
        "v_fma_f32 v75, v75, v165, -v159\n"
        "v_add_f32_dpp v161, v164, v164 row_ror:8 row_mask:0xf bank_mask:0xf\n"
        "v_add_f32 v162, v162, v73\n"
        "v_fma_f32 v80, v80, v165, -v159\n"
        "v_mfma_f32_16x16x32_bf16 v[64:67], v[224:227], v[112:115], v[64:67]\n"
        "v_exp_f32 v75, v75\n"
        "v_add_f32_dpp v161, v161, v161 row_ror:4 row_mask:0xf bank_mask:0xf\n"
        "v_add_f32 v162, v162, v74\n"
        "v_exp_f32 v80, v80\n"
        "v_add_f32_dpp v161, v161, v161 row_ror:2 row_mask:0xf bank_mask:0xf\n"
        "v_add_f32 v162, v162, v75\n"
        "v_mfma_f32_16x16x32_bf16 v[40:43], v[236:239], v[112:115], 0\n"
        "v_add_f32_dpp v161, v161, v161 row_ror:1 row_mask:0xf bank_mask:0xf\n"
        "v_cvt_pk_bf16_f32 v84, v64, v65\n"
        "v_cvt_pk_bf16_f32 v85, v66, v67\n"
        "v_add_f32 v162, v162, v80\n"
        "v_mul_f32 v68, v68, v20\n"
        "v_mul_f32 v69, v69, v21\n"
        "v_permlane32_swap_b32 v162, v161\n"
        "v_mfma_f32_16x16x32_bf16 v[40:43], v[232:235], v[152:155], v[40:43]\n"
        "v_add_f32 v162, v162, v161\n"
        "v_mov_b32 v165, v162\n"
        "v_mul_f32 v70, v70, v22\n"
        "v_mul_f32 v71, v71, v23\n"
        "v_permlane16_swap_b32 v162, v165\n"
        "v_add_f32 v162, v162, v165\n"
        "v_mul_f32 v76, v76, v24\n"
        "v_mfma_f32_16x16x32_bf16 v[40:43], v[228:231], v[148:151], v[40:43]\n"
        "v_rcp_f32 v162, v162\n"
        "v_mul_f32 v77, v77, v25\n"
        "v_mov_b32 v161, v162\n"
        "v_mul_f32 v78, v78, v26\n"
        "v_mul_f32 v79, v79, v27\n"
        "v_permlane32_swap_b32 v162, v161\n"
        "v_mfma_f32_16x16x32_bf16 v[64:67], v[104:107], v[44:47], 0\n"
        "v_mul_f32 v68, v68, v162\n"
        "v_cvt_pk_bf16_f32 v86, v40, v41\n"
        "v_cvt_pk_bf16_f32 v87, v42, v43\n"
        "v_mul_f32 v69, v69, v162\n"
        "v_mul_f32 v70, v70, v162\n"
        "v_mul_f32 v71, v71, v162\n"
        "v_mul_f32 v76, v76, v162\n"
        "v_mul_f32 v77, v77, v162\n"
        "v_mfma_f32_16x16x32_bf16 v[64:67], v[108:111], v[48:51], v[64:67]\n"
        "v_mul_f32 v78, v78, v162\n"
        "v_mul_f32 v79, v79, v162\n"
        "v_cvt_pk_bf16_f32 v40, v68, v69\n"
        "v_cvt_pk_bf16_f32 v41, v70, v71\n"
        "v_cvt_pk_bf16_f32 v42, v76, v77\n"
        "v_cvt_pk_bf16_f32 v43, v78, v79\n"
        "v_mul_f32 v72, v72, v28\n"
        "v_mul_f32 v73, v73, v29\n"
        "v_mfma_f32_16x16x32_bf16 v[64:67], v[100:103], v[84:87], v[64:67]\n"
        "v_mul_f32 v74, v74, v30\n"
        "v_mul_f32 v75, v75, v31\n"
        "v_mul_f32 v80, s58, v80\n"
        "v_mul_f32 v72, v72, v162\n"
        "v_mul_f32 v73, v73, v162\n"
        "v_mul_f32 v74, v74, v162\n"
        "v_mul_f32 v75, v75, v162\n"
        "v_fma_f32 v80, v80, v162, v19\n"
        "v_mfma_f32_16x16x32_bf16 v[88:91], v[112:115], v[84:87], 0\n"
        "v_mul_f32 v64, v64, v20\n"
        "v_mul_f32 v65, v65, v21\n"
        "v_mul_f32 v66, v66, v22\n"
        "v_mul_f32 v67, v67, v23\n"
        "v_max3_f32 v165, v64, v65, v66\n"
        "v_cvt_pk_bf16_f32 v68, v72, v73\n"
        "v_cvt_pk_bf16_f32 v70, v74, v75\n"
        "v_cvt_pk_bf16_f32 v69, v80, 0\n"
        "v_mfma_f32_16x16x32_bf16 v[88:91], v[152:155], v[48:51], v[88:91]\n"
        "v_mul_f32 v164, v164, v163\n"
        "v_mul_f32 v164, v164, v161\n"
        "v_cvt_pk_bf16_f32 v164, v164, 0\n"
        "ds_write_b16 v1, v164\n"
        "v_mfma_f32_16x16x32_bf16 v[88:91], v[148:151], v[44:47], v[88:91]\n"
        "v_mfma_f32_16x16x32_bf16 v[72:75], v[32:35], v[44:47], 0\n"
        "v_mfma_f32_16x16x32_bf16 v[72:75], v[36:39], v[48:51], v[72:75]\n"
        "v_mfma_f32_16x16x32_bf16 v[72:75], v[52:55], v[84:87], v[72:75]\n"
        "v_mfma_f32_16x16x32_bf16 v[80:83], v[56:59], v[84:87], 0\n"
        "v_smfmac_f32_16x16x64_bf16 v[80:83], v[60:63], v[44:51], v4\n"
        "v_mfma_f32_16x16x32_bf16 v[76:79], v[168:171], v[32:35], 0\n"
        "v_mfma_f32_16x16x32_bf16 v[76:79], v[172:175], v[36:39], v[76:79]\n"
        "v_mfma_f32_16x16x32_bf16 v[76:79], v[176:179], v[52:55], v[76:79]\n"
        "v_mul_f32 v88, v88, v24\n"
        "v_mul_f32 v89, v89, v25\n"
        "v_mul_f32 v90, v90, v26\n"
        "v_max3_f32 v165, v165, v67, v88\n"
        "v_mul_f32 v91, v91, v27\n"
        "v_add_f32 v80, v80, v81\n"
        "v_mul_f32 v72, v72, v28\n"
        "v_mul_f32 v80, s58, v80\n"
        "v_mfma_f32_16x16x32_bf16 v[48:51], v[188:191], v[52:55], 0\n"
        "s_mov_b64 exec, s[48:49]\n"
        "v_mov_b32 v80, 0xff800000\n"
        "s_mov_b64 exec, -1\n"
        "v_cvt_pk_bf16_f32 v44, v76, v77\n"
        "v_cvt_pk_bf16_f32 v45, v78, v79\n"
        "v_max3_f32 v165, v165, v89, v90\n"
        "v_mul_f32 v73, v73, v29\n"
        "v_mul_f32 v74, v74, v30\n"
        "v_max3_f32 v165, v165, v91, v72\n"
        "v_mfma_f32_16x16x32_bf16 v[48:51], v[184:187], v[36:39], v[48:51]\n"
        "v_mul_f32 v75, v75, v31\n"
        "v_max3_f32 v165, v165, v73, v74\n"
        "v_max3_f32 v165, v165, v75, v80\n"
        "v_mfma_f32_16x16x32_bf16 v[48:51], v[180:183], v[32:35], v[48:51]\n"
        "v_mfma_f32_16x16x32_bf16 v[84:87], v[192:195], v[32:35], 0\n"
        "v_mfma_f32_16x16x32_bf16 v[84:87], v[196:199], v[36:39], v[84:87]\n"
        "v_mfma_f32_16x16x32_bf16 v[84:87], v[200:203], v[52:55], v[84:87]\n"
        "v_mfma_f32_16x16x32_bf16 v[92:95], v[212:215], v[52:55], 0\n"
        "v_mfma_f32_16x16x32_bf16 v[92:95], v[208:211], v[36:39], v[92:95]\n"
        "v_mfma_f32_16x16x32_bf16 v[92:95], v[204:207], v[32:35], v[92:95]\n"
        "v_mfma_f32_16x16x32_bf16 v[76:79], v[216:219], v[32:35], 0\n"
        "v_mfma_f32_16x16x32_bf16 v[76:79], v[220:223], v[36:39], v[76:79]\n"
        "v_cvt_pk_bf16_f32 v46, v48, v49\n"
        "v_cvt_pk_bf16_f32 v47, v50, v51\n"
        "v_mfma_f32_16x16x32_bf16 v[76:79], v[224:227], v[52:55], v[76:79]\n"
        "v_cvt_pk_bf16_f32 v48, v84, v85\n"
        "v_cvt_pk_bf16_f32 v49, v86, v87\n"
        "v_mfma_f32_16x16x32_bf16 v[84:87], v[236:239], v[52:55], 0\n"
        "v_cvt_pk_bf16_f32 v50, v92, v93\n"
        "v_cvt_pk_bf16_f32 v51, v94, v95\n"
        "v_mfma_f32_16x16x32_bf16 v[84:87], v[232:235], v[36:39], v[84:87]\n"
        "v_mfma_f32_16x16x32_bf16 v[84:87], v[228:231], v[32:35], v[84:87]\n"
        "v_mfma_f32_16x16x32_bf16 v[132:135], v[104:107], v[44:47], 0\n"
        "v_cvt_pk_bf16_f32 v92, v76, v77\n"
        "v_cvt_pk_bf16_f32 v93, v78, v79\n"
        "v_mfma_f32_16x16x32_bf16 v[132:135], v[108:111], v[48:51], v[132:135]\n"
        "s_nop 3\n"
        "v_cvt_pk_bf16_f32 v94, v84, v85\n"
        "v_cvt_pk_bf16_f32 v95, v86, v87\n"
        "s_nop 1\n"
        "v_mfma_f32_16x16x32_bf16 v[132:135], v[100:103], v[92:95], v[132:135]\n"
        "v_mfma_f32_16x16x32_bf16 v[136:139], v[112:115], v[92:95], 0\n"
        "v_mfma_f32_16x16x32_bf16 v[136:139], v[152:155], v[48:51], v[136:139]\n"
        "v_mfma_f32_16x16x32_bf16 v[136:139], v[148:151], v[44:47], v[136:139]\n"
        "v_mfma_f32_16x16x32_bf16 v[120:123], v[32:35], v[44:47], 0\n"
        "v_mfma_f32_16x16x32_bf16 v[120:123], v[36:39], v[48:51], v[120:123]\n"
        "v_mfma_f32_16x16x32_bf16 v[120:123], v[52:55], v[92:95], v[120:123]\n"
        "v_mfma_f32_16x16x32_bf16 v[36:39], v[56:59], v[92:95], 0\n"
        "v_smfmac_f32_16x16x64_bf16 v[36:39], v[60:63], v[44:51], v4\n"
        "v_mul_f32 v132, v132, v20\n"
        "v_mul_f32 v133, v133, v21\n"
        "v_mul_f32 v134, v134, v22\n"
        "v_mul_f32 v135, v135, v23\n"
        "v_mul_f32 v136, v136, v24\n"
        "v_max3_f32 v163, v132, v133, v134\n"
        "v_mul_f32 v137, v137, v25\n"
        "v_mfma_f32_16x16x32_bf16 v[48:51], v[104:107], v[240:243], 0\n"
        "v_add_f32 v36, v36, v37\n"
        "v_mul_f32 v138, v138, v26\n"
        "v_max3_f32 v163, v163, v135, v136\n"
        "v_mul_f32 v139, v139, v27\n"
        "v_mul_f32 v120, v120, v28\n"
        "v_mul_f32 v36, s58, v36\n"
        "v_max3_f32 v163, v163, v137, v138\n"
        "v_mul_f32 v121, v121, v29\n"
        "v_mfma_f32_16x16x32_bf16 v[48:51], v[108:111], v[244:247], v[48:51]\n"
        "s_mov_b64 exec, s[48:49]\n"
        "v_mov_b32 v36, 0xff800000\n"
        "s_mov_b64 exec, -1\n"
        "v_mul_f32 v122, v122, v30\n"
        "v_max3_f32 v163, v163, v139, v120\n"
        "v_mul_f32 v123, v123, v31\n"
        "v_max3_f32 v163, v163, v121, v122\n"
        "v_max3_f32 v163, v163, v123, v36\n"
        "v_mfma_f32_16x16x32_bf16 v[48:51], v[100:103], v[248:251], v[48:51]\n"
        "ds_read_b128 v[76:79], v17\n"
        "v_permlane32_swap_b32 v165, v163\n"
        "v_max_f32 v165, v165, v163\n"
        "v_mov_b32 v164, v165\n"
        "v_mfma_f32_16x16x32_bf16 v[44:47], v[112:115], v[248:251], 0\n"
        "ds_read_b128 v[116:119], v17 offset:64\n"
        "v_permlane16_swap_b32 v165, v164\n"
        "v_max_f32 v165, v165, v164\n"
        "v_cvt_pk_bf16_f32 v32, v48, v49\n"
        "v_cvt_pk_bf16_f32 v33, v50, v51\n"
        "v_mov_b32 v163, v165\n"
        "v_mfma_f32_16x16x32_bf16 v[44:47], v[152:155], v[244:247], v[44:47]\n"
        "ds_read_b128 v[124:127], v17 offset:128\n"
        "v_permlane32_swap_b32 v165, v163\n"
        "v_mul_f32 v165, v165, v166\n"
        "v_mul_f32 v163, v163, v167\n"
        "v_fma_f32 v64, v64, v166, -v165\n"
        "v_fma_f32 v65, v65, v166, -v165\n"
        "v_fma_f32 v132, v132, v167, -v163\n"
        "v_mfma_f32_16x16x32_bf16 v[44:47], v[148:151], v[240:243], v[44:47]\n"
        "s_waitcnt lgkmcnt(2)\n"
        "ds_read_b128 v[60:63], v0 offset:5136\n"
        "v_fma_f32 v133, v133, v167, -v163\n"
        "v_exp_f32 v64, v64\n"
        "v_exp_f32 v65, v65\n"
        "v_fma_f32 v66, v66, v166, -v165\n"
        "v_fma_f32 v134, v134, v167, -v163\n"
        "v_mfma_f32_16x16x32_bf16 v[48:51], v[76:79], v[240:243], 0\n"
        "s_waitcnt lgkmcnt(2)\n"
        "ds_read_b128 v[84:87], v0 offset:4112\n"
        "v_cvt_pk_bf16_f32 v34, v44, v45\n"
        "v_cvt_pk_bf16_f32 v35, v46, v47\n"
        "v_exp_f32 v132, v132\n"
        "v_exp_f32 v133, v133\n"
        "v_fma_f32 v67, v67, v166, -v165\n"
        "v_mfma_f32_16x16x32_bf16 v[48:51], v[116:119], v[244:247], v[48:51]\n"
        "s_waitcnt lgkmcnt(2)\n"
        "v_exp_f32 v66, v66\n"
        "v_exp_f32 v134, v134\n"
        "v_fma_f32 v135, v135, v167, -v163\n"
        "v_add_f32 v83, v64, v65\n"
        "v_exp_f32 v67, v67\n"
        "v_mfma_f32_16x16x32_bf16 v[48:51], v[124:127], v[248:251], v[48:51]\n"
        "s_waitcnt lgkmcnt(1)\n"
        "v_fma_f32 v88, v88, v166, -v165\n"
        "v_add_f32 v164, v132, v133\n"
        "v_exp_f32 v135, v135\n"
        "v_fma_f32 v136, v136, v167, -v163\n"
        "v_add_f32 v83, v83, v66\n"
        "v_exp_f32 v88, v88\n"
        "v_mfma_f32_16x16x32_bf16 v[92:95], v[124:127], v[60:63], 0\n"
        "s_waitcnt lgkmcnt(0)\n"
        "v_cvt_pk_bf16_f32 v44, v48, v49\n"
        "v_cvt_pk_bf16_f32 v48, v50, v51\n"
        "v_fma_f32 v89, v89, v166, -v165\n"
        "v_add_f32 v164, v164, v134\n"
        "v_exp_f32 v136, v136\n"
        "v_fma_f32 v137, v137, v167, -v163\n"
        "v_add_f32 v83, v83, v67\n"
        "v_mfma_f32_16x16x32_bf16 v[92:95], v[116:119], v[84:87], v[92:95]\n"
        "v_exp_f32 v89, v89\n"
        "v_fma_f32 v90, v90, v166, -v165\n"
        "v_add_f32 v164, v164, v135\n"
        "v_exp_f32 v137, v137\n"
        "v_fma_f32 v138, v138, v167, -v163\n"
        "v_add_f32 v83, v83, v88\n"
        "v_mfma_f32_16x16x32_bf16 v[92:95], v[76:79], v[252:255], v[92:95]\n"
        "v_exp_f32 v90, v90\n"
        "v_fma_f32 v91, v91, v166, -v165\n"
        "v_add_f32 v164, v164, v136\n"
        "v_exp_f32 v138, v138\n"
        "v_fma_f32 v139, v139, v167, -v163\n"
        "v_add_f32 v83, v83, v89\n"
        "v_mfma_f32_16x16x32_bf16 v[128:131], v[148:151], v[252:255], 0\n"
        "v_exp_f32 v91, v91\n"
        "v_cvt_pk_bf16_f32 v52, v92, v93\n"
        "v_cvt_pk_bf16_f32 v56, v94, v95\n"
        "v_fma_f32 v72, v72, v166, -v165\n"
        "v_add_f32 v164, v164, v137\n"
        "v_exp_f32 v139, v139\n"
        "v_mfma_f32_16x16x32_bf16 v[128:131], v[152:155], v[84:87], v[128:131]\n"
        "v_fma_f32 v120, v120, v167, -v163\n"
        "v_add_f32 v83, v83, v90\n"
        "v_exp_f32 v72, v72\n"
        "v_fma_f32 v73, v73, v166, -v165\n"
        "v_add_f32 v164, v164, v138\n"
        "v_exp_f32 v120, v120\n"
        "v_mfma_f32_16x16x32_bf16 v[128:131], v[112:115], v[60:63], v[128:131]\n"
        "ds_read_b128 v[140:143], v0 offset:6176\n"
        "v_fma_f32 v121, v121, v167, -v163\n"
        "v_add_f32 v83, v83, v91\n"
        "v_exp_f32 v73, v73\n"
        "v_fma_f32 v74, v74, v166, -v165\n"
        "v_add_f32 v164, v164, v139\n"
        "v_fma_f32 v122, v122, v167, -v163\n"
        "v_mfma_f32_16x16x32_bf16 v[144:147], v[100:103], v[60:63], 0\n"
        "ds_read_b128 v[96:99], v0 offset:7200\n"
        "v_cvt_pk_bf16_f32 v62, v128, v129\n"
        "v_cvt_pk_bf16_f32 v63, v130, v131\n"
        "v_exp_f32 v121, v121\n"
        "v_add_f32 v83, v83, v72\n"
        "v_exp_f32 v74, v74\n"
        "v_mfma_f32_16x16x32_bf16 v[144:147], v[108:111], v[84:87], v[144:147]\n"
        "ds_read_b128 v[92:95], v0 offset:8224\n"
        "v_fma_f32 v75, v75, v166, -v165\n"
        "v_add_f32 v164, v164, v120\n"
        "v_exp_f32 v122, v122\n"
        "v_fma_f32 v123, v123, v167, -v163\n"
        "v_add_f32 v83, v83, v73\n"
        "v_fma_f32 v80, v80, v166, -v165\n"
        "v_mfma_f32_16x16x32_bf16 v[144:147], v[104:107], v[252:255], v[144:147]\n"
        "s_waitcnt lgkmcnt(2)\n"
        "v_exp_f32 v75, v75\n"
        "v_add_f32 v164, v164, v121\n"
        "v_exp_f32 v123, v123\n"
        "v_fma_f32 v36, v36, v167, -v163\n"
        "v_add_f32 v83, v83, v74\n"
        "v_add_f32 v164, v164, v122\n"
        "v_mfma_f32_16x16x32_bf16 v[128:131], v[104:107], v[140:143], 0\n"
        "s_waitcnt lgkmcnt(1)\n"
        "v_cvt_pk_bf16_f32 v60, v144, v145\n"
        "v_cvt_pk_bf16_f32 v61, v146, v147\n"
        "v_exp_f32 v80, v80\n"
        "v_exp_f32 v36, v36\n"
        "v_add_f32 v83, v83, v75\n"
        "v_add_f32 v164, v164, v123\n"
        "v_mfma_f32_16x16x32_bf16 v[128:131], v[108:111], v[96:99], v[128:131]\n"
        "s_waitcnt lgkmcnt(0)\n"
        "v_add_f32 v83, v83, v80\n"
        "v_add_f32 v164, v164, v36\n"
        "v_mul_f32 v64, v64, v20\n"
        "v_mul_f32 v65, v65, v21\n"
        "v_permlane32_swap_b32 v83, v164\n"
        "v_add_f32 v83, v83, v164\n"
        "v_mov_b32 v167, v83\n"
        "v_mfma_f32_16x16x32_bf16 v[128:131], v[100:103], v[92:95], v[128:131]\n"
        "v_mul_f32 v66, v66, v22\n"
        "v_permlane16_swap_b32 v83, v167\n"
        "v_add_f32 v83, v83, v167\n"
        "v_rcp_f32 v83, v83\n"
        "v_mul_f32 v67, v67, v23\n"
        "v_mov_b32 v164, v83\n"
        "v_mfma_f32_16x16x32_bf16 v[160:163], v[112:115], v[92:95], 0\n"
        "v_mul_f32 v88, v88, v24\n"
        "v_cvt_pk_bf16_f32 v84, v128, v129\n"
        "v_cvt_pk_bf16_f32 v85, v130, v131\n"
        "v_permlane32_swap_b32 v83, v164\n"
        "v_mul_f32 v89, v89, v25\n"
        "v_mul_f32 v90, v90, v26\n"
        "v_mul_f32 v91, v91, v27\n"
        "v_mfma_f32_16x16x32_bf16 v[160:163], v[152:155], v[96:99], v[160:163]\n"
        "v_mul_f32 v64, v64, v83\n"
        "v_mul_f32 v65, v65, v83\n"
        "v_mul_f32 v66, v66, v83\n"
        "v_mul_f32 v67, v67, v83\n"
        "v_mul_f32 v88, v88, v83\n"
        "v_mul_f32 v89, v89, v83\n"
        "v_mul_f32 v90, v90, v83\n"
        "v_mul_f32 v91, v91, v83\n"
        "v_mfma_f32_16x16x32_bf16 v[160:163], v[148:151], v[140:143], v[160:163]\n"
        "ds_read_b128 v[144:147], v0 offset:11312\n"
        "v_cvt_pk_bf16_f32 v128, v64, v65\n"
        "v_cvt_pk_bf16_f32 v129, v66, v67\n"
        "v_cvt_pk_bf16_f32 v130, v88, v89\n"
        "v_cvt_pk_bf16_f32 v131, v90, v91\n"
        "v_mul_f32 v72, v72, v28\n"
        "v_mul_f32 v73, v73, v29\n"
        "v_mul_f32 v74, v74, v30\n"
        "v_mfma_f32_16x16x32_bf16 v[156:159], v[76:79], v[140:143], 0\n"
        "ds_read_b128 v[64:67], v0 offset:10288\n"
        "v_cvt_pk_bf16_f32 v86, v160, v161\n"
        "v_cvt_pk_bf16_f32 v87, v162, v163\n"
        "v_mul_f32 v75, v75, v31\n"
        "v_mul_f32 v80, s58, v80\n"
        "v_mul_f32 v72, v72, v83\n"
        "v_mul_f32 v73, v73, v83\n"
        "v_mul_f32 v74, v74, v83\n"
        "v_mfma_f32_16x16x32_bf16 v[156:159], v[116:119], v[96:99], v[156:159]\n"
        "ds_read_b128 v[88:91], v0 offset:9264\n"
        "v_mul_f32 v75, v75, v83\n"
        "v_fma_f32 v80, v80, v83, v19\n"
        "v_cvt_pk_bf16_f32 v140, v72, v73\n"
        "v_cvt_pk_bf16_f32 v142, v74, v75\n"
        "v_cvt_pk_bf16_f32 v141, v80, 0\n"
        "v_mul_f32 v132, v132, v20\n"
        "v_mul_f32 v133, v133, v21\n"
        "v_mfma_f32_16x16x32_bf16 v[156:159], v[124:127], v[92:95], v[156:159]\n"
        "s_waitcnt lgkmcnt(2)\n"
        "v_mul_f32 v134, v134, v22\n"
        "v_mul_f32 v135, v135, v23\n"
        "v_mul_f32 v136, v136, v24\n"
        "v_mul_f32 v137, v137, v25\n"
        "v_mul_f32 v138, v138, v26\n"
        "v_mul_f32 v139, v139, v27\n"
        "v_mul_f32 v132, v132, v164\n"
        "v_cvt_pk_bf16_f32 v92, v156, v157\n"
        "v_mfma_f32_16x16x32_bf16 v[20:23], v[124:127], v[144:147], 0\n"
        "s_waitcnt lgkmcnt(1)\n"
        "v_cvt_pk_bf16_f32 v96, v158, v159\n"
        "v_mul_f32 v133, v133, v164\n"
        "v_mul_f32 v134, v134, v164\n"
        "v_mul_f32 v135, v135, v164\n"
        "v_mul_f32 v136, v136, v164\n"
        "v_mul_f32 v137, v137, v164\n"
        "v_mul_f32 v138, v138, v164\n"
        "v_mul_f32 v139, v139, v164\n"
        "v_mfma_f32_16x16x32_bf16 v[20:23], v[116:119], v[64:67], v[20:23]\n"
        "s_waitcnt lgkmcnt(0)\n"
        "v_cvt_pk_bf16_f32 v24, v132, v133\n"
        "v_cvt_pk_bf16_f32 v25, v134, v135\n"
        "v_cvt_pk_bf16_f32 v26, v136, v137\n"
        "v_cvt_pk_bf16_f32 v27, v138, v139\n"
        "v_mul_f32 v120, v120, v28\n"
        "v_mul_f32 v121, v121, v29\n"
        "v_mul_f32 v122, v122, v30\n"
        "v_mul_f32 v123, v123, v31\n"
        "v_mfma_f32_16x16x32_bf16 v[20:23], v[76:79], v[88:91], v[20:23]\n"
        "v_mul_f32 v36, s58, v36\n"
        "v_mul_f32 v120, v120, v164\n"
        "v_mul_f32 v121, v121, v164\n"
        "v_mul_f32 v122, v122, v164\n"
        "v_mul_f32 v123, v123, v164\n"
        "v_fma_f32 v36, v36, v164, v19\n"
        "v_cvt_pk_bf16_f32 v28, v120, v121\n"
        "v_cvt_pk_bf16_f32 v30, v122, v123\n"
        "v_mfma_f32_16x16x32_bf16 v[72:75], v[148:151], v[88:91], 0\n"
        "v_cvt_pk_bf16_f32 v45, v20, v21\n"
        "v_cvt_pk_bf16_f32 v49, v22, v23\n"
        "v_cvt_pk_bf16_f32 v29, v36, 0\n"
        "v_add_u32 v17, s69, v17\n"
        "v_mfma_f32_16x16x32_bf16 v[72:75], v[152:155], v[64:67], v[72:75]\n"
        "v_mfma_f32_16x16x32_bf16 v[72:75], v[112:115], v[144:147], v[72:75]\n"
        "ds_read_b128 v[80:83], v0 offset:12352\n"
        "v_mfma_f32_16x16x32_bf16 v[20:23], v[100:103], v[144:147], 0\n"
        "ds_read_b128 v[120:123], v0 offset:13376\n"
        "v_mfma_f32_16x16x32_bf16 v[20:23], v[108:111], v[64:67], v[20:23]\n"
        "ds_read_b128 v[132:135], v0 offset:14400\n"
        "v_mfma_f32_16x16x32_bf16 v[20:23], v[104:107], v[88:91], v[20:23]\n"
        "s_waitcnt lgkmcnt(2)\n"
        "v_mfma_f32_16x16x32_bf16 v[136:139], v[104:107], v[80:83], 0\n"
        "v_cvt_pk_bf16_f32 v38, v72, v73\n"
        "v_cvt_pk_bf16_f32 v39, v74, v75\n"
        "s_waitcnt lgkmcnt(1)\n"
        "v_mfma_f32_16x16x32_bf16 v[136:139], v[108:111], v[120:123], v[136:139]\n"
        "s_waitcnt lgkmcnt(0)\n"
        "v_mfma_f32_16x16x32_bf16 v[136:139], v[100:103], v[132:135], v[136:139]\n"
        "v_cvt_pk_bf16_f32 v36, v20, v21\n"
        "v_cvt_pk_bf16_f32 v37, v22, v23\n"
        "v_mfma_f32_16x16x32_bf16 v[144:147], v[112:115], v[132:135], 0\n"
        "v_mfma_f32_16x16x32_bf16 v[144:147], v[152:155], v[120:123], v[144:147]\n"
        "v_mfma_f32_16x16x32_bf16 v[144:147], v[148:151], v[80:83], v[144:147]\n"
        "ds_read_b128 v[72:75], v0 offset:17488\n"
        "v_mfma_f32_16x16x32_bf16 v[88:91], v[76:79], v[80:83], 0\n"
        "ds_read_b128 v[20:23], v0 offset:16464\n"
        "v_cvt_pk_bf16_f32 v64, v136, v137\n"
        "v_cvt_pk_bf16_f32 v65, v138, v139\n"
        "v_mfma_f32_16x16x32_bf16 v[88:91], v[116:119], v[120:123], v[88:91]\n"
        "ds_read_b128 v[80:83], v0 offset:15440\n"
        "v_mfma_f32_16x16x32_bf16 v[88:91], v[124:127], v[132:135], v[88:91]\n"
        "v_cvt_pk_bf16_f32 v66, v144, v145\n"
        "v_cvt_pk_bf16_f32 v67, v146, v147\n"
        "s_waitcnt lgkmcnt(2)\n"
        "v_mfma_f32_16x16x32_bf16 v[120:123], v[124:127], v[72:75], 0\n"
        "s_waitcnt lgkmcnt(1)\n"
        "v_mfma_f32_16x16x32_bf16 v[120:123], v[116:119], v[20:23], v[120:123]\n"
        "s_waitcnt lgkmcnt(0)\n"
        "v_mfma_f32_16x16x32_bf16 v[120:123], v[76:79], v[80:83], v[120:123]\n"
        "ds_read2_b32 v[50:51], v11 offset0:0 offset1:3\n"
        "v_cvt_pk_bf16_f32 v53, v88, v89\n"
        "v_cvt_pk_bf16_f32 v57, v90, v91\n"
        "v_mov_b32_dpp v71, v19 row_shr:4 row_mask:0xf bank_mask:0xf bound_ctrl:0\n"
        "v_mfma_f32_16x16x32_bf16 v[76:79], v[148:151], v[80:83], 0\n"
        "ds_read_b64_tr_b16 v[46:47], v9 offset:0\n"
        "v_mfma_f32_16x16x32_bf16 v[76:79], v[152:155], v[20:23], v[76:79]\n"
        "ds_read2_b32 v[58:59], v11 offset0:1 offset1:4\n"
        "v_cvt_pk_bf16_f32 v93, v120, v121\n"
        "v_cvt_pk_bf16_f32 v97, v122, v123\n"
        "v_mfma_f32_16x16x32_bf16 v[76:79], v[112:115], v[72:75], v[76:79]\n"
        "ds_read_b64_tr_b16 v[54:55], v9 offset:16\n"
        "v_mfma_f32_16x16x32_bf16 v[112:115], v[100:103], v[72:75], 0\n"
        "ds_read2_b32 v[98:99], v11 offset0:2 offset1:5\n"
        "v_mfma_f32_16x16x32_bf16 v[112:115], v[108:111], v[20:23], v[112:115]\n"
        "s_add_u32 s31, s31, 1\n"
        "ds_read_b64_tr_b16 v[94:95], v9 offset:32\n"
        "v_mfma_f32_16x16x32_bf16 v[112:115], v[104:107], v[80:83], v[112:115]\n"
        "s_mul_i32 s38, s31, 1344\n"
        "v_cvt_pk_bf16_f32 v90, v76, v77\n"
        "v_cvt_pk_bf16_f32 v91, v78, v79\n"
        "v_mfma_f32_16x16x32_bf16 v[20:23], v[40:43], v[32:35], 0\n"
        "s_waitcnt lgkmcnt(3)\n"
        "s_cmp_ge_u32 s31, s76\n"
        "v_smfmac_f32_16x16x64_bf16 v[20:23], v[68:71], v[44:51], v18\n"
        "s_cbranch_scc1 L50_%=\n"
        "L51_%=:\n"
        "v_cvt_pk_bf16_f32 v88, v112, v113\n"
        "v_cvt_pk_bf16_f32 v89, v114, v115\n"
        "v_mfma_f32_16x16x32_bf16 v[72:75], v[40:43], v[60:63], 0\n"
        "s_waitcnt lgkmcnt(1)\n"
        "s_cmp_ge_u32 s31, s71\n"
        "v_smfmac_f32_16x16x64_bf16 v[72:75], v[68:71], v[52:59], v18\n"
        "s_cselect_b32 s39, s70, s72\n"
        "ds_read_b64_tr_b16 v[46:47], v9 offset:48\n"
        "v_mfma_f32_16x16x32_bf16 v[100:103], v[40:43], v[84:87], 0\n"
        "s_waitcnt lgkmcnt(1)\n"
        "s_cmp_ge_u32 s31, 20\n"
        "v_smfmac_f32_16x16x64_bf16 v[100:103], v[68:71], v[92:99], v18\n"
        "s_cselect_b32 s39, s22, s39\n"
        "ds_read_b64_tr_b16 v[54:55], v9 offset:64\n"
        "v_cvt_pk_bf16_f32 v152, v20, v72\n"
        "v_cvt_pk_bf16_f32 v160, v21, v73\n"
        "v_cvt_pk_bf16_f32 v156, v22, v74\n"
        "v_cvt_pk_bf16_f32 v164, v23, v75\n"
        "v_mfma_f32_16x16x32_bf16 v[104:107], v[40:43], v[36:39], 0\n"
        "s_cmp_ge_u32 s31, 21\n"
        "s_waitcnt lgkmcnt(1)\n"
        "v_smfmac_f32_16x16x64_bf16 v[104:107], v[68:71], v[44:51], v18 abid:1\n"
        "s_cselect_b32 s39, s23, s39\n"
        "ds_read_b64_tr_b16 v[94:95], v9 offset:80\n"
        "v_mfma_f32_16x16x32_bf16 v[80:83], v[40:43], v[64:67], 0\n"
        "s_add_u32 s39, s39, s38\n"
        "ds_read_b128 v[72:75], v2\n"
        "v_mov_b64 v[20:21], 0\n"
        "v_mov_b64 v[22:23], 0\n"
        "s_waitcnt lgkmcnt(2)\n"
        "v_smfmac_f32_16x16x64_bf16 v[80:83], v[68:71], v[52:59], v18 abid:1\n"
        "s_mul_i32 s68, s31, 3\n"
        "ds_read_b128 v[76:79], v3\n"
        "v_cvt_pk_bf16_f32 v153, v100, v104\n"
        "v_cvt_pk_bf16_f32 v161, v101, v105\n"
        "v_cvt_pk_bf16_f32 v157, v102, v106\n"
        "v_cvt_pk_bf16_f32 v165, v103, v107\n"
        "v_mfma_f32_16x16x32_bf16 v[100:103], v[40:43], v[88:91], 0\n"
        "s_lshr_b32 s38, s31, 3\n"
        "v_mov_b64 v[40:41], 0\n"
        "v_mov_b64 v[42:43], 0\n"
        "s_waitcnt lgkmcnt(2)\n"
        "v_smfmac_f32_16x16x64_bf16 v[100:103], v[68:71], v[92:99], v18 abid:1\n"
        "s_add_u32 s38, s38, 11\n"
        "s_waitcnt lgkmcnt(1)\n"
        "v_mov_b32_dpp v143, v19 row_shr:4 row_mask:0xf bank_mask:0xf bound_ctrl:0\n"
        "v_smfmac_f32_16x16x64_bf16 v[20:23], v[72:75], v[32:39], v4\n"
        "s_bitcmp1_b32 s31, 0\n"
        "v_mov_b64 v[68:69], 0\n"
        "v_mov_b64 v[70:71], 0\n"
        "s_waitcnt lgkmcnt(0)\n"
        "v_smfmac_f32_16x16x64_bf16 v[20:23], v[76:79], v[44:51], v4 abid:1\n"
        "s_cselect_b32 s38, 3, s38\n"
        "s_add_u32 s37, s35, 0\n"
        "v_cvt_pk_bf16_f32 v154, v80, v100\n"
        "v_cvt_pk_bf16_f32 v162, v81, v101\n"
        "v_cvt_pk_bf16_f32 v158, v82, v102\n"
        "v_cvt_pk_bf16_f32 v166, v83, v103\n"
        "v_smfmac_f32_16x16x64_bf16 v[40:43], v[72:75], v[60:67], v4\n"
        "s_add_u32 s68, s68, s38\n"
        "v_add_f32 v20, v20, v21\n"
        "v_add_f32 v22, v22, v23\n"
        "v_smfmac_f32_16x16x64_bf16 v[40:43], v[76:79], v[52:59], v4 abid:1\n"
        "s_cmp_lt_u32 s31, s14\n"
        "ds_read_b64_tr_b16 v[46:47], v9 offset:3328\n"
        "v_smfmac_f32_16x16x64_bf16 v[68:71], v[72:75], v[84:91], v4\n"
        "buffer_store_dwordx3 v[152:154], v12, s[8:11], s37 offen sc0 sc1\n"
        "s_cselect_b32 s68, s68, 0\n"
        "v_smfmac_f32_16x16x64_bf16 v[68:71], v[76:79], v[92:99], v4 abid:1\n"
        "s_add_u32 s37, s35, 0\n"
        "s_and_b32 s38, s31, 7\n"
        "ds_read_b64_tr_b16 v[54:55], v9 offset:3344\n"
        "v_add_f32 v40, v40, v41\n"
        "v_add_f32 v42, v42, v43\n"
        "v_cvt_pk_bf16_f32 v152, v20, v40\n"
        "v_mfma_f32_16x16x32_bf16 v[136:139], v[128:131], v[32:35], 0\n"
        "s_cselect_b32 s30, 1, -7\n"
        "v_add_f32 v68, v68, v69\n"
        "v_add_f32 v70, v70, v71\n"
        "v_cvt_pk_bf16_f32 v153, v68, v22\n"
        "v_cvt_pk_bf16_f32 v154, v42, v70\n"
        "s_waitcnt lgkmcnt(1)\n"
        "v_smfmac_f32_16x16x64_bf16 v[136:139], v[140:143], v[44:51], v18\n"
        "s_mul_i32 s29, s30, 192\n"
        "ds_read_b64_tr_b16 v[94:95], v9 offset:3360\n"
        "v_mfma_f32_16x16x32_bf16 v[132:135], v[128:131], v[60:63], 0\n"
        "buffer_store_dwordx3 v[160:162], v15, s[8:11], s37 offen offset:768 sc0 sc1\n"
        "s_mul_i32 s30, s30, 392\n"
        "s_waitcnt lgkmcnt(1)\n"
        "v_smfmac_f32_16x16x64_bf16 v[132:135], v[140:143], v[52:59], v18\n"
        "s_add_u32 s37, s35, 98112\n"
        "s_cmp_eq_u32 s73, s18\n"
        "ds_read_b64_tr_b16 v[46:47], v9 offset:3376\n"
        "v_mfma_f32_16x16x32_bf16 v[20:23], v[128:131], v[84:87], 0\n"
        "s_cselect_b32 s34, -7488, s75\n"
        "s_waitcnt lgkmcnt(1)\n"
        "v_smfmac_f32_16x16x64_bf16 v[20:23], v[140:143], v[92:99], v18\n"
        "s_add_u32 s73, s73, s34\n"
        "ds_read_b64_tr_b16 v[54:55], v9 offset:3392\n"
        "v_mov_b32_dpp v31, v19 row_shr:4 row_mask:0xf bank_mask:0xf bound_ctrl:0\n"
        "v_mfma_f32_16x16x32_bf16 v[160:163], v[128:131], v[36:39], 0\n"
        "buffer_store_dwordx3 v[156:158], v12, s[8:11], s37 offen offset:192 sc0 sc1 nt\n"
        "s_xor_b32 s74, s74, 4\n"
        "v_writelane_b32 v10, s73, 56\n"
        "v_add_u32 v11, s30, v11\n"
        "s_mov_b64 exec, s[62:63]\n"
        "v_add_u32 v16, s29, v16\n"
        "v_add_u32 v8, s34, v8\n"
        "s_mov_b64 exec, -1\n"
        "s_waitcnt lgkmcnt(1)\n"
        "v_smfmac_f32_16x16x64_bf16 v[160:163], v[140:143], v[44:51], v18 abid:1\n"
        "s_add_u32 s37, s35, 98112\n"
        "ds_read_b64_tr_b16 v[94:95], v9 offset:3408\n"
        "v_mov_b32 v159, s74\n"
        "s_mov_b64 exec, s[50:51]\n"
        "v_add_u32 v5, s29, v5\n"
        "v_add_u32 v6, s34, v6\n"
        "s_mov_b64 exec, -1\n"
        "v_mfma_f32_16x16x32_bf16 v[116:119], v[128:131], v[64:67], 0\n"
        "s_waitcnt lgkmcnt(1)\n"
        "v_smfmac_f32_16x16x64_bf16 v[116:119], v[140:143], v[52:59], v18 abid:1\n"
        "ds_read_b64_tr_b16 v[46:47], v9 offset:6656\n"
        "v_mfma_f32_16x16x32_bf16 v[40:43], v[128:131], v[88:91], 0\n"
        "buffer_store_dwordx3 v[164:166], v14, s[8:11], s37 offen offset:960 sc0 sc1 nt\n"
        "s_add_u32 s37, s35, 588672\n"
        "s_add_u32 s46, s37, 1344\n"
        "ds_read_b32 v158, v159\n"
        "s_waitcnt lgkmcnt(2)\n"
        "v_smfmac_f32_16x16x64_bf16 v[40:43], v[140:143], v[92:99], v18 abid:1\n"
        "buffer_store_dwordx3 v[152:154], v12, s[44:47], s37 offen offset:1152 sc0 sc1 nt\n"
        "ds_read_b64_tr_b16 v[54:55], v9 offset:6672\n"
        "v_mfma_f32_16x16x32_bf16 v[80:83], v[24:27], v[32:35], 0\n"
        "s_waitcnt lgkmcnt(2)\n"
        "v_smfmac_f32_16x16x64_bf16 v[80:83], v[28:31], v[44:51], v18\n"
        "ds_read_b64_tr_b16 v[94:95], v9 offset:6688\n"
        "v_mfma_f32_16x16x32_bf16 v[72:75], v[24:27], v[60:63], 0\n"
        "s_waitcnt lgkmcnt(1)\n"
        "v_smfmac_f32_16x16x64_bf16 v[72:75], v[28:31], v[52:59], v18\n"
        "ds_read_b64_tr_b16 v[46:47], v9 offset:6704\n"
        "v_readfirstlane_b32 s40, v158\n"
        "v_mfma_f32_16x16x32_bf16 v[60:63], v[24:27], v[84:87], 0\n"
        "s_cmp_lt_i32 s40, s68\n"
        "s_waitcnt lgkmcnt(1)\n"
        "v_smfmac_f32_16x16x64_bf16 v[60:63], v[28:31], v[92:99], v18\n"
        "ds_read_b64_tr_b16 v[54:55], v9 offset:6720\n"
        "v_mfma_f32_16x16x32_bf16 v[164:167], v[24:27], v[36:39], 0\n"
        "v_xor_b32 v157, 4, v159\n"
        "s_cbranch_scc1 L54_%=\n"
        "L55_%=:\n"
        "ds_read_b128 v[104:107], v7\n"
        "ds_read_b128 v[148:151], v7 offset:3328\n"
        "ds_read_b128 v[108:111], v7 offset:64\n"
        "ds_read_b128 v[152:155], v7 offset:3392\n"
        "ds_read_b128 v[100:103], v7 offset:128\n"
        "ds_read_b128 v[112:115], v7 offset:3456\n"
        "s_waitcnt lgkmcnt(7)\n"
        "v_smfmac_f32_16x16x64_bf16 v[164:167], v[28:31], v[44:51], v18 abid:1\n"
        "ds_read_b64_tr_b16 v[94:95], v9 offset:6736\n"
        "v_add_u32 v9, s69, v9\n"
        "s_mov_b64 exec, 1\n"
        "ds_inc_u32 v157, v1 offset:8\n"
        "s_mov_b64 exec, -1\n"
        "v_mfma_f32_16x16x32_bf16 v[140:143], v[24:27], v[64:67], 0\n"
        "s_waitcnt lgkmcnt(8)\n"
        "s_sub_u32 s69, 0, s69\n"
        "v_smfmac_f32_16x16x64_bf16 v[140:143], v[28:31], v[52:59], v18 abid:1\n"
        "s_mov_b32 s36, s35\n"
        "v_mfma_f32_16x16x32_bf16 v[76:79], v[24:27], v[88:91], 0\n"
        "s_mov_b32 s35, s39\n"
        "s_waitcnt lgkmcnt(1)\n"
        "v_smfmac_f32_16x16x64_bf16 v[76:79], v[28:31], v[92:99], v18 abid:1\n"
        "s_setprio 0\n"
        "s_waitcnt lgkmcnt(0)\n"
        "s_nop 15\n"
        "s_branch CNEXT_%=\n"
        :: "{v0}"(tid), "{s[0:1]}"(kap), "{s2}"(bid), "{s[4:5]}"(tab) : CLOBBERS);
}

extern "C" void kernel_launch(void* const* d_in, const int* in_sizes, int n_in, void* d_out, int out_size, void* d_ws, size_t ws_size, hipStream_t stream) {
    (void)in_sizes; (void)n_in; (void)out_size; (void)ws_size;
    (void)hipFuncSetAttribute((const void*)swin_kernel, hipFuncAttributeMaxDynamicSharedMemorySize, LDS_BYTES);
    swin_kernel<<<256, 512, LDS_BYTES, stream>>>((const char*)d_in[0], (const char*)d_in[1], (const char*)d_in[2], (const char*)d_in[3], (const char*)d_in[4], (char*)d_out, (const char*)d_ws);
}
```

```cpp
#define LTOK_ 208
#define FPAD 32
#define NPR 36
#define NWC 22
#define LK0 0
#define LK1 0
#define LK2 1
#define LK3 2
#include <hip/hip_runtime.h>
#include <hip/hip_bf16.h>

constexpr int TOKB = 192, ROWB = 511 * TOKB;
constexpr unsigned FRAMEB = 511u * ROWB;
constexpr int LTOK = LTOK_;
constexpr int LFRAME = 48 * LTOK + FPAD;
constexpr int LBUF = 4 * LFRAME;
constexpr int LSTASH = 10 * 4 * LTOK;
constexpr int LSTG = 6208;
constexpr int VST = 392, LEXT = 1536 + 8 * VST;
constexpr int WT = 3088;
constexpr int LDS_AUX = (54 - NPR) / 3 * WT, LDS_TAB = LDS_AUX + 1024, LDS_CNT = LDS_TAB + 2048, LDS_BUF = LDS_CNT + 128, LDS_STASH = LDS_BUF + 2 * LBUF,
              LDS_STG = LDS_STASH + LSTASH, LDS_SCRAP = LDS_STG + 4 * LSTG, LDS_EXT = LDS_SCRAP + 512, LDS_BYTES = 163840;
static_assert(NWC >= 18 && NPR % 3 == 0 && LDS_EXT + 4 * LEXT <= LDS_BYTES && NPR * 1024 <= LBUF, "LDS");
constexpr unsigned RAWM = 18432, RAW_T = LDS_BUF, RAW_TV = RAW_T + 2 * RAWM + 128, RAW_SV = (36 - NPR) / 3 * WT, RAW_SO = LDS_EXT, RAW_SQ = LDS_BUF + LBUF, RAW_SK = RAW_SQ + RAWM;
static_assert(RAW_SV + RAWM <= LDS_AUX && RAW_SO + RAWM <= LDS_BYTES && RAW_SK + RAWM <= LDS_STASH && RAW_TV + 2 * RAWM <= LDS_STASH && RAWM + 6 * WT <= 2 * RAWM + 128, "fold LDS");

constexpr int phi(int v) { return v ^ (v >> 1); }
struct Tab {
    unsigned img[3][64][4];
    unsigned p[4][7][64][4];
    unsigned c[4][6][64][4];
    unsigned d[4][7][64][4];
    static constexpr void job(unsigned (*q)[64][4], int lane, unsigned L, unsigned R, int mode, int tau, unsigned st0, unsigned st1) {
        const int i = lane & 15, g = lane >> 4;
        for (int a = 0; a < 2; ++a) {
            const unsigned ra = (8 * (i >> 2) + 4 * a + (i & 3)) * 192 + ((g ^ ((2 * (i >> 2) + a) & 3)) * 16);
            q[0][lane][a] = L + ra;
            q[0][lane][2 + a] = mode == 0 ? R + tau * 6144 + ra : mode == 2 ? R + tau * 6144 + (16 * a + i) * 192 + ((g ^ (i >> 2)) * 16)
                                          : R + tau * 64 + (8 * g + 4 * a + (i >> 2)) * 192 + (((i & 3) ^ ((2 * g + a) & 3)) * 16);
        }
        q[1][lane][0] = st0; q[1][lane][1] = st1; q[1][lane][2] = LDS_SCRAP + g * 32;
    }
    static constexpr unsigned scat(bool win, int lane, int tau, int tb) {
        const int i = lane & 15, c = 32 * tau + 8 * (i >> 2) + 4 * tb + (i & 3), r = c / 24, o = c % 24, q = win ? o / 6 : o & 3, t = win ? o % 6 : o >> 2;
        return (win ? RAW_SV : 3 * t < NPR - 18 ? LDS_BUF + 18 * 1024 : -(NPR - 18) / 3 * WT) + t * WT + ((lane & 48) + 4 * q + r) * 16;
    }
    constexpr Tab() : img{}, p{}, c{}, d{} {
        for (int e = 0; e < 48; ++e) img[0][e >> 2][e & 3] = 0x3f803f80u;
        img[0][12][0] = 0x7f800000u;
        for (int e = 0; e < 8; ++e) img[0][36 + 10 + e][e >> 1] = e & 1 ? 0x3f800000u : 0x3f80u;
        for (int k = 0; k < 4; ++k) for (int lane = 0; lane < 64; ++lane) {
            const int i = lane & 15, g = lane >> 4, rowv = 8 * (i >> 2) + (i & 3);
            unsigned vi[20] = {};
            {
                const int pp = i >> 2, t = i & 3;
                const int t1 = (k + 3 - LK1) & 3;
                if (t1 < 3) job(p[k], lane, RAW_TV, RAW_TV + RAWM, 1, t1, scat(false, lane, t1, 0), scat(false, lane, t1, 1));
                const unsigned wl = lane * 16, lm = pp == g ? ~0u : 0u, idp = LDS_AUX + 576 + (t == g ? 10 + pp : 0) * 16, sidx = 0xdddd;
                unsigned vA[3] = {};
                for (int m = 0; m < 3; ++m) {
                    const int n = 64 * m + lane, tf = n / 96, q = n % 96 / 12, s = (n % 12) ^ tf;
                    const unsigned cc = tf * FRAMEB + (3 * (s & 3) + (s >> 2)) * 16 + 2048 - 1024 * m;
                    vA[m] = cc + q * TOKB;
                    img[1][lane][m] = (cc + 6 * ROWB + 6 * TOKB) | ((unsigned)(2 * (q & 3) + (q >> 2)) << 28);
                }
                const unsigned vA0 = vA[0], vA1 = vA[1], vA2 = vA[2], vB0 = vA0 + (ROWB - 7 * TOKB), vB1 = vA1 + (ROWB - 7 * TOKB), vB2 = vA2 + (ROWB - 7 * TOKB);
                const unsigned dA = LDS_BUF + t * LFRAME + pp * LTOK + g * 16, rA = LDS_BUF + t * LFRAME + (pp == g ? pp : 4 + g) * LTOK + 192;
                img[1][lane][3] = (2 * pp * (4 * LTOK) + t * LTOK + g * 16) | (((pp == g ? 2 * pp : 2 * g + 1) * (4 * LTOK) + t * LTOK + 192) << 16);
                const unsigned cd0 = dA + k * 8 * LTOK, cr0 = rA + k * 8 * LTOK;
                const unsigned xr = LDS_STG + k * LSTG + (t >> 1) * 3104 + ((t & 1) * 8 + pp) * TOKB + (g ^ (t & 1)) * 16;
                const unsigned axp = LDS_AUX + (t ? 384 : 0);
                vi[0] = xr; vi[1] = lm; vi[2] = rA; vi[3] = dA; vi[4] = vA0; vi[5] = vA1; vi[6] = vA2; vi[7] = vB0; vi[8] = vB1; vi[9] = vB2; vi[10] = sidx; vi[11] = cd0; vi[12] = cr0; vi[13] = wl; vi[14] = idp; vi[15] = axp;
                for (int e = 0; e < 16; ++e) p[k][2 + (e >> 2)][lane][e & 3] = vi[e];
            }
            {
                const int t0 = (k + 3 - LK0) & 3, t2 = (k + 3 - LK2) & 3, t3 = (k + 3 - LK3) & 3;
                const unsigned s0 = LDS_BUF + t0 * 6144 + lane * 16, s3 = LDS_BUF + LBUF + t3 * 6144 + lane * 16;
                if (t0 < 3) job(c[k], lane, RAW_T, RAW_T + RAWM, 2, t0, s0, s0 + 3072); else job(c[k], lane, RAW_T, RAW_T + RAWM, 1, 0, LDS_SCRAP + rowv * 4, 0);
                if (t2 < 3) job(c[k] + 4, lane, RAW_SV, RAW_SO, 1, t2, scat(true, lane, t2, 0), scat(true, lane, t2, 1));
                if (t3 < 3) job(d[k], lane, RAW_SQ, RAW_SK, 0, t3, s3, s3 + 3072); else job(d[k], lane, RAW_SQ, RAW_SK, 1, 0, LDS_SCRAP + rowv * 4, 0);
                for (int m = 0; m < 3; ++m) {
                    const int n = 64 * m + lane, row = n / 12, pc = ((n % 12) ^ ((row >> 2) & 3)) * 16;
                    c[k][3][lane][m] = (24 * (row & 3) + 12 * ((row >> 2) & 1) + 2 * (row >> 3)) * 576 + pc;
                    c[k][2][lane][m] = row * 192 + pc;
                }
                const int dg = 16 * (2 * (g & 1) + (g >> 1)), pix = 4 * (i & 3) + phi(i >> 2);
                const unsigned wl = lane * 16, fr = LDS_BUF + k * LFRAME, ext = LDS_EXT + k * LEXT;
                const unsigned ta = fr + pix * LTOK + dg, zr = LDS_AUX + 384 + g * 16, t30 = i & 3 ? zr : LDS_STASH + k * LTOK + dg;
                const unsigned xa = fr + (4 * (i >> 2) + phi(g)) * LTOK + 96 * (i & 1) + 8 * ((i >> 1) & 1);
                const int sg = 8 * ((i >> 1) & 1) + 4 * (i >> 3) + 2 * ((i >> 2) & 1) + (i & 1);
                const unsigned tv = fr + (32 + 4 * (sg & 3) + phi(sg >> 2)) * LTOK + dg, c1f = (i >> 2) == 2 * g ? 0x3f800000u : 0u;
                const unsigned sidx = g < 2 ? 0x0e0e0404u | ((i >> 2) == 2 * g ? (i & 3) * 0x00100010u : (i >> 2) == 2 * g + 1 ? (i & 3) * 0x40004000u : 0u) : 0xeeee4444u;
                const unsigned ga0 = phi(g) * TOKB + 48 * (i & 3) + 12 * (i >> 2), wr = ROWB - 7 * TOKB;
                const unsigned ga1 = ga0 + (phi(g) >= 1 ? wr : 0), ga2 = ga0 + (phi(g) >= 2 ? wr : 0), ga3 = ga0 + (phi(g) >= 3 ? wr : 0);
                const unsigned pb = LDS_SCRAP + k * 128, pr0 = i < 4 && (i >> 1) == (g >> 1) ? pb + (i & 1) * 32 + (g & 1) * 16 : zr, pr1 = i == 0 || i == 2 ? pb + 64 + g * 16 : zr;
                const unsigned pw = g < 2 ? pb + ((i >> 1) & 1) * 32 + (i >> 3) * 16 + ((i >> 2) & 1) * 8 + g * 4 + (i & 1) * 2
                                          : pb + 64 + (i >> 2) * 16 + (g & 1) * 4 + ((i >> 1) & 1) * 8 + (i & 1) * 2 + (lane == 57 ? 6 : 0);
                const unsigned six2 = (i & 1 ? 0xeeeeu : 0x4444u) | ((i & 2 ? 0xeeee0000u : 0x44440000u) & (g < 2 ? 0x0f0f0000u : 0xffff0000u));
                const unsigned exs0 = i & 2 ? LDS_AUX + g * 16 : ext + (i & 1) * 64 + g * 16, t3s0 = i & 2 ? zr : LDS_STASH + k * LTOK + 64 * (g >> 1) + 16 * (g & 1) + 4 * (i & 1);
                const unsigned ex0 = i & 1 ? zr : i & 2 ? LDS_AUX + g * 16 : ext + g * 16, exv0 = ext + 1536 + i * 24;
                const unsigned rla0 = lane < 48 ? fr + (16 * g + pix) * LTOK + 192 : lane == 56 ? LDS_STASH + k * LTOK + 192 : LDS_AUX + 192;
                img[2][lane][0] = ((i & 7) * (4 * LTOK) + dg) | ((((4 * g + (i & 3)) & 7) * (4 * LTOK) + 96 * ((i >> 2) & 1) + 8 * (i >> 3)) << 16);
                img[2][lane][1] = ((i & 7) * 192u + g * 16) | ((1536u + i * 24 + (g & 1) * 4 * VST) << 16);
                img[2][lane][2] = ((fr + phi(g) * LTOK + 192 - xa) & 0xffffu) | ((fr + pix * LTOK + 192 - xa) << 16);
                img[2][lane][3] = (LDS_AUX + 576 + (10 + (i & 3)) * 16) | ((1536u + (i & 7) * VST + 384) << 16);
                vi[0] = wl; vi[1] = pw; vi[2] = pr0; vi[3] = pr1; vi[4] = six2; vi[5] = exs0; vi[6] = t3s0; vi[7] = ta; vi[8] = t30; vi[9] = xa; vi[10] = rla0; vi[11] = exv0; vi[12] = ga0; vi[13] = ga1; vi[14] = ga2; vi[15] = ga3; vi[16] = ex0; vi[17] = tv; vi[18] = sidx; vi[19] = c1f;
                for (int e = 0; e < 20; ++e) d[k][2 + (e >> 2)][lane][e & 3] = vi[e];
            }
        }
    }
};
static __device__ const Tab TAB __attribute__((aligned(128))) = Tab();

#define CLOBBERS "v1", "v2", "v3", "v4", "v5", "v6", "v7", "v8", "v9", "v10", "v11", "v12", "v13", "v14", "v15", "v16", "v17", "v18", "v19", "v20", "v21", "v22", "v23", "v24", "v25", "v26", "v27", "v28", "v29", "v30", "v31", "v32", "v33", "v34", "v35", "v36", "v37", "v38", "v39", "v40", "v41", "v42", "v43", "v44", "v45", "v46", "v47", "v48", "v49", "v50", "v51", "v52", "v53", "v54", "v55", "v56", "v57", "v58", "v59", "v60", "v61", "v62", "v63", "v64", "v65", "v66", "v67", "v68", "v69", "v70", "v71", "v72", "v73", "v74", "v75", "v76", "v77", "v78", "v79", "v80", "v81", "v82", "v83", "v84", "v85", "v86", "v87", "v88", "v89", "v90", "v91", "v92", "v93", "v94", "v95", "v96", "v97", "v98", "v99", "v100", "v101", "v102", "v103", "v104", "v105", "v106", "v107", "v108", "v109", "v110", "v111", "v112", "v113", "v114", "v115", "v116", "v117", "v118", "v119", "v120", "v121", "v122", "v123", "v124", "v125", "v126", "v127", "v128", "v129", "v130", "v131", "v132", "v133", "v134", "v135", "v136", "v137", "v138", "v139", "v140", "v141", "v142", "v143", "v144", "v145", "v146", "v147", "v148", "v149", "v150", "v151", "v152", "v153", "v154", "v155", "v156", "v157", "v158", "v159", "v160", "v161", "v162", "v163", "v164", "v165", "v166", "v167", "v168", "v169", "v170", "v171", "v172", "v173", "v174", "v175", "v176", "v177", "v178", "v179", "v180", "v181", "v182", "v183", "v184", "v185", "v186", "v187", "v188", "v189", "v190", "v191", "v192", "v193", "v194", "v195", "v196", "v197", "v198", "v199", "v200", "v201", "v202", "v203", "v204", "v205", "v206", "v207", "v208", "v209", "v210", "v211", "v212", "v213", "v214", "v215", "v216", "v217", "v218", "v219", "v220", "v221", "v222", "v223", "v224", "v225", "v226", "v227", "v228", "v229", "v230", "v231", "v232", "v233", "v234", "v235", "v236", "v237", "v238", "v239", "v240", "v241", "v242", "v243", "v244", "v245", "v246", "v247", "v248", "v249", "v250", "v251", "v252", "v253", "v254", "v255", "s3", "s6", "s7", "s8", "s9", "s10", "s11", "s12", "s13", "s14", "s15", "s16", "s17", "s18", "s19", "s20", "s21", "s22", "s23", "s24", "s25", "s26", "s27", "s28", "s29", "s30", "s31", "s34", "s35", "s36", "s37", "s38", "s39", "s40", "s41", "s42", "s43", "s44", "s45", "s46", "s47", "s48", "s49", "s50", "s51", "s52", "s53", "s54", "s55", "s56", "s57", "s58", "s59", "s60", "s61", "s62", "s63", "s64", "s65", "s66", "s67", "s68", "s69", "s70", "s71", "s72", "s73", "s74", "s75", "s76", "s77", "vcc", "scc", "memory"
__global__ __launch_bounds__(512, 2) void swin_kernel(const char* x, const char* wqkv_t, const char* wo_t, const char* wqkv_s, const char* wo_s, char* out, const char* ws) {
    const unsigned long long kap = (unsigned long long)__builtin_amdgcn_kernarg_segment_ptr(), tab = (unsigned long long)&TAB;
    const unsigned tid = threadIdx.x, bid = blockIdx.x;
    asm volatile(
        "s_load_dwordx8 s[44:51], s[0:1], 0x0\n"
        "s_load_dwordx4 s[52:55], s[0:1], 0x20\n"
        "s_load_dwordx2 s[56:57], s[0:1], 0x30\n"
        "v_readfirstlane_b32 s3, v0\n"
        "v_and_b32 v56, 63, v0\n"
        "v_lshlrev_b32 v57, 4, v56\n"
        "s_bfe_u32 s3, s3, 0x30006\n"
        "s_and_b32 s6, s3, 3\n"
        "s_mul_i32 s64, s3, 9216\n"
        "s_add_u32 s64, s64, 21728\n"
        "s_mov_b32 s27, 0x20000\n"
        "s_cmp_lt_u32 s3, 4\n"
        "s_cbranch_scc0 PPRO_%=\n"
        "s_mul_i32 s7, s6, 6144\n"
        "s_add_u32 s7, s7, 33792\n"
        "s_add_u32 s22, s4, s7\n"
        "s_addc_u32 s23, s5, 0\n"
        "global_load_dwordx4 v[240:243], v57, s[22:23] offset:-2048\n"
        "global_load_dwordx4 v[244:247], v57, s[22:23] offset:-1024\n"
        "global_load_dwordx4 v[36:39], v57, s[22:23] offset:1024\n"
        "global_load_dwordx4 v[48:51], v57, s[22:23] offset:2048\n"
        "global_load_dwordx4 v[52:55], v57, s[22:23] offset:3072\n"
        "s_mov_b32 s65, 13824\n"
        "v_add_u32 v24, 0, v56\n"
        "v_mul_u32_u24 v21, 171, v24\n"
        "v_lshrrev_b32 v21, 11, v21\n"
        "v_mad_i32_i24 v24, v21, -12, v24\n"
        "v_bfe_u32 v58, v21, 2, 2\n"
        "v_xor_b32 v58, v24, v58\n"
        "v_and_b32 v27, 7, v21\n"
        "v_mul_u32_u24 v27, 576, v27\n"
        "v_lshl_add_u32 v58, v58, 4, v27\n"
        "v_add_u32 v25, 64, v56\n"
        "v_mul_u32_u24 v22, 171, v25\n"
        "v_lshrrev_b32 v22, 11, v22\n"
        "v_mad_i32_i24 v25, v22, -12, v25\n"
        "v_bfe_u32 v59, v22, 2, 2\n"
        "v_xor_b32 v59, v25, v59\n"
        "v_and_b32 v27, 7, v22\n"
        "v_mul_u32_u24 v27, 576, v27\n"
        "v_lshl_add_u32 v59, v59, 4, v27\n"
        "v_lshrrev_b32 v27, 3, v22\n"
        "v_mad_u32_u24 v59, v27, s65, v59\n"
        "v_add_u32 v26, 128, v56\n"
        "v_mul_u32_u24 v23, 171, v26\n"
        "v_lshrrev_b32 v23, 11, v23\n"
        "v_mad_i32_i24 v26, v23, -12, v26\n"
        "v_bfe_u32 v60, v23, 2, 2\n"
        "v_xor_b32 v60, v26, v60\n"
        "v_and_b32 v27, 7, v23\n"
        "v_mul_u32_u24 v27, 576, v27\n"
        "v_lshl_add_u32 v60, v60, 4, v27\n"
        "v_add_u32 v60, s65, v60\n"
        "s_lshr_b32 s61, s6, 1\n"
        "s_mul_i32 s61, s61, 192\n"
        "s_mov_b32 s62, 27648\n"
        "s_mov_b32 s63, 4608\n"
        "s_add_u32 s7, s62, s63\n"
        "s_lshl_b32 s29, s63, 1\n"
        "s_add_u32 s60, s7, s63\n"
        "s_bitcmp1_b32 s6, 0\n"
        "s_cselect_b32 s28, s7, 0\n"
        "s_cselect_b32 s29, s29, s62\n"
        "s_cselect_b32 s60, s60, s63\n"
        "s_add_u32 s28, s28, s61\n"
        "s_add_u32 s29, s29, s61\n"
        "s_add_u32 s60, s60, s61\n"
        "s_mov_b32 s26, 55296\n"
        "s_waitcnt lgkmcnt(0)\n"
        "s_mov_b32 s24, s46\n"
        "s_and_b32 s25, s47, 0xffff\n"
        "s_add_u32 m0, s64, 0\n"
        "s_nop 0\n"
        "buffer_load_dwordx4 v58, s[24:27], s28 offen lds\n"
        "s_add_u32 m0, s64, 1024\n"
        "s_nop 0\n"
        "buffer_load_dwordx4 v59, s[24:27], s28 offen lds\n"
        "s_add_u32 m0, s64, 2048\n"
        "s_nop 0\n"
        "buffer_load_dwordx4 v60, s[24:27], s28 offen lds\n"
        "s_add_u32 m0, s64, 3072\n"
        "s_nop 0\n"
        "buffer_load_dwordx4 v58, s[24:27], s29 offen lds\n"
        "s_add_u32 m0, s64, 4096\n"
        "s_nop 0\n"
        "buffer_load_dwordx4 v59, s[24:27], s29 offen lds\n"
        "s_add_u32 m0, s64, 5120\n"
        "s_nop 0\n"
        "buffer_load_dwordx4 v60, s[24:27], s29 offen lds\n"
        "s_add_u32 m0, s64, 6144\n"
        "s_nop 0\n"
        "buffer_load_dwordx4 v58, s[24:27], s60 offen lds\n"
        "s_add_u32 m0, s64, 7168\n"
        "s_nop 0\n"
        "buffer_load_dwordx4 v59, s[24:27], s60 offen lds\n"
        "s_add_u32 m0, s64, 8192\n"
        "s_nop 0\n"
        "buffer_load_dwordx4 v60, s[24:27], s60 offen lds\n"
        "s_barrier\n"
        "s_setprio 1\n"
        "v_mov_b32 v250, 21600\n"
        "s_mov_b32 s41, 0x400000\n"
        "s_and_b32 s7, s2, 7\n"
        "s_lshl_b32 s7, s7, 5\n"
        "s_lshr_b32 s14, s2, 3\n"
        "s_add_u32 s7, s7, s14\n"
        "s_mul_i32 s7, s7, 20\n"
        "s_mov_b32 s14, 22\n"
        "s_mul_i32 s15, s7, 57457\n"
        "s_lshr_b32 s15, s15, 22\n"
        "s_mul_i32 s16, s15, 73\n"
        "s_sub_u32 s16, s7, s16\n"
        "s_mul_i32 s15, s15, 686784\n"
        "s_mul_i32 s7, s16, 1344\n"
        "s_add_u32 s15, s15, s7\n"
        "s_mov_b32 s8, s54\n"
        "s_and_b32 s9, s55, 0xffff\n"
        "s_mov_b32 s10, 200540928\n"
        "s_mov_b32 s11, 0x20000\n"
        "s_mov_b64 s[12:13], s[54:55]\n"
        "s_mul_i32 s17, s6, 50135232\n"
        "s_mul_i32 s18, s6, 208\n"
        "s_add_u32 s18, s18, 109536\n"
        "s_mul_i32 s19, s6, 4672\n"
        "s_add_u32 s19, s19, 135520\n"
        "s_mul_i32 s7, s6, 7168\n"
        "s_add_u32 s7, s7, 59392\n"
        "s_add_u32 s22, s4, s7\n"
        "s_addc_u32 s23, s5, 0\n"
        "s_waitcnt vmcnt(0)\n"
        "s_mov_b64 exec, 1\n"
        "ds_inc_u32 v250, v250 offset:16\n"
        "s_mov_b64 exec, -1\n"
        "L65_%=:\n"
        "ds_read_b32 v251, v250 offset:16\n"
        "s_waitcnt lgkmcnt(0)\n"
        "v_readfirstlane_b32 s40, v251\n"
        "s_nop 3\n"
        "s_cmp_ge_i32 s40, 4\n"
        "s_cbranch_scc1 L66_%=\n"
        "s_nop 0\n"
        "s_sub_u32 s41, s41, 1\n"
        "s_cbranch_scc0 L65_%=\n"
        "s_endpgm\n"
        "L66_%=:\n"
        "s_cmp_eq_u32 s6, 0\n"
        "s_cbranch_scc1 L67_%=\n"
        "s_mov_b32 s34, 0x4162ccfd\n"
        "s_mov_b32 s35, 21624\n"
        "s_mov_b32 s36, 21624\n"
        "s_mov_b32 s37, 21628\n"
        "s_call_b64 s[30:31], FOLDM_%=\n"
        "s_branch L68_%=\n"
        "L67_%=:\n"
        "s_mov_b32 s34, 0xbe173353\n"
        "s_mov_b32 s35, 21656\n"
        "s_mov_b32 s36, 21656\n"
        "s_mov_b32 s37, 21624\n"
        "s_call_b64 s[30:31], FOLDR_%=\n"
        "s_cmp_lg_u32 s2, 0\n"
        "s_cbranch_scc1 L64_%=\n"
        "s_mov_b64 s[24:25], s[56:57]\n"
        "s_mov_b64 exec, 1\n"
        "v_mov_b32 v27, 0\n"
        "v_mov_b32 v28, 0\n"
        "global_atomic_swap v252, v27, v28, s[24:25] offset:0 sc0\n"
        "s_mov_b64 exec, -1\n"
        "s_waitcnt vmcnt(0)\n"
        "s_mov_b64 exec, 1\n"
        "v_mov_b32 v27, 0\n"
        "v_mov_b32 v28, 1\n"
        "global_atomic_add v252, v27, v28, s[24:25] offset:256 sc0\n"
        "s_mov_b64 exec, -1\n"
        "L64_%=:\n"
        "L68_%=:\n"
        "s_setprio 3\n"
        "s_waitcnt vmcnt(0)\n"
        "v_mov_b64 v[240:241], v[48:49]\n"
        "v_mov_b64 v[242:243], v[50:51]\n"
        "v_mov_b64 v[244:245], v[52:53]\n"
        "v_mov_b64 v[246:247], v[54:55]\n"
        "L69_%=:\n"
        "ds_read_b32 v251, v250 offset:32\n"
        "s_waitcnt lgkmcnt(0)\n"
        "v_readfirstlane_b32 s40, v251\n"
        "s_nop 3\n"
        "s_cmp_ge_i32 s40, 4\n"
        "s_cbranch_scc1 L70_%=\n"
        "s_nop 0\n"
        "s_sub_u32 s41, s41, 1\n"
        "s_cbranch_scc0 L69_%=\n"
        "s_endpgm\n"
        "L70_%=:\n"
        "s_cmp_eq_u32 s6, 1\n"
        "s_cbranch_scc1 L71_%=\n"
        "s_mov_b32 s34, 0x411cc471\n"
        "s_mov_b32 s35, 21640\n"
        "s_mov_b32 s36, 21640\n"
        "s_mov_b32 s37, 21652\n"
        "s_call_b64 s[30:31], FOLDW_%=\n"
        "s_branch L72_%=\n"
        "L71_%=:\n"
        "s_mov_b64 exec, 1\n"
        "ds_inc_u32 v250, v250 offset:40\n"
        "s_mov_b64 exec, -1\n"
        "s_mov_b32 s24, s50\n"
        "s_and_b32 s25, s51, 0xffff\n"
        "s_mov_b32 s26, 55296\n"
        "s_mov_b32 m0, 61792\n"
        "s_mov_b32 s28, 0\n"
        "buffer_load_dwordx4 v36, s[24:27], s28 offen lds\n"
        "s_mov_b32 m0, 62816\n"
        "s_nop 0\n"
        "buffer_load_dwordx4 v37, s[24:27], s28 offen lds\n"
        "s_mov_b32 m0, 63840\n"
        "s_nop 0\n"
        "buffer_load_dwordx4 v38, s[24:27], s28 offen lds\n"
        "s_mov_b32 m0, 64864\n"
        "s_mov_b32 s28, 576\n"
        "buffer_load_dwordx4 v36, s[24:27], s28 offen lds\n"
        "s_mov_b32 m0, 65888\n"
        "s_nop 0\n"
        "buffer_load_dwordx4 v37, s[24:27], s28 offen lds\n"
        "s_mov_b32 m0, 66912\n"
        "s_nop 0\n"
        "buffer_load_dwordx4 v38, s[24:27], s28 offen lds\n"
        "s_mov_b32 m0, 67936\n"
        "s_mov_b32 s28, 2304\n"
        "buffer_load_dwordx4 v36, s[24:27], s28 offen lds\n"
        "s_mov_b32 m0, 68960\n"
        "s_nop 0\n"
        "buffer_load_dwordx4 v37, s[24:27], s28 offen lds\n"
        "s_mov_b32 m0, 69984\n"
        "s_nop 0\n"
        "buffer_load_dwordx4 v38, s[24:27], s28 offen lds\n"
        "s_mov_b32 m0, 71008\n"
        "s_mov_b32 s28, 2880\n"
        "buffer_load_dwordx4 v36, s[24:27], s28 offen lds\n"
        "s_mov_b32 m0, 72032\n"
        "s_nop 0\n"
        "buffer_load_dwordx4 v37, s[24:27], s28 offen lds\n"
        "s_mov_b32 m0, 73056\n"
        "s_nop 0\n"
        "buffer_load_dwordx4 v38, s[24:27], s28 offen lds\n"
        "s_mov_b32 m0, 74080\n"
        "s_mov_b32 s28, 4608\n"
        "buffer_load_dwordx4 v36, s[24:27], s28 offen lds\n"
        "s_mov_b32 m0, 75104\n"
        "s_nop 0\n"
        "buffer_load_dwordx4 v37, s[24:27], s28 offen lds\n"
        "s_mov_b32 m0, 76128\n"
        "s_nop 0\n"
        "buffer_load_dwordx4 v38, s[24:27], s28 offen lds\n"
        "s_mov_b32 m0, 77152\n"
        "s_mov_b32 s28, 5184\n"
        "buffer_load_dwordx4 v36, s[24:27], s28 offen lds\n"
        "s_mov_b32 m0, 78176\n"
        "s_nop 0\n"
        "buffer_load_dwordx4 v37, s[24:27], s28 offen lds\n"
        "s_mov_b32 m0, 79200\n"
        "s_nop 0\n"
        "buffer_load_dwordx4 v38, s[24:27], s28 offen lds\n"
        "s_mov_b32 s24, s50\n"
        "s_and_b32 s25, s51, 0xffff\n"
        "s_mov_b32 s26, 55296\n"
        "s_mov_b32 m0, 80224\n"
        "s_mov_b32 s28, 192\n"
        "buffer_load_dwordx4 v36, s[24:27], s28 offen lds\n"
        "s_mov_b32 m0, 81248\n"
        "s_nop 0\n"
        "buffer_load_dwordx4 v37, s[24:27], s28 offen lds\n"
        "s_mov_b32 m0, 82272\n"
        "s_nop 0\n"
        "buffer_load_dwordx4 v38, s[24:27], s28 offen lds\n"
        "s_mov_b32 m0, 83296\n"
        "s_mov_b32 s28, 768\n"
        "buffer_load_dwordx4 v36, s[24:27], s28 offen lds\n"
        "s_mov_b32 m0, 84320\n"
        "s_nop 0\n"
        "buffer_load_dwordx4 v37, s[24:27], s28 offen lds\n"
        "s_mov_b32 m0, 85344\n"
        "s_nop 0\n"
        "buffer_load_dwordx4 v38, s[24:27], s28 offen lds\n"
        "s_mov_b32 m0, 86368\n"
        "s_mov_b32 s28, 2496\n"
        "buffer_load_dwordx4 v36, s[24:27], s28 offen lds\n"
        "s_mov_b32 m0, 87392\n"
        "s_nop 0\n"
        "buffer_load_dwordx4 v37, s[24:27], s28 offen lds\n"
        "s_mov_b32 m0, 88416\n"
        "s_nop 0\n"
        "buffer_load_dwordx4 v38, s[24:27], s28 offen lds\n"
        "s_mov_b32 m0, 89440\n"
        "s_mov_b32 s28, 3072\n"
        "buffer_load_dwordx4 v36, s[24:27], s28 offen lds\n"
        "s_mov_b32 m0, 90464\n"
        "s_nop 0\n"
        "buffer_load_dwordx4 v37, s[24:27], s28 offen lds\n"
        "s_mov_b32 m0, 91488\n"
        "s_nop 0\n"
        "buffer_load_dwordx4 v38, s[24:27], s28 offen lds\n"
        "s_mov_b32 m0, 92512\n"
        "s_mov_b32 s28, 4800\n"
        "buffer_load_dwordx4 v36, s[24:27], s28 offen lds\n"
        "s_mov_b32 m0, 93536\n"
        "s_nop 0\n"
        "buffer_load_dwordx4 v37, s[24:27], s28 offen lds\n"
        "s_mov_b32 m0, 94560\n"
        "s_nop 0\n"
        "buffer_load_dwordx4 v38, s[24:27], s28 offen lds\n"
        "s_mov_b32 m0, 95584\n"
        "s_mov_b32 s28, 5376\n"
        "buffer_load_dwordx4 v36, s[24:27], s28 offen lds\n"
        "s_mov_b32 m0, 96608\n"
        "s_nop 0\n"
        "buffer_load_dwordx4 v37, s[24:27], s28 offen lds\n"
        "s_mov_b32 m0, 97632\n"
        "s_nop 0\n"
        "buffer_load_dwordx4 v38, s[24:27], s28 offen lds\n"
        "global_load_dwordx4 v[40:43], v57, s[22:23] offset:-3072\n"
        "global_load_dwordx4 v[44:47], v57, s[22:23] offset:-2048\n"
        "global_load_dwordx4 v[0:3], v57, s[22:23] offset:-1024\n"
        "global_load_dwordx4 v[4:7], v57, s[22:23] offset:0\n"
        "global_load_dwordx4 v[8:11], v57, s[22:23] offset:1024\n"
        "global_load_dwordx4 v[12:15], v57, s[22:23] offset:2048\n"
        "global_load_dwordx4 v[16:19], v57, s[22:23] offset:3072\n"
        "L72_%=:\n"
        "s_waitcnt vmcnt(0)\n"
        "s_mov_b64 exec, 1\n"
        "ds_inc_u32 v250, v250 offset:44\n"
        "s_mov_b64 exec, -1\n"
        "v_mov_b64 v[240:241], v[40:41]\n"
        "v_mov_b64 v[242:243], v[42:43]\n"
        "v_mov_b64 v[244:245], v[44:45]\n"
        "v_mov_b64 v[246:247], v[46:47]\n"
        "L73_%=:\n"
        "ds_read_b32 v251, v250 offset:44\n"
        "s_waitcnt lgkmcnt(0)\n"
        "v_readfirstlane_b32 s40, v251\n"
        "s_nop 3\n"
        "s_cmp_ge_i32 s40, 4\n"
        "s_cbranch_scc1 L74_%=\n"
        "s_nop 0\n"
        "s_sub_u32 s41, s41, 1\n"
        "s_cbranch_scc0 L73_%=\n"
        "s_endpgm\n"
        "L74_%=:\n"
        "s_cmp_eq_u32 s6, 2\n"
        "s_cbranch_scc1 L75_%=\n"
        "s_mov_b32 s34, 0x41622adf\n"
        "s_mov_b32 s35, 21648\n"
        "s_mov_b32 s36, 21648\n"
        "s_mov_b32 s37, 21652\n"
        "s_call_b64 s[30:31], FOLDM_%=\n"
        "s_branch L76_%=\n"
        "L75_%=:\n"
        "s_mov_b32 s34, 0xbe16c740\n"
        "s_mov_b32 s35, 21656\n"
        "s_mov_b32 s36, 21656\n"
        "s_mov_b32 s37, 21648\n"
        "s_call_b64 s[30:31], FOLDR_%=\n"
        "L76_%=:\n"
        "s_branch CONS_%=\n"
        "FOLDM_%=:\n"
        "ds_read_b128 v[88:91], v242\n"
        "ds_read_b128 v[92:95], v240\n"
        "ds_read_b128 v[180:183], v242 offset:64\n"
        "ds_read_b128 v[172:175], v240 offset:64\n"
        "ds_read_b128 v[184:187], v242 offset:128\n"
        "ds_read_b128 v[164:167], v240 offset:128\n"
        "ds_read_b128 v[168:171], v241\n"
        "ds_read_b128 v[100:103], v241 offset:64\n"
        "ds_read_b128 v[104:107], v241 offset:128\n"
        "ds_read_b128 v[140:143], v240 offset:6144\n"
        "ds_read_b128 v[132:135], v240 offset:6208\n"
        "ds_read_b128 v[96:99], v240 offset:6272\n"
        "ds_read_b128 v[136:139], v241 offset:6144\n"
        "ds_read_b128 v[128:131], v241 offset:6208\n"
        "ds_read_b128 v[108:111], v241 offset:6272\n"
        "s_waitcnt lgkmcnt(13)\n"
        "v_mfma_f32_16x16x32_bf16 v[64:67], v[92:95], v[88:91], 0\n"
        "s_waitcnt lgkmcnt(11)\n"
        "ds_read_b128 v[152:155], v240 offset:12288\n"
        "v_mfma_f32_16x16x32_bf16 v[64:67], v[172:175], v[180:183], v[64:67]\n"
        "s_waitcnt lgkmcnt(8)\n"
        "ds_read_b128 v[160:163], v240 offset:12352\n"
        "v_mfma_f32_16x16x32_bf16 v[64:67], v[164:167], v[184:187], v[64:67]\n"
        "ds_read_b128 v[112:115], v240 offset:12416\n"
        "v_mfma_f32_16x16x32_bf16 v[68:71], v[168:171], v[88:91], 0\n"
        "ds_read_b128 v[116:119], v241 offset:12288\n"
        "v_mfma_f32_16x16x32_bf16 v[68:71], v[100:103], v[180:183], v[68:71]\n"
        "s_waitcnt lgkmcnt(4)\n"
        "ds_read_b128 v[120:123], v241 offset:12352\n"
        "v_mfma_f32_16x16x32_bf16 v[68:71], v[104:107], v[184:187], v[68:71]\n"
        "ds_read_b128 v[124:127], v241 offset:12416\n"
        "v_mfma_f32_16x16x32_bf16 v[72:75], v[140:143], v[88:91], 0\n"
        "ds_read_b128 v[144:147], v243\n"
        "v_mfma_f32_16x16x32_bf16 v[72:75], v[132:135], v[180:183], v[72:75]\n"
        "ds_read_b128 v[148:151], v243 offset:64\n"
        "v_mfma_f32_16x16x32_bf16 v[72:75], v[96:99], v[184:187], v[72:75]\n"
        "ds_read_b128 v[156:159], v243 offset:128\n"
        "v_mfma_f32_16x16x32_bf16 v[76:79], v[136:139], v[88:91], 0\n"
        "v_add_f32 v176, v64, v68\n"
        "v_add_f32 v177, v65, v69\n"
        "v_add_f32 v178, v66, v70\n"
        "v_add_f32 v179, v67, v71\n"
        "v_mfma_f32_16x16x32_bf16 v[76:79], v[128:131], v[180:183], v[76:79]\n"
        "v_mfma_f32_16x16x32_bf16 v[76:79], v[108:111], v[184:187], v[76:79]\n"
        "s_waitcnt lgkmcnt(3)\n"
        "v_add_f32 v176, v176, v72\n"
        "v_add_f32 v177, v177, v73\n"
        "v_add_f32 v178, v178, v74\n"
        "v_add_f32 v179, v179, v75\n"
        "v_mfma_f32_16x16x32_bf16 v[80:83], v[152:155], v[88:91], 0\n"
        "v_mfma_f32_16x16x32_bf16 v[80:83], v[160:163], v[180:183], v[80:83]\n"
        "v_mfma_f32_16x16x32_bf16 v[80:83], v[112:115], v[184:187], v[80:83]\n"
        "v_add_f32 v176, v176, v76\n"
        "v_add_f32 v177, v177, v77\n"
        "v_add_f32 v178, v178, v78\n"
        "v_add_f32 v179, v179, v79\n"
        "v_mfma_f32_16x16x32_bf16 v[84:87], v[116:119], v[88:91], 0\n"
        "v_mfma_f32_16x16x32_bf16 v[84:87], v[120:123], v[180:183], v[84:87]\n"
        "v_mfma_f32_16x16x32_bf16 v[84:87], v[124:127], v[184:187], v[84:87]\n"
        "s_waitcnt lgkmcnt(0)\n"
        "v_add_f32 v176, v176, v80\n"
        "v_add_f32 v177, v177, v81\n"
        "v_add_f32 v178, v178, v82\n"
        "v_add_f32 v179, v179, v83\n"
        "v_mov_b32 v239, s35\n"
        "s_waitcnt vmcnt(6)\n"
        "s_mov_b64 exec, 1\n"
        "ds_inc_u32 v239, v239 offset:0\n"
        "s_mov_b64 exec, -1\n"
        "v_add_f32 v176, v176, v84\n"
        "v_add_f32 v177, v177, v85\n"
        "v_add_f32 v178, v178, v86\n"
        "v_add_f32 v179, v179, v87\n"
        "v_add_f32 v176, v176, v177\n"
        "v_add_f32 v178, v178, v179\n"
        "v_add_f32 v176, v176, v178\n"
        "v_mov_b32 v177, v176\n"
        "v_mfma_f32_16x16x32_bf16 v[88:91], v[92:95], v[144:147], 0\n"
        "v_mfma_f32_16x16x32_bf16 v[88:91], v[172:175], v[148:151], v[88:91]\n"
        "v_permlane32_swap_b32 v176, v177\n"
        "v_add_f32 v176, v176, v177\n"
        "v_mov_b32 v177, v176\n"
        "v_mfma_f32_16x16x32_bf16 v[88:91], v[164:167], v[156:159], v[88:91]\n"
        "v_mfma_f32_16x16x32_bf16 v[92:95], v[168:171], v[144:147], 0\n"
        "v_permlane16_swap_b32 v176, v177\n"
        "v_add_f32 v176, v176, v177\n"
        "v_mul_f32 v176, 0xbc2aaaab, v176\n"
        "v_mul_f32 v176, s34, v176\n"
        "v_fma_f32 v64, v64, s34, v176\n"
        "v_fma_f32 v65, v65, s34, v176\n"
        "v_fma_f32 v66, v66, s34, v176\n"
        "v_fma_f32 v67, v67, s34, v176\n"
        "v_fma_f32 v68, v68, s34, v176\n"
        "v_fma_f32 v69, v69, s34, v176\n"
        "v_fma_f32 v70, v70, s34, v176\n"
        "v_fma_f32 v71, v71, s34, v176\n"
        "v_fma_f32 v72, v72, s34, v176\n"
        "v_fma_f32 v73, v73, s34, v176\n"
        "v_fma_f32 v74, v74, s34, v176\n"
        "v_fma_f32 v75, v75, s34, v176\n"
        "v_fma_f32 v76, v76, s34, v176\n"
        "v_fma_f32 v77, v77, s34, v176\n"
        "v_fma_f32 v78, v78, s34, v176\n"
        "v_fma_f32 v79, v79, s34, v176\n"
        "v_fma_f32 v80, v80, s34, v176\n"
        "v_fma_f32 v81, v81, s34, v176\n"
        "v_fma_f32 v82, v82, s34, v176\n"
        "v_fma_f32 v83, v83, s34, v176\n"
        "v_fma_f32 v84, v84, s34, v176\n"
        "v_fma_f32 v85, v85, s34, v176\n"
        "v_fma_f32 v86, v86, s34, v176\n"
        "v_fma_f32 v87, v87, s34, v176\n"
        "v_mfma_f32_16x16x32_bf16 v[92:95], v[100:103], v[148:151], v[92:95]\n"
        "v_mfma_f32_16x16x32_bf16 v[92:95], v[104:107], v[156:159], v[92:95]\n"
        "v_mfma_f32_16x16x32_bf16 v[100:103], v[140:143], v[144:147], 0\n"
        "v_mfma_f32_16x16x32_bf16 v[100:103], v[132:135], v[148:151], v[100:103]\n"
        "v_mfma_f32_16x16x32_bf16 v[100:103], v[96:99], v[156:159], v[100:103]\n"
        "v_mfma_f32_16x16x32_bf16 v[104:107], v[136:139], v[144:147], 0\n"
        "v_mfma_f32_16x16x32_bf16 v[104:107], v[128:131], v[148:151], v[104:107]\n"
        "v_mfma_f32_16x16x32_bf16 v[104:107], v[108:111], v[156:159], v[104:107]\n"
        "v_mfma_f32_16x16x32_bf16 v[96:99], v[152:155], v[144:147], 0\n"
        "v_mfma_f32_16x16x32_bf16 v[96:99], v[160:163], v[148:151], v[96:99]\n"
        "v_add_f32 v128, v88, v92\n"
        "v_add_f32 v129, v89, v93\n"
        "v_add_f32 v130, v90, v94\n"
        "v_add_f32 v131, v91, v95\n"
        "v_add_f32 v128, v128, v100\n"
        "v_add_f32 v129, v129, v101\n"
        "v_add_f32 v130, v130, v102\n"
        "v_add_f32 v131, v131, v103\n"
        "v_add_f32 v128, v128, v104\n"
        "v_add_f32 v129, v129, v105\n"
        "v_add_f32 v130, v130, v106\n"
        "v_add_f32 v131, v131, v107\n"
        "v_mfma_f32_16x16x32_bf16 v[96:99], v[112:115], v[156:159], v[96:99]\n"
        "v_mfma_f32_16x16x32_bf16 v[108:111], v[116:119], v[144:147], 0\n"
        "v_mfma_f32_16x16x32_bf16 v[108:111], v[120:123], v[148:151], v[108:111]\n"
        "v_mfma_f32_16x16x32_bf16 v[108:111], v[124:127], v[156:159], v[108:111]\n"
        "v_mov_b32 v239, s37\n"
        "s_nop 3\n"
        "v_add_f32 v128, v128, v96\n"
        "v_add_f32 v129, v129, v97\n"
        "v_add_f32 v130, v130, v98\n"
        "v_add_f32 v131, v131, v99\n"
        "v_add_f32 v128, v128, v108\n"
        "v_add_f32 v129, v129, v109\n"
        "v_add_f32 v130, v130, v110\n"
        "v_add_f32 v131, v131, v111\n"
        "v_add_f32 v128, v128, v129\n"
        "v_add_f32 v130, v130, v131\n"
        "v_add_f32 v128, v128, v130\n"
        "v_mov_b32 v129, v128\n"
        "s_nop 1\n"
        "v_permlane32_swap_b32 v128, v129\n"
        "v_add_f32 v128, v128, v129\n"
        "v_mov_b32 v129, v128\n"
        "s_nop 1\n"
        "v_permlane16_swap_b32 v128, v129\n"
        "v_add_f32 v128, v128, v129\n"
        "v_mul_f32 v128, 0xbc2aaaab, v128\n"
        "v_mul_f32 v128, s34, v128\n"
        "v_fma_f32 v88, v88, s34, v128\n"
        "v_fma_f32 v89, v89, s34, v128\n"
        "v_fma_f32 v90, v90, s34, v128\n"
        "v_fma_f32 v91, v91, s34, v128\n"
        "v_fma_f32 v92, v92, s34, v128\n"
        "v_fma_f32 v93, v93, s34, v128\n"
        "v_fma_f32 v94, v94, s34, v128\n"
        "v_fma_f32 v95, v95, s34, v128\n"
        "v_fma_f32 v100, v100, s34, v128\n"
        "v_fma_f32 v101, v101, s34, v128\n"
        "v_fma_f32 v102, v102, s34, v128\n"
        "v_fma_f32 v103, v103, s34, v128\n"
        "v_fma_f32 v104, v104, s34, v128\n"
        "v_fma_f32 v105, v105, s34, v128\n"
        "v_fma_f32 v106, v106, s34, v128\n"
        "v_fma_f32 v107, v107, s34, v128\n"
        "v_fma_f32 v96, v96, s34, v128\n"
        "v_fma_f32 v97, v97, s34, v128\n"
        "v_fma_f32 v98, v98, s34, v128\n"
        "v_fma_f32 v99, v99, s34, v128\n"
        "v_fma_f32 v108, v108, s34, v128\n"
        "v_fma_f32 v109, v109, s34, v128\n"
        "v_fma_f32 v110, v110, s34, v128\n"
        "v_fma_f32 v111, v111, s34, v128\n"
        "v_mov_b32 v237, s36\n"
        "L60_%=:\n"
        "ds_read_b32 v238, v237 offset:0\n"
        "s_waitcnt lgkmcnt(0)\n"
        "v_readfirstlane_b32 s40, v238\n"
        "s_nop 3\n"
        "s_cmp_ge_i32 s40, 4\n"
        "s_cbranch_scc1 L61_%=\n"
        "s_nop 0\n"
        "s_sub_u32 s41, s41, 1\n"
        "s_cbranch_scc0 L60_%=\n"
        "s_endpgm\n"
        "L61_%=:\n"
        "ds_read_b128 v[132:135], v246\n"
        "ds_read_b128 v[128:131], v246 offset:16\n"
        "ds_read_b128 v[124:127], v246 offset:128\n"
        "ds_read_b128 v[120:123], v246 offset:144\n"
        "ds_read_b128 v[116:119], v246 offset:256\n"
        "ds_read_b128 v[112:115], v246 offset:272\n"
        "s_waitcnt lgkmcnt(5)\n"
        "v_add_f32 v64, v64, v132\n"
        "v_add_f32 v65, v65, v133\n"
        "v_add_f32 v66, v66, v134\n"
        "v_add_f32 v67, v67, v135\n"
        "s_waitcnt lgkmcnt(0)\n"
        "v_add_f32 v68, v68, v128\n"
        "v_add_f32 v69, v69, v129\n"
        "v_add_f32 v70, v70, v130\n"
        "v_add_f32 v71, v71, v131\n"
        "v_add_f32 v88, v88, v132\n"
        "v_add_f32 v89, v89, v133\n"
        "v_add_f32 v90, v90, v134\n"
        "v_add_f32 v91, v91, v135\n"
        "v_add_f32 v92, v92, v128\n"
        "v_add_f32 v93, v93, v129\n"
        "v_add_f32 v94, v94, v130\n"
        "v_add_f32 v95, v95, v131\n"
        "v_add_f32 v72, v72, v124\n"
        "v_add_f32 v73, v73, v125\n"
        "v_add_f32 v74, v74, v126\n"
        "v_add_f32 v75, v75, v127\n"
        "v_add_f32 v76, v76, v120\n"
        "v_add_f32 v77, v77, v121\n"
        "v_add_f32 v78, v78, v122\n"
        "v_add_f32 v79, v79, v123\n"
        "v_add_f32 v100, v100, v124\n"
        "v_add_f32 v101, v101, v125\n"
        "v_add_f32 v102, v102, v126\n"
        "v_add_f32 v103, v103, v127\n"
        "v_add_f32 v104, v104, v120\n"
        "v_add_f32 v105, v105, v121\n"
        "v_add_f32 v106, v106, v122\n"
        "v_add_f32 v107, v107, v123\n"
        "v_add_f32 v80, v80, v116\n"
        "v_add_f32 v81, v81, v117\n"
        "v_add_f32 v82, v82, v118\n"
        "v_add_f32 v83, v83, v119\n"
        "v_add_f32 v84, v84, v112\n"
        "v_add_f32 v85, v85, v113\n"
        "v_add_f32 v86, v86, v114\n"
        "v_add_f32 v87, v87, v115\n"
        "v_add_f32 v96, v96, v116\n"
        "v_add_f32 v97, v97, v117\n"
        "v_add_f32 v98, v98, v118\n"
        "v_add_f32 v99, v99, v119\n"
        "v_add_f32 v108, v108, v112\n"
        "v_add_f32 v109, v109, v113\n"
        "v_add_f32 v110, v110, v114\n"
        "v_add_f32 v111, v111, v115\n"
        "v_cvt_pk_bf16_f32 v112, v64, v65\n"
        "v_cvt_pk_bf16_f32 v113, v66, v67\n"
        "v_cvt_pk_bf16_f32 v114, v68, v69\n"
        "v_cvt_pk_bf16_f32 v115, v70, v71\n"
        "v_cvt_pk_bf16_f32 v64, v88, v89\n"
        "v_cvt_pk_bf16_f32 v65, v90, v91\n"
        "v_cvt_pk_bf16_f32 v66, v92, v93\n"
        "v_cvt_pk_bf16_f32 v67, v94, v95\n"
        "v_cvt_pk_bf16_f32 v68, v72, v73\n"
        "v_cvt_pk_bf16_f32 v69, v74, v75\n"
        "v_cvt_pk_bf16_f32 v70, v76, v77\n"
        "v_cvt_pk_bf16_f32 v71, v78, v79\n"
        "v_cvt_pk_bf16_f32 v72, v100, v101\n"
        "v_cvt_pk_bf16_f32 v73, v102, v103\n"
        "v_cvt_pk_bf16_f32 v74, v104, v105\n"
        "v_cvt_pk_bf16_f32 v75, v106, v107\n"
        "v_cvt_pk_bf16_f32 v76, v80, v81\n"
        "v_cvt_pk_bf16_f32 v77, v82, v83\n"
        "v_cvt_pk_bf16_f32 v78, v84, v85\n"
        "v_cvt_pk_bf16_f32 v79, v86, v87\n"
        "v_cvt_pk_bf16_f32 v80, v96, v97\n"
        "v_cvt_pk_bf16_f32 v81, v98, v99\n"
        "v_cvt_pk_bf16_f32 v82, v108, v109\n"
        "v_cvt_pk_bf16_f32 v83, v110, v111\n"
        "ds_write_b128 v244, v[112:115]\n"
        "ds_write_b128 v245, v[64:67]\n"
        "ds_write_b128 v244, v[68:71] offset:1024\n"
        "ds_write_b128 v245, v[72:75] offset:1024\n"
        "ds_write_b128 v244, v[76:79] offset:2048\n"
        "ds_write_b128 v245, v[80:83] offset:2048\n"
        "s_mov_b64 exec, 1\n"
        "ds_inc_u32 v239, v239 offset:0\n"
        "s_mov_b64 exec, -1\n"
        "s_setpc_b64 s[30:31]\n"
        "FOLDR_%=:\n"
        "ds_read_b64_tr_b16 v[84:85], v242 offset:0\n"
        "ds_read_b64_tr_b16 v[86:87], v243 offset:0\n"
        "ds_read_b64_tr_b16 v[92:93], v242 offset:6144\n"
        "ds_read_b64_tr_b16 v[94:95], v243 offset:6144\n"
        "ds_read_b64_tr_b16 v[80:81], v242 offset:12288\n"
        "ds_read_b64_tr_b16 v[82:83], v243 offset:12288\n"
        "ds_read_b64_tr_b16 v[76:77], v242 offset:8\n"
        "ds_read_b64_tr_b16 v[78:79], v243 offset:8\n"
        "ds_read_b64_tr_b16 v[68:69], v242 offset:6152\n"
        "ds_read_b64_tr_b16 v[70:71], v243 offset:6152\n"
        "ds_read_b64_tr_b16 v[72:73], v242 offset:12296\n"
        "ds_read_b64_tr_b16 v[74:75], v243 offset:12296\n"
        "v_mov_b32 v64, 0x3f803f80\n"
        "v_mov_b32 v65, 0x3f803f80\n"
        "v_mov_b32 v66, 0x3f803f80\n"
        "v_mov_b32 v67, 0x3f803f80\n"
        "ds_read_b64_tr_b16 v[88:89], v242 offset:64\n"
        "ds_read_b64_tr_b16 v[90:91], v243 offset:64\n"
        "s_waitcnt lgkmcnt(12)\n"
        "v_mfma_f32_16x16x32_bf16 v[104:107], v[84:87], v[64:67], 0\n"
        "s_waitcnt lgkmcnt(10)\n"
        "v_mfma_f32_16x16x32_bf16 v[104:107], v[92:95], v[64:67], v[104:107]\n"
        "s_waitcnt lgkmcnt(6)\n"
        "ds_read_b64_tr_b16 v[96:97], v242 offset:6208\n"
        "ds_read_b64_tr_b16 v[98:99], v243 offset:6208\n"
        "v_mfma_f32_16x16x32_bf16 v[104:107], v[80:83], v[64:67], v[104:107]\n"
        "ds_read_b64_tr_b16 v[92:93], v242 offset:12352\n"
        "ds_read_b64_tr_b16 v[94:95], v243 offset:12352\n"
        "v_mfma_f32_16x16x32_bf16 v[100:103], v[76:79], v[64:67], 0\n"
        "s_waitcnt lgkmcnt(6)\n"
        "ds_read_b64_tr_b16 v[80:81], v242 offset:72\n"
        "ds_read_b64_tr_b16 v[82:83], v243 offset:72\n"
        "v_mfma_f32_16x16x32_bf16 v[100:103], v[68:71], v[64:67], v[100:103]\n"
        "ds_read_b64_tr_b16 v[76:77], v242 offset:6216\n"
        "ds_read_b64_tr_b16 v[78:79], v243 offset:6216\n"
        "v_cvt_pk_bf16_f32 v68, v104, v105\n"
        "v_cvt_pk_bf16_f32 v69, v106, v107\n"
        "v_mfma_f32_16x16x32_bf16 v[100:103], v[72:75], v[64:67], v[100:103]\n"
        "s_waitcnt lgkmcnt(8)\n"
        "ds_read_b64_tr_b16 v[84:85], v242 offset:12360\n"
        "ds_read_b64_tr_b16 v[86:87], v243 offset:12360\n"
        "v_mfma_f32_16x16x32_bf16 v[104:107], v[88:91], v[64:67], 0\n"
        "ds_read_b64_tr_b16 v[72:73], v242 offset:128\n"
        "ds_read_b64_tr_b16 v[74:75], v243 offset:128\n"
        "s_waitcnt lgkmcnt(10)\n"
        "v_mfma_f32_16x16x32_bf16 v[104:107], v[96:99], v[64:67], v[104:107]\n"
        "ds_read_b64_tr_b16 v[88:89], v242 offset:6272\n"
        "ds_read_b64_tr_b16 v[90:91], v243 offset:6272\n"
        "v_cvt_pk_bf16_f32 v70, v100, v101\n"
        "v_cvt_pk_bf16_f32 v71, v102, v103\n"
        "s_waitcnt lgkmcnt(9)\n"
        "v_mfma_f32_16x16x32_bf16 v[104:107], v[92:95], v[64:67], v[104:107]\n"
        "s_waitcnt lgkmcnt(8)\n"
        "ds_read_b64_tr_b16 v[100:101], v242 offset:12416\n"
        "ds_read_b64_tr_b16 v[102:103], v243 offset:12416\n"
        "v_mfma_f32_16x16x32_bf16 v[108:111], v[80:83], v[64:67], 0\n"
        "s_waitcnt lgkmcnt(8)\n"
        "ds_read_b64_tr_b16 v[92:93], v242 offset:136\n"
        "ds_read_b64_tr_b16 v[94:95], v243 offset:136\n"
        "v_mfma_f32_16x16x32_bf16 v[108:111], v[76:79], v[64:67], v[108:111]\n"
        "ds_read_b64_tr_b16 v[96:97], v242 offset:6280\n"
        "ds_read_b64_tr_b16 v[98:99], v243 offset:6280\n"
        "v_cvt_pk_bf16_f32 v80, v104, v105\n"
        "v_cvt_pk_bf16_f32 v81, v106, v107\n"
        "s_waitcnt lgkmcnt(8)\n"
        "v_mfma_f32_16x16x32_bf16 v[108:111], v[84:87], v[64:67], v[108:111]\n"
        "ds_read_b64_tr_b16 v[76:77], v242 offset:12424\n"
        "ds_read_b64_tr_b16 v[78:79], v243 offset:12424\n"
        "v_mfma_f32_16x16x32_bf16 v[116:119], v[72:75], v[64:67], 0\n"
        "s_waitcnt lgkmcnt(8)\n"
        "ds_read_b128 v[72:75], v240\n"
        "v_mfma_f32_16x16x32_bf16 v[116:119], v[88:91], v[64:67], v[116:119]\n"
        "ds_read_b128 v[104:107], v241\n"
        "s_waitcnt lgkmcnt(8)\n"
        "v_mfma_f32_16x16x32_bf16 v[116:119], v[100:103], v[64:67], v[116:119]\n"
        "ds_read_b128 v[84:87], v240 offset:6144\n"
        "v_cvt_pk_bf16_f32 v82, v108, v109\n"
        "v_cvt_pk_bf16_f32 v83, v110, v111\n"
        "s_waitcnt lgkmcnt(7)\n"
        "v_mfma_f32_16x16x32_bf16 v[112:115], v[92:95], v[64:67], 0\n"
        "s_waitcnt lgkmcnt(5)\n"
        "ds_read_b128 v[92:95], v241 offset:6144\n"
        "v_mfma_f32_16x16x32_bf16 v[112:115], v[96:99], v[64:67], v[112:115]\n"
        "ds_read_b128 v[100:103], v240 offset:12288\n"
        "v_cvt_pk_bf16_f32 v88, v116, v117\n"
        "v_cvt_pk_bf16_f32 v89, v118, v119\n"
        "s_waitcnt lgkmcnt(5)\n"
        "v_mfma_f32_16x16x32_bf16 v[112:115], v[76:79], v[64:67], v[112:115]\n"
        "s_waitcnt lgkmcnt(4)\n"
        "ds_read_b128 v[116:119], v241 offset:12288\n"
        "v_mfma_f32_16x16x32_bf16 v[64:67], v[68:71], v[72:75], 0\n"
        "s_waitcnt lgkmcnt(4)\n"
        "ds_read_b128 v[120:123], v240 offset:64\n"
        "v_mfma_f32_16x16x32_bf16 v[72:75], v[68:71], v[104:107], 0\n"
        "ds_read_b128 v[108:111], v241 offset:64\n"
        "s_waitcnt lgkmcnt(5)\n"
        "v_mfma_f32_16x16x32_bf16 v[76:79], v[68:71], v[84:87], 0\n"
        "ds_read_b128 v[104:107], v240 offset:6208\n"
        "v_cvt_pk_bf16_f32 v90, v112, v113\n"
        "v_cvt_pk_bf16_f32 v91, v114, v115\n"
        "s_waitcnt lgkmcnt(5)\n"
        "v_mfma_f32_16x16x32_bf16 v[84:87], v[68:71], v[92:95], 0\n"
        "ds_read_b128 v[124:127], v241 offset:6208\n"
        "s_waitcnt lgkmcnt(5)\n"
        "v_mfma_f32_16x16x32_bf16 v[92:95], v[68:71], v[100:103], 0\n"
        "ds_read_b128 v[112:115], v240 offset:12352\n"
        "s_waitcnt lgkmcnt(5)\n"
        "v_mfma_f32_16x16x32_bf16 v[96:99], v[68:71], v[116:119], 0\n"
        "ds_read_b128 v[100:103], v241 offset:12352\n"
        "s_waitcnt lgkmcnt(5)\n"
        "v_mfma_f32_16x16x32_bf16 v[64:67], v[80:83], v[120:123], v[64:67]\n"
        "ds_read_b128 v[68:71], v240 offset:128\n"
        "s_waitcnt lgkmcnt(5)\n"
        "v_mfma_f32_16x16x32_bf16 v[72:75], v[80:83], v[108:111], v[72:75]\n"
        "ds_read_b128 v[116:119], v241 offset:128\n"
        "s_waitcnt lgkmcnt(5)\n"
        "v_mfma_f32_16x16x32_bf16 v[76:79], v[80:83], v[104:107], v[76:79]\n"
        "ds_read_b128 v[108:111], v240 offset:6272\n"
        "s_waitcnt lgkmcnt(5)\n"
        "v_mfma_f32_16x16x32_bf16 v[84:87], v[80:83], v[124:127], v[84:87]\n"
        "ds_read_b128 v[104:107], v241 offset:6272\n"
        "s_waitcnt lgkmcnt(5)\n"
        "v_mfma_f32_16x16x32_bf16 v[92:95], v[80:83], v[112:115], v[92:95]\n"
        "ds_read_b128 v[120:123], v240 offset:12416\n"
        "s_waitcnt lgkmcnt(5)\n"
        "v_mfma_f32_16x16x32_bf16 v[96:99], v[80:83], v[100:103], v[96:99]\n"
        "ds_read_b128 v[112:115], v241 offset:12416\n"
        "s_waitcnt lgkmcnt(5)\n"
        "v_mfma_f32_16x16x32_bf16 v[64:67], v[88:91], v[68:71], v[64:67]\n"
        "s_waitcnt lgkmcnt(4)\n"
        "v_mfma_f32_16x16x32_bf16 v[72:75], v[88:91], v[116:119], v[72:75]\n"
        "v_mov_b32 v239, s35\n"
        "s_waitcnt lgkmcnt(3)\n"
        "v_mfma_f32_16x16x32_bf16 v[76:79], v[88:91], v[108:111], v[76:79]\n"
        "s_waitcnt vmcnt(6)\n"
        "s_mov_b64 exec, 1\n"
        "ds_inc_u32 v239, v239 offset:0\n"
        "s_mov_b64 exec, -1\n"
        "s_waitcnt lgkmcnt(3)\n"
        "v_mfma_f32_16x16x32_bf16 v[84:87], v[88:91], v[104:107], v[84:87]\n"
        "v_add_f32 v238, v64, v72\n"
        "s_waitcnt lgkmcnt(2)\n"
        "v_mfma_f32_16x16x32_bf16 v[92:95], v[88:91], v[120:123], v[92:95]\n"
        "v_add_f32 v238, v238, v76\n"
        "s_waitcnt lgkmcnt(1)\n"
        "v_mfma_f32_16x16x32_bf16 v[96:99], v[88:91], v[112:115], v[96:99]\n"
        "v_mov_b32 v239, s37\n"
        "s_nop 0\n"
        "v_add_f32 v238, v238, v84\n"
        "s_nop 1\n"
        "v_add_f32 v238, v238, v92\n"
        "s_nop 1\n"
        "v_add_f32 v238, v238, v96\n"
        "s_nop 1\n"
        "v_add_f32_dpp v238, v238, v238 row_ror:8 row_mask:0xf bank_mask:0xf\n"
        "s_nop 1\n"
        "v_add_f32_dpp v238, v238, v238 row_ror:4 row_mask:0xf bank_mask:0xf\n"
        "s_nop 1\n"
        "v_add_f32_dpp v238, v238, v238 row_ror:2 row_mask:0xf bank_mask:0xf\n"
        "s_nop 1\n"
        "v_add_f32_dpp v238, v238, v238 row_ror:1 row_mask:0xf bank_mask:0xf\n"
        "v_mul_f32 v238, 0xbc2aaaab, v238\n"
        "v_mul_f32 v238, s34, v238\n"
        "v_fma_f32 v64, v64, s34, v238\n"
        "v_fma_f32 v72, v72, s34, v238\n"
        "v_fma_f32 v76, v76, s34, v238\n"
        "v_fma_f32 v84, v84, s34, v238\n"
        "v_fma_f32 v92, v92, s34, v238\n"
        "v_fma_f32 v96, v96, s34, v238\n"
        "ds_write_b32 v244, v64\n"
        "ds_write_b32 v244, v72 offset:16\n"
        "ds_write_b32 v244, v76 offset:128\n"
        "ds_write_b32 v244, v84 offset:144\n"
        "ds_write_b32 v244, v92 offset:256\n"
        "ds_write_b32 v244, v96 offset:272\n"
        "s_mov_b64 exec, 1\n"
        "ds_inc_u32 v239, v239 offset:0\n"
        "s_mov_b64 exec, -1\n"
        "s_setpc_b64 s[30:31]\n"
        "FOLDW_%=:\n"
        "ds_read_b64_tr_b16 v[64:65], v242 offset:0\n"
        "ds_read_b64_tr_b16 v[66:67], v243 offset:0\n"
        "ds_read_b128 v[172:175], v240\n"
        "ds_read_b64_tr_b16 v[180:181], v242 offset:6144\n"
        "ds_read_b64_tr_b16 v[182:183], v243 offset:6144\n"
        "ds_read_b128 v[120:123], v240 offset:64\n"
        "ds_read_b64_tr_b16 v[184:185], v242 offset:12288\n"
        "ds_read_b64_tr_b16 v[186:187], v243 offset:12288\n"
        "ds_read_b128 v[168:171], v240 offset:128\n"
        "ds_read_b128 v[160:163], v241\n"
        "ds_read_b128 v[148:151], v241 offset:64\n"
        "ds_read_b128 v[132:135], v241 offset:128\n"
        "ds_read_b128 v[124:127], v240 offset:6144\n"
        "ds_read_b128 v[104:107], v240 offset:6208\n"
        "ds_read_b128 v[92:95], v240 offset:6272\n"
        "ds_read_b128 v[96:99], v241 offset:6144\n"
        "ds_read_b128 v[88:91], v241 offset:6208\n"
        "ds_read_b128 v[84:87], v241 offset:6272\n"
        "s_waitcnt lgkmcnt(15)\n"
        "v_mfma_f32_16x16x32_bf16 v[136:139], v[172:175], v[64:67], 0\n"
        "ds_read_b128 v[100:103], v240 offset:12288\n"
        "s_waitcnt lgkmcnt(12)\n"
        "v_mfma_f32_16x16x32_bf16 v[136:139], v[120:123], v[180:183], v[136:139]\n"
        "s_waitcnt lgkmcnt(10)\n"
        "ds_read_b128 v[108:111], v240 offset:12352\n"
        "v_mfma_f32_16x16x32_bf16 v[136:139], v[168:171], v[184:187], v[136:139]\n"
        "s_waitcnt lgkmcnt(7)\n"
        "ds_read_b128 v[68:71], v240 offset:12416\n"
        "v_mfma_f32_16x16x32_bf16 v[140:143], v[160:163], v[64:67], 0\n"
        "ds_read_b128 v[72:75], v241 offset:12288\n"
        "v_mfma_f32_16x16x32_bf16 v[140:143], v[148:151], v[180:183], v[140:143]\n"
        "ds_read_b128 v[76:79], v241 offset:12352\n"
        "v_mfma_f32_16x16x32_bf16 v[140:143], v[132:135], v[184:187], v[140:143]\n"
        "ds_read_b128 v[80:83], v241 offset:12416\n"
        "v_mfma_f32_16x16x32_bf16 v[144:147], v[124:127], v[64:67], 0\n"
        "s_waitcnt lgkmcnt(6)\n"
        "ds_read_b64_tr_b16 v[112:113], v242 offset:8\n"
        "ds_read_b64_tr_b16 v[114:115], v243 offset:8\n"
        "v_mfma_f32_16x16x32_bf16 v[144:147], v[104:107], v[180:183], v[144:147]\n"
        "ds_read_b64_tr_b16 v[116:117], v242 offset:6152\n"
        "ds_read_b64_tr_b16 v[118:119], v243 offset:6152\n"
        "v_add_f32 v164, v136, v140\n"
        "v_add_f32 v165, v137, v141\n"
        "v_add_f32 v166, v138, v142\n"
        "v_add_f32 v167, v139, v143\n"
        "v_mfma_f32_16x16x32_bf16 v[144:147], v[92:95], v[184:187], v[144:147]\n"
        "ds_read_b64_tr_b16 v[128:129], v242 offset:12296\n"
        "ds_read_b64_tr_b16 v[130:131], v243 offset:12296\n"
        "v_mfma_f32_16x16x32_bf16 v[152:155], v[96:99], v[64:67], 0\n"
        "v_mfma_f32_16x16x32_bf16 v[152:155], v[88:91], v[180:183], v[152:155]\n"
        "v_mfma_f32_16x16x32_bf16 v[152:155], v[84:87], v[184:187], v[152:155]\n"
        "s_waitcnt lgkmcnt(6)\n"
        "v_mfma_f32_16x16x32_bf16 v[156:159], v[100:103], v[64:67], 0\n"
        "v_mfma_f32_16x16x32_bf16 v[156:159], v[108:111], v[180:183], v[156:159]\n"
        "v_add_f32 v164, v164, v144\n"
        "v_add_f32 v165, v165, v145\n"
        "v_add_f32 v166, v166, v146\n"
        "v_add_f32 v167, v167, v147\n"
        "v_mfma_f32_16x16x32_bf16 v[156:159], v[68:71], v[184:187], v[156:159]\n"
        "v_add_f32 v164, v164, v152\n"
        "v_add_f32 v165, v165, v153\n"
        "v_add_f32 v166, v166, v154\n"
        "v_add_f32 v167, v167, v155\n"
        "v_mfma_f32_16x16x32_bf16 v[176:179], v[72:75], v[64:67], 0\n"
        "v_mfma_f32_16x16x32_bf16 v[176:179], v[76:79], v[180:183], v[176:179]\n"
        "v_mfma_f32_16x16x32_bf16 v[176:179], v[80:83], v[184:187], v[176:179]\n"
        "s_waitcnt lgkmcnt(0)\n"
        "v_mov_b32 v239, s35\n"
        "v_add_f32 v164, v164, v156\n"
        "v_add_f32 v165, v165, v157\n"
        "v_add_f32 v166, v166, v158\n"
        "v_add_f32 v167, v167, v159\n"
        "s_waitcnt vmcnt(6)\n"
        "s_mov_b64 exec, 1\n"
        "ds_inc_u32 v239, v239 offset:0\n"
        "s_mov_b64 exec, -1\n"
        "v_add_f32 v164, v164, v176\n"
        "v_add_f32 v165, v165, v177\n"
        "v_mfma_f32_16x16x32_bf16 v[64:67], v[172:175], v[112:115], 0\n"
        "v_add_f32 v166, v166, v178\n"
        "v_add_f32 v167, v167, v179\n"
        "v_add_f32 v164, v164, v165\n"
        "v_add_f32 v166, v166, v167\n"
        "v_add_f32 v164, v164, v166\n"
        "v_mov_b32 v165, v164\n"
        "v_mfma_f32_16x16x32_bf16 v[64:67], v[120:123], v[116:119], v[64:67]\n"
        "v_mfma_f32_16x16x32_bf16 v[64:67], v[168:171], v[128:131], v[64:67]\n"
        "v_permlane32_swap_b32 v164, v165\n"
        "v_add_f32 v164, v164, v165\n"
        "v_mov_b32 v165, v164\n"
        "v_mfma_f32_16x16x32_bf16 v[120:123], v[160:163], v[112:115], 0\n"
        "global_load_dwordx4 v[40:43], v57, s[22:23] offset:-3072\n"
        "v_permlane16_swap_b32 v164, v165\n"
        "v_add_f32 v164, v164, v165\n"
        "v_mul_f32 v164, 0xbc2aaaab, v164\n"
        "v_mul_f32 v164, s34, v164\n"
        "v_fma_f32 v136, v136, s34, v164\n"
        "v_fma_f32 v137, v137, s34, v164\n"
        "v_fma_f32 v138, v138, s34, v164\n"
        "v_fma_f32 v139, v139, s34, v164\n"
        "v_fma_f32 v140, v140, s34, v164\n"
        "v_fma_f32 v141, v141, s34, v164\n"
        "v_mfma_f32_16x16x32_bf16 v[120:123], v[148:151], v[116:119], v[120:123]\n"
        "v_fma_f32 v142, v142, s34, v164\n"
        "v_fma_f32 v143, v143, s34, v164\n"
        "v_fma_f32 v144, v144, s34, v164\n"
        "v_fma_f32 v145, v145, s34, v164\n"
        "v_fma_f32 v146, v146, s34, v164\n"
        "v_fma_f32 v147, v147, s34, v164\n"
        "v_fma_f32 v152, v152, s34, v164\n"
        "v_fma_f32 v153, v153, s34, v164\n"
        "v_fma_f32 v154, v154, s34, v164\n"
        "v_fma_f32 v155, v155, s34, v164\n"
        "v_fma_f32 v156, v156, s34, v164\n"
        "v_fma_f32 v157, v157, s34, v164\n"
        "v_fma_f32 v158, v158, s34, v164\n"
        "v_fma_f32 v159, v159, s34, v164\n"
        "v_mfma_f32_16x16x32_bf16 v[120:123], v[132:135], v[128:131], v[120:123]\n"
        "global_load_dwordx4 v[44:47], v57, s[22:23] offset:-2048\n"
        "v_fma_f32 v176, v176, s34, v164\n"
        "v_fma_f32 v177, v177, s34, v164\n"
        "v_fma_f32 v178, v178, s34, v164\n"
        "v_fma_f32 v179, v179, s34, v164\n"
        "v_cvt_pk_bf16_f32 v132, v136, v137\n"
        "v_cvt_pk_bf16_f32 v133, v138, v139\n"
        "v_cvt_pk_bf16_f32 v134, v140, v141\n"
        "v_add_f32 v148, v64, v120\n"
        "v_add_f32 v149, v65, v121\n"
        "v_add_f32 v150, v66, v122\n"
        "v_add_f32 v151, v67, v123\n"
        "v_cvt_pk_bf16_f32 v135, v142, v143\n"
        "v_mfma_f32_16x16x32_bf16 v[140:143], v[124:127], v[112:115], 0\n"
        "v_cvt_pk_bf16_f32 v136, v144, v145\n"
        "v_cvt_pk_bf16_f32 v137, v146, v147\n"
        "v_cvt_pk_bf16_f32 v138, v152, v153\n"
        "v_cvt_pk_bf16_f32 v139, v154, v155\n"
        "v_cvt_pk_bf16_f32 v124, v156, v157\n"
        "v_cvt_pk_bf16_f32 v125, v158, v159\n"
        "v_cvt_pk_bf16_f32 v126, v176, v177\n"
        "v_cvt_pk_bf16_f32 v127, v178, v179\n"
        "v_mfma_f32_16x16x32_bf16 v[140:143], v[104:107], v[116:119], v[140:143]\n"
        "global_load_dwordx4 v[0:3], v57, s[22:23] offset:-1024\n"
        "v_mfma_f32_16x16x32_bf16 v[140:143], v[92:95], v[128:131], v[140:143]\n"
        "v_mfma_f32_16x16x32_bf16 v[104:107], v[96:99], v[112:115], 0\n"
        "global_load_dwordx4 v[4:7], v57, s[22:23] offset:0\n"
        "v_mfma_f32_16x16x32_bf16 v[104:107], v[88:91], v[116:119], v[104:107]\n"
        "v_mfma_f32_16x16x32_bf16 v[104:107], v[84:87], v[128:131], v[104:107]\n"
        "global_load_dwordx4 v[8:11], v57, s[22:23] offset:1024\n"
        "v_mfma_f32_16x16x32_bf16 v[84:87], v[100:103], v[112:115], 0\n"
        "v_mfma_f32_16x16x32_bf16 v[84:87], v[108:111], v[116:119], v[84:87]\n"
        "global_load_dwordx4 v[12:15], v57, s[22:23] offset:2048\n"
        "v_add_f32 v148, v148, v140\n"
        "v_add_f32 v149, v149, v141\n"
        "v_add_f32 v150, v150, v142\n"
        "v_add_f32 v151, v151, v143\n"
        "v_add_f32 v148, v148, v104\n"
        "v_add_f32 v149, v149, v105\n"
        "v_add_f32 v150, v150, v106\n"
        "v_add_f32 v151, v151, v107\n"
        "v_mfma_f32_16x16x32_bf16 v[84:87], v[68:71], v[128:131], v[84:87]\n"
        "v_mfma_f32_16x16x32_bf16 v[88:91], v[72:75], v[112:115], 0\n"
        "global_load_dwordx4 v[16:19], v57, s[22:23] offset:3072\n"
        "v_mfma_f32_16x16x32_bf16 v[88:91], v[76:79], v[116:119], v[88:91]\n"
        "v_mfma_f32_16x16x32_bf16 v[88:91], v[80:83], v[128:131], v[88:91]\n"
        "v_mov_b32 v239, s37\n"
        "s_nop 2\n"
        "v_add_f32 v148, v148, v84\n"
        "v_add_f32 v149, v149, v85\n"
        "v_add_f32 v150, v150, v86\n"
        "v_add_f32 v151, v151, v87\n"
        "v_add_f32 v148, v148, v88\n"
        "v_add_f32 v149, v149, v89\n"
        "v_add_f32 v150, v150, v90\n"
        "v_add_f32 v151, v151, v91\n"
        "v_add_f32 v148, v148, v149\n"
        "v_add_f32 v150, v150, v151\n"
        "v_add_f32 v148, v148, v150\n"
        "v_mov_b32 v149, v148\n"
        "s_nop 1\n"
        "v_permlane32_swap_b32 v148, v149\n"
        "v_add_f32 v148, v148, v149\n"
        "v_mov_b32 v149, v148\n"
        "s_nop 1\n"
        "v_permlane16_swap_b32 v148, v149\n"
        "v_add_f32 v148, v148, v149\n"
        "v_mul_f32 v148, 0xbc2aaaab, v148\n"
        "v_mul_f32 v148, s34, v148\n"
        "v_fma_f32 v64, v64, s34, v148\n"
        "v_fma_f32 v65, v65, s34, v148\n"
        "v_fma_f32 v66, v66, s34, v148\n"
        "v_fma_f32 v67, v67, s34, v148\n"
        "v_fma_f32 v120, v120, s34, v148\n"
        "v_fma_f32 v121, v121, s34, v148\n"
        "v_fma_f32 v122, v122, s34, v148\n"
        "v_fma_f32 v123, v123, s34, v148\n"
        "v_fma_f32 v140, v140, s34, v148\n"
        "v_fma_f32 v141, v141, s34, v148\n"
        "v_fma_f32 v142, v142, s34, v148\n"
        "v_fma_f32 v143, v143, s34, v148\n"
        "v_fma_f32 v104, v104, s34, v148\n"
        "v_fma_f32 v105, v105, s34, v148\n"
        "v_fma_f32 v106, v106, s34, v148\n"
        "v_fma_f32 v107, v107, s34, v148\n"
        "v_fma_f32 v84, v84, s34, v148\n"
        "v_fma_f32 v85, v85, s34, v148\n"
        "v_fma_f32 v86, v86, s34, v148\n"
        "v_fma_f32 v87, v87, s34, v148\n"
        "v_fma_f32 v88, v88, s34, v148\n"
        "v_fma_f32 v89, v89, s34, v148\n"
        "v_fma_f32 v90, v90, s34, v148\n"
        "v_fma_f32 v91, v91, s34, v148\n"
        "v_cvt_pk_bf16_f32 v68, v64, v65\n"
        "v_cvt_pk_bf16_f32 v69, v66, v67\n"
        "v_cvt_pk_bf16_f32 v70, v120, v121\n"
        "v_cvt_pk_bf16_f32 v71, v122, v123\n"
        "v_cvt_pk_bf16_f32 v64, v140, v141\n"
        "v_cvt_pk_bf16_f32 v65, v142, v143\n"
        "v_cvt_pk_bf16_f32 v66, v104, v105\n"
        "v_cvt_pk_bf16_f32 v67, v106, v107\n"
        "v_cvt_pk_bf16_f32 v72, v84, v85\n"
        "v_cvt_pk_bf16_f32 v73, v86, v87\n"
        "v_cvt_pk_bf16_f32 v74, v88, v89\n"
        "v_cvt_pk_bf16_f32 v75, v90, v91\n"
        "s_nop 1\n"
        "v_mov_b32 v238, s36\n"
        "L62_%=:\n"
        "ds_read_b32 v237, v238 offset:0\n"
        "s_waitcnt lgkmcnt(0)\n"
        "v_readfirstlane_b32 s40, v237\n"
        "s_nop 3\n"
        "s_cmp_ge_i32 s40, 4\n"
        "s_cbranch_scc1 L63_%=\n"
        "s_nop 0\n"
        "s_sub_u32 s41, s41, 1\n"
        "s_cbranch_scc0 L62_%=\n"
        "s_endpgm\n"
        "L63_%=:\n"
        "ds_write_b128 v244, v[132:135]\n"
        "ds_write_b128 v245, v[68:71]\n"
        "ds_write_b128 v244, v[136:139] offset:1024\n"
        "ds_write_b128 v245, v[64:67] offset:1024\n"
        "ds_write_b128 v244, v[124:127] offset:2048\n"
        "ds_write_b128 v245, v[72:75] offset:2048\n"
        "s_mov_b64 exec, 1\n"
        "ds_inc_u32 v239, v239 offset:0\n"
        "s_mov_b64 exec, -1\n"
        "s_setpc_b64 s[30:31]\n"
        "PPRO_%=:\n"
        "s_add_u32 s64, s64, 128\n"
        "s_cmp_lg_u32 s6, 0\n"
        "s_cbranch_scc1 L78_%=\n"
        "s_mov_b32 exec_hi, 0\n"
        "v_lshlrev_b32 v27, 2, v56\n"
        "v_add_u32 v27, 21600, v27\n"
        "v_mov_b32 v28, 0\n"
        "ds_write_b32 v27, v28\n"
        "s_mov_b64 exec, 0x1fffff\n"
        "s_bfe_u32 s7, s2, 0x50003\n"
        "s_mul_i32 s7, s7, 2656\n"
        "v_lshlrev_b32 v27, 7, v56\n"
        "v_add_u32 v27, s7, v27\n"
        "global_load_dword v252, v27, s[4:5]\n"
        "s_mov_b64 exec, -1\n"
        "L78_%=:\n"
        "s_cmp_lt_u32 s6, 2\n"
        "s_cselect_b32 s65, 3, 1\n"
        "s_cselect_b32 s61, 384, 0\n"
        "s_cselect_b32 s62, 9, 1\n"
        "s_cselect_b32 s63, 3, 4\n"
        "s_cselect_b32 s26, 3, 1\n"
        "s_cselect_b32 s34, 9, 1\n"
        "s_cselect_b32 s70, 72, 1\n"
        "s_cselect_b32 s71, 36, 4\n"
        "s_cselect_b32 s72, 6, 8\n"
        "s_mul_i32 s70, s70, 192\n"
        "s_mul_i32 s71, s71, 192\n"
        "s_mul_i32 s72, s72, 192\n"
        "s_mul_i32 s65, s65, 192\n"
        "s_mul_i32 s62, s62, 3072\n"
        "s_mul_i32 s63, s63, 1536\n"
        "s_mul_i32 s26, s26, 18432\n"
        "s_mul_i32 s34, s34, 1536\n"
        "s_add_u32 s7, s62, s63\n"
        "s_lshl_b32 s29, s63, 1\n"
        "s_add_u32 s60, s7, s63\n"
        "s_bitcmp1_b32 s6, 0\n"
        "s_cselect_b32 s28, s7, 0\n"
        "s_cselect_b32 s29, s29, s62\n"
        "s_cselect_b32 s60, s60, s63\n"
        "s_add_u32 s28, s28, s61\n"
        "s_add_u32 s29, s29, s61\n"
        "s_add_u32 s60, s60, s61\n"
        "v_add_u32 v24, 0, v56\n"
        "v_mul_u32_u24 v21, 171, v24\n"
        "v_lshrrev_b32 v21, 11, v21\n"
        "v_mad_i32_i24 v24, v21, -12, v24\n"
        "v_bfe_u32 v58, v21, 2, 2\n"
        "v_xor_b32 v58, v24, v58\n"
        "v_and_b32 v27, 3, v21\n"
        "v_mul_u32_u24 v27, s70, v27\n"
        "v_lshl_add_u32 v32, v58, 4, v27\n"
        "v_bfe_u32 v27, v21, 2, 1\n"
        "v_mad_u32_u24 v32, v27, s71, v32\n"
        "v_lshrrev_b32 v27, 3, v21\n"
        "v_mad_u32_u24 v32, v27, s72, v32\n"
        "v_and_b32 v27, 7, v21\n"
        "v_mul_u32_u24 v27, s65, v27\n"
        "v_lshl_add_u32 v58, v58, 4, v27\n"
        "v_add_u32 v25, 64, v56\n"
        "v_mul_u32_u24 v22, 171, v25\n"
        "v_lshrrev_b32 v22, 11, v22\n"
        "v_mad_i32_i24 v25, v22, -12, v25\n"
        "v_bfe_u32 v59, v22, 2, 2\n"
        "v_xor_b32 v59, v25, v59\n"
        "v_and_b32 v27, 3, v22\n"
        "v_mul_u32_u24 v27, s70, v27\n"
        "v_lshl_add_u32 v33, v59, 4, v27\n"
        "v_bfe_u32 v27, v22, 2, 1\n"
        "v_mad_u32_u24 v33, v27, s71, v33\n"
        "v_lshrrev_b32 v27, 3, v22\n"
        "v_mad_u32_u24 v33, v27, s72, v33\n"
        "v_and_b32 v27, 7, v22\n"
        "v_mul_u32_u24 v27, s65, v27\n"
        "v_lshl_add_u32 v59, v59, 4, v27\n"
        "v_lshrrev_b32 v27, 3, v22\n"
        "v_mad_u32_u24 v59, v27, s34, v59\n"
        "v_add_u32 v26, 128, v56\n"
        "v_mul_u32_u24 v23, 171, v26\n"
        "v_lshrrev_b32 v23, 11, v23\n"
        "v_mad_i32_i24 v26, v23, -12, v26\n"
        "v_bfe_u32 v60, v23, 2, 2\n"
        "v_xor_b32 v60, v26, v60\n"
        "v_and_b32 v27, 3, v23\n"
        "v_mul_u32_u24 v27, s70, v27\n"
        "v_lshl_add_u32 v34, v60, 4, v27\n"
        "v_bfe_u32 v27, v23, 2, 1\n"
        "v_mad_u32_u24 v34, v27, s71, v34\n"
        "v_lshrrev_b32 v27, 3, v23\n"
        "v_mad_u32_u24 v34, v27, s72, v34\n"
        "v_and_b32 v27, 7, v23\n"
        "v_mul_u32_u24 v27, s65, v27\n"
        "v_lshl_add_u32 v60, v60, 4, v27\n"
        "v_add_u32 v60, s34, v60\n"
        "s_and_b32 s7, s2, 7\n"
        "s_lshl_b32 s7, s7, 5\n"
        "s_lshr_b32 s14, s2, 3\n"
        "s_add_u32 s7, s7, s14\n"
        "s_mul_i32 s7, s7, 20\n"
        "s_mov_b32 s14, 22\n"
        "s_mul_i32 s15, s7, 57457\n"
        "s_lshr_b32 s15, s15, 22\n"
        "s_mul_i32 s16, s15, 73\n"
        "s_sub_u32 s16, s7, s16\n"
        "s_mul_i32 s15, s15, 686784\n"
        "s_mul_i32 s7, s16, 1344\n"
        "s_add_u32 s15, s15, s7\n"
        "s_sub_u32 s7, 6, s6\n"
        "v_mov_b32 v28, 96768\n"
        "s_mov_b32 s35, 50135232\n"
        "v_mov_b32 v29, 2048\n"
        "v_and_b32 v21, 7, v21\n"
        "v_cmp_lt_u32 vcc, s7, v21\n"
        "v_and_b32 v27, 3, v24\n"
        "v_mad_u32_u24 v29, v27, 48, v29\n"
        "v_lshrrev_b32 v27, 2, v24\n"
        "v_lshl_add_u32 v29, v27, 4, v29\n"
        "v_mul_u32_u24 v21, 192, v21\n"
        "v_cndmask_b32 v27, 0, v28, vcc\n"
        "v_add3_u32 v29, v29, v21, v27\n"
        "v_lshrrev_b32 v27, 3, v22\n"
        "v_xor_b32 v25, v25, v27\n"
        "v_mul_lo_u32 v30, v27, s35\n"
        "v_add_u32 v30, 1024, v30\n"
        "v_and_b32 v22, 7, v22\n"
        "v_cmp_lt_u32 vcc, s7, v22\n"
        "v_and_b32 v27, 3, v25\n"
        "v_mad_u32_u24 v30, v27, 48, v30\n"
        "v_lshrrev_b32 v27, 2, v25\n"
        "v_lshl_add_u32 v30, v27, 4, v30\n"
        "v_mul_u32_u24 v22, 192, v22\n"
        "v_cndmask_b32 v27, 0, v28, vcc\n"
        "v_add3_u32 v30, v30, v22, v27\n"
        "v_mov_b32 v31, 50135232\n"
        "v_xor_b32 v26, 1, v26\n"
        "v_and_b32 v23, 7, v23\n"
        "v_cmp_lt_u32 vcc, s7, v23\n"
        "v_and_b32 v27, 3, v26\n"
        "v_mad_u32_u24 v31, v27, 48, v31\n"
        "v_lshrrev_b32 v27, 2, v26\n"
        "v_lshl_add_u32 v31, v27, 4, v31\n"
        "v_mul_u32_u24 v23, 192, v23\n"
        "v_cndmask_b32 v27, 0, v28, vcc\n"
        "v_add3_u32 v31, v31, v23, v27\n"
        "s_mul_i32 s17, s6, 6208\n"
        "s_add_u32 s17, s17, 110176\n"
        "s_mov_b32 s18, s6\n"
        "s_mul_i32 s74, s6, 98304\n"
        "s_add_u32 s74, s74, s15\n"
        "s_add_u32 s75, s74, 100270464\n"
        "v_mov_b32 v250, 21600\n"
        "s_mov_b32 s41, 0x400000\n"
        "s_waitcnt lgkmcnt(0)\n"
        "s_cmp_lg_u32 s6, 0\n"
        "s_cbranch_scc1 L79_%=\n"
        "s_bitcmp1_b32 s2, 7\n"
        "s_cselect_b64 s[8:9], s[50:51], s[46:47]\n"
        "s_cselect_b64 s[10:11], s[52:53], s[48:49]\n"
        "s_bfe_u32 s7, s2, 0x40003\n"
        "s_cmp_ge_u32 s7, 12\n"
        "s_cselect_b64 s[8:9], s[10:11], s[8:9]\n"
        "s_cselect_b32 s10, 12, 0\n"
        "s_sub_u32 s7, s7, s10\n"
        "s_mul_i32 s7, s7, 4608\n"
        "v_lshlrev_b32 v27, 7, v56\n"
        "v_add_u32 v27, s7, v27\n"
        "s_mov_b32 exec_hi, 0xf\n"
        "global_load_dword v252, v27, s[8:9]\n"
        "s_mov_b32 exec_hi, -1\n"
        "s_mov_b64 s[24:25], s[56:57]\n"
        "s_mov_b64 exec, 1\n"
        "v_mov_b32 v27, 0\n"
        "global_load_dword v253, v27, s[24:25] offset:256 sc1\n"
        "s_mov_b64 exec, -1\n"
        "L79_%=:\n"
        "s_cmp_lt_u32 s6, 2\n"
        "s_cselect_b64 s[24:25], s[46:47], s[48:49]\n"
        "s_and_b32 s25, s25, 0xffff\n"
        "s_sub_u32 s8, s44, 2048\n"
        "s_subb_u32 s9, s45, 0\n"
        "s_and_b32 s9, s9, 0xffff\n"
        "s_mov_b32 s10, 200542976\n"
        "s_mov_b32 s11, 0x20000\n"
        "s_mul_i32 s7, s6, 7168\n"
        "s_add_u32 s7, s7, 6144\n"
        "s_add_u32 s22, s4, s7\n"
        "s_addc_u32 s23, s5, 0\n"
        "s_sub_u32 s7, s6, 0\n"
        "s_and_b32 s7, s7, 3\n"
        "s_min_u32 s7, s7, 2\n"
        "s_lshl_b32 s7, s7, 10\n"
        "s_add_u32 s73, s7, 18528\n"
        "v_add_u32 v27, s7, v57\n"
        "s_barrier\n"
        "s_mov_b32 m0, s73\n"
        "s_nop 0\n"
        "global_load_lds_dwordx4 v27, s[4:5]\n"
        "global_load_dwordx4 v[240:243], v57, s[22:23] offset:-3072\n"
        "global_load_dwordx4 v[244:247], v57, s[22:23] offset:-2048\n"
        "s_add_u32 m0, s64, 0\n"
        "s_nop 0\n"
        "buffer_load_dwordx4 v58, s[24:27], s28 offen lds\n"
        "s_add_u32 m0, s64, 1024\n"
        "s_nop 0\n"
        "buffer_load_dwordx4 v59, s[24:27], s28 offen lds\n"
        "s_add_u32 m0, s64, 2048\n"
        "s_nop 0\n"
        "buffer_load_dwordx4 v60, s[24:27], s28 offen lds\n"
        "s_add_u32 m0, s64, 3072\n"
        "s_nop 0\n"
        "buffer_load_dwordx4 v58, s[24:27], s29 offen lds\n"
        "s_add_u32 m0, s64, 4096\n"
        "s_nop 0\n"
        "buffer_load_dwordx4 v59, s[24:27], s29 offen lds\n"
        "s_add_u32 m0, s64, 5120\n"
        "s_nop 0\n"
        "buffer_load_dwordx4 v60, s[24:27], s29 offen lds\n"
        "s_add_u32 m0, s64, 6144\n"
        "s_nop 0\n"
        "buffer_load_dwordx4 v58, s[24:27], s60 offen lds\n"
        "s_add_u32 m0, s64, 7168\n"
        "s_nop 0\n"
        "buffer_load_dwordx4 v59, s[24:27], s60 offen lds\n"
        "s_add_u32 m0, s64, 8192\n"
        "s_nop 0\n"
        "buffer_load_dwordx4 v60, s[24:27], s60 offen lds\n"
        "s_cmp_lt_u32 s6, 2\n"
        "s_cselect_b32 s62, 3, 16\n"
        "s_cselect_b32 s63, 12, 32\n"
        "s_cselect_b64 s[24:25], s[50:51], s[52:53]\n"
        "s_cselect_b32 s64, 0, 117088\n"
        "s_mul_i32 s62, s62, 192\n"
        "s_mul_i32 s63, s63, 192\n"
        "s_and_b32 s25, s25, 0xffff\n"
        "s_mul_i32 s7, s6, 9216\n"
        "s_add_u32 s64, s64, s7\n"
        "s_add_u32 s64, s64, 0\n"
        "s_add_u32 s7, s62, s63\n"
        "s_lshl_b32 s29, s63, 1\n"
        "s_add_u32 s60, s7, s63\n"
        "s_bitcmp1_b32 s6, 0\n"
        "s_cselect_b32 s28, s7, 0\n"
        "s_cselect_b32 s29, s29, s62\n"
        "s_cselect_b32 s60, s60, s63\n"
        "s_add_u32 s28, s28, s61\n"
        "s_add_u32 s29, s29, s61\n"
        "s_add_u32 s60, s60, s61\n"
        "s_add_u32 m0, s64, 0\n"
        "s_nop 0\n"
        "buffer_load_dwordx4 v32, s[24:27], s28 offen lds\n"
        "s_add_u32 m0, s64, 1024\n"
        "s_nop 0\n"
        "buffer_load_dwordx4 v33, s[24:27], s28 offen lds\n"
        "s_add_u32 m0, s64, 2048\n"
        "s_nop 0\n"
        "buffer_load_dwordx4 v34, s[24:27], s28 offen lds\n"
        "s_add_u32 m0, s64, 3072\n"
        "s_nop 0\n"
        "buffer_load_dwordx4 v32, s[24:27], s29 offen lds\n"
        "s_add_u32 m0, s64, 4096\n"
        "s_nop 0\n"
        "buffer_load_dwordx4 v33, s[24:27], s29 offen lds\n"
        "s_add_u32 m0, s64, 5120\n"
        "s_nop 0\n"
        "buffer_load_dwordx4 v34, s[24:27], s29 offen lds\n"
        "s_add_u32 m0, s64, 6144\n"
        "s_nop 0\n"
        "buffer_load_dwordx4 v32, s[24:27], s60 offen lds\n"
        "s_add_u32 m0, s64, 7168\n"
        "s_nop 0\n"
        "buffer_load_dwordx4 v33, s[24:27], s60 offen lds\n"
        "s_add_u32 m0, s64, 8192\n"
        "s_nop 0\n"
        "buffer_load_dwordx4 v34, s[24:27], s60 offen lds\n"
        "s_setprio 2\n"
        "s_waitcnt vmcnt(9)\n"
        "s_mov_b64 exec, 1\n"
        "ds_inc_u32 v250, v250 offset:20\n"
        "s_mov_b64 exec, -1\n"
        "L80_%=:\n"
        "ds_read_b32 v251, v250 offset:20\n"
        "s_waitcnt lgkmcnt(0)\n"
        "v_readfirstlane_b32 s40, v251\n"
        "s_nop 3\n"
        "s_cmp_ge_i32 s40, 4\n"
        "s_cbranch_scc1 L81_%=\n"
        "s_nop 0\n"
        "s_sub_u32 s41, s41, 1\n"
        "s_cbranch_scc0 L80_%=\n"
        "s_endpgm\n"
        "L81_%=:\n"
        "s_add_u32 m0, s17, 0\n"
        "s_nop 0\n"
        "buffer_load_dwordx4 v29, s[8:11], s74 offen lds\n"
        "buffer_load_dwordx4 v30, s[8:11], s74 offen offset:1024 lds\n"
        "buffer_load_dwordx4 v31, s[8:11], s74 offen offset:2048 lds\n"
        "s_add_u32 m0, s17, 3104\n"
        "s_nop 0\n"
        "buffer_load_dwordx4 v29, s[8:11], s75 offen lds\n"
        "buffer_load_dwordx4 v30, s[8:11], s75 offen offset:1024 lds\n"
        "buffer_load_dwordx4 v31, s[8:11], s75 offen offset:2048 lds\n"
        "s_cmp_eq_u32 s6, 0\n"
        "s_cbranch_scc1 L82_%=\n"
        "s_mov_b32 s34, 0x411cc471\n"
        "s_mov_b32 s35, 21632\n"
        "s_mov_b32 s36, 21624\n"
        "s_mov_b32 s37, 21628\n"
        "s_call_b64 s[30:31], FOLDW_%=\n"
        "s_branch L83_%=\n"
        "L82_%=:\n"
        "s_waitcnt vmcnt(6)\n"
        "s_mov_b64 exec, 1\n"
        "ds_inc_u32 v250, v250 offset:32\n"
        "s_mov_b64 exec, -1\n"
        "s_mov_b64 exec, 1\n"
        "ds_inc_u32 v250, v250 offset:28\n"
        "s_mov_b64 exec, -1\n"
        "global_load_dwordx4 v[40:43], v57, s[22:23] offset:-3072\n"
        "global_load_dwordx4 v[44:47], v57, s[22:23] offset:-2048\n"
        "global_load_dwordx4 v[0:3], v57, s[22:23] offset:-1024\n"
        "global_load_dwordx4 v[4:7], v57, s[22:23] offset:0\n"
        "global_load_dwordx4 v[8:11], v57, s[22:23] offset:1024\n"
        "global_load_dwordx4 v[12:15], v57, s[22:23] offset:2048\n"
        "global_load_dwordx4 v[16:19], v57, s[22:23] offset:3072\n"
        "L83_%=:\n"
        "s_mov_b64 s[24:25], s[56:57]\n"
        "s_mov_b32 s44, 0xf0000f\n"
        "s_mov_b32 s45, 0xf0000f00\n"
        "s_mov_b32 s46, 0xff000ff\n"
        "s_mov_b32 s47, 0xf00fff00\n"
        "s_mov_b32 s48, 0x22222222\n"
        "s_mov_b32 s49, 0x22222222\n"
        "s_mov_b32 s50, 0x44444444\n"
        "s_mov_b32 s51, 0x44444444\n"
        "s_mov_b32 s52, 0x88888888\n"
        "s_mov_b32 s53, 0x88888888\n"
        "s_mov_b64 vcc, s[44:45]\n"
        "s_mov_b32 s28, 0x3c2aaaab\n"
        "s_mov_b32 s29, 0x3a7ba882\n"
        "s_mov_b32 s30, 0\n"
        "s_mov_b32 s34, 0\n"
        "s_mov_b32 s31, s18\n"
        "s_mov_b32 s36, s18\n"
        "s_mov_b32 s37, s18\n"
        "s_add_u32 s71, s15, 588672\n"
        "s_sub_u32 s72, 73, s16\n"
        "s_add_u32 s73, s17, 3104\n"
        "s_mov_b32 s68, 4\n"
        "s_mov_b32 s76, 16\n"
        "s_mov_b32 s74, -1\n"
        "s_mov_b32 s41, 0x400000\n"
        "L23_%=:\n"
        "ds_read_b32 v251, v250 offset:28\n"
        "s_waitcnt lgkmcnt(0)\n"
        "v_readfirstlane_b32 s40, v251\n"
        "s_nop 3\n"
        "s_cmp_ge_i32 s40, 7\n"
        "s_cbranch_scc1 L24_%=\n"
        "s_nop 0\n"
        "s_sub_u32 s41, s41, 1\n"
        "s_cbranch_scc0 L23_%=\n"
        "s_endpgm\n"
        "L24_%=:\n"
        "s_waitcnt vmcnt(0)\n"
        "v_readfirstlane_b32 s26, v253\n"
        "ds_read_b64 v[60:61], v14\n"
        "ds_read_b64 v[78:79], v14\n"
        "ds_read_b64 v[92:93], v14\n"
        "ds_read_b64 v[82:83], v14\n"
        "ds_read_b128 v[16:19], v15\n"
        "v_mov_b32 v15, 21600\n"
        "v_add_u32 v62, 21728, v13\n"
        "ds_read_b128 v[112:115], v62 offset:0\n"
        "ds_read_b128 v[116:119], v62 offset:1024\n"
        "ds_read_b128 v[120:123], v62 offset:2048\n"
        "ds_read_b128 v[124:127], v62 offset:3072\n"
        "ds_read_b128 v[128:131], v62 offset:4096\n"
        "ds_read_b128 v[132:135], v62 offset:5120\n"
        "ds_read_b128 v[136:139], v62 offset:6144\n"
        "ds_read_b128 v[140:143], v62 offset:7168\n"
        "ds_read_b128 v[144:147], v62 offset:8192\n"
        "ds_read_b128 v[148:151], v62 offset:9216\n"
        "ds_read_b128 v[152:155], v62 offset:10240\n"
        "ds_read_b128 v[156:159], v62 offset:11264\n"
        "ds_read_b128 v[160:163], v62 offset:12288\n"
        "ds_read_b128 v[164:167], v62 offset:13312\n"
        "ds_read_b128 v[168:171], v62 offset:14336\n"
        "ds_read_b128 v[172:175], v62 offset:15360\n"
        "ds_read_b128 v[176:179], v62 offset:16384\n"
        "ds_read_b128 v[180:183], v62 offset:17408\n"
        "ds_read_b128 v[184:187], v62 offset:18432\n"
        "ds_read_b128 v[188:191], v62 offset:19456\n"
        "ds_read_b128 v[192:195], v62 offset:20480\n"
        "ds_read_b128 v[196:199], v62 offset:21520\n"
        "ds_read_b128 v[200:203], v62 offset:22544\n"
        "ds_read_b128 v[204:207], v62 offset:23568\n"
        "ds_read_b128 v[208:211], v62 offset:24608\n"
        "ds_read_b128 v[212:215], v62 offset:25632\n"
        "ds_read_b128 v[216:219], v62 offset:26656\n"
        "ds_read_b128 v[220:223], v62 offset:27696\n"
        "ds_read_b128 v[224:227], v62 offset:28720\n"
        "ds_read_b128 v[228:231], v62 offset:29744\n"
        "ds_read_b128 v[232:235], v62 offset:30784\n"
        "ds_read_b128 v[236:239], v62 offset:31808\n"
        "ds_read_b128 v[240:243], v62 offset:32832\n"
        "ds_read_b128 v[244:247], v62 offset:33872\n"
        "ds_read_b128 v[248:251], v62 offset:34896\n"
        "ds_read_b128 v[252:255], v62 offset:35920\n"
        "ds_read_b128 v[40:43], v0 offset:0\n"
        "ds_read_b128 v[46:49], v0 offset:64\n"
        "ds_read_b128 v[52:55], v0 offset:128\n"
        "ds_read_b128 v[22:25], v0 offset:768\n"
        "ds_read_b128 v[28:31], v0 offset:832\n"
        "ds_read_b128 v[34:37], v0 offset:896\n"
        "s_add_u32 s37, s37, 4\n"
        "s_cmp_ge_u32 s37, 49\n"
        "s_cselect_b32 s69, 49, 0\n"
        "s_sub_u32 s37, s37, s69\n"
        "s_sub_u32 s69, s37, 5\n"
        "s_cmp_lt_u32 s69, 4\n"
        "s_subb_u32 s36, s36, -4\n"
        "s_cmp_ge_u32 s36, 6\n"
        "s_cselect_b32 s69, 6, 0\n"
        "s_addc_u32 s34, s34, 0\n"
        "s_sub_u32 s36, s36, s69\n"
        "s_mul_i32 s38, s34, 1344\n"
        "s_cmp_ge_u32 s34, s76\n"
        "s_cbranch_scc0 L27_%=\n"
        "s_nop 1\n"
        "v_mov_b32 v64, 21600\n"
        "L25_%=:\n"
        "ds_read_b32 v65, v64 offset:64\n"
        "s_waitcnt lgkmcnt(0)\n"
        "v_readfirstlane_b32 s40, v65\n"
        "s_nop 3\n"
        "s_cmp_ge_i32 s40, 1\n"
        "s_cbranch_scc1 L26_%=\n"
        "s_sleep 1\n"
        "s_sub_u32 s41, s41, 1\n"
        "s_cbranch_scc0 L25_%=\n"
        "s_endpgm\n"
        "L26_%=:\n"
        "v_mbcnt_lo_u32_b32 v65, -1, 0\n"
        "v_lshl_add_u32 v64, v65, 2, v64\n"
        "ds_read_b32 v65, v64 offset:68\n"
        "s_mov_b32 s14, s40\n"
        "s_mov_b32 s76, -1\n"
        "s_waitcnt lgkmcnt(0)\n"
        "v_readlane_b32 s22, v65, 0\n"
        "v_readlane_b32 s23, v65, 1\n"
        "s_nop 3\n"
        "L27_%=:\n"
        "s_cmp_ge_u32 s34, s72\n"
        "s_cselect_b32 s70, s71, s15\n"
        "s_cmp_ge_u32 s34, 20\n"
        "s_cselect_b32 s70, s22, s70\n"
        "s_cmp_ge_u32 s34, 21\n"
        "s_cselect_b32 s70, s23, s70\n"
        "s_add_u32 s38, s38, s70\n"
        "s_mul_i32 s69, s36, 98304\n"
        "s_add_u32 s38, s38, s69\n"
        "s_mov_b32 s35, s36\n"
        "s_mul_i32 s69, s36, -12\n"
        "s_add_u32 s69, s69, 60\n"
        "s_lshl_b64 s[54:55], -1, s69\n"
        "s_lshl_b64 s[56:57], s[54:55], 24\n"
        "s_ashr_i64 s[60:61], s[54:55], 8\n"
        "s_mov_b32 s58, s61\n"
        "s_mov_b32 s59, s56\n"
        "v_cndmask_b32_e64 v69, v4, v7, s[56:57]\n"
        "v_cndmask_b32_e64 v20, v5, v8, s[58:59]\n"
        "v_cndmask_b32_e64 v21, v6, v9, s[60:61]\n"
        "s_cmp_eq_u32 s37, 4\n"
        "s_cbranch_scc0 L28_%=\n"
        "s_nop 1\n"
        "ds_read_b32 v69, v13 offset:19552\n"
        "ds_read_b32 v20, v13 offset:19556\n"
        "ds_read_b32 v21, v13 offset:19560\n"
        "s_mov_b32 s35, 6\n"
        "s_mov_b32 s38, 0\n"
        "s_mov_b32 s69, 1344\n"
        "s_waitcnt lgkmcnt(0)\n"
        "v_lshrrev_b32 v64, 28, v69\n"
        "v_add_u32 v64, s34, v64\n"
        "v_and_b32 v69, 0x0fffffff, v69\n"
        "v_mad_u32_u24 v69, v64, s69, v69\n"
        "v_mov_b32 v65, s15\n"
        "v_cmpx_le_u32 vcc, s72, v64\n"
        "v_mov_b32 v65, s71\n"
        "s_mov_b64 exec, -1\n"
        "v_cmpx_le_u32 vcc, 20, v64\n"
        "v_mov_b32 v65, s22\n"
        "s_mov_b64 exec, -1\n"
        "v_cmpx_le_u32 vcc, 21, v64\n"
        "v_mov_b32 v65, s23\n"
        "s_mov_b64 exec, -1\n"
        "v_add_u32 v69, v69, v65\n"
        "v_lshrrev_b32 v64, 28, v20\n"
        "v_add_u32 v64, s34, v64\n"
        "v_and_b32 v20, 0x0fffffff, v20\n"
        "v_mad_u32_u24 v20, v64, s69, v20\n"
        "v_mov_b32 v65, s15\n"
        "v_cmpx_le_u32 vcc, s72, v64\n"
        "v_mov_b32 v65, s71\n"
        "s_mov_b64 exec, -1\n"
        "v_cmpx_le_u32 vcc, 20, v64\n"
        "v_mov_b32 v65, s22\n"
        "s_mov_b64 exec, -1\n"
        "v_cmpx_le_u32 vcc, 21, v64\n"
        "v_mov_b32 v65, s23\n"
        "s_mov_b64 exec, -1\n"
        "v_add_u32 v20, v20, v65\n"
        "v_lshrrev_b32 v64, 28, v21\n"
        "v_add_u32 v64, s34, v64\n"
        "v_and_b32 v21, 0x0fffffff, v21\n"
        "v_mad_u32_u24 v21, v64, s69, v21\n"
        "v_mov_b32 v65, s15\n"
        "v_cmpx_le_u32 vcc, s72, v64\n"
        "v_mov_b32 v65, s71\n"
        "s_mov_b64 exec, -1\n"
        "v_cmpx_le_u32 vcc, 20, v64\n"
        "v_mov_b32 v65, s22\n"
        "s_mov_b64 exec, -1\n"
        "v_cmpx_le_u32 vcc, 21, v64\n"
        "v_mov_b32 v65, s23\n"
        "s_mov_b64 exec, -1\n"
        "v_add_u32 v21, v21, v65\n"
        "s_nop 4\n"
        "s_mov_b64 vcc, s[44:45]\n"
        "L28_%=:\n"
        "s_cmp_lt_u32 s34, s14\n"
        "s_cselect_b32 s38, s38, 0x80000000\n"
        "s_add_u32 s39, s38, 100270464\n"
        "s_waitcnt lgkmcnt(0)\n"
        "s_mov_b64 exec, 1\n"
        "ds_inc_u32 v15, v10 offset:8\n"
        "s_mov_b64 exec, -1\n"
        "s_cmp_eq_u32 s18, 0\n"
        "s_cselect_b32 s75, -20, 8\n"
        "s_setprio 1\n"
        "PTOP_%=:\n"
        "s_waitcnt lgkmcnt(5)\n"
        "v_mfma_f32_16x16x32_bf16 v[100:103], v[112:115], v[40:43], 0\n"
        "s_mov_b32 m0, s17\n"
        "v_cndmask_b32_e64 v95, v84, v85, s[48:49]\n"
        "s_nop 1\n"
        "v_cndmask_b32_e64 v81, v88, v89, s[48:49]\n"
        "s_waitcnt lgkmcnt(3)\n"
        "v_mfma_f32_16x16x32_bf16 v[100:103], v[116:119], v[46:49], v[100:103]\n"
        "ds_read_b32 v80, v15 offset:8\n"
        "v_cndmask_b32_e64 v95, v95, v86, s[50:51]\n"
        "s_waitcnt lgkmcnt(2)\n"
        "v_mfma_f32_16x16x32_bf16 v[100:103], v[120:123], v[52:55], v[100:103]\n"
        "s_waitcnt lgkmcnt(1)\n"
        "buffer_load_dwordx4 v69, s[8:11], s38 offen lds\n"
        "v_mfma_f32_16x16x32_bf16 v[104:107], v[132:135], v[52:55], 0\n"
        "v_cndmask_b32_e64 v81, v81, v90, s[50:51]\n"
        "v_cndmask_b32_e64 v95, v95, v87, s[52:53]\n"
        "v_mfma_f32_16x16x32_bf16 v[104:107], v[128:131], v[46:49], v[104:107]\n"
        "s_setprio 3 \n"
        "v_cndmask_b32_e64 v81, v81, v91, s[52:53]\n"
        "v_mfma_f32_16x16x32_bf16 v[104:107], v[124:127], v[40:43], v[104:107]\n"
        "buffer_load_dwordx4 v20, s[8:11], s38 offen offset:1024 lds\n"
        "v_mfma_f32_16x16x32_bf16 v[84:87], v[136:139], v[40:43], 0\n"
        "s_waitcnt lgkmcnt(0)\n"
        "v_readfirstlane_b32 s40, v80\n"
        "v_cndmask_b32_dpp v95, v81, v95, vcc row_ror:4 row_mask:0xf bank_mask:0xf\n"
        "v_mfma_f32_16x16x32_bf16 v[84:87], v[140:143], v[46:49], v[84:87]\n"
        "s_cmp_lt_i32 s40, s68\n"
        "v_mfma_f32_16x16x32_bf16 v[84:87], v[144:147], v[52:55], v[84:87]\n"
        "s_cbranch_scc1 L3_%=\n"
        "L4_%=:\n"
        "ds_write_b128 v11, v[96:99]\n"
        "s_mov_b64 exec, s[46:47]\n"
        "ds_write_b32 v12, v95\n"
        "s_mov_b64 exec, -1\n"
        "v_mfma_f32_16x16x32_bf16 v[88:91], v[156:159], v[52:55], 0\n"
        "v_mfma_f32_16x16x32_bf16 v[88:91], v[152:155], v[46:49], v[88:91]\n"
        "v_mfma_f32_16x16x32_bf16 v[88:91], v[148:151], v[40:43], v[88:91]\n"
        "buffer_load_dwordx4 v21, s[8:11], s38 offen offset:2048 lds\n"
        "s_mov_b32 m0, s73\n"
        "ds_write_b128 v11, v[64:67] offset:832\n"
        "s_cmp_eq_u32 s74, s30\n"
        "v_mfma_f32_16x16x32_bf16 v[62:65], v[160:163], v[40:43], 0\n"
        "s_setprio 3\n"
        "s_cbranch_scc1 L5_%=\n"
        "s_setprio 1\n"
        "L5_%=:\n"
        "v_mfma_f32_16x16x32_bf16 v[62:65], v[164:167], v[46:49], v[62:65]\n"
        "v_mfma_f32_16x16x32_bf16 v[62:65], v[168:171], v[52:55], v[62:65]\n"
        "buffer_load_dwordx4 v69, s[8:11], s39 offen lds\n"
        "ds_write_b128 v11, v[74:77] offset:64\n"
        "v_mfma_f32_16x16x32_bf16 v[66:69], v[180:183], v[52:55], 0\n"
        "v_mfma_f32_16x16x32_bf16 v[66:69], v[176:179], v[46:49], v[66:69]\n"
        "v_mfma_f32_16x16x32_bf16 v[66:69], v[172:175], v[40:43], v[66:69]\n"
        "buffer_load_dwordx4 v20, s[8:11], s39 offen offset:1024 lds\n"
        "ds_write_b128 v11, v[70:73] offset:896\n"
        "v_mfma_f32_16x16x32_bf16 v[96:99], v[40:43], v[40:43], 0\n"
        "v_mfma_f32_16x16x32_bf16 v[96:99], v[46:49], v[46:49], v[96:99]\n"
        "v_mfma_f32_16x16x32_bf16 v[96:99], v[52:55], v[52:55], v[96:99]\n"
        "buffer_load_dwordx4 v21, s[8:11], s39 offen offset:2048 lds\n"
        "ds_write_b128 v11, v[56:59] offset:128\n"
        "v_mov_b64 v[56:57], 0\n"
        "v_mov_b64 v[58:59], 0\n"
        "v_mfma_f32_16x16x32_bf16 v[72:75], v[22:25], v[22:25], 0\n"
        "v_mfma_f32_16x16x32_bf16 v[72:75], v[28:31], v[28:31], v[72:75]\n"
        "v_mfma_f32_16x16x32_bf16 v[72:75], v[34:37], v[34:37], v[72:75]\n"
        "ds_write_b128 v11, v[108:111] offset:960\n"
        "v_smfmac_f32_16x16x64_bf16 v[56:59], v[40:43], v[100:107], v10\n"
        "v_cndmask_b32_e64 v80, v96, v97, s[48:49]\n"
        "v_smfmac_f32_16x16x64_bf16 v[56:59], v[46:49], v[84:91], v10\n"
        "v_cndmask_b32_e64 v80, v80, v98, s[50:51]\n"
        "v_smfmac_f32_16x16x64_bf16 v[56:59], v[52:55], v[62:69], v10\n"
        "s_mov_b64 exec, 1\n"
        "ds_inc_u32 v15, v10 offset:0\n"
        "s_mov_b64 exec, -1\n"
        "v_mfma_f32_16x16x32_bf16 v[104:107], v[112:115], v[22:25], 0\n"
        "v_cndmask_b32_e64 v102, v72, v73, s[48:49]\n"
        "v_cndmask_b32_e64 v102, v102, v74, s[50:51]\n"
        "v_mfma_f32_16x16x32_bf16 v[104:107], v[116:119], v[28:31], v[104:107]\n"
        "v_cndmask_b32_e64 v80, v80, v99, s[52:53]\n"
        "v_cndmask_b32_e64 v102, v102, v75, s[52:53]\n"
        "v_mfma_f32_16x16x32_bf16 v[104:107], v[120:123], v[34:37], v[104:107]\n"
        "v_mfma_f32_16x16x32_bf16 v[108:111], v[132:135], v[34:37], 0\n"
        "v_cndmask_b32_dpp v80, v102, v80, vcc row_ror:4 row_mask:0xf bank_mask:0xf\n"
        "v_mfma_f32_16x16x32_bf16 v[108:111], v[128:131], v[28:31], v[108:111]\n"
        "v_mfma_f32_16x16x32_bf16 v[108:111], v[124:127], v[22:25], v[108:111]\n"
        "v_mfma_f32_16x16x32_bf16 v[96:99], v[136:139], v[22:25], 0\n"
        "v_mfma_f32_16x16x32_bf16 v[96:99], v[140:143], v[28:31], v[96:99]\n"
        "v_mfma_f32_16x16x32_bf16 v[96:99], v[144:147], v[34:37], v[96:99]\n"
        "v_mfma_f32_16x16x32_bf16 v[100:103], v[156:159], v[34:37], 0\n"
        "v_mfma_f32_16x16x32_bf16 v[100:103], v[152:155], v[28:31], v[100:103]\n"
        "v_mfma_f32_16x16x32_bf16 v[100:103], v[148:151], v[22:25], v[100:103]\n"
        "v_mfma_f32_16x16x32_bf16 v[84:87], v[160:163], v[22:25], 0\n"
        "v_mfma_f32_16x16x32_bf16 v[84:87], v[164:167], v[28:31], v[84:87]\n"
        "s_and_b32 s69, s30, 1\n"
        "v_mfma_f32_16x16x32_bf16 v[84:87], v[168:171], v[34:37], v[84:87]\n"
        "s_lshl2_add_u32 s70, s69, 21600\n"
        "v_mfma_f32_16x16x32_bf16 v[88:91], v[180:183], v[34:37], 0\n"
        "v_mov_b32 v15, s70\n"
        "v_mfma_f32_16x16x32_bf16 v[88:91], v[176:179], v[28:31], v[88:91]\n"
        "s_mul_i32 s69, s69, 40064\n"
        "v_mfma_f32_16x16x32_bf16 v[88:91], v[172:175], v[22:25], v[88:91]\n"
        "s_mul_i32 s70, s31, 1664\n"
        "v_mfma_f32_16x16x32_bf16 v[68:71], v[16:19], v[40:43], 0\n"
        "s_add_u32 s69, s69, s70\n"
        "v_mfma_f32_16x16x32_bf16 v[68:71], v[16:19], v[46:49], v[68:71]\n"
        "v_add_u32 v11, s69, v3\n"
        "v_mfma_f32_16x16x32_bf16 v[68:71], v[16:19], v[52:55], v[68:71]\n"
        "v_add_u32 v12, s69, v2\n"
        "v_mov_b64 v[64:65], 0\n"
        "v_mfma_f32_16x16x32_bf16 v[72:75], v[16:19], v[22:25], 0\n"
        "s_lshr_b32 s68, s30, 1\n"
        "v_mov_b64 v[66:67], 0\n"
        "v_mfma_f32_16x16x32_bf16 v[72:75], v[16:19], v[28:31], v[72:75]\n"
        "s_lshl2_add_u32 s68, s68, 4\n"
        "v_mfma_f32_16x16x32_bf16 v[72:75], v[16:19], v[34:37], v[72:75]\n"
        "s_cmp_eq_u32 s31, 6\n"
        "v_smfmac_f32_16x16x64_bf16 v[64:67], v[22:25], v[104:111], v10\n"
        "s_cbranch_scc1 L8_%=\n"
        "L9_%=:\n"
        "v_smfmac_f32_16x16x64_bf16 v[64:67], v[28:31], v[96:103], v10\n"
        "s_mov_b32 s74, s30\n"
        "v_smfmac_f32_16x16x64_bf16 v[64:67], v[34:37], v[84:91], v10\n"
        "s_mov_b32 s30, s34\n"
        "v_mfma_f32_16x16x32_bf16 v[86:89], v[34:37], v[192:195], 0\n"
        "s_mov_b32 s31, s35\n"
        "v_cndmask_b32_dpp v110, v72, v68, vcc row_ror:4 row_mask:0xf bank_mask:0xf\n"
        "v_mul_f32 v111, s28, v110\n"
        "v_mfma_f32_16x16x32_bf16 v[86:89], v[28:31], v[188:191], v[86:89]\n"
        "s_add_u32 s37, s37, 4\n"
        "v_fma_f32 v111, -v111, v110, v80\n"
        "v_add_f32_e64 v111, s29, |v111|\n"
        "v_mfma_f32_16x16x32_bf16 v[86:89], v[22:25], v[184:187], v[86:89]\n"
        "s_cmp_ge_u32 s37, 49\n"
        "v_rsq_f32 v91, v111\n"
        "v_cndmask_b32_dpp v96, v64, v56, vcc row_ror:4 row_mask:0xf bank_mask:0xf\n"
        "v_mfma_f32_16x16x32_bf16 v[74:77], v[52:55], v[192:195], 0\n"
        "s_cselect_b32 s69, 49, 0\n"
        "v_cndmask_b32_e64 v91, 0, v91, s[46:47]\n"
        "v_cndmask_b32_dpp v98, v65, v57, vcc row_ror:4 row_mask:0xf bank_mask:0xf\n"
        "v_mfma_f32_16x16x32_bf16 v[74:77], v[46:49], v[188:191], v[74:77]\n"
        "s_sub_u32 s37, s37, s69\n"
        "v_cndmask_b32_dpp v99, v66, v58, vcc row_ror:4 row_mask:0xf bank_mask:0xf\n"
        "v_mul_f32_dpp v96, v91, v96 quad_perm:[0,0,0,0] row_mask:0xf bank_mask:0xf\n"
        "v_mfma_f32_16x16x32_bf16 v[74:77], v[40:43], v[184:187], v[74:77]\n"
        "s_sub_u32 s69, s37, 5\n"
        "v_mul_f32_dpp v98, v91, v98 quad_perm:[1,1,1,1] row_mask:0xf bank_mask:0xf\n"
        "v_mul_f32_dpp v99, v91, v99 quad_perm:[2,2,2,2] row_mask:0xf bank_mask:0xf\n"
        "v_mfma_f32_16x16x32_bf16 v[108:111], v[40:43], v[220:223], 0\n"
        "s_cmp_lt_u32 s69, 4\n"
        "v_cndmask_b32_dpp v95, v67, v59, vcc row_ror:4 row_mask:0xf bank_mask:0xf\n"
        "v_max3_f32 v101, v96, v98, v99\n"
        "v_mfma_f32_16x16x32_bf16 v[108:111], v[46:49], v[224:227], v[108:111]\n"
        "s_subb_u32 s36, s36, -4\n"
        "v_mul_f32_dpp v95, v91, v95 quad_perm:[3,3,3,3] row_mask:0xf bank_mask:0xf\n"
        "v_max_f32 v101, v101, v95\n"
        "v_mfma_f32_16x16x32_bf16 v[108:111], v[52:55], v[228:231], v[108:111]\n"
        "s_cmp_ge_u32 s36, 6\n"
        "v_mul_f32 v101, v101, v91\n"
        "v_fma_f32 v96, v96, v91, -v101\n"
        "v_fma_f32 v98, v98, v91, -v101\n"
        "v_mfma_f32_16x16x32_bf16 v[104:107], v[22:25], v[220:223], 0\n"
        "s_cselect_b32 s69, 6, 0\n"
        "v_fma_f32 v99, v99, v91, -v101\n"
        "v_fma_f32 v95, v95, v91, -v101\n"
        "v_mfma_f32_16x16x32_bf16 v[104:107], v[28:31], v[224:227], v[104:107]\n"
        "s_addc_u32 s34, s34, 0\n"
        "v_exp_f32 v96, v96\n"
        "v_mfma_f32_16x16x32_bf16 v[104:107], v[34:37], v[228:231], v[104:107]\n"
        "s_sub_u32 s36, s36, s69\n"
        "v_exp_f32 v98, v98\n"
        "v_cvt_pk_bf16_f32 v58, v74, v75\n"
        "v_mfma_f32_16x16x32_bf16 v[68:71], v[34:37], v[204:207], 0\n"
        "s_mul_i32 s38, s34, 1344\n"
        "v_exp_f32 v99, v99\n"
        "v_mfma_f32_16x16x32_bf16 v[68:71], v[28:31], v[200:203], v[68:71]\n"
        "s_cmp_ge_u32 s34, s76\n"
        "v_exp_f32 v95, v95\n"
        "v_mfma_f32_16x16x32_bf16 v[68:71], v[22:25], v[196:199], v[68:71]\n"
        "s_cbranch_scc1 L10_%=\n"
        "L11_%=:\n"
        "v_add_f32 v85, v96, v98\n"
        "v_add_f32 v97, v99, v95\n"
        "v_mfma_f32_16x16x32_bf16 v[100:103], v[52:55], v[204:207], 0\n"
        "s_cmp_ge_u32 s34, s72\n"
        "v_add_f32 v85, v85, v97\n"
        "v_mul_f32_dpp v96, v91, v96 quad_perm:[0,0,0,0] row_mask:0xf bank_mask:0xf\n"
        "v_mfma_f32_16x16x32_bf16 v[100:103], v[46:49], v[200:203], v[100:103]\n"
        "s_cselect_b32 s70, s71, s15\n"
        "v_rcp_f32 v85, v85\n"
        "v_mfma_f32_16x16x32_bf16 v[100:103], v[40:43], v[196:199], v[100:103]\n"
        "s_cmp_ge_u32 s34, 20\n"
        "v_mul_f32_dpp v98, v91, v98 quad_perm:[1,1,1,1] row_mask:0xf bank_mask:0xf\n"
        "v_mul_f32_dpp v99, v91, v99 quad_perm:[2,2,2,2] row_mask:0xf bank_mask:0xf\n"
        "v_mul_f32_dpp v95, v91, v95 quad_perm:[3,3,3,3] row_mask:0xf bank_mask:0xf\n"
        "v_mfma_f32_16x16x32_bf16 v[64:67], v[40:43], v[232:235], 0\n"
        "s_cselect_b32 s70, s22, s70\n"
        "v_mul_f32 v96, v96, v85\n"
        "v_mul_f32 v98, v98, v85\n"
        "v_mfma_f32_16x16x32_bf16 v[64:67], v[46:49], v[236:239], v[64:67]\n"
        "s_cmp_ge_u32 s34, 21\n"
        "v_mul_f32 v99, v99, v85\n"
        "v_mul_f32 v95, v95, v85\n"
        "v_mfma_f32_16x16x32_bf16 v[64:67], v[52:55], v[240:243], v[64:67]\n"
        "s_cselect_b32 s70, s23, s70\n"
        "v_cvt_pk_bf16_f32 v91, v96, v98\n"
        "v_cvt_pk_bf16_f32 v94, v99, v95\n"
        "v_and_b32 v38, v1, v91\n"
        "v_mfma_f32_16x16x32_bf16 v[96:99], v[22:25], v[232:235], 0\n"
        "s_add_u32 s38, s38, s70\n"
        "v_and_b32 v39, v1, v94\n"
        "v_cvt_pk_bf16_f32 v59, v76, v77\n"
        "v_mfma_f32_16x16x32_bf16 v[96:99], v[28:31], v[236:239], v[96:99]\n"
        "s_mul_i32 s69, s36, 98304\n"
        "v_and_b32_dpp v20, v91, v1 row_ror:12 row_mask:0xf bank_mask:0xf\n"
        "v_and_b32_dpp v21, v94, v1 row_ror:12 row_mask:0xf bank_mask:0xf\n"
        "v_mfma_f32_16x16x32_bf16 v[96:99], v[34:37], v[240:243], v[96:99]\n"
        "s_add_u32 s38, s38, s69\n"
        "v_cvt_pk_bf16_f32 v76, v86, v87\n"
        "v_cvt_pk_bf16_f32 v77, v88, v89\n"
        "v_mov_b64 v[50:51], v[38:39]\n"
        "v_mfma_f32_16x16x32_bf16 v[72:75], v[58:61], v[38:41], 0\n"
        "s_mov_b32 s35, s36\n"
        "v_cvt_pk_bf16_f32 v62, v108, v109\n"
        "v_cvt_pk_bf16_f32 v63, v110, v111\n"
        "v_mfma_f32_16x16x32_bf16 v[108:111], v[76:79], v[20:23], 0\n"
        "s_mul_i32 s69, s36, -12\n"
        "v_mov_b64 v[32:33], v[20:21]\n"
        "v_cvt_pk_bf16_f32 v80, v104, v105\n"
        "v_mfma_f32_16x16x32_bf16 v[86:89], v[60:63], v[48:51], 0\n"
        "s_add_u32 s69, s69, 60\n"
        "v_cvt_pk_bf16_f32 v81, v106, v107\n"
        "v_mov_b64 v[44:45], v[38:39]\n"
        "v_cvt_pk_bf16_f32 v94, v100, v101\n"
        "v_mfma_f32_16x16x32_bf16 v[104:107], v[78:81], v[30:33], 0\n"
        "s_lshl_b64 s[54:55], -1, s69\n"
        "v_cvt_pk_bf16_f32 v95, v102, v103\n"
        "v_mov_b64 v[26:27], v[20:21]\n"
        "v_mfma_f32_16x16x32_bf16 v[100:103], v[34:37], v[216:219], 0\n"
        "s_lshl_b64 s[56:57], s[54:55], 24\n"
        "v_cvt_pk_bf16_f32 v84, v68, v69\n"
        "v_cvt_pk_bf16_f32 v85, v70, v71\n"
        "v_mfma_f32_16x16x32_bf16 v[100:103], v[28:31], v[212:215], v[100:103]\n"
        "s_ashr_i64 s[60:61], s[54:55], 8\n"
        "v_cvt_pk_bf16_f32 v90, v64, v65\n"
        "v_cvt_pk_bf16_f32 v91, v66, v67\n"
        "v_mfma_f32_16x16x32_bf16 v[100:103], v[22:25], v[208:211], v[100:103]\n"
        "s_mov_b32 s58, s61\n"
        "v_cvt_pk_bf16_f32 v80, v96, v97\n"
        "v_cvt_pk_bf16_f32 v81, v98, v99\n"
        "v_mov_b64 v[56:57], v[38:39]\n"
        "v_mfma_f32_16x16x32_bf16 v[64:67], v[52:55], v[216:219], 0\n"
        "s_mov_b32 s59, s56\n"
        "v_mov_b64 v[38:39], v[20:21]\n"
        "v_cvt_pk_bf16_f32 v96, v72, v73\n"
        "v_mfma_f32_16x16x32_bf16 v[64:67], v[46:49], v[212:215], v[64:67]\n"
        "v_cndmask_b32_e64 v69, v4, v7, s[56:57]\n"
        "v_cvt_pk_bf16_f32 v76, v100, v101\n"
        "v_mfma_f32_16x16x32_bf16 v[64:67], v[40:43], v[208:211], v[64:67]\n"
        "v_cndmask_b32_e64 v20, v5, v8, s[58:59]\n"
        "v_cvt_pk_bf16_f32 v77, v102, v103\n"
        "v_mfma_f32_16x16x32_bf16 v[70:73], v[92:95], v[42:45], 0\n"
        "v_cndmask_b32_e64 v21, v6, v9, s[60:61]\n"
        "v_cvt_pk_bf16_f32 v97, v74, v75\n"
        "v_mfma_f32_16x16x32_bf16 v[100:103], v[82:85], v[24:27], 0\n"
        "s_cmp_eq_u32 s37, 4\n"
        "v_cvt_pk_bf16_f32 v98, v86, v87\n"
        "v_cvt_pk_bf16_f32 v58, v64, v65\n"
        "v_mfma_f32_16x16x32_bf16 v[84:87], v[40:43], v[244:247], 0\n"
        "s_cbranch_scc1 L12_%=\n"
        "L13_%=:\n"
        "v_cvt_pk_bf16_f32 v59, v66, v67\n"
        "v_cvt_pk_bf16_f32 v99, v88, v89\n"
        "v_mfma_f32_16x16x32_bf16 v[84:87], v[46:49], v[248:251], v[84:87]\n"
        "s_cmp_lt_u32 s34, s14\n"
        "v_cvt_pk_bf16_f32 v74, v70, v71\n"
        "v_cvt_pk_bf16_f32 v75, v72, v73\n"
        "v_mfma_f32_16x16x32_bf16 v[84:87], v[52:55], v[252:255], v[84:87]\n"
        "s_cselect_b32 s38, s38, 0x80000000\n"
        "v_cvt_pk_bf16_f32 v64, v108, v109\n"
        "v_cvt_pk_bf16_f32 v65, v110, v111\n"
        "v_cvt_pk_bf16_f32 v66, v104, v105\n"
        "v_mfma_f32_16x16x32_bf16 v[108:111], v[22:25], v[244:247], 0\n"
        "s_add_u32 s39, s38, 100270464\n"
        "v_cvt_pk_bf16_f32 v67, v106, v107\n"
        "v_cvt_pk_bf16_f32 v70, v100, v101\n"
        "v_mfma_f32_16x16x32_bf16 v[108:111], v[28:31], v[248:251], v[108:111]\n"
        "v_cvt_pk_bf16_f32 v62, v84, v85\n"
        "v_cvt_pk_bf16_f32 v63, v86, v87\n"
        "v_mfma_f32_16x16x32_bf16 v[108:111], v[34:37], v[252:255], v[108:111]\n"
        "v_cvt_pk_bf16_f32 v71, v102, v103\n"
        "v_mfma_f32_16x16x32_bf16 v[84:87], v[90:93], v[50:53], 0\n"
        "v_mfma_f32_16x16x32_bf16 v[100:103], v[80:83], v[32:35], 0\n"
        "v_mfma_f32_16x16x32_bf16 v[88:91], v[58:61], v[44:47], 0\n"
        "s_waitcnt vmcnt(0)\n"
        "ds_read_b128 v[40:43], v0\n"
        "ds_read_b128 v[22:25], v0 offset:768\n"
        "ds_read_b128 v[46:49], v0 offset:64\n"
        "v_cvt_pk_bf16_f32 v80, v108, v109\n"
        "v_mfma_f32_16x16x32_bf16 v[104:107], v[76:79], v[26:29], 0\n"
        "s_add_u32 s75, s75, 1\n"
        "ds_read_b128 v[28:31], v0 offset:832\n"
        "v_cvt_pk_bf16_f32 v81, v110, v111\n"
        "v_cvt_pk_bf16_f32 v76, v84, v85\n"
        "v_cvt_pk_bf16_f32 v77, v86, v87\n"
        "v_mfma_f32_16x16x32_bf16 v[84:87], v[60:63], v[54:57], 0\n"
        "s_cmp_lt_u32 s75, 4\n"
        "ds_read_b128 v[52:55], v0 offset:128\n"
        "v_cvt_pk_bf16_f32 v56, v88, v89\n"
        "v_cvt_pk_bf16_f32 v57, v90, v91\n"
        "v_cvt_pk_bf16_f32 v72, v100, v101\n"
        "v_mfma_f32_16x16x32_bf16 v[88:91], v[78:81], v[36:39], 0\n"
        "s_cbranch_scc1 L19_%=\n"
        "L20_%=:\n"
        "ds_read_b128 v[34:37], v0 offset:896\n"
        "v_cvt_pk_bf16_f32 v58, v84, v85\n"
        "v_cvt_pk_bf16_f32 v59, v86, v87\n"
        "v_cvt_pk_bf16_f32 v73, v102, v103\n"
        "v_mfma_f32_16x16x32_bf16 v[84:87], v[96:99], v[96:99], 0\n"
        "v_cvt_pk_bf16_f32 v108, v104, v105\n"
        "v_cvt_pk_bf16_f32 v109, v106, v107\n"
        "v_mfma_f32_16x16x32_bf16 v[84:87], v[74:77], v[74:77], v[84:87]\n"
        "v_cvt_pk_bf16_f32 v110, v88, v89\n"
        "v_cvt_pk_bf16_f32 v111, v90, v91\n"
        "v_mfma_f32_16x16x32_bf16 v[84:87], v[56:59], v[56:59], v[84:87]\n"
        "v_mfma_f32_16x16x32_bf16 v[88:91], v[64:67], v[64:67], 0\n"
        "v_mfma_f32_16x16x32_bf16 v[88:91], v[70:73], v[70:73], v[88:91]\n"
        "v_mfma_f32_16x16x32_bf16 v[88:91], v[108:111], v[108:111], v[88:91]\n"
        "s_cmp_lt_u32 s30, s14\n"
        "s_cbranch_scc1 PTOP_%=\n"
        "s_waitcnt lgkmcnt(0)\n"
        "s_nop 15\n"
        "L21_%=:\n"
        "ds_read_b32 v255, v15 offset:8\n"
        "s_waitcnt lgkmcnt(0)\n"
        "v_readfirstlane_b32 s40, v255\n"
        "s_nop 3\n"
        "s_cmp_ge_i32 s40, s68\n"
        "s_cbranch_scc1 L22_%=\n"
        "s_sleep 1\n"
        "s_sub_u32 s41, s41, 1\n"
        "s_cbranch_scc0 L21_%=\n"
        "s_endpgm\n"
        "L22_%=:\n"
        "v_cndmask_b32_e64 v255, v84, v85, s[48:49]\n"
        "v_cndmask_b32_e64 v254, v88, v89, s[48:49]\n"
        "v_cndmask_b32_e64 v255, v255, v86, s[50:51]\n"
        "v_cndmask_b32_e64 v254, v254, v90, s[50:51]\n"
        "v_cndmask_b32_e64 v255, v255, v87, s[52:53]\n"
        "v_cndmask_b32_e64 v254, v254, v91, s[52:53]\n"
        "ds_write_b128 v11, v[96:99]\n"
        "ds_write_b128 v11, v[64:67] offset:832\n"
        "v_cndmask_b32_dpp v255, v254, v255, vcc row_ror:4 row_mask:0xf bank_mask:0xf\n"
        "ds_write_b128 v11, v[74:77] offset:64\n"
        "ds_write_b128 v11, v[70:73] offset:896\n"
        "ds_write_b128 v11, v[56:59] offset:128\n"
        "ds_write_b128 v11, v[108:111] offset:960\n"
        "s_mov_b64 exec, s[46:47]\n"
        "ds_write_b32 v12, v255\n"
        "s_mov_b64 exec, -1\n"
        "s_mov_b64 exec, 1\n"
        "ds_inc_u32 v15, v10 offset:0\n"
        "s_mov_b64 exec, -1\n"
        "s_or_b32 s40, s18, s2\n"
        "s_cbranch_scc1 L31_%=\n"
        "L30_%=:\n"
        "s_mov_b64 exec, 1\n"
        "v_mov_b32 v62, 0\n"
        "v_mov_b32 v63, 1\n"
        "global_atomic_add v64, v62, v63, s[24:25] offset:256 sc0\n"
        "s_mov_b64 exec, -1\n"
        "s_mov_b64 exec, 1\n"
        "v_mov_b32 v62, 0\n"
        "v_mov_b32 v63, 0\n"
        "global_atomic_add v64, v62, v63, s[24:25] offset:0 sc0\n"
        "s_mov_b64 exec, -1\n"
        "s_waitcnt vmcnt(0)\n"
        "v_readfirstlane_b32 s40, v64\n"
        "s_nop 3\n"
        "s_cmp_ge_u32 s40, 512\n"
        "s_cbranch_scc1 L31_%=\n"
        "s_sleep 8\n"
        "s_sub_u32 s41, s41, 64\n"
        "s_cbranch_scc0 L30_%=\n"
        "L31_%=:\n"
        "s_endpgm\n"
        "L3_%=:\n"
        "s_nop 7\n"
        "L1_%=:\n"
        "ds_read_b32 v80, v15 offset:8\n"
        "s_waitcnt lgkmcnt(0)\n"
        "v_readfirstlane_b32 s40, v80\n"
        "s_nop 3\n"
        "s_cmp_ge_i32 s40, s68\n"
        "s_cbranch_scc1 L2_%=\n"
        "s_sleep 1\n"
        "s_sub_u32 s41, s41, 1\n"
        "s_cbranch_scc0 L1_%=\n"
        "s_endpgm\n"
        "L2_%=:\n"
        "s_nop 1\n"
        "s_branch L4_%=\n"
        "L8_%=:\n"
        "s_nop 7\n"
        "ds_read_u16 v11, v13 offset:19564\n"
        "ds_read_u16 v12, v13 offset:19566\n"
        "s_cmp_ge_u32 s30, 10\n"
        "s_cselect_b32 s69, 10, 0\n"
        "s_cmp_ge_u32 s30, 20\n"
        "s_cselect_b32 s69, 20, s69\n"
        "s_sub_u32 s69, s30, s69\n"
        "s_mul_i32 s69, s69, 832\n"
        "s_waitcnt lgkmcnt(0)\n"
        "v_add_u32 v11, s69, v11\n"
        "v_subrev_u32 v94, 8320, v11\n"
        "v_min_u32 v11, v11, v94\n"
        "v_add_u32 v11, 101856, v11\n"
        "v_add_u32 v12, s69, v12\n"
        "v_subrev_u32 v94, 8320, v12\n"
        "v_min_u32 v12, v12, v94\n"
        "v_add_u32 v12, 101856, v12\n"
        "s_nop 1\n"
        "s_branch L9_%=\n"
        "L10_%=:\n"
        "s_nop 7\n"
        "v_mov_b32 v94, 21600\n"
        "L6_%=:\n"
        "ds_read_b32 v63, v94 offset:64\n"
        "s_waitcnt lgkmcnt(0)\n"
        "v_readfirstlane_b32 s40, v63\n"
        "s_nop 3\n"
        "s_cmp_ge_i32 s40, 1\n"
        "s_cbranch_scc1 L7_%=\n"
        "s_sleep 1\n"
        "s_sub_u32 s41, s41, 1\n"
        "s_cbranch_scc0 L6_%=\n"
        "s_endpgm\n"
        "L7_%=:\n"
        "v_mbcnt_lo_u32_b32 v63, -1, 0\n"
        "v_lshl_add_u32 v94, v63, 2, v94\n"
        "ds_read_b32 v63, v94 offset:68\n"
        "s_mov_b32 s14, s40\n"
        "s_mov_b32 s76, -1\n"
        "s_waitcnt lgkmcnt(0)\n"
        "v_readlane_b32 s22, v63, 0\n"
        "v_readlane_b32 s23, v63, 1\n"
        "s_nop 3\n"
        "s_nop 1\n"
        "s_branch L11_%=\n"
        "L12_%=:\n"
        "s_nop 7\n"
        "ds_read_b32 v69, v13 offset:19552\n"
        "ds_read_b32 v20, v13 offset:19556\n"
        "ds_read_b32 v21, v13 offset:19560\n"
        "s_mov_b32 s35, 6\n"
        "s_mov_b32 s38, 0\n"
        "s_mov_b32 s69, 1344\n"
        "s_waitcnt lgkmcnt(0)\n"
        "v_lshrrev_b32 v94, 28, v69\n"
        "v_add_u32 v94, s34, v94\n"
        "v_and_b32 v69, 0x0fffffff, v69\n"
        "v_mad_u32_u24 v69, v94, s69, v69\n"
        "v_mov_b32 v63, s15\n"
        "v_cmpx_le_u32 vcc, s72, v94\n"
        "v_mov_b32 v63, s71\n"
        "s_mov_b64 exec, -1\n"
        "v_cmpx_le_u32 vcc, 20, v94\n"
        "v_mov_b32 v63, s22\n"
        "s_mov_b64 exec, -1\n"
        "v_cmpx_le_u32 vcc, 21, v94\n"
        "v_mov_b32 v63, s23\n"
        "s_mov_b64 exec, -1\n"
        "v_add_u32 v69, v69, v63\n"
        "v_lshrrev_b32 v94, 28, v20\n"
        "v_add_u32 v94, s34, v94\n"
        "v_and_b32 v20, 0x0fffffff, v20\n"
        "v_mad_u32_u24 v20, v94, s69, v20\n"
        "v_mov_b32 v63, s15\n"
        "v_cmpx_le_u32 vcc, s72, v94\n"
        "v_mov_b32 v63, s71\n"
        "s_mov_b64 exec, -1\n"
        "v_cmpx_le_u32 vcc, 20, v94\n"
        "v_mov_b32 v63, s22\n"
        "s_mov_b64 exec, -1\n"
        "v_cmpx_le_u32 vcc, 21, v94\n"
        "v_mov_b32 v63, s23\n"
        "s_mov_b64 exec, -1\n"
        "v_add_u32 v20, v20, v63\n"
        "v_lshrrev_b32 v94, 28, v21\n"
        "v_add_u32 v94, s34, v94\n"
        "v_and_b32 v21, 0x0fffffff, v21\n"
        "v_mad_u32_u24 v21, v94, s69, v21\n"
        "v_mov_b32 v63, s15\n"
        "v_cmpx_le_u32 vcc, s72, v94\n"
        "v_mov_b32 v63, s71\n"
        "s_mov_b64 exec, -1\n"
        "v_cmpx_le_u32 vcc, 20, v94\n"
        "v_mov_b32 v63, s22\n"
        "s_mov_b64 exec, -1\n"
        "v_cmpx_le_u32 vcc, 21, v94\n"
        "v_mov_b32 v63, s23\n"
        "s_mov_b64 exec, -1\n"
        "v_add_u32 v21, v21, v63\n"
        "s_nop 4\n"
        "s_mov_b64 vcc, s[44:45]\n"
        "s_nop 1\n"
        "s_branch L13_%=\n"
        "L19_%=:\n"
        "s_nop 7\n"
        "v_readfirstlane_b32 s62, v14\n"
        "s_cmp_eq_u32 s75, 1\n"
        "s_cbranch_scc1 L14_%=\n"
        "s_cmp_ge_u32 s75, 2\n"
        "s_cbranch_scc1 L15_%=\n"
        "s_mov_b32 s27, 20\n"
        "s_cmp_lg_u32 s2, 0\n"
        "s_cbranch_scc1 L18_%=\n"
        "s_not_b32 s26, s26\n"
        "s_mov_b64 exec, 1\n"
        "v_mov_b32 v94, 0\n"
        "v_mov_b32 v63, 1\n"
        "global_atomic_add v14, v94, v63, s[24:25] offset:256 sc0\n"
        "s_mov_b64 exec, -1\n"
        "s_branch L16_%=\n"
        "L18_%=:\n"
        "s_mov_b64 exec, 1\n"
        "v_mov_b32 v94, 0\n"
        "global_load_dword v14, v94, s[24:25] offset:256 sc1\n"
        "s_mov_b64 exec, -1\n"
        "s_branch L16_%=\n"
        "L14_%=:\n"
        "s_cmp_lg_u32 s62, s26\n"
        "s_cbranch_scc1 L17_%=\n"
        "s_sleep 8\n"
        "s_mov_b64 exec, 1\n"
        "v_mov_b32 v94, 0\n"
        "global_load_dword v14, v94, s[24:25] offset:256 sc1\n"
        "s_mov_b64 exec, -1\n"
        "s_waitcnt vmcnt(0)\n"
        "v_readfirstlane_b32 s62, v14\n"
        "s_sub_u32 s41, s41, 64\n"
        "s_cbranch_scc0 L14_%=\n"
        "s_endpgm\n"
        "L15_%=:\n"
        "s_cmp_lt_u32 s62, 209\n"
        "s_addc_u32 s27, s27, 0\n"
        "s_min_u32 s62, s62, 208\n"
        "s_add_u32 s62, s62, 5120\n"
        "s_mul_i32 s64, s62, 57457\n"
        "s_lshr_b32 s64, s64, 22\n"
        "s_sub_u32 s62, s62, 18\n"
        "s_sub_u32 s62, s62, s75\n"
        "s_mul_i32 s62, s62, 1344\n"
        "s_mul_i32 s64, s64, 588672\n"
        "s_add_u32 s62, s62, s64\n"
        "s_lshl2_add_u32 s64, s75, 21600\n"
        "v_mov_b32 v94, s64\n"
        "v_mov_b32 v63, s62\n"
        "ds_write_b32 v94, v63 offset:60\n"
        "s_waitcnt lgkmcnt(0)\n"
        "s_cmp_lt_u32 s75, 3\n"
        "s_cbranch_scc1 L17_%=\n"
        "v_mov_b32 v63, s27\n"
        "ds_write_b32 v94, v63 offset:52\n"
        "s_waitcnt lgkmcnt(0)\n"
        "s_branch L16_%=\n"
        "L17_%=:\n"
        "s_mov_b64 exec, 1\n"
        "v_mov_b32 v94, 0\n"
        "v_mov_b32 v63, 1\n"
        "global_atomic_add v14, v94, v63, s[24:25] offset:0 sc0\n"
        "s_mov_b64 exec, -1\n"
        "L16_%=:\n"
        "s_nop 1\n"
        "s_branch L20_%=\n"
        "CONS_%=:\n"
        "s_mov_b32 s41, 0x400000\n"
        "s_mov_b32 s48, 0xffffffff\n"
        "s_mov_b32 s49, 0xffff0000\n"
        "s_mov_b32 s50, 0x33333333\n"
        "s_mov_b32 s51, 0x33333333\n"
        "s_mov_b32 s52, 0x3c2aaaab\n"
        "s_mov_b32 s53, 0x3a7ba882\n"
        "s_mov_b32 s56, 0x0\n"
        "s_mov_b32 s57, 0xfeff0000\n"
        "s_mov_b32 s62, 0x11111111\n"
        "s_mov_b32 s63, 0x11111111\n"
        "s_mov_b64 s[44:45], s[12:13]\n"
        "s_and_b32 s45, s45, 0xffff\n"
        "s_mov_b32 s47, 0x20000\n"
        "s_mov_b32 s75, 832\n"
        "s_mov_b32 s76, 17\n"
        "s_sub_u32 s73, s18, 7488\n"
        "s_mov_b32 s28, 0xffff0000\n"
        "s_mov_b32 s31, 0\n"
        "s_add_u32 s72, s15, s17\n"
        "s_add_u32 s70, s72, 588672\n"
        "s_sub_u32 s71, 73, s16\n"
        "s_mov_b32 s35, s72\n"
        "s_mov_b32 s36, 0x20000000\n"
        "L56_%=:\n"
        "ds_read_b32 v251, v250 offset:52\n"
        "s_waitcnt lgkmcnt(0)\n"
        "v_readfirstlane_b32 s40, v251\n"
        "s_nop 3\n"
        "s_cmp_ge_i32 s40, 6\n"
        "s_cbranch_scc1 L57_%=\n"
        "s_nop 0\n"
        "s_sub_u32 s41, s41, 1\n"
        "s_cbranch_scc0 L56_%=\n"
        "s_endpgm\n"
        "L57_%=:\n"
        "s_waitcnt vmcnt(0)\n"
        "s_setprio 0\n"
        "v_add_u32 v24, 2, v1\n"
        "v_mov_b32 v25, 0x3f80\n"
        "s_mov_b32 exec_lo, 0\n"
        "s_mov_b32 exec_hi, 0x1000000\n"
        "ds_write_b16 v24, v25\n"
        "s_mov_b64 exec, -1\n"
        "v_add_u32 v24, 61792, v0\n"
        "ds_read_b128 v[168:171], v24 offset:0\n"
        "ds_read_b128 v[172:175], v24 offset:1024\n"
        "ds_read_b128 v[176:179], v24 offset:2048\n"
        "ds_read_b128 v[180:183], v24 offset:3072\n"
        "ds_read_b128 v[184:187], v24 offset:4096\n"
        "ds_read_b128 v[188:191], v24 offset:5120\n"
        "ds_read_b128 v[192:195], v24 offset:6144\n"
        "ds_read_b128 v[196:199], v24 offset:7168\n"
        "ds_read_b128 v[200:203], v24 offset:8192\n"
        "ds_read_b128 v[204:207], v24 offset:9216\n"
        "ds_read_b128 v[208:211], v24 offset:10240\n"
        "ds_read_b128 v[212:215], v24 offset:11264\n"
        "ds_read_b128 v[216:219], v24 offset:12288\n"
        "ds_read_b128 v[220:223], v24 offset:13312\n"
        "ds_read_b128 v[224:227], v24 offset:14336\n"
        "ds_read_b128 v[228:231], v24 offset:15360\n"
        "ds_read_b128 v[232:235], v24 offset:16384\n"
        "ds_read_b128 v[236:239], v24 offset:17408\n"
        "ds_read_b128 v[240:243], v0 offset:0\n"
        "ds_read_b128 v[244:247], v0 offset:1024\n"
        "ds_read_b128 v[248:251], v0 offset:2048\n"
        "ds_read_b128 v[252:255], v0 offset:3088\n"
        "v_mov_b32 v25, 21600\n"
        "s_waitcnt lgkmcnt(0)\n"
        "s_mov_b64 exec, 1\n"
        "ds_inc_u32 v25, v1 offset:12\n"
        "s_mov_b64 exec, -1\n"
        "L58_%=:\n"
        "ds_read_b32 v24, v25 offset:0\n"
        "s_waitcnt lgkmcnt(0)\n"
        "v_readfirstlane_b32 s40, v24\n"
        "s_nop 3\n"
        "s_cmp_ge_i32 s40, 11\n"
        "s_cbranch_scc1 L59_%=\n"
        "s_sleep 1\n"
        "s_sub_u32 s41, s41, 1\n"
        "s_cbranch_scc0 L58_%=\n"
        "s_endpgm\n"
        "L59_%=:\n"
        "ds_read_b128 v[36:39], v7 offset:0\n"
        "ds_read_b128 v[28:31], v7 offset:3328\n"
        "ds_read_b128 v[40:43], v7 offset:64\n"
        "ds_read_b128 v[32:35], v7 offset:3392\n"
        "ds_read_b128 v[164:167], v7 offset:128\n"
        "ds_read_b128 v[136:139], v7 offset:3456\n"
        "s_waitcnt lgkmcnt(0)\n"
        "CFULL_%=:\n"
        "s_call_b64 s[64:65], CGRP_%=\n"
        "CTOP_%=:\n"
        "s_waitcnt lgkmcnt(5)\n"
        "v_mfma_f32_16x16x32_bf16 v[84:87], v[168:171], v[36:39], 0\n"
        "v_cvt_pk_bf16_f32 v146, v20, v160\n"
        "v_cvt_pk_bf16_f32 v147, v96, v128\n"
        "s_waitcnt lgkmcnt(3)\n"
        "v_mfma_f32_16x16x32_bf16 v[84:87], v[172:175], v[40:43], v[84:87]\n"
        "ds_read_b128 v[64:67], v16 offset:128\n"
        "v_cvt_pk_bf16_f32 v148, v92, v120\n"
        "s_waitcnt lgkmcnt(2)\n"
        "v_mfma_f32_16x16x32_bf16 v[84:87], v[176:179], v[164:167], v[84:87]\n"
        "ds_read_b128 v[44:47], v5\n"
        "v_cvt_pk_bf16_f32 v152, v21, v161\n"
        "v_mfma_f32_16x16x32_bf16 v[24:27], v[188:191], v[164:167], 0\n"
        "v_cvt_pk_bf16_f32 v153, v97, v129\n"
        "v_cvt_pk_bf16_f32 v154, v93, v121\n"
        "v_mfma_f32_16x16x32_bf16 v[24:27], v[184:187], v[40:43], v[24:27]\n"
        "v_cvt_pk_bf16_f32 v140, v22, v162\n"
        "v_cvt_pk_bf16_f32 v141, v98, v130\n"
        "v_mfma_f32_16x16x32_bf16 v[24:27], v[180:183], v[36:39], v[24:27]\n"
        "s_add_u32 s37, s36, 196224\n"
        "ds_read_b128 v[108:111], v7 offset:6656\n"
        "ds_read_b128 v[112:115], v7 offset:6720\n"
        "ds_read_b128 v[80:83], v7 offset:6784\n"
        "v_mfma_f32_16x16x32_bf16 v[68:71], v[192:195], v[36:39], 0\n"
        "v_cvt_pk_bf16_f32 v116, v84, v85\n"
        "v_cvt_pk_bf16_f32 v117, v86, v87\n"
        "v_mfma_f32_16x16x32_bf16 v[68:71], v[196:199], v[40:43], v[68:71]\n"
        "v_cvt_pk_bf16_f32 v118, v24, v25\n"
        "v_cvt_pk_bf16_f32 v119, v26, v27\n"
        "v_mfma_f32_16x16x32_bf16 v[68:71], v[200:203], v[164:167], v[68:71]\n"
        "buffer_store_dwordx3 v[146:148], v12, s[8:11], s37 offen offset:384 sc0 sc1 nt\n"
        "v_cvt_pk_bf16_f32 v142, v94, v122\n"
        "v_cvt_pk_bf16_f32 v160, v23, v163\n"
        "v_cvt_pk_bf16_f32 v161, v99, v131\n"
        "v_cvt_pk_bf16_f32 v162, v95, v123\n"
        "s_waitcnt lgkmcnt(3)\n"
        "v_mfma_f32_16x16x32_bf16 v[24:27], v[64:67], v[164:167], 0\n"
        "v_cvt_pk_bf16_f32 v146, v72, v132\n"
        "v_cvt_pk_bf16_f32 v120, v68, v69\n"
        "v_smfmac_f32_16x16x64_bf16 v[24:27], v[44:47], v[36:43], v4\n"
        "s_add_u32 s37, s36, 196224\n"
        "v_cvt_pk_bf16_f32 v121, v70, v71\n"
        "v_mfma_f32_16x16x32_bf16 v[68:71], v[64:67], v[136:139], 0\n"
        "v_cvt_pk_bf16_f32 v147, v156, v124\n"
        "v_cvt_pk_bf16_f32 v148, v100, v88\n"
        "v_smfmac_f32_16x16x64_bf16 v[68:71], v[44:47], v[28:35], v4\n"
        "s_waitcnt lgkmcnt(0)\n"
        "v_cvt_pk_bf16_f32 v96, v73, v133\n"
        "v_cvt_pk_bf16_f32 v97, v157, v125\n"
        "v_mfma_f32_16x16x32_bf16 v[48:51], v[64:67], v[80:83], 0\n"
        "v_cvt_pk_bf16_f32 v98, v101, v89\n"
        "v_cvt_pk_bf16_f32 v124, v74, v134\n"
        "v_smfmac_f32_16x16x64_bf16 v[48:51], v[44:47], v[108:115], v4\n"
        "buffer_store_dwordx3 v[152:154], v13, s[8:11], s37 offen offset:1152 sc0 sc1 nt\n"
        "v_cvt_pk_bf16_f32 v125, v158, v126\n"
        "v_cvt_pk_bf16_f32 v126, v102, v90\n"
        "v_cvt_pk_bf16_f32 v156, v75, v135\n"
        "v_cvt_pk_bf16_f32 v157, v159, v127\n"
        "v_cvt_pk_bf16_f32 v158, v103, v91\n"
        "v_mfma_f32_16x16x32_bf16 v[44:47], v[212:215], v[164:167], 0\n"
        "s_add_u32 s37, s36, 294336\n"
        "ds_read_b64 v[154:155], v11 offset:192\n"
        "v_mfma_f32_16x16x32_bf16 v[44:47], v[208:211], v[40:43], v[44:47]\n"
        "ds_read_b32 v85, v10\n"
        "v_mfma_f32_16x16x32_bf16 v[44:47], v[204:207], v[36:39], v[44:47]\n"
        "v_mfma_f32_16x16x32_bf16 v[20:23], v[216:219], v[36:39], 0\n"
        "s_setprio 2\n"
        "v_mfma_f32_16x16x32_bf16 v[20:23], v[220:223], v[40:43], v[20:23]\n"
        "v_mfma_f32_16x16x32_bf16 v[20:23], v[224:227], v[164:167], v[20:23]\n"
        "buffer_store_dwordx3 v[140:142], v12, s[8:11], s37 offen offset:576 sc0 sc1 nt\n"
        "s_add_u32 s37, s36, 392448\n"
        "s_waitcnt lgkmcnt(1)\n"
        "v_add_f32_dpp v155, v26, v27 quad_perm:[0,1,2,3] row_mask:0x1 bank_mask:0xf\n"
        "v_cvt_pk_bf16_f32 v122, v44, v45\n"
        "v_cvt_pk_bf16_f32 v123, v46, v47\n"
        "v_mfma_f32_16x16x32_bf16 v[44:47], v[236:239], v[164:167], 0\n"
        "v_add_f32_dpp v154, v24, v25 quad_perm:[0,1,2,3] row_mask:0x1 bank_mask:0xf\n"
        "v_cvt_pk_bf16_f32 v24, v20, v21\n"
        "v_mfma_f32_16x16x32_bf16 v[44:47], v[232:235], v[40:43], v[44:47]\n"
        "v_cvt_pk_bf16_f32 v25, v22, v23\n"
        "v_add_f32_dpp v155, v70, v71 quad_perm:[0,1,2,3] row_mask:0x2 bank_mask:0xf\n"
        "v_mfma_f32_16x16x32_bf16 v[44:47], v[228:231], v[36:39], v[44:47]\n"
        "buffer_store_dwordx3 v[160:162], v12, s[8:11], s37 offen sc0 sc1 nt\n"
        "v_add_f32_dpp v154, v68, v69 quad_perm:[0,1,2,3] row_mask:0x2 bank_mask:0xf\n"
        "v_add_f32_dpp v155, v50, v51 quad_perm:[0,1,2,3] row_mask:0x4 bank_mask:0xf\n"
        "v_mul_f32 v163, s52, v155\n"
        "v_add_f32_dpp v154, v48, v49 quad_perm:[0,1,2,3] row_mask:0x4 bank_mask:0xf\n"
        "s_waitcnt lgkmcnt(0)\n"
        "v_fma_f32 v163, -v163, v155, v85\n"
        "v_add_f32_e64 v163, s53, |v163|\n"
        "v_cvt_pk_bf16_f32 v27, v46, v47\n"
        "v_cvt_pk_bf16_f32 v26, v44, v45\n"
        "v_mfma_f32_16x16x32_bf16 v[72:75], v[36:39], v[116:119], 0\n"
        "v_rsq_f32 v85, v163\n"
        "v_mfma_f32_16x16x32_bf16 v[72:75], v[40:43], v[120:123], v[72:75]\n"
        "s_add_u32 s37, s36, 392448\n"
        "ds_read_b128 v[150:153], v8 offset:128\n"
        "v_and_b32 v104, s28, v85\n"
        "v_mfma_f32_16x16x32_bf16 v[72:75], v[164:167], v[24:27], v[72:75]\n"
        "ds_read2_b32 v[140:141], v6 offset0:0 offset1:2\n"
        "ds_read2_b32 v[142:143], v6 offset0:8 offset1:10\n"
        "v_mfma_f32_16x16x32_bf16 v[100:103], v[136:139], v[24:27], 0\n"
        "v_and_b32 v106, s28, v85\n"
        "v_sub_f32 v163, v85, v104\n"
        "v_mfma_f32_16x16x32_bf16 v[100:103], v[32:35], v[120:123], v[100:103]\n"
        "v_cvt_pk_bf16_f32 v105, v163, v104\n"
        "v_cvt_pk_bf16_f32 v107, v106, v163\n"
        "v_mfma_f32_16x16x32_bf16 v[100:103], v[28:31], v[116:119], v[100:103]\n"
        "buffer_store_dwordx3 v[146:148], v15, s[8:11], s37 offen offset:768 sc0 sc1 nt\n"
        "v_mul_f32 v154, v154, v85\n"
        "v_readlane_b32 s58, v85, 56\n"
        "s_mov_b64 exec, s[56:57]\n"
        "v_mov_b32 v154, 0xff800000\n"
        "s_mov_b64 exec, -1\n"
        "v_mfma_f32_16x16x4_4b_bf16 v[56:71], v[104:105], v[106:107], 0 blgp:4\n"
        "v_mul_f32 v154, s58, v154\n"
        "v_exp_f32 v154, v154\n"
        "v_mfma_f32_16x16x32_bf16 v[146:149], v[108:111], v[116:119], 0\n"
        "s_add_u32 s37, s36, 490560\n"
        "v_add_f32_dpp v163, v154, v154 row_ror:8 row_mask:0xf bank_mask:0xf\n"
        "v_mul_f32 v154, v154, v85\n"
        "v_mfma_f32_16x16x32_bf16 v[146:149], v[112:115], v[120:123], v[146:149]\n"
        "v_add_f32_dpp v163, v163, v163 row_ror:4 row_mask:0xf bank_mask:0xf\n"
        "v_mfma_f32_16x16x32_bf16 v[146:149], v[80:83], v[24:27], v[146:149]\n"
        "s_waitcnt lgkmcnt(0)\n"
        "v_add_f32_dpp v163, v163, v163 row_ror:2 row_mask:0xf bank_mask:0xf\n"
        "v_mfma_f32_16x16x32_bf16 v[52:55], v[150:153], v[24:27], 0\n"
        "v_mul_f32 v72, v72, v56\n"
        "v_mul_f32 v73, v73, v57\n"
        "v_smfmac_f32_16x16x64_bf16 v[52:55], v[140:143], v[116:123], v4\n"
        "buffer_store_dwordx3 v[96:98], v12, s[8:11], s37 offen offset:192 sc0 sc1 nt\n"
        "v_mul_f32 v74, v74, v58\n"
        "v_mul_f32 v75, v75, v59\n"
        "v_exp_f32 v72, v72\n"
        "v_mul_f32 v100, v100, v60\n"
        "v_mfma_f32_16x16x32_bf16 v[20:23], v[168:171], v[28:31], 0\n"
        "s_add_u32 s37, s36, 490560\n"
        "v_exp_f32 v73, v73\n"
        "v_mfma_f32_16x16x32_bf16 v[20:23], v[172:175], v[32:35], v[20:23]\n"
        "v_exp_f32 v74, v74\n"
        "v_mfma_f32_16x16x32_bf16 v[20:23], v[176:179], v[136:139], v[20:23]\n"
        "v_exp_f32 v75, v75\n"
        "v_add_f32 v144, v72, v73\n"
        "v_mfma_f32_16x16x32_bf16 v[24:27], v[188:191], v[136:139], 0\n"
        "v_add_f32 v144, v144, v74\n"
        "v_add_f32 v144, v144, v75\n"
        "v_mfma_f32_16x16x32_bf16 v[24:27], v[184:187], v[32:35], v[24:27]\n"
        "v_mul_f32 v101, v101, v61\n"
        "v_mul_f32 v102, v102, v62\n"
        "v_mfma_f32_16x16x32_bf16 v[24:27], v[180:183], v[28:31], v[24:27]\n"
        "buffer_store_dwordx3 v[124:126], v14, s[8:11], s37 offen offset:960 sc0 sc1 nt\n"
        "v_cvt_pk_bf16_f32 v56, v20, v21\n"
        "v_cvt_pk_bf16_f32 v57, v22, v23\n"
        "v_mul_f32 v103, v103, v63\n"
        "v_exp_f32 v100, v100\n"
        "v_mfma_f32_16x16x32_bf16 v[20:23], v[192:195], v[28:31], 0\n"
        "s_add_u32 s37, s36, 588672\n"
        "v_exp_f32 v101, v101\n"
        "v_mfma_f32_16x16x32_bf16 v[20:23], v[196:199], v[32:35], v[20:23]\n"
        "v_cvt_pk_bf16_f32 v58, v24, v25\n"
        "v_cvt_pk_bf16_f32 v59, v26, v27\n"
        "v_mfma_f32_16x16x32_bf16 v[20:23], v[200:203], v[136:139], v[20:23]\n"
        "buffer_store_dwordx3 v[156:158], v12, s[8:11], s37 offen offset:384 sc0 sc1 nt\n"
        "v_exp_f32 v102, v102\n"
        "v_exp_f32 v103, v103\n"
        "v_add_f32 v144, v144, v100\n"
        "v_mfma_f32_16x16x32_bf16 v[24:27], v[212:215], v[136:139], 0\n"
        "v_add_f32 v144, v144, v101\n"
        "v_add_f32 v144, v144, v102\n"
        "v_mfma_f32_16x16x32_bf16 v[24:27], v[208:211], v[32:35], v[24:27]\n"
        "v_cvt_pk_bf16_f32 v60, v20, v21\n"
        "v_cvt_pk_bf16_f32 v61, v22, v23\n"
        "v_mfma_f32_16x16x32_bf16 v[24:27], v[204:207], v[28:31], v[24:27]\n"
        "v_add_f32 v144, v144, v103\n"
        "v_mul_f32 v146, v146, v64\n"
        "v_mul_f32 v147, v147, v65\n"
        "v_mul_f32 v148, v148, v66\n"
        "v_mfma_f32_16x16x32_bf16 v[20:23], v[216:219], v[28:31], 0\n"
        "v_mul_f32 v149, v149, v67\n"
        "v_add_f32 v52, v52, v53\n"
        "v_mfma_f32_16x16x32_bf16 v[20:23], v[220:223], v[32:35], v[20:23]\n"
        "v_cvt_pk_bf16_f32 v62, v24, v25\n"
        "v_cvt_pk_bf16_f32 v63, v26, v27\n"
        "v_mfma_f32_16x16x32_bf16 v[20:23], v[224:227], v[136:139], v[20:23]\n"
        "v_exp_f32 v146, v146\n"
        "v_mul_f32 v52, v52, v68\n"
        "v_mfma_f32_16x16x32_bf16 v[24:27], v[236:239], v[136:139], 0\n"
        "v_exp_f32 v147, v147\n"
        "v_mfma_f32_16x16x32_bf16 v[24:27], v[232:235], v[32:35], v[24:27]\n"
        "v_exp_f32 v148, v148\n"
        "v_mfma_f32_16x16x32_bf16 v[24:27], v[228:231], v[28:31], v[24:27]\n"
        "v_exp_f32 v149, v149\n"
        "v_cvt_pk_bf16_f32 v64, v20, v21\n"
        "v_cvt_pk_bf16_f32 v65, v22, v23\n"
        "v_mfma_f32_16x16x4_4b_bf16 v[120:135], v[104:105], v[106:107], 0 blgp:5\n"
        "v_add_f32 v144, v144, v146\n"
        "v_add_f32 v144, v144, v147\n"
        "v_add_f32 v144, v144, v148\n"
        "v_exp_f32 v52, v52\n"
        "v_cvt_pk_bf16_f32 v66, v24, v25\n"
        "v_cvt_pk_bf16_f32 v67, v26, v27\n"
        "v_add_f32 v144, v144, v149\n"
        "v_add_f32_dpp v163, v163, v163 row_ror:1 row_mask:0xf bank_mask:0xf\n"
        "v_mfma_f32_16x16x32_bf16 v[116:119], v[36:39], v[56:59], 0\n"
        "v_add_f32_dpp v144, v52, v144 quad_perm:[0,1,2,3] row_mask:0x4 bank_mask:0xf\n"
        "v_mfma_f32_16x16x32_bf16 v[116:119], v[40:43], v[60:63], v[116:119]\n"
        "v_mfma_f32_16x16x32_bf16 v[116:119], v[164:167], v[64:67], v[116:119]\n"
        "v_permlane32_swap_b32 v144, v163\n"
        "v_add_f32 v144, v144, v163\n"
        "v_mov_b32 v159, v144\n"
        "v_mfma_f32_16x16x32_bf16 v[96:99], v[136:139], v[64:67], 0\n"
        "v_mfma_f32_16x16x32_bf16 v[96:99], v[32:35], v[60:63], v[96:99]\n"
        "v_permlane16_swap_b32 v144, v159\n"
        "v_mfma_f32_16x16x32_bf16 v[96:99], v[28:31], v[56:59], v[96:99]\n"
        "v_add_f32 v144, v144, v159\n"
        "v_rcp_f32 v158, v144\n"
        "v_mfma_f32_16x16x32_bf16 v[160:163], v[108:111], v[56:59], 0\n"
        "v_and_b32 v134, s28, v158\n"
        "v_sub_f32 v159, v158, v134\n"
        "v_mfma_f32_16x16x32_bf16 v[160:163], v[112:115], v[60:63], v[160:163]\n"
        "v_cvt_pk_bf16_f32 v135, v134, v159\n"
        "v_mul_f32 v116, v116, v120\n"
        "v_mfma_f32_16x16x32_bf16 v[160:163], v[80:83], v[64:67], v[160:163]\n"
        "v_mul_f32 v117, v117, v121\n"
        "v_mul_f32 v118, v118, v122\n"
        "v_mul_f32 v119, v119, v123\n"
        "v_mfma_f32_16x16x32_bf16 v[76:79], v[150:153], v[64:67], 0\n"
        "v_exp_f32 v116, v116\n"
        "v_smfmac_f32_16x16x64_bf16 v[76:79], v[140:143], v[56:63], v4\n"
        "v_exp_f32 v117, v117\n"
        "v_mfma_f32_16x16x32_bf16 v[64:67], v[168:171], v[108:111], 0\n"
        "v_exp_f32 v118, v118\n"
        "v_mfma_f32_16x16x32_bf16 v[64:67], v[172:175], v[112:115], v[64:67]\n"
        "v_exp_f32 v119, v119\n"
        "v_mfma_f32_16x16x32_bf16 v[64:67], v[176:179], v[80:83], v[64:67]\n"
        "v_add_f32 v145, v116, v117\n"
        "v_add_f32 v145, v145, v118\n"
        "v_add_f32 v145, v145, v119\n"
        "v_mfma_f32_16x16x32_bf16 v[20:23], v[188:191], v[80:83], 0\n"
        "v_mul_f32 v96, v96, v124\n"
        "v_mul_f32 v97, v97, v125\n"
        "v_mfma_f32_16x16x32_bf16 v[20:23], v[184:187], v[112:115], v[20:23]\n"
        "v_mul_f32 v98, v98, v126\n"
        "v_cvt_pk_bf16_f32 v44, v64, v65\n"
        "v_mfma_f32_16x16x32_bf16 v[20:23], v[180:183], v[108:111], v[20:23]\n"
        "v_cvt_pk_bf16_f32 v45, v66, v67\n"
        "v_mul_f32 v99, v99, v127\n"
        "v_exp_f32 v96, v96\n"
        "v_mfma_f32_16x16x32_bf16 v[24:27], v[192:195], v[108:111], 0\n"
        "v_exp_f32 v97, v97\n"
        "v_mfma_f32_16x16x32_bf16 v[24:27], v[196:199], v[112:115], v[24:27]\n"
        "v_exp_f32 v98, v98\n"
        "v_mfma_f32_16x16x32_bf16 v[24:27], v[200:203], v[80:83], v[24:27]\n"
        "v_cvt_pk_bf16_f32 v46, v20, v21\n"
        "v_cvt_pk_bf16_f32 v47, v22, v23\n"
        "v_add_f32 v145, v145, v96\n"
        "v_mfma_f32_16x16x32_bf16 v[20:23], v[212:215], v[80:83], 0\n"
        "v_exp_f32 v99, v99\n"
        "v_mfma_f32_16x16x32_bf16 v[20:23], v[208:211], v[112:115], v[20:23]\n"
        "v_add_f32 v145, v145, v97\n"
        "v_add_f32 v145, v145, v98\n"
        "v_mfma_f32_16x16x32_bf16 v[20:23], v[204:207], v[108:111], v[20:23]\n"
        "v_cvt_pk_bf16_f32 v48, v24, v25\n"
        "v_cvt_pk_bf16_f32 v49, v26, v27\n"
        "v_add_f32 v145, v145, v99\n"
        "v_mfma_f32_16x16x32_bf16 v[120:123], v[216:219], v[108:111], 0\n"
        "v_mul_f32 v160, v160, v128\n"
        "v_mul_f32 v161, v161, v129\n"
        "v_mfma_f32_16x16x32_bf16 v[120:123], v[220:223], v[112:115], v[120:123]\n"
        "v_mul_f32 v162, v162, v130\n"
        "v_cvt_pk_bf16_f32 v50, v20, v21\n"
        "v_mfma_f32_16x16x32_bf16 v[120:123], v[224:227], v[80:83], v[120:123]\n"
        "v_cvt_pk_bf16_f32 v51, v22, v23\n"
        "v_mfma_f32_16x16x4_4b_bf16 v[56:71], v[104:105], v[134:135], 0 blgp:4\n"
        "v_mul_f32 v163, v163, v131\n"
        "v_exp_f32 v160, v160\n"
        "v_exp_f32 v161, v161\n"
        "v_exp_f32 v162, v162\n"
        "v_exp_f32 v163, v163\n"
        "v_add_f32 v145, v145, v160\n"
        "v_cvt_pk_bf16_f32 v24, v120, v121\n"
        "v_mfma_f32_16x16x32_bf16 v[20:23], v[236:239], v[80:83], 0\n"
        "v_cvt_pk_bf16_f32 v25, v122, v123\n"
        "v_add_f32 v145, v145, v161\n"
        "v_mfma_f32_16x16x32_bf16 v[20:23], v[232:235], v[112:115], v[20:23]\n"
        "v_add_f32 v145, v145, v162\n"
        "v_mul_f32 v72, v72, v56\n"
        "v_mfma_f32_16x16x32_bf16 v[20:23], v[228:231], v[108:111], v[20:23]\n"
        "v_mul_f32 v73, v73, v57\n"
        "v_mul_f32 v74, v74, v58\n"
        "v_mul_f32 v75, v75, v59\n"
        "v_mul_f32 v100, v100, v60\n"
        "v_mul_f32 v101, v101, v61\n"
        "v_add_f32 v145, v145, v163\n"
        "v_add_f32 v76, v76, v77\n"
        "v_mul_f32 v76, v76, v132\n"
        "v_cvt_pk_bf16_f32 v27, v22, v23\n"
        "v_cvt_pk_bf16_f32 v26, v20, v21\n"
        "v_mfma_f32_16x16x32_bf16 v[120:123], v[36:39], v[44:47], 0\n"
        "v_exp_f32 v76, v76\n"
        "v_mfma_f32_16x16x32_bf16 v[120:123], v[40:43], v[48:51], v[120:123]\n"
        "v_mul_f32 v102, v102, v62\n"
        "v_add_f32_dpp v145, v76, v145 quad_perm:[0,1,2,3] row_mask:0x4 bank_mask:0xf\n"
        "v_mfma_f32_16x16x32_bf16 v[120:123], v[164:167], v[24:27], v[120:123]\n"
        "v_mul_f32 v103, v103, v63\n"
        "v_mul_f32 v146, v146, v64\n"
        "v_mul_f32 v147, v147, v65\n"
        "v_mfma_f32_16x16x32_bf16 v[88:91], v[136:139], v[24:27], 0\n"
        "v_mul_f32 v148, v148, v66\n"
        "v_mul_f32 v149, v149, v67\n"
        "v_mfma_f32_16x16x32_bf16 v[88:91], v[32:35], v[48:51], v[88:91]\n"
        "v_fma_f32 v52, v52, v68, v19\n"
        "v_cvt_pk_bf16_f32 v92, v72, v73\n"
        "v_mfma_f32_16x16x32_bf16 v[88:91], v[28:31], v[44:47], v[88:91]\n"
        "v_cvt_pk_bf16_f32 v93, v74, v75\n"
        "v_mfma_f32_16x16x4_4b_bf16 v[58:73], v[104:105], v[106:107], 0 blgp:6\n"
        "v_cvt_pk_bf16_f32 v94, v100, v101\n"
        "v_cvt_pk_bf16_f32 v95, v102, v103\n"
        "v_cvt_pk_bf16_f32 v100, v146, v147\n"
        "v_cvt_pk_bf16_f32 v102, v148, v149\n"
        "v_cvt_pk_bf16_f32 v101, v52, 0\n"
        "v_min3_f32 v77, v144, v158, v85\n"
        "v_readlane_b32 s59, v158, 32\n"
        "v_mfma_f32_16x16x32_bf16 v[156:159], v[108:111], v[44:47], 0\n"
        "v_mfma_f32_16x16x32_bf16 v[156:159], v[112:115], v[48:51], v[156:159]\n"
        "v_mul_f32 v154, s59, v154\n"
        "v_cvt_pk_bf16_f32 v154, v154, 0\n"
        "v_mfma_f32_16x16x32_bf16 v[156:159], v[80:83], v[24:27], v[156:159]\n"
        "v_mul_f32 v120, v120, v58\n"
        "v_mul_f32 v121, v121, v59\n"
        "v_mul_f32 v122, v122, v60\n"
        "v_mfma_f32_16x16x32_bf16 v[106:109], v[150:153], v[24:27], 0\n"
        "ds_read_b128 v[128:131], v17 offset:0\n"
        "v_mul_f32 v123, v123, v61\n"
        "v_smfmac_f32_16x16x64_bf16 v[106:109], v[140:143], v[44:51], v4\n"
        "ds_read_b128 v[24:27], v17 offset:64\n"
        "ds_read_b128 v[150:153], v17 offset:128\n"
        "v_mfma_f32_16x16x32_bf16 v[44:47], v[36:39], v[240:243], 0\n"
        "v_exp_f32 v120, v120\n"
        "v_mfma_f32_16x16x32_bf16 v[44:47], v[40:43], v[244:247], v[44:47]\n"
        "ds_read_b128 v[80:83], v0 offset:5136\n"
        "v_mul_f32 v88, v88, v62\n"
        "v_mfma_f32_16x16x32_bf16 v[44:47], v[164:167], v[248:251], v[44:47]\n"
        "ds_read_b128 v[112:115], v0 offset:4112\n"
        "v_mul_f32 v89, v89, v63\n"
        "v_mfma_f32_16x16x32_bf16 v[48:51], v[136:139], v[248:251], 0\n"
        "v_exp_f32 v121, v121\n"
        "v_mfma_f32_16x16x32_bf16 v[48:51], v[32:35], v[244:247], v[48:51]\n"
        "v_exp_f32 v122, v122\n"
        "v_mfma_f32_16x16x32_bf16 v[48:51], v[28:31], v[240:243], v[48:51]\n"
        "s_waitcnt lgkmcnt(2)\n"
        "v_cvt_pk_bf16_f32 v54, v44, v45\n"
        "v_cvt_pk_bf16_f32 v55, v46, v47\n"
        "v_add_f32 v103, v120, v121\n"
        "v_mfma_f32_16x16x32_bf16 v[60:63], v[128:131], v[240:243], 0\n"
        "v_exp_f32 v123, v123\n"
        "v_mfma_f32_16x16x32_bf16 v[60:63], v[24:27], v[244:247], v[60:63]\n"
        "v_add_f32 v103, v103, v122\n"
        "v_cvt_pk_bf16_f32 v56, v48, v49\n"
        "v_mfma_f32_16x16x32_bf16 v[60:63], v[150:153], v[248:251], v[60:63]\n"
        "s_waitcnt lgkmcnt(0)\n"
        "v_cvt_pk_bf16_f32 v57, v50, v51\n"
        "v_add_f32 v103, v103, v123\n"
        "v_mul_f32 v90, v90, v64\n"
        "v_mfma_f32_16x16x32_bf16 v[20:23], v[150:153], v[80:83], 0\n"
        "v_mul_f32 v91, v91, v65\n"
        "v_mul_f32 v156, v156, v66\n"
        "v_mfma_f32_16x16x32_bf16 v[20:23], v[24:27], v[112:115], v[20:23]\n"
        "ds_read_b128 v[140:143], v0 offset:6176\n"
        "v_cvt_pk_bf16_f32 v44, v60, v61\n"
        "v_mfma_f32_16x16x32_bf16 v[20:23], v[128:131], v[252:255], v[20:23]\n"
        "ds_read_b128 v[146:149], v0 offset:7200\n"
        "ds_read_b128 v[84:87], v0 offset:8224\n"
        "v_mfma_f32_16x16x32_bf16 v[108:111], v[28:31], v[252:255], 0\n"
        "v_cvt_pk_bf16_f32 v48, v62, v63\n"
        "v_mul_f32 v157, v157, v67\n"
        "v_mfma_f32_16x16x32_bf16 v[108:111], v[32:35], v[112:115], v[108:111]\n"
        "v_exp_f32 v88, v88\n"
        "v_mfma_f32_16x16x32_bf16 v[108:111], v[136:139], v[80:83], v[108:111]\n"
        "v_cvt_pk_bf16_f32 v62, v20, v21\n"
        "v_cvt_pk_bf16_f32 v66, v22, v23\n"
        "v_add_f32 v103, v103, v88\n"
        "v_mfma_f32_16x16x32_bf16 v[58:61], v[164:167], v[80:83], 0\n"
        "v_exp_f32 v89, v89\n"
        "v_mfma_f32_16x16x32_bf16 v[58:61], v[40:43], v[112:115], v[58:61]\n"
        "v_exp_f32 v90, v90\n"
        "v_mfma_f32_16x16x32_bf16 v[58:61], v[36:39], v[252:255], v[58:61]\n"
        "s_waitcnt lgkmcnt(0)\n"
        "v_cvt_pk_bf16_f32 v80, v108, v109\n"
        "v_cvt_pk_bf16_f32 v81, v110, v111\n"
        "v_add_f32 v103, v103, v89\n"
        "v_mfma_f32_16x16x32_bf16 v[112:115], v[36:39], v[140:143], 0\n"
        "v_exp_f32 v91, v91\n"
        "v_mfma_f32_16x16x32_bf16 v[112:115], v[40:43], v[146:149], v[112:115]\n"
        "ds_read_b128 v[132:135], v0 offset:11312\n"
        "v_cvt_pk_bf16_f32 v78, v58, v59\n"
        "v_mfma_f32_16x16x32_bf16 v[112:115], v[164:167], v[84:87], v[112:115]\n"
        "ds_read_b128 v[124:127], v0 offset:10288\n"
        "ds_read_b128 v[50:53], v0 offset:9264\n"
        "v_mfma_f32_16x16x32_bf16 v[72:75], v[136:139], v[84:87], 0\n"
        "v_cvt_pk_bf16_f32 v79, v60, v61\n"
        "v_add_f32 v103, v103, v90\n"
        "v_mfma_f32_16x16x32_bf16 v[72:75], v[32:35], v[146:149], v[72:75]\n"
        "v_add_f32 v103, v103, v91\n"
        "v_mul_f32 v158, v158, v68\n"
        "v_mfma_f32_16x16x32_bf16 v[72:75], v[28:31], v[140:143], v[72:75]\n"
        "v_cvt_pk_bf16_f32 v108, v112, v113\n"
        "v_cvt_pk_bf16_f32 v109, v114, v115\n"
        "v_mul_f32 v159, v159, v69\n"
        "v_mfma_f32_16x16x32_bf16 v[112:115], v[128:131], v[140:143], 0\n"
        "v_exp_f32 v156, v156\n"
        "v_mfma_f32_16x16x32_bf16 v[112:115], v[24:27], v[146:149], v[112:115]\n"
        "v_exp_f32 v157, v157\n"
        "v_mfma_f32_16x16x32_bf16 v[112:115], v[150:153], v[84:87], v[112:115]\n"
        "s_waitcnt lgkmcnt(0)\n"
        "v_cvt_pk_bf16_f32 v110, v72, v73\n"
        "v_cvt_pk_bf16_f32 v111, v74, v75\n"
        "v_exp_f32 v158, v158\n"
        "v_mfma_f32_16x16x32_bf16 v[58:61], v[150:153], v[132:135], 0\n"
        "v_exp_f32 v159, v159\n"
        "v_mfma_f32_16x16x32_bf16 v[58:61], v[24:27], v[124:127], v[58:61]\n"
        "ds_read_b128 v[84:87], v0 offset:12352\n"
        "v_cvt_pk_bf16_f32 v144, v112, v113\n"
        "v_mfma_f32_16x16x32_bf16 v[58:61], v[128:131], v[50:53], v[58:61]\n"
        "ds_read_b128 v[140:143], v0 offset:13376\n"
        "ds_read_b128 v[20:23], v0 offset:14400\n"
        "v_mfma_f32_16x16x32_bf16 v[72:75], v[28:31], v[50:53], 0\n"
        "v_cvt_pk_bf16_f32 v148, v114, v115\n"
        "v_add_f32 v103, v103, v156\n"
        "v_mfma_f32_16x16x32_bf16 v[72:75], v[32:35], v[124:127], v[72:75]\n"
        "v_add_f32 v103, v103, v157\n"
        "v_add_f32 v103, v103, v158\n"
        "v_mfma_f32_16x16x32_bf16 v[72:75], v[136:139], v[132:135], v[72:75]\n"
        "v_add_f32 v103, v103, v159\n"
        "v_add_f32 v106, v106, v107\n"
        "v_mul_f32 v106, v106, v70\n"
        "v_cvt_pk_bf16_f32 v45, v58, v59\n"
        "v_mfma_f32_16x16x32_bf16 v[112:115], v[164:167], v[132:135], 0\n"
        "v_cvt_pk_bf16_f32 v49, v60, v61\n"
        "v_mfma_f32_16x16x32_bf16 v[112:115], v[40:43], v[124:127], v[112:115]\n"
        "v_exp_f32 v106, v106\n"
        "v_mfma_f32_16x16x32_bf16 v[112:115], v[36:39], v[50:53], v[112:115]\n"
        "s_waitcnt lgkmcnt(0)\n"
        "v_cvt_pk_bf16_f32 v60, v72, v73\n"
        "v_cvt_pk_bf16_f32 v61, v74, v75\n"
        "v_add_f32_dpp v103, v106, v103 quad_perm:[0,1,2,3] row_mask:0x4 bank_mask:0xf\n"
        "v_mfma_f32_16x16x32_bf16 v[50:53], v[36:39], v[84:87], 0\n"
        "v_mfma_f32_16x16x32_bf16 v[50:53], v[40:43], v[140:143], v[50:53]\n"
        "ds_read_b128 v[124:127], v0 offset:17488\n"
        "v_mfma_f32_16x16x32_bf16 v[50:53], v[164:167], v[20:23], v[50:53]\n"
        "ds_read_b128 v[132:135], v0 offset:16464\n"
        "ds_read_b128 v[72:75], v0 offset:15440\n"
        "v_mfma_f32_16x16x32_bf16 v[68:71], v[136:139], v[20:23], 0\n"
        "v_cvt_pk_bf16_f32 v58, v112, v113\n"
        "v_cvt_pk_bf16_f32 v59, v114, v115\n"
        "v_mfma_f32_16x16x32_bf16 v[68:71], v[32:35], v[140:143], v[68:71]\n"
        "v_permlane32_swap_b32 v145, v103\n"
        "v_mfma_f32_16x16x32_bf16 v[68:71], v[28:31], v[84:87], v[68:71]\n"
        "v_add_f32 v145, v145, v103\n"
        "v_mov_b32 v147, v145\n"
        "v_cvt_pk_bf16_f32 v82, v50, v51\n"
        "v_mfma_f32_16x16x32_bf16 v[112:115], v[128:131], v[84:87], 0\n"
        "ds_read2_b32 v[50:51], v11 offset0:0 offset1:3\n"
        "v_cvt_pk_bf16_f32 v83, v52, v53\n"
        "v_mfma_f32_16x16x32_bf16 v[112:115], v[24:27], v[140:143], v[112:115]\n"
        "ds_read_b64_tr_b16 v[46:47], v9 offset:0\n"
        "v_cvt_pk_bf16_f32 v84, v68, v69\n"
        "v_mfma_f32_16x16x32_bf16 v[112:115], v[150:153], v[20:23], v[112:115]\n"
        "s_waitcnt lgkmcnt(2)\n"
        "ds_read2_b32 v[68:69], v11 offset0:1 offset1:4\n"
        "v_permlane16_swap_b32 v145, v147\n"
        "v_add_f32 v145, v145, v147\n"
        "v_mfma_f32_16x16x32_bf16 v[140:143], v[150:153], v[124:127], 0\n"
        "ds_read_b64_tr_b16 v[64:65], v9 offset:16\n"
        "v_cvt_pk_bf16_f32 v85, v70, v71\n"
        "v_mfma_f32_16x16x32_bf16 v[140:143], v[24:27], v[132:135], v[140:143]\n"
        "ds_read2_b32 v[150:151], v11 offset0:2 offset1:5\n"
        "v_cvt_pk_bf16_f32 v63, v112, v113\n"
        "v_mfma_f32_16x16x32_bf16 v[140:143], v[128:131], v[72:75], v[140:143]\n"
        "ds_read_b64_tr_b16 v[146:147], v9 offset:32\n"
        "v_rcp_f32 v149, v145\n"
        "v_cvt_pk_bf16_f32 v67, v114, v115\n"
        "v_mfma_f32_16x16x32_bf16 v[128:131], v[28:31], v[72:75], 0\n"
        "v_and_b32 v152, s28, v149\n"
        "v_sub_f32 v155, v149, v152\n"
        "v_mfma_f32_16x16x32_bf16 v[128:131], v[32:35], v[132:135], v[128:131]\n"
        "v_cvt_pk_bf16_f32 v153, v152, v155\n"
        "v_mov_b32_dpp v103, v19 row_shr:4 row_mask:0xf bank_mask:0xf bound_ctrl:0\n"
        "v_mfma_f32_16x16x32_bf16 v[128:131], v[136:139], v[124:127], v[128:131]\n"
        "v_min3_f32 v77, v77, v145, v149\n"
        "v_mfma_f32_16x16x4_4b_bf16 v[20:35], v[104:105], v[152:153], 0 blgp:4\n"
        "v_cmp_nle_f32 vcc, 0x2b800000, v77\n"
        "ds_write_b16 v1, v154\n"
        "v_cvt_pk_bf16_f32 v145, v140, v141\n"
        "v_cvt_pk_bf16_f32 v149, v142, v143\n"
        "s_andn2_b64 vcc, vcc, s[56:57]\n"
        "s_cbranch_scc1 CREDOA_%=\n"
        "v_cvt_pk_bf16_f32 v114, v128, v129\n"
        "v_cvt_pk_bf16_f32 v115, v130, v131\n"
        "v_mfma_f32_16x16x32_bf16 v[136:139], v[164:167], v[124:127], 0\n"
        "s_mov_b32 exec_hi, 0xffff\n"
        "v_add_u32 v10, 40064, v10\n"
        "s_mov_b32 exec_hi, -1\n"
        "v_mfma_f32_16x16x32_bf16 v[136:139], v[40:43], v[132:135], v[136:139]\n"
        "s_add_u32 s31, s31, 1\n"
        "v_mul_f32 v116, v116, v20\n"
        "v_mul_f32 v117, v117, v21\n"
        "v_mfma_f32_16x16x32_bf16 v[136:139], v[36:39], v[72:75], v[136:139]\n"
        "s_mul_i32 s38, s31, 1344\n"
        "v_mul_f32 v118, v118, v22\n"
        "v_mul_f32 v119, v119, v23\n"
        "v_mfma_f32_16x16x32_bf16 v[124:127], v[92:95], v[54:57], 0\n"
        "s_waitcnt lgkmcnt(1)\n"
        "v_mul_f32 v96, v96, v24\n"
        "v_mul_f32 v97, v97, v25\n"
        "v_smfmac_f32_16x16x64_bf16 v[124:127], v[100:103], v[44:51], v18\n"
        "s_cmp_ge_u32 s31, s76\n"
        "s_cbranch_scc1 L34A_%=\n"
        "L35A_%=:\n"
        "ds_read_b64_tr_b16 v[46:47], v9 offset:48\n"
        "v_mul_f32 v98, v98, v26\n"
        "v_mfma_f32_16x16x32_bf16 v[164:167], v[92:95], v[78:81], 0\n"
        "s_cmp_ge_u32 s31, s71\n"
        "v_mul_f32 v99, v99, v27\n"
        "v_mul_f32 v160, v160, v28\n"
        "v_smfmac_f32_16x16x64_bf16 v[164:167], v[100:103], v[62:69], v18\n"
        "s_cselect_b32 s39, s70, s72\n"
        "ds_read_b64_tr_b16 v[64:65], v9 offset:64\n"
        "v_mfma_f32_16x16x32_bf16 v[36:39], v[92:95], v[108:111], 0\n"
        "s_cmp_ge_u32 s31, 20\n"
        "ds_read_b128 v[40:43], v2\n"
        "v_cvt_pk_bf16_f32 v112, v136, v137\n"
        "v_smfmac_f32_16x16x64_bf16 v[36:39], v[100:103], v[144:151], v18\n"
        "s_cselect_b32 s39, s22, s39\n"
        "ds_read_b64_tr_b16 v[146:147], v9 offset:80\n"
        "ds_read_b128 v[20:23], v3\n"
        "v_cvt_pk_bf16_f32 v113, v138, v139\n"
        "v_mfma_f32_16x16x32_bf16 v[72:75], v[92:95], v[58:61], 0\n"
        "s_cmp_ge_u32 s31, 21\n"
        "v_mul_f32 v161, v161, v29\n"
        "s_waitcnt lgkmcnt(4)\n"
        "v_smfmac_f32_16x16x64_bf16 v[72:75], v[100:103], v[44:51], v18 abid:1\n"
        "s_cselect_b32 s39, s23, s39\n"
        "v_mul_f32 v162, v162, v30\n"
        "v_mfma_f32_16x16x4_4b_bf16 v[128:143], v[104:105], v[152:153], 0 blgp:6\n"
        "v_mul_f32 v163, v163, v31\n"
        "v_fma_f32 v76, v76, v32, v19\n"
        "v_cvt_pk_bf16_f32 v32, v116, v117\n"
        "v_cvt_pk_bf16_f32 v33, v118, v119\n"
        "v_cvt_pk_bf16_f32 v34, v96, v97\n"
        "v_cvt_pk_bf16_f32 v35, v98, v99\n"
        "v_cvt_pk_bf16_f32 v116, v160, v161\n"
        "v_cvt_pk_bf16_f32 v118, v162, v163\n"
        "v_cvt_pk_bf16_f32 v117, v76, 0\n"
        "v_cvt_pk_bf16_f32 v160, v124, v164\n"
        "v_cvt_pk_bf16_f32 v161, v36, v72\n"
        "v_mfma_f32_16x16x32_bf16 v[152:155], v[92:95], v[82:85], 0\n"
        "s_waitcnt lgkmcnt(0)\n"
        "v_mul_f32 v120, v120, v128\n"
        "v_mul_f32 v121, v121, v129\n"
        "v_smfmac_f32_16x16x64_bf16 v[152:155], v[100:103], v[62:69], v18 abid:1\n"
        "s_add_u32 s39, s39, s38\n"
        "s_mul_i32 s68, s31, 3\n"
        "v_mul_f32 v122, v122, v130\n"
        "v_mul_f32 v123, v123, v131\n"
        "v_mul_f32 v88, v88, v132\n"
        "v_mov_b64 v[96:97], 0\n"
        "v_mfma_f32_16x16x32_bf16 v[28:31], v[92:95], v[112:115], 0\n"
        "s_lshr_b32 s38, s31, 3\n"
        "v_mov_b64 v[98:99], 0\n"
        "v_mov_b64 v[92:93], 0\n"
        "v_smfmac_f32_16x16x64_bf16 v[28:31], v[100:103], v[144:151], v18 abid:1\n"
        "s_add_u32 s38, s38, 11\n"
        "v_mov_b64 v[94:95], 0\n"
        "v_smfmac_f32_16x16x64_bf16 v[96:99], v[40:43], v[54:61], v4\n"
        "s_bitcmp1_b32 s31, 0\n"
        "v_mov_b64 v[24:25], 0\n"
        "v_smfmac_f32_16x16x64_bf16 v[96:99], v[20:23], v[44:51], v4 abid:1\n"
        "s_cselect_b32 s38, 3, s38\n"
        "s_add_u32 s37, s35, 0\n"
        "ds_read_b64_tr_b16 v[46:47], v9 offset:3328\n"
        "v_mov_b64 v[26:27], 0\n"
        "v_smfmac_f32_16x16x64_bf16 v[92:95], v[40:43], v[78:85], v4\n"
        "s_add_u32 s68, s68, s38\n"
        "v_cvt_pk_bf16_f32 v162, v152, v28\n"
        "v_smfmac_f32_16x16x64_bf16 v[92:95], v[20:23], v[62:69], v4 abid:1\n"
        "s_cmp_lt_u32 s31, s14\n"
        "ds_read_b64_tr_b16 v[64:65], v9 offset:3344\n"
        "v_mov_b32_dpp v119, v19 row_shr:4 row_mask:0xf bank_mask:0xf bound_ctrl:0\n"
        "v_mul_f32 v89, v89, v133\n"
        "v_cvt_pk_bf16_f32 v124, v125, v165\n"
        "v_smfmac_f32_16x16x64_bf16 v[24:27], v[40:43], v[108:115], v4\n"
        "s_cselect_b32 s68, s68, 0\n"
        "v_cvt_pk_bf16_f32 v100, v126, v166\n"
        "v_smfmac_f32_16x16x64_bf16 v[24:27], v[20:23], v[144:151], v4 abid:1\n"
        "buffer_store_dwordx3 v[160:162], v12, s[8:11], s37 offen sc0 sc1\n"
        "ds_read_b64_tr_b16 v[146:147], v9 offset:3360\n"
        "v_cvt_pk_bf16_f32 v164, v127, v167\n"
        "v_cvt_pk_bf16_f32 v125, v37, v73\n"
        "v_cvt_pk_bf16_f32 v101, v38, v74\n"
        "v_cvt_pk_bf16_f32 v165, v39, v75\n"
        "v_cvt_pk_bf16_f32 v126, v153, v29\n"
        "v_mfma_f32_16x16x32_bf16 v[20:23], v[32:35], v[54:57], 0\n"
        "s_waitcnt lgkmcnt(2)\n"
        "v_cvt_pk_bf16_f32 v102, v154, v30\n"
        "v_cvt_pk_bf16_f32 v166, v155, v31\n"
        "v_smfmac_f32_16x16x64_bf16 v[20:23], v[116:119], v[44:51], v18\n"
        "s_and_b32 s38, s31, 7\n"
        "s_cselect_b32 s30, 1, -7\n"
        "ds_read_b64_tr_b16 v[46:47], v9 offset:3376\n"
        "v_add_f32 v96, v96, v97\n"
        "v_add_f32 v98, v98, v99\n"
        "v_add_f32 v92, v92, v93\n"
        "v_mfma_f32_16x16x32_bf16 v[160:163], v[32:35], v[78:81], 0\n"
        "s_waitcnt lgkmcnt(2)\n"
        "v_add_f32 v94, v94, v95\n"
        "v_cvt_pk_bf16_f32 v152, v96, v92\n"
        "v_smfmac_f32_16x16x64_bf16 v[160:163], v[116:119], v[62:69], v18\n"
        "s_add_u32 s37, s35, 0\n"
        "s_mul_i32 s29, s30, 192\n"
        "ds_read_b64_tr_b16 v[64:65], v9 offset:3392\n"
        "v_add_f32 v24, v24, v25\n"
        "v_add_f32 v26, v26, v27\n"
        "v_cvt_pk_bf16_f32 v153, v24, v98\n"
        "v_mfma_f32_16x16x32_bf16 v[96:99], v[32:35], v[108:111], 0\n"
        "s_waitcnt lgkmcnt(2)\n"
        "v_cvt_pk_bf16_f32 v154, v94, v26\n"
        "v_mul_f32 v90, v90, v134\n"
        "v_smfmac_f32_16x16x64_bf16 v[96:99], v[116:119], v[144:151], v18\n"
        "buffer_store_dwordx3 v[124:126], v15, s[8:11], s37 offen offset:768 sc0 sc1\n"
        "ds_read_b64_tr_b16 v[146:147], v9 offset:3408\n"
        "s_mul_i32 s30, s30, 392\n"
        "v_mul_f32 v91, v91, v135\n"
        "v_mul_f32 v156, v156, v136\n"
        "v_mul_f32 v157, v157, v137\n"
        "v_mul_f32 v158, v158, v138\n"
        "v_mfma_f32_16x16x32_bf16 v[128:131], v[32:35], v[58:61], 0\n"
        "s_waitcnt lgkmcnt(2)\n"
        "s_cmp_eq_u32 s73, s18\n"
        "v_mul_f32 v159, v159, v139\n"
        "v_smfmac_f32_16x16x64_bf16 v[128:131], v[116:119], v[44:51], v18 abid:1\n"
        "s_cselect_b32 s34, -7488, s75\n"
        "s_add_u32 s37, s35, 98112\n"
        "ds_read_b64_tr_b16 v[46:47], v9 offset:6656\n"
        "v_mfma_f32_16x16x32_bf16 v[92:95], v[32:35], v[82:85], 0\n"
        "s_waitcnt lgkmcnt(2)\n"
        "v_fma_f32 v106, v106, v140, v19\n"
        "v_cvt_pk_bf16_f32 v24, v120, v121\n"
        "v_smfmac_f32_16x16x64_bf16 v[92:95], v[116:119], v[62:69], v18 abid:1\n"
        "s_add_u32 s73, s73, s34\n"
        "ds_read_b64_tr_b16 v[64:65], v9 offset:6672\n"
        "v_cvt_pk_bf16_f32 v25, v122, v123\n"
        "v_cvt_pk_bf16_f32 v26, v88, v89\n"
        "v_mfma_f32_16x16x32_bf16 v[120:123], v[32:35], v[112:115], 0\n"
        "s_waitcnt lgkmcnt(2)\n"
        "v_cvt_pk_bf16_f32 v27, v90, v91\n"
        "v_cvt_pk_bf16_f32 v140, v156, v157\n"
        "v_smfmac_f32_16x16x64_bf16 v[120:123], v[116:119], v[144:151], v18 abid:1\n"
        "buffer_store_dwordx3 v[100:102], v12, s[8:11], s37 offen offset:192 sc0 sc1 nt\n"
        "ds_read_b64_tr_b16 v[146:147], v9 offset:6688\n"
        "ds_read_b32 v167, v0 offset:21604\n"
        "v_cvt_pk_bf16_f32 v142, v158, v159\n"
        "v_cvt_pk_bf16_f32 v141, v106, 0\n"
        "v_mov_b32_dpp v143, v19 row_shr:4 row_mask:0xf bank_mask:0xf bound_ctrl:0\n"
        "v_mfma_f32_16x16x32_bf16 v[72:75], v[24:27], v[54:57], 0\n"
        "s_waitcnt lgkmcnt(3)\n"
        "v_writelane_b32 v10, s73, 56\n"
        "v_add_u32 v11, s30, v11\n"
        "v_smfmac_f32_16x16x64_bf16 v[72:75], v[140:143], v[44:51], v18\n"
        "s_add_u32 s37, s35, 98112\n"
        "ds_read_b64_tr_b16 v[46:47], v9 offset:6704\n"
        "v_mfma_f32_16x16x32_bf16 v[132:135], v[24:27], v[78:81], 0\n"
        "s_waitcnt lgkmcnt(3)\n"
        "v_smfmac_f32_16x16x64_bf16 v[132:135], v[140:143], v[62:69], v18\n"
        "ds_read_b64_tr_b16 v[64:65], v9 offset:6720\n"
        "v_mfma_f32_16x16x32_bf16 v[156:159], v[24:27], v[108:111], 0\n"
        "s_waitcnt lgkmcnt(3)\n"
        "s_waitcnt lgkmcnt(2)\n"
        "v_readfirstlane_b32 s40, v167\n"
        "v_smfmac_f32_16x16x64_bf16 v[156:159], v[140:143], v[144:151], v18\n"
        "buffer_store_dwordx3 v[164:166], v14, s[8:11], s37 offen offset:960 sc0 sc1 nt\n"
        "ds_read_b64_tr_b16 v[146:147], v9 offset:6736\n"
        "s_add_u32 s37, s35, 588672\n"
        "s_add_u32 s46, s37, 1344\n"
        "s_cmp_lt_i32 s40, s68\n"
        "s_mov_b64 exec, 1\n"
        "ds_inc_u32 v0, v1 offset:21608\n"
        "s_mov_b64 exec, -1\n"
        "s_cbranch_scc1 L38A_%=\n"
        "L39A_%=:\n"
        "ds_read_b128 v[36:39], v7 offset:40064\n"
        "v_mfma_f32_16x16x32_bf16 v[124:127], v[24:27], v[58:61], 0\n"
        "s_waitcnt lgkmcnt(4)\n"
        "ds_read_b128 v[28:31], v7 offset:43392\n"
        "v_smfmac_f32_16x16x64_bf16 v[124:127], v[140:143], v[44:51], v18 abid:1\n"
        "buffer_store_dwordx3 v[152:154], v12, s[44:47], s37 offen offset:1152 sc0 sc1 nt\n"
        "ds_read_b128 v[40:43], v7 offset:40128\n"
        "ds_read_b128 v[32:35], v7 offset:43456\n"
        "ds_read_b128 v[164:167], v7 offset:40192\n"
        "ds_read_b128 v[136:139], v7 offset:43520\n"
        "v_mfma_f32_16x16x32_bf16 v[100:103], v[24:27], v[82:85], 0\n"
        "s_waitcnt lgkmcnt(7)\n"
        "s_mov_b32 s36, s35\n"
        "v_smfmac_f32_16x16x64_bf16 v[100:103], v[140:143], v[62:69], v18 abid:1\n"
        "s_mov_b64 exec, s[62:63]\n"
        "v_add_u32 v16, s29, v16\n"
        "v_add_u32 v8, s34, v8\n"
        "s_mov_b64 exec, -1\n"
        "v_mfma_f32_16x16x32_bf16 v[88:91], v[24:27], v[112:115], 0\n"
        "s_mov_b32 s35, s39\n"
        "v_smfmac_f32_16x16x64_bf16 v[88:91], v[140:143], v[144:151], v18 abid:1\n"
        "s_mov_b64 exec, s[50:51]\n"
        "v_add_u32 v5, s29, v5\n"
        "v_add_u32 v6, s34, v6\n"
        "s_mov_b64 exec, -1\n"
        "s_setprio 0\n"
        "CNEXTA_%=:\n"
        "s_cmp_ge_u32 s31, s14\n"
        "s_cbranch_scc1 CEND_%=\n"
        "s_waitcnt lgkmcnt(5)\n"
        "v_mfma_f32_16x16x32_bf16 v[84:87], v[168:171], v[36:39], 0\n"
        "v_cvt_pk_bf16_f32 v146, v20, v160\n"
        "v_cvt_pk_bf16_f32 v147, v96, v128\n"
        "s_waitcnt lgkmcnt(3)\n"
        "v_mfma_f32_16x16x32_bf16 v[84:87], v[172:175], v[40:43], v[84:87]\n"
        "ds_read_b128 v[64:67], v16 offset:128\n"
        "v_cvt_pk_bf16_f32 v148, v92, v120\n"
        "s_waitcnt lgkmcnt(2)\n"
        "v_mfma_f32_16x16x32_bf16 v[84:87], v[176:179], v[164:167], v[84:87]\n"
        "ds_read_b128 v[44:47], v5\n"
        "v_cvt_pk_bf16_f32 v152, v21, v161\n"
        "v_mfma_f32_16x16x32_bf16 v[24:27], v[188:191], v[164:167], 0\n"
        "v_cvt_pk_bf16_f32 v153, v97, v129\n"
        "v_cvt_pk_bf16_f32 v154, v93, v121\n"
        "v_mfma_f32_16x16x32_bf16 v[24:27], v[184:187], v[40:43], v[24:27]\n"
        "v_cvt_pk_bf16_f32 v140, v22, v162\n"
        "v_cvt_pk_bf16_f32 v141, v98, v130\n"
        "v_mfma_f32_16x16x32_bf16 v[24:27], v[180:183], v[36:39], v[24:27]\n"
        "s_add_u32 s37, s36, 196224\n"
        "ds_read_b128 v[108:111], v7 offset:46720\n"
        "ds_read_b128 v[112:115], v7 offset:46784\n"
        "ds_read_b128 v[80:83], v7 offset:46848\n"
        "v_mfma_f32_16x16x32_bf16 v[68:71], v[192:195], v[36:39], 0\n"
        "v_cvt_pk_bf16_f32 v116, v84, v85\n"
        "v_cvt_pk_bf16_f32 v117, v86, v87\n"
        "v_mfma_f32_16x16x32_bf16 v[68:71], v[196:199], v[40:43], v[68:71]\n"
        "v_cvt_pk_bf16_f32 v118, v24, v25\n"
        "v_cvt_pk_bf16_f32 v119, v26, v27\n"
        "v_mfma_f32_16x16x32_bf16 v[68:71], v[200:203], v[164:167], v[68:71]\n"
        "buffer_store_dwordx3 v[146:148], v12, s[8:11], s37 offen offset:384 sc0 sc1 nt\n"
        "v_cvt_pk_bf16_f32 v142, v94, v122\n"
        "v_cvt_pk_bf16_f32 v160, v23, v163\n"
        "v_cvt_pk_bf16_f32 v161, v99, v131\n"
        "v_cvt_pk_bf16_f32 v162, v95, v123\n"
        "s_waitcnt lgkmcnt(3)\n"
        "v_mfma_f32_16x16x32_bf16 v[24:27], v[64:67], v[164:167], 0\n"
        "v_cvt_pk_bf16_f32 v146, v72, v132\n"
        "v_cvt_pk_bf16_f32 v120, v68, v69\n"
        "v_smfmac_f32_16x16x64_bf16 v[24:27], v[44:47], v[36:43], v4\n"
        "s_add_u32 s37, s36, 196224\n"
        "v_cvt_pk_bf16_f32 v121, v70, v71\n"
        "v_mfma_f32_16x16x32_bf16 v[68:71], v[64:67], v[136:139], 0\n"
        "v_cvt_pk_bf16_f32 v147, v156, v124\n"
        "v_cvt_pk_bf16_f32 v148, v100, v88\n"
        "v_smfmac_f32_16x16x64_bf16 v[68:71], v[44:47], v[28:35], v4\n"
        "s_waitcnt lgkmcnt(0)\n"
        "v_cvt_pk_bf16_f32 v96, v73, v133\n"
        "v_cvt_pk_bf16_f32 v97, v157, v125\n"
        "v_mfma_f32_16x16x32_bf16 v[48:51], v[64:67], v[80:83], 0\n"
        "v_cvt_pk_bf16_f32 v98, v101, v89\n"
        "v_cvt_pk_bf16_f32 v124, v74, v134\n"
        "v_smfmac_f32_16x16x64_bf16 v[48:51], v[44:47], v[108:115], v4\n"
        "buffer_store_dwordx3 v[152:154], v13, s[8:11], s37 offen offset:1152 sc0 sc1 nt\n"
        "v_cvt_pk_bf16_f32 v125, v158, v126\n"
        "v_cvt_pk_bf16_f32 v126, v102, v90\n"
        "v_cvt_pk_bf16_f32 v156, v75, v135\n"
        "v_cvt_pk_bf16_f32 v157, v159, v127\n"
        "v_cvt_pk_bf16_f32 v158, v103, v91\n"
        "v_mfma_f32_16x16x32_bf16 v[44:47], v[212:215], v[164:167], 0\n"
        "s_add_u32 s37, s36, 294336\n"
        "ds_read_b64 v[154:155], v11 offset:192\n"
        "v_mfma_f32_16x16x32_bf16 v[44:47], v[208:211], v[40:43], v[44:47]\n"
        "ds_read_b32 v85, v10\n"
        "v_mfma_f32_16x16x32_bf16 v[44:47], v[204:207], v[36:39], v[44:47]\n"
        "v_mfma_f32_16x16x32_bf16 v[20:23], v[216:219], v[36:39], 0\n"
        "s_setprio 2\n"
        "v_mfma_f32_16x16x32_bf16 v[20:23], v[220:223], v[40:43], v[20:23]\n"
        "v_mfma_f32_16x16x32_bf16 v[20:23], v[224:227], v[164:167], v[20:23]\n"
        "buffer_store_dwordx3 v[140:142], v12, s[8:11], s37 offen offset:576 sc0 sc1 nt\n"
        "s_add_u32 s37, s36, 392448\n"
        "s_waitcnt lgkmcnt(1)\n"
        "v_add_f32_dpp v155, v26, v27 quad_perm:[0,1,2,3] row_mask:0x1 bank_mask:0xf\n"
        "v_cvt_pk_bf16_f32 v122, v44, v45\n"
        "v_cvt_pk_bf16_f32 v123, v46, v47\n"
        "v_mfma_f32_16x16x32_bf16 v[44:47], v[236:239], v[164:167], 0\n"
        "v_add_f32_dpp v154, v24, v25 quad_perm:[0,1,2,3] row_mask:0x1 bank_mask:0xf\n"
        "v_cvt_pk_bf16_f32 v24, v20, v21\n"
        "v_mfma_f32_16x16x32_bf16 v[44:47], v[232:235], v[40:43], v[44:47]\n"
        "v_cvt_pk_bf16_f32 v25, v22, v23\n"
        "v_add_f32_dpp v155, v70, v71 quad_perm:[0,1,2,3] row_mask:0x2 bank_mask:0xf\n"
        "v_mfma_f32_16x16x32_bf16 v[44:47], v[228:231], v[36:39], v[44:47]\n"
        "buffer_store_dwordx3 v[160:162], v12, s[8:11], s37 offen sc0 sc1 nt\n"
        "v_add_f32_dpp v154, v68, v69 quad_perm:[0,1,2,3] row_mask:0x2 bank_mask:0xf\n"
        "v_add_f32_dpp v155, v50, v51 quad_perm:[0,1,2,3] row_mask:0x4 bank_mask:0xf\n"
        "v_mul_f32 v163, s52, v155\n"
        "v_add_f32_dpp v154, v48, v49 quad_perm:[0,1,2,3] row_mask:0x4 bank_mask:0xf\n"
        "s_waitcnt lgkmcnt(0)\n"
        "v_fma_f32 v163, -v163, v155, v85\n"
        "v_add_f32_e64 v163, s53, |v163|\n"
        "v_cvt_pk_bf16_f32 v27, v46, v47\n"
        "v_cvt_pk_bf16_f32 v26, v44, v45\n"
        "v_mfma_f32_16x16x32_bf16 v[72:75], v[36:39], v[116:119], 0\n"
        "v_rsq_f32 v85, v163\n"
        "v_mfma_f32_16x16x32_bf16 v[72:75], v[40:43], v[120:123], v[72:75]\n"
        "s_add_u32 s37, s36, 392448\n"
        "ds_read_b128 v[150:153], v8 offset:128\n"
        "v_and_b32 v104, s28, v85\n"
        "v_mfma_f32_16x16x32_bf16 v[72:75], v[164:167], v[24:27], v[72:75]\n"
        "ds_read2_b32 v[140:141], v6 offset0:0 offset1:2\n"
        "ds_read2_b32 v[142:143], v6 offset0:8 offset1:10\n"
        "v_mfma_f32_16x16x32_bf16 v[100:103], v[136:139], v[24:27], 0\n"
        "v_and_b32 v106, s28, v85\n"
        "v_sub_f32 v163, v85, v104\n"
        "v_mfma_f32_16x16x32_bf16 v[100:103], v[32:35], v[120:123], v[100:103]\n"
        "v_cvt_pk_bf16_f32 v105, v163, v104\n"
        "v_cvt_pk_bf16_f32 v107, v106, v163\n"
        "v_mfma_f32_16x16x32_bf16 v[100:103], v[28:31], v[116:119], v[100:103]\n"
        "buffer_store_dwordx3 v[146:148], v15, s[8:11], s37 offen offset:768 sc0 sc1 nt\n"
        "v_mul_f32 v154, v154, v85\n"
        "v_readlane_b32 s58, v85, 56\n"
        "s_mov_b64 exec, s[56:57]\n"
        "v_mov_b32 v154, 0xff800000\n"
        "s_mov_b64 exec, -1\n"
        "v_mfma_f32_16x16x4_4b_bf16 v[56:71], v[104:105], v[106:107], 0 blgp:4\n"
        "v_mul_f32 v154, s58, v154\n"
        "v_exp_f32 v154, v154\n"
        "v_mfma_f32_16x16x32_bf16 v[146:149], v[108:111], v[116:119], 0\n"
        "s_add_u32 s37, s36, 490560\n"
        "v_add_f32_dpp v163, v154, v154 row_ror:8 row_mask:0xf bank_mask:0xf\n"
        "v_mul_f32 v154, v154, v85\n"
        "v_mfma_f32_16x16x32_bf16 v[146:149], v[112:115], v[120:123], v[146:149]\n"
        "v_add_f32_dpp v163, v163, v163 row_ror:4 row_mask:0xf bank_mask:0xf\n"
        "v_mfma_f32_16x16x32_bf16 v[146:149], v[80:83], v[24:27], v[146:149]\n"
        "s_waitcnt lgkmcnt(0)\n"
        "v_add_f32_dpp v163, v163, v163 row_ror:2 row_mask:0xf bank_mask:0xf\n"
        "v_mfma_f32_16x16x32_bf16 v[52:55], v[150:153], v[24:27], 0\n"
        "v_mul_f32 v72, v72, v56\n"
        "v_mul_f32 v73, v73, v57\n"
        "v_smfmac_f32_16x16x64_bf16 v[52:55], v[140:143], v[116:123], v4\n"
        "buffer_store_dwordx3 v[96:98], v12, s[8:11], s37 offen offset:192 sc0 sc1 nt\n"
        "v_mul_f32 v74, v74, v58\n"
        "v_mul_f32 v75, v75, v59\n"
        "v_exp_f32 v72, v72\n"
        "v_mul_f32 v100, v100, v60\n"
        "v_mfma_f32_16x16x32_bf16 v[20:23], v[168:171], v[28:31], 0\n"
        "s_add_u32 s37, s36, 490560\n"
        "v_exp_f32 v73, v73\n"
        "v_mfma_f32_16x16x32_bf16 v[20:23], v[172:175], v[32:35], v[20:23]\n"
        "v_exp_f32 v74, v74\n"
        "v_mfma_f32_16x16x32_bf16 v[20:23], v[176:179], v[136:139], v[20:23]\n"
        "v_exp_f32 v75, v75\n"
        "v_add_f32 v144, v72, v73\n"
        "v_mfma_f32_16x16x32_bf16 v[24:27], v[188:191], v[136:139], 0\n"
        "v_add_f32 v144, v144, v74\n"
        "v_add_f32 v144, v144, v75\n"
        "v_mfma_f32_16x16x32_bf16 v[24:27], v[184:187], v[32:35], v[24:27]\n"
        "v_mul_f32 v101, v101, v61\n"
        "v_mul_f32 v102, v102, v62\n"
        "v_mfma_f32_16x16x32_bf16 v[24:27], v[180:183], v[28:31], v[24:27]\n"
        "buffer_store_dwordx3 v[124:126], v14, s[8:11], s37 offen offset:960 sc0 sc1 nt\n"
        "v_cvt_pk_bf16_f32 v56, v20, v21\n"
        "v_cvt_pk_bf16_f32 v57, v22, v23\n"
        "v_mul_f32 v103, v103, v63\n"
        "v_exp_f32 v100, v100\n"
        "v_mfma_f32_16x16x32_bf16 v[20:23], v[192:195], v[28:31], 0\n"
        "s_add_u32 s37, s36, 588672\n"
        "v_exp_f32 v101, v101\n"
        "v_mfma_f32_16x16x32_bf16 v[20:23], v[196:199], v[32:35], v[20:23]\n"
        "v_cvt_pk_bf16_f32 v58, v24, v25\n"
        "v_cvt_pk_bf16_f32 v59, v26, v27\n"
        "v_mfma_f32_16x16x32_bf16 v[20:23], v[200:203], v[136:139], v[20:23]\n"
        "buffer_store_dwordx3 v[156:158], v12, s[8:11], s37 offen offset:384 sc0 sc1 nt\n"
        "v_exp_f32 v102, v102\n"
        "v_exp_f32 v103, v103\n"
        "v_add_f32 v144, v144, v100\n"
        "v_mfma_f32_16x16x32_bf16 v[24:27], v[212:215], v[136:139], 0\n"
        "v_add_f32 v144, v144, v101\n"
        "v_add_f32 v144, v144, v102\n"
        "v_mfma_f32_16x16x32_bf16 v[24:27], v[208:211], v[32:35], v[24:27]\n"
        "v_cvt_pk_bf16_f32 v60, v20, v21\n"
        "v_cvt_pk_bf16_f32 v61, v22, v23\n"
        "v_mfma_f32_16x16x32_bf16 v[24:27], v[204:207], v[28:31], v[24:27]\n"
        "v_add_f32 v144, v144, v103\n"
        "v_mul_f32 v146, v146, v64\n"
        "v_mul_f32 v147, v147, v65\n"
        "v_mul_f32 v148, v148, v66\n"
        "v_mfma_f32_16x16x32_bf16 v[20:23], v[216:219], v[28:31], 0\n"
        "v_mul_f32 v149, v149, v67\n"
        "v_add_f32 v52, v52, v53\n"
        "v_mfma_f32_16x16x32_bf16 v[20:23], v[220:223], v[32:35], v[20:23]\n"
        "v_cvt_pk_bf16_f32 v62, v24, v25\n"
        "v_cvt_pk_bf16_f32 v63, v26, v27\n"
        "v_mfma_f32_16x16x32_bf16 v[20:23], v[224:227], v[136:139], v[20:23]\n"
        "v_exp_f32 v146, v146\n"
        "v_mul_f32 v52, v52, v68\n"
        "v_mfma_f32_16x16x32_bf16 v[24:27], v[236:239], v[136:139], 0\n"
        "v_exp_f32 v147, v147\n"
        "v_mfma_f32_16x16x32_bf16 v[24:27], v[232:235], v[32:35], v[24:27]\n"
        "v_exp_f32 v148, v148\n"
        "v_mfma_f32_16x16x32_bf16 v[24:27], v[228:231], v[28:31], v[24:27]\n"
        "v_exp_f32 v149, v149\n"
        "v_cvt_pk_bf16_f32 v64, v20, v21\n"
        "v_cvt_pk_bf16_f32 v65, v22, v23\n"
        "v_mfma_f32_16x16x4_4b_bf16 v[120:135], v[104:105], v[106:107], 0 blgp:5\n"
        "v_add_f32 v144, v144, v146\n"
        "v_add_f32 v144, v144, v147\n"
        "v_add_f32 v144, v144, v148\n"
        "v_exp_f32 v52, v52\n"
        "v_cvt_pk_bf16_f32 v66, v24, v25\n"
        "v_cvt_pk_bf16_f32 v67, v26, v27\n"
        "v_add_f32 v144, v144, v149\n"
        "v_add_f32_dpp v163, v163, v163 row_ror:1 row_mask:0xf bank_mask:0xf\n"
        "v_mfma_f32_16x16x32_bf16 v[116:119], v[36:39], v[56:59], 0\n"
        "v_add_f32_dpp v144, v52, v144 quad_perm:[0,1,2,3] row_mask:0x4 bank_mask:0xf\n"
        "v_mfma_f32_16x16x32_bf16 v[116:119], v[40:43], v[60:63], v[116:119]\n"
        "v_mfma_f32_16x16x32_bf16 v[116:119], v[164:167], v[64:67], v[116:119]\n"
        "v_permlane32_swap_b32 v144, v163\n"
        "v_add_f32 v144, v144, v163\n"
        "v_mov_b32 v159, v144\n"
        "v_mfma_f32_16x16x32_bf16 v[96:99], v[136:139], v[64:67], 0\n"
        "v_mfma_f32_16x16x32_bf16 v[96:99], v[32:35], v[60:63], v[96:99]\n"
        "v_permlane16_swap_b32 v144, v159\n"
        "v_mfma_f32_16x16x32_bf16 v[96:99], v[28:31], v[56:59], v[96:99]\n"
        "v_add_f32 v144, v144, v159\n"
        "v_rcp_f32 v158, v144\n"
        "v_mfma_f32_16x16x32_bf16 v[160:163], v[108:111], v[56:59], 0\n"
        "v_and_b32 v134, s28, v158\n"
        "v_sub_f32 v159, v158, v134\n"
        "v_mfma_f32_16x16x32_bf16 v[160:163], v[112:115], v[60:63], v[160:163]\n"
        "v_cvt_pk_bf16_f32 v135, v134, v159\n"
        "v_mul_f32 v116, v116, v120\n"
        "v_mfma_f32_16x16x32_bf16 v[160:163], v[80:83], v[64:67], v[160:163]\n"
        "v_mul_f32 v117, v117, v121\n"
        "v_mul_f32 v118, v118, v122\n"
        "v_mul_f32 v119, v119, v123\n"
        "v_mfma_f32_16x16x32_bf16 v[76:79], v[150:153], v[64:67], 0\n"
        "v_exp_f32 v116, v116\n"
        "v_smfmac_f32_16x16x64_bf16 v[76:79], v[140:143], v[56:63], v4\n"
        "v_exp_f32 v117, v117\n"
        "v_mfma_f32_16x16x32_bf16 v[64:67], v[168:171], v[108:111], 0\n"
        "v_exp_f32 v118, v118\n"
        "v_mfma_f32_16x16x32_bf16 v[64:67], v[172:175], v[112:115], v[64:67]\n"
        "v_exp_f32 v119, v119\n"
        "v_mfma_f32_16x16x32_bf16 v[64:67], v[176:179], v[80:83], v[64:67]\n"
        "v_add_f32 v145, v116, v117\n"
        "v_add_f32 v145, v145, v118\n"
        "v_add_f32 v145, v145, v119\n"
        "v_mfma_f32_16x16x32_bf16 v[20:23], v[188:191], v[80:83], 0\n"
        "v_mul_f32 v96, v96, v124\n"
        "v_mul_f32 v97, v97, v125\n"
        "v_mfma_f32_16x16x32_bf16 v[20:23], v[184:187], v[112:115], v[20:23]\n"
        "v_mul_f32 v98, v98, v126\n"
        "v_cvt_pk_bf16_f32 v44, v64, v65\n"
        "v_mfma_f32_16x16x32_bf16 v[20:23], v[180:183], v[108:111], v[20:23]\n"
        "v_cvt_pk_bf16_f32 v45, v66, v67\n"
        "v_mul_f32 v99, v99, v127\n"
        "v_exp_f32 v96, v96\n"
        "v_mfma_f32_16x16x32_bf16 v[24:27], v[192:195], v[108:111], 0\n"
        "v_exp_f32 v97, v97\n"
        "v_mfma_f32_16x16x32_bf16 v[24:27], v[196:199], v[112:115], v[24:27]\n"
        "v_exp_f32 v98, v98\n"
        "v_mfma_f32_16x16x32_bf16 v[24:27], v[200:203], v[80:83], v[24:27]\n"
        "v_cvt_pk_bf16_f32 v46, v20, v21\n"
        "v_cvt_pk_bf16_f32 v47, v22, v23\n"
        "v_add_f32 v145, v145, v96\n"
        "v_mfma_f32_16x16x32_bf16 v[20:23], v[212:215], v[80:83], 0\n"
        "v_exp_f32 v99, v99\n"
        "v_mfma_f32_16x16x32_bf16 v[20:23], v[208:211], v[112:115], v[20:23]\n"
        "v_add_f32 v145, v145, v97\n"
        "v_add_f32 v145, v145, v98\n"
        "v_mfma_f32_16x16x32_bf16 v[20:23], v[204:207], v[108:111], v[20:23]\n"
        "v_cvt_pk_bf16_f32 v48, v24, v25\n"
        "v_cvt_pk_bf16_f32 v49, v26, v27\n"
        "v_add_f32 v145, v145, v99\n"
        "v_mfma_f32_16x16x32_bf16 v[120:123], v[216:219], v[108:111], 0\n"
        "v_mul_f32 v160, v160, v128\n"
        "v_mul_f32 v161, v161, v129\n"
        "v_mfma_f32_16x16x32_bf16 v[120:123], v[220:223], v[112:115], v[120:123]\n"
        "v_mul_f32 v162, v162, v130\n"
        "v_cvt_pk_bf16_f32 v50, v20, v21\n"
        "v_mfma_f32_16x16x32_bf16 v[120:123], v[224:227], v[80:83], v[120:123]\n"
        "v_cvt_pk_bf16_f32 v51, v22, v23\n"
        "v_mfma_f32_16x16x4_4b_bf16 v[56:71], v[104:105], v[134:135], 0 blgp:4\n"
        "v_mul_f32 v163, v163, v131\n"
        "v_exp_f32 v160, v160\n"
        "v_exp_f32 v161, v161\n"
        "v_exp_f32 v162, v162\n"
        "v_exp_f32 v163, v163\n"
        "v_add_f32 v145, v145, v160\n"
        "v_cvt_pk_bf16_f32 v24, v120, v121\n"
        "v_mfma_f32_16x16x32_bf16 v[20:23], v[236:239], v[80:83], 0\n"
        "v_cvt_pk_bf16_f32 v25, v122, v123\n"
        "v_add_f32 v145, v145, v161\n"
        "v_mfma_f32_16x16x32_bf16 v[20:23], v[232:235], v[112:115], v[20:23]\n"
        "v_add_f32 v145, v145, v162\n"
        "v_mul_f32 v72, v72, v56\n"
        "v_mfma_f32_16x16x32_bf16 v[20:23], v[228:231], v[108:111], v[20:23]\n"
        "v_mul_f32 v73, v73, v57\n"
        "v_mul_f32 v74, v74, v58\n"
        "v_mul_f32 v75, v75, v59\n"
        "v_mul_f32 v100, v100, v60\n"
        "v_mul_f32 v101, v101, v61\n"
        "v_add_f32 v145, v145, v163\n"
        "v_add_f32 v76, v76, v77\n"
        "v_mul_f32 v76, v76, v132\n"
        "v_cvt_pk_bf16_f32 v27, v22, v23\n"
        "v_cvt_pk_bf16_f32 v26, v20, v21\n"
        "v_mfma_f32_16x16x32_bf16 v[120:123], v[36:39], v[44:47], 0\n"
        "v_exp_f32 v76, v76\n"
        "v_mfma_f32_16x16x32_bf16 v[120:123], v[40:43], v[48:51], v[120:123]\n"
        "v_mul_f32 v102, v102, v62\n"
        "v_add_f32_dpp v145, v76, v145 quad_perm:[0,1,2,3] row_mask:0x4 bank_mask:0xf\n"
        "v_mfma_f32_16x16x32_bf16 v[120:123], v[164:167], v[24:27], v[120:123]\n"
        "v_mul_f32 v103, v103, v63\n"
        "v_mul_f32 v146, v146, v64\n"
        "v_mul_f32 v147, v147, v65\n"
        "v_mfma_f32_16x16x32_bf16 v[88:91], v[136:139], v[24:27], 0\n"
        "v_mul_f32 v148, v148, v66\n"
        "v_mul_f32 v149, v149, v67\n"
        "v_mfma_f32_16x16x32_bf16 v[88:91], v[32:35], v[48:51], v[88:91]\n"
        "v_fma_f32 v52, v52, v68, v19\n"
        "v_cvt_pk_bf16_f32 v92, v72, v73\n"
        "v_mfma_f32_16x16x32_bf16 v[88:91], v[28:31], v[44:47], v[88:91]\n"
        "v_cvt_pk_bf16_f32 v93, v74, v75\n"
        "v_mfma_f32_16x16x4_4b_bf16 v[58:73], v[104:105], v[106:107], 0 blgp:6\n"
        "v_cvt_pk_bf16_f32 v94, v100, v101\n"
        "v_cvt_pk_bf16_f32 v95, v102, v103\n"
        "v_cvt_pk_bf16_f32 v100, v146, v147\n"
        "v_cvt_pk_bf16_f32 v102, v148, v149\n"
        "v_cvt_pk_bf16_f32 v101, v52, 0\n"
        "v_min3_f32 v77, v144, v158, v85\n"
        "v_readlane_b32 s59, v158, 32\n"
        "v_mfma_f32_16x16x32_bf16 v[156:159], v[108:111], v[44:47], 0\n"
        "v_mfma_f32_16x16x32_bf16 v[156:159], v[112:115], v[48:51], v[156:159]\n"
        "v_mul_f32 v154, s59, v154\n"
        "v_cvt_pk_bf16_f32 v154, v154, 0\n"
        "v_mfma_f32_16x16x32_bf16 v[156:159], v[80:83], v[24:27], v[156:159]\n"
        "v_mul_f32 v120, v120, v58\n"
        "v_mul_f32 v121, v121, v59\n"
        "v_mul_f32 v122, v122, v60\n"
        "v_mfma_f32_16x16x32_bf16 v[106:109], v[150:153], v[24:27], 0\n"
        "ds_read_b128 v[128:131], v17 offset:40064\n"
        "v_mul_f32 v123, v123, v61\n"
        "v_smfmac_f32_16x16x64_bf16 v[106:109], v[140:143], v[44:51], v4\n"
        "ds_read_b128 v[24:27], v17 offset:40128\n"
        "ds_read_b128 v[150:153], v17 offset:40192\n"
        "v_mfma_f32_16x16x32_bf16 v[44:47], v[36:39], v[240:243], 0\n"
        "v_exp_f32 v120, v120\n"
        "v_mfma_f32_16x16x32_bf16 v[44:47], v[40:43], v[244:247], v[44:47]\n"
        "ds_read_b128 v[80:83], v0 offset:5136\n"
        "v_mul_f32 v88, v88, v62\n"
        "v_mfma_f32_16x16x32_bf16 v[44:47], v[164:167], v[248:251], v[44:47]\n"
        "ds_read_b128 v[112:115], v0 offset:4112\n"
        "v_mul_f32 v89, v89, v63\n"
        "v_mfma_f32_16x16x32_bf16 v[48:51], v[136:139], v[248:251], 0\n"
        "v_exp_f32 v121, v121\n"
        "v_mfma_f32_16x16x32_bf16 v[48:51], v[32:35], v[244:247], v[48:51]\n"
        "v_exp_f32 v122, v122\n"
        "v_mfma_f32_16x16x32_bf16 v[48:51], v[28:31], v[240:243], v[48:51]\n"
        "s_waitcnt lgkmcnt(2)\n"
        "v_cvt_pk_bf16_f32 v54, v44, v45\n"
        "v_cvt_pk_bf16_f32 v55, v46, v47\n"
        "v_add_f32 v103, v120, v121\n"
        "v_mfma_f32_16x16x32_bf16 v[60:63], v[128:131], v[240:243], 0\n"
        "v_exp_f32 v123, v123\n"
        "v_mfma_f32_16x16x32_bf16 v[60:63], v[24:27], v[244:247], v[60:63]\n"
        "v_add_f32 v103, v103, v122\n"
        "v_cvt_pk_bf16_f32 v56, v48, v49\n"
        "v_mfma_f32_16x16x32_bf16 v[60:63], v[150:153], v[248:251], v[60:63]\n"
        "s_waitcnt lgkmcnt(0)\n"
        "v_cvt_pk_bf16_f32 v57, v50, v51\n"
        "v_add_f32 v103, v103, v123\n"
        "v_mul_f32 v90, v90, v64\n"
        "v_mfma_f32_16x16x32_bf16 v[20:23], v[150:153], v[80:83], 0\n"
        "v_mul_f32 v91, v91, v65\n"
        "v_mul_f32 v156, v156, v66\n"
        "v_mfma_f32_16x16x32_bf16 v[20:23], v[24:27], v[112:115], v[20:23]\n"
        "ds_read_b128 v[140:143], v0 offset:6176\n"
        "v_cvt_pk_bf16_f32 v44, v60, v61\n"
        "v_mfma_f32_16x16x32_bf16 v[20:23], v[128:131], v[252:255], v[20:23]\n"
        "ds_read_b128 v[146:149], v0 offset:7200\n"
        "ds_read_b128 v[84:87], v0 offset:8224\n"
        "v_mfma_f32_16x16x32_bf16 v[108:111], v[28:31], v[252:255], 0\n"
        "v_cvt_pk_bf16_f32 v48, v62, v63\n"
        "v_mul_f32 v157, v157, v67\n"
        "v_mfma_f32_16x16x32_bf16 v[108:111], v[32:35], v[112:115], v[108:111]\n"
        "v_exp_f32 v88, v88\n"
        "v_mfma_f32_16x16x32_bf16 v[108:111], v[136:139], v[80:83], v[108:111]\n"
        "v_cvt_pk_bf16_f32 v62, v20, v21\n"
        "v_cvt_pk_bf16_f32 v66, v22, v23\n"
        "v_add_f32 v103, v103, v88\n"
        "v_mfma_f32_16x16x32_bf16 v[58:61], v[164:167], v[80:83], 0\n"
        "v_exp_f32 v89, v89\n"
        "v_mfma_f32_16x16x32_bf16 v[58:61], v[40:43], v[112:115], v[58:61]\n"
        "v_exp_f32 v90, v90\n"
        "v_mfma_f32_16x16x32_bf16 v[58:61], v[36:39], v[252:255], v[58:61]\n"
        "s_waitcnt lgkmcnt(0)\n"
        "v_cvt_pk_bf16_f32 v80, v108, v109\n"
        "v_cvt_pk_bf16_f32 v81, v110, v111\n"
        "v_add_f32 v103, v103, v89\n"
        "v_mfma_f32_16x16x32_bf16 v[112:115], v[36:39], v[140:143], 0\n"
        "v_exp_f32 v91, v91\n"
        "v_mfma_f32_16x16x32_bf16 v[112:115], v[40:43], v[146:149], v[112:115]\n"
        "ds_read_b128 v[132:135], v0 offset:11312\n"
        "v_cvt_pk_bf16_f32 v78, v58, v59\n"
        "v_mfma_f32_16x16x32_bf16 v[112:115], v[164:167], v[84:87], v[112:115]\n"
        "ds_read_b128 v[124:127], v0 offset:10288\n"
        "ds_read_b128 v[50:53], v0 offset:9264\n"
        "v_mfma_f32_16x16x32_bf16 v[72:75], v[136:139], v[84:87], 0\n"
        "v_cvt_pk_bf16_f32 v79, v60, v61\n"
        "v_add_f32 v103, v103, v90\n"
        "v_mfma_f32_16x16x32_bf16 v[72:75], v[32:35], v[146:149], v[72:75]\n"
        "v_add_f32 v103, v103, v91\n"
        "v_mul_f32 v158, v158, v68\n"
        "v_mfma_f32_16x16x32_bf16 v[72:75], v[28:31], v[140:143], v[72:75]\n"
        "v_cvt_pk_bf16_f32 v108, v112, v113\n"
        "v_cvt_pk_bf16_f32 v109, v114, v115\n"
        "v_mul_f32 v159, v159, v69\n"
        "v_mfma_f32_16x16x32_bf16 v[112:115], v[128:131], v[140:143], 0\n"
        "v_exp_f32 v156, v156\n"
        "v_mfma_f32_16x16x32_bf16 v[112:115], v[24:27], v[146:149], v[112:115]\n"
        "v_exp_f32 v157, v157\n"
        "v_mfma_f32_16x16x32_bf16 v[112:115], v[150:153], v[84:87], v[112:115]\n"
        "s_waitcnt lgkmcnt(0)\n"
        "v_cvt_pk_bf16_f32 v110, v72, v73\n"
        "v_cvt_pk_bf16_f32 v111, v74, v75\n"
        "v_exp_f32 v158, v158\n"
        "v_mfma_f32_16x16x32_bf16 v[58:61], v[150:153], v[132:135], 0\n"
        "v_exp_f32 v159, v159\n"
        "v_mfma_f32_16x16x32_bf16 v[58:61], v[24:27], v[124:127], v[58:61]\n"
        "ds_read_b128 v[84:87], v0 offset:12352\n"
        "v_cvt_pk_bf16_f32 v144, v112, v113\n"
        "v_mfma_f32_16x16x32_bf16 v[58:61], v[128:131], v[50:53], v[58:61]\n"
        "ds_read_b128 v[140:143], v0 offset:13376\n"
        "ds_read_b128 v[20:23], v0 offset:14400\n"
        "v_mfma_f32_16x16x32_bf16 v[72:75], v[28:31], v[50:53], 0\n"
        "v_cvt_pk_bf16_f32 v148, v114, v115\n"
        "v_add_f32 v103, v103, v156\n"
        "v_mfma_f32_16x16x32_bf16 v[72:75], v[32:35], v[124:127], v[72:75]\n"
        "v_add_f32 v103, v103, v157\n"
        "v_add_f32 v103, v103, v158\n"
        "v_mfma_f32_16x16x32_bf16 v[72:75], v[136:139], v[132:135], v[72:75]\n"
        "v_add_f32 v103, v103, v159\n"
        "v_add_f32 v106, v106, v107\n"
        "v_mul_f32 v106, v106, v70\n"
        "v_cvt_pk_bf16_f32 v45, v58, v59\n"
        "v_mfma_f32_16x16x32_bf16 v[112:115], v[164:167], v[132:135], 0\n"
        "v_cvt_pk_bf16_f32 v49, v60, v61\n"
        "v_mfma_f32_16x16x32_bf16 v[112:115], v[40:43], v[124:127], v[112:115]\n"
        "v_exp_f32 v106, v106\n"
        "v_mfma_f32_16x16x32_bf16 v[112:115], v[36:39], v[50:53], v[112:115]\n"
        "s_waitcnt lgkmcnt(0)\n"
        "v_cvt_pk_bf16_f32 v60, v72, v73\n"
        "v_cvt_pk_bf16_f32 v61, v74, v75\n"
        "v_add_f32_dpp v103, v106, v103 quad_perm:[0,1,2,3] row_mask:0x4 bank_mask:0xf\n"
        "v_mfma_f32_16x16x32_bf16 v[50:53], v[36:39], v[84:87], 0\n"
        "v_mfma_f32_16x16x32_bf16 v[50:53], v[40:43], v[140:143], v[50:53]\n"
        "ds_read_b128 v[124:127], v0 offset:17488\n"
        "v_mfma_f32_16x16x32_bf16 v[50:53], v[164:167], v[20:23], v[50:53]\n"
        "ds_read_b128 v[132:135], v0 offset:16464\n"
        "ds_read_b128 v[72:75], v0 offset:15440\n"
        "v_mfma_f32_16x16x32_bf16 v[68:71], v[136:139], v[20:23], 0\n"
        "v_cvt_pk_bf16_f32 v58, v112, v113\n"
        "v_cvt_pk_bf16_f32 v59, v114, v115\n"
        "v_mfma_f32_16x16x32_bf16 v[68:71], v[32:35], v[140:143], v[68:71]\n"
        "v_permlane32_swap_b32 v145, v103\n"
        "v_mfma_f32_16x16x32_bf16 v[68:71], v[28:31], v[84:87], v[68:71]\n"
        "v_add_f32 v145, v145, v103\n"
        "v_mov_b32 v147, v145\n"
        "v_cvt_pk_bf16_f32 v82, v50, v51\n"
        "v_mfma_f32_16x16x32_bf16 v[112:115], v[128:131], v[84:87], 0\n"
        "ds_read2_b32 v[50:51], v11 offset0:0 offset1:3\n"
        "v_cvt_pk_bf16_f32 v83, v52, v53\n"
        "v_mfma_f32_16x16x32_bf16 v[112:115], v[24:27], v[140:143], v[112:115]\n"
        "ds_read_b64_tr_b16 v[46:47], v9 offset:40064\n"
        "v_cvt_pk_bf16_f32 v84, v68, v69\n"
        "v_mfma_f32_16x16x32_bf16 v[112:115], v[150:153], v[20:23], v[112:115]\n"
        "s_waitcnt lgkmcnt(2)\n"
        "ds_read2_b32 v[68:69], v11 offset0:1 offset1:4\n"
        "v_permlane16_swap_b32 v145, v147\n"
        "v_add_f32 v145, v145, v147\n"
        "v_mfma_f32_16x16x32_bf16 v[140:143], v[150:153], v[124:127], 0\n"
        "ds_read_b64_tr_b16 v[64:65], v9 offset:40080\n"
        "v_cvt_pk_bf16_f32 v85, v70, v71\n"
        "v_mfma_f32_16x16x32_bf16 v[140:143], v[24:27], v[132:135], v[140:143]\n"
        "ds_read2_b32 v[150:151], v11 offset0:2 offset1:5\n"
        "v_cvt_pk_bf16_f32 v63, v112, v113\n"
        "v_mfma_f32_16x16x32_bf16 v[140:143], v[128:131], v[72:75], v[140:143]\n"
        "ds_read_b64_tr_b16 v[146:147], v9 offset:40096\n"
        "v_rcp_f32 v149, v145\n"
        "v_cvt_pk_bf16_f32 v67, v114, v115\n"
        "v_mfma_f32_16x16x32_bf16 v[128:131], v[28:31], v[72:75], 0\n"
        "v_and_b32 v152, s28, v149\n"
        "v_sub_f32 v155, v149, v152\n"
        "v_mfma_f32_16x16x32_bf16 v[128:131], v[32:35], v[132:135], v[128:131]\n"
        "v_cvt_pk_bf16_f32 v153, v152, v155\n"
        "v_mov_b32_dpp v103, v19 row_shr:4 row_mask:0xf bank_mask:0xf bound_ctrl:0\n"
        "v_mfma_f32_16x16x32_bf16 v[128:131], v[136:139], v[124:127], v[128:131]\n"
        "v_min3_f32 v77, v77, v145, v149\n"
        "v_mfma_f32_16x16x4_4b_bf16 v[20:35], v[104:105], v[152:153], 0 blgp:4\n"
        "v_cmp_nle_f32 vcc, 0x2b800000, v77\n"
        "ds_write_b16 v1, v154\n"
        "v_cvt_pk_bf16_f32 v145, v140, v141\n"
        "v_cvt_pk_bf16_f32 v149, v142, v143\n"
        "s_andn2_b64 vcc, vcc, s[56:57]\n"
        "s_cbranch_scc1 CREDOB_%=\n"
        "v_cvt_pk_bf16_f32 v114, v128, v129\n"
        "v_cvt_pk_bf16_f32 v115, v130, v131\n"
        "v_mfma_f32_16x16x32_bf16 v[136:139], v[164:167], v[124:127], 0\n"
        "s_mov_b32 exec_hi, 0xffff\n"
        "v_add_u32 v10, -40064, v10\n"
        "s_mov_b32 exec_hi, -1\n"
        "v_mfma_f32_16x16x32_bf16 v[136:139], v[40:43], v[132:135], v[136:139]\n"
        "s_add_u32 s31, s31, 1\n"
        "v_mul_f32 v116, v116, v20\n"
        "v_mul_f32 v117, v117, v21\n"
        "v_mfma_f32_16x16x32_bf16 v[136:139], v[36:39], v[72:75], v[136:139]\n"
        "s_mul_i32 s38, s31, 1344\n"
        "v_mul_f32 v118, v118, v22\n"
        "v_mul_f32 v119, v119, v23\n"
        "v_mfma_f32_16x16x32_bf16 v[124:127], v[92:95], v[54:57], 0\n"
        "s_waitcnt lgkmcnt(1)\n"
        "v_mul_f32 v96, v96, v24\n"
        "v_mul_f32 v97, v97, v25\n"
        "v_smfmac_f32_16x16x64_bf16 v[124:127], v[100:103], v[44:51], v18\n"
        "s_cmp_ge_u32 s31, s76\n"
        "s_cbranch_scc1 L34B_%=\n"
        "L35B_%=:\n"
        "ds_read_b64_tr_b16 v[46:47], v9 offset:40112\n"
        "v_mul_f32 v98, v98, v26\n"
        "v_mfma_f32_16x16x32_bf16 v[164:167], v[92:95], v[78:81], 0\n"
        "s_cmp_ge_u32 s31, s71\n"
        "v_mul_f32 v99, v99, v27\n"
        "v_mul_f32 v160, v160, v28\n"
        "v_smfmac_f32_16x16x64_bf16 v[164:167], v[100:103], v[62:69], v18\n"
        "s_cselect_b32 s39, s70, s72\n"
        "ds_read_b64_tr_b16 v[64:65], v9 offset:40128\n"
        "v_mfma_f32_16x16x32_bf16 v[36:39], v[92:95], v[108:111], 0\n"
        "s_cmp_ge_u32 s31, 20\n"
        "ds_read_b128 v[40:43], v2\n"
        "v_cvt_pk_bf16_f32 v112, v136, v137\n"
        "v_smfmac_f32_16x16x64_bf16 v[36:39], v[100:103], v[144:151], v18\n"
        "s_cselect_b32 s39, s22, s39\n"
        "ds_read_b64_tr_b16 v[146:147], v9 offset:40144\n"
        "ds_read_b128 v[20:23], v3\n"
        "v_cvt_pk_bf16_f32 v113, v138, v139\n"
        "v_mfma_f32_16x16x32_bf16 v[72:75], v[92:95], v[58:61], 0\n"
        "s_cmp_ge_u32 s31, 21\n"
        "v_mul_f32 v161, v161, v29\n"
        "s_waitcnt lgkmcnt(4)\n"
        "v_smfmac_f32_16x16x64_bf16 v[72:75], v[100:103], v[44:51], v18 abid:1\n"
        "s_cselect_b32 s39, s23, s39\n"
        "v_mul_f32 v162, v162, v30\n"
        "v_mfma_f32_16x16x4_4b_bf16 v[128:143], v[104:105], v[152:153], 0 blgp:6\n"
        "v_mul_f32 v163, v163, v31\n"
        "v_fma_f32 v76, v76, v32, v19\n"
        "v_cvt_pk_bf16_f32 v32, v116, v117\n"
        "v_cvt_pk_bf16_f32 v33, v118, v119\n"
        "v_cvt_pk_bf16_f32 v34, v96, v97\n"
        "v_cvt_pk_bf16_f32 v35, v98, v99\n"
        "v_cvt_pk_bf16_f32 v116, v160, v161\n"
        "v_cvt_pk_bf16_f32 v118, v162, v163\n"
        "v_cvt_pk_bf16_f32 v117, v76, 0\n"
        "v_cvt_pk_bf16_f32 v160, v124, v164\n"
        "v_cvt_pk_bf16_f32 v161, v36, v72\n"
        "v_mfma_f32_16x16x32_bf16 v[152:155], v[92:95], v[82:85], 0\n"
        "s_waitcnt lgkmcnt(0)\n"
        "v_mul_f32 v120, v120, v128\n"
        "v_mul_f32 v121, v121, v129\n"
        "v_smfmac_f32_16x16x64_bf16 v[152:155], v[100:103], v[62:69], v18 abid:1\n"
        "s_add_u32 s39, s39, s38\n"
        "s_mul_i32 s68, s31, 3\n"
        "v_mul_f32 v122, v122, v130\n"
        "v_mul_f32 v123, v123, v131\n"
        "v_mul_f32 v88, v88, v132\n"
        "v_mov_b64 v[96:97], 0\n"
        "v_mfma_f32_16x16x32_bf16 v[28:31], v[92:95], v[112:115], 0\n"
        "s_lshr_b32 s38, s31, 3\n"
        "v_mov_b64 v[98:99], 0\n"
        "v_mov_b64 v[92:93], 0\n"
        "v_smfmac_f32_16x16x64_bf16 v[28:31], v[100:103], v[144:151], v18 abid:1\n"
        "s_add_u32 s38, s38, 11\n"
        "v_mov_b64 v[94:95], 0\n"
        "v_smfmac_f32_16x16x64_bf16 v[96:99], v[40:43], v[54:61], v4\n"
        "s_bitcmp1_b32 s31, 0\n"
        "v_mov_b64 v[24:25], 0\n"
        "v_smfmac_f32_16x16x64_bf16 v[96:99], v[20:23], v[44:51], v4 abid:1\n"
        "s_cselect_b32 s38, 3, s38\n"
        "s_add_u32 s37, s35, 0\n"
        "ds_read_b64_tr_b16 v[46:47], v9 offset:43392\n"
        "v_mov_b64 v[26:27], 0\n"
        "v_smfmac_f32_16x16x64_bf16 v[92:95], v[40:43], v[78:85], v4\n"
        "s_add_u32 s68, s68, s38\n"
        "v_cvt_pk_bf16_f32 v162, v152, v28\n"
        "v_smfmac_f32_16x16x64_bf16 v[92:95], v[20:23], v[62:69], v4 abid:1\n"
        "s_cmp_lt_u32 s31, s14\n"
        "ds_read_b64_tr_b16 v[64:65], v9 offset:43408\n"
        "v_mov_b32_dpp v119, v19 row_shr:4 row_mask:0xf bank_mask:0xf bound_ctrl:0\n"
        "v_mul_f32 v89, v89, v133\n"
        "v_cvt_pk_bf16_f32 v124, v125, v165\n"
        "v_smfmac_f32_16x16x64_bf16 v[24:27], v[40:43], v[108:115], v4\n"
        "s_cselect_b32 s68, s68, 0\n"
        "v_cvt_pk_bf16_f32 v100, v126, v166\n"
        "v_smfmac_f32_16x16x64_bf16 v[24:27], v[20:23], v[144:151], v4 abid:1\n"
        "buffer_store_dwordx3 v[160:162], v12, s[8:11], s37 offen sc0 sc1\n"
        "ds_read_b64_tr_b16 v[146:147], v9 offset:43424\n"
        "v_cvt_pk_bf16_f32 v164, v127, v167\n"
        "v_cvt_pk_bf16_f32 v125, v37, v73\n"
        "v_cvt_pk_bf16_f32 v101, v38, v74\n"
        "v_cvt_pk_bf16_f32 v165, v39, v75\n"
        "v_cvt_pk_bf16_f32 v126, v153, v29\n"
        "v_mfma_f32_16x16x32_bf16 v[20:23], v[32:35], v[54:57], 0\n"
        "s_waitcnt lgkmcnt(2)\n"
        "v_cvt_pk_bf16_f32 v102, v154, v30\n"
        "v_cvt_pk_bf16_f32 v166, v155, v31\n"
        "v_smfmac_f32_16x16x64_bf16 v[20:23], v[116:119], v[44:51], v18\n"
        "s_and_b32 s38, s31, 7\n"
        "s_cselect_b32 s30, 1, -7\n"
        "ds_read_b64_tr_b16 v[46:47], v9 offset:43440\n"
        "v_add_f32 v96, v96, v97\n"
        "v_add_f32 v98, v98, v99\n"
        "v_add_f32 v92, v92, v93\n"
        "v_mfma_f32_16x16x32_bf16 v[160:163], v[32:35], v[78:81], 0\n"
        "s_waitcnt lgkmcnt(2)\n"
        "v_add_f32 v94, v94, v95\n"
        "v_cvt_pk_bf16_f32 v152, v96, v92\n"
        "v_smfmac_f32_16x16x64_bf16 v[160:163], v[116:119], v[62:69], v18\n"
        "s_add_u32 s37, s35, 0\n"
        "s_mul_i32 s29, s30, 192\n"
        "ds_read_b64_tr_b16 v[64:65], v9 offset:43456\n"
        "v_add_f32 v24, v24, v25\n"
        "v_add_f32 v26, v26, v27\n"
        "v_cvt_pk_bf16_f32 v153, v24, v98\n"
        "v_mfma_f32_16x16x32_bf16 v[96:99], v[32:35], v[108:111], 0\n"
        "s_waitcnt lgkmcnt(2)\n"
        "v_cvt_pk_bf16_f32 v154, v94, v26\n"
        "v_mul_f32 v90, v90, v134\n"
        "v_smfmac_f32_16x16x64_bf16 v[96:99], v[116:119], v[144:151], v18\n"
        "buffer_store_dwordx3 v[124:126], v15, s[8:11], s37 offen offset:768 sc0 sc1\n"
        "ds_read_b64_tr_b16 v[146:147], v9 offset:43472\n"
        "s_mul_i32 s30, s30, 392\n"
        "v_mul_f32 v91, v91, v135\n"
        "v_mul_f32 v156, v156, v136\n"
        "v_mul_f32 v157, v157, v137\n"
        "v_mul_f32 v158, v158, v138\n"
        "v_mfma_f32_16x16x32_bf16 v[128:131], v[32:35], v[58:61], 0\n"
        "s_waitcnt lgkmcnt(2)\n"
        "s_cmp_eq_u32 s73, s18\n"
        "v_mul_f32 v159, v159, v139\n"
        "v_smfmac_f32_16x16x64_bf16 v[128:131], v[116:119], v[44:51], v18 abid:1\n"
        "s_cselect_b32 s34, -7488, s75\n"
        "s_add_u32 s37, s35, 98112\n"
        "ds_read_b64_tr_b16 v[46:47], v9 offset:46720\n"
        "v_mfma_f32_16x16x32_bf16 v[92:95], v[32:35], v[82:85], 0\n"
        "s_waitcnt lgkmcnt(2)\n"
        "v_fma_f32 v106, v106, v140, v19\n"
        "v_cvt_pk_bf16_f32 v24, v120, v121\n"
        "v_smfmac_f32_16x16x64_bf16 v[92:95], v[116:119], v[62:69], v18 abid:1\n"
        "s_add_u32 s73, s73, s34\n"
        "ds_read_b64_tr_b16 v[64:65], v9 offset:46736\n"
        "v_cvt_pk_bf16_f32 v25, v122, v123\n"
        "v_cvt_pk_bf16_f32 v26, v88, v89\n"
        "v_mfma_f32_16x16x32_bf16 v[120:123], v[32:35], v[112:115], 0\n"
        "s_waitcnt lgkmcnt(2)\n"
        "v_cvt_pk_bf16_f32 v27, v90, v91\n"
        "v_cvt_pk_bf16_f32 v140, v156, v157\n"
        "v_smfmac_f32_16x16x64_bf16 v[120:123], v[116:119], v[144:151], v18 abid:1\n"
        "buffer_store_dwordx3 v[100:102], v12, s[8:11], s37 offen offset:192 sc0 sc1 nt\n"
        "ds_read_b64_tr_b16 v[146:147], v9 offset:46752\n"
        "ds_read_b32 v167, v0 offset:21600\n"
        "v_cvt_pk_bf16_f32 v142, v158, v159\n"
        "v_cvt_pk_bf16_f32 v141, v106, 0\n"
        "v_mov_b32_dpp v143, v19 row_shr:4 row_mask:0xf bank_mask:0xf bound_ctrl:0\n"
        "v_mfma_f32_16x16x32_bf16 v[72:75], v[24:27], v[54:57], 0\n"
        "s_waitcnt lgkmcnt(3)\n"
        "v_writelane_b32 v10, s73, 56\n"
        "v_add_u32 v11, s30, v11\n"
        "v_smfmac_f32_16x16x64_bf16 v[72:75], v[140:143], v[44:51], v18\n"
        "s_add_u32 s37, s35, 98112\n"
        "ds_read_b64_tr_b16 v[46:47], v9 offset:46768\n"
        "v_mfma_f32_16x16x32_bf16 v[132:135], v[24:27], v[78:81], 0\n"
        "s_waitcnt lgkmcnt(3)\n"
        "v_smfmac_f32_16x16x64_bf16 v[132:135], v[140:143], v[62:69], v18\n"
        "ds_read_b64_tr_b16 v[64:65], v9 offset:46784\n"
        "v_mfma_f32_16x16x32_bf16 v[156:159], v[24:27], v[108:111], 0\n"
        "s_waitcnt lgkmcnt(3)\n"
        "s_waitcnt lgkmcnt(2)\n"
        "v_readfirstlane_b32 s40, v167\n"
        "v_smfmac_f32_16x16x64_bf16 v[156:159], v[140:143], v[144:151], v18\n"
        "buffer_store_dwordx3 v[164:166], v14, s[8:11], s37 offen offset:960 sc0 sc1 nt\n"
        "ds_read_b64_tr_b16 v[146:147], v9 offset:46800\n"
        "s_add_u32 s37, s35, 588672\n"
        "s_add_u32 s46, s37, 1344\n"
        "s_cmp_lt_i32 s40, s68\n"
        "s_mov_b64 exec, 1\n"
        "ds_inc_u32 v0, v1 offset:21612\n"
        "s_mov_b64 exec, -1\n"
        "s_cbranch_scc1 L38B_%=\n"
        "L39B_%=:\n"
        "ds_read_b128 v[36:39], v7 offset:0\n"
        "v_mfma_f32_16x16x32_bf16 v[124:127], v[24:27], v[58:61], 0\n"
        "s_waitcnt lgkmcnt(4)\n"
        "ds_read_b128 v[28:31], v7 offset:3328\n"
        "v_smfmac_f32_16x16x64_bf16 v[124:127], v[140:143], v[44:51], v18 abid:1\n"
        "buffer_store_dwordx3 v[152:154], v12, s[44:47], s37 offen offset:1152 sc0 sc1 nt\n"
        "ds_read_b128 v[40:43], v7 offset:64\n"
        "ds_read_b128 v[32:35], v7 offset:3392\n"
        "ds_read_b128 v[164:167], v7 offset:128\n"
        "ds_read_b128 v[136:139], v7 offset:3456\n"
        "v_mfma_f32_16x16x32_bf16 v[100:103], v[24:27], v[82:85], 0\n"
        "s_waitcnt lgkmcnt(7)\n"
        "s_mov_b32 s36, s35\n"
        "v_smfmac_f32_16x16x64_bf16 v[100:103], v[140:143], v[62:69], v18 abid:1\n"
        "s_mov_b64 exec, s[62:63]\n"
        "v_add_u32 v16, s29, v16\n"
        "v_add_u32 v8, s34, v8\n"
        "s_mov_b64 exec, -1\n"
        "v_mfma_f32_16x16x32_bf16 v[88:91], v[24:27], v[112:115], 0\n"
        "s_mov_b32 s35, s39\n"
        "v_smfmac_f32_16x16x64_bf16 v[88:91], v[140:143], v[144:151], v18 abid:1\n"
        "s_mov_b64 exec, s[50:51]\n"
        "v_add_u32 v5, s29, v5\n"
        "v_add_u32 v6, s34, v6\n"
        "s_mov_b64 exec, -1\n"
        "s_setprio 0\n"
        "CNEXTB_%=:\n"
        "s_cmp_ge_u32 s31, s14\n"
        "s_cbranch_scc1 CEND_%=\n"
        "s_cmp_gt_i32 s29, 0\n"
        "s_cbranch_scc1 CTOP_%=\n"
        "s_branch CFULL_%=\n"
        "CEND_%=:\n"
        "s_waitcnt lgkmcnt(0)\n"
        "s_nop 15\n"
        "v_cvt_pk_bf16_f32 v248, v20, v160\n"
        "v_cvt_pk_bf16_f32 v249, v96, v128\n"
        "v_cvt_pk_bf16_f32 v250, v92, v120\n"
        "s_add_u32 s37, s36, 196224\n"
        "buffer_store_dwordx3 v[248:250], v12, s[8:11], s37 offen offset:384 sc0 sc1 nt\n"
        "v_cvt_pk_bf16_f32 v252, v21, v161\n"
        "v_cvt_pk_bf16_f32 v253, v97, v129\n"
        "v_cvt_pk_bf16_f32 v254, v93, v121\n"
        "s_add_u32 s37, s36, 196224\n"
        "buffer_store_dwordx3 v[252:254], v13, s[8:11], s37 offen offset:1152 sc0 sc1 nt\n"
        "v_cvt_pk_bf16_f32 v248, v22, v162\n"
        "v_cvt_pk_bf16_f32 v249, v98, v130\n"
        "v_cvt_pk_bf16_f32 v250, v94, v122\n"
        "s_add_u32 s37, s36, 294336\n"
        "buffer_store_dwordx3 v[248:250], v12, s[8:11], s37 offen offset:576 sc0 sc1 nt\n"
        "v_cvt_pk_bf16_f32 v252, v23, v163\n"
        "v_cvt_pk_bf16_f32 v253, v99, v131\n"
        "v_cvt_pk_bf16_f32 v254, v95, v123\n"
        "s_add_u32 s37, s36, 392448\n"
        "buffer_store_dwordx3 v[252:254], v12, s[8:11], s37 offen sc0 sc1 nt\n"
        "v_cvt_pk_bf16_f32 v248, v72, v132\n"
        "v_cvt_pk_bf16_f32 v249, v156, v124\n"
        "v_cvt_pk_bf16_f32 v250, v100, v88\n"
        "s_add_u32 s37, s36, 392448\n"
        "v_cvt_pk_bf16_f32 v252, v73, v133\n"
        "v_cvt_pk_bf16_f32 v253, v157, v125\n"
        "v_cvt_pk_bf16_f32 v254, v101, v89\n"
        "buffer_store_dwordx3 v[248:250], v15, s[8:11], s37 offen offset:768 sc0 sc1 nt\n"
        "s_add_u32 s37, s36, 490560\n"
        "v_cvt_pk_bf16_f32 v246, v74, v134\n"
        "v_cvt_pk_bf16_f32 v247, v158, v126\n"
        "v_cvt_pk_bf16_f32 v248, v102, v90\n"
        "buffer_store_dwordx3 v[252:254], v12, s[8:11], s37 offen offset:192 sc0 sc1 nt\n"
        "s_add_u32 s37, s36, 490560\n"
        "buffer_store_dwordx3 v[246:248], v14, s[8:11], s37 offen offset:960 sc0 sc1 nt\n"
        "s_add_u32 s37, s36, 588672\n"
        "v_cvt_pk_bf16_f32 v252, v75, v135\n"
        "v_cvt_pk_bf16_f32 v253, v159, v127\n"
        "v_cvt_pk_bf16_f32 v254, v103, v91\n"
        "buffer_store_dwordx3 v[252:254], v12, s[8:11], s37 offen offset:384 sc0 sc1 nt\n"
        "s_nop 1\n"
        "s_endpgm\n"
        "L34A_%=:\n"
        "s_nop 7\n"
        "v_mov_b32 v166, 21600\n"
        "L32A_%=:\n"
        "ds_read_b32 v167, v166 offset:64\n"
        "s_waitcnt lgkmcnt(0)\n"
        "v_readfirstlane_b32 s40, v167\n"
        "s_nop 3\n"
        "s_cmp_ge_i32 s40, 1\n"
        "s_cbranch_scc1 L33A_%=\n"
        "s_sleep 1\n"
        "s_sub_u32 s41, s41, 1\n"
        "s_cbranch_scc0 L32A_%=\n"
        "s_endpgm\n"
        "L33A_%=:\n"
        "v_mbcnt_lo_u32_b32 v167, -1, 0\n"
        "v_lshl_add_u32 v166, v167, 2, v166\n"
        "ds_read_b32 v167, v166 offset:68\n"
        "s_mov_b32 s14, s40\n"
        "s_mov_b32 s76, -1\n"
        "s_waitcnt lgkmcnt(0)\n"
        "v_readlane_b32 s22, v167, 0\n"
        "v_readlane_b32 s23, v167, 1\n"
        "s_nop 3\n"
        "s_add_u32 s22, s22, s17\n"
        "s_add_u32 s23, s23, s17\n"
        "s_nop 1\n"
        "s_branch L35A_%=\n"
        "L38A_%=:\n"
        "s_nop 7\n"
        "L36A_%=:\n"
        "ds_read_b32 v167, v0 offset:21604\n"
        "s_waitcnt lgkmcnt(0)\n"
        "v_readfirstlane_b32 s40, v167\n"
        "s_nop 3\n"
        "s_cmp_ge_i32 s40, s68\n"
        "s_cbranch_scc1 L37A_%=\n"
        "s_sleep 1\n"
        "s_sub_u32 s41, s41, 1\n"
        "s_cbranch_scc0 L36A_%=\n"
        "s_endpgm\n"
        "L37A_%=:\n"
        "s_nop 1\n"
        "s_branch L39A_%=\n"
        "L34B_%=:\n"
        "s_nop 7\n"
        "v_mov_b32 v166, 21600\n"
        "L32B_%=:\n"
        "ds_read_b32 v167, v166 offset:64\n"
        "s_waitcnt lgkmcnt(0)\n"
        "v_readfirstlane_b32 s40, v167\n"
        "s_nop 3\n"
        "s_cmp_ge_i32 s40, 1\n"
        "s_cbranch_scc1 L33B_%=\n"
        "s_sleep 1\n"
        "s_sub_u32 s41, s41, 1\n"
        "s_cbranch_scc0 L32B_%=\n"
        "s_endpgm\n"
        "L33B_%=:\n"
        "v_mbcnt_lo_u32_b32 v167, -1, 0\n"
        "v_lshl_add_u32 v166, v167, 2, v166\n"
        "ds_read_b32 v167, v166 offset:68\n"
        "s_mov_b32 s14, s40\n"
        "s_mov_b32 s76, -1\n"
        "s_waitcnt lgkmcnt(0)\n"
        "v_readlane_b32 s22, v167, 0\n"
        "v_readlane_b32 s23, v167, 1\n"
        "s_nop 3\n"
        "s_add_u32 s22, s22, s17\n"
        "s_add_u32 s23, s23, s17\n"
        "s_nop 1\n"
        "s_branch L35B_%=\n"
        "L38B_%=:\n"
        "s_nop 7\n"
        "L36B_%=:\n"
        "ds_read_b32 v167, v0 offset:21600\n"
        "s_waitcnt lgkmcnt(0)\n"
        "v_readfirstlane_b32 s40, v167\n"
        "s_nop 3\n"
        "s_cmp_ge_i32 s40, s68\n"
        "s_cbranch_scc1 L37B_%=\n"
        "s_sleep 1\n"
        "s_sub_u32 s41, s41, 1\n"
        "s_cbranch_scc0 L36B_%=\n"
        "s_endpgm\n"
        "L37B_%=:\n"
        "s_nop 1\n"
        "s_branch L39B_%=\n"
        "CGRP_%=:\n"
        "s_setprio 3\n"
        "s_waitcnt lgkmcnt(0)\n"
        "s_nop 6\n"
        "ds_read_u16 v152, v0 offset:20576\n"
        "s_sub_u32 s60, s73, 102048\n"
        "ds_read_u16 v154, v0 offset:20578\n"
        "ds_read_u16 v150, v0 offset:20580\n"
        "ds_read_u16 v155, v0 offset:20582\n"
        "ds_read_u16 v149, v0 offset:20588\n"
        "ds_read_u16 v151, v0 offset:20590\n"
        "v_lshrrev_b32 v153, 8, v0\n"
        "s_waitcnt lgkmcnt(5)\n"
        "v_add_u32 v152, s60, v152\n"
        "v_subrev_u32 v148, 8320, v152\n"
        "v_min_u32 v152, v152, v148\n"
        "v_add_u32 v152, 101856, v152\n"
        "ds_read_b128 v[24:27], v152 offset:64\n"
        "ds_read_b128 v[44:47], v152 offset:128\n"
        "ds_read_b128 v[48:51], v152\n"
        "s_waitcnt lgkmcnt(1)\n"
        "s_waitcnt lgkmcnt(0)\n"
        "v_mfma_f32_16x16x32_bf16 v[56:59], v[168:171], v[48:51], 0\n"
        "v_add_u32 v154, s60, v154\n"
        "v_subrev_u32 v148, 8320, v154\n"
        "v_mfma_f32_16x16x32_bf16 v[56:59], v[172:175], v[24:27], v[56:59]\n"
        "v_min_u32 v154, v154, v148\n"
        "v_add_u32 v154, 101856, v154\n"
        "v_mfma_f32_16x16x32_bf16 v[56:59], v[176:179], v[44:47], v[56:59]\n"
        "v_add_u32 v150, s19, v150\n"
        "v_mad_i32_i24 v153, v153, -8, v150\n"
        "v_add_u32 v151, s19, v151\n"
        "v_add_u32 v155, s19, v155\n"
        "v_mfma_f32_16x16x32_bf16 v[64:67], v[180:183], v[48:51], 0\n"
        "v_mfma_f32_16x16x32_bf16 v[64:67], v[184:187], v[24:27], v[64:67]\n"
        "v_mfma_f32_16x16x32_bf16 v[64:67], v[188:191], v[44:47], v[64:67]\n"
        "v_mfma_f32_16x16x32_bf16 v[60:63], v[192:195], v[48:51], 0\n"
        "v_cvt_pk_bf16_f32 v52, v56, v57\n"
        "v_cvt_pk_bf16_f32 v53, v58, v59\n"
        "v_mfma_f32_16x16x32_bf16 v[60:63], v[196:199], v[24:27], v[60:63]\n"
        "v_mfma_f32_16x16x32_bf16 v[60:63], v[200:203], v[44:47], v[60:63]\n"
        "v_mfma_f32_16x16x32_bf16 v[56:59], v[204:207], v[48:51], 0\n"
        "v_mfma_f32_16x16x32_bf16 v[56:59], v[208:211], v[24:27], v[56:59]\n"
        "v_mfma_f32_16x16x32_bf16 v[56:59], v[212:215], v[44:47], v[56:59]\n"
        "v_cvt_pk_bf16_f32 v54, v64, v65\n"
        "v_cvt_pk_bf16_f32 v55, v66, v67\n"
        "ds_write2_b32 v153, v52, v54 offset0:0 offset1:1\n"
        "v_mfma_f32_16x16x32_bf16 v[68:71], v[216:219], v[48:51], 0\n"
        "v_mfma_f32_16x16x32_bf16 v[68:71], v[220:223], v[24:27], v[68:71]\n"
        "v_cvt_pk_bf16_f32 v64, v60, v61\n"
        "v_cvt_pk_bf16_f32 v65, v62, v63\n"
        "v_mfma_f32_16x16x32_bf16 v[68:71], v[224:227], v[44:47], v[68:71]\n"
        "v_cvt_pk_bf16_f32 v66, v56, v57\n"
        "v_cvt_pk_bf16_f32 v67, v58, v59\n"
        "v_mov_b32 v56, 0x3f803f80\n"
        "v_mov_b32 v57, 0x3f803f80\n"
        "v_mov_b32 v58, 0x3f803f80\n"
        "v_mov_b32 v59, 0x3f803f80\n"
        "v_mfma_f32_16x16x32_bf16 v[80:83], v[228:231], v[48:51], 0\n"
        "v_mfma_f32_16x16x32_bf16 v[80:83], v[232:235], v[24:27], v[80:83]\n"
        "v_cvt_pk_bf16_f32 v76, v68, v69\n"
        "v_cvt_pk_bf16_f32 v77, v70, v71\n"
        "v_mfma_f32_16x16x32_bf16 v[80:83], v[236:239], v[44:47], v[80:83]\n"
        "ds_write2_b32 v153, v53, v55 offset0:16 offset1:17\n"
        "ds_write2_b32 v153, v64, v66 offset0:8 offset1:9\n"
        "ds_write2_b32 v153, v65, v67 offset0:24 offset1:25\n"
        "s_nop 4\n"
        "v_cvt_pk_bf16_f32 v79, v82, v83\n"
        "v_cvt_pk_bf16_f32 v78, v80, v81\n"
        "v_mfma_f32_16x16x32_bf16 v[60:63], v[48:51], v[52:55], 0\n"
        "v_mfma_f32_16x16x32_bf16 v[60:63], v[24:27], v[64:67], v[60:63]\n"
        "ds_read_b64 v[152:153], v149\n"
        "v_mfma_f32_16x16x32_bf16 v[60:63], v[44:47], v[76:79], v[60:63]\n"
        "ds_read_b64 v[144:145], v154\n"
        "ds_read_b64 v[146:147], v154 offset:16\n"
        "ds_write_b128 v150, v[76:79] offset:128\n"
        "v_mfma_f32_16x16x32_bf16 v[68:71], v[56:59], v[48:51], 0\n"
        "ds_read_b64 v[148:149], v154 offset:32\n"
        "v_mfma_f32_16x16x32_bf16 v[68:71], v[56:59], v[24:27], v[68:71]\n"
        "v_mfma_f32_16x16x32_bf16 v[68:71], v[56:59], v[44:47], v[68:71]\n"
        "s_waitcnt lgkmcnt(3)\n"
        "v_mfma_f32_4x4x4_16b_bf16 v[64:67], v[144:145], v[152:153], 0\n"
        "s_waitcnt lgkmcnt(1)\n"
        "v_mfma_f32_4x4x4_16b_bf16 v[56:59], v[146:147], v[152:153], 0\n"
        "ds_read_b128 v[84:87], v0 offset:4112\n"
        "ds_read_b128 v[76:79], v0 offset:5136\n"
        "s_waitcnt lgkmcnt(2)\n"
        "v_mfma_f32_4x4x4_16b_bf16 v[52:55], v[148:149], v[152:153], 0\n"
        "s_mov_b32 vcc_lo, 0x22222222\n"
        "s_mov_b32 vcc_hi, 0\n"
        "v_cndmask_b32 v150, v60, v61, vcc\n"
        "v_mfma_f32_16x16x32_bf16 v[108:111], v[48:51], v[240:243], 0\n"
        "s_mov_b32 vcc_lo, 0x44444444\n"
        "s_mov_b32 vcc_hi, 0\n"
        "v_cndmask_b32 v150, v150, v62, vcc\n"
        "v_mfma_f32_16x16x32_bf16 v[108:111], v[24:27], v[244:247], v[108:111]\n"
        "ds_read_b128 v[104:107], v0 offset:6176\n"
        "v_mfma_f32_16x16x32_bf16 v[108:111], v[44:47], v[248:251], v[108:111]\n"
        "ds_read_b128 v[112:115], v0 offset:7200\n"
        "ds_read_b128 v[80:83], v0 offset:8224\n"
        "s_mov_b32 vcc_lo, 0x88888888\n"
        "s_mov_b32 vcc_hi, 0\n"
        "v_cndmask_b32 v150, v150, v63, vcc\n"
        "s_mov_b64 exec, 0x00f0000f\n"
        "ds_write2_b32 v151, v150, v68 offset0:0 offset1:1\n"
        "s_mov_b64 exec, -1\n"
        "s_waitcnt lgkmcnt(4)\n"
        "v_mfma_f32_16x16x32_bf16 v[60:63], v[48:51], v[252:255], 0\n"
        "v_cvt_pk_bf16_f32 v142, v108, v64\n"
        "v_cvt_pk_bf16_f32 v148, v109, v65\n"
        "v_mfma_f32_16x16x32_bf16 v[60:63], v[24:27], v[84:87], v[60:63]\n"
        "ds_read_b64 v[144:145], v154 offset:48\n"
        "v_cvt_pk_bf16_f32 v146, v110, v66\n"
        "v_mfma_f32_16x16x32_bf16 v[60:63], v[44:47], v[76:79], v[60:63]\n"
        "ds_read_b64 v[140:141], v154 offset:64\n"
        "v_cvt_pk_bf16_f32 v150, v111, v67\n"
        "s_waitcnt lgkmcnt(3)\n"
        "v_mfma_f32_16x16x32_bf16 v[76:79], v[48:51], v[104:107], 0\n"
        "ds_read_b64 v[118:119], v154 offset:80\n"
        "v_mfma_f32_16x16x32_bf16 v[76:79], v[24:27], v[112:115], v[76:79]\n"
        "ds_read_b128 v[68:71], v0 offset:9264\n"
        "v_mfma_f32_16x16x32_bf16 v[76:79], v[44:47], v[80:83], v[76:79]\n"
        "ds_read_b128 v[84:87], v0 offset:10288\n"
        "ds_read_b128 v[80:83], v0 offset:11312\n"
        "v_cvt_pk_bf16_f32 v143, v60, v56\n"
        "v_cvt_pk_bf16_f32 v149, v61, v57\n"
        "v_cvt_pk_bf16_f32 v147, v62, v58\n"
        "v_cvt_pk_bf16_f32 v151, v63, v59\n"
        "ds_write_b64 v155, v[142:143]\n"
        "s_waitcnt lgkmcnt(5)\n"
        "v_mfma_f32_4x4x4_16b_bf16 v[64:67], v[144:145], v[152:153], 0\n"
        "v_mfma_f32_4x4x4_16b_bf16 v[60:63], v[140:141], v[152:153], 0\n"
        "ds_read_b128 v[112:115], v0 offset:12352\n"
        "ds_read_b128 v[108:111], v0 offset:13376\n"
        "ds_read_b128 v[104:107], v0 offset:14400\n"
        "s_waitcnt lgkmcnt(7)\n"
        "v_mfma_f32_4x4x4_16b_bf16 v[56:59], v[118:119], v[152:153], 0\n"
        "v_cvt_pk_bf16_f32 v140, v76, v52\n"
        "v_cvt_pk_bf16_f32 v142, v77, v53\n"
        "v_cvt_pk_bf16_f32 v144, v78, v54\n"
        "v_cvt_pk_bf16_f32 v152, v79, v55\n"
        "s_waitcnt lgkmcnt(4)\n"
        "v_mfma_f32_16x16x32_bf16 v[116:119], v[48:51], v[68:71], 0\n"
        "v_mfma_f32_16x16x32_bf16 v[116:119], v[24:27], v[84:87], v[116:119]\n"
        "ds_read_b128 v[52:55], v0 offset:15440\n"
        "v_mfma_f32_16x16x32_bf16 v[116:119], v[44:47], v[80:83], v[116:119]\n"
        "ds_read_b128 v[68:71], v0 offset:16464\n"
        "ds_read_b128 v[76:79], v0 offset:17488\n"
        "ds_write_b64 v155, v[148:149] offset:392\n"
        "ds_write_b64 v155, v[146:147] offset:784\n"
        "s_waitcnt lgkmcnt(5)\n"
        "v_mfma_f32_16x16x32_bf16 v[80:83], v[48:51], v[112:115], 0\n"
        "v_mfma_f32_16x16x32_bf16 v[80:83], v[24:27], v[108:111], v[80:83]\n"
        "v_mfma_f32_16x16x32_bf16 v[80:83], v[44:47], v[104:107], v[80:83]\n"
        "v_cvt_pk_bf16_f32 v141, v116, v64\n"
        "v_cvt_pk_bf16_f32 v143, v117, v65\n"
        "v_cvt_pk_bf16_f32 v145, v118, v66\n"
        "v_cvt_pk_bf16_f32 v153, v119, v67\n"
        "s_waitcnt lgkmcnt(2)\n"
        "v_mfma_f32_16x16x32_bf16 v[64:67], v[48:51], v[52:55], 0\n"
        "v_mfma_f32_16x16x32_bf16 v[64:67], v[24:27], v[68:71], v[64:67]\n"
        "v_mfma_f32_16x16x32_bf16 v[64:67], v[44:47], v[76:79], v[64:67]\n"
        "v_cvt_pk_bf16_f32 v148, v80, v60\n"
        "v_cvt_pk_bf16_f32 v146, v81, v61\n"
        "v_cvt_pk_bf16_f32 v118, v82, v62\n"
        "v_cvt_pk_bf16_f32 v116, v83, v63\n"
        "ds_write_b64 v155, v[150:151] offset:1176\n"
        "ds_write_b64 v155, v[140:141] offset:8\n"
        "ds_write_b64 v155, v[142:143] offset:400\n"
        "ds_write_b64 v155, v[144:145] offset:792\n"
        "v_cvt_pk_bf16_f32 v149, v64, v56\n"
        "v_cvt_pk_bf16_f32 v147, v65, v57\n"
        "v_cvt_pk_bf16_f32 v119, v66, v58\n"
        "v_cvt_pk_bf16_f32 v117, v67, v59\n"
        "ds_write_b64 v155, v[152:153] offset:1184\n"
        "ds_write_b64 v155, v[148:149] offset:16\n"
        "ds_write_b64 v155, v[146:147] offset:408\n"
        "ds_write_b64 v155, v[118:119] offset:800\n"
        "ds_write_b64 v155, v[116:117] offset:1192\n"
        "s_waitcnt lgkmcnt(0)\n"
        "s_nop 6\n"
        "s_setprio 0\n"
        "s_setpc_b64 s[64:65]\n"
        "L50A_%=:\n"
        "s_nop 7\n"
        "v_mov_b32 v166, 21600\n"
        "L48A_%=:\n"
        "ds_read_b32 v167, v166 offset:64\n"
        "s_waitcnt lgkmcnt(0)\n"
        "v_readfirstlane_b32 s40, v167\n"
        "s_nop 3\n"
        "s_cmp_ge_i32 s40, 1\n"
        "s_cbranch_scc1 L49A_%=\n"
        "s_sleep 1\n"
        "s_sub_u32 s41, s41, 1\n"
        "s_cbranch_scc0 L48A_%=\n"
        "s_endpgm\n"
        "L49A_%=:\n"
        "v_mbcnt_lo_u32_b32 v167, -1, 0\n"
        "v_lshl_add_u32 v166, v167, 2, v166\n"
        "ds_read_b32 v167, v166 offset:68\n"
        "s_mov_b32 s14, s40\n"
        "s_mov_b32 s76, -1\n"
        "s_waitcnt lgkmcnt(0)\n"
        "v_readlane_b32 s22, v167, 0\n"
        "v_readlane_b32 s23, v167, 1\n"
        "s_nop 3\n"
        "s_add_u32 s22, s22, s17\n"
        "s_add_u32 s23, s23, s17\n"
        "s_nop 1\n"
        "s_branch L51A_%=\n"
        "L54A_%=:\n"
        "s_nop 7\n"
        "L52A_%=:\n"
        "ds_read_b32 v167, v0 offset:21604\n"
        "s_waitcnt lgkmcnt(0)\n"
        "v_readfirstlane_b32 s40, v167\n"
        "s_nop 3\n"
        "s_cmp_ge_i32 s40, s68\n"
        "s_cbranch_scc1 L53A_%=\n"
        "s_sleep 1\n"
        "s_sub_u32 s41, s41, 1\n"
        "s_cbranch_scc0 L52A_%=\n"
        "s_endpgm\n"
        "L53A_%=:\n"
        "s_nop 1\n"
        "s_branch L55A_%=\n"
        "L50B_%=:\n"
        "s_nop 7\n"
        "v_mov_b32 v166, 21600\n"
        "L48B_%=:\n"
        "ds_read_b32 v167, v166 offset:64\n"
        "s_waitcnt lgkmcnt(0)\n"
        "v_readfirstlane_b32 s40, v167\n"
        "s_nop 3\n"
        "s_cmp_ge_i32 s40, 1\n"
        "s_cbranch_scc1 L49B_%=\n"
        "s_sleep 1\n"
        "s_sub_u32 s41, s41, 1\n"
        "s_cbranch_scc0 L48B_%=\n"
        "s_endpgm\n"
        "L49B_%=:\n"
        "v_mbcnt_lo_u32_b32 v167, -1, 0\n"
        "v_lshl_add_u32 v166, v167, 2, v166\n"
        "ds_read_b32 v167, v166 offset:68\n"
        "s_mov_b32 s14, s40\n"
        "s_mov_b32 s76, -1\n"
        "s_waitcnt lgkmcnt(0)\n"
        "v_readlane_b32 s22, v167, 0\n"
        "v_readlane_b32 s23, v167, 1\n"
        "s_nop 3\n"
        "s_add_u32 s22, s22, s17\n"
        "s_add_u32 s23, s23, s17\n"
        "s_nop 1\n"
        "s_branch L51B_%=\n"
        "L54B_%=:\n"
        "s_nop 7\n"
        "L52B_%=:\n"
        "ds_read_b32 v167, v0 offset:21600\n"
        "s_waitcnt lgkmcnt(0)\n"
        "v_readfirstlane_b32 s40, v167\n"
        "s_nop 3\n"
        "s_cmp_ge_i32 s40, s68\n"
        "s_cbranch_scc1 L53B_%=\n"
        "s_sleep 1\n"
        "s_sub_u32 s41, s41, 1\n"
        "s_cbranch_scc0 L52B_%=\n"
        "s_endpgm\n"
        "L53B_%=:\n"
        "s_nop 1\n"
        "s_branch L55B_%=\n"
        "CREDOA_%=:\n"
        "s_waitcnt lgkmcnt(0)\n"
        "s_nop 15\n"
        "s_mov_b32 s36, 0x20000000\n"
        "ds_read_b128 v[36:39], v7 offset:0\n"
        "ds_read_b128 v[28:31], v7 offset:3328\n"
        "ds_read_b128 v[40:43], v7 offset:64\n"
        "ds_read_b128 v[32:35], v7 offset:3392\n"
        "ds_read_b128 v[164:167], v7 offset:128\n"
        "ds_read_b128 v[136:139], v7 offset:3456\n"
        "s_waitcnt lgkmcnt(0)\n"
        "s_branch MTOPA_%=\n"
        "CREDOB_%=:\n"
        "s_waitcnt lgkmcnt(0)\n"
        "s_nop 15\n"
        "s_mov_b32 s36, 0x20000000\n"
        "ds_read_b128 v[36:39], v7 offset:40064\n"
        "ds_read_b128 v[28:31], v7 offset:43392\n"
        "ds_read_b128 v[40:43], v7 offset:40128\n"
        "ds_read_b128 v[32:35], v7 offset:43456\n"
        "ds_read_b128 v[164:167], v7 offset:40192\n"
        "ds_read_b128 v[136:139], v7 offset:43520\n"
        "s_waitcnt lgkmcnt(0)\n"
        "s_branch MTOPB_%=\n"
        "MTOPA_%=:\n"
        "s_waitcnt lgkmcnt(5)\n"
        "v_mfma_f32_16x16x32_bf16 v[24:27], v[168:171], v[36:39], 0\n"
        "v_cvt_pk_bf16_f32 v114, v20, v160\n"
        "v_cvt_pk_bf16_f32 v115, v96, v128\n"
        "v_cvt_pk_bf16_f32 v116, v92, v120\n"
        "v_cvt_pk_bf16_f32 v118, v21, v161\n"
        "v_cvt_pk_bf16_f32 v119, v97, v129\n"
        "v_cvt_pk_bf16_f32 v120, v93, v121\n"
        "v_cvt_pk_bf16_f32 v128, v22, v162\n"
        "v_cvt_pk_bf16_f32 v129, v98, v130\n"
        "s_waitcnt lgkmcnt(3)\n"
        "v_mfma_f32_16x16x32_bf16 v[24:27], v[172:175], v[40:43], v[24:27]\n"
        "v_cvt_pk_bf16_f32 v130, v94, v122\n"
        "v_cvt_pk_bf16_f32 v140, v23, v163\n"
        "v_cvt_pk_bf16_f32 v141, v99, v131\n"
        "v_cvt_pk_bf16_f32 v142, v95, v123\n"
        "v_cvt_pk_bf16_f32 v144, v72, v132\n"
        "v_cvt_pk_bf16_f32 v145, v156, v124\n"
        "v_cvt_pk_bf16_f32 v146, v100, v88\n"
        "v_cvt_pk_bf16_f32 v148, v73, v133\n"
        "s_waitcnt lgkmcnt(1)\n"
        "v_mfma_f32_16x16x32_bf16 v[24:27], v[176:179], v[164:167], v[24:27]\n"
        "v_cvt_pk_bf16_f32 v149, v157, v125\n"
        "v_cvt_pk_bf16_f32 v150, v101, v89\n"
        "v_cvt_pk_bf16_f32 v152, v74, v134\n"
        "v_cvt_pk_bf16_f32 v153, v158, v126\n"
        "v_cvt_pk_bf16_f32 v154, v102, v90\n"
        "v_cvt_pk_bf16_f32 v156, v75, v135\n"
        "v_cvt_pk_bf16_f32 v157, v159, v127\n"
        "v_cvt_pk_bf16_f32 v158, v103, v91\n"
        "v_mfma_f32_16x16x32_bf16 v[20:23], v[188:191], v[164:167], 0\n"
        "v_cvt_pk_bf16_f32 v56, v24, v25\n"
        "v_cvt_pk_bf16_f32 v57, v26, v27\n"
        "v_mfma_f32_16x16x32_bf16 v[20:23], v[184:187], v[40:43], v[20:23]\n"
        "v_mfma_f32_16x16x32_bf16 v[20:23], v[180:183], v[36:39], v[20:23]\n"
        "s_add_u32 s37, s36, 196224\n"
        "ds_read_b128 v[52:55], v16 offset:128\n"
        "ds_read_b32 v159, v10\n"
        "v_mfma_f32_16x16x32_bf16 v[24:27], v[192:195], v[36:39], 0\n"
        "ds_read_b128 v[44:47], v5\n"
        "v_mfma_f32_16x16x32_bf16 v[24:27], v[196:199], v[40:43], v[24:27]\n"
        "ds_read_b64 v[160:161], v11 offset:192\n"
        "v_mfma_f32_16x16x32_bf16 v[24:27], v[200:203], v[164:167], v[24:27]\n"
        "buffer_store_dwordx3 v[114:116], v12, s[8:11], s37 offen offset:384 sc0 sc1 nt\n"
        "v_cvt_pk_bf16_f32 v58, v20, v21\n"
        "v_cvt_pk_bf16_f32 v59, v22, v23\n"
        "s_waitcnt lgkmcnt(3)\n"
        "v_mfma_f32_16x16x32_bf16 v[68:71], v[52:55], v[164:167], 0\n"
        "ds_read_b128 v[64:67], v7 offset:6784\n"
        "ds_read_i16 v163, v0 offset:20584\n"
        "ds_read_i16 v151, v0 offset:20586\n"
        "v_cvt_pk_bf16_f32 v60, v24, v25\n"
        "v_cvt_pk_bf16_f32 v61, v26, v27\n"
        "s_waitcnt lgkmcnt(3)\n"
        "v_smfmac_f32_16x16x64_bf16 v[68:71], v[44:47], v[36:43], v4\n"
        "s_add_u32 s37, s36, 196224\n"
        "ds_read_b128 v[20:23], v7 offset:6656\n"
        "ds_read_b128 v[24:27], v7 offset:6720\n"
        "v_mfma_f32_16x16x32_bf16 v[72:75], v[52:55], v[136:139], 0\n"
        "v_smfmac_f32_16x16x64_bf16 v[72:75], v[44:47], v[28:35], v4\n"
        "s_waitcnt lgkmcnt(4)\n"
        "v_mfma_f32_16x16x32_bf16 v[48:51], v[52:55], v[64:67], 0\n"
        "buffer_store_dwordx3 v[118:120], v13, s[8:11], s37 offen offset:1152 sc0 sc1 nt\n"
        "v_add_f32_dpp v161, v70, v71 quad_perm:[0,1,2,3] row_mask:0x1 bank_mask:0xf\n"
        "s_waitcnt lgkmcnt(2)\n"
        "v_add_u32 v163, v9, v163\n"
        "v_add_f32_dpp v160, v68, v69 quad_perm:[0,1,2,3] row_mask:0x1 bank_mask:0xf\n"
        "v_add_u32 v151, v9, v151\n"
        "v_add_f32_dpp v161, v74, v75 quad_perm:[0,1,2,3] row_mask:0x2 bank_mask:0xf\n"
        "v_add_f32_dpp v160, v72, v73 quad_perm:[0,1,2,3] row_mask:0x2 bank_mask:0xf\n"
        "s_waitcnt lgkmcnt(0)\n"
        "v_smfmac_f32_16x16x64_bf16 v[48:51], v[44:47], v[20:27], v4\n"
        "v_mfma_f32_16x16x32_bf16 v[68:71], v[212:215], v[164:167], 0\n"
        "s_add_u32 s37, s36, 294336\n"
        "v_mfma_f32_16x16x32_bf16 v[68:71], v[208:211], v[40:43], v[68:71]\n"
        "v_mfma_f32_16x16x32_bf16 v[68:71], v[204:207], v[36:39], v[68:71]\n"
        "v_mfma_f32_16x16x32_bf16 v[52:55], v[216:219], v[36:39], 0\n"
        "buffer_store_dwordx3 v[128:130], v12, s[8:11], s37 offen offset:576 sc0 sc1 nt\n"
        "s_setprio 2\n"
        "v_mfma_f32_16x16x32_bf16 v[52:55], v[220:223], v[40:43], v[52:55]\n"
        "v_add_f32_dpp v161, v50, v51 quad_perm:[0,1,2,3] row_mask:0x4 bank_mask:0xf\n"
        "v_mul_f32 v162, s52, v161\n"
        "v_fma_f32 v162, -v162, v161, v159\n"
        "v_add_f32_e64 v162, s53, |v162|\n"
        "v_cvt_pk_bf16_f32 v62, v68, v69\n"
        "v_cvt_pk_bf16_f32 v63, v70, v71\n"
        "v_rsq_f32 v159, v162\n"
        "v_mfma_f32_16x16x32_bf16 v[52:55], v[224:227], v[164:167], v[52:55]\n"
        "s_add_u32 s37, s36, 392448\n"
        "v_add_f32_dpp v160, v48, v49 quad_perm:[0,1,2,3] row_mask:0x4 bank_mask:0xf\n"
        "v_mul_f32 v160, v160, v159\n"
        "v_readlane_b32 s58, v159, 56\n"
        "s_mov_b32 exec_hi, 0xffff\n"
        "ds_write_b32 v10, v159\n"
        "s_mov_b32 exec_hi, -1\n"
        "v_mfma_f32_16x16x32_bf16 v[68:71], v[236:239], v[164:167], 0\n"
        "s_mov_b64 exec, s[56:57]\n"
        "v_mov_b32 v160, 0xff800000\n"
        "s_mov_b64 exec, -1\n"
        "v_cvt_pk_bf16_f32 v72, v52, v53\n"
        "v_cvt_pk_bf16_f32 v73, v54, v55\n"
        "ds_read_b32 v44, v163 offset:0\n"
        "ds_read_b32 v45, v163 offset:832\n"
        "v_max_f32_dpp v155, v160, v160 row_ror:8 row_mask:0xf bank_mask:0xf\n"
        "v_mfma_f32_16x16x32_bf16 v[68:71], v[232:235], v[40:43], v[68:71]\n"
        "s_mov_b32 exec_hi, 0xffff\n"
        "v_add_u32 v10, 40064, v10\n"
        "s_mov_b32 exec_hi, -1\n"
        "ds_read_b32 v46, v163 offset:1664\n"
        "ds_read_b32 v47, v163 offset:2496\n"
        "ds_read_b32 v48, v163 offset:3328\n"
        "ds_read_b32 v49, v163 offset:4160\n"
        "v_max_f32_dpp v155, v155, v155 row_ror:4 row_mask:0xf bank_mask:0xf\n"
        "v_mfma_f32_16x16x32_bf16 v[68:71], v[228:231], v[36:39], v[68:71]\n"
        "buffer_store_dwordx3 v[140:142], v12, s[8:11], s37 offen sc0 sc1 nt\n"
        "ds_read_b32 v50, v163 offset:4992\n"
        "ds_read_b32 v51, v163 offset:5824\n"
        "ds_read_b32 v52, v163 offset:6656\n"
        "ds_read_b32 v53, v163 offset:7488\n"
        "ds_read_b32 v54, v163 offset:8320\n"
        "ds_read_b32 v55, v163 offset:9152\n"
        "ds_read_b32 v161, v151 offset:0\n"
        "v_cvt_pk_bf16_f32 v74, v68, v69\n"
        "v_cvt_pk_bf16_f32 v75, v70, v71\n"
        "v_max_f32_dpp v155, v155, v155 row_ror:2 row_mask:0xf bank_mask:0xf\n"
        "v_mfma_f32_16x16x32_bf16 v[80:83], v[36:39], v[56:59], 0\n"
        "ds_read_b32 v162, v151 offset:3328\n"
        "v_max_f32_dpp v155, v155, v155 row_ror:1 row_mask:0xf bank_mask:0xf\n"
        "ds_read_b32 v163, v151 offset:6656\n"
        "v_mfma_f32_16x16x32_bf16 v[80:83], v[40:43], v[60:63], v[80:83]\n"
        "s_add_u32 s37, s36, 392448\n"
        "v_mfma_f32_16x16x32_bf16 v[80:83], v[164:167], v[72:75], v[80:83]\n"
        "v_mfma_f32_16x16x32_bf16 v[92:95], v[136:139], v[72:75], 0\n"
        "v_mfma_f32_16x16x32_bf16 v[92:95], v[32:35], v[60:63], v[92:95]\n"
        "buffer_store_dwordx3 v[144:146], v15, s[8:11], s37 offen offset:768 sc0 sc1 nt\n"
        "v_mfma_f32_16x16x32_bf16 v[92:95], v[28:31], v[56:59], v[92:95]\n"
        "ds_read_b128 v[68:71], v8 offset:128\n"
        "v_mfma_f32_16x16x32_bf16 v[76:79], v[20:23], v[56:59], 0\n"
        "s_add_u32 s37, s36, 490560\n"
        "ds_read2_b32 v[84:85], v6 offset0:0 offset1:2\n"
        "ds_read2_b32 v[86:87], v6 offset0:8 offset1:10\n"
        "s_waitcnt lgkmcnt(3)\n"
        "v_mul_f32 v80, v80, v44\n"
        "v_mul_f32 v81, v81, v45\n"
        "v_mul_f32 v82, v82, v46\n"
        "v_mul_f32 v83, v83, v47\n"
        "v_mul_f32 v92, v92, v48\n"
        "v_mfma_f32_16x16x32_bf16 v[76:79], v[24:27], v[60:63], v[76:79]\n"
        "v_max3_f32 v151, v80, v81, v82\n"
        "v_mul_f32 v93, v93, v49\n"
        "v_mul_f32 v94, v94, v50\n"
        "v_max3_f32 v151, v151, v83, v92\n"
        "v_mul_f32 v95, v95, v51\n"
        "v_max3_f32 v151, v151, v93, v94\n"
        "v_mfma_f32_16x16x32_bf16 v[76:79], v[64:67], v[72:75], v[76:79]\n"
        "s_waitcnt lgkmcnt(2)\n"
        "v_mfma_f32_16x16x32_bf16 v[96:99], v[68:71], v[72:75], 0\n"
        "buffer_store_dwordx3 v[148:150], v12, s[8:11], s37 offen offset:192 sc0 sc1 nt\n"
        "s_waitcnt lgkmcnt(0)\n"
        "v_smfmac_f32_16x16x64_bf16 v[96:99], v[84:87], v[56:63], v4\n"
        "v_mfma_f32_16x16x32_bf16 v[72:75], v[168:171], v[28:31], 0\n"
        "s_add_u32 s37, s36, 490560\n"
        "v_mfma_f32_16x16x32_bf16 v[72:75], v[172:175], v[32:35], v[72:75]\n"
        "v_mul_f32 v76, v76, v52\n"
        "v_mul_f32 v77, v77, v53\n"
        "v_mul_f32 v78, v78, v54\n"
        "v_max3_f32 v151, v151, v95, v76\n"
        "v_mul_f32 v79, v79, v55\n"
        "v_add_f32 v96, v96, v97\n"
        "v_mul_f32 v96, s58, v96\n"
        "v_max3_f32 v151, v151, v77, v78\n"
        "v_mfma_f32_16x16x32_bf16 v[72:75], v[176:179], v[136:139], v[72:75]\n"
        "s_mov_b64 exec, s[48:49]\n"
        "v_mov_b32 v96, 0xff800000\n"
        "s_mov_b64 exec, -1\n"
        "v_max3_f32 v151, v151, v79, v96\n"
        "v_mfma_f32_16x16x32_bf16 v[60:63], v[188:191], v[136:139], 0\n"
        "buffer_store_dwordx3 v[152:154], v14, s[8:11], s37 offen offset:960 sc0 sc1 nt\n"
        "v_permlane32_swap_b32 v151, v155\n"
        "v_max_f32 v151, v151, v155\n"
        "v_cvt_pk_bf16_f32 v56, v72, v73\n"
        "v_cvt_pk_bf16_f32 v57, v74, v75\n"
        "v_mov_b32 v155, v151\n"
        "v_mfma_f32_16x16x32_bf16 v[60:63], v[184:187], v[32:35], v[60:63]\n"
        "v_mfma_f32_16x16x32_bf16 v[60:63], v[180:183], v[28:31], v[60:63]\n"
        "s_add_u32 s37, s36, 588672\n"
        "v_permlane16_swap_b32 v151, v155\n"
        "v_max_f32 v151, v151, v155\n"
        "v_mov_b32 v155, v151\n"
        "v_mfma_f32_16x16x32_bf16 v[88:91], v[192:195], v[28:31], 0\n"
        "v_mfma_f32_16x16x32_bf16 v[88:91], v[196:199], v[32:35], v[88:91]\n"
        "v_permlane32_swap_b32 v151, v155\n"
        "v_mul_f32 v151, v151, v161\n"
        "v_cvt_pk_bf16_f32 v58, v60, v61\n"
        "v_cvt_pk_bf16_f32 v59, v62, v63\n"
        "v_fma_f32 v80, v80, v161, -v151\n"
        "v_fma_f32 v81, v81, v161, -v151\n"
        "v_fma_f32 v82, v82, v161, -v151\n"
        "v_mfma_f32_16x16x32_bf16 v[88:91], v[200:203], v[136:139], v[88:91]\n"
        "buffer_store_dwordx3 v[156:158], v12, s[8:11], s37 offen offset:384 sc0 sc1 nt\n"
        "v_exp_f32 v80, v80\n"
        "v_exp_f32 v81, v81\n"
        "v_exp_f32 v82, v82\n"
        "v_fma_f32 v83, v83, v161, -v151\n"
        "v_add_f32 v158, v80, v81\n"
        "v_exp_f32 v83, v83\n"
        "v_mfma_f32_16x16x32_bf16 v[72:75], v[212:215], v[136:139], 0\n"
        "v_cvt_pk_bf16_f32 v60, v88, v89\n"
        "v_cvt_pk_bf16_f32 v61, v90, v91\n"
        "v_fma_f32 v92, v92, v161, -v151\n"
        "v_add_f32 v158, v158, v82\n"
        "v_exp_f32 v92, v92\n"
        "v_fma_f32 v93, v93, v161, -v151\n"
        "v_add_f32 v158, v158, v83\n"
        "v_mfma_f32_16x16x32_bf16 v[72:75], v[208:211], v[32:35], v[72:75]\n"
        "v_exp_f32 v93, v93\n"
        "v_fma_f32 v94, v94, v161, -v151\n"
        "v_add_f32 v158, v158, v92\n"
        "v_exp_f32 v94, v94\n"
        "v_fma_f32 v95, v95, v161, -v151\n"
        "v_sub_f32 v160, v160, v155\n"
        "v_mfma_f32_16x16x32_bf16 v[72:75], v[204:207], v[28:31], v[72:75]\n"
        "v_add_f32 v158, v158, v93\n"
        "v_exp_f32 v95, v95\n"
        "v_fma_f32 v76, v76, v161, -v151\n"
        "v_mul_f32 v160, s58, v160\n"
        "v_add_f32 v158, v158, v94\n"
        "v_exp_f32 v76, v76\n"
        "v_mfma_f32_16x16x32_bf16 v[88:91], v[216:219], v[28:31], 0\n"
        "v_fma_f32 v77, v77, v161, -v151\n"
        "v_cvt_pk_bf16_f32 v62, v72, v73\n"
        "v_cvt_pk_bf16_f32 v63, v74, v75\n"
        "v_exp_f32 v160, v160\n"
        "v_add_f32 v158, v158, v95\n"
        "v_exp_f32 v77, v77\n"
        "v_mfma_f32_16x16x32_bf16 v[88:91], v[220:223], v[32:35], v[88:91]\n"
        "v_fma_f32 v78, v78, v161, -v151\n"
        "v_add_f32 v158, v158, v76\n"
        "v_exp_f32 v78, v78\n"
        "v_fma_f32 v79, v79, v161, -v151\n"
        "v_add_f32_dpp v157, v160, v160 row_ror:8 row_mask:0xf bank_mask:0xf\n"
        "v_add_f32 v158, v158, v77\n"
        "v_fma_f32 v96, v96, v161, -v151\n"
        "v_mfma_f32_16x16x32_bf16 v[88:91], v[224:227], v[136:139], v[88:91]\n"
        "v_exp_f32 v79, v79\n"
        "v_add_f32_dpp v157, v157, v157 row_ror:4 row_mask:0xf bank_mask:0xf\n"
        "v_add_f32 v158, v158, v78\n"
        "v_exp_f32 v96, v96\n"
        "v_add_f32_dpp v157, v157, v157 row_ror:2 row_mask:0xf bank_mask:0xf\n"
        "v_add_f32 v158, v158, v79\n"
        "v_mfma_f32_16x16x32_bf16 v[72:75], v[236:239], v[136:139], 0\n"
        "v_add_f32_dpp v157, v157, v157 row_ror:1 row_mask:0xf bank_mask:0xf\n"
        "v_cvt_pk_bf16_f32 v100, v88, v89\n"
        "v_cvt_pk_bf16_f32 v101, v90, v91\n"
        "v_add_f32 v158, v158, v96\n"
        "v_mul_f32 v80, v80, v44\n"
        "v_mul_f32 v81, v81, v45\n"
        "v_permlane32_swap_b32 v158, v157\n"
        "v_mfma_f32_16x16x32_bf16 v[72:75], v[232:235], v[32:35], v[72:75]\n"
        "v_add_f32 v158, v158, v157\n"
        "v_mov_b32 v161, v158\n"
        "v_mul_f32 v82, v82, v46\n"
        "v_mul_f32 v83, v83, v47\n"
        "v_permlane16_swap_b32 v158, v161\n"
        "v_add_f32 v158, v158, v161\n"
        "v_mul_f32 v92, v92, v48\n"
        "v_mfma_f32_16x16x32_bf16 v[72:75], v[228:231], v[28:31], v[72:75]\n"
        "v_rcp_f32 v158, v158\n"
        "v_mul_f32 v93, v93, v49\n"
        "v_mov_b32 v157, v158\n"
        "v_mul_f32 v94, v94, v50\n"
        "v_mul_f32 v95, v95, v51\n"
        "v_permlane32_swap_b32 v158, v157\n"
        "v_mfma_f32_16x16x32_bf16 v[88:91], v[36:39], v[56:59], 0\n"
        "v_mul_f32 v80, v80, v158\n"
        "v_cvt_pk_bf16_f32 v102, v72, v73\n"
        "v_cvt_pk_bf16_f32 v103, v74, v75\n"
        "v_mul_f32 v81, v81, v158\n"
        "v_mul_f32 v82, v82, v158\n"
        "v_mul_f32 v83, v83, v158\n"
        "v_mul_f32 v92, v92, v158\n"
        "v_mul_f32 v93, v93, v158\n"
        "v_mfma_f32_16x16x32_bf16 v[88:91], v[40:43], v[60:63], v[88:91]\n"
        "v_mul_f32 v94, v94, v158\n"
        "v_mul_f32 v95, v95, v158\n"
        "v_cvt_pk_bf16_f32 v72, v80, v81\n"
        "v_cvt_pk_bf16_f32 v73, v82, v83\n"
        "v_cvt_pk_bf16_f32 v74, v92, v93\n"
        "v_cvt_pk_bf16_f32 v75, v94, v95\n"
        "v_mul_f32 v76, v76, v52\n"
        "v_mul_f32 v77, v77, v53\n"
        "v_mfma_f32_16x16x32_bf16 v[88:91], v[164:167], v[100:103], v[88:91]\n"
        "v_mul_f32 v78, v78, v54\n"
        "v_mul_f32 v79, v79, v55\n"
        "v_mul_f32 v96, s58, v96\n"
        "v_mul_f32 v76, v76, v158\n"
        "v_mul_f32 v77, v77, v158\n"
        "v_mul_f32 v78, v78, v158\n"
        "v_mul_f32 v79, v79, v158\n"
        "v_fma_f32 v96, v96, v158, v19\n"
        "v_mfma_f32_16x16x32_bf16 v[108:111], v[136:139], v[100:103], 0\n"
        "v_mul_f32 v88, v88, v44\n"
        "v_mul_f32 v89, v89, v45\n"
        "v_mul_f32 v90, v90, v46\n"
        "v_mul_f32 v91, v91, v47\n"
        "v_max3_f32 v161, v88, v89, v90\n"
        "v_cvt_pk_bf16_f32 v92, v76, v77\n"
        "v_cvt_pk_bf16_f32 v94, v78, v79\n"
        "v_cvt_pk_bf16_f32 v93, v96, 0\n"
        "v_mfma_f32_16x16x32_bf16 v[108:111], v[32:35], v[60:63], v[108:111]\n"
        "v_mul_f32 v160, v160, v159\n"
        "v_mul_f32 v160, v160, v157\n"
        "v_cvt_pk_bf16_f32 v160, v160, 0\n"
        "ds_write_b16 v1, v160\n"
        "v_mfma_f32_16x16x32_bf16 v[108:111], v[28:31], v[56:59], v[108:111]\n"
        "v_mfma_f32_16x16x32_bf16 v[96:99], v[20:23], v[56:59], 0\n"
        "v_mfma_f32_16x16x32_bf16 v[96:99], v[24:27], v[60:63], v[96:99]\n"
        "v_mfma_f32_16x16x32_bf16 v[96:99], v[64:67], v[100:103], v[96:99]\n"
        "v_mfma_f32_16x16x32_bf16 v[104:107], v[68:71], v[100:103], 0\n"
        "v_smfmac_f32_16x16x64_bf16 v[104:107], v[84:87], v[56:63], v4\n"
        "v_mfma_f32_16x16x32_bf16 v[80:83], v[168:171], v[20:23], 0\n"
        "v_mfma_f32_16x16x32_bf16 v[80:83], v[172:175], v[24:27], v[80:83]\n"
        "v_mfma_f32_16x16x32_bf16 v[80:83], v[176:179], v[64:67], v[80:83]\n"
        "v_mul_f32 v108, v108, v48\n"
        "v_mul_f32 v109, v109, v49\n"
        "v_mul_f32 v110, v110, v50\n"
        "v_max3_f32 v161, v161, v91, v108\n"
        "v_mul_f32 v111, v111, v51\n"
        "v_add_f32 v104, v104, v105\n"
        "v_mul_f32 v96, v96, v52\n"
        "v_mul_f32 v104, s58, v104\n"
        "v_mfma_f32_16x16x32_bf16 v[56:59], v[188:191], v[64:67], 0\n"
        "s_mov_b64 exec, s[48:49]\n"
        "v_mov_b32 v104, 0xff800000\n"
        "s_mov_b64 exec, -1\n"
        "v_cvt_pk_bf16_f32 v76, v80, v81\n"
        "v_cvt_pk_bf16_f32 v77, v82, v83\n"
        "v_max3_f32 v161, v161, v109, v110\n"
        "v_mul_f32 v97, v97, v53\n"
        "v_mul_f32 v98, v98, v54\n"
        "v_max3_f32 v161, v161, v111, v96\n"
        "v_mfma_f32_16x16x32_bf16 v[56:59], v[184:187], v[24:27], v[56:59]\n"
        "v_mul_f32 v99, v99, v55\n"
        "v_max3_f32 v161, v161, v97, v98\n"
        "v_max3_f32 v161, v161, v99, v104\n"
        "v_mfma_f32_16x16x32_bf16 v[56:59], v[180:183], v[20:23], v[56:59]\n"
        "v_mfma_f32_16x16x32_bf16 v[100:103], v[192:195], v[20:23], 0\n"
        "v_mfma_f32_16x16x32_bf16 v[100:103], v[196:199], v[24:27], v[100:103]\n"
        "v_mfma_f32_16x16x32_bf16 v[100:103], v[200:203], v[64:67], v[100:103]\n"
        "v_mfma_f32_16x16x32_bf16 v[112:115], v[212:215], v[64:67], 0\n"
        "v_mfma_f32_16x16x32_bf16 v[112:115], v[208:211], v[24:27], v[112:115]\n"
        "v_mfma_f32_16x16x32_bf16 v[112:115], v[204:207], v[20:23], v[112:115]\n"
        "v_mfma_f32_16x16x32_bf16 v[60:63], v[216:219], v[20:23], 0\n"
        "v_mfma_f32_16x16x32_bf16 v[60:63], v[220:223], v[24:27], v[60:63]\n"
        "v_cvt_pk_bf16_f32 v78, v56, v57\n"
        "v_cvt_pk_bf16_f32 v79, v58, v59\n"
        "v_mfma_f32_16x16x32_bf16 v[60:63], v[224:227], v[64:67], v[60:63]\n"
        "v_cvt_pk_bf16_f32 v80, v100, v101\n"
        "v_cvt_pk_bf16_f32 v81, v102, v103\n"
        "v_mfma_f32_16x16x32_bf16 v[56:59], v[236:239], v[64:67], 0\n"
        "v_cvt_pk_bf16_f32 v82, v112, v113\n"
        "v_cvt_pk_bf16_f32 v83, v114, v115\n"
        "v_mfma_f32_16x16x32_bf16 v[56:59], v[232:235], v[24:27], v[56:59]\n"
        "v_mfma_f32_16x16x32_bf16 v[56:59], v[228:231], v[20:23], v[56:59]\n"
        "v_mfma_f32_16x16x32_bf16 v[120:123], v[36:39], v[76:79], 0\n"
        "v_cvt_pk_bf16_f32 v100, v60, v61\n"
        "v_cvt_pk_bf16_f32 v101, v62, v63\n"
        "v_mfma_f32_16x16x32_bf16 v[120:123], v[40:43], v[80:83], v[120:123]\n"
        "s_nop 3\n"
        "v_cvt_pk_bf16_f32 v102, v56, v57\n"
        "v_cvt_pk_bf16_f32 v103, v58, v59\n"
        "s_nop 1\n"
        "v_mfma_f32_16x16x32_bf16 v[120:123], v[164:167], v[100:103], v[120:123]\n"
        "v_mfma_f32_16x16x32_bf16 v[128:131], v[136:139], v[100:103], 0\n"
        "v_mfma_f32_16x16x32_bf16 v[128:131], v[32:35], v[80:83], v[128:131]\n"
        "v_mfma_f32_16x16x32_bf16 v[128:131], v[28:31], v[76:79], v[128:131]\n"
        "v_mfma_f32_16x16x32_bf16 v[112:115], v[20:23], v[76:79], 0\n"
        "v_mfma_f32_16x16x32_bf16 v[112:115], v[24:27], v[80:83], v[112:115]\n"
        "v_mfma_f32_16x16x32_bf16 v[112:115], v[64:67], v[100:103], v[112:115]\n"
        "v_mfma_f32_16x16x32_bf16 v[60:63], v[68:71], v[100:103], 0\n"
        "v_smfmac_f32_16x16x64_bf16 v[60:63], v[84:87], v[76:83], v4\n"
        "v_mul_f32 v120, v120, v44\n"
        "v_mul_f32 v121, v121, v45\n"
        "v_mul_f32 v122, v122, v46\n"
        "v_mul_f32 v123, v123, v47\n"
        "v_mul_f32 v128, v128, v48\n"
        "v_max3_f32 v158, v120, v121, v122\n"
        "v_mul_f32 v129, v129, v49\n"
        "v_mfma_f32_16x16x32_bf16 v[68:71], v[36:39], v[240:243], 0\n"
        "v_add_f32 v60, v60, v61\n"
        "v_mul_f32 v130, v130, v50\n"
        "v_max3_f32 v158, v158, v123, v128\n"
        "v_mul_f32 v131, v131, v51\n"
        "v_mul_f32 v112, v112, v52\n"
        "v_mul_f32 v60, s58, v60\n"
        "v_max3_f32 v158, v158, v129, v130\n"
        "v_mul_f32 v113, v113, v53\n"
        "v_mfma_f32_16x16x32_bf16 v[68:71], v[40:43], v[244:247], v[68:71]\n"
        "s_mov_b64 exec, s[48:49]\n"
        "v_mov_b32 v60, 0xff800000\n"
        "s_mov_b64 exec, -1\n"
        "v_mul_f32 v114, v114, v54\n"
        "v_max3_f32 v158, v158, v131, v112\n"
        "v_mul_f32 v115, v115, v55\n"
        "v_max3_f32 v158, v158, v113, v114\n"
        "v_max3_f32 v158, v158, v115, v60\n"
        "v_mfma_f32_16x16x32_bf16 v[68:71], v[164:167], v[248:251], v[68:71]\n"
        "ds_read_b128 v[20:23], v17 offset:0\n"
        "v_permlane32_swap_b32 v161, v158\n"
        "v_max_f32 v161, v161, v158\n"
        "v_mov_b32 v160, v161\n"
        "v_mfma_f32_16x16x32_bf16 v[64:67], v[136:139], v[248:251], 0\n"
        "ds_read_b128 v[24:27], v17 offset:64\n"
        "v_permlane16_swap_b32 v161, v160\n"
        "v_max_f32 v161, v161, v160\n"
        "v_cvt_pk_bf16_f32 v56, v68, v69\n"
        "v_cvt_pk_bf16_f32 v57, v70, v71\n"
        "v_mov_b32 v158, v161\n"
        "v_mfma_f32_16x16x32_bf16 v[64:67], v[32:35], v[244:247], v[64:67]\n"
        "ds_read_b128 v[116:119], v17 offset:128\n"
        "v_permlane32_swap_b32 v161, v158\n"
        "v_mul_f32 v161, v161, v162\n"
        "v_mul_f32 v158, v158, v163\n"
        "v_fma_f32 v88, v88, v162, -v161\n"
        "v_fma_f32 v89, v89, v162, -v161\n"
        "v_fma_f32 v120, v120, v163, -v158\n"
        "v_mfma_f32_16x16x32_bf16 v[64:67], v[28:31], v[240:243], v[64:67]\n"
        "s_waitcnt lgkmcnt(2)\n"
        "ds_read_b128 v[84:87], v0 offset:5136\n"
        "v_fma_f32 v121, v121, v163, -v158\n"
        "v_exp_f32 v88, v88\n"
        "v_exp_f32 v89, v89\n"
        "v_fma_f32 v90, v90, v162, -v161\n"
        "v_fma_f32 v122, v122, v163, -v158\n"
        "v_mfma_f32_16x16x32_bf16 v[68:71], v[20:23], v[240:243], 0\n"
        "s_waitcnt lgkmcnt(2)\n"
        "ds_read_b128 v[100:103], v0 offset:4112\n"
        "v_cvt_pk_bf16_f32 v58, v64, v65\n"
        "v_cvt_pk_bf16_f32 v59, v66, v67\n"
        "v_exp_f32 v120, v120\n"
        "v_exp_f32 v121, v121\n"
        "v_fma_f32 v91, v91, v162, -v161\n"
        "v_mfma_f32_16x16x32_bf16 v[68:71], v[24:27], v[244:247], v[68:71]\n"
        "s_waitcnt lgkmcnt(2)\n"
        "v_exp_f32 v90, v90\n"
        "v_exp_f32 v122, v122\n"
        "v_fma_f32 v123, v123, v163, -v158\n"
        "v_add_f32 v159, v88, v89\n"
        "v_exp_f32 v91, v91\n"
        "v_mfma_f32_16x16x32_bf16 v[68:71], v[116:119], v[248:251], v[68:71]\n"
        "s_waitcnt lgkmcnt(1)\n"
        "v_fma_f32 v108, v108, v162, -v161\n"
        "v_add_f32 v160, v120, v121\n"
        "v_exp_f32 v123, v123\n"
        "v_fma_f32 v128, v128, v163, -v158\n"
        "v_add_f32 v159, v159, v90\n"
        "v_exp_f32 v108, v108\n"
        "v_mfma_f32_16x16x32_bf16 v[124:127], v[116:119], v[84:87], 0\n"
        "s_waitcnt lgkmcnt(0)\n"
        "v_cvt_pk_bf16_f32 v64, v68, v69\n"
        "v_cvt_pk_bf16_f32 v68, v70, v71\n"
        "v_fma_f32 v109, v109, v162, -v161\n"
        "v_add_f32 v160, v160, v122\n"
        "v_exp_f32 v128, v128\n"
        "v_fma_f32 v129, v129, v163, -v158\n"
        "v_add_f32 v159, v159, v91\n"
        "v_mfma_f32_16x16x32_bf16 v[124:127], v[24:27], v[100:103], v[124:127]\n"
        "v_exp_f32 v109, v109\n"
        "v_fma_f32 v110, v110, v162, -v161\n"
        "v_add_f32 v160, v160, v123\n"
        "v_exp_f32 v129, v129\n"
        "v_fma_f32 v130, v130, v163, -v158\n"
        "v_add_f32 v159, v159, v108\n"
        "v_mfma_f32_16x16x32_bf16 v[124:127], v[20:23], v[252:255], v[124:127]\n"
        "v_exp_f32 v110, v110\n"
        "v_fma_f32 v111, v111, v162, -v161\n"
        "v_add_f32 v160, v160, v128\n"
        "v_exp_f32 v130, v130\n"
        "v_fma_f32 v131, v131, v163, -v158\n"
        "v_add_f32 v159, v159, v109\n"
        "v_mfma_f32_16x16x32_bf16 v[140:143], v[28:31], v[252:255], 0\n"
        "v_exp_f32 v111, v111\n"
        "v_cvt_pk_bf16_f32 v76, v124, v125\n"
        "v_cvt_pk_bf16_f32 v80, v126, v127\n"
        "v_fma_f32 v96, v96, v162, -v161\n"
        "v_add_f32 v160, v160, v129\n"
        "v_exp_f32 v131, v131\n"
        "v_mfma_f32_16x16x32_bf16 v[140:143], v[32:35], v[100:103], v[140:143]\n"
        "v_fma_f32 v112, v112, v163, -v158\n"
        "v_add_f32 v159, v159, v110\n"
        "v_exp_f32 v96, v96\n"
        "v_fma_f32 v97, v97, v162, -v161\n"
        "v_add_f32 v160, v160, v130\n"
        "v_exp_f32 v112, v112\n"
        "v_mfma_f32_16x16x32_bf16 v[140:143], v[136:139], v[84:87], v[140:143]\n"
        "ds_read_b128 v[144:147], v0 offset:6176\n"
        "v_fma_f32 v113, v113, v163, -v158\n"
        "v_add_f32 v159, v159, v111\n"
        "v_exp_f32 v97, v97\n"
        "v_fma_f32 v98, v98, v162, -v161\n"
        "v_add_f32 v160, v160, v131\n"
        "v_fma_f32 v114, v114, v163, -v158\n"
        "v_mfma_f32_16x16x32_bf16 v[148:151], v[164:167], v[84:87], 0\n"
        "ds_read_b128 v[132:135], v0 offset:7200\n"
        "v_cvt_pk_bf16_f32 v86, v140, v141\n"
        "v_cvt_pk_bf16_f32 v87, v142, v143\n"
        "v_exp_f32 v113, v113\n"
        "v_add_f32 v159, v159, v96\n"
        "v_exp_f32 v98, v98\n"
        "v_mfma_f32_16x16x32_bf16 v[148:151], v[40:43], v[100:103], v[148:151]\n"
        "ds_read_b128 v[140:143], v0 offset:8224\n"
        "v_fma_f32 v99, v99, v162, -v161\n"
        "v_add_f32 v160, v160, v112\n"
        "v_exp_f32 v114, v114\n"
        "v_fma_f32 v115, v115, v163, -v158\n"
        "v_add_f32 v159, v159, v97\n"
        "v_fma_f32 v104, v104, v162, -v161\n"
        "v_mfma_f32_16x16x32_bf16 v[148:151], v[36:39], v[252:255], v[148:151]\n"
        "s_waitcnt lgkmcnt(2)\n"
        "v_exp_f32 v99, v99\n"
        "v_add_f32 v160, v160, v113\n"
        "v_exp_f32 v115, v115\n"
        "v_fma_f32 v60, v60, v163, -v158\n"
        "v_add_f32 v159, v159, v98\n"
        "v_add_f32 v160, v160, v114\n"
        "v_mfma_f32_16x16x32_bf16 v[124:127], v[36:39], v[144:147], 0\n"
        "s_waitcnt lgkmcnt(1)\n"
        "v_cvt_pk_bf16_f32 v84, v148, v149\n"
        "v_cvt_pk_bf16_f32 v85, v150, v151\n"
        "v_exp_f32 v104, v104\n"
        "v_exp_f32 v60, v60\n"
        "v_add_f32 v159, v159, v99\n"
        "v_add_f32 v160, v160, v115\n"
        "v_mfma_f32_16x16x32_bf16 v[124:127], v[40:43], v[132:135], v[124:127]\n"
        "s_waitcnt lgkmcnt(0)\n"
        "v_add_f32 v159, v159, v104\n"
        "v_add_f32 v160, v160, v60\n"
        "v_mul_f32 v88, v88, v44\n"
        "v_mul_f32 v89, v89, v45\n"
        "v_permlane32_swap_b32 v159, v160\n"
        "v_add_f32 v159, v159, v160\n"
        "v_mov_b32 v163, v159\n"
        "v_mfma_f32_16x16x32_bf16 v[124:127], v[164:167], v[140:143], v[124:127]\n"
        "v_mul_f32 v90, v90, v46\n"
        "v_permlane16_swap_b32 v159, v163\n"
        "v_add_f32 v159, v159, v163\n"
        "v_rcp_f32 v159, v159\n"
        "v_mul_f32 v91, v91, v47\n"
        "v_mov_b32 v160, v159\n"
        "v_mfma_f32_16x16x32_bf16 v[152:155], v[136:139], v[140:143], 0\n"
        "v_mul_f32 v108, v108, v48\n"
        "v_cvt_pk_bf16_f32 v100, v124, v125\n"
        "v_cvt_pk_bf16_f32 v101, v126, v127\n"
        "v_permlane32_swap_b32 v159, v160\n"
        "v_mul_f32 v109, v109, v49\n"
        "v_mul_f32 v110, v110, v50\n"
        "v_mul_f32 v111, v111, v51\n"
        "v_mfma_f32_16x16x32_bf16 v[152:155], v[32:35], v[132:135], v[152:155]\n"
        "v_mul_f32 v88, v88, v159\n"
        "v_mul_f32 v89, v89, v159\n"
        "v_mul_f32 v90, v90, v159\n"
        "v_mul_f32 v91, v91, v159\n"
        "v_mul_f32 v108, v108, v159\n"
        "v_mul_f32 v109, v109, v159\n"
        "v_mul_f32 v110, v110, v159\n"
        "v_mul_f32 v111, v111, v159\n"
        "v_mfma_f32_16x16x32_bf16 v[152:155], v[28:31], v[144:147], v[152:155]\n"
        "ds_read_b128 v[148:151], v0 offset:11312\n"
        "v_cvt_pk_bf16_f32 v124, v88, v89\n"
        "v_cvt_pk_bf16_f32 v125, v90, v91\n"
        "v_cvt_pk_bf16_f32 v126, v108, v109\n"
        "v_cvt_pk_bf16_f32 v127, v110, v111\n"
        "v_mul_f32 v96, v96, v52\n"
        "v_mul_f32 v97, v97, v53\n"
        "v_mul_f32 v98, v98, v54\n"
        "v_mfma_f32_16x16x32_bf16 v[108:111], v[20:23], v[144:147], 0\n"
        "ds_read_b128 v[88:91], v0 offset:10288\n"
        "v_cvt_pk_bf16_f32 v102, v152, v153\n"
        "v_cvt_pk_bf16_f32 v103, v154, v155\n"
        "v_mul_f32 v99, v99, v55\n"
        "v_mul_f32 v104, s58, v104\n"
        "v_mul_f32 v96, v96, v159\n"
        "v_mul_f32 v97, v97, v159\n"
        "v_mul_f32 v98, v98, v159\n"
        "v_mfma_f32_16x16x32_bf16 v[108:111], v[24:27], v[132:135], v[108:111]\n"
        "ds_read_b128 v[144:147], v0 offset:9264\n"
        "v_mul_f32 v99, v99, v159\n"
        "v_fma_f32 v104, v104, v159, v19\n"
        "v_cvt_pk_bf16_f32 v132, v96, v97\n"
        "v_cvt_pk_bf16_f32 v134, v98, v99\n"
        "v_cvt_pk_bf16_f32 v133, v104, 0\n"
        "v_mul_f32 v120, v120, v44\n"
        "v_mul_f32 v121, v121, v45\n"
        "v_mfma_f32_16x16x32_bf16 v[108:111], v[116:119], v[140:143], v[108:111]\n"
        "s_waitcnt lgkmcnt(2)\n"
        "v_mul_f32 v122, v122, v46\n"
        "v_mul_f32 v123, v123, v47\n"
        "v_mul_f32 v128, v128, v48\n"
        "v_mul_f32 v129, v129, v49\n"
        "v_mul_f32 v130, v130, v50\n"
        "v_mul_f32 v131, v131, v51\n"
        "v_mul_f32 v120, v120, v160\n"
        "v_cvt_pk_bf16_f32 v44, v108, v109\n"
        "v_mfma_f32_16x16x32_bf16 v[96:99], v[116:119], v[148:151], 0\n"
        "s_waitcnt lgkmcnt(1)\n"
        "v_cvt_pk_bf16_f32 v48, v110, v111\n"
        "v_mul_f32 v121, v121, v160\n"
        "v_mul_f32 v122, v122, v160\n"
        "v_mul_f32 v123, v123, v160\n"
        "v_mul_f32 v128, v128, v160\n"
        "v_mul_f32 v129, v129, v160\n"
        "v_mul_f32 v130, v130, v160\n"
        "v_mul_f32 v131, v131, v160\n"
        "v_mfma_f32_16x16x32_bf16 v[96:99], v[24:27], v[88:91], v[96:99]\n"
        "s_waitcnt lgkmcnt(0)\n"
        "v_cvt_pk_bf16_f32 v108, v120, v121\n"
        "v_cvt_pk_bf16_f32 v109, v122, v123\n"
        "v_cvt_pk_bf16_f32 v110, v128, v129\n"
        "v_cvt_pk_bf16_f32 v111, v130, v131\n"
        "v_mul_f32 v112, v112, v52\n"
        "v_mul_f32 v113, v113, v53\n"
        "v_mul_f32 v114, v114, v54\n"
        "v_mul_f32 v115, v115, v55\n"
        "v_mfma_f32_16x16x32_bf16 v[96:99], v[20:23], v[144:147], v[96:99]\n"
        "v_mul_f32 v60, s58, v60\n"
        "v_mul_f32 v112, v112, v160\n"
        "v_mul_f32 v113, v113, v160\n"
        "v_mul_f32 v114, v114, v160\n"
        "v_mul_f32 v115, v115, v160\n"
        "v_fma_f32 v60, v60, v160, v19\n"
        "v_cvt_pk_bf16_f32 v52, v112, v113\n"
        "v_cvt_pk_bf16_f32 v54, v114, v115\n"
        "v_mfma_f32_16x16x32_bf16 v[104:107], v[28:31], v[144:147], 0\n"
        "v_cvt_pk_bf16_f32 v65, v96, v97\n"
        "v_cvt_pk_bf16_f32 v69, v98, v99\n"
        "v_cvt_pk_bf16_f32 v53, v60, 0\n"
        "v_mfma_f32_16x16x32_bf16 v[104:107], v[32:35], v[88:91], v[104:107]\n"
        "v_mfma_f32_16x16x32_bf16 v[104:107], v[136:139], v[148:151], v[104:107]\n"
        "ds_read_b128 v[112:115], v0 offset:12352\n"
        "v_mfma_f32_16x16x32_bf16 v[96:99], v[164:167], v[148:151], 0\n"
        "ds_read_b128 v[120:123], v0 offset:13376\n"
        "v_mfma_f32_16x16x32_bf16 v[96:99], v[40:43], v[88:91], v[96:99]\n"
        "ds_read_b128 v[128:131], v0 offset:14400\n"
        "v_mfma_f32_16x16x32_bf16 v[96:99], v[36:39], v[144:147], v[96:99]\n"
        "s_waitcnt lgkmcnt(2)\n"
        "v_mfma_f32_16x16x32_bf16 v[144:147], v[36:39], v[112:115], 0\n"
        "v_cvt_pk_bf16_f32 v62, v104, v105\n"
        "v_cvt_pk_bf16_f32 v63, v106, v107\n"
        "s_waitcnt lgkmcnt(1)\n"
        "v_mfma_f32_16x16x32_bf16 v[144:147], v[40:43], v[120:123], v[144:147]\n"
        "s_waitcnt lgkmcnt(0)\n"
        "v_mfma_f32_16x16x32_bf16 v[144:147], v[164:167], v[128:131], v[144:147]\n"
        "v_cvt_pk_bf16_f32 v60, v96, v97\n"
        "v_cvt_pk_bf16_f32 v61, v98, v99\n"
        "v_mfma_f32_16x16x32_bf16 v[148:151], v[136:139], v[128:131], 0\n"
        "v_mfma_f32_16x16x32_bf16 v[148:151], v[32:35], v[120:123], v[148:151]\n"
        "v_mfma_f32_16x16x32_bf16 v[148:151], v[28:31], v[112:115], v[148:151]\n"
        "ds_read_b128 v[104:107], v0 offset:17488\n"
        "v_mfma_f32_16x16x32_bf16 v[140:143], v[20:23], v[112:115], 0\n"
        "ds_read_b128 v[96:99], v0 offset:16464\n"
        "v_cvt_pk_bf16_f32 v88, v144, v145\n"
        "v_cvt_pk_bf16_f32 v89, v146, v147\n"
        "v_mfma_f32_16x16x32_bf16 v[140:143], v[24:27], v[120:123], v[140:143]\n"
        "ds_read_b128 v[112:115], v0 offset:15440\n"
        "v_mfma_f32_16x16x32_bf16 v[140:143], v[116:119], v[128:131], v[140:143]\n"
        "v_cvt_pk_bf16_f32 v90, v148, v149\n"
        "v_cvt_pk_bf16_f32 v91, v150, v151\n"
        "s_waitcnt lgkmcnt(2)\n"
        "v_mfma_f32_16x16x32_bf16 v[120:123], v[116:119], v[104:107], 0\n"
        "s_waitcnt lgkmcnt(1)\n"
        "v_mfma_f32_16x16x32_bf16 v[120:123], v[24:27], v[96:99], v[120:123]\n"
        "s_waitcnt lgkmcnt(0)\n"
        "v_mfma_f32_16x16x32_bf16 v[120:123], v[20:23], v[112:115], v[120:123]\n"
        "ds_read2_b32 v[70:71], v11 offset0:0 offset1:3\n"
        "v_cvt_pk_bf16_f32 v77, v140, v141\n"
        "v_cvt_pk_bf16_f32 v81, v142, v143\n"
        "v_mov_b32_dpp v95, v19 row_shr:4 row_mask:0xf bank_mask:0xf bound_ctrl:0\n"
        "v_mfma_f32_16x16x32_bf16 v[20:23], v[28:31], v[112:115], 0\n"
        "ds_read_b64_tr_b16 v[66:67], v9 offset:0\n"
        "v_mfma_f32_16x16x32_bf16 v[20:23], v[32:35], v[96:99], v[20:23]\n"
        "ds_read2_b32 v[82:83], v11 offset0:1 offset1:4\n"
        "v_cvt_pk_bf16_f32 v45, v120, v121\n"
        "v_cvt_pk_bf16_f32 v49, v122, v123\n"
        "v_mfma_f32_16x16x32_bf16 v[20:23], v[136:139], v[104:107], v[20:23]\n"
        "ds_read_b64_tr_b16 v[78:79], v9 offset:16\n"
        "v_mfma_f32_16x16x32_bf16 v[24:27], v[164:167], v[104:107], 0\n"
        "ds_read2_b32 v[50:51], v11 offset0:2 offset1:5\n"
        "v_mfma_f32_16x16x32_bf16 v[24:27], v[40:43], v[96:99], v[24:27]\n"
        "s_add_u32 s31, s31, 1\n"
        "ds_read_b64_tr_b16 v[46:47], v9 offset:32\n"
        "v_mfma_f32_16x16x32_bf16 v[24:27], v[36:39], v[112:115], v[24:27]\n"
        "s_mul_i32 s38, s31, 1344\n"
        "v_cvt_pk_bf16_f32 v106, v20, v21\n"
        "v_cvt_pk_bf16_f32 v107, v22, v23\n"
        "v_mfma_f32_16x16x32_bf16 v[20:23], v[72:75], v[56:59], 0\n"
        "s_waitcnt lgkmcnt(4)\n"
        "s_cmp_ge_u32 s31, s76\n"
        "v_smfmac_f32_16x16x64_bf16 v[20:23], v[92:95], v[64:71], v18\n"
        "s_cbranch_scc1 L50A_%=\n"
        "L51A_%=:\n"
        "v_cvt_pk_bf16_f32 v104, v24, v25\n"
        "v_cvt_pk_bf16_f32 v105, v26, v27\n"
        "v_mfma_f32_16x16x32_bf16 v[24:27], v[72:75], v[84:87], 0\n"
        "s_waitcnt lgkmcnt(2)\n"
        "s_cmp_ge_u32 s31, s71\n"
        "v_smfmac_f32_16x16x64_bf16 v[24:27], v[92:95], v[76:83], v18\n"
        "s_cselect_b32 s39, s70, s72\n"
        "ds_read_b64_tr_b16 v[66:67], v9 offset:48\n"
        "v_mfma_f32_16x16x32_bf16 v[28:31], v[72:75], v[100:103], 0\n"
        "s_waitcnt lgkmcnt(1)\n"
        "s_cmp_ge_u32 s31, 20\n"
        "v_smfmac_f32_16x16x64_bf16 v[28:31], v[92:95], v[44:51], v18\n"
        "s_cselect_b32 s39, s22, s39\n"
        "ds_read_b64_tr_b16 v[78:79], v9 offset:64\n"
        "v_cvt_pk_bf16_f32 v160, v20, v24\n"
        "v_cvt_pk_bf16_f32 v152, v21, v25\n"
        "v_cvt_pk_bf16_f32 v156, v22, v26\n"
        "v_cvt_pk_bf16_f32 v164, v23, v27\n"
        "v_mfma_f32_16x16x32_bf16 v[36:39], v[72:75], v[60:63], 0\n"
        "s_cmp_ge_u32 s31, 21\n"
        "s_waitcnt lgkmcnt(1)\n"
        "v_smfmac_f32_16x16x64_bf16 v[36:39], v[92:95], v[64:71], v18 abid:1\n"
        "s_cselect_b32 s39, s23, s39\n"
        "ds_read_b64_tr_b16 v[46:47], v9 offset:80\n"
        "v_mfma_f32_16x16x32_bf16 v[40:43], v[72:75], v[88:91], 0\n"
        "s_add_u32 s39, s39, s38\n"
        "ds_read_b128 v[20:23], v2\n"
        "v_mov_b64 v[24:25], 0\n"
        "v_mov_b64 v[26:27], 0\n"
        "s_waitcnt lgkmcnt(2)\n"
        "v_smfmac_f32_16x16x64_bf16 v[40:43], v[92:95], v[76:83], v18 abid:1\n"
        "s_mul_i32 s68, s31, 3\n"
        "ds_read_b128 v[32:35], v3\n"
        "v_cvt_pk_bf16_f32 v161, v28, v36\n"
        "v_cvt_pk_bf16_f32 v153, v29, v37\n"
        "v_cvt_pk_bf16_f32 v157, v30, v38\n"
        "v_cvt_pk_bf16_f32 v165, v31, v39\n"
        "v_mfma_f32_16x16x32_bf16 v[96:99], v[72:75], v[104:107], 0\n"
        "s_lshr_b32 s38, s31, 3\n"
        "v_mov_b64 v[28:29], 0\n"
        "v_mov_b64 v[30:31], 0\n"
        "s_waitcnt lgkmcnt(2)\n"
        "v_smfmac_f32_16x16x64_bf16 v[96:99], v[92:95], v[44:51], v18 abid:1\n"
        "s_add_u32 s38, s38, 11\n"
        "s_waitcnt lgkmcnt(1)\n"
        "v_mov_b32_dpp v135, v19 row_shr:4 row_mask:0xf bank_mask:0xf bound_ctrl:0\n"
        "v_smfmac_f32_16x16x64_bf16 v[24:27], v[20:23], v[56:63], v4\n"
        "s_bitcmp1_b32 s31, 0\n"
        "v_mov_b64 v[36:37], 0\n"
        "v_mov_b64 v[38:39], 0\n"
        "s_waitcnt lgkmcnt(0)\n"
        "v_smfmac_f32_16x16x64_bf16 v[24:27], v[32:35], v[64:71], v4 abid:1\n"
        "s_cselect_b32 s38, 3, s38\n"
        "s_add_u32 s37, s35, 0\n"
        "v_cvt_pk_bf16_f32 v162, v40, v96\n"
        "v_cvt_pk_bf16_f32 v154, v41, v97\n"
        "v_cvt_pk_bf16_f32 v158, v42, v98\n"
        "v_cvt_pk_bf16_f32 v166, v43, v99\n"
        "v_smfmac_f32_16x16x64_bf16 v[28:31], v[20:23], v[84:91], v4\n"
        "s_add_u32 s68, s68, s38\n"
        "v_add_f32 v24, v24, v25\n"
        "v_add_f32 v26, v26, v27\n"
        "v_smfmac_f32_16x16x64_bf16 v[28:31], v[32:35], v[76:83], v4 abid:1\n"
        "s_cmp_lt_u32 s31, s14\n"
        "ds_read_b64_tr_b16 v[66:67], v9 offset:3328\n"
        "v_smfmac_f32_16x16x64_bf16 v[36:39], v[20:23], v[100:107], v4\n"
        "buffer_store_dwordx3 v[160:162], v12, s[8:11], s37 offen sc0 sc1\n"
        "s_cselect_b32 s68, s68, 0\n"
        "v_smfmac_f32_16x16x64_bf16 v[36:39], v[32:35], v[44:51], v4 abid:1\n"
        "s_and_b32 s38, s31, 7\n"
        "ds_read_b64_tr_b16 v[78:79], v9 offset:3344\n"
        "v_add_f32 v28, v28, v29\n"
        "v_add_f32 v30, v30, v31\n"
        "v_cvt_pk_bf16_f32 v148, v24, v28\n"
        "v_mfma_f32_16x16x32_bf16 v[20:23], v[124:127], v[56:59], 0\n"
        "s_cselect_b32 s30, 1, -7\n"
        "s_waitcnt lgkmcnt(1)\n"
        "v_smfmac_f32_16x16x64_bf16 v[20:23], v[132:135], v[64:71], v18\n"
        "s_add_u32 s37, s35, 0\n"
        "s_mul_i32 s29, s30, 192\n"
        "ds_read_b64_tr_b16 v[46:47], v9 offset:3360\n"
        "v_add_f32 v36, v36, v37\n"
        "v_add_f32 v38, v38, v39\n"
        "v_cvt_pk_bf16_f32 v149, v36, v26\n"
        "v_cvt_pk_bf16_f32 v150, v30, v38\n"
        "v_mfma_f32_16x16x32_bf16 v[160:163], v[124:127], v[84:87], 0\n"
        "s_mul_i32 s30, s30, 392\n"
        "s_waitcnt lgkmcnt(1)\n"
        "v_smfmac_f32_16x16x64_bf16 v[160:163], v[132:135], v[76:83], v18\n"
        "s_cmp_eq_u32 s73, s18\n"
        "ds_read_b64_tr_b16 v[66:67], v9 offset:3376\n"
        "v_mfma_f32_16x16x32_bf16 v[96:99], v[124:127], v[100:103], 0\n"
        "buffer_store_dwordx3 v[152:154], v15, s[8:11], s37 offen offset:768 sc0 sc1\n"
        "s_cselect_b32 s34, -7488, s75\n"
        "s_waitcnt lgkmcnt(1)\n"
        "v_smfmac_f32_16x16x64_bf16 v[96:99], v[132:135], v[44:51], v18\n"
        "s_add_u32 s73, s73, s34\n"
        "ds_read_b64_tr_b16 v[78:79], v9 offset:3392\n"
        "v_mov_b32_dpp v55, v19 row_shr:4 row_mask:0xf bank_mask:0xf bound_ctrl:0\n"
        "v_writelane_b32 v10, s73, 56\n"
        "v_add_u32 v11, s30, v11\n"
        "v_mfma_f32_16x16x32_bf16 v[128:131], v[124:127], v[60:63], 0\n"
        "s_mov_b64 exec, s[62:63]\n"
        "v_add_u32 v16, s29, v16\n"
        "v_add_u32 v8, s34, v8\n"
        "s_mov_b64 exec, -1\n"
        "s_mov_b64 exec, s[50:51]\n"
        "v_add_u32 v5, s29, v5\n"
        "v_add_u32 v6, s34, v6\n"
        "s_mov_b64 exec, -1\n"
        "s_waitcnt lgkmcnt(1)\n"
        "v_smfmac_f32_16x16x64_bf16 v[128:131], v[132:135], v[64:71], v18 abid:1\n"
        "s_add_u32 s37, s35, 98112\n"
        "ds_read_b64_tr_b16 v[46:47], v9 offset:3408\n"
        "v_mfma_f32_16x16x32_bf16 v[92:95], v[124:127], v[88:91], 0\n"
        "s_waitcnt lgkmcnt(1)\n"
        "v_smfmac_f32_16x16x64_bf16 v[92:95], v[132:135], v[76:83], v18 abid:1\n"
        "ds_read_b64_tr_b16 v[66:67], v9 offset:6656\n"
        "v_mfma_f32_16x16x32_bf16 v[120:123], v[124:127], v[104:107], 0\n"
        "buffer_store_dwordx3 v[156:158], v12, s[8:11], s37 offen offset:192 sc0 sc1 nt\n"
        "ds_read_b32 v167, v0 offset:21604\n"
        "s_waitcnt lgkmcnt(2)\n"
        "v_smfmac_f32_16x16x64_bf16 v[120:123], v[132:135], v[44:51], v18 abid:1\n"
        "ds_read_b64_tr_b16 v[78:79], v9 offset:6672\n"
        "v_mfma_f32_16x16x32_bf16 v[72:75], v[108:111], v[56:59], 0\n"
        "s_waitcnt lgkmcnt(2)\n"
        "v_smfmac_f32_16x16x64_bf16 v[72:75], v[52:55], v[64:71], v18\n"
        "s_add_u32 s37, s35, 98112\n"
        "ds_read_b64_tr_b16 v[46:47], v9 offset:6688\n"
        "v_mfma_f32_16x16x32_bf16 v[132:135], v[108:111], v[84:87], 0\n"
        "s_waitcnt lgkmcnt(1)\n"
        "v_smfmac_f32_16x16x64_bf16 v[132:135], v[52:55], v[76:83], v18\n"
        "ds_read_b64_tr_b16 v[66:67], v9 offset:6704\n"
        "v_readfirstlane_b32 s40, v167\n"
        "v_mfma_f32_16x16x32_bf16 v[156:159], v[108:111], v[100:103], 0\n"
        "buffer_store_dwordx3 v[164:166], v14, s[8:11], s37 offen offset:960 sc0 sc1 nt\n"
        "s_add_u32 s37, s35, 588672\n"
        "s_add_u32 s46, s37, 1344\n"
        "s_cmp_lt_i32 s40, s68\n"
        "s_waitcnt lgkmcnt(1)\n"
        "v_smfmac_f32_16x16x64_bf16 v[156:159], v[52:55], v[44:51], v18\n"
        "buffer_store_dwordx3 v[148:150], v12, s[44:47], s37 offen offset:1152 sc0 sc1 nt\n"
        "ds_read_b64_tr_b16 v[78:79], v9 offset:6720\n"
        "v_mfma_f32_16x16x32_bf16 v[124:127], v[108:111], v[60:63], 0\n"
        "s_waitcnt lgkmcnt(1)\n"
        "v_smfmac_f32_16x16x64_bf16 v[124:127], v[52:55], v[64:71], v18 abid:1\n"
        "ds_read_b64_tr_b16 v[46:47], v9 offset:6736\n"
        "s_mov_b64 exec, 1\n"
        "ds_inc_u32 v0, v1 offset:21608\n"
        "s_mov_b64 exec, -1\n"
        "s_cbranch_scc1 L54A_%=\n"
        "L55A_%=:\n"
        "ds_read_b128 v[36:39], v7 offset:40064\n"
        "ds_read_b128 v[28:31], v7 offset:43392\n"
        "ds_read_b128 v[40:43], v7 offset:40128\n"
        "ds_read_b128 v[32:35], v7 offset:43456\n"
        "v_mfma_f32_16x16x32_bf16 v[100:103], v[108:111], v[88:91], 0\n"
        "s_waitcnt lgkmcnt(6)\n"
        "ds_read_b128 v[164:167], v7 offset:40192\n"
        "ds_read_b128 v[136:139], v7 offset:43520\n"
        "s_mov_b32 s36, s35\n"
        "v_smfmac_f32_16x16x64_bf16 v[100:103], v[52:55], v[76:83], v18 abid:1\n"
        "s_mov_b32 s35, s39\n"
        "v_mfma_f32_16x16x32_bf16 v[88:91], v[108:111], v[104:107], 0\n"
        "s_waitcnt lgkmcnt(7)\n"
        "v_smfmac_f32_16x16x64_bf16 v[88:91], v[52:55], v[44:51], v18 abid:1\n"
        "s_setprio 0\n"
        "s_waitcnt lgkmcnt(0)\n"
        "s_nop 15\n"
        "s_branch CNEXTA_%=\n"
        "MTOPB_%=:\n"
        "s_waitcnt lgkmcnt(5)\n"
        "v_mfma_f32_16x16x32_bf16 v[24:27], v[168:171], v[36:39], 0\n"
        "v_cvt_pk_bf16_f32 v114, v20, v160\n"
        "v_cvt_pk_bf16_f32 v115, v96, v128\n"
        "v_cvt_pk_bf16_f32 v116, v92, v120\n"
        "v_cvt_pk_bf16_f32 v118, v21, v161\n"
        "v_cvt_pk_bf16_f32 v119, v97, v129\n"
        "v_cvt_pk_bf16_f32 v120, v93, v121\n"
        "v_cvt_pk_bf16_f32 v128, v22, v162\n"
        "v_cvt_pk_bf16_f32 v129, v98, v130\n"
        "s_waitcnt lgkmcnt(3)\n"
        "v_mfma_f32_16x16x32_bf16 v[24:27], v[172:175], v[40:43], v[24:27]\n"
        "v_cvt_pk_bf16_f32 v130, v94, v122\n"
        "v_cvt_pk_bf16_f32 v140, v23, v163\n"
        "v_cvt_pk_bf16_f32 v141, v99, v131\n"
        "v_cvt_pk_bf16_f32 v142, v95, v123\n"
        "v_cvt_pk_bf16_f32 v144, v72, v132\n"
        "v_cvt_pk_bf16_f32 v145, v156, v124\n"
        "v_cvt_pk_bf16_f32 v146, v100, v88\n"
        "v_cvt_pk_bf16_f32 v148, v73, v133\n"
        "s_waitcnt lgkmcnt(1)\n"
        "v_mfma_f32_16x16x32_bf16 v[24:27], v[176:179], v[164:167], v[24:27]\n"
        "v_cvt_pk_bf16_f32 v149, v157, v125\n"
        "v_cvt_pk_bf16_f32 v150, v101, v89\n"
        "v_cvt_pk_bf16_f32 v152, v74, v134\n"
        "v_cvt_pk_bf16_f32 v153, v158, v126\n"
        "v_cvt_pk_bf16_f32 v154, v102, v90\n"
        "v_cvt_pk_bf16_f32 v156, v75, v135\n"
        "v_cvt_pk_bf16_f32 v157, v159, v127\n"
        "v_cvt_pk_bf16_f32 v158, v103, v91\n"
        "v_mfma_f32_16x16x32_bf16 v[20:23], v[188:191], v[164:167], 0\n"
        "v_cvt_pk_bf16_f32 v56, v24, v25\n"
        "v_cvt_pk_bf16_f32 v57, v26, v27\n"
        "v_mfma_f32_16x16x32_bf16 v[20:23], v[184:187], v[40:43], v[20:23]\n"
        "v_mfma_f32_16x16x32_bf16 v[20:23], v[180:183], v[36:39], v[20:23]\n"
        "s_add_u32 s37, s36, 196224\n"
        "ds_read_b128 v[52:55], v16 offset:128\n"
        "ds_read_b32 v159, v10\n"
        "v_mfma_f32_16x16x32_bf16 v[24:27], v[192:195], v[36:39], 0\n"
        "ds_read_b128 v[44:47], v5\n"
        "v_mfma_f32_16x16x32_bf16 v[24:27], v[196:199], v[40:43], v[24:27]\n"
        "ds_read_b64 v[160:161], v11 offset:192\n"
        "v_mfma_f32_16x16x32_bf16 v[24:27], v[200:203], v[164:167], v[24:27]\n"
        "buffer_store_dwordx3 v[114:116], v12, s[8:11], s37 offen offset:384 sc0 sc1 nt\n"
        "v_cvt_pk_bf16_f32 v58, v20, v21\n"
        "v_cvt_pk_bf16_f32 v59, v22, v23\n"
        "s_waitcnt lgkmcnt(3)\n"
        "v_mfma_f32_16x16x32_bf16 v[68:71], v[52:55], v[164:167], 0\n"
        "ds_read_b128 v[64:67], v7 offset:46848\n"
        "ds_read_i16 v163, v0 offset:20584\n"
        "ds_read_i16 v151, v0 offset:20586\n"
        "v_cvt_pk_bf16_f32 v60, v24, v25\n"
        "v_cvt_pk_bf16_f32 v61, v26, v27\n"
        "s_waitcnt lgkmcnt(3)\n"
        "v_smfmac_f32_16x16x64_bf16 v[68:71], v[44:47], v[36:43], v4\n"
        "s_add_u32 s37, s36, 196224\n"
        "ds_read_b128 v[20:23], v7 offset:46720\n"
        "ds_read_b128 v[24:27], v7 offset:46784\n"
        "v_mfma_f32_16x16x32_bf16 v[72:75], v[52:55], v[136:139], 0\n"
        "v_smfmac_f32_16x16x64_bf16 v[72:75], v[44:47], v[28:35], v4\n"
        "s_waitcnt lgkmcnt(4)\n"
        "v_mfma_f32_16x16x32_bf16 v[48:51], v[52:55], v[64:67], 0\n"
        "buffer_store_dwordx3 v[118:120], v13, s[8:11], s37 offen offset:1152 sc0 sc1 nt\n"
        "v_add_f32_dpp v161, v70, v71 quad_perm:[0,1,2,3] row_mask:0x1 bank_mask:0xf\n"
        "s_waitcnt lgkmcnt(2)\n"
        "v_add_u32 v163, v9, v163\n"
        "v_add_f32_dpp v160, v68, v69 quad_perm:[0,1,2,3] row_mask:0x1 bank_mask:0xf\n"
        "v_add_u32 v151, v9, v151\n"
        "v_add_f32_dpp v161, v74, v75 quad_perm:[0,1,2,3] row_mask:0x2 bank_mask:0xf\n"
        "v_add_f32_dpp v160, v72, v73 quad_perm:[0,1,2,3] row_mask:0x2 bank_mask:0xf\n"
        "s_waitcnt lgkmcnt(0)\n"
        "v_smfmac_f32_16x16x64_bf16 v[48:51], v[44:47], v[20:27], v4\n"
        "v_mfma_f32_16x16x32_bf16 v[68:71], v[212:215], v[164:167], 0\n"
        "s_add_u32 s37, s36, 294336\n"
        "v_mfma_f32_16x16x32_bf16 v[68:71], v[208:211], v[40:43], v[68:71]\n"
        "v_mfma_f32_16x16x32_bf16 v[68:71], v[204:207], v[36:39], v[68:71]\n"
        "v_mfma_f32_16x16x32_bf16 v[52:55], v[216:219], v[36:39], 0\n"
        "buffer_store_dwordx3 v[128:130], v12, s[8:11], s37 offen offset:576 sc0 sc1 nt\n"
        "s_setprio 2\n"
        "v_mfma_f32_16x16x32_bf16 v[52:55], v[220:223], v[40:43], v[52:55]\n"
        "v_add_f32_dpp v161, v50, v51 quad_perm:[0,1,2,3] row_mask:0x4 bank_mask:0xf\n"
        "v_mul_f32 v162, s52, v161\n"
        "v_fma_f32 v162, -v162, v161, v159\n"
        "v_add_f32_e64 v162, s53, |v162|\n"
        "v_cvt_pk_bf16_f32 v62, v68, v69\n"
        "v_cvt_pk_bf16_f32 v63, v70, v71\n"
        "v_rsq_f32 v159, v162\n"
        "v_mfma_f32_16x16x32_bf16 v[52:55], v[224:227], v[164:167], v[52:55]\n"
        "s_add_u32 s37, s36, 392448\n"
        "v_add_f32_dpp v160, v48, v49 quad_perm:[0,1,2,3] row_mask:0x4 bank_mask:0xf\n"
        "v_mul_f32 v160, v160, v159\n"
        "v_readlane_b32 s58, v159, 56\n"
        "s_mov_b32 exec_hi, 0xffff\n"
        "ds_write_b32 v10, v159\n"
        "s_mov_b32 exec_hi, -1\n"
        "v_mfma_f32_16x16x32_bf16 v[68:71], v[236:239], v[164:167], 0\n"
        "s_mov_b64 exec, s[56:57]\n"
        "v_mov_b32 v160, 0xff800000\n"
        "s_mov_b64 exec, -1\n"
        "v_cvt_pk_bf16_f32 v72, v52, v53\n"
        "v_cvt_pk_bf16_f32 v73, v54, v55\n"
        "ds_read_b32 v44, v163 offset:40064\n"
        "ds_read_b32 v45, v163 offset:40896\n"
        "v_max_f32_dpp v155, v160, v160 row_ror:8 row_mask:0xf bank_mask:0xf\n"
        "v_mfma_f32_16x16x32_bf16 v[68:71], v[232:235], v[40:43], v[68:71]\n"
        "s_mov_b32 exec_hi, 0xffff\n"
        "v_add_u32 v10, -40064, v10\n"
        "s_mov_b32 exec_hi, -1\n"
        "ds_read_b32 v46, v163 offset:41728\n"
        "ds_read_b32 v47, v163 offset:42560\n"
        "ds_read_b32 v48, v163 offset:43392\n"
        "ds_read_b32 v49, v163 offset:44224\n"
        "v_max_f32_dpp v155, v155, v155 row_ror:4 row_mask:0xf bank_mask:0xf\n"
        "v_mfma_f32_16x16x32_bf16 v[68:71], v[228:231], v[36:39], v[68:71]\n"
        "buffer_store_dwordx3 v[140:142], v12, s[8:11], s37 offen sc0 sc1 nt\n"
        "ds_read_b32 v50, v163 offset:45056\n"
        "ds_read_b32 v51, v163 offset:45888\n"
        "ds_read_b32 v52, v163 offset:46720\n"
        "ds_read_b32 v53, v163 offset:47552\n"
        "ds_read_b32 v54, v163 offset:48384\n"
        "ds_read_b32 v55, v163 offset:49216\n"
        "ds_read_b32 v161, v151 offset:40064\n"
        "v_cvt_pk_bf16_f32 v74, v68, v69\n"
        "v_cvt_pk_bf16_f32 v75, v70, v71\n"
        "v_max_f32_dpp v155, v155, v155 row_ror:2 row_mask:0xf bank_mask:0xf\n"
        "v_mfma_f32_16x16x32_bf16 v[80:83], v[36:39], v[56:59], 0\n"
        "ds_read_b32 v162, v151 offset:43392\n"
        "v_max_f32_dpp v155, v155, v155 row_ror:1 row_mask:0xf bank_mask:0xf\n"
        "ds_read_b32 v163, v151 offset:46720\n"
        "v_mfma_f32_16x16x32_bf16 v[80:83], v[40:43], v[60:63], v[80:83]\n"
        "s_add_u32 s37, s36, 392448\n"
        "v_mfma_f32_16x16x32_bf16 v[80:83], v[164:167], v[72:75], v[80:83]\n"
        "v_mfma_f32_16x16x32_bf16 v[92:95], v[136:139], v[72:75], 0\n"
        "v_mfma_f32_16x16x32_bf16 v[92:95], v[32:35], v[60:63], v[92:95]\n"
        "buffer_store_dwordx3 v[144:146], v15, s[8:11], s37 offen offset:768 sc0 sc1 nt\n"
        "v_mfma_f32_16x16x32_bf16 v[92:95], v[28:31], v[56:59], v[92:95]\n"
        "ds_read_b128 v[68:71], v8 offset:128\n"
        "v_mfma_f32_16x16x32_bf16 v[76:79], v[20:23], v[56:59], 0\n"
        "s_add_u32 s37, s36, 490560\n"
        "ds_read2_b32 v[84:85], v6 offset0:0 offset1:2\n"
        "ds_read2_b32 v[86:87], v6 offset0:8 offset1:10\n"
        "s_waitcnt lgkmcnt(3)\n"
        "v_mul_f32 v80, v80, v44\n"
        "v_mul_f32 v81, v81, v45\n"
        "v_mul_f32 v82, v82, v46\n"
        "v_mul_f32 v83, v83, v47\n"
        "v_mul_f32 v92, v92, v48\n"
        "v_mfma_f32_16x16x32_bf16 v[76:79], v[24:27], v[60:63], v[76:79]\n"
        "v_max3_f32 v151, v80, v81, v82\n"
        "v_mul_f32 v93, v93, v49\n"
        "v_mul_f32 v94, v94, v50\n"
        "v_max3_f32 v151, v151, v83, v92\n"
        "v_mul_f32 v95, v95, v51\n"
        "v_max3_f32 v151, v151, v93, v94\n"
        "v_mfma_f32_16x16x32_bf16 v[76:79], v[64:67], v[72:75], v[76:79]\n"
        "s_waitcnt lgkmcnt(2)\n"
        "v_mfma_f32_16x16x32_bf16 v[96:99], v[68:71], v[72:75], 0\n"
        "buffer_store_dwordx3 v[148:150], v12, s[8:11], s37 offen offset:192 sc0 sc1 nt\n"
        "s_waitcnt lgkmcnt(0)\n"
        "v_smfmac_f32_16x16x64_bf16 v[96:99], v[84:87], v[56:63], v4\n"
        "v_mfma_f32_16x16x32_bf16 v[72:75], v[168:171], v[28:31], 0\n"
        "s_add_u32 s37, s36, 490560\n"
        "v_mfma_f32_16x16x32_bf16 v[72:75], v[172:175], v[32:35], v[72:75]\n"
        "v_mul_f32 v76, v76, v52\n"
        "v_mul_f32 v77, v77, v53\n"
        "v_mul_f32 v78, v78, v54\n"
        "v_max3_f32 v151, v151, v95, v76\n"
        "v_mul_f32 v79, v79, v55\n"
        "v_add_f32 v96, v96, v97\n"
        "v_mul_f32 v96, s58, v96\n"
        "v_max3_f32 v151, v151, v77, v78\n"
        "v_mfma_f32_16x16x32_bf16 v[72:75], v[176:179], v[136:139], v[72:75]\n"
        "s_mov_b64 exec, s[48:49]\n"
        "v_mov_b32 v96, 0xff800000\n"
        "s_mov_b64 exec, -1\n"
        "v_max3_f32 v151, v151, v79, v96\n"
        "v_mfma_f32_16x16x32_bf16 v[60:63], v[188:191], v[136:139], 0\n"
        "buffer_store_dwordx3 v[152:154], v14, s[8:11], s37 offen offset:960 sc0 sc1 nt\n"
        "v_permlane32_swap_b32 v151, v155\n"
        "v_max_f32 v151, v151, v155\n"
        "v_cvt_pk_bf16_f32 v56, v72, v73\n"
        "v_cvt_pk_bf16_f32 v57, v74, v75\n"
        "v_mov_b32 v155, v151\n"
        "v_mfma_f32_16x16x32_bf16 v[60:63], v[184:187], v[32:35], v[60:63]\n"
        "v_mfma_f32_16x16x32_bf16 v[60:63], v[180:183], v[28:31], v[60:63]\n"
        "s_add_u32 s37, s36, 588672\n"
        "v_permlane16_swap_b32 v151, v155\n"
        "v_max_f32 v151, v151, v155\n"
        "v_mov_b32 v155, v151\n"
        "v_mfma_f32_16x16x32_bf16 v[88:91], v[192:195], v[28:31], 0\n"
        "v_mfma_f32_16x16x32_bf16 v[88:91], v[196:199], v[32:35], v[88:91]\n"
        "v_permlane32_swap_b32 v151, v155\n"
        "v_mul_f32 v151, v151, v161\n"
        "v_cvt_pk_bf16_f32 v58, v60, v61\n"
        "v_cvt_pk_bf16_f32 v59, v62, v63\n"
        "v_fma_f32 v80, v80, v161, -v151\n"
        "v_fma_f32 v81, v81, v161, -v151\n"
        "v_fma_f32 v82, v82, v161, -v151\n"
        "v_mfma_f32_16x16x32_bf16 v[88:91], v[200:203], v[136:139], v[88:91]\n"
        "buffer_store_dwordx3 v[156:158], v12, s[8:11], s37 offen offset:384 sc0 sc1 nt\n"
        "v_exp_f32 v80, v80\n"
        "v_exp_f32 v81, v81\n"
        "v_exp_f32 v82, v82\n"
        "v_fma_f32 v83, v83, v161, -v151\n"
        "v_add_f32 v158, v80, v81\n"
        "v_exp_f32 v83, v83\n"
        "v_mfma_f32_16x16x32_bf16 v[72:75], v[212:215], v[136:139], 0\n"
        "v_cvt_pk_bf16_f32 v60, v88, v89\n"
        "v_cvt_pk_bf16_f32 v61, v90, v91\n"
        "v_fma_f32 v92, v92, v161, -v151\n"
        "v_add_f32 v158, v158, v82\n"
        "v_exp_f32 v92, v92\n"
        "v_fma_f32 v93, v93, v161, -v151\n"
        "v_add_f32 v158, v158, v83\n"
        "v_mfma_f32_16x16x32_bf16 v[72:75], v[208:211], v[32:35], v[72:75]\n"
        "v_exp_f32 v93, v93\n"
        "v_fma_f32 v94, v94, v161, -v151\n"
        "v_add_f32 v158, v158, v92\n"
        "v_exp_f32 v94, v94\n"
        "v_fma_f32 v95, v95, v161, -v151\n"
        "v_sub_f32 v160, v160, v155\n"
        "v_mfma_f32_16x16x32_bf16 v[72:75], v[204:207], v[28:31], v[72:75]\n"
        "v_add_f32 v158, v158, v93\n"
        "v_exp_f32 v95, v95\n"
        "v_fma_f32 v76, v76, v161, -v151\n"
        "v_mul_f32 v160, s58, v160\n"
        "v_add_f32 v158, v158, v94\n"
        "v_exp_f32 v76, v76\n"
        "v_mfma_f32_16x16x32_bf16 v[88:91], v[216:219], v[28:31], 0\n"
        "v_fma_f32 v77, v77, v161, -v151\n"
        "v_cvt_pk_bf16_f32 v62, v72, v73\n"
        "v_cvt_pk_bf16_f32 v63, v74, v75\n"
        "v_exp_f32 v160, v160\n"
        "v_add_f32 v158, v158, v95\n"
        "v_exp_f32 v77, v77\n"
        "v_mfma_f32_16x16x32_bf16 v[88:91], v[220:223], v[32:35], v[88:91]\n"
        "v_fma_f32 v78, v78, v161, -v151\n"
        "v_add_f32 v158, v158, v76\n"
        "v_exp_f32 v78, v78\n"
        "v_fma_f32 v79, v79, v161, -v151\n"
        "v_add_f32_dpp v157, v160, v160 row_ror:8 row_mask:0xf bank_mask:0xf\n"
        "v_add_f32 v158, v158, v77\n"
        "v_fma_f32 v96, v96, v161, -v151\n"
        "v_mfma_f32_16x16x32_bf16 v[88:91], v[224:227], v[136:139], v[88:91]\n"
        "v_exp_f32 v79, v79\n"
        "v_add_f32_dpp v157, v157, v157 row_ror:4 row_mask:0xf bank_mask:0xf\n"
        "v_add_f32 v158, v158, v78\n"
        "v_exp_f32 v96, v96\n"
        "v_add_f32_dpp v157, v157, v157 row_ror:2 row_mask:0xf bank_mask:0xf\n"
        "v_add_f32 v158, v158, v79\n"
        "v_mfma_f32_16x16x32_bf16 v[72:75], v[236:239], v[136:139], 0\n"
        "v_add_f32_dpp v157, v157, v157 row_ror:1 row_mask:0xf bank_mask:0xf\n"
        "v_cvt_pk_bf16_f32 v100, v88, v89\n"
        "v_cvt_pk_bf16_f32 v101, v90, v91\n"
        "v_add_f32 v158, v158, v96\n"
        "v_mul_f32 v80, v80, v44\n"
        "v_mul_f32 v81, v81, v45\n"
        "v_permlane32_swap_b32 v158, v157\n"
        "v_mfma_f32_16x16x32_bf16 v[72:75], v[232:235], v[32:35], v[72:75]\n"
        "v_add_f32 v158, v158, v157\n"
        "v_mov_b32 v161, v158\n"
        "v_mul_f32 v82, v82, v46\n"
        "v_mul_f32 v83, v83, v47\n"
        "v_permlane16_swap_b32 v158, v161\n"
        "v_add_f32 v158, v158, v161\n"
        "v_mul_f32 v92, v92, v48\n"
        "v_mfma_f32_16x16x32_bf16 v[72:75], v[228:231], v[28:31], v[72:75]\n"
        "v_rcp_f32 v158, v158\n"
        "v_mul_f32 v93, v93, v49\n"
        "v_mov_b32 v157, v158\n"
        "v_mul_f32 v94, v94, v50\n"
        "v_mul_f32 v95, v95, v51\n"
        "v_permlane32_swap_b32 v158, v157\n"
        "v_mfma_f32_16x16x32_bf16 v[88:91], v[36:39], v[56:59], 0\n"
        "v_mul_f32 v80, v80, v158\n"
        "v_cvt_pk_bf16_f32 v102, v72, v73\n"
        "v_cvt_pk_bf16_f32 v103, v74, v75\n"
        "v_mul_f32 v81, v81, v158\n"
        "v_mul_f32 v82, v82, v158\n"
        "v_mul_f32 v83, v83, v158\n"
        "v_mul_f32 v92, v92, v158\n"
        "v_mul_f32 v93, v93, v158\n"
        "v_mfma_f32_16x16x32_bf16 v[88:91], v[40:43], v[60:63], v[88:91]\n"
        "v_mul_f32 v94, v94, v158\n"
        "v_mul_f32 v95, v95, v158\n"
        "v_cvt_pk_bf16_f32 v72, v80, v81\n"
        "v_cvt_pk_bf16_f32 v73, v82, v83\n"
        "v_cvt_pk_bf16_f32 v74, v92, v93\n"
        "v_cvt_pk_bf16_f32 v75, v94, v95\n"
        "v_mul_f32 v76, v76, v52\n"
        "v_mul_f32 v77, v77, v53\n"
        "v_mfma_f32_16x16x32_bf16 v[88:91], v[164:167], v[100:103], v[88:91]\n"
        "v_mul_f32 v78, v78, v54\n"
        "v_mul_f32 v79, v79, v55\n"
        "v_mul_f32 v96, s58, v96\n"
        "v_mul_f32 v76, v76, v158\n"
        "v_mul_f32 v77, v77, v158\n"
        "v_mul_f32 v78, v78, v158\n"
        "v_mul_f32 v79, v79, v158\n"
        "v_fma_f32 v96, v96, v158, v19\n"
        "v_mfma_f32_16x16x32_bf16 v[108:111], v[136:139], v[100:103], 0\n"
        "v_mul_f32 v88, v88, v44\n"
        "v_mul_f32 v89, v89, v45\n"
        "v_mul_f32 v90, v90, v46\n"
        "v_mul_f32 v91, v91, v47\n"
        "v_max3_f32 v161, v88, v89, v90\n"
        "v_cvt_pk_bf16_f32 v92, v76, v77\n"
        "v_cvt_pk_bf16_f32 v94, v78, v79\n"
        "v_cvt_pk_bf16_f32 v93, v96, 0\n"
        "v_mfma_f32_16x16x32_bf16 v[108:111], v[32:35], v[60:63], v[108:111]\n"
        "v_mul_f32 v160, v160, v159\n"
        "v_mul_f32 v160, v160, v157\n"
        "v_cvt_pk_bf16_f32 v160, v160, 0\n"
        "ds_write_b16 v1, v160\n"
        "v_mfma_f32_16x16x32_bf16 v[108:111], v[28:31], v[56:59], v[108:111]\n"
        "v_mfma_f32_16x16x32_bf16 v[96:99], v[20:23], v[56:59], 0\n"
        "v_mfma_f32_16x16x32_bf16 v[96:99], v[24:27], v[60:63], v[96:99]\n"
        "v_mfma_f32_16x16x32_bf16 v[96:99], v[64:67], v[100:103], v[96:99]\n"
        "v_mfma_f32_16x16x32_bf16 v[104:107], v[68:71], v[100:103], 0\n"
        "v_smfmac_f32_16x16x64_bf16 v[104:107], v[84:87], v[56:63], v4\n"
        "v_mfma_f32_16x16x32_bf16 v[80:83], v[168:171], v[20:23], 0\n"
        "v_mfma_f32_16x16x32_bf16 v[80:83], v[172:175], v[24:27], v[80:83]\n"
        "v_mfma_f32_16x16x32_bf16 v[80:83], v[176:179], v[64:67], v[80:83]\n"
        "v_mul_f32 v108, v108, v48\n"
        "v_mul_f32 v109, v109, v49\n"
        "v_mul_f32 v110, v110, v50\n"
        "v_max3_f32 v161, v161, v91, v108\n"
        "v_mul_f32 v111, v111, v51\n"
        "v_add_f32 v104, v104, v105\n"
        "v_mul_f32 v96, v96, v52\n"
        "v_mul_f32 v104, s58, v104\n"
        "v_mfma_f32_16x16x32_bf16 v[56:59], v[188:191], v[64:67], 0\n"
        "s_mov_b64 exec, s[48:49]\n"
        "v_mov_b32 v104, 0xff800000\n"
        "s_mov_b64 exec, -1\n"
        "v_cvt_pk_bf16_f32 v76, v80, v81\n"
        "v_cvt_pk_bf16_f32 v77, v82, v83\n"
        "v_max3_f32 v161, v161, v109, v110\n"
        "v_mul_f32 v97, v97, v53\n"
        "v_mul_f32 v98, v98, v54\n"
        "v_max3_f32 v161, v161, v111, v96\n"
        "v_mfma_f32_16x16x32_bf16 v[56:59], v[184:187], v[24:27], v[56:59]\n"
        "v_mul_f32 v99, v99, v55\n"
        "v_max3_f32 v161, v161, v97, v98\n"
        "v_max3_f32 v161, v161, v99, v104\n"
        "v_mfma_f32_16x16x32_bf16 v[56:59], v[180:183], v[20:23], v[56:59]\n"
        "v_mfma_f32_16x16x32_bf16 v[100:103], v[192:195], v[20:23], 0\n"
        "v_mfma_f32_16x16x32_bf16 v[100:103], v[196:199], v[24:27], v[100:103]\n"
        "v_mfma_f32_16x16x32_bf16 v[100:103], v[200:203], v[64:67], v[100:103]\n"
        "v_mfma_f32_16x16x32_bf16 v[112:115], v[212:215], v[64:67], 0\n"
        "v_mfma_f32_16x16x32_bf16 v[112:115], v[208:211], v[24:27], v[112:115]\n"
        "v_mfma_f32_16x16x32_bf16 v[112:115], v[204:207], v[20:23], v[112:115]\n"
        "v_mfma_f32_16x16x32_bf16 v[60:63], v[216:219], v[20:23], 0\n"
        "v_mfma_f32_16x16x32_bf16 v[60:63], v[220:223], v[24:27], v[60:63]\n"
        "v_cvt_pk_bf16_f32 v78, v56, v57\n"
        "v_cvt_pk_bf16_f32 v79, v58, v59\n"
        "v_mfma_f32_16x16x32_bf16 v[60:63], v[224:227], v[64:67], v[60:63]\n"
        "v_cvt_pk_bf16_f32 v80, v100, v101\n"
        "v_cvt_pk_bf16_f32 v81, v102, v103\n"
        "v_mfma_f32_16x16x32_bf16 v[56:59], v[236:239], v[64:67], 0\n"
        "v_cvt_pk_bf16_f32 v82, v112, v113\n"
        "v_cvt_pk_bf16_f32 v83, v114, v115\n"
        "v_mfma_f32_16x16x32_bf16 v[56:59], v[232:235], v[24:27], v[56:59]\n"
        "v_mfma_f32_16x16x32_bf16 v[56:59], v[228:231], v[20:23], v[56:59]\n"
        "v_mfma_f32_16x16x32_bf16 v[120:123], v[36:39], v[76:79], 0\n"
        "v_cvt_pk_bf16_f32 v100, v60, v61\n"
        "v_cvt_pk_bf16_f32 v101, v62, v63\n"
        "v_mfma_f32_16x16x32_bf16 v[120:123], v[40:43], v[80:83], v[120:123]\n"
        "s_nop 3\n"
        "v_cvt_pk_bf16_f32 v102, v56, v57\n"
        "v_cvt_pk_bf16_f32 v103, v58, v59\n"
        "s_nop 1\n"
        "v_mfma_f32_16x16x32_bf16 v[120:123], v[164:167], v[100:103], v[120:123]\n"
        "v_mfma_f32_16x16x32_bf16 v[128:131], v[136:139], v[100:103], 0\n"
        "v_mfma_f32_16x16x32_bf16 v[128:131], v[32:35], v[80:83], v[128:131]\n"
        "v_mfma_f32_16x16x32_bf16 v[128:131], v[28:31], v[76:79], v[128:131]\n"
        "v_mfma_f32_16x16x32_bf16 v[112:115], v[20:23], v[76:79], 0\n"
        "v_mfma_f32_16x16x32_bf16 v[112:115], v[24:27], v[80:83], v[112:115]\n"
        "v_mfma_f32_16x16x32_bf16 v[112:115], v[64:67], v[100:103], v[112:115]\n"
        "v_mfma_f32_16x16x32_bf16 v[60:63], v[68:71], v[100:103], 0\n"
        "v_smfmac_f32_16x16x64_bf16 v[60:63], v[84:87], v[76:83], v4\n"
        "v_mul_f32 v120, v120, v44\n"
        "v_mul_f32 v121, v121, v45\n"
        "v_mul_f32 v122, v122, v46\n"
        "v_mul_f32 v123, v123, v47\n"
        "v_mul_f32 v128, v128, v48\n"
        "v_max3_f32 v158, v120, v121, v122\n"
        "v_mul_f32 v129, v129, v49\n"
        "v_mfma_f32_16x16x32_bf16 v[68:71], v[36:39], v[240:243], 0\n"
        "v_add_f32 v60, v60, v61\n"
        "v_mul_f32 v130, v130, v50\n"
        "v_max3_f32 v158, v158, v123, v128\n"
        "v_mul_f32 v131, v131, v51\n"
        "v_mul_f32 v112, v112, v52\n"
        "v_mul_f32 v60, s58, v60\n"
        "v_max3_f32 v158, v158, v129, v130\n"
        "v_mul_f32 v113, v113, v53\n"
        "v_mfma_f32_16x16x32_bf16 v[68:71], v[40:43], v[244:247], v[68:71]\n"
        "s_mov_b64 exec, s[48:49]\n"
        "v_mov_b32 v60, 0xff800000\n"
        "s_mov_b64 exec, -1\n"
        "v_mul_f32 v114, v114, v54\n"
        "v_max3_f32 v158, v158, v131, v112\n"
        "v_mul_f32 v115, v115, v55\n"
        "v_max3_f32 v158, v158, v113, v114\n"
        "v_max3_f32 v158, v158, v115, v60\n"
        "v_mfma_f32_16x16x32_bf16 v[68:71], v[164:167], v[248:251], v[68:71]\n"
        "ds_read_b128 v[20:23], v17 offset:40064\n"
        "v_permlane32_swap_b32 v161, v158\n"
        "v_max_f32 v161, v161, v158\n"
        "v_mov_b32 v160, v161\n"
        "v_mfma_f32_16x16x32_bf16 v[64:67], v[136:139], v[248:251], 0\n"
        "ds_read_b128 v[24:27], v17 offset:40128\n"
        "v_permlane16_swap_b32 v161, v160\n"
        "v_max_f32 v161, v161, v160\n"
        "v_cvt_pk_bf16_f32 v56, v68, v69\n"
        "v_cvt_pk_bf16_f32 v57, v70, v71\n"
        "v_mov_b32 v158, v161\n"
        "v_mfma_f32_16x16x32_bf16 v[64:67], v[32:35], v[244:247], v[64:67]\n"
        "ds_read_b128 v[116:119], v17 offset:40192\n"
        "v_permlane32_swap_b32 v161, v158\n"
        "v_mul_f32 v161, v161, v162\n"
        "v_mul_f32 v158, v158, v163\n"
        "v_fma_f32 v88, v88, v162, -v161\n"
        "v_fma_f32 v89, v89, v162, -v161\n"
        "v_fma_f32 v120, v120, v163, -v158\n"
        "v_mfma_f32_16x16x32_bf16 v[64:67], v[28:31], v[240:243], v[64:67]\n"
        "s_waitcnt lgkmcnt(2)\n"
        "ds_read_b128 v[84:87], v0 offset:5136\n"
        "v_fma_f32 v121, v121, v163, -v158\n"
        "v_exp_f32 v88, v88\n"
        "v_exp_f32 v89, v89\n"
        "v_fma_f32 v90, v90, v162, -v161\n"
        "v_fma_f32 v122, v122, v163, -v158\n"
        "v_mfma_f32_16x16x32_bf16 v[68:71], v[20:23], v[240:243], 0\n"
        "s_waitcnt lgkmcnt(2)\n"
        "ds_read_b128 v[100:103], v0 offset:4112\n"
        "v_cvt_pk_bf16_f32 v58, v64, v65\n"
        "v_cvt_pk_bf16_f32 v59, v66, v67\n"
        "v_exp_f32 v120, v120\n"
        "v_exp_f32 v121, v121\n"
        "v_fma_f32 v91, v91, v162, -v161\n"
        "v_mfma_f32_16x16x32_bf16 v[68:71], v[24:27], v[244:247], v[68:71]\n"
        "s_waitcnt lgkmcnt(2)\n"
        "v_exp_f32 v90, v90\n"
        "v_exp_f32 v122, v122\n"
        "v_fma_f32 v123, v123, v163, -v158\n"
        "v_add_f32 v159, v88, v89\n"
        "v_exp_f32 v91, v91\n"
        "v_mfma_f32_16x16x32_bf16 v[68:71], v[116:119], v[248:251], v[68:71]\n"
        "s_waitcnt lgkmcnt(1)\n"
        "v_fma_f32 v108, v108, v162, -v161\n"
        "v_add_f32 v160, v120, v121\n"
        "v_exp_f32 v123, v123\n"
        "v_fma_f32 v128, v128, v163, -v158\n"
        "v_add_f32 v159, v159, v90\n"
        "v_exp_f32 v108, v108\n"
        "v_mfma_f32_16x16x32_bf16 v[124:127], v[116:119], v[84:87], 0\n"
        "s_waitcnt lgkmcnt(0)\n"
        "v_cvt_pk_bf16_f32 v64, v68, v69\n"
        "v_cvt_pk_bf16_f32 v68, v70, v71\n"
        "v_fma_f32 v109, v109, v162, -v161\n"
        "v_add_f32 v160, v160, v122\n"
        "v_exp_f32 v128, v128\n"
        "v_fma_f32 v129, v129, v163, -v158\n"
        "v_add_f32 v159, v159, v91\n"
        "v_mfma_f32_16x16x32_bf16 v[124:127], v[24:27], v[100:103], v[124:127]\n"
        "v_exp_f32 v109, v109\n"
        "v_fma_f32 v110, v110, v162, -v161\n"
        "v_add_f32 v160, v160, v123\n"
        "v_exp_f32 v129, v129\n"
        "v_fma_f32 v130, v130, v163, -v158\n"
        "v_add_f32 v159, v159, v108\n"
        "v_mfma_f32_16x16x32_bf16 v[124:127], v[20:23], v[252:255], v[124:127]\n"
        "v_exp_f32 v110, v110\n"
        "v_fma_f32 v111, v111, v162, -v161\n"
        "v_add_f32 v160, v160, v128\n"
        "v_exp_f32 v130, v130\n"
        "v_fma_f32 v131, v131, v163, -v158\n"
        "v_add_f32 v159, v159, v109\n"
        "v_mfma_f32_16x16x32_bf16 v[140:143], v[28:31], v[252:255], 0\n"
        "v_exp_f32 v111, v111\n"
        "v_cvt_pk_bf16_f32 v76, v124, v125\n"
        "v_cvt_pk_bf16_f32 v80, v126, v127\n"
        "v_fma_f32 v96, v96, v162, -v161\n"
        "v_add_f32 v160, v160, v129\n"
        "v_exp_f32 v131, v131\n"
        "v_mfma_f32_16x16x32_bf16 v[140:143], v[32:35], v[100:103], v[140:143]\n"
        "v_fma_f32 v112, v112, v163, -v158\n"
        "v_add_f32 v159, v159, v110\n"
        "v_exp_f32 v96, v96\n"
        "v_fma_f32 v97, v97, v162, -v161\n"
        "v_add_f32 v160, v160, v130\n"
        "v_exp_f32 v112, v112\n"
        "v_mfma_f32_16x16x32_bf16 v[140:143], v[136:139], v[84:87], v[140:143]\n"
        "ds_read_b128 v[144:147], v0 offset:6176\n"
        "v_fma_f32 v113, v113, v163, -v158\n"
        "v_add_f32 v159, v159, v111\n"
        "v_exp_f32 v97, v97\n"
        "v_fma_f32 v98, v98, v162, -v161\n"
        "v_add_f32 v160, v160, v131\n"
        "v_fma_f32 v114, v114, v163, -v158\n"
        "v_mfma_f32_16x16x32_bf16 v[148:151], v[164:167], v[84:87], 0\n"
        "ds_read_b128 v[132:135], v0 offset:7200\n"
        "v_cvt_pk_bf16_f32 v86, v140, v141\n"
        "v_cvt_pk_bf16_f32 v87, v142, v143\n"
        "v_exp_f32 v113, v113\n"
        "v_add_f32 v159, v159, v96\n"
        "v_exp_f32 v98, v98\n"
        "v_mfma_f32_16x16x32_bf16 v[148:151], v[40:43], v[100:103], v[148:151]\n"
        "ds_read_b128 v[140:143], v0 offset:8224\n"
        "v_fma_f32 v99, v99, v162, -v161\n"
        "v_add_f32 v160, v160, v112\n"
        "v_exp_f32 v114, v114\n"
        "v_fma_f32 v115, v115, v163, -v158\n"
        "v_add_f32 v159, v159, v97\n"
        "v_fma_f32 v104, v104, v162, -v161\n"
        "v_mfma_f32_16x16x32_bf16 v[148:151], v[36:39], v[252:255], v[148:151]\n"
        "s_waitcnt lgkmcnt(2)\n"
        "v_exp_f32 v99, v99\n"
        "v_add_f32 v160, v160, v113\n"
        "v_exp_f32 v115, v115\n"
        "v_fma_f32 v60, v60, v163, -v158\n"
        "v_add_f32 v159, v159, v98\n"
        "v_add_f32 v160, v160, v114\n"
        "v_mfma_f32_16x16x32_bf16 v[124:127], v[36:39], v[144:147], 0\n"
        "s_waitcnt lgkmcnt(1)\n"
        "v_cvt_pk_bf16_f32 v84, v148, v149\n"
        "v_cvt_pk_bf16_f32 v85, v150, v151\n"
        "v_exp_f32 v104, v104\n"
        "v_exp_f32 v60, v60\n"
        "v_add_f32 v159, v159, v99\n"
        "v_add_f32 v160, v160, v115\n"
        "v_mfma_f32_16x16x32_bf16 v[124:127], v[40:43], v[132:135], v[124:127]\n"
        "s_waitcnt lgkmcnt(0)\n"
        "v_add_f32 v159, v159, v104\n"
        "v_add_f32 v160, v160, v60\n"
        "v_mul_f32 v88, v88, v44\n"
        "v_mul_f32 v89, v89, v45\n"
        "v_permlane32_swap_b32 v159, v160\n"
        "v_add_f32 v159, v159, v160\n"
        "v_mov_b32 v163, v159\n"
        "v_mfma_f32_16x16x32_bf16 v[124:127], v[164:167], v[140:143], v[124:127]\n"
        "v_mul_f32 v90, v90, v46\n"
        "v_permlane16_swap_b32 v159, v163\n"
        "v_add_f32 v159, v159, v163\n"
        "v_rcp_f32 v159, v159\n"
        "v_mul_f32 v91, v91, v47\n"
        "v_mov_b32 v160, v159\n"
        "v_mfma_f32_16x16x32_bf16 v[152:155], v[136:139], v[140:143], 0\n"
        "v_mul_f32 v108, v108, v48\n"
        "v_cvt_pk_bf16_f32 v100, v124, v125\n"
        "v_cvt_pk_bf16_f32 v101, v126, v127\n"
        "v_permlane32_swap_b32 v159, v160\n"
        "v_mul_f32 v109, v109, v49\n"
        "v_mul_f32 v110, v110, v50\n"
        "v_mul_f32 v111, v111, v51\n"
        "v_mfma_f32_16x16x32_bf16 v[152:155], v[32:35], v[132:135], v[152:155]\n"
        "v_mul_f32 v88, v88, v159\n"
        "v_mul_f32 v89, v89, v159\n"
        "v_mul_f32 v90, v90, v159\n"
        "v_mul_f32 v91, v91, v159\n"
        "v_mul_f32 v108, v108, v159\n"
        "v_mul_f32 v109, v109, v159\n"
        "v_mul_f32 v110, v110, v159\n"
        "v_mul_f32 v111, v111, v159\n"
        "v_mfma_f32_16x16x32_bf16 v[152:155], v[28:31], v[144:147], v[152:155]\n"
        "ds_read_b128 v[148:151], v0 offset:11312\n"
        "v_cvt_pk_bf16_f32 v124, v88, v89\n"
        "v_cvt_pk_bf16_f32 v125, v90, v91\n"
        "v_cvt_pk_bf16_f32 v126, v108, v109\n"
        "v_cvt_pk_bf16_f32 v127, v110, v111\n"
        "v_mul_f32 v96, v96, v52\n"
        "v_mul_f32 v97, v97, v53\n"
        "v_mul_f32 v98, v98, v54\n"
        "v_mfma_f32_16x16x32_bf16 v[108:111], v[20:23], v[144:147], 0\n"
        "ds_read_b128 v[88:91], v0 offset:10288\n"
        "v_cvt_pk_bf16_f32 v102, v152, v153\n"
        "v_cvt_pk_bf16_f32 v103, v154, v155\n"
        "v_mul_f32 v99, v99, v55\n"
        "v_mul_f32 v104, s58, v104\n"
        "v_mul_f32 v96, v96, v159\n"
        "v_mul_f32 v97, v97, v159\n"
        "v_mul_f32 v98, v98, v159\n"
        "v_mfma_f32_16x16x32_bf16 v[108:111], v[24:27], v[132:135], v[108:111]\n"
        "ds_read_b128 v[144:147], v0 offset:9264\n"
        "v_mul_f32 v99, v99, v159\n"
        "v_fma_f32 v104, v104, v159, v19\n"
        "v_cvt_pk_bf16_f32 v132, v96, v97\n"
        "v_cvt_pk_bf16_f32 v134, v98, v99\n"
        "v_cvt_pk_bf16_f32 v133, v104, 0\n"
        "v_mul_f32 v120, v120, v44\n"
        "v_mul_f32 v121, v121, v45\n"
        "v_mfma_f32_16x16x32_bf16 v[108:111], v[116:119], v[140:143], v[108:111]\n"
        "s_waitcnt lgkmcnt(2)\n"
        "v_mul_f32 v122, v122, v46\n"
        "v_mul_f32 v123, v123, v47\n"
        "v_mul_f32 v128, v128, v48\n"
        "v_mul_f32 v129, v129, v49\n"
        "v_mul_f32 v130, v130, v50\n"
        "v_mul_f32 v131, v131, v51\n"
        "v_mul_f32 v120, v120, v160\n"
        "v_cvt_pk_bf16_f32 v44, v108, v109\n"
        "v_mfma_f32_16x16x32_bf16 v[96:99], v[116:119], v[148:151], 0\n"
        "s_waitcnt lgkmcnt(1)\n"
        "v_cvt_pk_bf16_f32 v48, v110, v111\n"
        "v_mul_f32 v121, v121, v160\n"
        "v_mul_f32 v122, v122, v160\n"
        "v_mul_f32 v123, v123, v160\n"
        "v_mul_f32 v128, v128, v160\n"
        "v_mul_f32 v129, v129, v160\n"
        "v_mul_f32 v130, v130, v160\n"
        "v_mul_f32 v131, v131, v160\n"
        "v_mfma_f32_16x16x32_bf16 v[96:99], v[24:27], v[88:91], v[96:99]\n"
        "s_waitcnt lgkmcnt(0)\n"
        "v_cvt_pk_bf16_f32 v108, v120, v121\n"
        "v_cvt_pk_bf16_f32 v109, v122, v123\n"
        "v_cvt_pk_bf16_f32 v110, v128, v129\n"
        "v_cvt_pk_bf16_f32 v111, v130, v131\n"
        "v_mul_f32 v112, v112, v52\n"
        "v_mul_f32 v113, v113, v53\n"
        "v_mul_f32 v114, v114, v54\n"
        "v_mul_f32 v115, v115, v55\n"
        "v_mfma_f32_16x16x32_bf16 v[96:99], v[20:23], v[144:147], v[96:99]\n"
        "v_mul_f32 v60, s58, v60\n"
        "v_mul_f32 v112, v112, v160\n"
        "v_mul_f32 v113, v113, v160\n"
        "v_mul_f32 v114, v114, v160\n"
        "v_mul_f32 v115, v115, v160\n"
        "v_fma_f32 v60, v60, v160, v19\n"
        "v_cvt_pk_bf16_f32 v52, v112, v113\n"
        "v_cvt_pk_bf16_f32 v54, v114, v115\n"
        "v_mfma_f32_16x16x32_bf16 v[104:107], v[28:31], v[144:147], 0\n"
        "v_cvt_pk_bf16_f32 v65, v96, v97\n"
        "v_cvt_pk_bf16_f32 v69, v98, v99\n"
        "v_cvt_pk_bf16_f32 v53, v60, 0\n"
        "v_mfma_f32_16x16x32_bf16 v[104:107], v[32:35], v[88:91], v[104:107]\n"
        "v_mfma_f32_16x16x32_bf16 v[104:107], v[136:139], v[148:151], v[104:107]\n"
        "ds_read_b128 v[112:115], v0 offset:12352\n"
        "v_mfma_f32_16x16x32_bf16 v[96:99], v[164:167], v[148:151], 0\n"
        "ds_read_b128 v[120:123], v0 offset:13376\n"
        "v_mfma_f32_16x16x32_bf16 v[96:99], v[40:43], v[88:91], v[96:99]\n"
        "ds_read_b128 v[128:131], v0 offset:14400\n"
        "v_mfma_f32_16x16x32_bf16 v[96:99], v[36:39], v[144:147], v[96:99]\n"
        "s_waitcnt lgkmcnt(2)\n"
        "v_mfma_f32_16x16x32_bf16 v[144:147], v[36:39], v[112:115], 0\n"
        "v_cvt_pk_bf16_f32 v62, v104, v105\n"
        "v_cvt_pk_bf16_f32 v63, v106, v107\n"
        "s_waitcnt lgkmcnt(1)\n"
        "v_mfma_f32_16x16x32_bf16 v[144:147], v[40:43], v[120:123], v[144:147]\n"
        "s_waitcnt lgkmcnt(0)\n"
        "v_mfma_f32_16x16x32_bf16 v[144:147], v[164:167], v[128:131], v[144:147]\n"
        "v_cvt_pk_bf16_f32 v60, v96, v97\n"
        "v_cvt_pk_bf16_f32 v61, v98, v99\n"
        "v_mfma_f32_16x16x32_bf16 v[148:151], v[136:139], v[128:131], 0\n"
        "v_mfma_f32_16x16x32_bf16 v[148:151], v[32:35], v[120:123], v[148:151]\n"
        "v_mfma_f32_16x16x32_bf16 v[148:151], v[28:31], v[112:115], v[148:151]\n"
        "ds_read_b128 v[104:107], v0 offset:17488\n"
        "v_mfma_f32_16x16x32_bf16 v[140:143], v[20:23], v[112:115], 0\n"
        "ds_read_b128 v[96:99], v0 offset:16464\n"
        "v_cvt_pk_bf16_f32 v88, v144, v145\n"
        "v_cvt_pk_bf16_f32 v89, v146, v147\n"
        "v_mfma_f32_16x16x32_bf16 v[140:143], v[24:27], v[120:123], v[140:143]\n"
        "ds_read_b128 v[112:115], v0 offset:15440\n"
        "v_mfma_f32_16x16x32_bf16 v[140:143], v[116:119], v[128:131], v[140:143]\n"
        "v_cvt_pk_bf16_f32 v90, v148, v149\n"
        "v_cvt_pk_bf16_f32 v91, v150, v151\n"
        "s_waitcnt lgkmcnt(2)\n"
        "v_mfma_f32_16x16x32_bf16 v[120:123], v[116:119], v[104:107], 0\n"
        "s_waitcnt lgkmcnt(1)\n"
        "v_mfma_f32_16x16x32_bf16 v[120:123], v[24:27], v[96:99], v[120:123]\n"
        "s_waitcnt lgkmcnt(0)\n"
        "v_mfma_f32_16x16x32_bf16 v[120:123], v[20:23], v[112:115], v[120:123]\n"
        "ds_read2_b32 v[70:71], v11 offset0:0 offset1:3\n"
        "v_cvt_pk_bf16_f32 v77, v140, v141\n"
        "v_cvt_pk_bf16_f32 v81, v142, v143\n"
        "v_mov_b32_dpp v95, v19 row_shr:4 row_mask:0xf bank_mask:0xf bound_ctrl:0\n"
        "v_mfma_f32_16x16x32_bf16 v[20:23], v[28:31], v[112:115], 0\n"
        "ds_read_b64_tr_b16 v[66:67], v9 offset:40064\n"
        "v_mfma_f32_16x16x32_bf16 v[20:23], v[32:35], v[96:99], v[20:23]\n"
        "ds_read2_b32 v[82:83], v11 offset0:1 offset1:4\n"
        "v_cvt_pk_bf16_f32 v45, v120, v121\n"
        "v_cvt_pk_bf16_f32 v49, v122, v123\n"
        "v_mfma_f32_16x16x32_bf16 v[20:23], v[136:139], v[104:107], v[20:23]\n"
        "ds_read_b64_tr_b16 v[78:79], v9 offset:40080\n"
        "v_mfma_f32_16x16x32_bf16 v[24:27], v[164:167], v[104:107], 0\n"
        "ds_read2_b32 v[50:51], v11 offset0:2 offset1:5\n"
        "v_mfma_f32_16x16x32_bf16 v[24:27], v[40:43], v[96:99], v[24:27]\n"
        "s_add_u32 s31, s31, 1\n"
        "ds_read_b64_tr_b16 v[46:47], v9 offset:40096\n"
        "v_mfma_f32_16x16x32_bf16 v[24:27], v[36:39], v[112:115], v[24:27]\n"
        "s_mul_i32 s38, s31, 1344\n"
        "v_cvt_pk_bf16_f32 v106, v20, v21\n"
        "v_cvt_pk_bf16_f32 v107, v22, v23\n"
        "v_mfma_f32_16x16x32_bf16 v[20:23], v[72:75], v[56:59], 0\n"
        "s_waitcnt lgkmcnt(4)\n"
        "s_cmp_ge_u32 s31, s76\n"
        "v_smfmac_f32_16x16x64_bf16 v[20:23], v[92:95], v[64:71], v18\n"
        "s_cbranch_scc1 L50B_%=\n"
        "L51B_%=:\n"
        "v_cvt_pk_bf16_f32 v104, v24, v25\n"
        "v_cvt_pk_bf16_f32 v105, v26, v27\n"
        "v_mfma_f32_16x16x32_bf16 v[24:27], v[72:75], v[84:87], 0\n"
        "s_waitcnt lgkmcnt(2)\n"
        "s_cmp_ge_u32 s31, s71\n"
        "v_smfmac_f32_16x16x64_bf16 v[24:27], v[92:95], v[76:83], v18\n"
        "s_cselect_b32 s39, s70, s72\n"
        "ds_read_b64_tr_b16 v[66:67], v9 offset:40112\n"
        "v_mfma_f32_16x16x32_bf16 v[28:31], v[72:75], v[100:103], 0\n"
        "s_waitcnt lgkmcnt(1)\n"
        "s_cmp_ge_u32 s31, 20\n"
        "v_smfmac_f32_16x16x64_bf16 v[28:31], v[92:95], v[44:51], v18\n"
        "s_cselect_b32 s39, s22, s39\n"
        "ds_read_b64_tr_b16 v[78:79], v9 offset:40128\n"
        "v_cvt_pk_bf16_f32 v160, v20, v24\n"
        "v_cvt_pk_bf16_f32 v152, v21, v25\n"
        "v_cvt_pk_bf16_f32 v156, v22, v26\n"
        "v_cvt_pk_bf16_f32 v164, v23, v27\n"
        "v_mfma_f32_16x16x32_bf16 v[36:39], v[72:75], v[60:63], 0\n"
        "s_cmp_ge_u32 s31, 21\n"
        "s_waitcnt lgkmcnt(1)\n"
        "v_smfmac_f32_16x16x64_bf16 v[36:39], v[92:95], v[64:71], v18 abid:1\n"
        "s_cselect_b32 s39, s23, s39\n"
        "ds_read_b64_tr_b16 v[46:47], v9 offset:40144\n"
        "v_mfma_f32_16x16x32_bf16 v[40:43], v[72:75], v[88:91], 0\n"
        "s_add_u32 s39, s39, s38\n"
        "ds_read_b128 v[20:23], v2\n"
        "v_mov_b64 v[24:25], 0\n"
        "v_mov_b64 v[26:27], 0\n"
        "s_waitcnt lgkmcnt(2)\n"
        "v_smfmac_f32_16x16x64_bf16 v[40:43], v[92:95], v[76:83], v18 abid:1\n"
        "s_mul_i32 s68, s31, 3\n"
        "ds_read_b128 v[32:35], v3\n"
        "v_cvt_pk_bf16_f32 v161, v28, v36\n"
        "v_cvt_pk_bf16_f32 v153, v29, v37\n"
        "v_cvt_pk_bf16_f32 v157, v30, v38\n"
        "v_cvt_pk_bf16_f32 v165, v31, v39\n"
        "v_mfma_f32_16x16x32_bf16 v[96:99], v[72:75], v[104:107], 0\n"
        "s_lshr_b32 s38, s31, 3\n"
        "v_mov_b64 v[28:29], 0\n"
        "v_mov_b64 v[30:31], 0\n"
        "s_waitcnt lgkmcnt(2)\n"
        "v_smfmac_f32_16x16x64_bf16 v[96:99], v[92:95], v[44:51], v18 abid:1\n"
        "s_add_u32 s38, s38, 11\n"
        "s_waitcnt lgkmcnt(1)\n"
        "v_mov_b32_dpp v135, v19 row_shr:4 row_mask:0xf bank_mask:0xf bound_ctrl:0\n"
        "v_smfmac_f32_16x16x64_bf16 v[24:27], v[20:23], v[56:63], v4\n"
        "s_bitcmp1_b32 s31, 0\n"
        "v_mov_b64 v[36:37], 0\n"
        "v_mov_b64 v[38:39], 0\n"
        "s_waitcnt lgkmcnt(0)\n"
        "v_smfmac_f32_16x16x64_bf16 v[24:27], v[32:35], v[64:71], v4 abid:1\n"
        "s_cselect_b32 s38, 3, s38\n"
        "s_add_u32 s37, s35, 0\n"
        "v_cvt_pk_bf16_f32 v162, v40, v96\n"
        "v_cvt_pk_bf16_f32 v154, v41, v97\n"
        "v_cvt_pk_bf16_f32 v158, v42, v98\n"
        "v_cvt_pk_bf16_f32 v166, v43, v99\n"
        "v_smfmac_f32_16x16x64_bf16 v[28:31], v[20:23], v[84:91], v4\n"
        "s_add_u32 s68, s68, s38\n"
        "v_add_f32 v24, v24, v25\n"
        "v_add_f32 v26, v26, v27\n"
        "v_smfmac_f32_16x16x64_bf16 v[28:31], v[32:35], v[76:83], v4 abid:1\n"
        "s_cmp_lt_u32 s31, s14\n"
        "ds_read_b64_tr_b16 v[66:67], v9 offset:43392\n"
        "v_smfmac_f32_16x16x64_bf16 v[36:39], v[20:23], v[100:107], v4\n"
        "buffer_store_dwordx3 v[160:162], v12, s[8:11], s37 offen sc0 sc1\n"
        "s_cselect_b32 s68, s68, 0\n"
        "v_smfmac_f32_16x16x64_bf16 v[36:39], v[32:35], v[44:51], v4 abid:1\n"
        "s_and_b32 s38, s31, 7\n"
        "ds_read_b64_tr_b16 v[78:79], v9 offset:43408\n"
        "v_add_f32 v28, v28, v29\n"
        "v_add_f32 v30, v30, v31\n"
        "v_cvt_pk_bf16_f32 v148, v24, v28\n"
        "v_mfma_f32_16x16x32_bf16 v[20:23], v[124:127], v[56:59], 0\n"
        "s_cselect_b32 s30, 1, -7\n"
        "s_waitcnt lgkmcnt(1)\n"
        "v_smfmac_f32_16x16x64_bf16 v[20:23], v[132:135], v[64:71], v18\n"
        "s_add_u32 s37, s35, 0\n"
        "s_mul_i32 s29, s30, 192\n"
        "ds_read_b64_tr_b16 v[46:47], v9 offset:43424\n"
        "v_add_f32 v36, v36, v37\n"
        "v_add_f32 v38, v38, v39\n"
        "v_cvt_pk_bf16_f32 v149, v36, v26\n"
        "v_cvt_pk_bf16_f32 v150, v30, v38\n"
        "v_mfma_f32_16x16x32_bf16 v[160:163], v[124:127], v[84:87], 0\n"
        "s_mul_i32 s30, s30, 392\n"
        "s_waitcnt lgkmcnt(1)\n"
        "v_smfmac_f32_16x16x64_bf16 v[160:163], v[132:135], v[76:83], v18\n"
        "s_cmp_eq_u32 s73, s18\n"
        "ds_read_b64_tr_b16 v[66:67], v9 offset:43440\n"
        "v_mfma_f32_16x16x32_bf16 v[96:99], v[124:127], v[100:103], 0\n"
        "buffer_store_dwordx3 v[152:154], v15, s[8:11], s37 offen offset:768 sc0 sc1\n"
        "s_cselect_b32 s34, -7488, s75\n"
        "s_waitcnt lgkmcnt(1)\n"
        "v_smfmac_f32_16x16x64_bf16 v[96:99], v[132:135], v[44:51], v18\n"
        "s_add_u32 s73, s73, s34\n"
        "ds_read_b64_tr_b16 v[78:79], v9 offset:43456\n"
        "v_mov_b32_dpp v55, v19 row_shr:4 row_mask:0xf bank_mask:0xf bound_ctrl:0\n"
        "v_writelane_b32 v10, s73, 56\n"
        "v_add_u32 v11, s30, v11\n"
        "v_mfma_f32_16x16x32_bf16 v[128:131], v[124:127], v[60:63], 0\n"
        "s_mov_b64 exec, s[62:63]\n"
        "v_add_u32 v16, s29, v16\n"
        "v_add_u32 v8, s34, v8\n"
        "s_mov_b64 exec, -1\n"
        "s_mov_b64 exec, s[50:51]\n"
        "v_add_u32 v5, s29, v5\n"
        "v_add_u32 v6, s34, v6\n"
        "s_mov_b64 exec, -1\n"
        "s_waitcnt lgkmcnt(1)\n"
        "v_smfmac_f32_16x16x64_bf16 v[128:131], v[132:135], v[64:71], v18 abid:1\n"
        "s_add_u32 s37, s35, 98112\n"
        "ds_read_b64_tr_b16 v[46:47], v9 offset:43472\n"
        "v_mfma_f32_16x16x32_bf16 v[92:95], v[124:127], v[88:91], 0\n"
        "s_waitcnt lgkmcnt(1)\n"
        "v_smfmac_f32_16x16x64_bf16 v[92:95], v[132:135], v[76:83], v18 abid:1\n"
        "ds_read_b64_tr_b16 v[66:67], v9 offset:46720\n"
        "v_mfma_f32_16x16x32_bf16 v[120:123], v[124:127], v[104:107], 0\n"
        "buffer_store_dwordx3 v[156:158], v12, s[8:11], s37 offen offset:192 sc0 sc1 nt\n"
        "ds_read_b32 v167, v0 offset:21600\n"
        "s_waitcnt lgkmcnt(2)\n"
        "v_smfmac_f32_16x16x64_bf16 v[120:123], v[132:135], v[44:51], v18 abid:1\n"
        "ds_read_b64_tr_b16 v[78:79], v9 offset:46736\n"
        "v_mfma_f32_16x16x32_bf16 v[72:75], v[108:111], v[56:59], 0\n"
        "s_waitcnt lgkmcnt(2)\n"
        "v_smfmac_f32_16x16x64_bf16 v[72:75], v[52:55], v[64:71], v18\n"
        "s_add_u32 s37, s35, 98112\n"
        "ds_read_b64_tr_b16 v[46:47], v9 offset:46752\n"
        "v_mfma_f32_16x16x32_bf16 v[132:135], v[108:111], v[84:87], 0\n"
        "s_waitcnt lgkmcnt(1)\n"
        "v_smfmac_f32_16x16x64_bf16 v[132:135], v[52:55], v[76:83], v18\n"
        "ds_read_b64_tr_b16 v[66:67], v9 offset:46768\n"
        "v_readfirstlane_b32 s40, v167\n"
        "v_mfma_f32_16x16x32_bf16 v[156:159], v[108:111], v[100:103], 0\n"
        "buffer_store_dwordx3 v[164:166], v14, s[8:11], s37 offen offset:960 sc0 sc1 nt\n"
        "s_add_u32 s37, s35, 588672\n"
        "s_add_u32 s46, s37, 1344\n"
        "s_cmp_lt_i32 s40, s68\n"
        "s_waitcnt lgkmcnt(1)\n"
        "v_smfmac_f32_16x16x64_bf16 v[156:159], v[52:55], v[44:51], v18\n"
        "buffer_store_dwordx3 v[148:150], v12, s[44:47], s37 offen offset:1152 sc0 sc1 nt\n"
        "ds_read_b64_tr_b16 v[78:79], v9 offset:46784\n"
        "v_mfma_f32_16x16x32_bf16 v[124:127], v[108:111], v[60:63], 0\n"
        "s_waitcnt lgkmcnt(1)\n"
        "v_smfmac_f32_16x16x64_bf16 v[124:127], v[52:55], v[64:71], v18 abid:1\n"
        "ds_read_b64_tr_b16 v[46:47], v9 offset:46800\n"
        "s_mov_b64 exec, 1\n"
        "ds_inc_u32 v0, v1 offset:21612\n"
        "s_mov_b64 exec, -1\n"
        "s_cbranch_scc1 L54B_%=\n"
        "L55B_%=:\n"
        "ds_read_b128 v[36:39], v7 offset:0\n"
        "ds_read_b128 v[28:31], v7 offset:3328\n"
        "ds_read_b128 v[40:43], v7 offset:64\n"
        "ds_read_b128 v[32:35], v7 offset:3392\n"
        "v_mfma_f32_16x16x32_bf16 v[100:103], v[108:111], v[88:91], 0\n"
        "s_waitcnt lgkmcnt(6)\n"
        "ds_read_b128 v[164:167], v7 offset:128\n"
        "ds_read_b128 v[136:139], v7 offset:3456\n"
        "s_mov_b32 s36, s35\n"
        "v_smfmac_f32_16x16x64_bf16 v[100:103], v[52:55], v[76:83], v18 abid:1\n"
        "s_mov_b32 s35, s39\n"
        "v_mfma_f32_16x16x32_bf16 v[88:91], v[108:111], v[104:107], 0\n"
        "s_waitcnt lgkmcnt(7)\n"
        "v_smfmac_f32_16x16x64_bf16 v[88:91], v[52:55], v[44:51], v18 abid:1\n"
        "s_setprio 0\n"
        "s_waitcnt lgkmcnt(0)\n"
        "s_nop 15\n"
        "s_branch CNEXTB_%=\n"
        :: "{v0}"(tid), "{s[0:1]}"(kap), "{s2}"(bid), "{s[4:5]}"(tab) : CLOBBERS);
}

extern "C" void kernel_launch(void* const* d_in, const int* in_sizes, int n_in, void* d_out, int out_size, void* d_ws, size_t ws_size, hipStream_t stream) {
    (void)in_sizes; (void)n_in; (void)out_size; (void)ws_size;
    (void)hipFuncSetAttribute((const void*)swin_kernel, hipFuncAttributeMaxDynamicSharedMemorySize, LDS_BYTES);
    swin_kernel<<<256, 512, LDS_BYTES, stream>>>((const char*)d_in[0], (const char*)d_in[1], (const char*)d_in[2], (const char*)d_in[3], (const char*)d_in[4], (char*)d_out, (const char*)d_ws);
}
```

```cpp
#define LTOK_ 208
#define FPAD 32
#define NPR 36
#define NWC 22
#define LK0 0
#define LK1 0
#define LK2 1
#define LK3 2
#include <hip/hip_runtime.h>
#include <hip/hip_bf16.h>

constexpr int TOKB = 192, ROWB = 511 * TOKB;
constexpr unsigned FRAMEB = 511u * ROWB;
constexpr int LTOK = LTOK_;
constexpr int LFRAME = 48 * LTOK + FPAD;
constexpr int LBUF = 4 * LFRAME;
constexpr int LSTASH = 10 * 4 * LTOK;
constexpr int LSTG = 6208;
constexpr int VST = 392, LEXT = 1536 + 8 * VST;
constexpr int WT = 3088;
constexpr int LDS_AUX = (54 - NPR) / 3 * WT, LDS_TAB = LDS_AUX + 1024, LDS_CNT = LDS_TAB + 2048, LDS_BUF = LDS_CNT + 128, LDS_STASH = LDS_BUF + 2 * LBUF,
              LDS_STG = LDS_STASH + LSTASH, LDS_SCRAP = LDS_STG + 4 * LSTG, LDS_EXT = LDS_SCRAP + 512, LDS_BYTES = 163840;
static_assert(NWC >= 18 && NPR % 3 == 0 && LDS_EXT + 4 * LEXT + 256 <= LDS_BYTES && NPR * 1024 <= LBUF, "LDS");
constexpr unsigned RAWM = 18432, RAW_T = LDS_BUF, RAW_TV = RAW_T + 2 * RAWM + 128, RAW_SV = (36 - NPR) / 3 * WT, RAW_SO = LDS_EXT, RAW_SQ = LDS_BUF + LBUF, RAW_SK = RAW_SQ + RAWM;
static_assert(RAW_SV + RAWM <= LDS_AUX && RAW_SO + RAWM <= LDS_BYTES && RAW_SK + RAWM <= LDS_STASH && RAW_TV + 2 * RAWM <= LDS_STASH && RAWM + 6 * WT <= 2 * RAWM + 128, "fold LDS");

constexpr int phi(int v) { return v ^ (v >> 1); }
struct Tab {
    unsigned img[3][64][4];
    unsigned p[4][7][64][4];
    unsigned c[4][6][64][4];
    unsigned d[4][7][64][4];
    static constexpr void job(unsigned (*q)[64][4], int lane, unsigned L, unsigned R, int mode, int tau, unsigned st0, unsigned st1) {
        const int i = lane & 15, g = lane >> 4;
        for (int a = 0; a < 2; ++a) {
            const unsigned ra = (8 * (i >> 2) + 4 * a + (i & 3)) * 192 + ((g ^ ((2 * (i >> 2) + a) & 3)) * 16);
            q[0][lane][a] = L + ra;
            q[0][lane][2 + a] = mode == 0 ? R + tau * 6144 + ra : mode == 2 ? R + tau * 6144 + (16 * a + i) * 192 + ((g ^ (i >> 2)) * 16)
                                          : R + tau * 64 + (8 * g + 4 * a + (i >> 2)) * 192 + (((i & 3) ^ ((2 * g + a) & 3)) * 16);
        }
        q[1][lane][0] = st0; q[1][lane][1] = st1; q[1][lane][2] = LDS_SCRAP + g * 32;
    }
    static constexpr unsigned scat(bool win, int lane, int tau, int tb) {
        const int i = lane & 15, c = 32 * tau + 8 * (i >> 2) + 4 * tb + (i & 3), r = c / 24, o = c % 24, q = win ? o / 6 : o & 3, t = win ? o % 6 : o >> 2;
        return (win ? RAW_SV : 3 * t < NPR - 18 ? LDS_BUF + 18 * 1024 : -(NPR - 18) / 3 * WT) + t * WT + ((lane & 48) + 4 * q + r) * 16;
    }
    constexpr Tab() : img{}, p{}, c{}, d{} {
        for (int e = 0; e < 48; ++e) img[0][e >> 2][e & 3] = 0x3f803f80u;
        img[0][12][0] = 0x7f800000u;
        for (int e = 0; e < 8; ++e) img[0][36 + 10 + e][e >> 1] = e & 1 ? 0x3f800000u : 0x3f80u;
        for (int k = 0; k < 4; ++k) for (int lane = 0; lane < 64; ++lane) {
            const int i = lane & 15, g = lane >> 4, rowv = 8 * (i >> 2) + (i & 3);
            unsigned vi[20] = {};
            {
                const int pp = i >> 2, t = i & 3;
                const int t1 = (k + 3 - LK1) & 3;
                if (t1 < 3) job(p[k], lane, RAW_TV, RAW_TV + RAWM, 1, t1, scat(false, lane, t1, 0), scat(false, lane, t1, 1));
                const unsigned wl = lane * 16, lm = pp == g ? ~0u : 0u, idp = LDS_AUX + 576 + (t == g ? 10 + pp : 0) * 16, sidx = 0xdddd;
                unsigned vA[3] = {};
                for (int m = 0; m < 3; ++m) {
                    const int n = 64 * m + lane, tf = n / 96, q = n % 96 / 12, s = (n % 12) ^ tf;
                    const unsigned cc = tf * FRAMEB + (3 * (s & 3) + (s >> 2)) * 16 + 2048 - 1024 * m;
                    vA[m] = cc + q * TOKB;
                    img[1][lane][m] = (cc + 6 * ROWB + 6 * TOKB) | ((unsigned)(2 * (q & 3) + (q >> 2)) << 28);
                }
                const unsigned vA0 = vA[0], vA1 = vA[1], vA2 = vA[2], vB0 = vA0 + (ROWB - 7 * TOKB), vB1 = vA1 + (ROWB - 7 * TOKB), vB2 = vA2 + (ROWB - 7 * TOKB);
                const unsigned dA = LDS_BUF + t * LFRAME + pp * LTOK + g * 16, rA = LDS_BUF + t * LFRAME + (pp == g ? pp : 4 + g) * LTOK + 192;
                img[1][lane][3] = (2 * pp * (4 * LTOK) + t * LTOK + g * 16) | (((pp == g ? 2 * pp : 2 * g + 1) * (4 * LTOK) + t * LTOK + 192) << 16);
                const unsigned cd0 = dA + k * 8 * LTOK, cr0 = rA + k * 8 * LTOK;
                const unsigned xr = LDS_STG + k * LSTG + (t >> 1) * 3104 + ((t & 1) * 8 + pp) * TOKB + (g ^ (t & 1)) * 16;
                const unsigned axp = LDS_AUX + (t ? 384 : 0);
                const unsigned pf = g * FRAMEB + (i >> 1) * TOKB + (i & 1) * 188 + 2048;
                vi[0] = xr; vi[1] = lm; vi[2] = rA; vi[3] = dA; vi[4] = vA0; vi[5] = vA1; vi[6] = vA2; vi[7] = vB0; vi[8] = vB1; vi[9] = vB2; vi[10] = sidx; vi[11] = cd0; vi[12] = cr0; vi[13] = wl; vi[14] = idp; vi[15] = pf; vi[16] = axp;
                for (int e = 0; e < 20; ++e) p[k][2 + (e >> 2)][lane][e & 3] = vi[e];
            }
            {
                const int t0 = (k + 3 - LK0) & 3, t2 = (k + 3 - LK2) & 3, t3 = (k + 3 - LK3) & 3;
                const unsigned s0 = LDS_BUF + t0 * 6144 + lane * 16, s3 = LDS_BUF + LBUF + t3 * 6144 + lane * 16;
                if (t0 < 3) job(c[k], lane, RAW_T, RAW_T + RAWM, 2, t0, s0, s0 + 3072); else job(c[k], lane, RAW_T, RAW_T + RAWM, 1, 0, LDS_SCRAP + rowv * 4, 0);
                if (t2 < 3) job(c[k] + 4, lane, RAW_SV, RAW_SO, 1, t2, scat(true, lane, t2, 0), scat(true, lane, t2, 1));
                if (t3 < 3) job(d[k], lane, RAW_SQ, RAW_SK, 0, t3, s3, s3 + 3072); else job(d[k], lane, RAW_SQ, RAW_SK, 1, 0, LDS_SCRAP + rowv * 4, 0);
                for (int m = 0; m < 3; ++m) {
                    const int n = 64 * m + lane, row = n / 12, pc = ((n % 12) ^ ((row >> 2) & 3)) * 16;
                    c[k][3][lane][m] = (24 * (row & 3) + 12 * ((row >> 2) & 1) + 2 * (row >> 3)) * 576 + pc;
                    c[k][2][lane][m] = row * 192 + pc;
                }
                const int dg = 16 * (2 * (g & 1) + (g >> 1)), pix = 4 * (i & 3) + phi(i >> 2);
                const unsigned wl = lane * 16, fr = LDS_BUF + k * LFRAME, ext = LDS_EXT + k * LEXT;
                const unsigned ta = fr + pix * LTOK + dg, zr = LDS_AUX + 384 + g * 16, t30 = i & 3 ? zr : LDS_STASH + k * LTOK + dg;
                const unsigned xa = fr + (4 * (i >> 2) + phi(g)) * LTOK + 96 * (i & 1) + 8 * ((i >> 1) & 1);
                const int sg = 8 * ((i >> 1) & 1) + 4 * (i >> 3) + 2 * ((i >> 2) & 1) + (i & 1);
                const unsigned tv = fr + (32 + 4 * (sg & 3) + phi(sg >> 2)) * LTOK + dg, c1f = (i >> 2) == 2 * g ? 0x3f800000u : 0u;
                const unsigned sidx = g < 2 ? 0x0e0e0404u | ((i >> 2) == 2 * g ? (i & 3) * 0x00100010u : (i >> 2) == 2 * g + 1 ? (i & 3) * 0x40004000u : 0u) : 0xeeee4444u;
                const unsigned ga0 = phi(g) * TOKB + 48 * (i & 3) + 12 * (i >> 2), wr = ROWB - 7 * TOKB;
                const unsigned ga1 = ga0 + (phi(g) >= 1 ? wr : 0), ga2 = ga0 + (phi(g) >= 2 ? wr : 0), ga3 = ga0 + (phi(g) >= 3 ? wr : 0);
                const unsigned pb = LDS_SCRAP + k * 128, pr0 = i < 4 && (i >> 1) == (g >> 1) ? pb + (i & 1) * 32 + (g & 1) * 16 : zr, pr1 = i == 0 || i == 2 ? pb + 64 + g * 16 : zr;
                const unsigned pw = g < 2 ? pb + ((i >> 1) & 1) * 32 + (i >> 3) * 16 + ((i >> 2) & 1) * 8 + g * 4 + (i & 1) * 2
                                          : pb + 64 + (i >> 2) * 16 + (g & 1) * 4 + ((i >> 1) & 1) * 8 + (i & 1) * 2 + (lane == 57 ? 6 : 0);
                const unsigned six2 = (i & 1 ? 0xeeeeu : 0x4444u) | ((i & 2 ? 0xeeee0000u : 0x44440000u) & (g < 2 ? 0x0f0f0000u : 0xffff0000u));
                const unsigned exs0 = i & 2 ? LDS_AUX + g * 16 : ext + (i & 1) * 64 + g * 16, t3s0 = i & 2 ? zr : LDS_STASH + k * LTOK + 64 * (g >> 1) + 16 * (g & 1) + 4 * (i & 1);
                const unsigned ex0 = i & 1 ? zr : i & 2 ? LDS_AUX + g * 16 : ext + g * 16, exv0 = ext + 1536 + i * 24;
                const unsigned rla0 = lane < 48 ? fr + (16 * g + pix) * LTOK + 192 : lane == 56 ? LDS_STASH + k * LTOK + 192 : LDS_AUX + 192;
                img[2][lane][0] = ((i & 7) * (4 * LTOK) + dg) | ((((4 * g + (i & 3)) & 7) * (4 * LTOK) + 96 * ((i >> 2) & 1) + 8 * (i >> 3)) << 16);
                img[2][lane][1] = ((i & 7) * 192u + g * 16) | ((1536u + i * 24 + (g & 1) * 4 * VST) << 16);
                img[2][lane][2] = ((fr + phi(g) * LTOK + 192 - xa) & 0xffffu) | ((fr + pix * LTOK + 192 - xa) << 16);
                img[2][lane][3] = (LDS_AUX + 576 + (10 + (i & 3)) * 16) | ((1536u + (i & 7) * VST + 384) << 16);
                vi[0] = wl; vi[1] = pw; vi[2] = pr0; vi[3] = pr1; vi[4] = six2; vi[5] = exs0; vi[6] = t3s0; vi[7] = ta; vi[8] = t30; vi[9] = xa; vi[10] = rla0; vi[11] = exv0; vi[12] = ga0; vi[13] = ga1; vi[14] = ga2; vi[15] = ga3; vi[16] = ex0; vi[17] = tv; vi[18] = sidx; vi[19] = c1f;
                for (int e = 0; e < 20; ++e) d[k][2 + (e >> 2)][lane][e & 3] = vi[e];
            }
        }
    }
};
static __device__ const Tab TAB __attribute__((aligned(128))) = Tab();

#define CLOBBERS "v1", "v2", "v3", "v4", "v5", "v6", "v7", "v8", "v9", "v10", "v11", "v12", "v13", "v14", "v15", "v16", "v17", "v18", "v19", "v20", "v21", "v22", "v23", "v24", "v25", "v26", "v27", "v28", "v29", "v30", "v31", "v32", "v33", "v34", "v35", "v36", "v37", "v38", "v39", "v40", "v41", "v42", "v43", "v44", "v45", "v46", "v47", "v48", "v49", "v50", "v51", "v52", "v53", "v54", "v55", "v56", "v57", "v58", "v59", "v60", "v61", "v62", "v63", "v64", "v65", "v66", "v67", "v68", "v69", "v70", "v71", "v72", "v73", "v74", "v75", "v76", "v77", "v78", "v79", "v80", "v81", "v82", "v83", "v84", "v85", "v86", "v87", "v88", "v89", "v90", "v91", "v92", "v93", "v94", "v95", "v96", "v97", "v98", "v99", "v100", "v101", "v102", "v103", "v104", "v105", "v106", "v107", "v108", "v109", "v110", "v111", "v112", "v113", "v114", "v115", "v116", "v117", "v118", "v119", "v120", "v121", "v122", "v123", "v124", "v125", "v126", "v127", "v128", "v129", "v130", "v131", "v132", "v133", "v134", "v135", "v136", "v137", "v138", "v139", "v140", "v141", "v142", "v143", "v144", "v145", "v146", "v147", "v148", "v149", "v150", "v151", "v152", "v153", "v154", "v155", "v156", "v157", "v158", "v159", "v160", "v161", "v162", "v163", "v164", "v165", "v166", "v167", "v168", "v169", "v170", "v171", "v172", "v173", "v174", "v175", "v176", "v177", "v178", "v179", "v180", "v181", "v182", "v183", "v184", "v185", "v186", "v187", "v188", "v189", "v190", "v191", "v192", "v193", "v194", "v195", "v196", "v197", "v198", "v199", "v200", "v201", "v202", "v203", "v204", "v205", "v206", "v207", "v208", "v209", "v210", "v211", "v212", "v213", "v214", "v215", "v216", "v217", "v218", "v219", "v220", "v221", "v222", "v223", "v224", "v225", "v226", "v227", "v228", "v229", "v230", "v231", "v232", "v233", "v234", "v235", "v236", "v237", "v238", "v239", "v240", "v241", "v242", "v243", "v244", "v245", "v246", "v247", "v248", "v249", "v250", "v251", "v252", "v253", "v254", "v255", "s3", "s6", "s7", "s8", "s9", "s10", "s11", "s12", "s13", "s14", "s15", "s16", "s17", "s18", "s19", "s20", "s21", "s22", "s23", "s24", "s25", "s26", "s27", "s28", "s29", "s30", "s31", "s34", "s35", "s36", "s37", "s38", "s39", "s40", "s41", "s42", "s43", "s44", "s45", "s46", "s47", "s48", "s49", "s50", "s51", "s52", "s53", "s54", "s55", "s56", "s57", "s58", "s59", "s60", "s61", "s62", "s63", "s64", "s65", "s66", "s67", "s68", "s69", "s70", "s71", "s72", "s73", "s74", "s75", "s76", "s77", "vcc", "scc", "memory"
__global__ __launch_bounds__(512, 2) void swin_kernel(const char* x, const char* wqkv_t, const char* wo_t, const char* wqkv_s, const char* wo_s, char* out, const char* ws) {
    const unsigned long long kap = (unsigned long long)__builtin_amdgcn_kernarg_segment_ptr(), tab = (unsigned long long)&TAB;
    const unsigned tid = threadIdx.x, bid = blockIdx.x;
    asm volatile(
        "s_load_dwordx8 s[44:51], s[0:1], 0x0\n"
        "s_load_dwordx4 s[52:55], s[0:1], 0x20\n"
        "s_load_dwordx2 s[56:57], s[0:1], 0x30\n"
        "v_readfirstlane_b32 s3, v0\n"
        "v_and_b32 v56, 63, v0\n"
        "v_lshlrev_b32 v57, 4, v56\n"
        "s_bfe_u32 s3, s3, 0x30006\n"
        "s_and_b32 s6, s3, 3\n"
        "s_mul_i32 s64, s3, 9216\n"
        "s_add_u32 s64, s64, 21728\n"
        "s_mov_b32 s27, 0x20000\n"
        "s_cmp_lt_u32 s3, 4\n"
        "s_cbranch_scc0 PPRO_%=\n"
        "s_mul_i32 s7, s6, 6144\n"
        "s_add_u32 s7, s7, 33792\n"
        "s_add_u32 s22, s4, s7\n"
        "s_addc_u32 s23, s5, 0\n"
        "global_load_dwordx4 v[240:243], v57, s[22:23] offset:-2048\n"
        "global_load_dwordx4 v[244:247], v57, s[22:23] offset:-1024\n"
        "global_load_dwordx4 v[36:39], v57, s[22:23] offset:1024\n"
        "global_load_dwordx4 v[48:51], v57, s[22:23] offset:2048\n"
        "global_load_dwordx4 v[52:55], v57, s[22:23] offset:3072\n"
        "s_mov_b32 s65, 13824\n"
        "v_add_u32 v24, 0, v56\n"
        "v_mul_u32_u24 v21, 171, v24\n"
        "v_lshrrev_b32 v21, 11, v21\n"
        "v_mad_i32_i24 v24, v21, -12, v24\n"
        "v_bfe_u32 v58, v21, 2, 2\n"
        "v_xor_b32 v58, v24, v58\n"
        "v_and_b32 v27, 7, v21\n"
        "v_mul_u32_u24 v27, 576, v27\n"
        "v_lshl_add_u32 v58, v58, 4, v27\n"
        "v_add_u32 v25, 64, v56\n"
        "v_mul_u32_u24 v22, 171, v25\n"
        "v_lshrrev_b32 v22, 11, v22\n"
        "v_mad_i32_i24 v25, v22, -12, v25\n"
        "v_bfe_u32 v59, v22, 2, 2\n"
        "v_xor_b32 v59, v25, v59\n"
        "v_and_b32 v27, 7, v22\n"
        "v_mul_u32_u24 v27, 576, v27\n"
        "v_lshl_add_u32 v59, v59, 4, v27\n"
        "v_lshrrev_b32 v27, 3, v22\n"
        "v_mad_u32_u24 v59, v27, s65, v59\n"
        "v_add_u32 v26, 128, v56\n"
        "v_mul_u32_u24 v23, 171, v26\n"
        "v_lshrrev_b32 v23, 11, v23\n"
        "v_mad_i32_i24 v26, v23, -12, v26\n"
        "v_bfe_u32 v60, v23, 2, 2\n"
        "v_xor_b32 v60, v26, v60\n"
        "v_and_b32 v27, 7, v23\n"
        "v_mul_u32_u24 v27, 576, v27\n"
        "v_lshl_add_u32 v60, v60, 4, v27\n"
        "v_add_u32 v60, s65, v60\n"
        "s_lshr_b32 s61, s6, 1\n"
        "s_mul_i32 s61, s61, 192\n"
        "s_mov_b32 s62, 27648\n"
        "s_mov_b32 s63, 4608\n"
        "s_add_u32 s7, s62, s63\n"
        "s_lshl_b32 s29, s63, 1\n"
        "s_add_u32 s60, s7, s63\n"
        "s_bitcmp1_b32 s6, 0\n"
        "s_cselect_b32 s28, s7, 0\n"
        "s_cselect_b32 s29, s29, s62\n"
        "s_cselect_b32 s60, s60, s63\n"
        "s_add_u32 s28, s28, s61\n"
        "s_add_u32 s29, s29, s61\n"
        "s_add_u32 s60, s60, s61\n"
        "s_mov_b32 s26, 55296\n"
        "s_waitcnt lgkmcnt(0)\n"
        "s_mov_b32 s24, s46\n"
        "s_and_b32 s25, s47, 0xffff\n"
        "s_add_u32 m0, s64, 0\n"
        "s_nop 0\n"
        "buffer_load_dwordx4 v58, s[24:27], s28 offen lds\n"
        "s_add_u32 m0, s64, 1024\n"
        "s_nop 0\n"
        "buffer_load_dwordx4 v59, s[24:27], s28 offen lds\n"
        "s_add_u32 m0, s64, 2048\n"
        "s_nop 0\n"
        "buffer_load_dwordx4 v60, s[24:27], s28 offen lds\n"
        "s_add_u32 m0, s64, 3072\n"
        "s_nop 0\n"
        "buffer_load_dwordx4 v58, s[24:27], s29 offen lds\n"
        "s_add_u32 m0, s64, 4096\n"
        "s_nop 0\n"
        "buffer_load_dwordx4 v59, s[24:27], s29 offen lds\n"
        "s_add_u32 m0, s64, 5120\n"
        "s_nop 0\n"
        "buffer_load_dwordx4 v60, s[24:27], s29 offen lds\n"
        "s_add_u32 m0, s64, 6144\n"
        "s_nop 0\n"
        "buffer_load_dwordx4 v58, s[24:27], s60 offen lds\n"
        "s_add_u32 m0, s64, 7168\n"
        "s_nop 0\n"
        "buffer_load_dwordx4 v59, s[24:27], s60 offen lds\n"
        "s_add_u32 m0, s64, 8192\n"
        "s_nop 0\n"
        "buffer_load_dwordx4 v60, s[24:27], s60 offen lds\n"
        "s_barrier\n"
        "s_setprio 1\n"
        "v_mov_b32 v250, 21600\n"
        "s_mov_b32 s41, 0x400000\n"
        "s_and_b32 s7, s2, 7\n"
        "s_lshl_b32 s7, s7, 5\n"
        "s_lshr_b32 s14, s2, 3\n"
        "s_add_u32 s7, s7, s14\n"
        "s_mul_i32 s7, s7, 20\n"
        "s_mov_b32 s14, 22\n"
        "s_mul_i32 s15, s7, 57457\n"
        "s_lshr_b32 s15, s15, 22\n"
        "s_mul_i32 s16, s15, 73\n"
        "s_sub_u32 s16, s7, s16\n"
        "s_mul_i32 s15, s15, 686784\n"
        "s_mul_i32 s7, s16, 1344\n"
        "s_add_u32 s15, s15, s7\n"
        "s_mov_b32 s8, s54\n"
        "s_and_b32 s9, s55, 0xffff\n"
        "s_mov_b32 s10, 200540928\n"
        "s_mov_b32 s11, 0x20000\n"
        "s_mov_b64 s[12:13], s[54:55]\n"
        "s_mul_i32 s17, s6, 50135232\n"
        "s_mul_i32 s18, s6, 208\n"
        "s_add_u32 s18, s18, 109536\n"
        "s_mul_i32 s19, s6, 4672\n"
        "s_add_u32 s19, s19, 135520\n"
        "s_mul_i32 s7, s6, 7168\n"
        "s_add_u32 s7, s7, 59392\n"
        "s_add_u32 s22, s4, s7\n"
        "s_addc_u32 s23, s5, 0\n"
        "s_waitcnt vmcnt(0)\n"
        "s_mov_b64 exec, 1\n"
        "ds_inc_u32 v250, v250 offset:16\n"
        "s_mov_b64 exec, -1\n"
        "L64_%=:\n"
        "ds_read_b32 v251, v250 offset:16\n"
        "s_waitcnt lgkmcnt(0)\n"
        "v_readfirstlane_b32 s40, v251\n"
        "s_nop 3\n"
        "s_cmp_ge_i32 s40, 4\n"
        "s_cbranch_scc1 L65_%=\n"
        "s_nop 0\n"
        "s_sub_u32 s41, s41, 1\n"
        "s_cbranch_scc0 L64_%=\n"
        "s_endpgm\n"
        "L65_%=:\n"
        "s_cmp_eq_u32 s6, 0\n"
        "s_cbranch_scc1 L66_%=\n"
        "s_mov_b32 s34, 0x4162ccfd\n"
        "s_mov_b32 s35, 21624\n"
        "s_mov_b32 s36, 21624\n"
        "s_mov_b32 s37, 21628\n"
        "s_call_b64 s[30:31], FOLDM_%=\n"
        "s_branch L67_%=\n"
        "L66_%=:\n"
        "s_mov_b32 s34, 0xbe173353\n"
        "s_mov_b32 s35, 21656\n"
        "s_mov_b32 s36, 21656\n"
        "s_mov_b32 s37, 21624\n"
        "s_call_b64 s[30:31], FOLDR_%=\n"
        "s_cmp_lg_u32 s2, 0\n"
        "s_cbranch_scc1 L63_%=\n"
        "s_mov_b64 s[24:25], s[56:57]\n"
        "s_mov_b64 exec, 1\n"
        "v_mov_b32 v27, 0\n"
        "v_mov_b32 v28, 0\n"
        "global_atomic_swap v252, v27, v28, s[24:25] offset:0 sc0\n"
        "s_mov_b64 exec, -1\n"
        "s_waitcnt vmcnt(0)\n"
        "s_mov_b64 exec, 1\n"
        "v_mov_b32 v27, 0\n"
        "v_mov_b32 v28, 1\n"
        "global_atomic_add v252, v27, v28, s[24:25] offset:256 sc0\n"
        "s_mov_b64 exec, -1\n"
        "L63_%=:\n"
        "L67_%=:\n"
        "s_setprio 3\n"
        "s_waitcnt vmcnt(0)\n"
        "v_mov_b64 v[240:241], v[48:49]\n"
        "v_mov_b64 v[242:243], v[50:51]\n"
        "v_mov_b64 v[244:245], v[52:53]\n"
        "v_mov_b64 v[246:247], v[54:55]\n"
        "L68_%=:\n"
        "ds_read_b32 v251, v250 offset:32\n"
        "s_waitcnt lgkmcnt(0)\n"
        "v_readfirstlane_b32 s40, v251\n"
        "s_nop 3\n"
        "s_cmp_ge_i32 s40, 4\n"
        "s_cbranch_scc1 L69_%=\n"
        "s_nop 0\n"
        "s_sub_u32 s41, s41, 1\n"
        "s_cbranch_scc0 L68_%=\n"
        "s_endpgm\n"
        "L69_%=:\n"
        "s_cmp_eq_u32 s6, 1\n"
        "s_cbranch_scc1 L70_%=\n"
        "s_mov_b32 s34, 0x411cc471\n"
        "s_mov_b32 s35, 21640\n"
        "s_mov_b32 s36, 21640\n"
        "s_mov_b32 s37, 21652\n"
        "s_call_b64 s[30:31], FOLDW_%=\n"
        "s_branch L71_%=\n"
        "L70_%=:\n"
        "s_mov_b64 exec, 1\n"
        "ds_inc_u32 v250, v250 offset:40\n"
        "s_mov_b64 exec, -1\n"
        "s_mov_b32 s24, s50\n"
        "s_and_b32 s25, s51, 0xffff\n"
        "s_mov_b32 s26, 55296\n"
        "s_mov_b32 m0, 61792\n"
        "s_mov_b32 s28, 0\n"
        "buffer_load_dwordx4 v36, s[24:27], s28 offen lds\n"
        "s_mov_b32 m0, 62816\n"
        "s_nop 0\n"
        "buffer_load_dwordx4 v37, s[24:27], s28 offen lds\n"
        "s_mov_b32 m0, 63840\n"
        "s_nop 0\n"
        "buffer_load_dwordx4 v38, s[24:27], s28 offen lds\n"
        "s_mov_b32 m0, 64864\n"
        "s_mov_b32 s28, 576\n"
        "buffer_load_dwordx4 v36, s[24:27], s28 offen lds\n"
        "s_mov_b32 m0, 65888\n"
        "s_nop 0\n"
        "buffer_load_dwordx4 v37, s[24:27], s28 offen lds\n"
        "s_mov_b32 m0, 66912\n"
        "s_nop 0\n"
        "buffer_load_dwordx4 v38, s[24:27], s28 offen lds\n"
        "s_mov_b32 m0, 67936\n"
        "s_mov_b32 s28, 2304\n"
        "buffer_load_dwordx4 v36, s[24:27], s28 offen lds\n"
        "s_mov_b32 m0, 68960\n"
        "s_nop 0\n"
        "buffer_load_dwordx4 v37, s[24:27], s28 offen lds\n"
        "s_mov_b32 m0, 69984\n"
        "s_nop 0\n"
        "buffer_load_dwordx4 v38, s[24:27], s28 offen lds\n"
        "s_mov_b32 m0, 71008\n"
        "s_mov_b32 s28, 2880\n"
        "buffer_load_dwordx4 v36, s[24:27], s28 offen lds\n"
        "s_mov_b32 m0, 72032\n"
        "s_nop 0\n"
        "buffer_load_dwordx4 v37, s[24:27], s28 offen lds\n"
        "s_mov_b32 m0, 73056\n"
        "s_nop 0\n"
        "buffer_load_dwordx4 v38, s[24:27], s28 offen lds\n"
        "s_mov_b32 m0, 74080\n"
        "s_mov_b32 s28, 4608\n"
        "buffer_load_dwordx4 v36, s[24:27], s28 offen lds\n"
        "s_mov_b32 m0, 75104\n"
        "s_nop 0\n"
        "buffer_load_dwordx4 v37, s[24:27], s28 offen lds\n"
        "s_mov_b32 m0, 76128\n"
        "s_nop 0\n"
        "buffer_load_dwordx4 v38, s[24:27], s28 offen lds\n"
        "s_mov_b32 m0, 77152\n"
        "s_mov_b32 s28, 5184\n"
        "buffer_load_dwordx4 v36, s[24:27], s28 offen lds\n"
        "s_mov_b32 m0, 78176\n"
        "s_nop 0\n"
        "buffer_load_dwordx4 v37, s[24:27], s28 offen lds\n"
        "s_mov_b32 m0, 79200\n"
        "s_nop 0\n"
        "buffer_load_dwordx4 v38, s[24:27], s28 offen lds\n"
        "s_mov_b32 s24, s50\n"
        "s_and_b32 s25, s51, 0xffff\n"
        "s_mov_b32 s26, 55296\n"
        "s_mov_b32 m0, 80224\n"
        "s_mov_b32 s28, 192\n"
        "buffer_load_dwordx4 v36, s[24:27], s28 offen lds\n"
        "s_mov_b32 m0, 81248\n"
        "s_nop 0\n"
        "buffer_load_dwordx4 v37, s[24:27], s28 offen lds\n"
        "s_mov_b32 m0, 82272\n"
        "s_nop 0\n"
        "buffer_load_dwordx4 v38, s[24:27], s28 offen lds\n"
        "s_mov_b32 m0, 83296\n"
        "s_mov_b32 s28, 768\n"
        "buffer_load_dwordx4 v36, s[24:27], s28 offen lds\n"
        "s_mov_b32 m0, 84320\n"
        "s_nop 0\n"
        "buffer_load_dwordx4 v37, s[24:27], s28 offen lds\n"
        "s_mov_b32 m0, 85344\n"
        "s_nop 0\n"
        "buffer_load_dwordx4 v38, s[24:27], s28 offen lds\n"
        "s_mov_b32 m0, 86368\n"
        "s_mov_b32 s28, 2496\n"
        "buffer_load_dwordx4 v36, s[24:27], s28 offen lds\n"
        "s_mov_b32 m0, 87392\n"
        "s_nop 0\n"
        "buffer_load_dwordx4 v37, s[24:27], s28 offen lds\n"
        "s_mov_b32 m0, 88416\n"
        "s_nop 0\n"
        "buffer_load_dwordx4 v38, s[24:27], s28 offen lds\n"
        "s_mov_b32 m0, 89440\n"
        "s_mov_b32 s28, 3072\n"
        "buffer_load_dwordx4 v36, s[24:27], s28 offen lds\n"
        "s_mov_b32 m0, 90464\n"
        "s_nop 0\n"
        "buffer_load_dwordx4 v37, s[24:27], s28 offen lds\n"
        "s_mov_b32 m0, 91488\n"
        "s_nop 0\n"
        "buffer_load_dwordx4 v38, s[24:27], s28 offen lds\n"
        "s_mov_b32 m0, 92512\n"
        "s_mov_b32 s28, 4800\n"
        "buffer_load_dwordx4 v36, s[24:27], s28 offen lds\n"
        "s_mov_b32 m0, 93536\n"
        "s_nop 0\n"
        "buffer_load_dwordx4 v37, s[24:27], s28 offen lds\n"
        "s_mov_b32 m0, 94560\n"
        "s_nop 0\n"
        "buffer_load_dwordx4 v38, s[24:27], s28 offen lds\n"
        "s_mov_b32 m0, 95584\n"
        "s_mov_b32 s28, 5376\n"
        "buffer_load_dwordx4 v36, s[24:27], s28 offen lds\n"
        "s_mov_b32 m0, 96608\n"
        "s_nop 0\n"
        "buffer_load_dwordx4 v37, s[24:27], s28 offen lds\n"
        "s_mov_b32 m0, 97632\n"
        "s_nop 0\n"
        "buffer_load_dwordx4 v38, s[24:27], s28 offen lds\n"
        "global_load_dwordx4 v[40:43], v57, s[22:23] offset:-3072\n"
        "global_load_dwordx4 v[44:47], v57, s[22:23] offset:-2048\n"
        "global_load_dwordx4 v[0:3], v57, s[22:23] offset:-1024\n"
        "global_load_dwordx4 v[4:7], v57, s[22:23] offset:0\n"
        "global_load_dwordx4 v[8:11], v57, s[22:23] offset:1024\n"
        "global_load_dwordx4 v[12:15], v57, s[22:23] offset:2048\n"
        "global_load_dwordx4 v[16:19], v57, s[22:23] offset:3072\n"
        "L71_%=:\n"
        "s_waitcnt vmcnt(0)\n"
        "s_mov_b64 exec, 1\n"
        "ds_inc_u32 v250, v250 offset:44\n"
        "s_mov_b64 exec, -1\n"
        "v_mov_b64 v[240:241], v[40:41]\n"
        "v_mov_b64 v[242:243], v[42:43]\n"
        "v_mov_b64 v[244:245], v[44:45]\n"
        "v_mov_b64 v[246:247], v[46:47]\n"
        "L72_%=:\n"
        "ds_read_b32 v251, v250 offset:44\n"
        "s_waitcnt lgkmcnt(0)\n"
        "v_readfirstlane_b32 s40, v251\n"
        "s_nop 3\n"
        "s_cmp_ge_i32 s40, 4\n"
        "s_cbranch_scc1 L73_%=\n"
        "s_nop 0\n"
        "s_sub_u32 s41, s41, 1\n"
        "s_cbranch_scc0 L72_%=\n"
        "s_endpgm\n"
        "L73_%=:\n"
        "s_cmp_eq_u32 s6, 2\n"
        "s_cbranch_scc1 L74_%=\n"
        "s_mov_b32 s34, 0x41622adf\n"
        "s_mov_b32 s35, 21648\n"
        "s_mov_b32 s36, 21648\n"
        "s_mov_b32 s37, 21652\n"
        "s_call_b64 s[30:31], FOLDM_%=\n"
        "s_branch L75_%=\n"
        "L74_%=:\n"
        "s_mov_b32 s34, 0xbe16c740\n"
        "s_mov_b32 s35, 21656\n"
        "s_mov_b32 s36, 21656\n"
        "s_mov_b32 s37, 21648\n"
        "s_call_b64 s[30:31], FOLDR_%=\n"
        "L75_%=:\n"
        "s_branch CONS_%=\n"
        "FOLDM_%=:\n"
        "ds_read_b128 v[88:91], v242\n"
        "ds_read_b128 v[92:95], v240\n"
        "ds_read_b128 v[180:183], v242 offset:64\n"
        "ds_read_b128 v[172:175], v240 offset:64\n"
        "ds_read_b128 v[184:187], v242 offset:128\n"
        "ds_read_b128 v[164:167], v240 offset:128\n"
        "ds_read_b128 v[168:171], v241\n"
        "ds_read_b128 v[100:103], v241 offset:64\n"
        "ds_read_b128 v[104:107], v241 offset:128\n"
        "ds_read_b128 v[140:143], v240 offset:6144\n"
        "ds_read_b128 v[132:135], v240 offset:6208\n"
        "ds_read_b128 v[96:99], v240 offset:6272\n"
        "ds_read_b128 v[136:139], v241 offset:6144\n"
        "ds_read_b128 v[128:131], v241 offset:6208\n"
        "ds_read_b128 v[108:111], v241 offset:6272\n"
        "s_waitcnt lgkmcnt(13)\n"
        "v_mfma_f32_16x16x32_bf16 v[64:67], v[92:95], v[88:91], 0\n"
        "s_waitcnt lgkmcnt(11)\n"
        "ds_read_b128 v[152:155], v240 offset:12288\n"
        "v_mfma_f32_16x16x32_bf16 v[64:67], v[172:175], v[180:183], v[64:67]\n"
        "s_waitcnt lgkmcnt(8)\n"
        "ds_read_b128 v[160:163], v240 offset:12352\n"
        "v_mfma_f32_16x16x32_bf16 v[64:67], v[164:167], v[184:187], v[64:67]\n"
        "ds_read_b128 v[112:115], v240 offset:12416\n"
        "v_mfma_f32_16x16x32_bf16 v[68:71], v[168:171], v[88:91], 0\n"
        "ds_read_b128 v[116:119], v241 offset:12288\n"
        "v_mfma_f32_16x16x32_bf16 v[68:71], v[100:103], v[180:183], v[68:71]\n"
        "s_waitcnt lgkmcnt(4)\n"
        "ds_read_b128 v[120:123], v241 offset:12352\n"
        "v_mfma_f32_16x16x32_bf16 v[68:71], v[104:107], v[184:187], v[68:71]\n"
        "ds_read_b128 v[124:127], v241 offset:12416\n"
        "v_mfma_f32_16x16x32_bf16 v[72:75], v[140:143], v[88:91], 0\n"
        "ds_read_b128 v[144:147], v243\n"
        "v_mfma_f32_16x16x32_bf16 v[72:75], v[132:135], v[180:183], v[72:75]\n"
        "ds_read_b128 v[148:151], v243 offset:64\n"
        "v_mfma_f32_16x16x32_bf16 v[72:75], v[96:99], v[184:187], v[72:75]\n"
        "ds_read_b128 v[156:159], v243 offset:128\n"
        "v_mfma_f32_16x16x32_bf16 v[76:79], v[136:139], v[88:91], 0\n"
        "v_add_f32 v176, v64, v68\n"
        "v_add_f32 v177, v65, v69\n"
        "v_add_f32 v178, v66, v70\n"
        "v_add_f32 v179, v67, v71\n"
        "v_mfma_f32_16x16x32_bf16 v[76:79], v[128:131], v[180:183], v[76:79]\n"
        "v_mfma_f32_16x16x32_bf16 v[76:79], v[108:111], v[184:187], v[76:79]\n"
        "s_waitcnt lgkmcnt(3)\n"
        "v_add_f32 v176, v176, v72\n"
        "v_add_f32 v177, v177, v73\n"
        "v_add_f32 v178, v178, v74\n"
        "v_add_f32 v179, v179, v75\n"
        "v_mfma_f32_16x16x32_bf16 v[80:83], v[152:155], v[88:91], 0\n"
        "v_mfma_f32_16x16x32_bf16 v[80:83], v[160:163], v[180:183], v[80:83]\n"
        "v_mfma_f32_16x16x32_bf16 v[80:83], v[112:115], v[184:187], v[80:83]\n"
        "v_add_f32 v176, v176, v76\n"
        "v_add_f32 v177, v177, v77\n"
        "v_add_f32 v178, v178, v78\n"
        "v_add_f32 v179, v179, v79\n"
        "v_mfma_f32_16x16x32_bf16 v[84:87], v[116:119], v[88:91], 0\n"
        "v_mfma_f32_16x16x32_bf16 v[84:87], v[120:123], v[180:183], v[84:87]\n"
        "v_mfma_f32_16x16x32_bf16 v[84:87], v[124:127], v[184:187], v[84:87]\n"
        "s_waitcnt lgkmcnt(0)\n"
        "v_add_f32 v176, v176, v80\n"
        "v_add_f32 v177, v177, v81\n"
        "v_add_f32 v178, v178, v82\n"
        "v_add_f32 v179, v179, v83\n"
        "v_mov_b32 v239, s35\n"
        "s_waitcnt vmcnt(6)\n"
        "s_mov_b64 exec, 1\n"
        "ds_inc_u32 v239, v239 offset:0\n"
        "s_mov_b64 exec, -1\n"
        "v_add_f32 v176, v176, v84\n"
        "v_add_f32 v177, v177, v85\n"
        "v_add_f32 v178, v178, v86\n"
        "v_add_f32 v179, v179, v87\n"
        "v_add_f32 v176, v176, v177\n"
        "v_add_f32 v178, v178, v179\n"
        "v_add_f32 v176, v176, v178\n"
        "v_mov_b32 v177, v176\n"
        "v_mfma_f32_16x16x32_bf16 v[88:91], v[92:95], v[144:147], 0\n"
        "v_mfma_f32_16x16x32_bf16 v[88:91], v[172:175], v[148:151], v[88:91]\n"
        "v_permlane32_swap_b32 v176, v177\n"
        "v_add_f32 v176, v176, v177\n"
        "v_mov_b32 v177, v176\n"
        "v_mfma_f32_16x16x32_bf16 v[88:91], v[164:167], v[156:159], v[88:91]\n"
        "v_mfma_f32_16x16x32_bf16 v[92:95], v[168:171], v[144:147], 0\n"
        "v_permlane16_swap_b32 v176, v177\n"
        "v_add_f32 v176, v176, v177\n"
        "v_mul_f32 v176, 0xbc2aaaab, v176\n"
        "v_mul_f32 v176, s34, v176\n"
        "v_fma_f32 v64, v64, s34, v176\n"
        "v_fma_f32 v65, v65, s34, v176\n"
        "v_fma_f32 v66, v66, s34, v176\n"
        "v_fma_f32 v67, v67, s34, v176\n"
        "v_fma_f32 v68, v68, s34, v176\n"
        "v_fma_f32 v69, v69, s34, v176\n"
        "v_fma_f32 v70, v70, s34, v176\n"
        "v_fma_f32 v71, v71, s34, v176\n"
        "v_fma_f32 v72, v72, s34, v176\n"
        "v_fma_f32 v73, v73, s34, v176\n"
        "v_fma_f32 v74, v74, s34, v176\n"
        "v_fma_f32 v75, v75, s34, v176\n"
        "v_fma_f32 v76, v76, s34, v176\n"
        "v_fma_f32 v77, v77, s34, v176\n"
        "v_fma_f32 v78, v78, s34, v176\n"
        "v_fma_f32 v79, v79, s34, v176\n"
        "v_fma_f32 v80, v80, s34, v176\n"
        "v_fma_f32 v81, v81, s34, v176\n"
        "v_fma_f32 v82, v82, s34, v176\n"
        "v_fma_f32 v83, v83, s34, v176\n"
        "v_fma_f32 v84, v84, s34, v176\n"
        "v_fma_f32 v85, v85, s34, v176\n"
        "v_fma_f32 v86, v86, s34, v176\n"
        "v_fma_f32 v87, v87, s34, v176\n"
        "v_mfma_f32_16x16x32_bf16 v[92:95], v[100:103], v[148:151], v[92:95]\n"
        "v_mfma_f32_16x16x32_bf16 v[92:95], v[104:107], v[156:159], v[92:95]\n"
        "v_mfma_f32_16x16x32_bf16 v[100:103], v[140:143], v[144:147], 0\n"
        "v_mfma_f32_16x16x32_bf16 v[100:103], v[132:135], v[148:151], v[100:103]\n"
        "v_mfma_f32_16x16x32_bf16 v[100:103], v[96:99], v[156:159], v[100:103]\n"
        "v_mfma_f32_16x16x32_bf16 v[104:107], v[136:139], v[144:147], 0\n"
        "v_mfma_f32_16x16x32_bf16 v[104:107], v[128:131], v[148:151], v[104:107]\n"
        "v_mfma_f32_16x16x32_bf16 v[104:107], v[108:111], v[156:159], v[104:107]\n"
        "v_mfma_f32_16x16x32_bf16 v[96:99], v[152:155], v[144:147], 0\n"
        "v_mfma_f32_16x16x32_bf16 v[96:99], v[160:163], v[148:151], v[96:99]\n"
        "v_add_f32 v128, v88, v92\n"
        "v_add_f32 v129, v89, v93\n"
        "v_add_f32 v130, v90, v94\n"
        "v_add_f32 v131, v91, v95\n"
        "v_add_f32 v128, v128, v100\n"
        "v_add_f32 v129, v129, v101\n"
        "v_add_f32 v130, v130, v102\n"
        "v_add_f32 v131, v131, v103\n"
        "v_add_f32 v128, v128, v104\n"
        "v_add_f32 v129, v129, v105\n"
        "v_add_f32 v130, v130, v106\n"
        "v_add_f32 v131, v131, v107\n"
        "v_mfma_f32_16x16x32_bf16 v[96:99], v[112:115], v[156:159], v[96:99]\n"
        "v_mfma_f32_16x16x32_bf16 v[108:111], v[116:119], v[144:147], 0\n"
        "v_mfma_f32_16x16x32_bf16 v[108:111], v[120:123], v[148:151], v[108:111]\n"
        "v_mfma_f32_16x16x32_bf16 v[108:111], v[124:127], v[156:159], v[108:111]\n"
        "v_mov_b32 v239, s37\n"
        "s_nop 3\n"
        "v_add_f32 v128, v128, v96\n"
        "v_add_f32 v129, v129, v97\n"
        "v_add_f32 v130, v130, v98\n"
        "v_add_f32 v131, v131, v99\n"
        "v_add_f32 v128, v128, v108\n"
        "v_add_f32 v129, v129, v109\n"
        "v_add_f32 v130, v130, v110\n"
        "v_add_f32 v131, v131, v111\n"
        "v_add_f32 v128, v128, v129\n"
        "v_add_f32 v130, v130, v131\n"
        "v_add_f32 v128, v128, v130\n"
        "v_mov_b32 v129, v128\n"
        "s_nop 1\n"
        "v_permlane32_swap_b32 v128, v129\n"
        "v_add_f32 v128, v128, v129\n"
        "v_mov_b32 v129, v128\n"
        "s_nop 1\n"
        "v_permlane16_swap_b32 v128, v129\n"
        "v_add_f32 v128, v128, v129\n"
        "v_mul_f32 v128, 0xbc2aaaab, v128\n"
        "v_mul_f32 v128, s34, v128\n"
        "v_fma_f32 v88, v88, s34, v128\n"
        "v_fma_f32 v89, v89, s34, v128\n"
        "v_fma_f32 v90, v90, s34, v128\n"
        "v_fma_f32 v91, v91, s34, v128\n"
        "v_fma_f32 v92, v92, s34, v128\n"
        "v_fma_f32 v93, v93, s34, v128\n"
        "v_fma_f32 v94, v94, s34, v128\n"
        "v_fma_f32 v95, v95, s34, v128\n"
        "v_fma_f32 v100, v100, s34, v128\n"
        "v_fma_f32 v101, v101, s34, v128\n"
        "v_fma_f32 v102, v102, s34, v128\n"
        "v_fma_f32 v103, v103, s34, v128\n"
        "v_fma_f32 v104, v104, s34, v128\n"
        "v_fma_f32 v105, v105, s34, v128\n"
        "v_fma_f32 v106, v106, s34, v128\n"
        "v_fma_f32 v107, v107, s34, v128\n"
        "v_fma_f32 v96, v96, s34, v128\n"
        "v_fma_f32 v97, v97, s34, v128\n"
        "v_fma_f32 v98, v98, s34, v128\n"
        "v_fma_f32 v99, v99, s34, v128\n"
        "v_fma_f32 v108, v108, s34, v128\n"
        "v_fma_f32 v109, v109, s34, v128\n"
        "v_fma_f32 v110, v110, s34, v128\n"
        "v_fma_f32 v111, v111, s34, v128\n"
        "v_mov_b32 v237, s36\n"
        "L59_%=:\n"
        "ds_read_b32 v238, v237 offset:0\n"
        "s_waitcnt lgkmcnt(0)\n"
        "v_readfirstlane_b32 s40, v238\n"
        "s_nop 3\n"
        "s_cmp_ge_i32 s40, 4\n"
        "s_cbranch_scc1 L60_%=\n"
        "s_nop 0\n"
        "s_sub_u32 s41, s41, 1\n"
        "s_cbranch_scc0 L59_%=\n"
        "s_endpgm\n"
        "L60_%=:\n"
        "ds_read_b128 v[132:135], v246\n"
        "ds_read_b128 v[128:131], v246 offset:16\n"
        "ds_read_b128 v[124:127], v246 offset:128\n"
        "ds_read_b128 v[120:123], v246 offset:144\n"
        "ds_read_b128 v[116:119], v246 offset:256\n"
        "ds_read_b128 v[112:115], v246 offset:272\n"
        "s_waitcnt lgkmcnt(5)\n"
        "v_add_f32 v64, v64, v132\n"
        "v_add_f32 v65, v65, v133\n"
        "v_add_f32 v66, v66, v134\n"
        "v_add_f32 v67, v67, v135\n"
        "s_waitcnt lgkmcnt(0)\n"
        "v_add_f32 v68, v68, v128\n"
        "v_add_f32 v69, v69, v129\n"
        "v_add_f32 v70, v70, v130\n"
        "v_add_f32 v71, v71, v131\n"
        "v_add_f32 v88, v88, v132\n"
        "v_add_f32 v89, v89, v133\n"
        "v_add_f32 v90, v90, v134\n"
        "v_add_f32 v91, v91, v135\n"
        "v_add_f32 v92, v92, v128\n"
        "v_add_f32 v93, v93, v129\n"
        "v_add_f32 v94, v94, v130\n"
        "v_add_f32 v95, v95, v131\n"
        "v_add_f32 v72, v72, v124\n"
        "v_add_f32 v73, v73, v125\n"
        "v_add_f32 v74, v74, v126\n"
        "v_add_f32 v75, v75, v127\n"
        "v_add_f32 v76, v76, v120\n"
        "v_add_f32 v77, v77, v121\n"
        "v_add_f32 v78, v78, v122\n"
        "v_add_f32 v79, v79, v123\n"
        "v_add_f32 v100, v100, v124\n"
        "v_add_f32 v101, v101, v125\n"
        "v_add_f32 v102, v102, v126\n"
        "v_add_f32 v103, v103, v127\n"
        "v_add_f32 v104, v104, v120\n"
        "v_add_f32 v105, v105, v121\n"
        "v_add_f32 v106, v106, v122\n"
        "v_add_f32 v107, v107, v123\n"
        "v_add_f32 v80, v80, v116\n"
        "v_add_f32 v81, v81, v117\n"
        "v_add_f32 v82, v82, v118\n"
        "v_add_f32 v83, v83, v119\n"
        "v_add_f32 v84, v84, v112\n"
        "v_add_f32 v85, v85, v113\n"
        "v_add_f32 v86, v86, v114\n"
        "v_add_f32 v87, v87, v115\n"
        "v_add_f32 v96, v96, v116\n"
        "v_add_f32 v97, v97, v117\n"
        "v_add_f32 v98, v98, v118\n"
        "v_add_f32 v99, v99, v119\n"
        "v_add_f32 v108, v108, v112\n"
        "v_add_f32 v109, v109, v113\n"
        "v_add_f32 v110, v110, v114\n"
        "v_add_f32 v111, v111, v115\n"
        "v_cvt_pk_bf16_f32 v112, v64, v65\n"
        "v_cvt_pk_bf16_f32 v113, v66, v67\n"
        "v_cvt_pk_bf16_f32 v114, v68, v69\n"
        "v_cvt_pk_bf16_f32 v115, v70, v71\n"
        "v_cvt_pk_bf16_f32 v64, v88, v89\n"
        "v_cvt_pk_bf16_f32 v65, v90, v91\n"
        "v_cvt_pk_bf16_f32 v66, v92, v93\n"
        "v_cvt_pk_bf16_f32 v67, v94, v95\n"
        "v_cvt_pk_bf16_f32 v68, v72, v73\n"
        "v_cvt_pk_bf16_f32 v69, v74, v75\n"
        "v_cvt_pk_bf16_f32 v70, v76, v77\n"
        "v_cvt_pk_bf16_f32 v71, v78, v79\n"
        "v_cvt_pk_bf16_f32 v72, v100, v101\n"
        "v_cvt_pk_bf16_f32 v73, v102, v103\n"
        "v_cvt_pk_bf16_f32 v74, v104, v105\n"
        "v_cvt_pk_bf16_f32 v75, v106, v107\n"
        "v_cvt_pk_bf16_f32 v76, v80, v81\n"
        "v_cvt_pk_bf16_f32 v77, v82, v83\n"
        "v_cvt_pk_bf16_f32 v78, v84, v85\n"
        "v_cvt_pk_bf16_f32 v79, v86, v87\n"
        "v_cvt_pk_bf16_f32 v80, v96, v97\n"
        "v_cvt_pk_bf16_f32 v81, v98, v99\n"
        "v_cvt_pk_bf16_f32 v82, v108, v109\n"
        "v_cvt_pk_bf16_f32 v83, v110, v111\n"
        "ds_write_b128 v244, v[112:115]\n"
        "ds_write_b128 v245, v[64:67]\n"
        "ds_write_b128 v244, v[68:71] offset:1024\n"
        "ds_write_b128 v245, v[72:75] offset:1024\n"
        "ds_write_b128 v244, v[76:79] offset:2048\n"
        "ds_write_b128 v245, v[80:83] offset:2048\n"
        "s_mov_b64 exec, 1\n"
        "ds_inc_u32 v239, v239 offset:0\n"
        "s_mov_b64 exec, -1\n"
        "s_setpc_b64 s[30:31]\n"
        "FOLDR_%=:\n"
        "ds_read_b64_tr_b16 v[84:85], v242 offset:0\n"
        "ds_read_b64_tr_b16 v[86:87], v243 offset:0\n"
        "ds_read_b64_tr_b16 v[92:93], v242 offset:6144\n"
        "ds_read_b64_tr_b16 v[94:95], v243 offset:6144\n"
        "ds_read_b64_tr_b16 v[80:81], v242 offset:12288\n"
        "ds_read_b64_tr_b16 v[82:83], v243 offset:12288\n"
        "ds_read_b64_tr_b16 v[76:77], v242 offset:8\n"
        "ds_read_b64_tr_b16 v[78:79], v243 offset:8\n"
        "ds_read_b64_tr_b16 v[68:69], v242 offset:6152\n"
        "ds_read_b64_tr_b16 v[70:71], v243 offset:6152\n"
        "ds_read_b64_tr_b16 v[72:73], v242 offset:12296\n"
        "ds_read_b64_tr_b16 v[74:75], v243 offset:12296\n"
        "v_mov_b32 v64, 0x3f803f80\n"
        "v_mov_b32 v65, 0x3f803f80\n"
        "v_mov_b32 v66, 0x3f803f80\n"
        "v_mov_b32 v67, 0x3f803f80\n"
        "ds_read_b64_tr_b16 v[88:89], v242 offset:64\n"
        "ds_read_b64_tr_b16 v[90:91], v243 offset:64\n"
        "s_waitcnt lgkmcnt(12)\n"
        "v_mfma_f32_16x16x32_bf16 v[104:107], v[84:87], v[64:67], 0\n"
        "s_waitcnt lgkmcnt(10)\n"
        "v_mfma_f32_16x16x32_bf16 v[104:107], v[92:95], v[64:67], v[104:107]\n"
        "s_waitcnt lgkmcnt(6)\n"
        "ds_read_b64_tr_b16 v[96:97], v242 offset:6208\n"
        "ds_read_b64_tr_b16 v[98:99], v243 offset:6208\n"
        "v_mfma_f32_16x16x32_bf16 v[104:107], v[80:83], v[64:67], v[104:107]\n"
        "ds_read_b64_tr_b16 v[92:93], v242 offset:12352\n"
        "ds_read_b64_tr_b16 v[94:95], v243 offset:12352\n"
        "v_mfma_f32_16x16x32_bf16 v[100:103], v[76:79], v[64:67], 0\n"
        "s_waitcnt lgkmcnt(6)\n"
        "ds_read_b64_tr_b16 v[80:81], v242 offset:72\n"
        "ds_read_b64_tr_b16 v[82:83], v243 offset:72\n"
        "v_mfma_f32_16x16x32_bf16 v[100:103], v[68:71], v[64:67], v[100:103]\n"
        "ds_read_b64_tr_b16 v[76:77], v242 offset:6216\n"
        "ds_read_b64_tr_b16 v[78:79], v243 offset:6216\n"
        "v_cvt_pk_bf16_f32 v68, v104, v105\n"
        "v_cvt_pk_bf16_f32 v69, v106, v107\n"
        "v_mfma_f32_16x16x32_bf16 v[100:103], v[72:75], v[64:67], v[100:103]\n"
        "s_waitcnt lgkmcnt(8)\n"
        "ds_read_b64_tr_b16 v[84:85], v242 offset:12360\n"
        "ds_read_b64_tr_b16 v[86:87], v243 offset:12360\n"
        "v_mfma_f32_16x16x32_bf16 v[104:107], v[88:91], v[64:67], 0\n"
        "ds_read_b64_tr_b16 v[72:73], v242 offset:128\n"
        "ds_read_b64_tr_b16 v[74:75], v243 offset:128\n"
        "s_waitcnt lgkmcnt(10)\n"
        "v_mfma_f32_16x16x32_bf16 v[104:107], v[96:99], v[64:67], v[104:107]\n"
        "ds_read_b64_tr_b16 v[88:89], v242 offset:6272\n"
        "ds_read_b64_tr_b16 v[90:91], v243 offset:6272\n"
        "v_cvt_pk_bf16_f32 v70, v100, v101\n"
        "v_cvt_pk_bf16_f32 v71, v102, v103\n"
        "s_waitcnt lgkmcnt(9)\n"
        "v_mfma_f32_16x16x32_bf16 v[104:107], v[92:95], v[64:67], v[104:107]\n"
        "s_waitcnt lgkmcnt(8)\n"
        "ds_read_b64_tr_b16 v[100:101], v242 offset:12416\n"
        "ds_read_b64_tr_b16 v[102:103], v243 offset:12416\n"
        "v_mfma_f32_16x16x32_bf16 v[108:111], v[80:83], v[64:67], 0\n"
        "s_waitcnt lgkmcnt(8)\n"
        "ds_read_b64_tr_b16 v[92:93], v242 offset:136\n"
        "ds_read_b64_tr_b16 v[94:95], v243 offset:136\n"
        "v_mfma_f32_16x16x32_bf16 v[108:111], v[76:79], v[64:67], v[108:111]\n"
        "ds_read_b64_tr_b16 v[96:97], v242 offset:6280\n"
        "ds_read_b64_tr_b16 v[98:99], v243 offset:6280\n"
        "v_cvt_pk_bf16_f32 v80, v104, v105\n"
        "v_cvt_pk_bf16_f32 v81, v106, v107\n"
        "s_waitcnt lgkmcnt(8)\n"
        "v_mfma_f32_16x16x32_bf16 v[108:111], v[84:87], v[64:67], v[108:111]\n"
        "ds_read_b64_tr_b16 v[76:77], v242 offset:12424\n"
        "ds_read_b64_tr_b16 v[78:79], v243 offset:12424\n"
        "v_mfma_f32_16x16x32_bf16 v[116:119], v[72:75], v[64:67], 0\n"
        "s_waitcnt lgkmcnt(8)\n"
        "ds_read_b128 v[72:75], v240\n"
        "v_mfma_f32_16x16x32_bf16 v[116:119], v[88:91], v[64:67], v[116:119]\n"
        "ds_read_b128 v[104:107], v241\n"
        "s_waitcnt lgkmcnt(8)\n"
        "v_mfma_f32_16x16x32_bf16 v[116:119], v[100:103], v[64:67], v[116:119]\n"
        "ds_read_b128 v[84:87], v240 offset:6144\n"
        "v_cvt_pk_bf16_f32 v82, v108, v109\n"
        "v_cvt_pk_bf16_f32 v83, v110, v111\n"
        "s_waitcnt lgkmcnt(7)\n"
        "v_mfma_f32_16x16x32_bf16 v[112:115], v[92:95], v[64:67], 0\n"
        "s_waitcnt lgkmcnt(5)\n"
        "ds_read_b128 v[92:95], v241 offset:6144\n"
        "v_mfma_f32_16x16x32_bf16 v[112:115], v[96:99], v[64:67], v[112:115]\n"
        "ds_read_b128 v[100:103], v240 offset:12288\n"
        "v_cvt_pk_bf16_f32 v88, v116, v117\n"
        "v_cvt_pk_bf16_f32 v89, v118, v119\n"
        "s_waitcnt lgkmcnt(5)\n"
        "v_mfma_f32_16x16x32_bf16 v[112:115], v[76:79], v[64:67], v[112:115]\n"
        "s_waitcnt lgkmcnt(4)\n"
        "ds_read_b128 v[116:119], v241 offset:12288\n"
        "v_mfma_f32_16x16x32_bf16 v[64:67], v[68:71], v[72:75], 0\n"
        "s_waitcnt lgkmcnt(4)\n"
        "ds_read_b128 v[120:123], v240 offset:64\n"
        "v_mfma_f32_16x16x32_bf16 v[72:75], v[68:71], v[104:107], 0\n"
        "ds_read_b128 v[108:111], v241 offset:64\n"
        "s_waitcnt lgkmcnt(5)\n"
        "v_mfma_f32_16x16x32_bf16 v[76:79], v[68:71], v[84:87], 0\n"
        "ds_read_b128 v[104:107], v240 offset:6208\n"
        "v_cvt_pk_bf16_f32 v90, v112, v113\n"
        "v_cvt_pk_bf16_f32 v91, v114, v115\n"
        "s_waitcnt lgkmcnt(5)\n"
        "v_mfma_f32_16x16x32_bf16 v[84:87], v[68:71], v[92:95], 0\n"
        "ds_read_b128 v[124:127], v241 offset:6208\n"
        "s_waitcnt lgkmcnt(5)\n"
        "v_mfma_f32_16x16x32_bf16 v[92:95], v[68:71], v[100:103], 0\n"
        "ds_read_b128 v[112:115], v240 offset:12352\n"
        "s_waitcnt lgkmcnt(5)\n"
        "v_mfma_f32_16x16x32_bf16 v[96:99], v[68:71], v[116:119], 0\n"
        "ds_read_b128 v[100:103], v241 offset:12352\n"
        "s_waitcnt lgkmcnt(5)\n"
        "v_mfma_f32_16x16x32_bf16 v[64:67], v[80:83], v[120:123], v[64:67]\n"
        "ds_read_b128 v[68:71], v240 offset:128\n"
        "s_waitcnt lgkmcnt(5)\n"
        "v_mfma_f32_16x16x32_bf16 v[72:75], v[80:83], v[108:111], v[72:75]\n"
        "ds_read_b128 v[116:119], v241 offset:128\n"
        "s_waitcnt lgkmcnt(5)\n"
        "v_mfma_f32_16x16x32_bf16 v[76:79], v[80:83], v[104:107], v[76:79]\n"
        "ds_read_b128 v[108:111], v240 offset:6272\n"
        "s_waitcnt lgkmcnt(5)\n"
        "v_mfma_f32_16x16x32_bf16 v[84:87], v[80:83], v[124:127], v[84:87]\n"
        "ds_read_b128 v[104:107], v241 offset:6272\n"
        "s_waitcnt lgkmcnt(5)\n"
        "v_mfma_f32_16x16x32_bf16 v[92:95], v[80:83], v[112:115], v[92:95]\n"
        "ds_read_b128 v[120:123], v240 offset:12416\n"
        "s_waitcnt lgkmcnt(5)\n"
        "v_mfma_f32_16x16x32_bf16 v[96:99], v[80:83], v[100:103], v[96:99]\n"
        "ds_read_b128 v[112:115], v241 offset:12416\n"
        "s_waitcnt lgkmcnt(5)\n"
        "v_mfma_f32_16x16x32_bf16 v[64:67], v[88:91], v[68:71], v[64:67]\n"
        "s_waitcnt lgkmcnt(4)\n"
        "v_mfma_f32_16x16x32_bf16 v[72:75], v[88:91], v[116:119], v[72:75]\n"
        "v_mov_b32 v239, s35\n"
        "s_waitcnt lgkmcnt(3)\n"
        "v_mfma_f32_16x16x32_bf16 v[76:79], v[88:91], v[108:111], v[76:79]\n"
        "s_waitcnt vmcnt(6)\n"
        "s_mov_b64 exec, 1\n"
        "ds_inc_u32 v239, v239 offset:0\n"
        "s_mov_b64 exec, -1\n"
        "s_waitcnt lgkmcnt(3)\n"
        "v_mfma_f32_16x16x32_bf16 v[84:87], v[88:91], v[104:107], v[84:87]\n"
        "v_add_f32 v238, v64, v72\n"
        "s_waitcnt lgkmcnt(2)\n"
        "v_mfma_f32_16x16x32_bf16 v[92:95], v[88:91], v[120:123], v[92:95]\n"
        "v_add_f32 v238, v238, v76\n"
        "s_waitcnt lgkmcnt(1)\n"
        "v_mfma_f32_16x16x32_bf16 v[96:99], v[88:91], v[112:115], v[96:99]\n"
        "v_mov_b32 v239, s37\n"
        "s_nop 0\n"
        "v_add_f32 v238, v238, v84\n"
        "s_nop 1\n"
        "v_add_f32 v238, v238, v92\n"
        "s_nop 1\n"
        "v_add_f32 v238, v238, v96\n"
        "s_nop 1\n"
        "v_add_f32_dpp v238, v238, v238 row_ror:8 row_mask:0xf bank_mask:0xf\n"
        "s_nop 1\n"
        "v_add_f32_dpp v238, v238, v238 row_ror:4 row_mask:0xf bank_mask:0xf\n"
        "s_nop 1\n"
        "v_add_f32_dpp v238, v238, v238 row_ror:2 row_mask:0xf bank_mask:0xf\n"
        "s_nop 1\n"
        "v_add_f32_dpp v238, v238, v238 row_ror:1 row_mask:0xf bank_mask:0xf\n"
        "v_mul_f32 v238, 0xbc2aaaab, v238\n"
        "v_mul_f32 v238, s34, v238\n"
        "v_fma_f32 v64, v64, s34, v238\n"
        "v_fma_f32 v72, v72, s34, v238\n"
        "v_fma_f32 v76, v76, s34, v238\n"
        "v_fma_f32 v84, v84, s34, v238\n"
        "v_fma_f32 v92, v92, s34, v238\n"
        "v_fma_f32 v96, v96, s34, v238\n"
        "ds_write_b32 v244, v64\n"
        "ds_write_b32 v244, v72 offset:16\n"
        "ds_write_b32 v244, v76 offset:128\n"
        "ds_write_b32 v244, v84 offset:144\n"
        "ds_write_b32 v244, v92 offset:256\n"
        "ds_write_b32 v244, v96 offset:272\n"
        "s_mov_b64 exec, 1\n"
        "ds_inc_u32 v239, v239 offset:0\n"
        "s_mov_b64 exec, -1\n"
        "s_setpc_b64 s[30:31]\n"
        "FOLDW_%=:\n"
        "ds_read_b64_tr_b16 v[64:65], v242 offset:0\n"
        "ds_read_b64_tr_b16 v[66:67], v243 offset:0\n"
        "ds_read_b128 v[172:175], v240\n"
        "ds_read_b64_tr_b16 v[180:181], v242 offset:6144\n"
        "ds_read_b64_tr_b16 v[182:183], v243 offset:6144\n"
        "ds_read_b128 v[120:123], v240 offset:64\n"
        "ds_read_b64_tr_b16 v[184:185], v242 offset:12288\n"
        "ds_read_b64_tr_b16 v[186:187], v243 offset:12288\n"
        "ds_read_b128 v[168:171], v240 offset:128\n"
        "ds_read_b128 v[160:163], v241\n"
        "ds_read_b128 v[148:151], v241 offset:64\n"
        "ds_read_b128 v[132:135], v241 offset:128\n"
        "ds_read_b128 v[124:127], v240 offset:6144\n"
        "ds_read_b128 v[104:107], v240 offset:6208\n"
        "ds_read_b128 v[92:95], v240 offset:6272\n"
        "ds_read_b128 v[96:99], v241 offset:6144\n"
        "ds_read_b128 v[88:91], v241 offset:6208\n"
        "ds_read_b128 v[84:87], v241 offset:6272\n"
        "s_waitcnt lgkmcnt(15)\n"
        "v_mfma_f32_16x16x32_bf16 v[136:139], v[172:175], v[64:67], 0\n"
        "ds_read_b128 v[100:103], v240 offset:12288\n"
        "s_waitcnt lgkmcnt(12)\n"
        "v_mfma_f32_16x16x32_bf16 v[136:139], v[120:123], v[180:183], v[136:139]\n"
        "s_waitcnt lgkmcnt(10)\n"
        "ds_read_b128 v[108:111], v240 offset:12352\n"
        "v_mfma_f32_16x16x32_bf16 v[136:139], v[168:171], v[184:187], v[136:139]\n"
        "s_waitcnt lgkmcnt(7)\n"
        "ds_read_b128 v[68:71], v240 offset:12416\n"
        "v_mfma_f32_16x16x32_bf16 v[140:143], v[160:163], v[64:67], 0\n"
        "ds_read_b128 v[72:75], v241 offset:12288\n"
        "v_mfma_f32_16x16x32_bf16 v[140:143], v[148:151], v[180:183], v[140:143]\n"
        "ds_read_b128 v[76:79], v241 offset:12352\n"
        "v_mfma_f32_16x16x32_bf16 v[140:143], v[132:135], v[184:187], v[140:143]\n"
        "ds_read_b128 v[80:83], v241 offset:12416\n"
        "v_mfma_f32_16x16x32_bf16 v[144:147], v[124:127], v[64:67], 0\n"
        "s_waitcnt lgkmcnt(6)\n"
        "ds_read_b64_tr_b16 v[112:113], v242 offset:8\n"
        "ds_read_b64_tr_b16 v[114:115], v243 offset:8\n"
        "v_mfma_f32_16x16x32_bf16 v[144:147], v[104:107], v[180:183], v[144:147]\n"
        "ds_read_b64_tr_b16 v[116:117], v242 offset:6152\n"
        "ds_read_b64_tr_b16 v[118:119], v243 offset:6152\n"
        "v_add_f32 v164, v136, v140\n"
        "v_add_f32 v165, v137, v141\n"
        "v_add_f32 v166, v138, v142\n"
        "v_add_f32 v167, v139, v143\n"
        "v_mfma_f32_16x16x32_bf16 v[144:147], v[92:95], v[184:187], v[144:147]\n"
        "ds_read_b64_tr_b16 v[128:129], v242 offset:12296\n"
        "ds_read_b64_tr_b16 v[130:131], v243 offset:12296\n"
        "v_mfma_f32_16x16x32_bf16 v[152:155], v[96:99], v[64:67], 0\n"
        "v_mfma_f32_16x16x32_bf16 v[152:155], v[88:91], v[180:183], v[152:155]\n"
        "v_mfma_f32_16x16x32_bf16 v[152:155], v[84:87], v[184:187], v[152:155]\n"
        "s_waitcnt lgkmcnt(6)\n"
        "v_mfma_f32_16x16x32_bf16 v[156:159], v[100:103], v[64:67], 0\n"
        "v_mfma_f32_16x16x32_bf16 v[156:159], v[108:111], v[180:183], v[156:159]\n"
        "v_add_f32 v164, v164, v144\n"
        "v_add_f32 v165, v165, v145\n"
        "v_add_f32 v166, v166, v146\n"
        "v_add_f32 v167, v167, v147\n"
        "v_mfma_f32_16x16x32_bf16 v[156:159], v[68:71], v[184:187], v[156:159]\n"
        "v_add_f32 v164, v164, v152\n"
        "v_add_f32 v165, v165, v153\n"
        "v_add_f32 v166, v166, v154\n"
        "v_add_f32 v167, v167, v155\n"
        "v_mfma_f32_16x16x32_bf16 v[176:179], v[72:75], v[64:67], 0\n"
        "v_mfma_f32_16x16x32_bf16 v[176:179], v[76:79], v[180:183], v[176:179]\n"
        "v_mfma_f32_16x16x32_bf16 v[176:179], v[80:83], v[184:187], v[176:179]\n"
        "s_waitcnt lgkmcnt(0)\n"
        "v_mov_b32 v239, s35\n"
        "v_add_f32 v164, v164, v156\n"
        "v_add_f32 v165, v165, v157\n"
        "v_add_f32 v166, v166, v158\n"
        "v_add_f32 v167, v167, v159\n"
        "s_waitcnt vmcnt(6)\n"
        "s_mov_b64 exec, 1\n"
        "ds_inc_u32 v239, v239 offset:0\n"
        "s_mov_b64 exec, -1\n"
        "v_add_f32 v164, v164, v176\n"
        "v_add_f32 v165, v165, v177\n"
        "v_mfma_f32_16x16x32_bf16 v[64:67], v[172:175], v[112:115], 0\n"
        "v_add_f32 v166, v166, v178\n"
        "v_add_f32 v167, v167, v179\n"
        "v_add_f32 v164, v164, v165\n"
        "v_add_f32 v166, v166, v167\n"
        "v_add_f32 v164, v164, v166\n"
        "v_mov_b32 v165, v164\n"
        "v_mfma_f32_16x16x32_bf16 v[64:67], v[120:123], v[116:119], v[64:67]\n"
        "v_mfma_f32_16x16x32_bf16 v[64:67], v[168:171], v[128:131], v[64:67]\n"
        "v_permlane32_swap_b32 v164, v165\n"
        "v_add_f32 v164, v164, v165\n"
        "v_mov_b32 v165, v164\n"
        "v_mfma_f32_16x16x32_bf16 v[120:123], v[160:163], v[112:115], 0\n"
        "global_load_dwordx4 v[40:43], v57, s[22:23] offset:-3072\n"
        "v_permlane16_swap_b32 v164, v165\n"
        "v_add_f32 v164, v164, v165\n"
        "v_mul_f32 v164, 0xbc2aaaab, v164\n"
        "v_mul_f32 v164, s34, v164\n"
        "v_fma_f32 v136, v136, s34, v164\n"
        "v_fma_f32 v137, v137, s34, v164\n"
        "v_fma_f32 v138, v138, s34, v164\n"
        "v_fma_f32 v139, v139, s34, v164\n"
        "v_fma_f32 v140, v140, s34, v164\n"
        "v_fma_f32 v141, v141, s34, v164\n"
        "v_mfma_f32_16x16x32_bf16 v[120:123], v[148:151], v[116:119], v[120:123]\n"
        "v_fma_f32 v142, v142, s34, v164\n"
        "v_fma_f32 v143, v143, s34, v164\n"
        "v_fma_f32 v144, v144, s34, v164\n"
        "v_fma_f32 v145, v145, s34, v164\n"
        "v_fma_f32 v146, v146, s34, v164\n"
        "v_fma_f32 v147, v147, s34, v164\n"
        "v_fma_f32 v152, v152, s34, v164\n"
        "v_fma_f32 v153, v153, s34, v164\n"
        "v_fma_f32 v154, v154, s34, v164\n"
        "v_fma_f32 v155, v155, s34, v164\n"
        "v_fma_f32 v156, v156, s34, v164\n"
        "v_fma_f32 v157, v157, s34, v164\n"
        "v_fma_f32 v158, v158, s34, v164\n"
        "v_fma_f32 v159, v159, s34, v164\n"
        "v_mfma_f32_16x16x32_bf16 v[120:123], v[132:135], v[128:131], v[120:123]\n"
        "global_load_dwordx4 v[44:47], v57, s[22:23] offset:-2048\n"
        "v_fma_f32 v176, v176, s34, v164\n"
        "v_fma_f32 v177, v177, s34, v164\n"
        "v_fma_f32 v178, v178, s34, v164\n"
        "v_fma_f32 v179, v179, s34, v164\n"
        "v_cvt_pk_bf16_f32 v132, v136, v137\n"
        "v_cvt_pk_bf16_f32 v133, v138, v139\n"
        "v_cvt_pk_bf16_f32 v134, v140, v141\n"
        "v_add_f32 v148, v64, v120\n"
        "v_add_f32 v149, v65, v121\n"
        "v_add_f32 v150, v66, v122\n"
        "v_add_f32 v151, v67, v123\n"
        "v_cvt_pk_bf16_f32 v135, v142, v143\n"
        "v_mfma_f32_16x16x32_bf16 v[140:143], v[124:127], v[112:115], 0\n"
        "v_cvt_pk_bf16_f32 v136, v144, v145\n"
        "v_cvt_pk_bf16_f32 v137, v146, v147\n"
        "v_cvt_pk_bf16_f32 v138, v152, v153\n"
        "v_cvt_pk_bf16_f32 v139, v154, v155\n"
        "v_cvt_pk_bf16_f32 v124, v156, v157\n"
        "v_cvt_pk_bf16_f32 v125, v158, v159\n"
        "v_cvt_pk_bf16_f32 v126, v176, v177\n"
        "v_cvt_pk_bf16_f32 v127, v178, v179\n"
        "v_mfma_f32_16x16x32_bf16 v[140:143], v[104:107], v[116:119], v[140:143]\n"
        "global_load_dwordx4 v[0:3], v57, s[22:23] offset:-1024\n"
        "v_mfma_f32_16x16x32_bf16 v[140:143], v[92:95], v[128:131], v[140:143]\n"
        "v_mfma_f32_16x16x32_bf16 v[104:107], v[96:99], v[112:115], 0\n"
        "global_load_dwordx4 v[4:7], v57, s[22:23] offset:0\n"
        "v_mfma_f32_16x16x32_bf16 v[104:107], v[88:91], v[116:119], v[104:107]\n"
        "v_mfma_f32_16x16x32_bf16 v[104:107], v[84:87], v[128:131], v[104:107]\n"
        "global_load_dwordx4 v[8:11], v57, s[22:23] offset:1024\n"
        "v_mfma_f32_16x16x32_bf16 v[84:87], v[100:103], v[112:115], 0\n"
        "v_mfma_f32_16x16x32_bf16 v[84:87], v[108:111], v[116:119], v[84:87]\n"
        "global_load_dwordx4 v[12:15], v57, s[22:23] offset:2048\n"
        "v_add_f32 v148, v148, v140\n"
        "v_add_f32 v149, v149, v141\n"
        "v_add_f32 v150, v150, v142\n"
        "v_add_f32 v151, v151, v143\n"
        "v_add_f32 v148, v148, v104\n"
        "v_add_f32 v149, v149, v105\n"
        "v_add_f32 v150, v150, v106\n"
        "v_add_f32 v151, v151, v107\n"
        "v_mfma_f32_16x16x32_bf16 v[84:87], v[68:71], v[128:131], v[84:87]\n"
        "v_mfma_f32_16x16x32_bf16 v[88:91], v[72:75], v[112:115], 0\n"
        "global_load_dwordx4 v[16:19], v57, s[22:23] offset:3072\n"
        "v_mfma_f32_16x16x32_bf16 v[88:91], v[76:79], v[116:119], v[88:91]\n"
        "v_mfma_f32_16x16x32_bf16 v[88:91], v[80:83], v[128:131], v[88:91]\n"
        "v_mov_b32 v239, s37\n"
        "s_nop 2\n"
        "v_add_f32 v148, v148, v84\n"
        "v_add_f32 v149, v149, v85\n"
        "v_add_f32 v150, v150, v86\n"
        "v_add_f32 v151, v151, v87\n"
        "v_add_f32 v148, v148, v88\n"
        "v_add_f32 v149, v149, v89\n"
        "v_add_f32 v150, v150, v90\n"
        "v_add_f32 v151, v151, v91\n"
        "v_add_f32 v148, v148, v149\n"
        "v_add_f32 v150, v150, v151\n"
        "v_add_f32 v148, v148, v150\n"
        "v_mov_b32 v149, v148\n"
        "s_nop 1\n"
        "v_permlane32_swap_b32 v148, v149\n"
        "v_add_f32 v148, v148, v149\n"
        "v_mov_b32 v149, v148\n"
        "s_nop 1\n"
        "v_permlane16_swap_b32 v148, v149\n"
        "v_add_f32 v148, v148, v149\n"
        "v_mul_f32 v148, 0xbc2aaaab, v148\n"
        "v_mul_f32 v148, s34, v148\n"
        "v_fma_f32 v64, v64, s34, v148\n"
        "v_fma_f32 v65, v65, s34, v148\n"
        "v_fma_f32 v66, v66, s34, v148\n"
        "v_fma_f32 v67, v67, s34, v148\n"
        "v_fma_f32 v120, v120, s34, v148\n"
        "v_fma_f32 v121, v121, s34, v148\n"
        "v_fma_f32 v122, v122, s34, v148\n"
        "v_fma_f32 v123, v123, s34, v148\n"
        "v_fma_f32 v140, v140, s34, v148\n"
        "v_fma_f32 v141, v141, s34, v148\n"
        "v_fma_f32 v142, v142, s34, v148\n"
        "v_fma_f32 v143, v143, s34, v148\n"
        "v_fma_f32 v104, v104, s34, v148\n"
        "v_fma_f32 v105, v105, s34, v148\n"
        "v_fma_f32 v106, v106, s34, v148\n"
        "v_fma_f32 v107, v107, s34, v148\n"
        "v_fma_f32 v84, v84, s34, v148\n"
        "v_fma_f32 v85, v85, s34, v148\n"
        "v_fma_f32 v86, v86, s34, v148\n"
        "v_fma_f32 v87, v87, s34, v148\n"
        "v_fma_f32 v88, v88, s34, v148\n"
        "v_fma_f32 v89, v89, s34, v148\n"
        "v_fma_f32 v90, v90, s34, v148\n"
        "v_fma_f32 v91, v91, s34, v148\n"
        "v_cvt_pk_bf16_f32 v68, v64, v65\n"
        "v_cvt_pk_bf16_f32 v69, v66, v67\n"
        "v_cvt_pk_bf16_f32 v70, v120, v121\n"
        "v_cvt_pk_bf16_f32 v71, v122, v123\n"
        "v_cvt_pk_bf16_f32 v64, v140, v141\n"
        "v_cvt_pk_bf16_f32 v65, v142, v143\n"
        "v_cvt_pk_bf16_f32 v66, v104, v105\n"
        "v_cvt_pk_bf16_f32 v67, v106, v107\n"
        "v_cvt_pk_bf16_f32 v72, v84, v85\n"
        "v_cvt_pk_bf16_f32 v73, v86, v87\n"
        "v_cvt_pk_bf16_f32 v74, v88, v89\n"
        "v_cvt_pk_bf16_f32 v75, v90, v91\n"
        "s_nop 1\n"
        "v_mov_b32 v238, s36\n"
        "L61_%=:\n"
        "ds_read_b32 v237, v238 offset:0\n"
        "s_waitcnt lgkmcnt(0)\n"
        "v_readfirstlane_b32 s40, v237\n"
        "s_nop 3\n"
        "s_cmp_ge_i32 s40, 4\n"
        "s_cbranch_scc1 L62_%=\n"
        "s_nop 0\n"
        "s_sub_u32 s41, s41, 1\n"
        "s_cbranch_scc0 L61_%=\n"
        "s_endpgm\n"
        "L62_%=:\n"
        "ds_write_b128 v244, v[132:135]\n"
        "ds_write_b128 v245, v[68:71]\n"
        "ds_write_b128 v244, v[136:139] offset:1024\n"
        "ds_write_b128 v245, v[64:67] offset:1024\n"
        "ds_write_b128 v244, v[124:127] offset:2048\n"
        "ds_write_b128 v245, v[72:75] offset:2048\n"
        "s_mov_b64 exec, 1\n"
        "ds_inc_u32 v239, v239 offset:0\n"
        "s_mov_b64 exec, -1\n"
        "s_setpc_b64 s[30:31]\n"
        "PPRO_%=:\n"
        "s_add_u32 s64, s64, 128\n"
        "s_cmp_lg_u32 s6, 0\n"
        "s_cbranch_scc1 L77_%=\n"
        "s_mov_b32 exec_hi, 0\n"
        "v_lshlrev_b32 v27, 2, v56\n"
        "v_add_u32 v27, 21600, v27\n"
        "v_mov_b32 v28, 0\n"
        "ds_write_b32 v27, v28\n"
        "s_mov_b64 exec, 0x1fffff\n"
        "s_bfe_u32 s7, s2, 0x50003\n"
        "s_mul_i32 s7, s7, 2656\n"
        "v_lshlrev_b32 v27, 7, v56\n"
        "v_add_u32 v27, s7, v27\n"
        "global_load_dword v252, v27, s[4:5]\n"
        "s_mov_b64 exec, -1\n"
        "L77_%=:\n"
        "s_cmp_lt_u32 s6, 2\n"
        "s_cselect_b32 s65, 3, 1\n"
        "s_cselect_b32 s61, 384, 0\n"
        "s_cselect_b32 s62, 9, 1\n"
        "s_cselect_b32 s63, 3, 4\n"
        "s_cselect_b32 s26, 3, 1\n"
        "s_cselect_b32 s34, 9, 1\n"
        "s_cselect_b32 s70, 72, 1\n"
        "s_cselect_b32 s71, 36, 4\n"
        "s_cselect_b32 s72, 6, 8\n"
        "s_mul_i32 s70, s70, 192\n"
        "s_mul_i32 s71, s71, 192\n"
        "s_mul_i32 s72, s72, 192\n"
        "s_mul_i32 s65, s65, 192\n"
        "s_mul_i32 s62, s62, 3072\n"
        "s_mul_i32 s63, s63, 1536\n"
        "s_mul_i32 s26, s26, 18432\n"
        "s_mul_i32 s34, s34, 1536\n"
        "s_add_u32 s7, s62, s63\n"
        "s_lshl_b32 s29, s63, 1\n"
        "s_add_u32 s60, s7, s63\n"
        "s_bitcmp1_b32 s6, 0\n"
        "s_cselect_b32 s28, s7, 0\n"
        "s_cselect_b32 s29, s29, s62\n"
        "s_cselect_b32 s60, s60, s63\n"
        "s_add_u32 s28, s28, s61\n"
        "s_add_u32 s29, s29, s61\n"
        "s_add_u32 s60, s60, s61\n"
        "v_add_u32 v24, 0, v56\n"
        "v_mul_u32_u24 v21, 171, v24\n"
        "v_lshrrev_b32 v21, 11, v21\n"
        "v_mad_i32_i24 v24, v21, -12, v24\n"
        "v_bfe_u32 v58, v21, 2, 2\n"
        "v_xor_b32 v58, v24, v58\n"
        "v_and_b32 v27, 3, v21\n"
        "v_mul_u32_u24 v27, s70, v27\n"
        "v_lshl_add_u32 v32, v58, 4, v27\n"
        "v_bfe_u32 v27, v21, 2, 1\n"
        "v_mad_u32_u24 v32, v27, s71, v32\n"
        "v_lshrrev_b32 v27, 3, v21\n"
        "v_mad_u32_u24 v32, v27, s72, v32\n"
        "v_and_b32 v27, 7, v21\n"
        "v_mul_u32_u24 v27, s65, v27\n"
        "v_lshl_add_u32 v58, v58, 4, v27\n"
        "v_add_u32 v25, 64, v56\n"
        "v_mul_u32_u24 v22, 171, v25\n"
        "v_lshrrev_b32 v22, 11, v22\n"
        "v_mad_i32_i24 v25, v22, -12, v25\n"
        "v_bfe_u32 v59, v22, 2, 2\n"
        "v_xor_b32 v59, v25, v59\n"
        "v_and_b32 v27, 3, v22\n"
        "v_mul_u32_u24 v27, s70, v27\n"
        "v_lshl_add_u32 v33, v59, 4, v27\n"
        "v_bfe_u32 v27, v22, 2, 1\n"
        "v_mad_u32_u24 v33, v27, s71, v33\n"
        "v_lshrrev_b32 v27, 3, v22\n"
        "v_mad_u32_u24 v33, v27, s72, v33\n"
        "v_and_b32 v27, 7, v22\n"
        "v_mul_u32_u24 v27, s65, v27\n"
        "v_lshl_add_u32 v59, v59, 4, v27\n"
        "v_lshrrev_b32 v27, 3, v22\n"
        "v_mad_u32_u24 v59, v27, s34, v59\n"
        "v_add_u32 v26, 128, v56\n"
        "v_mul_u32_u24 v23, 171, v26\n"
        "v_lshrrev_b32 v23, 11, v23\n"
        "v_mad_i32_i24 v26, v23, -12, v26\n"
        "v_bfe_u32 v60, v23, 2, 2\n"
        "v_xor_b32 v60, v26, v60\n"
        "v_and_b32 v27, 3, v23\n"
        "v_mul_u32_u24 v27, s70, v27\n"
        "v_lshl_add_u32 v34, v60, 4, v27\n"
        "v_bfe_u32 v27, v23, 2, 1\n"
        "v_mad_u32_u24 v34, v27, s71, v34\n"
        "v_lshrrev_b32 v27, 3, v23\n"
        "v_mad_u32_u24 v34, v27, s72, v34\n"
        "v_and_b32 v27, 7, v23\n"
        "v_mul_u32_u24 v27, s65, v27\n"
        "v_lshl_add_u32 v60, v60, 4, v27\n"
        "v_add_u32 v60, s34, v60\n"
        "s_and_b32 s7, s2, 7\n"
        "s_lshl_b32 s7, s7, 5\n"
        "s_lshr_b32 s14, s2, 3\n"
        "s_add_u32 s7, s7, s14\n"
        "s_mul_i32 s7, s7, 20\n"
        "s_mov_b32 s14, 22\n"
        "s_mul_i32 s15, s7, 57457\n"
        "s_lshr_b32 s15, s15, 22\n"
        "s_mul_i32 s16, s15, 73\n"
        "s_sub_u32 s16, s7, s16\n"
        "s_mul_i32 s15, s15, 686784\n"
        "s_mul_i32 s7, s16, 1344\n"
        "s_add_u32 s15, s15, s7\n"
        "s_sub_u32 s7, 6, s6\n"
        "v_mov_b32 v28, 96768\n"
        "s_mov_b32 s35, 50135232\n"
        "v_mov_b32 v29, 2048\n"
        "v_and_b32 v21, 7, v21\n"
        "v_cmp_lt_u32 vcc, s7, v21\n"
        "v_and_b32 v27, 3, v24\n"
        "v_mad_u32_u24 v29, v27, 48, v29\n"
        "v_lshrrev_b32 v27, 2, v24\n"
        "v_lshl_add_u32 v29, v27, 4, v29\n"
        "v_mul_u32_u24 v21, 192, v21\n"
        "v_cndmask_b32 v27, 0, v28, vcc\n"
        "v_add3_u32 v29, v29, v21, v27\n"
        "v_lshrrev_b32 v27, 3, v22\n"
        "v_xor_b32 v25, v25, v27\n"
        "v_mul_lo_u32 v30, v27, s35\n"
        "v_add_u32 v30, 1024, v30\n"
        "v_and_b32 v22, 7, v22\n"
        "v_cmp_lt_u32 vcc, s7, v22\n"
        "v_and_b32 v27, 3, v25\n"
        "v_mad_u32_u24 v30, v27, 48, v30\n"
        "v_lshrrev_b32 v27, 2, v25\n"
        "v_lshl_add_u32 v30, v27, 4, v30\n"
        "v_mul_u32_u24 v22, 192, v22\n"
        "v_cndmask_b32 v27, 0, v28, vcc\n"
        "v_add3_u32 v30, v30, v22, v27\n"
        "v_mov_b32 v31, 50135232\n"
        "v_xor_b32 v26, 1, v26\n"
        "v_and_b32 v23, 7, v23\n"
        "v_cmp_lt_u32 vcc, s7, v23\n"
        "v_and_b32 v27, 3, v26\n"
        "v_mad_u32_u24 v31, v27, 48, v31\n"
        "v_lshrrev_b32 v27, 2, v26\n"
        "v_lshl_add_u32 v31, v27, 4, v31\n"
        "v_mul_u32_u24 v23, 192, v23\n"
        "v_cndmask_b32 v27, 0, v28, vcc\n"
        "v_add3_u32 v31, v31, v23, v27\n"
        "s_mul_i32 s17, s6, 6208\n"
        "s_add_u32 s17, s17, 110176\n"
        "s_mov_b32 s18, s6\n"
        "s_mul_i32 s74, s6, 98304\n"
        "s_add_u32 s74, s74, s15\n"
        "s_add_u32 s75, s74, 100270464\n"
        "v_mov_b32 v250, 21600\n"
        "s_mov_b32 s41, 0x400000\n"
        "s_waitcnt lgkmcnt(0)\n"
        "s_cmp_lg_u32 s6, 0\n"
        "s_cbranch_scc1 L78_%=\n"
        "s_bitcmp1_b32 s2, 7\n"
        "s_cselect_b64 s[8:9], s[50:51], s[46:47]\n"
        "s_cselect_b64 s[10:11], s[52:53], s[48:49]\n"
        "s_bfe_u32 s7, s2, 0x40003\n"
        "s_cmp_ge_u32 s7, 12\n"
        "s_cselect_b64 s[8:9], s[10:11], s[8:9]\n"
        "s_cselect_b32 s10, 12, 0\n"
        "s_sub_u32 s7, s7, s10\n"
        "s_mul_i32 s7, s7, 4608\n"
        "v_lshlrev_b32 v27, 7, v56\n"
        "v_add_u32 v27, s7, v27\n"
        "s_mov_b32 exec_hi, 0xf\n"
        "global_load_dword v252, v27, s[8:9]\n"
        "s_mov_b32 exec_hi, -1\n"
        "s_mov_b64 s[24:25], s[56:57]\n"
        "s_mov_b64 exec, 1\n"
        "v_mov_b32 v27, 0\n"
        "global_load_dword v253, v27, s[24:25] offset:256 sc1\n"
        "s_mov_b64 exec, -1\n"
        "L78_%=:\n"
        "s_cmp_lt_u32 s6, 2\n"
        "s_cselect_b64 s[24:25], s[46:47], s[48:49]\n"
        "s_and_b32 s25, s25, 0xffff\n"
        "s_sub_u32 s8, s44, 2048\n"
        "s_subb_u32 s9, s45, 0\n"
        "s_and_b32 s9, s9, 0xffff\n"
        "s_mov_b32 s10, 200542976\n"
        "s_mov_b32 s11, 0x20000\n"
        "s_mul_i32 s7, s6, 7168\n"
        "s_add_u32 s7, s7, 6144\n"
        "s_add_u32 s22, s4, s7\n"
        "s_addc_u32 s23, s5, 0\n"
        "s_sub_u32 s7, s6, 0\n"
        "s_and_b32 s7, s7, 3\n"
        "s_min_u32 s7, s7, 2\n"
        "s_lshl_b32 s7, s7, 10\n"
        "s_add_u32 s73, s7, 18528\n"
        "v_add_u32 v27, s7, v57\n"
        "s_barrier\n"
        "s_mov_b32 m0, s73\n"
        "s_nop 0\n"
        "global_load_lds_dwordx4 v27, s[4:5]\n"
        "global_load_dwordx4 v[240:243], v57, s[22:23] offset:-3072\n"
        "global_load_dwordx4 v[244:247], v57, s[22:23] offset:-2048\n"
        "s_add_u32 m0, s64, 0\n"
        "s_nop 0\n"
        "buffer_load_dwordx4 v58, s[24:27], s28 offen lds\n"
        "s_add_u32 m0, s64, 1024\n"
        "s_nop 0\n"
        "buffer_load_dwordx4 v59, s[24:27], s28 offen lds\n"
        "s_add_u32 m0, s64, 2048\n"
        "s_nop 0\n"
        "buffer_load_dwordx4 v60, s[24:27], s28 offen lds\n"
        "s_add_u32 m0, s64, 3072\n"
        "s_nop 0\n"
        "buffer_load_dwordx4 v58, s[24:27], s29 offen lds\n"
        "s_add_u32 m0, s64, 4096\n"
        "s_nop 0\n"
        "buffer_load_dwordx4 v59, s[24:27], s29 offen lds\n"
        "s_add_u32 m0, s64, 5120\n"
        "s_nop 0\n"
        "buffer_load_dwordx4 v60, s[24:27], s29 offen lds\n"
        "s_add_u32 m0, s64, 6144\n"
        "s_nop 0\n"
        "buffer_load_dwordx4 v58, s[24:27], s60 offen lds\n"
        "s_add_u32 m0, s64, 7168\n"
        "s_nop 0\n"
        "buffer_load_dwordx4 v59, s[24:27], s60 offen lds\n"
        "s_add_u32 m0, s64, 8192\n"
        "s_nop 0\n"
        "buffer_load_dwordx4 v60, s[24:27], s60 offen lds\n"
        "s_cmp_lt_u32 s6, 2\n"
        "s_cselect_b32 s62, 3, 16\n"
        "s_cselect_b32 s63, 12, 32\n"
        "s_cselect_b64 s[24:25], s[50:51], s[52:53]\n"
        "s_cselect_b32 s64, 0, 117088\n"
        "s_mul_i32 s62, s62, 192\n"
        "s_mul_i32 s63, s63, 192\n"
        "s_and_b32 s25, s25, 0xffff\n"
        "s_mul_i32 s7, s6, 9216\n"
        "s_add_u32 s64, s64, s7\n"
        "s_add_u32 s64, s64, 0\n"
        "s_add_u32 s7, s62, s63\n"
        "s_lshl_b32 s29, s63, 1\n"
        "s_add_u32 s60, s7, s63\n"
        "s_bitcmp1_b32 s6, 0\n"
        "s_cselect_b32 s28, s7, 0\n"
        "s_cselect_b32 s29, s29, s62\n"
        "s_cselect_b32 s60, s60, s63\n"
        "s_add_u32 s28, s28, s61\n"
        "s_add_u32 s29, s29, s61\n"
        "s_add_u32 s60, s60, s61\n"
        "s_add_u32 m0, s64, 0\n"
        "s_nop 0\n"
        "buffer_load_dwordx4 v32, s[24:27], s28 offen lds\n"
        "s_add_u32 m0, s64, 1024\n"
        "s_nop 0\n"
        "buffer_load_dwordx4 v33, s[24:27], s28 offen lds\n"
        "s_add_u32 m0, s64, 2048\n"
        "s_nop 0\n"
        "buffer_load_dwordx4 v34, s[24:27], s28 offen lds\n"
        "s_add_u32 m0, s64, 3072\n"
        "s_nop 0\n"
        "buffer_load_dwordx4 v32, s[24:27], s29 offen lds\n"
        "s_add_u32 m0, s64, 4096\n"
        "s_nop 0\n"
        "buffer_load_dwordx4 v33, s[24:27], s29 offen lds\n"
        "s_add_u32 m0, s64, 5120\n"
        "s_nop 0\n"
        "buffer_load_dwordx4 v34, s[24:27], s29 offen lds\n"
        "s_add_u32 m0, s64, 6144\n"
        "s_nop 0\n"
        "buffer_load_dwordx4 v32, s[24:27], s60 offen lds\n"
        "s_add_u32 m0, s64, 7168\n"
        "s_nop 0\n"
        "buffer_load_dwordx4 v33, s[24:27], s60 offen lds\n"
        "s_add_u32 m0, s64, 8192\n"
        "s_nop 0\n"
        "buffer_load_dwordx4 v34, s[24:27], s60 offen lds\n"
        "s_setprio 2\n"
        "s_waitcnt vmcnt(9)\n"
        "s_mov_b64 exec, 1\n"
        "ds_inc_u32 v250, v250 offset:20\n"
        "s_mov_b64 exec, -1\n"
        "L79_%=:\n"
        "ds_read_b32 v251, v250 offset:20\n"
        "s_waitcnt lgkmcnt(0)\n"
        "v_readfirstlane_b32 s40, v251\n"
        "s_nop 3\n"
        "s_cmp_ge_i32 s40, 4\n"
        "s_cbranch_scc1 L80_%=\n"
        "s_nop 0\n"
        "s_sub_u32 s41, s41, 1\n"
        "s_cbranch_scc0 L79_%=\n"
        "s_endpgm\n"
        "L80_%=:\n"
        "s_add_u32 m0, s17, 0\n"
        "s_nop 0\n"
        "buffer_load_dwordx4 v29, s[8:11], s74 offen lds\n"
        "buffer_load_dwordx4 v30, s[8:11], s74 offen offset:1024 lds\n"
        "buffer_load_dwordx4 v31, s[8:11], s74 offen offset:2048 lds\n"
        "s_add_u32 m0, s17, 3104\n"
        "s_nop 0\n"
        "buffer_load_dwordx4 v29, s[8:11], s75 offen lds\n"
        "buffer_load_dwordx4 v30, s[8:11], s75 offen offset:1024 lds\n"
        "buffer_load_dwordx4 v31, s[8:11], s75 offen offset:2048 lds\n"
        "s_cmp_eq_u32 s6, 0\n"
        "s_cbranch_scc1 L81_%=\n"
        "s_mov_b32 s34, 0x411cc471\n"
        "s_mov_b32 s35, 21632\n"
        "s_mov_b32 s36, 21624\n"
        "s_mov_b32 s37, 21628\n"
        "s_call_b64 s[30:31], FOLDW_%=\n"
        "s_branch L82_%=\n"
        "L81_%=:\n"
        "s_waitcnt vmcnt(6)\n"
        "s_mov_b64 exec, 1\n"
        "ds_inc_u32 v250, v250 offset:32\n"
        "s_mov_b64 exec, -1\n"
        "s_mov_b64 exec, 1\n"
        "ds_inc_u32 v250, v250 offset:28\n"
        "s_mov_b64 exec, -1\n"
        "global_load_dwordx4 v[40:43], v57, s[22:23] offset:-3072\n"
        "global_load_dwordx4 v[44:47], v57, s[22:23] offset:-2048\n"
        "global_load_dwordx4 v[0:3], v57, s[22:23] offset:-1024\n"
        "global_load_dwordx4 v[4:7], v57, s[22:23] offset:0\n"
        "global_load_dwordx4 v[8:11], v57, s[22:23] offset:1024\n"
        "global_load_dwordx4 v[12:15], v57, s[22:23] offset:2048\n"
        "global_load_dwordx4 v[16:19], v57, s[22:23] offset:3072\n"
        "L82_%=:\n"
        "s_mov_b64 s[24:25], s[56:57]\n"
        "s_mov_b32 s44, 0xf0000f\n"
        "s_mov_b32 s45, 0xf0000f00\n"
        "s_mov_b32 s46, 0xff000ff\n"
        "s_mov_b32 s47, 0xf00fff00\n"
        "s_mov_b32 s48, 0x22222222\n"
        "s_mov_b32 s49, 0x22222222\n"
        "s_mov_b32 s50, 0x44444444\n"
        "s_mov_b32 s51, 0x44444444\n"
        "s_mov_b32 s52, 0x88888888\n"
        "s_mov_b32 s53, 0x88888888\n"
        "s_mov_b64 vcc, s[44:45]\n"
        "s_mov_b32 s28, 0x3c2aaaab\n"
        "s_mov_b32 s29, 0x3a7ba882\n"
        "s_mov_b32 s30, 0\n"
        "s_mov_b32 s34, 0\n"
        "s_mov_b32 s31, s18\n"
        "s_mov_b32 s36, s18\n"
        "s_mov_b32 s37, s18\n"
        "s_add_u32 s71, s15, 588672\n"
        "s_sub_u32 s72, 73, s16\n"
        "s_add_u32 s73, s17, 3104\n"
        "s_mov_b32 s68, 4\n"
        "s_mov_b32 s76, 16\n"
        "s_mov_b32 s74, -1\n"
        "s_mov_b32 s41, 0x400000\n"
        "L22_%=:\n"
        "ds_read_b32 v251, v250 offset:28\n"
        "s_waitcnt lgkmcnt(0)\n"
        "v_readfirstlane_b32 s40, v251\n"
        "s_nop 3\n"
        "s_cmp_ge_i32 s40, 7\n"
        "s_cbranch_scc1 L23_%=\n"
        "s_nop 0\n"
        "s_sub_u32 s41, s41, 1\n"
        "s_cbranch_scc0 L22_%=\n"
        "s_endpgm\n"
        "L23_%=:\n"
        "s_waitcnt vmcnt(0)\n"
        "v_readfirstlane_b32 s26, v253\n"
        "ds_read_b128 v[92:95], v16\n"
        "ds_read_b64 v[84:85], v14\n"
        "ds_read_b64 v[76:77], v14\n"
        "ds_read_b64 v[24:25], v14\n"
        "ds_read_b64 v[80:81], v14\n"
        "v_mov_b32 v91, 21600\n"
        "v_add_u32 v20, 21728, v13\n"
        "ds_read_b128 v[112:115], v20 offset:0\n"
        "ds_read_b128 v[116:119], v20 offset:1024\n"
        "ds_read_b128 v[120:123], v20 offset:2048\n"
        "ds_read_b128 v[124:127], v20 offset:3072\n"
        "ds_read_b128 v[128:131], v20 offset:4096\n"
        "ds_read_b128 v[132:135], v20 offset:5120\n"
        "ds_read_b128 v[136:139], v20 offset:6144\n"
        "ds_read_b128 v[140:143], v20 offset:7168\n"
        "ds_read_b128 v[144:147], v20 offset:8192\n"
        "ds_read_b128 v[148:151], v20 offset:9216\n"
        "ds_read_b128 v[152:155], v20 offset:10240\n"
        "ds_read_b128 v[156:159], v20 offset:11264\n"
        "ds_read_b128 v[160:163], v20 offset:12288\n"
        "ds_read_b128 v[164:167], v20 offset:13312\n"
        "ds_read_b128 v[168:171], v20 offset:14336\n"
        "ds_read_b128 v[172:175], v20 offset:15360\n"
        "ds_read_b128 v[176:179], v20 offset:16384\n"
        "ds_read_b128 v[180:183], v20 offset:17408\n"
        "ds_read_b128 v[184:187], v20 offset:18432\n"
        "ds_read_b128 v[188:191], v20 offset:19456\n"
        "ds_read_b128 v[192:195], v20 offset:20480\n"
        "ds_read_b128 v[196:199], v20 offset:21520\n"
        "ds_read_b128 v[200:203], v20 offset:22544\n"
        "ds_read_b128 v[204:207], v20 offset:23568\n"
        "ds_read_b128 v[208:211], v20 offset:24608\n"
        "ds_read_b128 v[212:215], v20 offset:25632\n"
        "ds_read_b128 v[216:219], v20 offset:26656\n"
        "ds_read_b128 v[220:223], v20 offset:27696\n"
        "ds_read_b128 v[224:227], v20 offset:28720\n"
        "ds_read_b128 v[228:231], v20 offset:29744\n"
        "ds_read_b128 v[232:235], v20 offset:30784\n"
        "ds_read_b128 v[236:239], v20 offset:31808\n"
        "ds_read_b128 v[240:243], v20 offset:32832\n"
        "ds_read_b128 v[244:247], v20 offset:33872\n"
        "ds_read_b128 v[248:251], v20 offset:34896\n"
        "ds_read_b128 v[252:255], v20 offset:35920\n"
        "ds_read_b128 v[56:59], v0 offset:0\n"
        "ds_read_b128 v[62:65], v0 offset:64\n"
        "ds_read_b128 v[68:71], v0 offset:128\n"
        "ds_read_b128 v[38:41], v0 offset:768\n"
        "ds_read_b128 v[44:47], v0 offset:832\n"
        "ds_read_b128 v[50:53], v0 offset:896\n"
        "s_add_u32 s37, s37, 4\n"
        "s_cmp_ge_u32 s37, 49\n"
        "s_cselect_b32 s69, 49, 0\n"
        "s_sub_u32 s37, s37, s69\n"
        "s_sub_u32 s69, s37, 5\n"
        "s_cmp_lt_u32 s69, 4\n"
        "s_subb_u32 s36, s36, -4\n"
        "s_cmp_ge_u32 s36, 6\n"
        "s_cselect_b32 s69, 6, 0\n"
        "s_addc_u32 s34, s34, 0\n"
        "s_sub_u32 s36, s36, s69\n"
        "s_mul_i32 s38, s34, 1344\n"
        "s_cmp_ge_u32 s34, s76\n"
        "s_cbranch_scc0 L26_%=\n"
        "s_nop 1\n"
        "v_mov_b32 v22, 21600\n"
        "L24_%=:\n"
        "ds_read_b32 v23, v22 offset:64\n"
        "s_waitcnt lgkmcnt(0)\n"
        "v_readfirstlane_b32 s40, v23\n"
        "s_nop 3\n"
        "s_cmp_ge_i32 s40, 1\n"
        "s_cbranch_scc1 L25_%=\n"
        "s_sleep 1\n"
        "s_sub_u32 s41, s41, 1\n"
        "s_cbranch_scc0 L24_%=\n"
        "s_endpgm\n"
        "L25_%=:\n"
        "v_mbcnt_lo_u32_b32 v23, -1, 0\n"
        "v_lshl_add_u32 v22, v23, 2, v22\n"
        "ds_read_b32 v23, v22 offset:68\n"
        "s_mov_b32 s14, s40\n"
        "s_mov_b32 s76, -1\n"
        "s_waitcnt lgkmcnt(0)\n"
        "v_readlane_b32 s22, v23, 0\n"
        "v_readlane_b32 s23, v23, 1\n"
        "s_nop 3\n"
        "L26_%=:\n"
        "s_cmp_ge_u32 s34, s72\n"
        "s_cselect_b32 s70, s71, s15\n"
        "s_cmp_ge_u32 s34, 20\n"
        "s_cselect_b32 s70, s22, s70\n"
        "s_cmp_ge_u32 s34, 21\n"
        "s_cselect_b32 s70, s23, s70\n"
        "s_add_u32 s38, s38, s70\n"
        "s_mul_i32 s69, s36, 98304\n"
        "s_add_u32 s38, s38, s69\n"
        "s_mov_b32 s35, s36\n"
        "s_mul_i32 s69, s36, -12\n"
        "s_add_u32 s69, s69, 60\n"
        "s_lshl_b64 s[54:55], -1, s69\n"
        "s_lshl_b64 s[56:57], s[54:55], 24\n"
        "s_ashr_i64 s[60:61], s[54:55], 8\n"
        "s_mov_b32 s58, s61\n"
        "s_mov_b32 s59, s56\n"
        "v_cndmask_b32_e64 v15, v4, v7, s[56:57]\n"
        "v_cndmask_b32_e64 v89, v5, v8, s[58:59]\n"
        "v_cndmask_b32_e64 v90, v6, v9, s[60:61]\n"
        "s_cmp_eq_u32 s37, 4\n"
        "s_cbranch_scc0 L27_%=\n"
        "s_nop 1\n"
        "ds_read_b32 v15, v13 offset:19552\n"
        "ds_read_b32 v89, v13 offset:19556\n"
        "ds_read_b32 v90, v13 offset:19560\n"
        "s_mov_b32 s35, 6\n"
        "s_mov_b32 s38, 0\n"
        "s_mov_b32 s69, 1344\n"
        "s_waitcnt lgkmcnt(0)\n"
        "v_lshrrev_b32 v22, 28, v15\n"
        "v_add_u32 v22, s34, v22\n"
        "v_and_b32 v15, 0x0fffffff, v15\n"
        "v_mad_u32_u24 v15, v22, s69, v15\n"
        "v_mov_b32 v23, s15\n"
        "v_cmpx_le_u32 vcc, s72, v22\n"
        "v_mov_b32 v23, s71\n"
        "s_mov_b64 exec, -1\n"
        "v_cmpx_le_u32 vcc, 20, v22\n"
        "v_mov_b32 v23, s22\n"
        "s_mov_b64 exec, -1\n"
        "v_cmpx_le_u32 vcc, 21, v22\n"
        "v_mov_b32 v23, s23\n"
        "s_mov_b64 exec, -1\n"
        "v_add_u32 v15, v15, v23\n"
        "v_lshrrev_b32 v22, 28, v89\n"
        "v_add_u32 v22, s34, v22\n"
        "v_and_b32 v89, 0x0fffffff, v89\n"
        "v_mad_u32_u24 v89, v22, s69, v89\n"
        "v_mov_b32 v23, s15\n"
        "v_cmpx_le_u32 vcc, s72, v22\n"
        "v_mov_b32 v23, s71\n"
        "s_mov_b64 exec, -1\n"
        "v_cmpx_le_u32 vcc, 20, v22\n"
        "v_mov_b32 v23, s22\n"
        "s_mov_b64 exec, -1\n"
        "v_cmpx_le_u32 vcc, 21, v22\n"
        "v_mov_b32 v23, s23\n"
        "s_mov_b64 exec, -1\n"
        "v_add_u32 v89, v89, v23\n"
        "v_lshrrev_b32 v22, 28, v90\n"
        "v_add_u32 v22, s34, v22\n"
        "v_and_b32 v90, 0x0fffffff, v90\n"
        "v_mad_u32_u24 v90, v22, s69, v90\n"
        "v_mov_b32 v23, s15\n"
        "v_cmpx_le_u32 vcc, s72, v22\n"
        "v_mov_b32 v23, s71\n"
        "s_mov_b64 exec, -1\n"
        "v_cmpx_le_u32 vcc, 20, v22\n"
        "v_mov_b32 v23, s22\n"
        "s_mov_b64 exec, -1\n"
        "v_cmpx_le_u32 vcc, 21, v22\n"
        "v_mov_b32 v23, s23\n"
        "s_mov_b64 exec, -1\n"
        "v_add_u32 v90, v90, v23\n"
        "s_nop 4\n"
        "s_mov_b64 vcc, s[44:45]\n"
        "L27_%=:\n"
        "s_cmp_lt_u32 s34, s14\n"
        "s_cselect_b32 s38, s38, 0x80000000\n"
        "s_add_u32 s39, s38, 100270464\n"
        "s_waitcnt lgkmcnt(0)\n"
        "s_mov_b64 exec, 1\n"
        "ds_inc_u32 v91, v10 offset:8\n"
        "s_mov_b64 exec, -1\n"
        "s_cmp_eq_u32 s18, 0\n"
        "s_cselect_b32 s75, -20, 8\n"
        "s_setprio 1\n"
        "PTOP_%=:\n"
        "s_waitcnt lgkmcnt(5)\n"
        "v_mfma_f32_16x16x32_bf16 v[96:99], v[112:115], v[56:59], 0\n"
        "s_setprio 3\n"
        "v_cndmask_b32_e64 v82, v30, v31, s[48:49]\n"
        "s_nop 0\n"
        "v_cndmask_b32_e64 v83, v104, v105, s[48:49]\n"
        "s_waitcnt lgkmcnt(3)\n"
        "v_mfma_f32_16x16x32_bf16 v[96:99], v[116:119], v[62:65], v[96:99]\n"
        "s_mov_b32 m0, s17\n"
        "ds_read_b32 v88, v91 offset:8\n"
        "v_cndmask_b32_e64 v82, v82, v32, s[50:51]\n"
        "s_waitcnt lgkmcnt(2)\n"
        "v_mfma_f32_16x16x32_bf16 v[96:99], v[120:123], v[68:71], v[96:99]\n"
        "s_setprio 1\n"
        "s_waitcnt lgkmcnt(1)\n"
        "buffer_load_dwordx4 v15, s[8:11], s38 offen lds\n"
        "v_mfma_f32_16x16x32_bf16 v[100:103], v[132:135], v[68:71], 0\n"
        "s_setprio 3\n"
        "v_cndmask_b32_e64 v83, v83, v106, s[50:51]\n"
        "v_cndmask_b32_e64 v82, v82, v33, s[52:53]\n"
        "v_mfma_f32_16x16x32_bf16 v[100:103], v[128:131], v[62:65], v[100:103]\n"
        "v_cndmask_b32_e64 v83, v83, v107, s[52:53]\n"
        "v_mfma_f32_16x16x32_bf16 v[100:103], v[124:127], v[56:59], v[100:103]\n"
        "s_setprio 1\n"
        "buffer_load_dwordx4 v89, s[8:11], s38 offen offset:1024 lds\n"
        "v_mfma_f32_16x16x32_bf16 v[30:33], v[136:139], v[56:59], 0\n"
        "s_setprio 3\n"
        "s_waitcnt lgkmcnt(0)\n"
        "v_readfirstlane_b32 s40, v88\n"
        "v_mfma_f32_16x16x32_bf16 v[30:33], v[140:143], v[62:65], v[30:33]\n"
        "s_cmp_lt_i32 s40, s68\n"
        "v_cndmask_b32_dpp v82, v83, v82, vcc row_ror:4 row_mask:0xf bank_mask:0xf\n"
        "v_mfma_f32_16x16x32_bf16 v[30:33], v[144:147], v[68:71], v[30:33]\n"
        "s_setprio 1\n"
        "s_cbranch_scc1 L3_%=\n"
        "L4_%=:\n"
        "ds_write_b128 v11, v[34:37]\n"
        "v_mfma_f32_16x16x32_bf16 v[34:37], v[156:159], v[68:71], 0\n"
        "s_setprio 3\n"
        "v_mfma_f32_16x16x32_bf16 v[34:37], v[152:155], v[62:65], v[34:37]\n"
        "v_mfma_f32_16x16x32_bf16 v[34:37], v[148:151], v[56:59], v[34:37]\n"
        "s_setprio 1\n"
        "buffer_load_dwordx4 v90, s[8:11], s38 offen offset:2048 lds\n"
        "s_mov_b32 m0, s73\n"
        "ds_write_b128 v11, v[16:19] offset:832\n"
        "v_mfma_f32_16x16x32_bf16 v[104:107], v[160:163], v[56:59], 0\n"
        "s_setprio 3\n"
        "v_mfma_f32_16x16x32_bf16 v[104:107], v[164:167], v[62:65], v[104:107]\n"
        "v_mfma_f32_16x16x32_bf16 v[104:107], v[168:171], v[68:71], v[104:107]\n"
        "s_setprio 1\n"
        "buffer_load_dwordx4 v15, s[8:11], s39 offen lds\n"
        "ds_write_b128 v11, v[108:111] offset:64\n"
        "s_mov_b64 exec, s[46:47]\n"
        "ds_write_b32 v12, v82\n"
        "s_mov_b64 exec, -1\n"
        "v_mfma_f32_16x16x32_bf16 v[108:111], v[180:183], v[68:71], 0\n"
        "s_setprio 3\n"
        "v_mfma_f32_16x16x32_bf16 v[108:111], v[176:179], v[62:65], v[108:111]\n"
        "v_mfma_f32_16x16x32_bf16 v[108:111], v[172:175], v[56:59], v[108:111]\n"
        "s_setprio 1\n"
        "buffer_load_dwordx4 v89, s[8:11], s39 offen offset:1024 lds\n"
        "ds_write_b128 v11, v[20:23] offset:896\n"
        "v_mfma_f32_16x16x32_bf16 v[16:19], v[56:59], v[56:59], 0\n"
        "s_setprio 3\n"
        "v_mfma_f32_16x16x32_bf16 v[16:19], v[62:65], v[62:65], v[16:19]\n"
        "v_mfma_f32_16x16x32_bf16 v[16:19], v[68:71], v[68:71], v[16:19]\n"
        "s_setprio 1\n"
        "buffer_load_dwordx4 v90, s[8:11], s39 offen offset:2048 lds\n"
        "ds_write_b128 v11, v[72:75] offset:128\n"
        "v_mov_b64 v[20:21], 0\n"
        "v_mfma_f32_16x16x32_bf16 v[72:75], v[38:41], v[38:41], 0\n"
        "s_setprio 3\n"
        "v_mov_b64 v[22:23], 0\n"
        "v_mfma_f32_16x16x32_bf16 v[72:75], v[44:47], v[44:47], v[72:75]\n"
        "v_cndmask_b32_e64 v79, v16, v17, s[48:49]\n"
        "v_cndmask_b32_e64 v79, v79, v18, s[50:51]\n"
        "v_mfma_f32_16x16x32_bf16 v[72:75], v[50:53], v[50:53], v[72:75]\n"
        "s_setprio 1\n"
        "ds_write_b128 v11, v[26:29] offset:960\n"
        "v_smfmac_f32_16x16x64_bf16 v[20:23], v[56:59], v[96:103], v10\n"
        "s_setprio 3\n"
        "v_cndmask_b32_e64 v79, v79, v19, s[52:53]\n"
        "v_smfmac_f32_16x16x64_bf16 v[20:23], v[62:65], v[30:37], v10\n"
        "v_smfmac_f32_16x16x64_bf16 v[20:23], v[68:71], v[104:111], v10\n"
        "s_setprio 1\n"
        "s_mov_b64 exec, 1\n"
        "ds_inc_u32 v91, v10 offset:0\n"
        "s_mov_b64 exec, -1\n"
        "v_mfma_f32_16x16x32_bf16 v[104:107], v[112:115], v[38:41], 0\n"
        "s_setprio 3\n"
        "v_cndmask_b32_e64 v111, v72, v73, s[48:49]\n"
        "v_cndmask_b32_e64 v111, v111, v74, s[50:51]\n"
        "v_mfma_f32_16x16x32_bf16 v[104:107], v[116:119], v[44:47], v[104:107]\n"
        "s_and_b32 s69, s30, 1\n"
        "v_cndmask_b32_e64 v111, v111, v75, s[52:53]\n"
        "v_mfma_f32_16x16x32_bf16 v[104:107], v[120:123], v[50:53], v[104:107]\n"
        "s_setprio 1\n"
        "v_cndmask_b32_dpp v79, v111, v79, vcc row_ror:4 row_mask:0xf bank_mask:0xf\n"
        "s_lshl2_add_u32 s70, s69, 21600\n"
        "v_mfma_f32_16x16x32_bf16 v[108:111], v[132:135], v[50:53], 0\n"
        "s_setprio 3\n"
        "v_mov_b32 v91, s70\n"
        "s_mul_i32 s69, s69, 40064\n"
        "v_mfma_f32_16x16x32_bf16 v[108:111], v[128:131], v[44:47], v[108:111]\n"
        "s_mul_i32 s70, s31, 1664\n"
        "s_add_u32 s69, s69, s70\n"
        "v_add_u32 v11, s69, v3\n"
        "v_mfma_f32_16x16x32_bf16 v[108:111], v[124:127], v[38:41], v[108:111]\n"
        "s_setprio 1\n"
        "v_add_u32 v12, s69, v2\n"
        "s_lshr_b32 s68, s30, 1\n"
        "v_mfma_f32_16x16x32_bf16 v[96:99], v[136:139], v[38:41], 0\n"
        "s_setprio 3\n"
        "s_lshl2_add_u32 s68, s68, 4\n"
        "s_cmp_eq_u32 s31, 6\n"
        "v_mfma_f32_16x16x32_bf16 v[96:99], v[140:143], v[44:47], v[96:99]\n"
        "s_cbranch_scc1 L7_%=\n"
        "L8_%=:\n"
        "s_mov_b32 s74, s30\n"
        "s_mov_b32 s30, s34\n"
        "v_mfma_f32_16x16x32_bf16 v[96:99], v[144:147], v[50:53], v[96:99]\n"
        "s_setprio 1\n"
        "s_mov_b32 s31, s35\n"
        "s_cmp_eq_u32 s74, s30\n"
        "v_mfma_f32_16x16x32_bf16 v[100:103], v[156:159], v[50:53], 0\n"
        "s_setprio 3\n"
        "s_cselect_b32 s65, 0, s14\n"
        "s_add_u32 s37, s37, 4\n"
        "v_mfma_f32_16x16x32_bf16 v[100:103], v[152:155], v[44:47], v[100:103]\n"
        "s_cmp_ge_u32 s37, 49\n"
        "s_cselect_b32 s69, 49, 0\n"
        "s_sub_u32 s37, s37, s69\n"
        "v_mfma_f32_16x16x32_bf16 v[100:103], v[148:151], v[38:41], v[100:103]\n"
        "s_setprio 1\n"
        "s_sub_u32 s69, s37, 5\n"
        "s_cmp_lt_u32 s69, 4\n"
        "v_mfma_f32_16x16x32_bf16 v[30:33], v[160:163], v[38:41], 0\n"
        "s_setprio 3\n"
        "s_subb_u32 s36, s36, -4\n"
        "s_cmp_ge_u32 s36, 6\n"
        "v_mfma_f32_16x16x32_bf16 v[30:33], v[164:167], v[44:47], v[30:33]\n"
        "s_cselect_b32 s69, 6, 0\n"
        "s_addc_u32 s34, s34, 0\n"
        "v_mfma_f32_16x16x32_bf16 v[30:33], v[168:171], v[50:53], v[30:33]\n"
        "s_setprio 1\n"
        "s_sub_u32 s36, s36, s69\n"
        "s_mul_i32 s38, s34, 1344\n"
        "v_mfma_f32_16x16x32_bf16 v[34:37], v[180:183], v[50:53], 0\n"
        "s_setprio 3\n"
        "s_cmp_ge_u32 s34, s76\n"
        "s_cbranch_scc1 L9_%=\n"
        "L10_%=:\n"
        "v_mfma_f32_16x16x32_bf16 v[34:37], v[176:179], v[44:47], v[34:37]\n"
        "s_cmp_ge_u32 s34, s72\n"
        "s_cselect_b32 s70, s71, s15\n"
        "s_cmp_ge_u32 s34, 20\n"
        "v_mfma_f32_16x16x32_bf16 v[34:37], v[172:175], v[38:41], v[34:37]\n"
        "s_setprio 1\n"
        "s_cselect_b32 s70, s22, s70\n"
        "s_cmp_ge_u32 s34, 21\n"
        "v_mfma_f32_16x16x32_bf16 v[86:89], v[92:95], v[56:59], 0\n"
        "s_setprio 3\n"
        "s_cselect_b32 s70, s23, s70\n"
        "s_add_u32 s38, s38, s70\n"
        "v_mfma_f32_16x16x32_bf16 v[86:89], v[92:95], v[62:65], v[86:89]\n"
        "s_mul_i32 s69, s36, 98304\n"
        "s_add_u32 s38, s38, s69\n"
        "s_mov_b32 s35, s36\n"
        "v_mfma_f32_16x16x32_bf16 v[86:89], v[92:95], v[68:71], v[86:89]\n"
        "s_setprio 1\n"
        "v_mov_b64 v[26:27], 0\n"
        "v_mov_b64 v[28:29], 0\n"
        "v_mfma_f32_16x16x32_bf16 v[72:75], v[92:95], v[38:41], 0\n"
        "s_setprio 3\n"
        "s_mul_i32 s69, s36, -12\n"
        "s_add_u32 s69, s69, 60\n"
        "v_mfma_f32_16x16x32_bf16 v[72:75], v[92:95], v[44:47], v[72:75]\n"
        "s_lshl_b64 s[54:55], -1, s69\n"
        "s_lshl_b64 s[56:57], s[54:55], 24\n"
        "s_ashr_i64 s[60:61], s[54:55], 8\n"
        "v_mfma_f32_16x16x32_bf16 v[72:75], v[92:95], v[50:53], v[72:75]\n"
        "s_setprio 1\n"
        "s_mov_b32 s58, s61\n"
        "s_mov_b32 s59, s56\n"
        "v_smfmac_f32_16x16x64_bf16 v[26:29], v[38:41], v[104:111], v10\n"
        "s_setprio 3\n"
        "v_cndmask_b32_e64 v15, v4, v7, s[56:57]\n"
        "v_smfmac_f32_16x16x64_bf16 v[26:29], v[44:47], v[96:103], v10\n"
        "v_cndmask_b32_e64 v89, v5, v8, s[58:59]\n"
        "v_smfmac_f32_16x16x64_bf16 v[26:29], v[50:53], v[30:37], v10\n"
        "s_setprio 1\n"
        "v_cndmask_b32_e64 v90, v6, v9, s[60:61]\n"
        "s_cmp_eq_u32 s37, 4\n"
        "v_mfma_f32_16x16x32_bf16 v[16:19], v[50:53], v[192:195], 0\n"
        "s_setprio 3\n"
        "s_cbranch_scc1 L11_%=\n"
        "L12_%=:\n"
        "s_cmp_lt_u32 s34, s14\n"
        "v_mfma_f32_16x16x32_bf16 v[16:19], v[44:47], v[188:191], v[16:19]\n"
        "s_cselect_b32 s38, s38, 0x80000000\n"
        "s_add_u32 s39, s38, 100270464\n"
        "v_cndmask_b32_dpp v106, v72, v86, vcc row_ror:4 row_mask:0xf bank_mask:0xf\n"
        "v_mfma_f32_16x16x32_bf16 v[16:19], v[38:41], v[184:187], v[16:19]\n"
        "s_setprio 1\n"
        "v_mul_f32 v107, s28, v106\n"
        "v_fma_f32 v107, -v107, v106, v79\n"
        "v_add_f32_e64 v107, s29, |v107|\n"
        "v_mfma_f32_16x16x32_bf16 v[72:75], v[68:71], v[192:195], 0\n"
        "s_setprio 3\n"
        "v_rsq_f32 v83, v107\n"
        "v_mfma_f32_16x16x32_bf16 v[72:75], v[62:65], v[188:191], v[72:75]\n"
        "v_cndmask_b32_e64 v83, 0, v83, s[46:47]\n"
        "v_cndmask_b32_dpp v88, v26, v20, vcc row_ror:4 row_mask:0xf bank_mask:0xf\n"
        "v_mfma_f32_16x16x32_bf16 v[72:75], v[56:59], v[184:187], v[72:75]\n"
        "s_setprio 1\n"
        "v_cndmask_b32_dpp v79, v27, v21, vcc row_ror:4 row_mask:0xf bank_mask:0xf\n"
        "v_cndmask_b32_dpp v82, v28, v22, vcc row_ror:4 row_mask:0xf bank_mask:0xf\n"
        "v_mul_f32_dpp v88, v83, v88 quad_perm:[0,0,0,0] row_mask:0xf bank_mask:0xf\n"
        "v_mfma_f32_16x16x32_bf16 v[96:99], v[56:59], v[220:223], 0\n"
        "s_setprio 3\n"
        "v_mul_f32_dpp v79, v83, v79 quad_perm:[1,1,1,1] row_mask:0xf bank_mask:0xf\n"
        "v_mul_f32_dpp v82, v83, v82 quad_perm:[2,2,2,2] row_mask:0xf bank_mask:0xf\n"
        "v_mfma_f32_16x16x32_bf16 v[96:99], v[62:65], v[224:227], v[96:99]\n"
        "v_cndmask_b32_dpp v86, v29, v23, vcc row_ror:4 row_mask:0xf bank_mask:0xf\n"
        "v_max3_f32 v111, v88, v79, v82\n"
        "v_mfma_f32_16x16x32_bf16 v[96:99], v[68:71], v[228:231], v[96:99]\n"
        "s_setprio 1\n"
        "v_mul_f32_dpp v86, v83, v86 quad_perm:[3,3,3,3] row_mask:0xf bank_mask:0xf\n"
        "v_max_f32 v111, v111, v86\n"
        "v_mfma_f32_16x16x32_bf16 v[100:103], v[38:41], v[220:223], 0\n"
        "s_setprio 3\n"
        "v_mul_f32 v111, v111, v83\n"
        "v_fma_f32 v88, v88, v83, -v111\n"
        "v_mfma_f32_16x16x32_bf16 v[100:103], v[44:47], v[224:227], v[100:103]\n"
        "v_fma_f32 v79, v79, v83, -v111\n"
        "v_fma_f32 v82, v82, v83, -v111\n"
        "v_mfma_f32_16x16x32_bf16 v[100:103], v[50:53], v[228:231], v[100:103]\n"
        "s_setprio 1\n"
        "v_fma_f32 v86, v86, v83, -v111\n"
        "v_exp_f32 v88, v88\n"
        "v_mfma_f32_16x16x32_bf16 v[20:23], v[50:53], v[204:207], 0\n"
        "s_setprio 3\n"
        "v_exp_f32 v79, v79\n"
        "v_mfma_f32_16x16x32_bf16 v[20:23], v[44:47], v[200:203], v[20:23]\n"
        "v_exp_f32 v82, v82\n"
        "v_mfma_f32_16x16x32_bf16 v[20:23], v[38:41], v[196:199], v[20:23]\n"
        "s_setprio 1\n"
        "v_exp_f32 v86, v86\n"
        "v_add_f32 v87, v88, v79\n"
        "v_mfma_f32_16x16x32_bf16 v[104:107], v[68:71], v[204:207], 0\n"
        "s_setprio 3\n"
        "v_add_f32 v111, v82, v86\n"
        "v_add_f32 v87, v87, v111\n"
        "v_mfma_f32_16x16x32_bf16 v[104:107], v[62:65], v[200:203], v[104:107]\n"
        "v_rcp_f32 v87, v87\n"
        "v_mfma_f32_16x16x32_bf16 v[104:107], v[56:59], v[196:199], v[104:107]\n"
        "s_setprio 1\n"
        "v_mul_f32_dpp v88, v83, v88 quad_perm:[0,0,0,0] row_mask:0xf bank_mask:0xf\n"
        "v_mul_f32_dpp v79, v83, v79 quad_perm:[1,1,1,1] row_mask:0xf bank_mask:0xf\n"
        "v_mul_f32_dpp v82, v83, v82 quad_perm:[2,2,2,2] row_mask:0xf bank_mask:0xf\n"
        "v_mfma_f32_16x16x32_bf16 v[32:35], v[56:59], v[232:235], 0\n"
        "s_setprio 3\n"
        "v_mul_f32_dpp v86, v83, v86 quad_perm:[3,3,3,3] row_mask:0xf bank_mask:0xf\n"
        "v_mul_f32 v88, v88, v87\n"
        "v_mfma_f32_16x16x32_bf16 v[32:35], v[62:65], v[236:239], v[32:35]\n"
        "v_mul_f32 v79, v79, v87\n"
        "v_mul_f32 v82, v82, v87\n"
        "v_mfma_f32_16x16x32_bf16 v[32:35], v[68:71], v[240:243], v[32:35]\n"
        "s_setprio 1\n"
        "v_mul_f32 v86, v86, v87\n"
        "v_cvt_pk_bf16_f32 v87, v88, v79\n"
        "v_cvt_pk_bf16_f32 v88, v82, v86\n"
        "v_and_b32 v54, v1, v87\n"
        "v_mfma_f32_16x16x32_bf16 v[28:31], v[38:41], v[232:235], 0\n"
        "s_setprio 3\n"
        "v_and_b32 v55, v1, v88\n"
        "v_cvt_pk_bf16_f32 v82, v72, v73\n"
        "v_mfma_f32_16x16x32_bf16 v[28:31], v[44:47], v[236:239], v[28:31]\n"
        "v_cvt_pk_bf16_f32 v83, v74, v75\n"
        "v_and_b32_dpp v36, v87, v1 row_ror:12 row_mask:0xf bank_mask:0xf\n"
        "v_mfma_f32_16x16x32_bf16 v[28:31], v[50:53], v[240:243], v[28:31]\n"
        "s_setprio 1\n"
        "v_and_b32_dpp v37, v88, v1 row_ror:12 row_mask:0xf bank_mask:0xf\n"
        "v_cvt_pk_bf16_f32 v74, v16, v17\n"
        "v_cvt_pk_bf16_f32 v75, v18, v19\n"
        "v_mfma_f32_16x16x32_bf16 v[16:19], v[82:85], v[54:57], 0\n"
        "v_mov_b64 v[66:67], v[54:55]\n"
        "v_cvt_pk_bf16_f32 v86, v96, v97\n"
        "v_cvt_pk_bf16_f32 v87, v98, v99\n"
        "v_mfma_f32_16x16x32_bf16 v[108:111], v[74:77], v[36:39], 0\n"
        "v_mov_b64 v[48:49], v[36:37]\n"
        "v_cvt_pk_bf16_f32 v78, v100, v101\n"
        "v_mfma_f32_16x16x32_bf16 v[96:99], v[84:87], v[64:67], 0\n"
        "v_cvt_pk_bf16_f32 v79, v102, v103\n"
        "v_mov_b64 v[60:61], v[54:55]\n"
        "v_cvt_pk_bf16_f32 v26, v104, v105\n"
        "v_mfma_f32_16x16x32_bf16 v[100:103], v[76:79], v[46:49], 0\n"
        "v_cvt_pk_bf16_f32 v27, v106, v107\n"
        "v_mov_b64 v[42:43], v[36:37]\n"
        "v_mfma_f32_16x16x32_bf16 v[104:107], v[50:53], v[216:219], 0\n"
        "s_setprio 3\n"
        "v_cvt_pk_bf16_f32 v82, v20, v21\n"
        "v_cvt_pk_bf16_f32 v83, v22, v23\n"
        "v_mfma_f32_16x16x32_bf16 v[104:107], v[44:47], v[212:215], v[104:107]\n"
        "v_cvt_pk_bf16_f32 v22, v32, v33\n"
        "v_cvt_pk_bf16_f32 v23, v34, v35\n"
        "v_mfma_f32_16x16x32_bf16 v[104:107], v[38:41], v[208:211], v[104:107]\n"
        "s_setprio 1\n"
        "v_cvt_pk_bf16_f32 v78, v28, v29\n"
        "v_cvt_pk_bf16_f32 v79, v30, v31\n"
        "v_mfma_f32_16x16x32_bf16 v[28:31], v[68:71], v[216:219], 0\n"
        "s_setprio 3\n"
        "v_mov_b64 v[72:73], v[54:55]\n"
        "v_mov_b64 v[54:55], v[36:37]\n"
        "v_mfma_f32_16x16x32_bf16 v[28:31], v[62:65], v[212:215], v[28:31]\n"
        "v_cvt_pk_bf16_f32 v74, v104, v105\n"
        "v_cvt_pk_bf16_f32 v75, v106, v107\n"
        "v_mfma_f32_16x16x32_bf16 v[28:31], v[56:59], v[208:211], v[28:31]\n"
        "s_setprio 1\n"
        "v_cvt_pk_bf16_f32 v34, v16, v17\n"
        "v_cvt_pk_bf16_f32 v35, v18, v19\n"
        "v_mfma_f32_16x16x32_bf16 v[18:21], v[24:27], v[58:61], 0\n"
        "v_cvt_pk_bf16_f32 v36, v96, v97\n"
        "v_cvt_pk_bf16_f32 v37, v98, v99\n"
        "v_mfma_f32_16x16x32_bf16 v[104:107], v[80:83], v[40:43], 0\n"
        "v_cvt_pk_bf16_f32 v16, v108, v109\n"
        "v_cvt_pk_bf16_f32 v82, v28, v29\n"
        "v_mfma_f32_16x16x32_bf16 v[96:99], v[56:59], v[244:247], 0\n"
        "s_setprio 3\n"
        "v_cvt_pk_bf16_f32 v83, v30, v31\n"
        "v_cvt_pk_bf16_f32 v108, v18, v19\n"
        "v_mfma_f32_16x16x32_bf16 v[96:99], v[62:65], v[248:251], v[96:99]\n"
        "v_cvt_pk_bf16_f32 v109, v20, v21\n"
        "v_cvt_pk_bf16_f32 v17, v110, v111\n"
        "v_mfma_f32_16x16x32_bf16 v[96:99], v[68:71], v[252:255], v[96:99]\n"
        "s_setprio 1\n"
        "v_cvt_pk_bf16_f32 v18, v100, v101\n"
        "v_cvt_pk_bf16_f32 v19, v102, v103\n"
        "v_cvt_pk_bf16_f32 v20, v104, v105\n"
        "v_mfma_f32_16x16x32_bf16 v[100:103], v[38:41], v[244:247], 0\n"
        "s_setprio 3\n"
        "v_cvt_pk_bf16_f32 v21, v106, v107\n"
        "v_mfma_f32_16x16x32_bf16 v[100:103], v[44:47], v[248:251], v[100:103]\n"
        "v_cvt_pk_bf16_f32 v86, v96, v97\n"
        "v_cvt_pk_bf16_f32 v87, v98, v99\n"
        "v_mfma_f32_16x16x32_bf16 v[100:103], v[50:53], v[252:255], v[100:103]\n"
        "s_setprio 1\n"
        "v_mfma_f32_16x16x32_bf16 v[96:99], v[22:25], v[66:69], 0\n"
        "v_mfma_f32_16x16x32_bf16 v[26:29], v[78:81], v[48:51], 0\n"
        "v_mfma_f32_16x16x32_bf16 v[30:33], v[82:85], v[60:63], 0\n"
        "s_waitcnt vmcnt(0)\n"
        "ds_read_b128 v[56:59], v0\n"
        "ds_read_b128 v[38:41], v0 offset:768\n"
        "ds_read_b128 v[62:65], v0 offset:64\n"
        "v_cvt_pk_bf16_f32 v78, v100, v101\n"
        "v_mfma_f32_16x16x32_bf16 v[104:107], v[74:77], v[42:45], 0\n"
        "s_add_u32 s75, s75, 1\n"
        "ds_read_b128 v[44:47], v0 offset:832\n"
        "v_cvt_pk_bf16_f32 v79, v102, v103\n"
        "v_cvt_pk_bf16_f32 v110, v96, v97\n"
        "v_cvt_pk_bf16_f32 v111, v98, v99\n"
        "v_mfma_f32_16x16x32_bf16 v[100:103], v[84:87], v[70:73], 0\n"
        "s_cmp_lt_u32 s75, 4\n"
        "ds_read_b128 v[68:71], v0 offset:128\n"
        "v_cvt_pk_bf16_f32 v72, v30, v31\n"
        "v_cvt_pk_bf16_f32 v73, v32, v33\n"
        "v_cvt_pk_bf16_f32 v22, v26, v27\n"
        "v_mfma_f32_16x16x32_bf16 v[96:99], v[76:79], v[52:55], 0\n"
        "s_cbranch_scc1 L18_%=\n"
        "L19_%=:\n"
        "ds_read_b128 v[50:53], v0 offset:896\n"
        "v_cvt_pk_bf16_f32 v74, v100, v101\n"
        "v_cvt_pk_bf16_f32 v75, v102, v103\n"
        "v_cvt_pk_bf16_f32 v23, v28, v29\n"
        "v_mfma_f32_16x16x32_bf16 v[30:33], v[34:37], v[34:37], 0\n"
        "s_setprio 3\n"
        "v_cvt_pk_bf16_f32 v26, v104, v105\n"
        "v_cvt_pk_bf16_f32 v27, v106, v107\n"
        "v_mfma_f32_16x16x32_bf16 v[30:33], v[108:111], v[108:111], v[30:33]\n"
        "v_cvt_pk_bf16_f32 v28, v96, v97\n"
        "v_cvt_pk_bf16_f32 v29, v98, v99\n"
        "v_mfma_f32_16x16x32_bf16 v[30:33], v[72:75], v[72:75], v[30:33]\n"
        "s_setprio 1\n"
        "v_mfma_f32_16x16x32_bf16 v[104:107], v[16:19], v[16:19], 0\n"
        "s_setprio 3\n"
        "v_mfma_f32_16x16x32_bf16 v[104:107], v[20:23], v[20:23], v[104:107]\n"
        "v_mfma_f32_16x16x32_bf16 v[104:107], v[26:29], v[26:29], v[104:107]\n"
        "s_setprio 1\n"
        "s_cmp_lt_u32 s30, s65\n"
        "s_cbranch_scc1 PTOP_%=\n"
        "s_cmp_lg_u32 s74, s30\n"
        "s_cbranch_scc1 PEXIT_%=\n"
        "s_waitcnt lgkmcnt(5)\n"
        "v_mfma_f32_16x16x32_bf16 v[96:99], v[112:115], v[56:59], 0\n"
        "s_setprio 3\n"
        "v_cndmask_b32_e64 v82, v30, v31, s[48:49]\n"
        "s_nop 0\n"
        "v_cndmask_b32_e64 v83, v104, v105, s[48:49]\n"
        "s_waitcnt lgkmcnt(3)\n"
        "v_mfma_f32_16x16x32_bf16 v[96:99], v[116:119], v[62:65], v[96:99]\n"
        "s_mov_b32 m0, s17\n"
        "ds_read_b32 v88, v91 offset:8\n"
        "v_cndmask_b32_e64 v82, v82, v32, s[50:51]\n"
        "s_waitcnt lgkmcnt(2)\n"
        "v_mfma_f32_16x16x32_bf16 v[96:99], v[120:123], v[68:71], v[96:99]\n"
        "s_setprio 1\n"
        "s_waitcnt lgkmcnt(1)\n"
        "buffer_load_dwordx4 v15, s[8:11], s38 offen lds\n"
        "v_mfma_f32_16x16x32_bf16 v[100:103], v[132:135], v[68:71], 0\n"
        "s_setprio 3\n"
        "v_cndmask_b32_e64 v83, v83, v106, s[50:51]\n"
        "v_cndmask_b32_e64 v82, v82, v33, s[52:53]\n"
        "v_mfma_f32_16x16x32_bf16 v[100:103], v[128:131], v[62:65], v[100:103]\n"
        "v_cndmask_b32_e64 v83, v83, v107, s[52:53]\n"
        "v_mfma_f32_16x16x32_bf16 v[100:103], v[124:127], v[56:59], v[100:103]\n"
        "s_setprio 1\n"
        "buffer_load_dwordx4 v89, s[8:11], s38 offen offset:1024 lds\n"
        "v_mfma_f32_16x16x32_bf16 v[30:33], v[136:139], v[56:59], 0\n"
        "s_setprio 3\n"
        "s_waitcnt lgkmcnt(0)\n"
        "v_readfirstlane_b32 s40, v88\n"
        "v_mfma_f32_16x16x32_bf16 v[30:33], v[140:143], v[62:65], v[30:33]\n"
        "s_cmp_lt_i32 s40, s68\n"
        "v_cndmask_b32_dpp v82, v83, v82, vcc row_ror:4 row_mask:0xf bank_mask:0xf\n"
        "v_mfma_f32_16x16x32_bf16 v[30:33], v[144:147], v[68:71], v[30:33]\n"
        "s_setprio 1\n"
        "s_cbranch_scc1 B3_%=\n"
        "B4_%=:\n"
        "ds_write_b128 v11, v[34:37]\n"
        "v_mfma_f32_16x16x32_bf16 v[34:37], v[156:159], v[68:71], 0\n"
        "s_setprio 3\n"
        "v_mfma_f32_16x16x32_bf16 v[34:37], v[152:155], v[62:65], v[34:37]\n"
        "v_mfma_f32_16x16x32_bf16 v[34:37], v[148:151], v[56:59], v[34:37]\n"
        "s_setprio 3\n"
        "buffer_load_dwordx4 v90, s[8:11], s38 offen offset:2048 lds\n"
        "s_mov_b32 m0, s73\n"
        "ds_write_b128 v11, v[16:19] offset:832\n"
        "v_mfma_f32_16x16x32_bf16 v[104:107], v[160:163], v[56:59], 0\n"
        "s_setprio 3\n"
        "v_mfma_f32_16x16x32_bf16 v[104:107], v[164:167], v[62:65], v[104:107]\n"
        "v_mfma_f32_16x16x32_bf16 v[104:107], v[168:171], v[68:71], v[104:107]\n"
        "s_setprio 3\n"
        "buffer_load_dwordx4 v15, s[8:11], s39 offen lds\n"
        "ds_write_b128 v11, v[108:111] offset:64\n"
        "s_mov_b64 exec, s[46:47]\n"
        "ds_write_b32 v12, v82\n"
        "s_mov_b64 exec, -1\n"
        "v_mfma_f32_16x16x32_bf16 v[108:111], v[180:183], v[68:71], 0\n"
        "s_setprio 3\n"
        "v_mfma_f32_16x16x32_bf16 v[108:111], v[176:179], v[62:65], v[108:111]\n"
        "v_mfma_f32_16x16x32_bf16 v[108:111], v[172:175], v[56:59], v[108:111]\n"
        "s_setprio 3\n"
        "buffer_load_dwordx4 v89, s[8:11], s39 offen offset:1024 lds\n"
        "ds_write_b128 v11, v[20:23] offset:896\n"
        "v_mfma_f32_16x16x32_bf16 v[16:19], v[56:59], v[56:59], 0\n"
        "s_setprio 3\n"
        "v_mfma_f32_16x16x32_bf16 v[16:19], v[62:65], v[62:65], v[16:19]\n"
        "v_mfma_f32_16x16x32_bf16 v[16:19], v[68:71], v[68:71], v[16:19]\n"
        "s_setprio 3\n"
        "buffer_load_dwordx4 v90, s[8:11], s39 offen offset:2048 lds\n"
        "ds_write_b128 v11, v[72:75] offset:128\n"
        "v_mov_b64 v[20:21], 0\n"
        "v_mfma_f32_16x16x32_bf16 v[72:75], v[38:41], v[38:41], 0\n"
        "s_setprio 3\n"
        "v_mov_b64 v[22:23], 0\n"
        "v_mfma_f32_16x16x32_bf16 v[72:75], v[44:47], v[44:47], v[72:75]\n"
        "v_cndmask_b32_e64 v79, v16, v17, s[48:49]\n"
        "v_cndmask_b32_e64 v79, v79, v18, s[50:51]\n"
        "v_mfma_f32_16x16x32_bf16 v[72:75], v[50:53], v[50:53], v[72:75]\n"
        "s_setprio 3\n"
        "ds_write_b128 v11, v[26:29] offset:960\n"
        "v_smfmac_f32_16x16x64_bf16 v[20:23], v[56:59], v[96:103], v10\n"
        "s_setprio 3\n"
        "v_cndmask_b32_e64 v79, v79, v19, s[52:53]\n"
        "v_smfmac_f32_16x16x64_bf16 v[20:23], v[62:65], v[30:37], v10\n"
        "v_smfmac_f32_16x16x64_bf16 v[20:23], v[68:71], v[104:111], v10\n"
        "s_setprio 3\n"
        "s_mov_b64 exec, 1\n"
        "ds_inc_u32 v91, v10 offset:0\n"
        "s_mov_b64 exec, -1\n"
        "v_mfma_f32_16x16x32_bf16 v[104:107], v[112:115], v[38:41], 0\n"
        "s_setprio 3\n"
        "v_cndmask_b32_e64 v111, v72, v73, s[48:49]\n"
        "v_cndmask_b32_e64 v111, v111, v74, s[50:51]\n"
        "v_mfma_f32_16x16x32_bf16 v[104:107], v[116:119], v[44:47], v[104:107]\n"
        "s_and_b32 s69, s30, 1\n"
        "v_cndmask_b32_e64 v111, v111, v75, s[52:53]\n"
        "v_mfma_f32_16x16x32_bf16 v[104:107], v[120:123], v[50:53], v[104:107]\n"
        "s_setprio 3\n"
        "v_cndmask_b32_dpp v79, v111, v79, vcc row_ror:4 row_mask:0xf bank_mask:0xf\n"
        "s_lshl2_add_u32 s70, s69, 21600\n"
        "v_mfma_f32_16x16x32_bf16 v[108:111], v[132:135], v[50:53], 0\n"
        "s_setprio 3\n"
        "v_mov_b32 v91, s70\n"
        "s_mul_i32 s69, s69, 40064\n"
        "v_mfma_f32_16x16x32_bf16 v[108:111], v[128:131], v[44:47], v[108:111]\n"
        "s_mul_i32 s70, s31, 1664\n"
        "s_add_u32 s69, s69, s70\n"
        "v_add_u32 v11, s69, v3\n"
        "v_mfma_f32_16x16x32_bf16 v[108:111], v[124:127], v[38:41], v[108:111]\n"
        "s_setprio 3\n"
        "v_add_u32 v12, s69, v2\n"
        "s_lshr_b32 s68, s30, 1\n"
        "v_mfma_f32_16x16x32_bf16 v[96:99], v[136:139], v[38:41], 0\n"
        "s_setprio 3\n"
        "s_lshl2_add_u32 s68, s68, 4\n"
        "s_cmp_eq_u32 s31, 6\n"
        "v_mfma_f32_16x16x32_bf16 v[96:99], v[140:143], v[44:47], v[96:99]\n"
        "s_cbranch_scc1 B7_%=\n"
        "B8_%=:\n"
        "s_mov_b32 s74, s30\n"
        "s_mov_b32 s30, s34\n"
        "v_mfma_f32_16x16x32_bf16 v[96:99], v[144:147], v[50:53], v[96:99]\n"
        "s_setprio 3\n"
        "s_mov_b32 s31, s35\n"
        "s_cmp_eq_u32 s74, s30\n"
        "v_mfma_f32_16x16x32_bf16 v[100:103], v[156:159], v[50:53], 0\n"
        "s_setprio 3\n"
        "s_cselect_b32 s65, 0, s14\n"
        "s_add_u32 s37, s37, 4\n"
        "v_mfma_f32_16x16x32_bf16 v[100:103], v[152:155], v[44:47], v[100:103]\n"
        "s_cmp_ge_u32 s37, 49\n"
        "s_cselect_b32 s69, 49, 0\n"
        "s_sub_u32 s37, s37, s69\n"
        "v_mfma_f32_16x16x32_bf16 v[100:103], v[148:151], v[38:41], v[100:103]\n"
        "s_setprio 3\n"
        "s_sub_u32 s69, s37, 5\n"
        "s_cmp_lt_u32 s69, 4\n"
        "v_mfma_f32_16x16x32_bf16 v[30:33], v[160:163], v[38:41], 0\n"
        "s_setprio 3\n"
        "s_subb_u32 s36, s36, -4\n"
        "s_cmp_ge_u32 s36, 6\n"
        "v_mfma_f32_16x16x32_bf16 v[30:33], v[164:167], v[44:47], v[30:33]\n"
        "s_cselect_b32 s69, 6, 0\n"
        "s_addc_u32 s34, s34, 0\n"
        "v_mfma_f32_16x16x32_bf16 v[30:33], v[168:171], v[50:53], v[30:33]\n"
        "s_setprio 3\n"
        "s_sub_u32 s36, s36, s69\n"
        "s_mul_i32 s38, s34, 1344\n"
        "v_mfma_f32_16x16x32_bf16 v[34:37], v[180:183], v[50:53], 0\n"
        "s_setprio 3\n"
        "s_cmp_ge_u32 s34, s76\n"
        "s_cbranch_scc1 B9_%=\n"
        "B10_%=:\n"
        "v_mfma_f32_16x16x32_bf16 v[34:37], v[176:179], v[44:47], v[34:37]\n"
        "s_cmp_ge_u32 s34, s72\n"
        "s_cselect_b32 s70, s71, s15\n"
        "s_cmp_ge_u32 s34, 20\n"
        "v_mfma_f32_16x16x32_bf16 v[34:37], v[172:175], v[38:41], v[34:37]\n"
        "s_setprio 3\n"
        "s_cselect_b32 s70, s22, s70\n"
        "s_cmp_ge_u32 s34, 21\n"
        "v_mfma_f32_16x16x32_bf16 v[86:89], v[92:95], v[56:59], 0\n"
        "s_setprio 3\n"
        "s_cselect_b32 s70, s23, s70\n"
        "s_add_u32 s38, s38, s70\n"
        "v_mfma_f32_16x16x32_bf16 v[86:89], v[92:95], v[62:65], v[86:89]\n"
        "s_mul_i32 s69, s36, 98304\n"
        "s_add_u32 s38, s38, s69\n"
        "s_mov_b32 s35, s36\n"
        "v_mfma_f32_16x16x32_bf16 v[86:89], v[92:95], v[68:71], v[86:89]\n"
        "s_setprio 3\n"
        "v_mov_b64 v[26:27], 0\n"
        "v_mov_b64 v[28:29], 0\n"
        "v_mfma_f32_16x16x32_bf16 v[72:75], v[92:95], v[38:41], 0\n"
        "s_setprio 3\n"
        "s_mul_i32 s69, s36, -12\n"
        "s_add_u32 s69, s69, 60\n"
        "v_mfma_f32_16x16x32_bf16 v[72:75], v[92:95], v[44:47], v[72:75]\n"
        "s_lshl_b64 s[54:55], -1, s69\n"
        "s_lshl_b64 s[56:57], s[54:55], 24\n"
        "s_ashr_i64 s[60:61], s[54:55], 8\n"
        "v_mfma_f32_16x16x32_bf16 v[72:75], v[92:95], v[50:53], v[72:75]\n"
        "s_setprio 3\n"
        "s_mov_b32 s58, s61\n"
        "s_mov_b32 s59, s56\n"
        "v_smfmac_f32_16x16x64_bf16 v[26:29], v[38:41], v[104:111], v10\n"
        "s_setprio 3\n"
        "v_cndmask_b32_e64 v15, v4, v7, s[56:57]\n"
        "v_smfmac_f32_16x16x64_bf16 v[26:29], v[44:47], v[96:103], v10\n"
        "v_cndmask_b32_e64 v89, v5, v8, s[58:59]\n"
        "v_smfmac_f32_16x16x64_bf16 v[26:29], v[50:53], v[30:37], v10\n"
        "s_setprio 3\n"
        "v_cndmask_b32_e64 v90, v6, v9, s[60:61]\n"
        "s_cmp_eq_u32 s37, 4\n"
        "v_mfma_f32_16x16x32_bf16 v[16:19], v[50:53], v[192:195], 0\n"
        "s_setprio 3\n"
        "s_cbranch_scc1 B11_%=\n"
        "B12_%=:\n"
        "s_cmp_lt_u32 s34, s14\n"
        "v_mfma_f32_16x16x32_bf16 v[16:19], v[44:47], v[188:191], v[16:19]\n"
        "s_cselect_b32 s38, s38, 0x80000000\n"
        "s_add_u32 s39, s38, 100270464\n"
        "v_cndmask_b32_dpp v106, v72, v86, vcc row_ror:4 row_mask:0xf bank_mask:0xf\n"
        "v_mfma_f32_16x16x32_bf16 v[16:19], v[38:41], v[184:187], v[16:19]\n"
        "s_setprio 3\n"
        "v_mul_f32 v107, s28, v106\n"
        "v_fma_f32 v107, -v107, v106, v79\n"
        "v_add_f32_e64 v107, s29, |v107|\n"
        "v_mfma_f32_16x16x32_bf16 v[72:75], v[68:71], v[192:195], 0\n"
        "s_setprio 3\n"
        "v_rsq_f32 v83, v107\n"
        "v_mfma_f32_16x16x32_bf16 v[72:75], v[62:65], v[188:191], v[72:75]\n"
        "v_cndmask_b32_e64 v83, 0, v83, s[46:47]\n"
        "v_cndmask_b32_dpp v88, v26, v20, vcc row_ror:4 row_mask:0xf bank_mask:0xf\n"
        "v_mfma_f32_16x16x32_bf16 v[72:75], v[56:59], v[184:187], v[72:75]\n"
        "s_setprio 3\n"
        "v_cndmask_b32_dpp v79, v27, v21, vcc row_ror:4 row_mask:0xf bank_mask:0xf\n"
        "v_cndmask_b32_dpp v82, v28, v22, vcc row_ror:4 row_mask:0xf bank_mask:0xf\n"
        "v_mul_f32_dpp v88, v83, v88 quad_perm:[0,0,0,0] row_mask:0xf bank_mask:0xf\n"
        "v_mfma_f32_16x16x32_bf16 v[96:99], v[56:59], v[220:223], 0\n"
        "s_setprio 3\n"
        "v_mul_f32_dpp v79, v83, v79 quad_perm:[1,1,1,1] row_mask:0xf bank_mask:0xf\n"
        "v_mul_f32_dpp v82, v83, v82 quad_perm:[2,2,2,2] row_mask:0xf bank_mask:0xf\n"
        "v_mfma_f32_16x16x32_bf16 v[96:99], v[62:65], v[224:227], v[96:99]\n"
        "v_cndmask_b32_dpp v86, v29, v23, vcc row_ror:4 row_mask:0xf bank_mask:0xf\n"
        "v_max3_f32 v111, v88, v79, v82\n"
        "v_mfma_f32_16x16x32_bf16 v[96:99], v[68:71], v[228:231], v[96:99]\n"
        "s_setprio 3\n"
        "v_mul_f32_dpp v86, v83, v86 quad_perm:[3,3,3,3] row_mask:0xf bank_mask:0xf\n"
        "v_max_f32 v111, v111, v86\n"
        "v_mfma_f32_16x16x32_bf16 v[100:103], v[38:41], v[220:223], 0\n"
        "s_setprio 3\n"
        "v_mul_f32 v111, v111, v83\n"
        "v_fma_f32 v88, v88, v83, -v111\n"
        "v_mfma_f32_16x16x32_bf16 v[100:103], v[44:47], v[224:227], v[100:103]\n"
        "v_fma_f32 v79, v79, v83, -v111\n"
        "v_fma_f32 v82, v82, v83, -v111\n"
        "v_mfma_f32_16x16x32_bf16 v[100:103], v[50:53], v[228:231], v[100:103]\n"
        "s_setprio 3\n"
        "v_fma_f32 v86, v86, v83, -v111\n"
        "v_exp_f32 v88, v88\n"
        "v_mfma_f32_16x16x32_bf16 v[20:23], v[50:53], v[204:207], 0\n"
        "s_setprio 3\n"
        "v_exp_f32 v79, v79\n"
        "v_mfma_f32_16x16x32_bf16 v[20:23], v[44:47], v[200:203], v[20:23]\n"
        "v_exp_f32 v82, v82\n"
        "v_mfma_f32_16x16x32_bf16 v[20:23], v[38:41], v[196:199], v[20:23]\n"
        "s_setprio 3\n"
        "v_exp_f32 v86, v86\n"
        "v_add_f32 v87, v88, v79\n"
        "v_mfma_f32_16x16x32_bf16 v[104:107], v[68:71], v[204:207], 0\n"
        "s_setprio 3\n"
        "v_add_f32 v111, v82, v86\n"
        "v_add_f32 v87, v87, v111\n"
        "v_mfma_f32_16x16x32_bf16 v[104:107], v[62:65], v[200:203], v[104:107]\n"
        "v_rcp_f32 v87, v87\n"
        "v_mfma_f32_16x16x32_bf16 v[104:107], v[56:59], v[196:199], v[104:107]\n"
        "s_setprio 3\n"
        "v_mul_f32_dpp v88, v83, v88 quad_perm:[0,0,0,0] row_mask:0xf bank_mask:0xf\n"
        "v_mul_f32_dpp v79, v83, v79 quad_perm:[1,1,1,1] row_mask:0xf bank_mask:0xf\n"
        "v_mul_f32_dpp v82, v83, v82 quad_perm:[2,2,2,2] row_mask:0xf bank_mask:0xf\n"
        "v_mfma_f32_16x16x32_bf16 v[32:35], v[56:59], v[232:235], 0\n"
        "s_setprio 3\n"
        "v_mul_f32_dpp v86, v83, v86 quad_perm:[3,3,3,3] row_mask:0xf bank_mask:0xf\n"
        "v_mul_f32 v88, v88, v87\n"
        "v_mfma_f32_16x16x32_bf16 v[32:35], v[62:65], v[236:239], v[32:35]\n"
        "v_mul_f32 v79, v79, v87\n"
        "v_mul_f32 v82, v82, v87\n"
        "v_mfma_f32_16x16x32_bf16 v[32:35], v[68:71], v[240:243], v[32:35]\n"
        "s_setprio 3\n"
        "v_mul_f32 v86, v86, v87\n"
        "v_cvt_pk_bf16_f32 v87, v88, v79\n"
        "v_cvt_pk_bf16_f32 v88, v82, v86\n"
        "v_and_b32 v54, v1, v87\n"
        "v_mfma_f32_16x16x32_bf16 v[28:31], v[38:41], v[232:235], 0\n"
        "s_setprio 3\n"
        "v_and_b32 v55, v1, v88\n"
        "v_cvt_pk_bf16_f32 v82, v72, v73\n"
        "v_mfma_f32_16x16x32_bf16 v[28:31], v[44:47], v[236:239], v[28:31]\n"
        "v_cvt_pk_bf16_f32 v83, v74, v75\n"
        "v_and_b32_dpp v36, v87, v1 row_ror:12 row_mask:0xf bank_mask:0xf\n"
        "v_mfma_f32_16x16x32_bf16 v[28:31], v[50:53], v[240:243], v[28:31]\n"
        "s_setprio 3\n"
        "v_and_b32_dpp v37, v88, v1 row_ror:12 row_mask:0xf bank_mask:0xf\n"
        "v_cvt_pk_bf16_f32 v74, v16, v17\n"
        "v_cvt_pk_bf16_f32 v75, v18, v19\n"
        "v_mfma_f32_16x16x32_bf16 v[16:19], v[82:85], v[54:57], 0\n"
        "v_mov_b64 v[66:67], v[54:55]\n"
        "v_cvt_pk_bf16_f32 v86, v96, v97\n"
        "v_cvt_pk_bf16_f32 v87, v98, v99\n"
        "v_mfma_f32_16x16x32_bf16 v[108:111], v[74:77], v[36:39], 0\n"
        "v_mov_b64 v[48:49], v[36:37]\n"
        "v_cvt_pk_bf16_f32 v78, v100, v101\n"
        "v_mfma_f32_16x16x32_bf16 v[96:99], v[84:87], v[64:67], 0\n"
        "v_cvt_pk_bf16_f32 v79, v102, v103\n"
        "v_mov_b64 v[60:61], v[54:55]\n"
        "v_cvt_pk_bf16_f32 v26, v104, v105\n"
        "v_mfma_f32_16x16x32_bf16 v[100:103], v[76:79], v[46:49], 0\n"
        "v_cvt_pk_bf16_f32 v27, v106, v107\n"
        "v_mov_b64 v[42:43], v[36:37]\n"
        "v_mfma_f32_16x16x32_bf16 v[104:107], v[50:53], v[216:219], 0\n"
        "s_setprio 3\n"
        "v_cvt_pk_bf16_f32 v82, v20, v21\n"
        "v_cvt_pk_bf16_f32 v83, v22, v23\n"
        "v_mfma_f32_16x16x32_bf16 v[104:107], v[44:47], v[212:215], v[104:107]\n"
        "v_cvt_pk_bf16_f32 v22, v32, v33\n"
        "v_cvt_pk_bf16_f32 v23, v34, v35\n"
        "v_mfma_f32_16x16x32_bf16 v[104:107], v[38:41], v[208:211], v[104:107]\n"
        "s_setprio 3\n"
        "v_cvt_pk_bf16_f32 v78, v28, v29\n"
        "v_cvt_pk_bf16_f32 v79, v30, v31\n"
        "v_mfma_f32_16x16x32_bf16 v[28:31], v[68:71], v[216:219], 0\n"
        "s_setprio 3\n"
        "v_mov_b64 v[72:73], v[54:55]\n"
        "v_mov_b64 v[54:55], v[36:37]\n"
        "v_mfma_f32_16x16x32_bf16 v[28:31], v[62:65], v[212:215], v[28:31]\n"
        "v_cvt_pk_bf16_f32 v74, v104, v105\n"
        "v_cvt_pk_bf16_f32 v75, v106, v107\n"
        "v_mfma_f32_16x16x32_bf16 v[28:31], v[56:59], v[208:211], v[28:31]\n"
        "s_setprio 3\n"
        "v_cvt_pk_bf16_f32 v34, v16, v17\n"
        "v_cvt_pk_bf16_f32 v35, v18, v19\n"
        "v_mfma_f32_16x16x32_bf16 v[18:21], v[24:27], v[58:61], 0\n"
        "v_cvt_pk_bf16_f32 v36, v96, v97\n"
        "v_cvt_pk_bf16_f32 v37, v98, v99\n"
        "v_mfma_f32_16x16x32_bf16 v[104:107], v[80:83], v[40:43], 0\n"
        "v_cvt_pk_bf16_f32 v16, v108, v109\n"
        "v_cvt_pk_bf16_f32 v82, v28, v29\n"
        "v_mfma_f32_16x16x32_bf16 v[96:99], v[56:59], v[244:247], 0\n"
        "s_setprio 3\n"
        "v_cvt_pk_bf16_f32 v83, v30, v31\n"
        "v_cvt_pk_bf16_f32 v108, v18, v19\n"
        "v_mfma_f32_16x16x32_bf16 v[96:99], v[62:65], v[248:251], v[96:99]\n"
        "v_cvt_pk_bf16_f32 v109, v20, v21\n"
        "v_cvt_pk_bf16_f32 v17, v110, v111\n"
        "v_mfma_f32_16x16x32_bf16 v[96:99], v[68:71], v[252:255], v[96:99]\n"
        "s_setprio 3\n"
        "v_cvt_pk_bf16_f32 v18, v100, v101\n"
        "v_cvt_pk_bf16_f32 v19, v102, v103\n"
        "v_cvt_pk_bf16_f32 v20, v104, v105\n"
        "v_mfma_f32_16x16x32_bf16 v[100:103], v[38:41], v[244:247], 0\n"
        "s_setprio 3\n"
        "v_cvt_pk_bf16_f32 v21, v106, v107\n"
        "v_mfma_f32_16x16x32_bf16 v[100:103], v[44:47], v[248:251], v[100:103]\n"
        "v_cvt_pk_bf16_f32 v86, v96, v97\n"
        "v_cvt_pk_bf16_f32 v87, v98, v99\n"
        "v_mfma_f32_16x16x32_bf16 v[100:103], v[50:53], v[252:255], v[100:103]\n"
        "s_setprio 3\n"
        "v_mfma_f32_16x16x32_bf16 v[96:99], v[22:25], v[66:69], 0\n"
        "v_mfma_f32_16x16x32_bf16 v[26:29], v[78:81], v[48:51], 0\n"
        "v_mfma_f32_16x16x32_bf16 v[30:33], v[82:85], v[60:63], 0\n"
        "s_waitcnt vmcnt(0)\n"
        "ds_read_b128 v[56:59], v0\n"
        "ds_read_b128 v[38:41], v0 offset:768\n"
        "ds_read_b128 v[62:65], v0 offset:64\n"
        "v_cvt_pk_bf16_f32 v78, v100, v101\n"
        "v_mfma_f32_16x16x32_bf16 v[104:107], v[74:77], v[42:45], 0\n"
        "s_add_u32 s75, s75, 1\n"
        "ds_read_b128 v[44:47], v0 offset:832\n"
        "v_cvt_pk_bf16_f32 v79, v102, v103\n"
        "v_cvt_pk_bf16_f32 v110, v96, v97\n"
        "v_cvt_pk_bf16_f32 v111, v98, v99\n"
        "v_mfma_f32_16x16x32_bf16 v[100:103], v[84:87], v[70:73], 0\n"
        "s_cmp_lt_u32 s75, 4\n"
        "ds_read_b128 v[68:71], v0 offset:128\n"
        "v_cvt_pk_bf16_f32 v72, v30, v31\n"
        "v_cvt_pk_bf16_f32 v73, v32, v33\n"
        "v_cvt_pk_bf16_f32 v22, v26, v27\n"
        "v_mfma_f32_16x16x32_bf16 v[96:99], v[76:79], v[52:55], 0\n"
        "s_cbranch_scc1 B18_%=\n"
        "B19_%=:\n"
        "ds_read_b128 v[50:53], v0 offset:896\n"
        "v_cvt_pk_bf16_f32 v74, v100, v101\n"
        "v_cvt_pk_bf16_f32 v75, v102, v103\n"
        "v_cvt_pk_bf16_f32 v23, v28, v29\n"
        "v_mfma_f32_16x16x32_bf16 v[30:33], v[34:37], v[34:37], 0\n"
        "s_setprio 3\n"
        "v_cvt_pk_bf16_f32 v26, v104, v105\n"
        "v_cvt_pk_bf16_f32 v27, v106, v107\n"
        "v_mfma_f32_16x16x32_bf16 v[30:33], v[108:111], v[108:111], v[30:33]\n"
        "v_cvt_pk_bf16_f32 v28, v96, v97\n"
        "v_cvt_pk_bf16_f32 v29, v98, v99\n"
        "v_mfma_f32_16x16x32_bf16 v[30:33], v[72:75], v[72:75], v[30:33]\n"
        "s_setprio 3\n"
        "v_mfma_f32_16x16x32_bf16 v[104:107], v[16:19], v[16:19], 0\n"
        "s_setprio 3\n"
        "v_mfma_f32_16x16x32_bf16 v[104:107], v[20:23], v[20:23], v[104:107]\n"
        "v_mfma_f32_16x16x32_bf16 v[104:107], v[26:29], v[26:29], v[104:107]\n"
        "s_setprio 3\n"
        "s_cmp_lt_u32 s30, s65\n"
        "s_cbranch_scc1 PTOP_%=\n"
        "PEXIT_%=:\n"
        "s_waitcnt lgkmcnt(0)\n"
        "s_nop 15\n"
        "L20_%=:\n"
        "ds_read_b32 v255, v91 offset:8\n"
        "s_waitcnt lgkmcnt(0)\n"
        "v_readfirstlane_b32 s40, v255\n"
        "s_nop 3\n"
        "s_cmp_ge_i32 s40, s68\n"
        "s_cbranch_scc1 L21_%=\n"
        "s_sleep 1\n"
        "s_sub_u32 s41, s41, 1\n"
        "s_cbranch_scc0 L20_%=\n"
        "s_endpgm\n"
        "L21_%=:\n"
        "v_cndmask_b32_e64 v255, v30, v31, s[48:49]\n"
        "v_cndmask_b32_e64 v254, v104, v105, s[48:49]\n"
        "v_cndmask_b32_e64 v255, v255, v32, s[50:51]\n"
        "v_cndmask_b32_e64 v254, v254, v106, s[50:51]\n"
        "v_cndmask_b32_e64 v255, v255, v33, s[52:53]\n"
        "v_cndmask_b32_e64 v254, v254, v107, s[52:53]\n"
        "ds_write_b128 v11, v[34:37]\n"
        "ds_write_b128 v11, v[16:19] offset:832\n"
        "v_cndmask_b32_dpp v255, v254, v255, vcc row_ror:4 row_mask:0xf bank_mask:0xf\n"
        "ds_write_b128 v11, v[108:111] offset:64\n"
        "ds_write_b128 v11, v[20:23] offset:896\n"
        "ds_write_b128 v11, v[72:75] offset:128\n"
        "ds_write_b128 v11, v[26:29] offset:960\n"
        "s_mov_b64 exec, s[46:47]\n"
        "ds_write_b32 v12, v255\n"
        "s_mov_b64 exec, -1\n"
        "s_mov_b64 exec, 1\n"
        "ds_inc_u32 v91, v10 offset:0\n"
        "s_mov_b64 exec, -1\n"
        "s_or_b32 s40, s18, s2\n"
        "s_cbranch_scc1 L30_%=\n"
        "L29_%=:\n"
        "s_mov_b64 exec, 1\n"
        "v_mov_b32 v20, 0\n"
        "v_mov_b32 v21, 1\n"
        "global_atomic_add v22, v20, v21, s[24:25] offset:256 sc0\n"
        "s_mov_b64 exec, -1\n"
        "s_mov_b64 exec, 1\n"
        "v_mov_b32 v20, 0\n"
        "v_mov_b32 v21, 0\n"
        "global_atomic_add v22, v20, v21, s[24:25] offset:0 sc0\n"
        "s_mov_b64 exec, -1\n"
        "s_waitcnt vmcnt(0)\n"
        "v_readfirstlane_b32 s40, v22\n"
        "s_nop 3\n"
        "s_cmp_ge_u32 s40, 512\n"
        "s_cbranch_scc1 L30_%=\n"
        "s_sleep 8\n"
        "s_sub_u32 s41, s41, 64\n"
        "s_cbranch_scc0 L29_%=\n"
        "L30_%=:\n"
        "s_endpgm\n"
        "L3_%=:\n"
        "s_nop 7\n"
        "L1_%=:\n"
        "ds_read_b32 v88, v91 offset:8\n"
        "s_waitcnt lgkmcnt(0)\n"
        "v_readfirstlane_b32 s40, v88\n"
        "s_nop 3\n"
        "s_cmp_ge_i32 s40, s68\n"
        "s_cbranch_scc1 L2_%=\n"
        "s_sleep 1\n"
        "s_sub_u32 s41, s41, 1\n"
        "s_cbranch_scc0 L1_%=\n"
        "s_endpgm\n"
        "L2_%=:\n"
        "s_nop 1\n"
        "s_branch L4_%=\n"
        "L7_%=:\n"
        "s_nop 7\n"
        "ds_read_u16 v11, v13 offset:19564\n"
        "ds_read_u16 v12, v13 offset:19566\n"
        "s_cmp_ge_u32 s30, 10\n"
        "s_cselect_b32 s69, 10, 0\n"
        "s_cmp_ge_u32 s30, 20\n"
        "s_cselect_b32 s69, 20, s69\n"
        "s_sub_u32 s69, s30, s69\n"
        "s_mul_i32 s69, s69, 832\n"
        "s_waitcnt lgkmcnt(0)\n"
        "v_add_u32 v11, s69, v11\n"
        "v_subrev_u32 v82, 8320, v11\n"
        "v_min_u32 v11, v11, v82\n"
        "v_add_u32 v11, 101856, v11\n"
        "v_add_u32 v12, s69, v12\n"
        "v_subrev_u32 v82, 8320, v12\n"
        "v_min_u32 v12, v12, v82\n"
        "v_add_u32 v12, 101856, v12\n"
        "s_nop 1\n"
        "s_branch L8_%=\n"
        "L9_%=:\n"
        "s_nop 7\n"
        "v_mov_b32 v82, 21600\n"
        "L5_%=:\n"
        "ds_read_b32 v83, v82 offset:64\n"
        "s_waitcnt lgkmcnt(0)\n"
        "v_readfirstlane_b32 s40, v83\n"
        "s_nop 3\n"
        "s_cmp_ge_i32 s40, 1\n"
        "s_cbranch_scc1 L6_%=\n"
        "s_sleep 1\n"
        "s_sub_u32 s41, s41, 1\n"
        "s_cbranch_scc0 L5_%=\n"
        "s_endpgm\n"
        "L6_%=:\n"
        "v_mbcnt_lo_u32_b32 v83, -1, 0\n"
        "v_lshl_add_u32 v82, v83, 2, v82\n"
        "ds_read_b32 v83, v82 offset:68\n"
        "s_mov_b32 s14, s40\n"
        "s_mov_b32 s76, -1\n"
        "s_waitcnt lgkmcnt(0)\n"
        "v_readlane_b32 s22, v83, 0\n"
        "v_readlane_b32 s23, v83, 1\n"
        "s_nop 3\n"
        "s_nop 1\n"
        "s_branch L10_%=\n"
        "L11_%=:\n"
        "s_nop 7\n"
        "ds_read_b32 v15, v13 offset:19552\n"
        "ds_read_b32 v89, v13 offset:19556\n"
        "ds_read_b32 v90, v13 offset:19560\n"
        "s_mov_b32 s35, 6\n"
        "s_mov_b32 s38, 0\n"
        "s_mov_b32 s69, 1344\n"
        "s_waitcnt lgkmcnt(0)\n"
        "v_lshrrev_b32 v82, 28, v15\n"
        "v_add_u32 v82, s34, v82\n"
        "v_and_b32 v15, 0x0fffffff, v15\n"
        "v_mad_u32_u24 v15, v82, s69, v15\n"
        "v_mov_b32 v83, s15\n"
        "v_cmpx_le_u32 vcc, s72, v82\n"
        "v_mov_b32 v83, s71\n"
        "s_mov_b64 exec, -1\n"
        "v_cmpx_le_u32 vcc, 20, v82\n"
        "v_mov_b32 v83, s22\n"
        "s_mov_b64 exec, -1\n"
        "v_cmpx_le_u32 vcc, 21, v82\n"
        "v_mov_b32 v83, s23\n"
        "s_mov_b64 exec, -1\n"
        "v_add_u32 v15, v15, v83\n"
        "v_lshrrev_b32 v82, 28, v89\n"
        "v_add_u32 v82, s34, v82\n"
        "v_and_b32 v89, 0x0fffffff, v89\n"
        "v_mad_u32_u24 v89, v82, s69, v89\n"
        "v_mov_b32 v83, s15\n"
        "v_cmpx_le_u32 vcc, s72, v82\n"
        "v_mov_b32 v83, s71\n"
        "s_mov_b64 exec, -1\n"
        "v_cmpx_le_u32 vcc, 20, v82\n"
        "v_mov_b32 v83, s22\n"
        "s_mov_b64 exec, -1\n"
        "v_cmpx_le_u32 vcc, 21, v82\n"
        "v_mov_b32 v83, s23\n"
        "s_mov_b64 exec, -1\n"
        "v_add_u32 v89, v89, v83\n"
        "v_lshrrev_b32 v82, 28, v90\n"
        "v_add_u32 v82, s34, v82\n"
        "v_and_b32 v90, 0x0fffffff, v90\n"
        "v_mad_u32_u24 v90, v82, s69, v90\n"
        "v_mov_b32 v83, s15\n"
        "v_cmpx_le_u32 vcc, s72, v82\n"
        "v_mov_b32 v83, s71\n"
        "s_mov_b64 exec, -1\n"
        "v_cmpx_le_u32 vcc, 20, v82\n"
        "v_mov_b32 v83, s22\n"
        "s_mov_b64 exec, -1\n"
        "v_cmpx_le_u32 vcc, 21, v82\n"
        "v_mov_b32 v83, s23\n"
        "s_mov_b64 exec, -1\n"
        "v_add_u32 v90, v90, v83\n"
        "s_nop 4\n"
        "s_mov_b64 vcc, s[44:45]\n"
        "s_nop 1\n"
        "s_branch L12_%=\n"
        "L18_%=:\n"
        "s_nop 7\n"
        "v_readfirstlane_b32 s62, v14\n"
        "s_cmp_eq_u32 s75, 1\n"
        "s_cbranch_scc1 L13_%=\n"
        "s_cmp_ge_u32 s75, 2\n"
        "s_cbranch_scc1 L14_%=\n"
        "s_mov_b32 s27, 20\n"
        "s_cmp_lg_u32 s2, 0\n"
        "s_cbranch_scc1 L17_%=\n"
        "s_not_b32 s26, s26\n"
        "s_mov_b64 exec, 1\n"
        "v_mov_b32 v82, 0\n"
        "v_mov_b32 v83, 1\n"
        "global_atomic_add v14, v82, v83, s[24:25] offset:256 sc0\n"
        "s_mov_b64 exec, -1\n"
        "s_branch L15_%=\n"
        "L17_%=:\n"
        "s_mov_b64 exec, 1\n"
        "v_mov_b32 v82, 0\n"
        "global_load_dword v14, v82, s[24:25] offset:256 sc1\n"
        "s_mov_b64 exec, -1\n"
        "s_branch L15_%=\n"
        "L13_%=:\n"
        "s_cmp_lg_u32 s62, s26\n"
        "s_cbranch_scc1 L16_%=\n"
        "s_sleep 8\n"
        "s_mov_b64 exec, 1\n"
        "v_mov_b32 v82, 0\n"
        "global_load_dword v14, v82, s[24:25] offset:256 sc1\n"
        "s_mov_b64 exec, -1\n"
        "s_waitcnt vmcnt(0)\n"
        "v_readfirstlane_b32 s62, v14\n"
        "s_sub_u32 s41, s41, 64\n"
        "s_cbranch_scc0 L13_%=\n"
        "s_endpgm\n"
        "L14_%=:\n"
        "s_cmp_lt_u32 s62, 209\n"
        "s_addc_u32 s27, s27, 0\n"
        "s_min_u32 s62, s62, 208\n"
        "s_add_u32 s62, s62, 5120\n"
        "s_mul_i32 s64, s62, 57457\n"
        "s_lshr_b32 s64, s64, 22\n"
        "s_sub_u32 s62, s62, 18\n"
        "s_sub_u32 s62, s62, s75\n"
        "s_mul_i32 s62, s62, 1344\n"
        "s_mul_i32 s64, s64, 588672\n"
        "s_add_u32 s62, s62, s64\n"
        "s_lshl2_add_u32 s64, s75, 21600\n"
        "v_mov_b32 v82, s64\n"
        "v_mov_b32 v83, s62\n"
        "ds_write_b32 v82, v83 offset:60\n"
        "s_waitcnt lgkmcnt(0)\n"
        "s_cmp_lt_u32 s75, 3\n"
        "s_cbranch_scc1 L16_%=\n"
        "v_mov_b32 v83, s27\n"
        "ds_write_b32 v82, v83 offset:52\n"
        "s_waitcnt lgkmcnt(0)\n"
        "s_branch L15_%=\n"
        "L16_%=:\n"
        "s_mov_b64 exec, 1\n"
        "v_mov_b32 v82, 0\n"
        "v_mov_b32 v83, 1\n"
        "global_atomic_add v14, v82, v83, s[24:25] offset:0 sc0\n"
        "s_mov_b64 exec, -1\n"
        "L15_%=:\n"
        "s_nop 1\n"
        "s_branch L19_%=\n"
        "B3_%=:\n"
        "s_nop 7\n"
        "B1_%=:\n"
        "ds_read_b32 v88, v91 offset:8\n"
        "s_waitcnt lgkmcnt(0)\n"
        "v_readfirstlane_b32 s40, v88\n"
        "s_nop 3\n"
        "s_cmp_ge_i32 s40, s68\n"
        "s_cbranch_scc1 B2_%=\n"
        "s_sleep 1\n"
        "s_sub_u32 s41, s41, 1\n"
        "s_cbranch_scc0 B1_%=\n"
        "s_endpgm\n"
        "B2_%=:\n"
        "s_nop 1\n"
        "s_branch B4_%=\n"
        "B7_%=:\n"
        "s_nop 7\n"
        "ds_read_u16 v11, v13 offset:19564\n"
        "ds_read_u16 v12, v13 offset:19566\n"
        "s_cmp_ge_u32 s30, 10\n"
        "s_cselect_b32 s69, 10, 0\n"
        "s_cmp_ge_u32 s30, 20\n"
        "s_cselect_b32 s69, 20, s69\n"
        "s_sub_u32 s69, s30, s69\n"
        "s_mul_i32 s69, s69, 832\n"
        "s_waitcnt lgkmcnt(0)\n"
        "v_add_u32 v11, s69, v11\n"
        "v_subrev_u32 v82, 8320, v11\n"
        "v_min_u32 v11, v11, v82\n"
        "v_add_u32 v11, 101856, v11\n"
        "v_add_u32 v12, s69, v12\n"
        "v_subrev_u32 v82, 8320, v12\n"
        "v_min_u32 v12, v12, v82\n"
        "v_add_u32 v12, 101856, v12\n"
        "s_nop 1\n"
        "s_branch B8_%=\n"
        "B9_%=:\n"
        "s_nop 7\n"
        "v_mov_b32 v82, 21600\n"
        "B5_%=:\n"
        "ds_read_b32 v83, v82 offset:64\n"
        "s_waitcnt lgkmcnt(0)\n"
        "v_readfirstlane_b32 s40, v83\n"
        "s_nop 3\n"
        "s_cmp_ge_i32 s40, 1\n"
        "s_cbranch_scc1 B6_%=\n"
        "s_sleep 1\n"
        "s_sub_u32 s41, s41, 1\n"
        "s_cbranch_scc0 B5_%=\n"
        "s_endpgm\n"
        "B6_%=:\n"
        "v_mbcnt_lo_u32_b32 v83, -1, 0\n"
        "v_lshl_add_u32 v82, v83, 2, v82\n"
        "ds_read_b32 v83, v82 offset:68\n"
        "s_mov_b32 s14, s40\n"
        "s_mov_b32 s76, -1\n"
        "s_waitcnt lgkmcnt(0)\n"
        "v_readlane_b32 s22, v83, 0\n"
        "v_readlane_b32 s23, v83, 1\n"
        "s_nop 3\n"
        "s_nop 1\n"
        "s_branch B10_%=\n"
        "B11_%=:\n"
        "s_nop 7\n"
        "ds_read_b32 v15, v13 offset:19552\n"
        "ds_read_b32 v89, v13 offset:19556\n"
        "ds_read_b32 v90, v13 offset:19560\n"
        "s_mov_b32 s35, 6\n"
        "s_mov_b32 s38, 0\n"
        "s_mov_b32 s69, 1344\n"
        "s_waitcnt lgkmcnt(0)\n"
        "v_lshrrev_b32 v82, 28, v15\n"
        "v_add_u32 v82, s34, v82\n"
        "v_and_b32 v15, 0x0fffffff, v15\n"
        "v_mad_u32_u24 v15, v82, s69, v15\n"
        "v_mov_b32 v83, s15\n"
        "v_cmpx_le_u32 vcc, s72, v82\n"
        "v_mov_b32 v83, s71\n"
        "s_mov_b64 exec, -1\n"
        "v_cmpx_le_u32 vcc, 20, v82\n"
        "v_mov_b32 v83, s22\n"
        "s_mov_b64 exec, -1\n"
        "v_cmpx_le_u32 vcc, 21, v82\n"
        "v_mov_b32 v83, s23\n"
        "s_mov_b64 exec, -1\n"
        "v_add_u32 v15, v15, v83\n"
        "v_lshrrev_b32 v82, 28, v89\n"
        "v_add_u32 v82, s34, v82\n"
        "v_and_b32 v89, 0x0fffffff, v89\n"
        "v_mad_u32_u24 v89, v82, s69, v89\n"
        "v_mov_b32 v83, s15\n"
        "v_cmpx_le_u32 vcc, s72, v82\n"
        "v_mov_b32 v83, s71\n"
        "s_mov_b64 exec, -1\n"
        "v_cmpx_le_u32 vcc, 20, v82\n"
        "v_mov_b32 v83, s22\n"
        "s_mov_b64 exec, -1\n"
        "v_cmpx_le_u32 vcc, 21, v82\n"
        "v_mov_b32 v83, s23\n"
        "s_mov_b64 exec, -1\n"
        "v_add_u32 v89, v89, v83\n"
        "v_lshrrev_b32 v82, 28, v90\n"
        "v_add_u32 v82, s34, v82\n"
        "v_and_b32 v90, 0x0fffffff, v90\n"
        "v_mad_u32_u24 v90, v82, s69, v90\n"
        "v_mov_b32 v83, s15\n"
        "v_cmpx_le_u32 vcc, s72, v82\n"
        "v_mov_b32 v83, s71\n"
        "s_mov_b64 exec, -1\n"
        "v_cmpx_le_u32 vcc, 20, v82\n"
        "v_mov_b32 v83, s22\n"
        "s_mov_b64 exec, -1\n"
        "v_cmpx_le_u32 vcc, 21, v82\n"
        "v_mov_b32 v83, s23\n"
        "s_mov_b64 exec, -1\n"
        "v_add_u32 v90, v90, v83\n"
        "s_nop 4\n"
        "s_mov_b64 vcc, s[44:45]\n"
        "s_nop 1\n"
        "s_branch B12_%=\n"
        "B18_%=:\n"
        "s_nop 7\n"
        "v_readfirstlane_b32 s62, v14\n"
        "s_cmp_eq_u32 s75, 1\n"
        "s_cbranch_scc1 B13_%=\n"
        "s_cmp_ge_u32 s75, 2\n"
        "s_cbranch_scc1 B14_%=\n"
        "s_mov_b32 s27, 20\n"
        "s_cmp_lg_u32 s2, 0\n"
        "s_cbranch_scc1 B17_%=\n"
        "s_not_b32 s26, s26\n"
        "s_mov_b64 exec, 1\n"
        "v_mov_b32 v82, 0\n"
        "v_mov_b32 v83, 1\n"
        "global_atomic_add v14, v82, v83, s[24:25] offset:256 sc0\n"
        "s_mov_b64 exec, -1\n"
        "s_branch B15_%=\n"
        "B17_%=:\n"
        "s_mov_b64 exec, 1\n"
        "v_mov_b32 v82, 0\n"
        "global_load_dword v14, v82, s[24:25] offset:256 sc1\n"
        "s_mov_b64 exec, -1\n"
        "s_branch B15_%=\n"
        "B13_%=:\n"
        "s_cmp_lg_u32 s62, s26\n"
        "s_cbranch_scc1 B16_%=\n"
        "s_sleep 8\n"
        "s_mov_b64 exec, 1\n"
        "v_mov_b32 v82, 0\n"
        "global_load_dword v14, v82, s[24:25] offset:256 sc1\n"
        "s_mov_b64 exec, -1\n"
        "s_waitcnt vmcnt(0)\n"
        "v_readfirstlane_b32 s62, v14\n"
        "s_sub_u32 s41, s41, 64\n"
        "s_cbranch_scc0 B13_%=\n"
        "s_endpgm\n"
        "B14_%=:\n"
        "s_cmp_lt_u32 s62, 209\n"
        "s_addc_u32 s27, s27, 0\n"
        "s_min_u32 s62, s62, 208\n"
        "s_add_u32 s62, s62, 5120\n"
        "s_mul_i32 s64, s62, 57457\n"
        "s_lshr_b32 s64, s64, 22\n"
        "s_sub_u32 s62, s62, 18\n"
        "s_sub_u32 s62, s62, s75\n"
        "s_mul_i32 s62, s62, 1344\n"
        "s_mul_i32 s64, s64, 588672\n"
        "s_add_u32 s62, s62, s64\n"
        "s_lshl2_add_u32 s64, s75, 21600\n"
        "v_mov_b32 v82, s64\n"
        "v_mov_b32 v83, s62\n"
        "ds_write_b32 v82, v83 offset:60\n"
        "s_waitcnt lgkmcnt(0)\n"
        "s_cmp_lt_u32 s75, 3\n"
        "s_cbranch_scc1 B16_%=\n"
        "v_mov_b32 v83, s27\n"
        "ds_write_b32 v82, v83 offset:52\n"
        "s_waitcnt lgkmcnt(0)\n"
        "s_branch B15_%=\n"
        "B16_%=:\n"
        "s_mov_b64 exec, 1\n"
        "v_mov_b32 v82, 0\n"
        "v_mov_b32 v83, 1\n"
        "global_atomic_add v14, v82, v83, s[24:25] offset:0 sc0\n"
        "s_mov_b64 exec, -1\n"
        "B15_%=:\n"
        "s_nop 1\n"
        "s_branch B19_%=\n"
        "CONS_%=:\n"
        "s_mov_b32 s41, 0x400000\n"
        "s_mov_b32 s48, 0xffffffff\n"
        "s_mov_b32 s49, 0xffff0000\n"
        "s_mov_b32 s50, 0x33333333\n"
        "s_mov_b32 s51, 0x33333333\n"
        "s_mov_b32 s52, 0x3c2aaaab\n"
        "s_mov_b32 s53, 0x3a7ba882\n"
        "s_mov_b32 s56, 0x0\n"
        "s_mov_b32 s57, 0xfeff0000\n"
        "s_mov_b32 s62, 0x11111111\n"
        "s_mov_b32 s63, 0x11111111\n"
        "s_mov_b64 s[44:45], s[12:13]\n"
        "s_and_b32 s45, s45, 0xffff\n"
        "s_mov_b32 s47, 0x20000\n"
        "s_mov_b32 s75, 832\n"
        "s_mov_b32 s76, 17\n"
        "s_sub_u32 s73, s18, 7488\n"
        "s_mov_b32 s28, 0xffff0000\n"
        "s_mov_b32 s31, 0\n"
        "s_add_u32 s72, s15, s17\n"
        "s_add_u32 s70, s72, 588672\n"
        "s_sub_u32 s71, 73, s16\n"
        "s_mov_b32 s35, s72\n"
        "s_mov_b32 s36, 0x20000000\n"
        "L55_%=:\n"
        "ds_read_b32 v251, v250 offset:52\n"
        "s_waitcnt lgkmcnt(0)\n"
        "v_readfirstlane_b32 s40, v251\n"
        "s_nop 3\n"
        "s_cmp_ge_i32 s40, 6\n"
        "s_cbranch_scc1 L56_%=\n"
        "s_nop 0\n"
        "s_sub_u32 s41, s41, 1\n"
        "s_cbranch_scc0 L55_%=\n"
        "s_endpgm\n"
        "L56_%=:\n"
        "s_waitcnt vmcnt(0)\n"
        "s_setprio 0\n"
        "v_add_u32 v20, 2, v1\n"
        "v_mov_b32 v21, 0x3f80\n"
        "s_mov_b32 exec_lo, 0\n"
        "s_mov_b32 exec_hi, 0x1000000\n"
        "ds_write_b16 v20, v21\n"
        "s_mov_b64 exec, -1\n"
        "v_add_u32 v20, 61792, v0\n"
        "ds_read_b128 v[168:171], v20 offset:0\n"
        "ds_read_b128 v[172:175], v20 offset:1024\n"
        "ds_read_b128 v[176:179], v20 offset:2048\n"
        "ds_read_b128 v[180:183], v20 offset:3072\n"
        "ds_read_b128 v[184:187], v20 offset:4096\n"
        "ds_read_b128 v[188:191], v20 offset:5120\n"
        "ds_read_b128 v[192:195], v20 offset:6144\n"
        "ds_read_b128 v[196:199], v20 offset:7168\n"
        "ds_read_b128 v[200:203], v20 offset:8192\n"
        "ds_read_b128 v[204:207], v20 offset:9216\n"
        "ds_read_b128 v[208:211], v20 offset:10240\n"
        "ds_read_b128 v[212:215], v20 offset:11264\n"
        "ds_read_b128 v[216:219], v20 offset:12288\n"
        "ds_read_b128 v[220:223], v20 offset:13312\n"
        "ds_read_b128 v[224:227], v20 offset:14336\n"
        "ds_read_b128 v[228:231], v20 offset:15360\n"
        "ds_read_b128 v[232:235], v20 offset:16384\n"
        "ds_read_b128 v[236:239], v20 offset:17408\n"
        "ds_read_b128 v[240:243], v0 offset:0\n"
        "ds_read_b128 v[244:247], v0 offset:1024\n"
        "ds_read_b128 v[248:251], v0 offset:2048\n"
        "ds_read_b128 v[252:255], v0 offset:3088\n"
        "v_mov_b32 v21, 21600\n"
        "s_waitcnt lgkmcnt(0)\n"
        "s_mov_b64 exec, 1\n"
        "ds_inc_u32 v21, v1 offset:12\n"
        "s_mov_b64 exec, -1\n"
        "L57_%=:\n"
        "ds_read_b32 v20, v21 offset:0\n"
        "s_waitcnt lgkmcnt(0)\n"
        "v_readfirstlane_b32 s40, v20\n"
        "s_nop 3\n"
        "s_cmp_ge_i32 s40, 11\n"
        "s_cbranch_scc1 L58_%=\n"
        "s_sleep 1\n"
        "s_sub_u32 s41, s41, 1\n"
        "s_cbranch_scc0 L57_%=\n"
        "s_endpgm\n"
        "L58_%=:\n"
        "ds_read_b128 v[88:91], v7 offset:0\n"
        "ds_read_b128 v[76:79], v7 offset:3328\n"
        "ds_read_b128 v[92:95], v7 offset:64\n"
        "ds_read_b128 v[80:83], v7 offset:3392\n"
        "ds_read_b128 v[100:103], v7 offset:128\n"
        "ds_read_b128 v[84:87], v7 offset:3456\n"
        "s_waitcnt lgkmcnt(0)\n"
        "CFULL_%=:\n"
        "s_call_b64 s[64:65], CGRP_%=\n"
        "CTOP_%=:\n"
        "s_waitcnt lgkmcnt(5)\n"
        "v_mfma_f32_16x16x32_bf16 v[48:51], v[168:171], v[88:91], 0\n"
        "v_cvt_pk_bf16_f32 v144, v140, v164\n"
        "v_cvt_pk_bf16_f32 v145, v108, v68\n"
        "s_waitcnt lgkmcnt(3)\n"
        "v_mfma_f32_16x16x32_bf16 v[48:51], v[172:175], v[92:95], v[48:51]\n"
        "ds_read_b128 v[24:27], v16 offset:128\n"
        "v_cvt_pk_bf16_f32 v146, v148, v72\n"
        "s_waitcnt lgkmcnt(2)\n"
        "v_mfma_f32_16x16x32_bf16 v[48:51], v[176:179], v[100:103], v[48:51]\n"
        "ds_read_b128 v[32:35], v5\n"
        "v_cvt_pk_bf16_f32 v136, v141, v165\n"
        "v_mfma_f32_16x16x32_bf16 v[20:23], v[188:191], v[100:103], 0\n"
        "v_cvt_pk_bf16_f32 v137, v109, v69\n"
        "v_cvt_pk_bf16_f32 v138, v149, v73\n"
        "v_mfma_f32_16x16x32_bf16 v[20:23], v[184:187], v[92:95], v[20:23]\n"
        "s_setprio 2\n"
        "v_cvt_pk_bf16_f32 v160, v142, v166\n"
        "v_cvt_pk_bf16_f32 v124, v48, v49\n"
        "v_mfma_f32_16x16x32_bf16 v[20:23], v[180:183], v[88:91], v[20:23]\n"
        "s_add_u32 s37, s36, 196224\n"
        "ds_read_b128 v[36:39], v7 offset:6656\n"
        "ds_read_b128 v[40:43], v7 offset:6720\n"
        "ds_read_b128 v[132:135], v7 offset:6784\n"
        "v_mfma_f32_16x16x32_bf16 v[44:47], v[192:195], v[88:91], 0\n"
        "v_cvt_pk_bf16_f32 v125, v50, v51\n"
        "v_cvt_pk_bf16_f32 v161, v110, v70\n"
        "v_mfma_f32_16x16x32_bf16 v[44:47], v[196:199], v[92:95], v[44:47]\n"
        "v_cvt_pk_bf16_f32 v126, v20, v21\n"
        "v_cvt_pk_bf16_f32 v127, v22, v23\n"
        "v_mfma_f32_16x16x32_bf16 v[44:47], v[200:203], v[100:103], v[44:47]\n"
        "buffer_store_dwordx3 v[144:146], v12, s[8:11], s37 offen offset:384 sc0 sc1 nt\n"
        "v_cvt_pk_bf16_f32 v162, v150, v74\n"
        "v_cvt_pk_bf16_f32 v140, v143, v167\n"
        "v_cvt_pk_bf16_f32 v141, v111, v71\n"
        "v_cvt_pk_bf16_f32 v142, v151, v75\n"
        "s_waitcnt lgkmcnt(3)\n"
        "v_mfma_f32_16x16x32_bf16 v[20:23], v[24:27], v[100:103], 0\n"
        "v_cvt_pk_bf16_f32 v164, v116, v28\n"
        "v_cvt_pk_bf16_f32 v128, v44, v45\n"
        "v_smfmac_f32_16x16x64_bf16 v[20:23], v[32:35], v[88:95], v4\n"
        "s_add_u32 s37, s36, 196224\n"
        "v_cvt_pk_bf16_f32 v129, v46, v47\n"
        "v_mfma_f32_16x16x32_bf16 v[48:51], v[24:27], v[84:87], 0\n"
        "v_cvt_pk_bf16_f32 v165, v112, v96\n"
        "v_cvt_pk_bf16_f32 v166, v52, v56\n"
        "v_smfmac_f32_16x16x64_bf16 v[48:51], v[32:35], v[76:83], v4\n"
        "s_waitcnt lgkmcnt(0)\n"
        "v_cvt_pk_bf16_f32 v144, v117, v29\n"
        "v_cvt_pk_bf16_f32 v145, v113, v97\n"
        "v_mfma_f32_16x16x32_bf16 v[60:63], v[24:27], v[132:135], 0\n"
        "v_cvt_pk_bf16_f32 v146, v53, v57\n"
        "v_cvt_pk_bf16_f32 v148, v118, v30\n"
        "v_smfmac_f32_16x16x64_bf16 v[60:63], v[32:35], v[36:43], v4\n"
        "buffer_store_dwordx3 v[136:138], v13, s[8:11], s37 offen offset:1152 sc0 sc1 nt\n"
        "v_cvt_pk_bf16_f32 v149, v114, v98\n"
        "v_cvt_pk_bf16_f32 v150, v54, v58\n"
        "v_cvt_pk_bf16_f32 v136, v119, v31\n"
        "v_cvt_pk_bf16_f32 v137, v115, v99\n"
        "v_cvt_pk_bf16_f32 v138, v55, v59\n"
        "v_mfma_f32_16x16x32_bf16 v[32:35], v[212:215], v[100:103], 0\n"
        "s_add_u32 s37, s36, 294336\n"
        "ds_read_b64 v[68:69], v11 offset:192\n"
        "v_mfma_f32_16x16x32_bf16 v[32:35], v[208:211], v[92:95], v[32:35]\n"
        "ds_read_b32 v123, v10\n"
        "v_mfma_f32_16x16x32_bf16 v[32:35], v[204:207], v[88:91], v[32:35]\n"
        "v_mfma_f32_16x16x32_bf16 v[24:27], v[216:219], v[88:91], 0\n"
        "v_mfma_f32_16x16x32_bf16 v[24:27], v[220:223], v[92:95], v[24:27]\n"
        "v_mfma_f32_16x16x32_bf16 v[24:27], v[224:227], v[100:103], v[24:27]\n"
        "buffer_store_dwordx3 v[160:162], v12, s[8:11], s37 offen offset:576 sc0 sc1 nt\n"
        "s_add_u32 s37, s36, 392448\n"
        "s_waitcnt lgkmcnt(1)\n"
        "v_add_f32_dpp v69, v22, v23 quad_perm:[0,1,2,3] row_mask:0x1 bank_mask:0xf\n"
        "v_add_f32_dpp v68, v20, v21 quad_perm:[0,1,2,3] row_mask:0x1 bank_mask:0xf\n"
        "v_cvt_pk_bf16_f32 v130, v32, v33\n"
        "v_mfma_f32_16x16x32_bf16 v[20:23], v[236:239], v[100:103], 0\n"
        "v_cvt_pk_bf16_f32 v131, v34, v35\n"
        "v_cvt_pk_bf16_f32 v56, v24, v25\n"
        "v_mfma_f32_16x16x32_bf16 v[20:23], v[232:235], v[92:95], v[20:23]\n"
        "v_cvt_pk_bf16_f32 v57, v26, v27\n"
        "v_add_f32_dpp v69, v50, v51 quad_perm:[0,1,2,3] row_mask:0x2 bank_mask:0xf\n"
        "v_mfma_f32_16x16x32_bf16 v[20:23], v[228:231], v[88:91], v[20:23]\n"
        "buffer_store_dwordx3 v[140:142], v12, s[8:11], s37 offen sc0 sc1 nt\n"
        "v_add_f32_dpp v68, v48, v49 quad_perm:[0,1,2,3] row_mask:0x2 bank_mask:0xf\n"
        "v_add_f32_dpp v69, v62, v63 quad_perm:[0,1,2,3] row_mask:0x4 bank_mask:0xf\n"
        "v_mul_f32 v167, s52, v69\n"
        "v_add_f32_dpp v68, v60, v61 quad_perm:[0,1,2,3] row_mask:0x4 bank_mask:0xf\n"
        "s_waitcnt lgkmcnt(0)\n"
        "v_fma_f32 v167, -v167, v69, v123\n"
        "v_add_f32_e64 v167, s53, |v167|\n"
        "v_cvt_pk_bf16_f32 v59, v22, v23\n"
        "v_cvt_pk_bf16_f32 v58, v20, v21\n"
        "v_mfma_f32_16x16x32_bf16 v[140:143], v[88:91], v[124:127], 0\n"
        "v_rsq_f32 v123, v167\n"
        "v_mfma_f32_16x16x32_bf16 v[140:143], v[92:95], v[128:131], v[140:143]\n"
        "s_add_u32 s37, s36, 392448\n"
        "ds_read_b128 v[116:119], v8 offset:128\n"
        "v_and_b32 v30, s28, v123\n"
        "v_mfma_f32_16x16x32_bf16 v[140:143], v[100:103], v[56:59], v[140:143]\n"
        "ds_read2_b32 v[48:49], v6 offset0:0 offset1:2\n"
        "ds_read2_b32 v[50:51], v6 offset0:8 offset1:10\n"
        "v_mfma_f32_16x16x32_bf16 v[44:47], v[84:87], v[56:59], 0\n"
        "v_and_b32 v28, s28, v123\n"
        "v_sub_f32 v167, v123, v30\n"
        "v_mfma_f32_16x16x32_bf16 v[44:47], v[80:83], v[128:131], v[44:47]\n"
        "v_cvt_pk_bf16_f32 v31, v167, v30\n"
        "v_cvt_pk_bf16_f32 v29, v28, v167\n"
        "v_mfma_f32_16x16x32_bf16 v[44:47], v[76:79], v[124:127], v[44:47]\n"
        "buffer_store_dwordx3 v[164:166], v15, s[8:11], s37 offen offset:768 sc0 sc1 nt\n"
        "v_mul_f32 v68, v68, v123\n"
        "v_readlane_b32 s58, v123, 56\n"
        "s_mov_b64 exec, s[56:57]\n"
        "v_mov_b32 v68, 0xff800000\n"
        "s_mov_b64 exec, -1\n"
        "v_mfma_f32_16x16x4_4b_bf16 v[152:167], v[30:31], v[28:29], 0 blgp:4\n"
        "v_mul_f32 v68, s58, v68\n"
        "v_exp_f32 v68, v68\n"
        "v_mfma_f32_16x16x32_bf16 v[96:99], v[36:39], v[124:127], 0\n"
        "s_add_u32 s37, s36, 490560\n"
        "v_add_f32_dpp v139, v68, v68 row_ror:8 row_mask:0xf bank_mask:0xf\n"
        "v_mul_f32 v68, v68, v123\n"
        "v_mfma_f32_16x16x32_bf16 v[96:99], v[40:43], v[128:131], v[96:99]\n"
        "v_add_f32_dpp v139, v139, v139 row_ror:4 row_mask:0xf bank_mask:0xf\n"
        "v_mfma_f32_16x16x32_bf16 v[96:99], v[132:135], v[56:59], v[96:99]\n"
        "s_waitcnt lgkmcnt(0)\n"
        "v_add_f32_dpp v139, v139, v139 row_ror:2 row_mask:0xf bank_mask:0xf\n"
        "v_mfma_f32_16x16x32_bf16 v[112:115], v[116:119], v[56:59], 0\n"
        "v_mul_f32 v140, v140, v152\n"
        "v_mul_f32 v141, v141, v153\n"
        "v_smfmac_f32_16x16x64_bf16 v[112:115], v[48:51], v[124:131], v4\n"
        "buffer_store_dwordx3 v[144:146], v12, s[8:11], s37 offen offset:192 sc0 sc1 nt\n"
        "v_mul_f32 v142, v142, v154\n"
        "v_mul_f32 v143, v143, v155\n"
        "v_exp_f32 v140, v140\n"
        "v_mul_f32 v44, v44, v156\n"
        "v_mfma_f32_16x16x32_bf16 v[24:27], v[168:171], v[76:79], 0\n"
        "s_add_u32 s37, s36, 490560\n"
        "v_exp_f32 v141, v141\n"
        "v_mfma_f32_16x16x32_bf16 v[24:27], v[172:175], v[80:83], v[24:27]\n"
        "v_exp_f32 v142, v142\n"
        "v_mfma_f32_16x16x32_bf16 v[24:27], v[176:179], v[84:87], v[24:27]\n"
        "v_exp_f32 v143, v143\n"
        "v_add_f32 v125, v140, v141\n"
        "v_mfma_f32_16x16x32_bf16 v[20:23], v[188:191], v[84:87], 0\n"
        "v_add_f32 v125, v125, v142\n"
        "v_add_f32 v125, v125, v143\n"
        "v_mfma_f32_16x16x32_bf16 v[20:23], v[184:187], v[80:83], v[20:23]\n"
        "v_mul_f32 v45, v45, v157\n"
        "v_mul_f32 v46, v46, v158\n"
        "v_mfma_f32_16x16x32_bf16 v[20:23], v[180:183], v[76:79], v[20:23]\n"
        "buffer_store_dwordx3 v[148:150], v14, s[8:11], s37 offen offset:960 sc0 sc1 nt\n"
        "v_cvt_pk_bf16_f32 v60, v24, v25\n"
        "v_cvt_pk_bf16_f32 v61, v26, v27\n"
        "v_mul_f32 v47, v47, v159\n"
        "v_exp_f32 v44, v44\n"
        "v_mfma_f32_16x16x32_bf16 v[24:27], v[192:195], v[76:79], 0\n"
        "s_add_u32 s37, s36, 588672\n"
        "v_exp_f32 v45, v45\n"
        "v_mfma_f32_16x16x32_bf16 v[24:27], v[196:199], v[80:83], v[24:27]\n"
        "v_cvt_pk_bf16_f32 v62, v20, v21\n"
        "v_cvt_pk_bf16_f32 v63, v22, v23\n"
        "v_mfma_f32_16x16x32_bf16 v[24:27], v[200:203], v[84:87], v[24:27]\n"
        "buffer_store_dwordx3 v[136:138], v12, s[8:11], s37 offen offset:384 sc0 sc1 nt\n"
        "v_exp_f32 v46, v46\n"
        "v_exp_f32 v47, v47\n"
        "v_add_f32 v125, v125, v44\n"
        "v_add_f32 v125, v125, v45\n"
        "v_mfma_f32_16x16x32_bf16 v[20:23], v[212:215], v[84:87], 0\n"
        "v_add_f32 v125, v125, v46\n"
        "v_add_f32 v125, v125, v47\n"
        "v_mfma_f32_16x16x32_bf16 v[20:23], v[208:211], v[80:83], v[20:23]\n"
        "v_cvt_pk_bf16_f32 v64, v24, v25\n"
        "v_cvt_pk_bf16_f32 v65, v26, v27\n"
        "v_mfma_f32_16x16x32_bf16 v[20:23], v[204:207], v[76:79], v[20:23]\n"
        "v_mul_f32 v96, v96, v160\n"
        "v_mul_f32 v97, v97, v161\n"
        "v_mul_f32 v98, v98, v162\n"
        "v_mfma_f32_16x16x32_bf16 v[24:27], v[216:219], v[76:79], 0\n"
        "v_mul_f32 v99, v99, v163\n"
        "v_add_f32 v112, v112, v113\n"
        "v_mfma_f32_16x16x32_bf16 v[24:27], v[220:223], v[80:83], v[24:27]\n"
        "v_exp_f32 v96, v96\n"
        "v_mfma_f32_16x16x32_bf16 v[24:27], v[224:227], v[84:87], v[24:27]\n"
        "v_cvt_pk_bf16_f32 v66, v20, v21\n"
        "v_cvt_pk_bf16_f32 v67, v22, v23\n"
        "v_add_f32 v125, v125, v96\n"
        "v_mfma_f32_16x16x32_bf16 v[32:35], v[236:239], v[84:87], 0\n"
        "v_exp_f32 v97, v97\n"
        "v_mfma_f32_16x16x32_bf16 v[32:35], v[232:235], v[80:83], v[32:35]\n"
        "v_exp_f32 v98, v98\n"
        "v_mfma_f32_16x16x32_bf16 v[32:35], v[228:231], v[76:79], v[32:35]\n"
        "v_cvt_pk_bf16_f32 v20, v24, v25\n"
        "v_cvt_pk_bf16_f32 v21, v26, v27\n"
        "v_mfma_f32_16x16x4_4b_bf16 v[148:163], v[30:31], v[28:29], 0 blgp:5\n"
        "v_exp_f32 v99, v99\n"
        "v_add_f32 v125, v125, v97\n"
        "v_add_f32 v125, v125, v98\n"
        "v_mul_f32 v112, v112, v164\n"
        "v_exp_f32 v112, v112\n"
        "v_cvt_pk_bf16_f32 v22, v32, v33\n"
        "v_cvt_pk_bf16_f32 v23, v34, v35\n"
        "v_mfma_f32_16x16x32_bf16 v[164:167], v[88:91], v[60:63], 0\n"
        "v_add_f32 v125, v125, v99\n"
        "v_add_f32_dpp v139, v139, v139 row_ror:1 row_mask:0xf bank_mask:0xf\n"
        "v_mfma_f32_16x16x32_bf16 v[164:167], v[92:95], v[64:67], v[164:167]\n"
        "v_add_f32_dpp v125, v112, v125 quad_perm:[0,1,2,3] row_mask:0x4 bank_mask:0xf\n"
        "v_mfma_f32_16x16x32_bf16 v[164:167], v[100:103], v[20:23], v[164:167]\n"
        "v_mfma_f32_16x16x32_bf16 v[144:147], v[84:87], v[20:23], 0\n"
        "v_permlane32_swap_b32 v125, v139\n"
        "v_mfma_f32_16x16x32_bf16 v[144:147], v[80:83], v[64:67], v[144:147]\n"
        "v_add_f32 v125, v125, v139\n"
        "v_mov_b32 v163, v125\n"
        "v_mfma_f32_16x16x32_bf16 v[144:147], v[76:79], v[60:63], v[144:147]\n"
        "v_mfma_f32_16x16x32_bf16 v[108:111], v[36:39], v[60:63], 0\n"
        "v_permlane16_swap_b32 v125, v163\n"
        "v_mfma_f32_16x16x32_bf16 v[108:111], v[40:43], v[64:67], v[108:111]\n"
        "v_add_f32 v125, v125, v163\n"
        "v_mul_f32 v164, v164, v148\n"
        "v_mfma_f32_16x16x32_bf16 v[108:111], v[132:135], v[20:23], v[108:111]\n"
        "v_rcp_f32 v130, v125\n"
        "v_mul_f32 v165, v165, v149\n"
        "v_and_b32 v138, s28, v130\n"
        "v_mfma_f32_16x16x32_bf16 v[70:73], v[116:119], v[20:23], 0\n"
        "v_sub_f32 v149, v130, v138\n"
        "v_cvt_pk_bf16_f32 v139, v138, v149\n"
        "v_smfmac_f32_16x16x64_bf16 v[70:73], v[48:51], v[60:67], v4\n"
        "v_mul_f32 v166, v166, v150\n"
        "v_mfma_f32_16x16x32_bf16 v[32:35], v[168:171], v[36:39], 0\n"
        "v_mul_f32 v167, v167, v151\n"
        "v_mul_f32 v144, v144, v152\n"
        "v_mfma_f32_16x16x32_bf16 v[32:35], v[172:175], v[40:43], v[32:35]\n"
        "v_exp_f32 v164, v164\n"
        "v_mfma_f32_16x16x32_bf16 v[32:35], v[176:179], v[132:135], v[32:35]\n"
        "v_exp_f32 v165, v165\n"
        "v_exp_f32 v166, v166\n"
        "v_mfma_f32_16x16x32_bf16 v[24:27], v[188:191], v[132:135], 0\n"
        "v_exp_f32 v167, v167\n"
        "v_mfma_f32_16x16x32_bf16 v[24:27], v[184:187], v[40:43], v[24:27]\n"
        "v_add_f32 v69, v164, v165\n"
        "v_add_f32 v69, v69, v166\n"
        "v_mfma_f32_16x16x32_bf16 v[24:27], v[180:183], v[36:39], v[24:27]\n"
        "v_cvt_pk_bf16_f32 v20, v32, v33\n"
        "v_cvt_pk_bf16_f32 v21, v34, v35\n"
        "v_add_f32 v69, v69, v167\n"
        "v_mfma_f32_16x16x32_bf16 v[56:59], v[192:195], v[36:39], 0\n"
        "v_mul_f32 v145, v145, v153\n"
        "v_mul_f32 v146, v146, v154\n"
        "v_mfma_f32_16x16x32_bf16 v[56:59], v[196:199], v[40:43], v[56:59]\n"
        "v_mul_f32 v147, v147, v155\n"
        "v_cvt_pk_bf16_f32 v22, v24, v25\n"
        "v_mfma_f32_16x16x32_bf16 v[56:59], v[200:203], v[132:135], v[56:59]\n"
        "v_cvt_pk_bf16_f32 v23, v26, v27\n"
        "v_exp_f32 v144, v144\n"
        "v_mul_f32 v108, v108, v156\n"
        "v_mfma_f32_16x16x32_bf16 v[32:35], v[212:215], v[132:135], 0\n"
        "v_exp_f32 v145, v145\n"
        "v_mfma_f32_16x16x32_bf16 v[32:35], v[208:211], v[40:43], v[32:35]\n"
        "v_exp_f32 v146, v146\n"
        "v_mfma_f32_16x16x32_bf16 v[32:35], v[204:207], v[36:39], v[32:35]\n"
        "v_cvt_pk_bf16_f32 v24, v56, v57\n"
        "v_cvt_pk_bf16_f32 v25, v58, v59\n"
        "v_add_f32 v69, v69, v144\n"
        "v_mfma_f32_16x16x32_bf16 v[56:59], v[216:219], v[36:39], 0\n"
        "v_exp_f32 v147, v147\n"
        "v_mfma_f32_16x16x32_bf16 v[56:59], v[220:223], v[40:43], v[56:59]\n"
        "v_add_f32 v69, v69, v145\n"
        "v_add_f32 v69, v69, v146\n"
        "v_mfma_f32_16x16x32_bf16 v[56:59], v[224:227], v[132:135], v[56:59]\n"
        "v_cvt_pk_bf16_f32 v26, v32, v33\n"
        "v_cvt_pk_bf16_f32 v27, v34, v35\n"
        "v_add_f32 v69, v69, v147\n"
        "v_mul_f32 v109, v109, v157\n"
        "v_mfma_f32_16x16x32_bf16 v[32:35], v[236:239], v[132:135], 0\n"
        "v_mul_f32 v110, v110, v158\n"
        "v_mul_f32 v111, v111, v159\n"
        "v_mfma_f32_16x16x32_bf16 v[32:35], v[232:235], v[40:43], v[32:35]\n"
        "v_cvt_pk_bf16_f32 v136, v56, v57\n"
        "v_cvt_pk_bf16_f32 v137, v58, v59\n"
        "v_mfma_f32_16x16x32_bf16 v[32:35], v[228:231], v[36:39], v[32:35]\n"
        "v_exp_f32 v108, v108\n"
        "v_mfma_f32_16x16x4_4b_bf16 v[52:67], v[30:31], v[138:139], 0 blgp:4\n"
        "v_exp_f32 v109, v109\n"
        "v_exp_f32 v110, v110\n"
        "v_exp_f32 v111, v111\n"
        "v_add_f32 v69, v69, v108\n"
        "v_add_f32 v69, v69, v109\n"
        "v_add_f32 v69, v69, v110\n"
        "v_cvt_pk_bf16_f32 v138, v32, v33\n"
        "v_cvt_pk_bf16_f32 v139, v34, v35\n"
        "v_mfma_f32_16x16x32_bf16 v[104:107], v[88:91], v[20:23], 0\n"
        "v_add_f32 v69, v69, v111\n"
        "v_add_f32 v70, v70, v71\n"
        "v_mfma_f32_16x16x32_bf16 v[104:107], v[92:95], v[24:27], v[104:107]\n"
        "v_mul_f32 v140, v140, v52\n"
        "v_mul_f32 v141, v141, v53\n"
        "v_mfma_f32_16x16x32_bf16 v[104:107], v[100:103], v[136:139], v[104:107]\n"
        "v_mul_f32 v142, v142, v54\n"
        "v_mul_f32 v143, v143, v55\n"
        "v_mul_f32 v44, v44, v56\n"
        "v_mul_f32 v45, v45, v57\n"
        "v_mfma_f32_16x16x32_bf16 v[126:129], v[84:87], v[136:139], 0\n"
        "v_mul_f32 v70, v70, v160\n"
        "v_mul_f32 v46, v46, v58\n"
        "v_mfma_f32_16x16x32_bf16 v[126:129], v[80:83], v[24:27], v[126:129]\n"
        "v_exp_f32 v70, v70\n"
        "v_mfma_f32_16x16x32_bf16 v[126:129], v[76:79], v[20:23], v[126:129]\n"
        "v_mul_f32 v47, v47, v59\n"
        "v_add_f32_dpp v69, v70, v69 quad_perm:[0,1,2,3] row_mask:0x4 bank_mask:0xf\n"
        "v_mul_f32 v96, v96, v60\n"
        "v_mfma_f32_16x16x32_bf16 v[152:155], v[36:39], v[20:23], 0\n"
        "v_mul_f32 v97, v97, v61\n"
        "v_mul_f32 v98, v98, v62\n"
        "v_mfma_f32_16x16x32_bf16 v[152:155], v[40:43], v[24:27], v[152:155]\n"
        "v_mul_f32 v99, v99, v63\n"
        "v_fma_f32 v112, v112, v64, v19\n"
        "v_mfma_f32_16x16x32_bf16 v[152:155], v[132:135], v[136:139], v[152:155]\n"
        "v_cvt_pk_bf16_f32 v148, v140, v141\n"
        "v_mfma_f32_16x16x4_4b_bf16 v[52:67], v[30:31], v[28:29], 0 blgp:6\n"
        "v_cvt_pk_bf16_f32 v149, v142, v143\n"
        "v_cvt_pk_bf16_f32 v150, v44, v45\n"
        "v_cvt_pk_bf16_f32 v151, v46, v47\n"
        "v_cvt_pk_bf16_f32 v140, v96, v97\n"
        "v_cvt_pk_bf16_f32 v142, v98, v99\n"
        "v_cvt_pk_bf16_f32 v141, v112, 0\n"
        "v_min3_f32 v135, v125, v130, v123\n"
        "v_readlane_b32 s59, v130, 32\n"
        "v_mfma_f32_16x16x32_bf16 v[44:47], v[116:119], v[136:139], 0\n"
        "ds_read_b128 v[40:43], v17 offset:0\n"
        "v_mul_f32 v68, s59, v68\n"
        "v_smfmac_f32_16x16x64_bf16 v[44:47], v[48:51], v[20:27], v4\n"
        "ds_read_b128 v[72:75], v17 offset:64\n"
        "ds_read_b128 v[136:139], v17 offset:128\n"
        "v_mfma_f32_16x16x32_bf16 v[24:27], v[88:91], v[240:243], 0\n"
        "v_mul_f32 v104, v104, v52\n"
        "v_mul_f32 v105, v105, v53\n"
        "v_mfma_f32_16x16x32_bf16 v[24:27], v[92:95], v[244:247], v[24:27]\n"
        "ds_read_b128 v[116:119], v0 offset:5136\n"
        "v_mul_f32 v106, v106, v54\n"
        "v_mfma_f32_16x16x32_bf16 v[24:27], v[100:103], v[248:251], v[24:27]\n"
        "ds_read_b128 v[112:115], v0 offset:4112\n"
        "v_mul_f32 v107, v107, v55\n"
        "v_mfma_f32_16x16x32_bf16 v[20:23], v[84:87], v[248:251], 0\n"
        "v_exp_f32 v104, v104\n"
        "v_mfma_f32_16x16x32_bf16 v[20:23], v[80:83], v[244:247], v[20:23]\n"
        "v_exp_f32 v105, v105\n"
        "v_mfma_f32_16x16x32_bf16 v[20:23], v[76:79], v[240:243], v[20:23]\n"
        "s_waitcnt lgkmcnt(2)\n"
        "v_cvt_pk_bf16_f32 v156, v24, v25\n"
        "v_cvt_pk_bf16_f32 v157, v26, v27\n"
        "v_exp_f32 v106, v106\n"
        "v_mfma_f32_16x16x32_bf16 v[36:39], v[40:43], v[240:243], 0\n"
        "v_exp_f32 v107, v107\n"
        "v_mfma_f32_16x16x32_bf16 v[36:39], v[72:75], v[244:247], v[36:39]\n"
        "v_add_f32 v143, v104, v105\n"
        "v_cvt_pk_bf16_f32 v158, v20, v21\n"
        "v_mfma_f32_16x16x32_bf16 v[36:39], v[136:139], v[248:251], v[36:39]\n"
        "s_waitcnt lgkmcnt(0)\n"
        "v_cvt_pk_bf16_f32 v159, v22, v23\n"
        "v_add_f32 v143, v143, v106\n"
        "v_add_f32 v143, v143, v107\n"
        "v_mfma_f32_16x16x32_bf16 v[52:55], v[136:139], v[116:119], 0\n"
        "v_mul_f32 v126, v126, v56\n"
        "v_mul_f32 v127, v127, v57\n"
        "v_mfma_f32_16x16x32_bf16 v[52:55], v[72:75], v[112:115], v[52:55]\n"
        "ds_read_b128 v[122:125], v0 offset:6176\n"
        "v_cvt_pk_bf16_f32 v46, v36, v37\n"
        "v_mfma_f32_16x16x32_bf16 v[52:55], v[40:43], v[252:255], v[52:55]\n"
        "ds_read_b128 v[26:29], v0 offset:7200\n"
        "ds_read_b128 v[130:133], v0 offset:8224\n"
        "v_mfma_f32_16x16x32_bf16 v[32:35], v[76:79], v[252:255], 0\n"
        "v_cvt_pk_bf16_f32 v50, v38, v39\n"
        "v_mul_f32 v128, v128, v58\n"
        "v_mfma_f32_16x16x32_bf16 v[32:35], v[80:83], v[112:115], v[32:35]\n"
        "v_mul_f32 v129, v129, v59\n"
        "v_mul_f32 v152, v152, v60\n"
        "v_mfma_f32_16x16x32_bf16 v[32:35], v[84:87], v[116:119], v[32:35]\n"
        "v_cvt_pk_bf16_f32 v20, v52, v53\n"
        "v_cvt_pk_bf16_f32 v24, v54, v55\n"
        "v_exp_f32 v126, v126\n"
        "v_mfma_f32_16x16x32_bf16 v[36:39], v[100:103], v[116:119], 0\n"
        "v_exp_f32 v127, v127\n"
        "v_mfma_f32_16x16x32_bf16 v[36:39], v[92:95], v[112:115], v[36:39]\n"
        "v_exp_f32 v128, v128\n"
        "v_mfma_f32_16x16x32_bf16 v[36:39], v[88:91], v[252:255], v[36:39]\n"
        "s_waitcnt lgkmcnt(0)\n"
        "v_cvt_pk_bf16_f32 v56, v32, v33\n"
        "v_cvt_pk_bf16_f32 v57, v34, v35\n"
        "v_add_f32 v143, v143, v126\n"
        "v_mfma_f32_16x16x32_bf16 v[112:115], v[88:91], v[122:125], 0\n"
        "v_exp_f32 v129, v129\n"
        "v_mfma_f32_16x16x32_bf16 v[112:115], v[92:95], v[26:29], v[112:115]\n"
        "ds_read_b128 v[118:121], v0 offset:11312\n"
        "v_cvt_pk_bf16_f32 v54, v36, v37\n"
        "v_mfma_f32_16x16x32_bf16 v[112:115], v[100:103], v[130:133], v[112:115]\n"
        "ds_read_b128 v[160:163], v0 offset:10288\n"
        "ds_read_b128 v[32:35], v0 offset:9264\n"
        "v_mfma_f32_16x16x32_bf16 v[96:99], v[84:87], v[130:133], 0\n"
        "v_cvt_pk_bf16_f32 v55, v38, v39\n"
        "v_add_f32 v143, v143, v127\n"
        "v_mfma_f32_16x16x32_bf16 v[96:99], v[80:83], v[26:29], v[96:99]\n"
        "v_add_f32 v143, v143, v128\n"
        "v_add_f32 v143, v143, v129\n"
        "v_mfma_f32_16x16x32_bf16 v[96:99], v[76:79], v[122:125], v[96:99]\n"
        "v_cvt_pk_bf16_f32 v36, v112, v113\n"
        "v_cvt_pk_bf16_f32 v37, v114, v115\n"
        "v_mul_f32 v153, v153, v61\n"
        "v_mul_f32 v154, v154, v62\n"
        "v_mfma_f32_16x16x32_bf16 v[112:115], v[40:43], v[122:125], 0\n"
        "v_mul_f32 v155, v155, v63\n"
        "v_add_f32 v44, v44, v45\n"
        "v_mfma_f32_16x16x32_bf16 v[112:115], v[72:75], v[26:29], v[112:115]\n"
        "v_cvt_pk_bf16_f32 v38, v96, v97\n"
        "v_cvt_pk_bf16_f32 v39, v98, v99\n"
        "v_mfma_f32_16x16x32_bf16 v[112:115], v[136:139], v[130:133], v[112:115]\n"
        "s_waitcnt lgkmcnt(0)\n"
        "v_exp_f32 v152, v152\n"
        "v_mul_f32 v44, v44, v64\n"
        "v_mfma_f32_16x16x32_bf16 v[64:67], v[136:139], v[118:121], 0\n"
        "v_exp_f32 v153, v153\n"
        "v_mfma_f32_16x16x32_bf16 v[64:67], v[72:75], v[160:163], v[64:67]\n"
        "ds_read_b128 v[96:99], v0 offset:12352\n"
        "v_add_f32 v143, v143, v152\n"
        "v_mfma_f32_16x16x32_bf16 v[64:67], v[40:43], v[32:35], v[64:67]\n"
        "ds_read_b128 v[60:63], v0 offset:13376\n"
        "ds_read_b128 v[26:29], v0 offset:14400\n"
        "v_mfma_f32_16x16x32_bf16 v[122:125], v[76:79], v[32:35], 0\n"
        "v_cvt_pk_bf16_f32 v130, v112, v113\n"
        "v_cvt_pk_bf16_f32 v134, v114, v115\n"
        "v_mfma_f32_16x16x32_bf16 v[122:125], v[80:83], v[160:163], v[122:125]\n"
        "v_exp_f32 v154, v154\n"
        "v_mfma_f32_16x16x32_bf16 v[122:125], v[84:87], v[118:121], v[122:125]\n"
        "v_exp_f32 v155, v155\n"
        "v_add_f32 v143, v143, v153\n"
        "v_mfma_f32_16x16x32_bf16 v[114:117], v[100:103], v[118:121], 0\n"
        "v_add_f32 v143, v143, v154\n"
        "v_add_f32 v143, v143, v155\n"
        "v_mfma_f32_16x16x32_bf16 v[114:117], v[92:95], v[160:163], v[114:117]\n"
        "v_cvt_pk_bf16_f32 v47, v64, v65\n"
        "v_cvt_pk_bf16_f32 v51, v66, v67\n"
        "v_mfma_f32_16x16x32_bf16 v[114:117], v[88:91], v[32:35], v[114:117]\n"
        "s_waitcnt lgkmcnt(0)\n"
        "v_cvt_pk_bf16_f32 v162, v122, v123\n"
        "v_cvt_pk_bf16_f32 v163, v124, v125\n"
        "v_exp_f32 v44, v44\n"
        "v_mfma_f32_16x16x32_bf16 v[118:121], v[88:91], v[96:99], 0\n"
        "v_cvt_pk_bf16_f32 v68, v68, 0\n"
        "v_add_f32_dpp v143, v44, v143 quad_perm:[0,1,2,3] row_mask:0x4 bank_mask:0xf\n"
        "v_mfma_f32_16x16x32_bf16 v[118:121], v[92:95], v[60:63], v[118:121]\n"
        "ds_read_b128 v[32:35], v0 offset:17488\n"
        "v_cvt_pk_bf16_f32 v160, v114, v115\n"
        "v_mfma_f32_16x16x32_bf16 v[118:121], v[100:103], v[26:29], v[118:121]\n"
        "ds_read_b128 v[112:115], v0 offset:16464\n"
        "ds_read_b128 v[64:67], v0 offset:15440\n"
        "v_mfma_f32_16x16x32_bf16 v[122:125], v[84:87], v[26:29], 0\n"
        "v_cvt_pk_bf16_f32 v161, v116, v117\n"
        "v_mfma_f32_16x16x32_bf16 v[122:125], v[80:83], v[60:63], v[122:125]\n"
        "v_permlane32_swap_b32 v69, v143\n"
        "v_mfma_f32_16x16x32_bf16 v[122:125], v[76:79], v[96:99], v[122:125]\n"
        "v_add_f32 v69, v69, v143\n"
        "v_mov_b32 v133, v69\n"
        "v_cvt_pk_bf16_f32 v58, v118, v119\n"
        "v_mfma_f32_16x16x32_bf16 v[116:119], v[40:43], v[96:99], 0\n"
        "ds_read2_b32 v[52:53], v11 offset0:0 offset1:3\n"
        "v_cvt_pk_bf16_f32 v59, v120, v121\n"
        "v_mfma_f32_16x16x32_bf16 v[116:119], v[72:75], v[60:63], v[116:119]\n"
        "ds_read_b64_tr_b16 v[48:49], v9 offset:0\n"
        "v_cvt_pk_bf16_f32 v60, v122, v123\n"
        "v_mfma_f32_16x16x32_bf16 v[116:119], v[136:139], v[26:29], v[116:119]\n"
        "s_waitcnt lgkmcnt(2)\n"
        "ds_read2_b32 v[26:27], v11 offset0:1 offset1:4\n"
        "v_permlane16_swap_b32 v69, v133\n"
        "v_add_f32 v69, v69, v133\n"
        "v_mfma_f32_16x16x32_bf16 v[96:99], v[136:139], v[32:35], 0\n"
        "ds_read_b64_tr_b16 v[22:23], v9 offset:16\n"
        "v_cvt_pk_bf16_f32 v61, v124, v125\n"
        "v_mfma_f32_16x16x32_bf16 v[96:99], v[72:75], v[112:115], v[96:99]\n"
        "ds_read2_b32 v[136:137], v11 offset0:2 offset1:5\n"
        "v_cvt_pk_bf16_f32 v21, v116, v117\n"
        "v_mfma_f32_16x16x32_bf16 v[96:99], v[40:43], v[64:67], v[96:99]\n"
        "ds_read_b64_tr_b16 v[132:133], v9 offset:32\n"
        "v_rcp_f32 v125, v69\n"
        "v_cvt_pk_bf16_f32 v25, v118, v119\n"
        "v_mfma_f32_16x16x32_bf16 v[116:119], v[76:79], v[64:67], 0\n"
        "v_and_b32 v138, s28, v125\n"
        "v_sub_f32 v143, v125, v138\n"
        "v_mfma_f32_16x16x32_bf16 v[116:119], v[80:83], v[112:115], v[116:119]\n"
        "v_cvt_pk_bf16_f32 v139, v138, v143\n"
        "v_mov_b32_dpp v143, v19 row_shr:4 row_mask:0xf bank_mask:0xf bound_ctrl:0\n"
        "v_mfma_f32_16x16x32_bf16 v[116:119], v[84:87], v[32:35], v[116:119]\n"
        "v_min3_f32 v135, v135, v69, v125\n"
        "v_mfma_f32_16x16x4_4b_bf16 v[72:87], v[30:31], v[138:139], 0 blgp:4\n"
        "v_cmp_nle_f32 vcc, 0x2b800000, v135\n"
        "ds_write_b16 v1, v68\n"
        "v_cvt_pk_bf16_f32 v131, v96, v97\n"
        "v_cvt_pk_bf16_f32 v135, v98, v99\n"
        "s_andn2_b64 vcc, vcc, s[56:57]\n"
        "s_cbranch_scc1 CREDOA_%=\n"
        "v_cvt_pk_bf16_f32 v42, v116, v117\n"
        "v_cvt_pk_bf16_f32 v43, v118, v119\n"
        "v_mfma_f32_16x16x32_bf16 v[96:99], v[100:103], v[32:35], 0\n"
        "s_mov_b32 exec_hi, 0xffff\n"
        "v_add_u32 v10, 40064, v10\n"
        "s_mov_b32 exec_hi, -1\n"
        "v_mfma_f32_16x16x32_bf16 v[96:99], v[92:95], v[112:115], v[96:99]\n"
        "s_add_u32 s31, s31, 1\n"
        "v_mul_f32 v164, v164, v72\n"
        "v_mul_f32 v165, v165, v73\n"
        "v_mfma_f32_16x16x32_bf16 v[96:99], v[88:91], v[64:67], v[96:99]\n"
        "s_mul_i32 s38, s31, 1344\n"
        "v_mul_f32 v166, v166, v74\n"
        "v_mul_f32 v167, v167, v75\n"
        "v_mfma_f32_16x16x32_bf16 v[112:115], v[148:151], v[156:159], 0\n"
        "s_waitcnt lgkmcnt(1)\n"
        "v_mul_f32 v144, v144, v76\n"
        "v_mul_f32 v145, v145, v77\n"
        "v_smfmac_f32_16x16x64_bf16 v[112:115], v[140:143], v[46:53], v18\n"
        "s_cmp_ge_u32 s31, s76\n"
        "s_cbranch_scc1 L33A_%=\n"
        "L34A_%=:\n"
        "ds_read_b64_tr_b16 v[48:49], v9 offset:48\n"
        "v_mul_f32 v146, v146, v78\n"
        "v_mfma_f32_16x16x32_bf16 v[122:125], v[148:151], v[54:57], 0\n"
        "s_cmp_ge_u32 s31, s71\n"
        "v_mul_f32 v147, v147, v79\n"
        "v_mul_f32 v108, v108, v80\n"
        "v_smfmac_f32_16x16x64_bf16 v[122:125], v[140:143], v[20:27], v18\n"
        "s_cselect_b32 s39, s70, s72\n"
        "ds_read_b64_tr_b16 v[22:23], v9 offset:64\n"
        "v_mfma_f32_16x16x32_bf16 v[76:79], v[148:151], v[36:39], 0\n"
        "s_cmp_ge_u32 s31, 20\n"
        "ds_read_b128 v[72:75], v2\n"
        "v_cvt_pk_bf16_f32 v40, v96, v97\n"
        "v_smfmac_f32_16x16x64_bf16 v[76:79], v[140:143], v[130:137], v18\n"
        "s_cselect_b32 s39, s22, s39\n"
        "ds_read_b64_tr_b16 v[132:133], v9 offset:80\n"
        "ds_read_b128 v[32:35], v3\n"
        "v_cvt_pk_bf16_f32 v41, v98, v99\n"
        "v_mfma_f32_16x16x32_bf16 v[116:119], v[148:151], v[160:163], 0\n"
        "s_cmp_ge_u32 s31, 21\n"
        "v_mul_f32 v109, v109, v81\n"
        "s_waitcnt lgkmcnt(4)\n"
        "v_smfmac_f32_16x16x64_bf16 v[116:119], v[140:143], v[46:53], v18 abid:1\n"
        "s_cselect_b32 s39, s23, s39\n"
        "v_mul_f32 v110, v110, v82\n"
        "v_mfma_f32_16x16x4_4b_bf16 v[88:103], v[30:31], v[138:139], 0 blgp:6\n"
        "v_mul_f32 v111, v111, v83\n"
        "v_fma_f32 v70, v70, v84, v19\n"
        "v_cvt_pk_bf16_f32 v80, v164, v165\n"
        "v_cvt_pk_bf16_f32 v81, v166, v167\n"
        "v_cvt_pk_bf16_f32 v82, v144, v145\n"
        "v_cvt_pk_bf16_f32 v83, v146, v147\n"
        "v_cvt_pk_bf16_f32 v84, v108, v109\n"
        "v_cvt_pk_bf16_f32 v86, v110, v111\n"
        "v_cvt_pk_bf16_f32 v85, v70, 0\n"
        "v_cvt_pk_bf16_f32 v164, v112, v122\n"
        "v_cvt_pk_bf16_f32 v165, v76, v116\n"
        "v_mfma_f32_16x16x32_bf16 v[108:111], v[148:151], v[58:61], 0\n"
        "s_waitcnt lgkmcnt(0)\n"
        "v_mul_f32 v104, v104, v88\n"
        "v_mul_f32 v105, v105, v89\n"
        "v_smfmac_f32_16x16x64_bf16 v[108:111], v[140:143], v[20:27], v18 abid:1\n"
        "s_add_u32 s39, s39, s38\n"
        "s_mul_i32 s68, s31, 3\n"
        "v_mul_f32 v106, v106, v90\n"
        "v_mul_f32 v107, v107, v91\n"
        "v_mul_f32 v126, v126, v92\n"
        "v_mov_b64 v[68:69], 0\n"
        "v_mfma_f32_16x16x32_bf16 v[144:147], v[148:151], v[40:43], 0\n"
        "s_lshr_b32 s38, s31, 3\n"
        "v_mov_b64 v[70:71], 0\n"
        "v_mov_b64 v[28:29], 0\n"
        "v_smfmac_f32_16x16x64_bf16 v[144:147], v[140:143], v[130:137], v18 abid:1\n"
        "s_add_u32 s38, s38, 11\n"
        "v_mov_b64 v[30:31], 0\n"
        "v_smfmac_f32_16x16x64_bf16 v[68:71], v[72:75], v[156:163], v4\n"
        "s_bitcmp1_b32 s31, 0\n"
        "v_mov_b64 v[64:65], 0\n"
        "v_smfmac_f32_16x16x64_bf16 v[68:71], v[32:35], v[46:53], v4 abid:1\n"
        "s_cselect_b32 s38, 3, s38\n"
        "s_add_u32 s37, s35, 0\n"
        "ds_read_b64_tr_b16 v[48:49], v9 offset:3328\n"
        "v_mov_b64 v[66:67], 0\n"
        "v_smfmac_f32_16x16x64_bf16 v[28:31], v[72:75], v[54:61], v4\n"
        "s_add_u32 s68, s68, s38\n"
        "v_cvt_pk_bf16_f32 v166, v108, v144\n"
        "v_smfmac_f32_16x16x64_bf16 v[28:31], v[32:35], v[20:27], v4 abid:1\n"
        "s_cmp_lt_u32 s31, s14\n"
        "ds_read_b64_tr_b16 v[22:23], v9 offset:3344\n"
        "v_mov_b32_dpp v87, v19 row_shr:4 row_mask:0xf bank_mask:0xf bound_ctrl:0\n"
        "v_mul_f32 v127, v127, v93\n"
        "v_cvt_pk_bf16_f32 v148, v113, v123\n"
        "v_smfmac_f32_16x16x64_bf16 v[64:67], v[72:75], v[36:43], v4\n"
        "s_cselect_b32 s68, s68, 0\n"
        "v_cvt_pk_bf16_f32 v116, v114, v124\n"
        "v_smfmac_f32_16x16x64_bf16 v[64:67], v[32:35], v[130:137], v4 abid:1\n"
        "buffer_store_dwordx3 v[164:166], v12, s[8:11], s37 offen sc0 sc1\n"
        "ds_read_b64_tr_b16 v[132:133], v9 offset:3360\n"
        "v_cvt_pk_bf16_f32 v122, v115, v125\n"
        "v_cvt_pk_bf16_f32 v149, v77, v117\n"
        "v_cvt_pk_bf16_f32 v117, v78, v118\n"
        "v_cvt_pk_bf16_f32 v123, v79, v119\n"
        "v_cvt_pk_bf16_f32 v150, v109, v145\n"
        "v_mfma_f32_16x16x32_bf16 v[140:143], v[80:83], v[156:159], 0\n"
        "s_waitcnt lgkmcnt(2)\n"
        "v_cvt_pk_bf16_f32 v118, v110, v146\n"
        "v_cvt_pk_bf16_f32 v124, v111, v147\n"
        "v_smfmac_f32_16x16x64_bf16 v[140:143], v[84:87], v[46:53], v18\n"
        "s_and_b32 s38, s31, 7\n"
        "s_cselect_b32 s30, 1, -7\n"
        "ds_read_b64_tr_b16 v[48:49], v9 offset:3376\n"
        "v_add_f32 v68, v68, v69\n"
        "v_add_f32 v70, v70, v71\n"
        "v_add_f32 v28, v28, v29\n"
        "v_mfma_f32_16x16x32_bf16 v[164:167], v[80:83], v[54:57], 0\n"
        "s_waitcnt lgkmcnt(2)\n"
        "v_add_f32 v30, v30, v31\n"
        "v_cvt_pk_bf16_f32 v144, v68, v28\n"
        "v_smfmac_f32_16x16x64_bf16 v[164:167], v[84:87], v[20:27], v18\n"
        "s_add_u32 s37, s35, 0\n"
        "s_mul_i32 s29, s30, 192\n"
        "ds_read_b64_tr_b16 v[22:23], v9 offset:3392\n"
        "v_add_f32 v64, v64, v65\n"
        "v_add_f32 v66, v66, v67\n"
        "v_cvt_pk_bf16_f32 v145, v64, v70\n"
        "v_mfma_f32_16x16x32_bf16 v[108:111], v[80:83], v[36:39], 0\n"
        "s_waitcnt lgkmcnt(2)\n"
        "v_cvt_pk_bf16_f32 v146, v30, v66\n"
        "v_mul_f32 v128, v128, v94\n"
        "v_smfmac_f32_16x16x64_bf16 v[108:111], v[84:87], v[130:137], v18\n"
        "buffer_store_dwordx3 v[148:150], v15, s[8:11], s37 offen offset:768 sc0 sc1 nt\n"
        "ds_read_b64_tr_b16 v[132:133], v9 offset:3408\n"
        "s_mul_i32 s30, s30, 392\n"
        "v_mul_f32 v129, v129, v95\n"
        "v_mul_f32 v152, v152, v96\n"
        "v_mul_f32 v153, v153, v97\n"
        "v_mul_f32 v154, v154, v98\n"
        "v_mfma_f32_16x16x32_bf16 v[68:71], v[80:83], v[160:163], 0\n"
        "s_waitcnt lgkmcnt(2)\n"
        "s_cmp_eq_u32 s73, s18\n"
        "v_mul_f32 v155, v155, v99\n"
        "v_smfmac_f32_16x16x64_bf16 v[68:71], v[84:87], v[46:53], v18 abid:1\n"
        "s_cselect_b32 s34, -7488, s75\n"
        "s_add_u32 s37, s35, 98112\n"
        "ds_read_b64_tr_b16 v[48:49], v9 offset:6656\n"
        "v_mfma_f32_16x16x32_bf16 v[148:151], v[80:83], v[58:61], 0\n"
        "s_waitcnt lgkmcnt(2)\n"
        "v_fma_f32 v44, v44, v100, v19\n"
        "v_cvt_pk_bf16_f32 v32, v104, v105\n"
        "v_smfmac_f32_16x16x64_bf16 v[148:151], v[84:87], v[20:27], v18 abid:1\n"
        "s_add_u32 s73, s73, s34\n"
        "ds_read_b64_tr_b16 v[22:23], v9 offset:6672\n"
        "v_cvt_pk_bf16_f32 v33, v106, v107\n"
        "v_cvt_pk_bf16_f32 v34, v126, v127\n"
        "v_mfma_f32_16x16x32_bf16 v[72:75], v[80:83], v[40:43], 0\n"
        "s_waitcnt lgkmcnt(2)\n"
        "v_cvt_pk_bf16_f32 v35, v128, v129\n"
        "v_cvt_pk_bf16_f32 v64, v152, v153\n"
        "v_smfmac_f32_16x16x64_bf16 v[72:75], v[84:87], v[130:137], v18 abid:1\n"
        "buffer_store_dwordx3 v[116:118], v12, s[8:11], s37 offen offset:192 sc0 sc1 nt\n"
        "ds_read_b64_tr_b16 v[132:133], v9 offset:6688\n"
        "ds_read_b32 v139, v0 offset:21604\n"
        "v_cvt_pk_bf16_f32 v66, v154, v155\n"
        "v_cvt_pk_bf16_f32 v65, v44, 0\n"
        "v_mov_b32_dpp v67, v19 row_shr:4 row_mask:0xf bank_mask:0xf bound_ctrl:0\n"
        "v_mfma_f32_16x16x32_bf16 v[116:119], v[32:35], v[156:159], 0\n"
        "s_waitcnt lgkmcnt(3)\n"
        "v_writelane_b32 v10, s73, 56\n"
        "v_add_u32 v11, s30, v11\n"
        "v_smfmac_f32_16x16x64_bf16 v[116:119], v[64:67], v[46:53], v18\n"
        "s_add_u32 s37, s35, 98112\n"
        "ds_read_b64_tr_b16 v[48:49], v9 offset:6704\n"
        "v_mfma_f32_16x16x32_bf16 v[28:31], v[32:35], v[54:57], 0\n"
        "s_waitcnt lgkmcnt(3)\n"
        "v_smfmac_f32_16x16x64_bf16 v[28:31], v[64:67], v[20:27], v18\n"
        "ds_read_b64_tr_b16 v[22:23], v9 offset:6720\n"
        "v_mfma_f32_16x16x32_bf16 v[112:115], v[32:35], v[36:39], 0\n"
        "s_waitcnt lgkmcnt(3)\n"
        "s_waitcnt lgkmcnt(2)\n"
        "v_readfirstlane_b32 s40, v139\n"
        "v_smfmac_f32_16x16x64_bf16 v[112:115], v[64:67], v[130:137], v18\n"
        "buffer_store_dwordx3 v[122:124], v14, s[8:11], s37 offen offset:960 sc0 sc1 nt\n"
        "ds_read_b64_tr_b16 v[132:133], v9 offset:6736\n"
        "s_add_u32 s37, s35, 588672\n"
        "s_add_u32 s46, s37, 1344\n"
        "s_cmp_lt_i32 s40, s68\n"
        "s_mov_b64 exec, 1\n"
        "ds_inc_u32 v0, v1 offset:21608\n"
        "s_mov_b64 exec, -1\n"
        "s_cbranch_scc1 L37A_%=\n"
        "L38A_%=:\n"
        "ds_read_b128 v[88:91], v7 offset:40064\n"
        "v_mfma_f32_16x16x32_bf16 v[96:99], v[32:35], v[160:163], 0\n"
        "s_waitcnt lgkmcnt(4)\n"
        "ds_read_b128 v[76:79], v7 offset:43392\n"
        "v_smfmac_f32_16x16x64_bf16 v[96:99], v[64:67], v[46:53], v18 abid:1\n"
        "buffer_store_dwordx3 v[144:146], v12, s[44:47], s37 offen offset:1152 sc0 sc1 nt\n"
        "ds_read_b128 v[92:95], v7 offset:40128\n"
        "ds_read_b128 v[80:83], v7 offset:43456\n"
        "ds_read_b128 v[100:103], v7 offset:40192\n"
        "ds_read_b128 v[84:87], v7 offset:43520\n"
        "v_mfma_f32_16x16x32_bf16 v[52:55], v[32:35], v[58:61], 0\n"
        "s_waitcnt lgkmcnt(7)\n"
        "s_mov_b32 s36, s35\n"
        "v_smfmac_f32_16x16x64_bf16 v[52:55], v[64:67], v[20:27], v18 abid:1\n"
        "s_mov_b64 exec, s[62:63]\n"
        "v_add_u32 v16, s29, v16\n"
        "v_add_u32 v8, s34, v8\n"
        "s_mov_b64 exec, -1\n"
        "v_mfma_f32_16x16x32_bf16 v[56:59], v[32:35], v[40:43], 0\n"
        "s_mov_b32 s35, s39\n"
        "v_smfmac_f32_16x16x64_bf16 v[56:59], v[64:67], v[130:137], v18 abid:1\n"
        "s_mov_b64 exec, s[50:51]\n"
        "v_add_u32 v5, s29, v5\n"
        "v_add_u32 v6, s34, v6\n"
        "s_mov_b64 exec, -1\n"
        "s_setprio 0\n"
        "CNEXTA_%=:\n"
        "s_cmp_ge_u32 s31, s14\n"
        "s_cbranch_scc1 CEND_%=\n"
        "s_waitcnt lgkmcnt(5)\n"
        "v_mfma_f32_16x16x32_bf16 v[48:51], v[168:171], v[88:91], 0\n"
        "v_cvt_pk_bf16_f32 v144, v140, v164\n"
        "v_cvt_pk_bf16_f32 v145, v108, v68\n"
        "s_waitcnt lgkmcnt(3)\n"
        "v_mfma_f32_16x16x32_bf16 v[48:51], v[172:175], v[92:95], v[48:51]\n"
        "ds_read_b128 v[24:27], v16 offset:128\n"
        "v_cvt_pk_bf16_f32 v146, v148, v72\n"
        "s_waitcnt lgkmcnt(2)\n"
        "v_mfma_f32_16x16x32_bf16 v[48:51], v[176:179], v[100:103], v[48:51]\n"
        "ds_read_b128 v[32:35], v5\n"
        "v_cvt_pk_bf16_f32 v136, v141, v165\n"
        "v_mfma_f32_16x16x32_bf16 v[20:23], v[188:191], v[100:103], 0\n"
        "v_cvt_pk_bf16_f32 v137, v109, v69\n"
        "v_cvt_pk_bf16_f32 v138, v149, v73\n"
        "v_mfma_f32_16x16x32_bf16 v[20:23], v[184:187], v[92:95], v[20:23]\n"
        "s_setprio 2\n"
        "v_cvt_pk_bf16_f32 v160, v142, v166\n"
        "v_cvt_pk_bf16_f32 v124, v48, v49\n"
        "v_mfma_f32_16x16x32_bf16 v[20:23], v[180:183], v[88:91], v[20:23]\n"
        "s_add_u32 s37, s36, 196224\n"
        "ds_read_b128 v[36:39], v7 offset:46720\n"
        "ds_read_b128 v[40:43], v7 offset:46784\n"
        "ds_read_b128 v[132:135], v7 offset:46848\n"
        "v_mfma_f32_16x16x32_bf16 v[44:47], v[192:195], v[88:91], 0\n"
        "v_cvt_pk_bf16_f32 v125, v50, v51\n"
        "v_cvt_pk_bf16_f32 v161, v110, v70\n"
        "v_mfma_f32_16x16x32_bf16 v[44:47], v[196:199], v[92:95], v[44:47]\n"
        "v_cvt_pk_bf16_f32 v126, v20, v21\n"
        "v_cvt_pk_bf16_f32 v127, v22, v23\n"
        "v_mfma_f32_16x16x32_bf16 v[44:47], v[200:203], v[100:103], v[44:47]\n"
        "buffer_store_dwordx3 v[144:146], v12, s[8:11], s37 offen offset:384 sc0 sc1 nt\n"
        "v_cvt_pk_bf16_f32 v162, v150, v74\n"
        "v_cvt_pk_bf16_f32 v140, v143, v167\n"
        "v_cvt_pk_bf16_f32 v141, v111, v71\n"
        "v_cvt_pk_bf16_f32 v142, v151, v75\n"
        "s_waitcnt lgkmcnt(3)\n"
        "v_mfma_f32_16x16x32_bf16 v[20:23], v[24:27], v[100:103], 0\n"
        "v_cvt_pk_bf16_f32 v164, v116, v28\n"
        "v_cvt_pk_bf16_f32 v128, v44, v45\n"
        "v_smfmac_f32_16x16x64_bf16 v[20:23], v[32:35], v[88:95], v4\n"
        "s_add_u32 s37, s36, 196224\n"
        "v_cvt_pk_bf16_f32 v129, v46, v47\n"
        "v_mfma_f32_16x16x32_bf16 v[48:51], v[24:27], v[84:87], 0\n"
        "v_cvt_pk_bf16_f32 v165, v112, v96\n"
        "v_cvt_pk_bf16_f32 v166, v52, v56\n"
        "v_smfmac_f32_16x16x64_bf16 v[48:51], v[32:35], v[76:83], v4\n"
        "s_waitcnt lgkmcnt(0)\n"
        "v_cvt_pk_bf16_f32 v144, v117, v29\n"
        "v_cvt_pk_bf16_f32 v145, v113, v97\n"
        "v_mfma_f32_16x16x32_bf16 v[60:63], v[24:27], v[132:135], 0\n"
        "v_cvt_pk_bf16_f32 v146, v53, v57\n"
        "v_cvt_pk_bf16_f32 v148, v118, v30\n"
        "v_smfmac_f32_16x16x64_bf16 v[60:63], v[32:35], v[36:43], v4\n"
        "buffer_store_dwordx3 v[136:138], v13, s[8:11], s37 offen offset:1152 sc0 sc1 nt\n"
        "v_cvt_pk_bf16_f32 v149, v114, v98\n"
        "v_cvt_pk_bf16_f32 v150, v54, v58\n"
        "v_cvt_pk_bf16_f32 v136, v119, v31\n"
        "v_cvt_pk_bf16_f32 v137, v115, v99\n"
        "v_cvt_pk_bf16_f32 v138, v55, v59\n"
        "v_mfma_f32_16x16x32_bf16 v[32:35], v[212:215], v[100:103], 0\n"
        "s_add_u32 s37, s36, 294336\n"
        "ds_read_b64 v[68:69], v11 offset:192\n"
        "v_mfma_f32_16x16x32_bf16 v[32:35], v[208:211], v[92:95], v[32:35]\n"
        "ds_read_b32 v123, v10\n"
        "v_mfma_f32_16x16x32_bf16 v[32:35], v[204:207], v[88:91], v[32:35]\n"
        "v_mfma_f32_16x16x32_bf16 v[24:27], v[216:219], v[88:91], 0\n"
        "v_mfma_f32_16x16x32_bf16 v[24:27], v[220:223], v[92:95], v[24:27]\n"
        "v_mfma_f32_16x16x32_bf16 v[24:27], v[224:227], v[100:103], v[24:27]\n"
        "buffer_store_dwordx3 v[160:162], v12, s[8:11], s37 offen offset:576 sc0 sc1 nt\n"
        "s_add_u32 s37, s36, 392448\n"
        "s_waitcnt lgkmcnt(1)\n"
        "v_add_f32_dpp v69, v22, v23 quad_perm:[0,1,2,3] row_mask:0x1 bank_mask:0xf\n"
        "v_add_f32_dpp v68, v20, v21 quad_perm:[0,1,2,3] row_mask:0x1 bank_mask:0xf\n"
        "v_cvt_pk_bf16_f32 v130, v32, v33\n"
        "v_mfma_f32_16x16x32_bf16 v[20:23], v[236:239], v[100:103], 0\n"
        "v_cvt_pk_bf16_f32 v131, v34, v35\n"
        "v_cvt_pk_bf16_f32 v56, v24, v25\n"
        "v_mfma_f32_16x16x32_bf16 v[20:23], v[232:235], v[92:95], v[20:23]\n"
        "v_cvt_pk_bf16_f32 v57, v26, v27\n"
        "v_add_f32_dpp v69, v50, v51 quad_perm:[0,1,2,3] row_mask:0x2 bank_mask:0xf\n"
        "v_mfma_f32_16x16x32_bf16 v[20:23], v[228:231], v[88:91], v[20:23]\n"
        "buffer_store_dwordx3 v[140:142], v12, s[8:11], s37 offen sc0 sc1 nt\n"
        "v_add_f32_dpp v68, v48, v49 quad_perm:[0,1,2,3] row_mask:0x2 bank_mask:0xf\n"
        "v_add_f32_dpp v69, v62, v63 quad_perm:[0,1,2,3] row_mask:0x4 bank_mask:0xf\n"
        "v_mul_f32 v167, s52, v69\n"
        "v_add_f32_dpp v68, v60, v61 quad_perm:[0,1,2,3] row_mask:0x4 bank_mask:0xf\n"
        "s_waitcnt lgkmcnt(0)\n"
        "v_fma_f32 v167, -v167, v69, v123\n"
        "v_add_f32_e64 v167, s53, |v167|\n"
        "v_cvt_pk_bf16_f32 v59, v22, v23\n"
        "v_cvt_pk_bf16_f32 v58, v20, v21\n"
        "v_mfma_f32_16x16x32_bf16 v[140:143], v[88:91], v[124:127], 0\n"
        "v_rsq_f32 v123, v167\n"
        "v_mfma_f32_16x16x32_bf16 v[140:143], v[92:95], v[128:131], v[140:143]\n"
        "s_add_u32 s37, s36, 392448\n"
        "ds_read_b128 v[116:119], v8 offset:128\n"
        "v_and_b32 v30, s28, v123\n"
        "v_mfma_f32_16x16x32_bf16 v[140:143], v[100:103], v[56:59], v[140:143]\n"
        "ds_read2_b32 v[48:49], v6 offset0:0 offset1:2\n"
        "ds_read2_b32 v[50:51], v6 offset0:8 offset1:10\n"
        "v_mfma_f32_16x16x32_bf16 v[44:47], v[84:87], v[56:59], 0\n"
        "v_and_b32 v28, s28, v123\n"
        "v_sub_f32 v167, v123, v30\n"
        "v_mfma_f32_16x16x32_bf16 v[44:47], v[80:83], v[128:131], v[44:47]\n"
        "v_cvt_pk_bf16_f32 v31, v167, v30\n"
        "v_cvt_pk_bf16_f32 v29, v28, v167\n"
        "v_mfma_f32_16x16x32_bf16 v[44:47], v[76:79], v[124:127], v[44:47]\n"
        "buffer_store_dwordx3 v[164:166], v15, s[8:11], s37 offen offset:768 sc0 sc1 nt\n"
        "v_mul_f32 v68, v68, v123\n"
        "v_readlane_b32 s58, v123, 56\n"
        "s_mov_b64 exec, s[56:57]\n"
        "v_mov_b32 v68, 0xff800000\n"
        "s_mov_b64 exec, -1\n"
        "v_mfma_f32_16x16x4_4b_bf16 v[152:167], v[30:31], v[28:29], 0 blgp:4\n"
        "v_mul_f32 v68, s58, v68\n"
        "v_exp_f32 v68, v68\n"
        "v_mfma_f32_16x16x32_bf16 v[96:99], v[36:39], v[124:127], 0\n"
        "s_add_u32 s37, s36, 490560\n"
        "v_add_f32_dpp v139, v68, v68 row_ror:8 row_mask:0xf bank_mask:0xf\n"
        "v_mul_f32 v68, v68, v123\n"
        "v_mfma_f32_16x16x32_bf16 v[96:99], v[40:43], v[128:131], v[96:99]\n"
        "v_add_f32_dpp v139, v139, v139 row_ror:4 row_mask:0xf bank_mask:0xf\n"
        "v_mfma_f32_16x16x32_bf16 v[96:99], v[132:135], v[56:59], v[96:99]\n"
        "s_waitcnt lgkmcnt(0)\n"
        "v_add_f32_dpp v139, v139, v139 row_ror:2 row_mask:0xf bank_mask:0xf\n"
        "v_mfma_f32_16x16x32_bf16 v[112:115], v[116:119], v[56:59], 0\n"
        "v_mul_f32 v140, v140, v152\n"
        "v_mul_f32 v141, v141, v153\n"
        "v_smfmac_f32_16x16x64_bf16 v[112:115], v[48:51], v[124:131], v4\n"
        "buffer_store_dwordx3 v[144:146], v12, s[8:11], s37 offen offset:192 sc0 sc1 nt\n"
        "v_mul_f32 v142, v142, v154\n"
        "v_mul_f32 v143, v143, v155\n"
        "v_exp_f32 v140, v140\n"
        "v_mul_f32 v44, v44, v156\n"
        "v_mfma_f32_16x16x32_bf16 v[24:27], v[168:171], v[76:79], 0\n"
        "s_add_u32 s37, s36, 490560\n"
        "v_exp_f32 v141, v141\n"
        "v_mfma_f32_16x16x32_bf16 v[24:27], v[172:175], v[80:83], v[24:27]\n"
        "v_exp_f32 v142, v142\n"
        "v_mfma_f32_16x16x32_bf16 v[24:27], v[176:179], v[84:87], v[24:27]\n"
        "v_exp_f32 v143, v143\n"
        "v_add_f32 v125, v140, v141\n"
        "v_mfma_f32_16x16x32_bf16 v[20:23], v[188:191], v[84:87], 0\n"
        "v_add_f32 v125, v125, v142\n"
        "v_add_f32 v125, v125, v143\n"
        "v_mfma_f32_16x16x32_bf16 v[20:23], v[184:187], v[80:83], v[20:23]\n"
        "v_mul_f32 v45, v45, v157\n"
        "v_mul_f32 v46, v46, v158\n"
        "v_mfma_f32_16x16x32_bf16 v[20:23], v[180:183], v[76:79], v[20:23]\n"
        "buffer_store_dwordx3 v[148:150], v14, s[8:11], s37 offen offset:960 sc0 sc1 nt\n"
        "v_cvt_pk_bf16_f32 v60, v24, v25\n"
        "v_cvt_pk_bf16_f32 v61, v26, v27\n"
        "v_mul_f32 v47, v47, v159\n"
        "v_exp_f32 v44, v44\n"
        "v_mfma_f32_16x16x32_bf16 v[24:27], v[192:195], v[76:79], 0\n"
        "s_add_u32 s37, s36, 588672\n"
        "v_exp_f32 v45, v45\n"
        "v_mfma_f32_16x16x32_bf16 v[24:27], v[196:199], v[80:83], v[24:27]\n"
        "v_cvt_pk_bf16_f32 v62, v20, v21\n"
        "v_cvt_pk_bf16_f32 v63, v22, v23\n"
        "v_mfma_f32_16x16x32_bf16 v[24:27], v[200:203], v[84:87], v[24:27]\n"
        "buffer_store_dwordx3 v[136:138], v12, s[8:11], s37 offen offset:384 sc0 sc1 nt\n"
        "v_exp_f32 v46, v46\n"
        "v_exp_f32 v47, v47\n"
        "v_add_f32 v125, v125, v44\n"
        "v_add_f32 v125, v125, v45\n"
        "v_mfma_f32_16x16x32_bf16 v[20:23], v[212:215], v[84:87], 0\n"
        "v_add_f32 v125, v125, v46\n"
        "v_add_f32 v125, v125, v47\n"
        "v_mfma_f32_16x16x32_bf16 v[20:23], v[208:211], v[80:83], v[20:23]\n"
        "v_cvt_pk_bf16_f32 v64, v24, v25\n"
        "v_cvt_pk_bf16_f32 v65, v26, v27\n"
        "v_mfma_f32_16x16x32_bf16 v[20:23], v[204:207], v[76:79], v[20:23]\n"
        "v_mul_f32 v96, v96, v160\n"
        "v_mul_f32 v97, v97, v161\n"
        "v_mul_f32 v98, v98, v162\n"
        "v_mfma_f32_16x16x32_bf16 v[24:27], v[216:219], v[76:79], 0\n"
        "v_mul_f32 v99, v99, v163\n"
        "v_add_f32 v112, v112, v113\n"
        "v_mfma_f32_16x16x32_bf16 v[24:27], v[220:223], v[80:83], v[24:27]\n"
        "v_exp_f32 v96, v96\n"
        "v_mfma_f32_16x16x32_bf16 v[24:27], v[224:227], v[84:87], v[24:27]\n"
        "v_cvt_pk_bf16_f32 v66, v20, v21\n"
        "v_cvt_pk_bf16_f32 v67, v22, v23\n"
        "v_add_f32 v125, v125, v96\n"
        "v_mfma_f32_16x16x32_bf16 v[32:35], v[236:239], v[84:87], 0\n"
        "v_exp_f32 v97, v97\n"
        "v_mfma_f32_16x16x32_bf16 v[32:35], v[232:235], v[80:83], v[32:35]\n"
        "v_exp_f32 v98, v98\n"
        "v_mfma_f32_16x16x32_bf16 v[32:35], v[228:231], v[76:79], v[32:35]\n"
        "v_cvt_pk_bf16_f32 v20, v24, v25\n"
        "v_cvt_pk_bf16_f32 v21, v26, v27\n"
        "v_mfma_f32_16x16x4_4b_bf16 v[148:163], v[30:31], v[28:29], 0 blgp:5\n"
        "v_exp_f32 v99, v99\n"
        "v_add_f32 v125, v125, v97\n"
        "v_add_f32 v125, v125, v98\n"
        "v_mul_f32 v112, v112, v164\n"
        "v_exp_f32 v112, v112\n"
        "v_cvt_pk_bf16_f32 v22, v32, v33\n"
        "v_cvt_pk_bf16_f32 v23, v34, v35\n"
        "v_mfma_f32_16x16x32_bf16 v[164:167], v[88:91], v[60:63], 0\n"
        "v_add_f32 v125, v125, v99\n"
        "v_add_f32_dpp v139, v139, v139 row_ror:1 row_mask:0xf bank_mask:0xf\n"
        "v_mfma_f32_16x16x32_bf16 v[164:167], v[92:95], v[64:67], v[164:167]\n"
        "v_add_f32_dpp v125, v112, v125 quad_perm:[0,1,2,3] row_mask:0x4 bank_mask:0xf\n"
        "v_mfma_f32_16x16x32_bf16 v[164:167], v[100:103], v[20:23], v[164:167]\n"
        "v_mfma_f32_16x16x32_bf16 v[144:147], v[84:87], v[20:23], 0\n"
        "v_permlane32_swap_b32 v125, v139\n"
        "v_mfma_f32_16x16x32_bf16 v[144:147], v[80:83], v[64:67], v[144:147]\n"
        "v_add_f32 v125, v125, v139\n"
        "v_mov_b32 v163, v125\n"
        "v_mfma_f32_16x16x32_bf16 v[144:147], v[76:79], v[60:63], v[144:147]\n"
        "v_mfma_f32_16x16x32_bf16 v[108:111], v[36:39], v[60:63], 0\n"
        "v_permlane16_swap_b32 v125, v163\n"
        "v_mfma_f32_16x16x32_bf16 v[108:111], v[40:43], v[64:67], v[108:111]\n"
        "v_add_f32 v125, v125, v163\n"
        "v_mul_f32 v164, v164, v148\n"
        "v_mfma_f32_16x16x32_bf16 v[108:111], v[132:135], v[20:23], v[108:111]\n"
        "v_rcp_f32 v130, v125\n"
        "v_mul_f32 v165, v165, v149\n"
        "v_and_b32 v138, s28, v130\n"
        "v_mfma_f32_16x16x32_bf16 v[70:73], v[116:119], v[20:23], 0\n"
        "v_sub_f32 v149, v130, v138\n"
        "v_cvt_pk_bf16_f32 v139, v138, v149\n"
        "v_smfmac_f32_16x16x64_bf16 v[70:73], v[48:51], v[60:67], v4\n"
        "v_mul_f32 v166, v166, v150\n"
        "v_mfma_f32_16x16x32_bf16 v[32:35], v[168:171], v[36:39], 0\n"
        "v_mul_f32 v167, v167, v151\n"
        "v_mul_f32 v144, v144, v152\n"
        "v_mfma_f32_16x16x32_bf16 v[32:35], v[172:175], v[40:43], v[32:35]\n"
        "v_exp_f32 v164, v164\n"
        "v_mfma_f32_16x16x32_bf16 v[32:35], v[176:179], v[132:135], v[32:35]\n"
        "v_exp_f32 v165, v165\n"
        "v_exp_f32 v166, v166\n"
        "v_mfma_f32_16x16x32_bf16 v[24:27], v[188:191], v[132:135], 0\n"
        "v_exp_f32 v167, v167\n"
        "v_mfma_f32_16x16x32_bf16 v[24:27], v[184:187], v[40:43], v[24:27]\n"
        "v_add_f32 v69, v164, v165\n"
        "v_add_f32 v69, v69, v166\n"
        "v_mfma_f32_16x16x32_bf16 v[24:27], v[180:183], v[36:39], v[24:27]\n"
        "v_cvt_pk_bf16_f32 v20, v32, v33\n"
        "v_cvt_pk_bf16_f32 v21, v34, v35\n"
        "v_add_f32 v69, v69, v167\n"
        "v_mfma_f32_16x16x32_bf16 v[56:59], v[192:195], v[36:39], 0\n"
        "v_mul_f32 v145, v145, v153\n"
        "v_mul_f32 v146, v146, v154\n"
        "v_mfma_f32_16x16x32_bf16 v[56:59], v[196:199], v[40:43], v[56:59]\n"
        "v_mul_f32 v147, v147, v155\n"
        "v_cvt_pk_bf16_f32 v22, v24, v25\n"
        "v_mfma_f32_16x16x32_bf16 v[56:59], v[200:203], v[132:135], v[56:59]\n"
        "v_cvt_pk_bf16_f32 v23, v26, v27\n"
        "v_exp_f32 v144, v144\n"
        "v_mul_f32 v108, v108, v156\n"
        "v_mfma_f32_16x16x32_bf16 v[32:35], v[212:215], v[132:135], 0\n"
        "v_exp_f32 v145, v145\n"
        "v_mfma_f32_16x16x32_bf16 v[32:35], v[208:211], v[40:43], v[32:35]\n"
        "v_exp_f32 v146, v146\n"
        "v_mfma_f32_16x16x32_bf16 v[32:35], v[204:207], v[36:39], v[32:35]\n"
        "v_cvt_pk_bf16_f32 v24, v56, v57\n"
        "v_cvt_pk_bf16_f32 v25, v58, v59\n"
        "v_add_f32 v69, v69, v144\n"
        "v_mfma_f32_16x16x32_bf16 v[56:59], v[216:219], v[36:39], 0\n"
        "v_exp_f32 v147, v147\n"
        "v_mfma_f32_16x16x32_bf16 v[56:59], v[220:223], v[40:43], v[56:59]\n"
        "v_add_f32 v69, v69, v145\n"
        "v_add_f32 v69, v69, v146\n"
        "v_mfma_f32_16x16x32_bf16 v[56:59], v[224:227], v[132:135], v[56:59]\n"
        "v_cvt_pk_bf16_f32 v26, v32, v33\n"
        "v_cvt_pk_bf16_f32 v27, v34, v35\n"
        "v_add_f32 v69, v69, v147\n"
        "v_mul_f32 v109, v109, v157\n"
        "v_mfma_f32_16x16x32_bf16 v[32:35], v[236:239], v[132:135], 0\n"
        "v_mul_f32 v110, v110, v158\n"
        "v_mul_f32 v111, v111, v159\n"
        "v_mfma_f32_16x16x32_bf16 v[32:35], v[232:235], v[40:43], v[32:35]\n"
        "v_cvt_pk_bf16_f32 v136, v56, v57\n"
        "v_cvt_pk_bf16_f32 v137, v58, v59\n"
        "v_mfma_f32_16x16x32_bf16 v[32:35], v[228:231], v[36:39], v[32:35]\n"
        "v_exp_f32 v108, v108\n"
        "v_mfma_f32_16x16x4_4b_bf16 v[52:67], v[30:31], v[138:139], 0 blgp:4\n"
        "v_exp_f32 v109, v109\n"
        "v_exp_f32 v110, v110\n"
        "v_exp_f32 v111, v111\n"
        "v_add_f32 v69, v69, v108\n"
        "v_add_f32 v69, v69, v109\n"
        "v_add_f32 v69, v69, v110\n"
        "v_cvt_pk_bf16_f32 v138, v32, v33\n"
        "v_cvt_pk_bf16_f32 v139, v34, v35\n"
        "v_mfma_f32_16x16x32_bf16 v[104:107], v[88:91], v[20:23], 0\n"
        "v_add_f32 v69, v69, v111\n"
        "v_add_f32 v70, v70, v71\n"
        "v_mfma_f32_16x16x32_bf16 v[104:107], v[92:95], v[24:27], v[104:107]\n"
        "v_mul_f32 v140, v140, v52\n"
        "v_mul_f32 v141, v141, v53\n"
        "v_mfma_f32_16x16x32_bf16 v[104:107], v[100:103], v[136:139], v[104:107]\n"
        "v_mul_f32 v142, v142, v54\n"
        "v_mul_f32 v143, v143, v55\n"
        "v_mul_f32 v44, v44, v56\n"
        "v_mul_f32 v45, v45, v57\n"
        "v_mfma_f32_16x16x32_bf16 v[126:129], v[84:87], v[136:139], 0\n"
        "v_mul_f32 v70, v70, v160\n"
        "v_mul_f32 v46, v46, v58\n"
        "v_mfma_f32_16x16x32_bf16 v[126:129], v[80:83], v[24:27], v[126:129]\n"
        "v_exp_f32 v70, v70\n"
        "v_mfma_f32_16x16x32_bf16 v[126:129], v[76:79], v[20:23], v[126:129]\n"
        "v_mul_f32 v47, v47, v59\n"
        "v_add_f32_dpp v69, v70, v69 quad_perm:[0,1,2,3] row_mask:0x4 bank_mask:0xf\n"
        "v_mul_f32 v96, v96, v60\n"
        "v_mfma_f32_16x16x32_bf16 v[152:155], v[36:39], v[20:23], 0\n"
        "v_mul_f32 v97, v97, v61\n"
        "v_mul_f32 v98, v98, v62\n"
        "v_mfma_f32_16x16x32_bf16 v[152:155], v[40:43], v[24:27], v[152:155]\n"
        "v_mul_f32 v99, v99, v63\n"
        "v_fma_f32 v112, v112, v64, v19\n"
        "v_mfma_f32_16x16x32_bf16 v[152:155], v[132:135], v[136:139], v[152:155]\n"
        "v_cvt_pk_bf16_f32 v148, v140, v141\n"
        "v_mfma_f32_16x16x4_4b_bf16 v[52:67], v[30:31], v[28:29], 0 blgp:6\n"
        "v_cvt_pk_bf16_f32 v149, v142, v143\n"
        "v_cvt_pk_bf16_f32 v150, v44, v45\n"
        "v_cvt_pk_bf16_f32 v151, v46, v47\n"
        "v_cvt_pk_bf16_f32 v140, v96, v97\n"
        "v_cvt_pk_bf16_f32 v142, v98, v99\n"
        "v_cvt_pk_bf16_f32 v141, v112, 0\n"
        "v_min3_f32 v135, v125, v130, v123\n"
        "v_readlane_b32 s59, v130, 32\n"
        "v_mfma_f32_16x16x32_bf16 v[44:47], v[116:119], v[136:139], 0\n"
        "ds_read_b128 v[40:43], v17 offset:40064\n"
        "v_mul_f32 v68, s59, v68\n"
        "v_smfmac_f32_16x16x64_bf16 v[44:47], v[48:51], v[20:27], v4\n"
        "ds_read_b128 v[72:75], v17 offset:40128\n"
        "ds_read_b128 v[136:139], v17 offset:40192\n"
        "v_mfma_f32_16x16x32_bf16 v[24:27], v[88:91], v[240:243], 0\n"
        "v_mul_f32 v104, v104, v52\n"
        "v_mul_f32 v105, v105, v53\n"
        "v_mfma_f32_16x16x32_bf16 v[24:27], v[92:95], v[244:247], v[24:27]\n"
        "ds_read_b128 v[116:119], v0 offset:5136\n"
        "v_mul_f32 v106, v106, v54\n"
        "v_mfma_f32_16x16x32_bf16 v[24:27], v[100:103], v[248:251], v[24:27]\n"
        "ds_read_b128 v[112:115], v0 offset:4112\n"
        "v_mul_f32 v107, v107, v55\n"
        "v_mfma_f32_16x16x32_bf16 v[20:23], v[84:87], v[248:251], 0\n"
        "v_exp_f32 v104, v104\n"
        "v_mfma_f32_16x16x32_bf16 v[20:23], v[80:83], v[244:247], v[20:23]\n"
        "v_exp_f32 v105, v105\n"
        "v_mfma_f32_16x16x32_bf16 v[20:23], v[76:79], v[240:243], v[20:23]\n"
        "s_waitcnt lgkmcnt(2)\n"
        "v_cvt_pk_bf16_f32 v156, v24, v25\n"
        "v_cvt_pk_bf16_f32 v157, v26, v27\n"
        "v_exp_f32 v106, v106\n"
        "v_mfma_f32_16x16x32_bf16 v[36:39], v[40:43], v[240:243], 0\n"
        "v_exp_f32 v107, v107\n"
        "v_mfma_f32_16x16x32_bf16 v[36:39], v[72:75], v[244:247], v[36:39]\n"
        "v_add_f32 v143, v104, v105\n"
        "v_cvt_pk_bf16_f32 v158, v20, v21\n"
        "v_mfma_f32_16x16x32_bf16 v[36:39], v[136:139], v[248:251], v[36:39]\n"
        "s_waitcnt lgkmcnt(0)\n"
        "v_cvt_pk_bf16_f32 v159, v22, v23\n"
        "v_add_f32 v143, v143, v106\n"
        "v_add_f32 v143, v143, v107\n"
        "v_mfma_f32_16x16x32_bf16 v[52:55], v[136:139], v[116:119], 0\n"
        "v_mul_f32 v126, v126, v56\n"
        "v_mul_f32 v127, v127, v57\n"
        "v_mfma_f32_16x16x32_bf16 v[52:55], v[72:75], v[112:115], v[52:55]\n"
        "ds_read_b128 v[122:125], v0 offset:6176\n"
        "v_cvt_pk_bf16_f32 v46, v36, v37\n"
        "v_mfma_f32_16x16x32_bf16 v[52:55], v[40:43], v[252:255], v[52:55]\n"
        "ds_read_b128 v[26:29], v0 offset:7200\n"
        "ds_read_b128 v[130:133], v0 offset:8224\n"
        "v_mfma_f32_16x16x32_bf16 v[32:35], v[76:79], v[252:255], 0\n"
        "v_cvt_pk_bf16_f32 v50, v38, v39\n"
        "v_mul_f32 v128, v128, v58\n"
        "v_mfma_f32_16x16x32_bf16 v[32:35], v[80:83], v[112:115], v[32:35]\n"
        "v_mul_f32 v129, v129, v59\n"
        "v_mul_f32 v152, v152, v60\n"
        "v_mfma_f32_16x16x32_bf16 v[32:35], v[84:87], v[116:119], v[32:35]\n"
        "v_cvt_pk_bf16_f32 v20, v52, v53\n"
        "v_cvt_pk_bf16_f32 v24, v54, v55\n"
        "v_exp_f32 v126, v126\n"
        "v_mfma_f32_16x16x32_bf16 v[36:39], v[100:103], v[116:119], 0\n"
        "v_exp_f32 v127, v127\n"
        "v_mfma_f32_16x16x32_bf16 v[36:39], v[92:95], v[112:115], v[36:39]\n"
        "v_exp_f32 v128, v128\n"
        "v_mfma_f32_16x16x32_bf16 v[36:39], v[88:91], v[252:255], v[36:39]\n"
        "s_waitcnt lgkmcnt(0)\n"
        "v_cvt_pk_bf16_f32 v56, v32, v33\n"
        "v_cvt_pk_bf16_f32 v57, v34, v35\n"
        "v_add_f32 v143, v143, v126\n"
        "v_mfma_f32_16x16x32_bf16 v[112:115], v[88:91], v[122:125], 0\n"
        "v_exp_f32 v129, v129\n"
        "v_mfma_f32_16x16x32_bf16 v[112:115], v[92:95], v[26:29], v[112:115]\n"
        "ds_read_b128 v[118:121], v0 offset:11312\n"
        "v_cvt_pk_bf16_f32 v54, v36, v37\n"
        "v_mfma_f32_16x16x32_bf16 v[112:115], v[100:103], v[130:133], v[112:115]\n"
        "ds_read_b128 v[160:163], v0 offset:10288\n"
        "ds_read_b128 v[32:35], v0 offset:9264\n"
        "v_mfma_f32_16x16x32_bf16 v[96:99], v[84:87], v[130:133], 0\n"
        "v_cvt_pk_bf16_f32 v55, v38, v39\n"
        "v_add_f32 v143, v143, v127\n"
        "v_mfma_f32_16x16x32_bf16 v[96:99], v[80:83], v[26:29], v[96:99]\n"
        "v_add_f32 v143, v143, v128\n"
        "v_add_f32 v143, v143, v129\n"
        "v_mfma_f32_16x16x32_bf16 v[96:99], v[76:79], v[122:125], v[96:99]\n"
        "v_cvt_pk_bf16_f32 v36, v112, v113\n"
        "v_cvt_pk_bf16_f32 v37, v114, v115\n"
        "v_mul_f32 v153, v153, v61\n"
        "v_mul_f32 v154, v154, v62\n"
        "v_mfma_f32_16x16x32_bf16 v[112:115], v[40:43], v[122:125], 0\n"
        "v_mul_f32 v155, v155, v63\n"
        "v_add_f32 v44, v44, v45\n"
        "v_mfma_f32_16x16x32_bf16 v[112:115], v[72:75], v[26:29], v[112:115]\n"
        "v_cvt_pk_bf16_f32 v38, v96, v97\n"
        "v_cvt_pk_bf16_f32 v39, v98, v99\n"
        "v_mfma_f32_16x16x32_bf16 v[112:115], v[136:139], v[130:133], v[112:115]\n"
        "s_waitcnt lgkmcnt(0)\n"
        "v_exp_f32 v152, v152\n"
        "v_mul_f32 v44, v44, v64\n"
        "v_mfma_f32_16x16x32_bf16 v[64:67], v[136:139], v[118:121], 0\n"
        "v_exp_f32 v153, v153\n"
        "v_mfma_f32_16x16x32_bf16 v[64:67], v[72:75], v[160:163], v[64:67]\n"
        "ds_read_b128 v[96:99], v0 offset:12352\n"
        "v_add_f32 v143, v143, v152\n"
        "v_mfma_f32_16x16x32_bf16 v[64:67], v[40:43], v[32:35], v[64:67]\n"
        "ds_read_b128 v[60:63], v0 offset:13376\n"
        "ds_read_b128 v[26:29], v0 offset:14400\n"
        "v_mfma_f32_16x16x32_bf16 v[122:125], v[76:79], v[32:35], 0\n"
        "v_cvt_pk_bf16_f32 v130, v112, v113\n"
        "v_cvt_pk_bf16_f32 v134, v114, v115\n"
        "v_mfma_f32_16x16x32_bf16 v[122:125], v[80:83], v[160:163], v[122:125]\n"
        "v_exp_f32 v154, v154\n"
        "v_mfma_f32_16x16x32_bf16 v[122:125], v[84:87], v[118:121], v[122:125]\n"
        "v_exp_f32 v155, v155\n"
        "v_add_f32 v143, v143, v153\n"
        "v_mfma_f32_16x16x32_bf16 v[114:117], v[100:103], v[118:121], 0\n"
        "v_add_f32 v143, v143, v154\n"
        "v_add_f32 v143, v143, v155\n"
        "v_mfma_f32_16x16x32_bf16 v[114:117], v[92:95], v[160:163], v[114:117]\n"
        "v_cvt_pk_bf16_f32 v47, v64, v65\n"
        "v_cvt_pk_bf16_f32 v51, v66, v67\n"
        "v_mfma_f32_16x16x32_bf16 v[114:117], v[88:91], v[32:35], v[114:117]\n"
        "s_waitcnt lgkmcnt(0)\n"
        "v_cvt_pk_bf16_f32 v162, v122, v123\n"
        "v_cvt_pk_bf16_f32 v163, v124, v125\n"
        "v_exp_f32 v44, v44\n"
        "v_mfma_f32_16x16x32_bf16 v[118:121], v[88:91], v[96:99], 0\n"
        "v_cvt_pk_bf16_f32 v68, v68, 0\n"
        "v_add_f32_dpp v143, v44, v143 quad_perm:[0,1,2,3] row_mask:0x4 bank_mask:0xf\n"
        "v_mfma_f32_16x16x32_bf16 v[118:121], v[92:95], v[60:63], v[118:121]\n"
        "ds_read_b128 v[32:35], v0 offset:17488\n"
        "v_cvt_pk_bf16_f32 v160, v114, v115\n"
        "v_mfma_f32_16x16x32_bf16 v[118:121], v[100:103], v[26:29], v[118:121]\n"
        "ds_read_b128 v[112:115], v0 offset:16464\n"
        "ds_read_b128 v[64:67], v0 offset:15440\n"
        "v_mfma_f32_16x16x32_bf16 v[122:125], v[84:87], v[26:29], 0\n"
        "v_cvt_pk_bf16_f32 v161, v116, v117\n"
        "v_mfma_f32_16x16x32_bf16 v[122:125], v[80:83], v[60:63], v[122:125]\n"
        "v_permlane32_swap_b32 v69, v143\n"
        "v_mfma_f32_16x16x32_bf16 v[122:125], v[76:79], v[96:99], v[122:125]\n"
        "v_add_f32 v69, v69, v143\n"
        "v_mov_b32 v133, v69\n"
        "v_cvt_pk_bf16_f32 v58, v118, v119\n"
        "v_mfma_f32_16x16x32_bf16 v[116:119], v[40:43], v[96:99], 0\n"
        "ds_read2_b32 v[52:53], v11 offset0:0 offset1:3\n"
        "v_cvt_pk_bf16_f32 v59, v120, v121\n"
        "v_mfma_f32_16x16x32_bf16 v[116:119], v[72:75], v[60:63], v[116:119]\n"
        "ds_read_b64_tr_b16 v[48:49], v9 offset:40064\n"
        "v_cvt_pk_bf16_f32 v60, v122, v123\n"
        "v_mfma_f32_16x16x32_bf16 v[116:119], v[136:139], v[26:29], v[116:119]\n"
        "s_waitcnt lgkmcnt(2)\n"
        "ds_read2_b32 v[26:27], v11 offset0:1 offset1:4\n"
        "v_permlane16_swap_b32 v69, v133\n"
        "v_add_f32 v69, v69, v133\n"
        "v_mfma_f32_16x16x32_bf16 v[96:99], v[136:139], v[32:35], 0\n"
        "ds_read_b64_tr_b16 v[22:23], v9 offset:40080\n"
        "v_cvt_pk_bf16_f32 v61, v124, v125\n"
        "v_mfma_f32_16x16x32_bf16 v[96:99], v[72:75], v[112:115], v[96:99]\n"
        "ds_read2_b32 v[136:137], v11 offset0:2 offset1:5\n"
        "v_cvt_pk_bf16_f32 v21, v116, v117\n"
        "v_mfma_f32_16x16x32_bf16 v[96:99], v[40:43], v[64:67], v[96:99]\n"
        "ds_read_b64_tr_b16 v[132:133], v9 offset:40096\n"
        "v_rcp_f32 v125, v69\n"
        "v_cvt_pk_bf16_f32 v25, v118, v119\n"
        "v_mfma_f32_16x16x32_bf16 v[116:119], v[76:79], v[64:67], 0\n"
        "v_and_b32 v138, s28, v125\n"
        "v_sub_f32 v143, v125, v138\n"
        "v_mfma_f32_16x16x32_bf16 v[116:119], v[80:83], v[112:115], v[116:119]\n"
        "v_cvt_pk_bf16_f32 v139, v138, v143\n"
        "v_mov_b32_dpp v143, v19 row_shr:4 row_mask:0xf bank_mask:0xf bound_ctrl:0\n"
        "v_mfma_f32_16x16x32_bf16 v[116:119], v[84:87], v[32:35], v[116:119]\n"
        "v_min3_f32 v135, v135, v69, v125\n"
        "v_mfma_f32_16x16x4_4b_bf16 v[72:87], v[30:31], v[138:139], 0 blgp:4\n"
        "v_cmp_nle_f32 vcc, 0x2b800000, v135\n"
        "ds_write_b16 v1, v68\n"
        "v_cvt_pk_bf16_f32 v131, v96, v97\n"
        "v_cvt_pk_bf16_f32 v135, v98, v99\n"
        "s_andn2_b64 vcc, vcc, s[56:57]\n"
        "s_cbranch_scc1 CREDOB_%=\n"
        "v_cvt_pk_bf16_f32 v42, v116, v117\n"
        "v_cvt_pk_bf16_f32 v43, v118, v119\n"
        "v_mfma_f32_16x16x32_bf16 v[96:99], v[100:103], v[32:35], 0\n"
        "s_mov_b32 exec_hi, 0xffff\n"
        "v_add_u32 v10, -40064, v10\n"
        "s_mov_b32 exec_hi, -1\n"
        "v_mfma_f32_16x16x32_bf16 v[96:99], v[92:95], v[112:115], v[96:99]\n"
        "s_add_u32 s31, s31, 1\n"
        "v_mul_f32 v164, v164, v72\n"
        "v_mul_f32 v165, v165, v73\n"
        "v_mfma_f32_16x16x32_bf16 v[96:99], v[88:91], v[64:67], v[96:99]\n"
        "s_mul_i32 s38, s31, 1344\n"
        "v_mul_f32 v166, v166, v74\n"
        "v_mul_f32 v167, v167, v75\n"
        "v_mfma_f32_16x16x32_bf16 v[112:115], v[148:151], v[156:159], 0\n"
        "s_waitcnt lgkmcnt(1)\n"
        "v_mul_f32 v144, v144, v76\n"
        "v_mul_f32 v145, v145, v77\n"
        "v_smfmac_f32_16x16x64_bf16 v[112:115], v[140:143], v[46:53], v18\n"
        "s_cmp_ge_u32 s31, s76\n"
        "s_cbranch_scc1 L33B_%=\n"
        "L34B_%=:\n"
        "ds_read_b64_tr_b16 v[48:49], v9 offset:40112\n"
        "v_mul_f32 v146, v146, v78\n"
        "v_mfma_f32_16x16x32_bf16 v[122:125], v[148:151], v[54:57], 0\n"
        "s_cmp_ge_u32 s31, s71\n"
        "v_mul_f32 v147, v147, v79\n"
        "v_mul_f32 v108, v108, v80\n"
        "v_smfmac_f32_16x16x64_bf16 v[122:125], v[140:143], v[20:27], v18\n"
        "s_cselect_b32 s39, s70, s72\n"
        "ds_read_b64_tr_b16 v[22:23], v9 offset:40128\n"
        "v_mfma_f32_16x16x32_bf16 v[76:79], v[148:151], v[36:39], 0\n"
        "s_cmp_ge_u32 s31, 20\n"
        "ds_read_b128 v[72:75], v2\n"
        "v_cvt_pk_bf16_f32 v40, v96, v97\n"
        "v_smfmac_f32_16x16x64_bf16 v[76:79], v[140:143], v[130:137], v18\n"
        "s_cselect_b32 s39, s22, s39\n"
        "ds_read_b64_tr_b16 v[132:133], v9 offset:40144\n"
        "ds_read_b128 v[32:35], v3\n"
        "v_cvt_pk_bf16_f32 v41, v98, v99\n"
        "v_mfma_f32_16x16x32_bf16 v[116:119], v[148:151], v[160:163], 0\n"
        "s_cmp_ge_u32 s31, 21\n"
        "v_mul_f32 v109, v109, v81\n"
        "s_waitcnt lgkmcnt(4)\n"
        "v_smfmac_f32_16x16x64_bf16 v[116:119], v[140:143], v[46:53], v18 abid:1\n"
        "s_cselect_b32 s39, s23, s39\n"
        "v_mul_f32 v110, v110, v82\n"
        "v_mfma_f32_16x16x4_4b_bf16 v[88:103], v[30:31], v[138:139], 0 blgp:6\n"
        "v_mul_f32 v111, v111, v83\n"
        "v_fma_f32 v70, v70, v84, v19\n"
        "v_cvt_pk_bf16_f32 v80, v164, v165\n"
        "v_cvt_pk_bf16_f32 v81, v166, v167\n"
        "v_cvt_pk_bf16_f32 v82, v144, v145\n"
        "v_cvt_pk_bf16_f32 v83, v146, v147\n"
        "v_cvt_pk_bf16_f32 v84, v108, v109\n"
        "v_cvt_pk_bf16_f32 v86, v110, v111\n"
        "v_cvt_pk_bf16_f32 v85, v70, 0\n"
        "v_cvt_pk_bf16_f32 v164, v112, v122\n"
        "v_cvt_pk_bf16_f32 v165, v76, v116\n"
        "v_mfma_f32_16x16x32_bf16 v[108:111], v[148:151], v[58:61], 0\n"
        "s_waitcnt lgkmcnt(0)\n"
        "v_mul_f32 v104, v104, v88\n"
        "v_mul_f32 v105, v105, v89\n"
        "v_smfmac_f32_16x16x64_bf16 v[108:111], v[140:143], v[20:27], v18 abid:1\n"
        "s_add_u32 s39, s39, s38\n"
        "s_mul_i32 s68, s31, 3\n"
        "v_mul_f32 v106, v106, v90\n"
        "v_mul_f32 v107, v107, v91\n"
        "v_mul_f32 v126, v126, v92\n"
        "v_mov_b64 v[68:69], 0\n"
        "v_mfma_f32_16x16x32_bf16 v[144:147], v[148:151], v[40:43], 0\n"
        "s_lshr_b32 s38, s31, 3\n"
        "v_mov_b64 v[70:71], 0\n"
        "v_mov_b64 v[28:29], 0\n"
        "v_smfmac_f32_16x16x64_bf16 v[144:147], v[140:143], v[130:137], v18 abid:1\n"
        "s_add_u32 s38, s38, 11\n"
        "v_mov_b64 v[30:31], 0\n"
        "v_smfmac_f32_16x16x64_bf16 v[68:71], v[72:75], v[156:163], v4\n"
        "s_bitcmp1_b32 s31, 0\n"
        "v_mov_b64 v[64:65], 0\n"
        "v_smfmac_f32_16x16x64_bf16 v[68:71], v[32:35], v[46:53], v4 abid:1\n"
        "s_cselect_b32 s38, 3, s38\n"
        "s_add_u32 s37, s35, 0\n"
        "ds_read_b64_tr_b16 v[48:49], v9 offset:43392\n"
        "v_mov_b64 v[66:67], 0\n"
        "v_smfmac_f32_16x16x64_bf16 v[28:31], v[72:75], v[54:61], v4\n"
        "s_add_u32 s68, s68, s38\n"
        "v_cvt_pk_bf16_f32 v166, v108, v144\n"
        "v_smfmac_f32_16x16x64_bf16 v[28:31], v[32:35], v[20:27], v4 abid:1\n"
        "s_cmp_lt_u32 s31, s14\n"
        "ds_read_b64_tr_b16 v[22:23], v9 offset:43408\n"
        "v_mov_b32_dpp v87, v19 row_shr:4 row_mask:0xf bank_mask:0xf bound_ctrl:0\n"
        "v_mul_f32 v127, v127, v93\n"
        "v_cvt_pk_bf16_f32 v148, v113, v123\n"
        "v_smfmac_f32_16x16x64_bf16 v[64:67], v[72:75], v[36:43], v4\n"
        "s_cselect_b32 s68, s68, 0\n"
        "v_cvt_pk_bf16_f32 v116, v114, v124\n"
        "v_smfmac_f32_16x16x64_bf16 v[64:67], v[32:35], v[130:137], v4 abid:1\n"
        "buffer_store_dwordx3 v[164:166], v12, s[8:11], s37 offen sc0 sc1\n"
        "ds_read_b64_tr_b16 v[132:133], v9 offset:43424\n"
        "v_cvt_pk_bf16_f32 v122, v115, v125\n"
        "v_cvt_pk_bf16_f32 v149, v77, v117\n"
        "v_cvt_pk_bf16_f32 v117, v78, v118\n"
        "v_cvt_pk_bf16_f32 v123, v79, v119\n"
        "v_cvt_pk_bf16_f32 v150, v109, v145\n"
        "v_mfma_f32_16x16x32_bf16 v[140:143], v[80:83], v[156:159], 0\n"
        "s_waitcnt lgkmcnt(2)\n"
        "v_cvt_pk_bf16_f32 v118, v110, v146\n"
        "v_cvt_pk_bf16_f32 v124, v111, v147\n"
        "v_smfmac_f32_16x16x64_bf16 v[140:143], v[84:87], v[46:53], v18\n"
        "s_and_b32 s38, s31, 7\n"
        "s_cselect_b32 s30, 1, -7\n"
        "ds_read_b64_tr_b16 v[48:49], v9 offset:43440\n"
        "v_add_f32 v68, v68, v69\n"
        "v_add_f32 v70, v70, v71\n"
        "v_add_f32 v28, v28, v29\n"
        "v_mfma_f32_16x16x32_bf16 v[164:167], v[80:83], v[54:57], 0\n"
        "s_waitcnt lgkmcnt(2)\n"
        "v_add_f32 v30, v30, v31\n"
        "v_cvt_pk_bf16_f32 v144, v68, v28\n"
        "v_smfmac_f32_16x16x64_bf16 v[164:167], v[84:87], v[20:27], v18\n"
        "s_add_u32 s37, s35, 0\n"
        "s_mul_i32 s29, s30, 192\n"
        "ds_read_b64_tr_b16 v[22:23], v9 offset:43456\n"
        "v_add_f32 v64, v64, v65\n"
        "v_add_f32 v66, v66, v67\n"
        "v_cvt_pk_bf16_f32 v145, v64, v70\n"
        "v_mfma_f32_16x16x32_bf16 v[108:111], v[80:83], v[36:39], 0\n"
        "s_waitcnt lgkmcnt(2)\n"
        "v_cvt_pk_bf16_f32 v146, v30, v66\n"
        "v_mul_f32 v128, v128, v94\n"
        "v_smfmac_f32_16x16x64_bf16 v[108:111], v[84:87], v[130:137], v18\n"
        "buffer_store_dwordx3 v[148:150], v15, s[8:11], s37 offen offset:768 sc0 sc1 nt\n"
        "ds_read_b64_tr_b16 v[132:133], v9 offset:43472\n"
        "s_mul_i32 s30, s30, 392\n"
        "v_mul_f32 v129, v129, v95\n"
        "v_mul_f32 v152, v152, v96\n"
        "v_mul_f32 v153, v153, v97\n"
        "v_mul_f32 v154, v154, v98\n"
        "v_mfma_f32_16x16x32_bf16 v[68:71], v[80:83], v[160:163], 0\n"
        "s_waitcnt lgkmcnt(2)\n"
        "s_cmp_eq_u32 s73, s18\n"
        "v_mul_f32 v155, v155, v99\n"
        "v_smfmac_f32_16x16x64_bf16 v[68:71], v[84:87], v[46:53], v18 abid:1\n"
        "s_cselect_b32 s34, -7488, s75\n"
        "s_add_u32 s37, s35, 98112\n"
        "ds_read_b64_tr_b16 v[48:49], v9 offset:46720\n"
        "v_mfma_f32_16x16x32_bf16 v[148:151], v[80:83], v[58:61], 0\n"
        "s_waitcnt lgkmcnt(2)\n"
        "v_fma_f32 v44, v44, v100, v19\n"
        "v_cvt_pk_bf16_f32 v32, v104, v105\n"
        "v_smfmac_f32_16x16x64_bf16 v[148:151], v[84:87], v[20:27], v18 abid:1\n"
        "s_add_u32 s73, s73, s34\n"
        "ds_read_b64_tr_b16 v[22:23], v9 offset:46736\n"
        "v_cvt_pk_bf16_f32 v33, v106, v107\n"
        "v_cvt_pk_bf16_f32 v34, v126, v127\n"
        "v_mfma_f32_16x16x32_bf16 v[72:75], v[80:83], v[40:43], 0\n"
        "s_waitcnt lgkmcnt(2)\n"
        "v_cvt_pk_bf16_f32 v35, v128, v129\n"
        "v_cvt_pk_bf16_f32 v64, v152, v153\n"
        "v_smfmac_f32_16x16x64_bf16 v[72:75], v[84:87], v[130:137], v18 abid:1\n"
        "buffer_store_dwordx3 v[116:118], v12, s[8:11], s37 offen offset:192 sc0 sc1 nt\n"
        "ds_read_b64_tr_b16 v[132:133], v9 offset:46752\n"
        "ds_read_b32 v139, v0 offset:21600\n"
        "v_cvt_pk_bf16_f32 v66, v154, v155\n"
        "v_cvt_pk_bf16_f32 v65, v44, 0\n"
        "v_mov_b32_dpp v67, v19 row_shr:4 row_mask:0xf bank_mask:0xf bound_ctrl:0\n"
        "v_mfma_f32_16x16x32_bf16 v[116:119], v[32:35], v[156:159], 0\n"
        "s_waitcnt lgkmcnt(3)\n"
        "v_writelane_b32 v10, s73, 56\n"
        "v_add_u32 v11, s30, v11\n"
        "v_smfmac_f32_16x16x64_bf16 v[116:119], v[64:67], v[46:53], v18\n"
        "s_add_u32 s37, s35, 98112\n"
        "ds_read_b64_tr_b16 v[48:49], v9 offset:46768\n"
        "v_mfma_f32_16x16x32_bf16 v[28:31], v[32:35], v[54:57], 0\n"
        "s_waitcnt lgkmcnt(3)\n"
        "v_smfmac_f32_16x16x64_bf16 v[28:31], v[64:67], v[20:27], v18\n"
        "ds_read_b64_tr_b16 v[22:23], v9 offset:46784\n"
        "v_mfma_f32_16x16x32_bf16 v[112:115], v[32:35], v[36:39], 0\n"
        "s_waitcnt lgkmcnt(3)\n"
        "s_waitcnt lgkmcnt(2)\n"
        "v_readfirstlane_b32 s40, v139\n"
        "v_smfmac_f32_16x16x64_bf16 v[112:115], v[64:67], v[130:137], v18\n"
        "buffer_store_dwordx3 v[122:124], v14, s[8:11], s37 offen offset:960 sc0 sc1 nt\n"
        "ds_read_b64_tr_b16 v[132:133], v9 offset:46800\n"
        "s_add_u32 s37, s35, 588672\n"
        "s_add_u32 s46, s37, 1344\n"
        "s_cmp_lt_i32 s40, s68\n"
        "s_mov_b64 exec, 1\n"
        "ds_inc_u32 v0, v1 offset:21612\n"
        "s_mov_b64 exec, -1\n"
        "s_cbranch_scc1 L37B_%=\n"
        "L38B_%=:\n"
        "ds_read_b128 v[88:91], v7 offset:0\n"
        "v_mfma_f32_16x16x32_bf16 v[96:99], v[32:35], v[160:163], 0\n"
        "s_waitcnt lgkmcnt(4)\n"
        "ds_read_b128 v[76:79], v7 offset:3328\n"
        "v_smfmac_f32_16x16x64_bf16 v[96:99], v[64:67], v[46:53], v18 abid:1\n"
        "buffer_store_dwordx3 v[144:146], v12, s[44:47], s37 offen offset:1152 sc0 sc1 nt\n"
        "ds_read_b128 v[92:95], v7 offset:64\n"
        "ds_read_b128 v[80:83], v7 offset:3392\n"
        "ds_read_b128 v[100:103], v7 offset:128\n"
        "ds_read_b128 v[84:87], v7 offset:3456\n"
        "v_mfma_f32_16x16x32_bf16 v[52:55], v[32:35], v[58:61], 0\n"
        "s_waitcnt lgkmcnt(7)\n"
        "s_mov_b32 s36, s35\n"
        "v_smfmac_f32_16x16x64_bf16 v[52:55], v[64:67], v[20:27], v18 abid:1\n"
        "s_mov_b64 exec, s[62:63]\n"
        "v_add_u32 v16, s29, v16\n"
        "v_add_u32 v8, s34, v8\n"
        "s_mov_b64 exec, -1\n"
        "v_mfma_f32_16x16x32_bf16 v[56:59], v[32:35], v[40:43], 0\n"
        "s_mov_b32 s35, s39\n"
        "v_smfmac_f32_16x16x64_bf16 v[56:59], v[64:67], v[130:137], v18 abid:1\n"
        "s_mov_b64 exec, s[50:51]\n"
        "v_add_u32 v5, s29, v5\n"
        "v_add_u32 v6, s34, v6\n"
        "s_mov_b64 exec, -1\n"
        "s_setprio 0\n"
        "CNEXTB_%=:\n"
        "s_cmp_ge_u32 s31, s14\n"
        "s_cbranch_scc1 CEND_%=\n"
        "s_cmp_gt_i32 s29, 0\n"
        "s_cbranch_scc1 CTOP_%=\n"
        "s_branch CFULL_%=\n"
        "CEND_%=:\n"
        "s_waitcnt lgkmcnt(0)\n"
        "s_nop 15\n"
        "v_cvt_pk_bf16_f32 v248, v140, v164\n"
        "v_cvt_pk_bf16_f32 v249, v108, v68\n"
        "v_cvt_pk_bf16_f32 v250, v148, v72\n"
        "s_add_u32 s37, s36, 196224\n"
        "buffer_store_dwordx3 v[248:250], v12, s[8:11], s37 offen offset:384 sc0 sc1 nt\n"
        "v_cvt_pk_bf16_f32 v252, v141, v165\n"
        "v_cvt_pk_bf16_f32 v253, v109, v69\n"
        "v_cvt_pk_bf16_f32 v254, v149, v73\n"
        "s_add_u32 s37, s36, 196224\n"
        "buffer_store_dwordx3 v[252:254], v13, s[8:11], s37 offen offset:1152 sc0 sc1 nt\n"
        "v_cvt_pk_bf16_f32 v248, v142, v166\n"
        "v_cvt_pk_bf16_f32 v249, v110, v70\n"
        "v_cvt_pk_bf16_f32 v250, v150, v74\n"
        "s_add_u32 s37, s36, 294336\n"
        "buffer_store_dwordx3 v[248:250], v12, s[8:11], s37 offen offset:576 sc0 sc1 nt\n"
        "v_cvt_pk_bf16_f32 v252, v143, v167\n"
        "v_cvt_pk_bf16_f32 v253, v111, v71\n"
        "v_cvt_pk_bf16_f32 v254, v151, v75\n"
        "s_add_u32 s37, s36, 392448\n"
        "buffer_store_dwordx3 v[252:254], v12, s[8:11], s37 offen sc0 sc1 nt\n"
        "v_cvt_pk_bf16_f32 v248, v116, v28\n"
        "v_cvt_pk_bf16_f32 v249, v112, v96\n"
        "v_cvt_pk_bf16_f32 v250, v52, v56\n"
        "s_add_u32 s37, s36, 392448\n"
        "v_cvt_pk_bf16_f32 v252, v117, v29\n"
        "v_cvt_pk_bf16_f32 v253, v113, v97\n"
        "v_cvt_pk_bf16_f32 v254, v53, v57\n"
        "buffer_store_dwordx3 v[248:250], v15, s[8:11], s37 offen offset:768 sc0 sc1 nt\n"
        "s_add_u32 s37, s36, 490560\n"
        "v_cvt_pk_bf16_f32 v246, v118, v30\n"
        "v_cvt_pk_bf16_f32 v247, v114, v98\n"
        "v_cvt_pk_bf16_f32 v248, v54, v58\n"
        "buffer_store_dwordx3 v[252:254], v12, s[8:11], s37 offen offset:192 sc0 sc1 nt\n"
        "s_add_u32 s37, s36, 490560\n"
        "buffer_store_dwordx3 v[246:248], v14, s[8:11], s37 offen offset:960 sc0 sc1 nt\n"
        "s_add_u32 s37, s36, 588672\n"
        "v_cvt_pk_bf16_f32 v252, v119, v31\n"
        "v_cvt_pk_bf16_f32 v253, v115, v99\n"
        "v_cvt_pk_bf16_f32 v254, v55, v59\n"
        "buffer_store_dwordx3 v[252:254], v12, s[8:11], s37 offen offset:384 sc0 sc1 nt\n"
        "s_nop 1\n"
        "s_endpgm\n"
        "L33A_%=:\n"
        "s_nop 7\n"
        "v_mov_b32 v124, 21600\n"
        "L31A_%=:\n"
        "ds_read_b32 v125, v124 offset:64\n"
        "s_waitcnt lgkmcnt(0)\n"
        "v_readfirstlane_b32 s40, v125\n"
        "s_nop 3\n"
        "s_cmp_ge_i32 s40, 1\n"
        "s_cbranch_scc1 L32A_%=\n"
        "s_sleep 1\n"
        "s_sub_u32 s41, s41, 1\n"
        "s_cbranch_scc0 L31A_%=\n"
        "s_endpgm\n"
        "L32A_%=:\n"
        "v_mbcnt_lo_u32_b32 v125, -1, 0\n"
        "v_lshl_add_u32 v124, v125, 2, v124\n"
        "ds_read_b32 v125, v124 offset:68\n"
        "s_mov_b32 s14, s40\n"
        "s_mov_b32 s76, -1\n"
        "s_waitcnt lgkmcnt(0)\n"
        "v_readlane_b32 s22, v125, 0\n"
        "v_readlane_b32 s23, v125, 1\n"
        "s_nop 3\n"
        "s_add_u32 s22, s22, s17\n"
        "s_add_u32 s23, s23, s17\n"
        "s_nop 1\n"
        "s_branch L34A_%=\n"
        "L37A_%=:\n"
        "s_nop 7\n"
        "L35A_%=:\n"
        "ds_read_b32 v139, v0 offset:21604\n"
        "s_waitcnt lgkmcnt(0)\n"
        "v_readfirstlane_b32 s40, v139\n"
        "s_nop 3\n"
        "s_cmp_ge_i32 s40, s68\n"
        "s_cbranch_scc1 L36A_%=\n"
        "s_sleep 1\n"
        "s_sub_u32 s41, s41, 1\n"
        "s_cbranch_scc0 L35A_%=\n"
        "s_endpgm\n"
        "L36A_%=:\n"
        "s_nop 1\n"
        "s_branch L38A_%=\n"
        "L33B_%=:\n"
        "s_nop 7\n"
        "v_mov_b32 v124, 21600\n"
        "L31B_%=:\n"
        "ds_read_b32 v125, v124 offset:64\n"
        "s_waitcnt lgkmcnt(0)\n"
        "v_readfirstlane_b32 s40, v125\n"
        "s_nop 3\n"
        "s_cmp_ge_i32 s40, 1\n"
        "s_cbranch_scc1 L32B_%=\n"
        "s_sleep 1\n"
        "s_sub_u32 s41, s41, 1\n"
        "s_cbranch_scc0 L31B_%=\n"
        "s_endpgm\n"
        "L32B_%=:\n"
        "v_mbcnt_lo_u32_b32 v125, -1, 0\n"
        "v_lshl_add_u32 v124, v125, 2, v124\n"
        "ds_read_b32 v125, v124 offset:68\n"
        "s_mov_b32 s14, s40\n"
        "s_mov_b32 s76, -1\n"
        "s_waitcnt lgkmcnt(0)\n"
        "v_readlane_b32 s22, v125, 0\n"
        "v_readlane_b32 s23, v125, 1\n"
        "s_nop 3\n"
        "s_add_u32 s22, s22, s17\n"
        "s_add_u32 s23, s23, s17\n"
        "s_nop 1\n"
        "s_branch L34B_%=\n"
        "L37B_%=:\n"
        "s_nop 7\n"
        "L35B_%=:\n"
        "ds_read_b32 v139, v0 offset:21600\n"
        "s_waitcnt lgkmcnt(0)\n"
        "v_readfirstlane_b32 s40, v139\n"
        "s_nop 3\n"
        "s_cmp_ge_i32 s40, s68\n"
        "s_cbranch_scc1 L36B_%=\n"
        "s_sleep 1\n"
        "s_sub_u32 s41, s41, 1\n"
        "s_cbranch_scc0 L35B_%=\n"
        "s_endpgm\n"
        "L36B_%=:\n"
        "s_nop 1\n"
        "s_branch L38B_%=\n"
        "CGRP_%=:\n"
        "s_setprio 3\n"
        "s_waitcnt lgkmcnt(0)\n"
        "s_nop 6\n"
        "ds_read_u16 v160, v0 offset:20576\n"
        "s_sub_u32 s60, s73, 102048\n"
        "ds_read_u16 v162, v0 offset:20578\n"
        "ds_read_u16 v158, v0 offset:20580\n"
        "ds_read_u16 v163, v0 offset:20582\n"
        "ds_read_u16 v157, v0 offset:20588\n"
        "ds_read_u16 v159, v0 offset:20590\n"
        "v_lshrrev_b32 v161, 8, v0\n"
        "s_waitcnt lgkmcnt(5)\n"
        "v_add_u32 v160, s60, v160\n"
        "v_subrev_u32 v156, 8320, v160\n"
        "v_min_u32 v160, v160, v156\n"
        "v_add_u32 v160, 101856, v160\n"
        "ds_read_b128 v[20:23], v160 offset:64\n"
        "ds_read_b128 v[24:27], v160 offset:128\n"
        "ds_read_b128 v[32:35], v160\n"
        "s_waitcnt lgkmcnt(1)\n"
        "s_waitcnt lgkmcnt(0)\n"
        "v_mfma_f32_16x16x32_bf16 v[40:43], v[168:171], v[32:35], 0\n"
        "v_add_u32 v162, s60, v162\n"
        "v_subrev_u32 v156, 8320, v162\n"
        "v_mfma_f32_16x16x32_bf16 v[40:43], v[172:175], v[20:23], v[40:43]\n"
        "v_min_u32 v162, v162, v156\n"
        "v_add_u32 v162, 101856, v162\n"
        "v_mfma_f32_16x16x32_bf16 v[40:43], v[176:179], v[24:27], v[40:43]\n"
        "v_add_u32 v158, s19, v158\n"
        "v_mad_i32_i24 v161, v161, -8, v158\n"
        "v_add_u32 v159, s19, v159\n"
        "v_add_u32 v163, s19, v163\n"
        "v_mfma_f32_16x16x32_bf16 v[48:51], v[180:183], v[32:35], 0\n"
        "v_mfma_f32_16x16x32_bf16 v[48:51], v[184:187], v[20:23], v[48:51]\n"
        "v_mfma_f32_16x16x32_bf16 v[48:51], v[188:191], v[24:27], v[48:51]\n"
        "v_mfma_f32_16x16x32_bf16 v[44:47], v[192:195], v[32:35], 0\n"
        "v_cvt_pk_bf16_f32 v36, v40, v41\n"
        "v_cvt_pk_bf16_f32 v37, v42, v43\n"
        "v_mfma_f32_16x16x32_bf16 v[44:47], v[196:199], v[20:23], v[44:47]\n"
        "v_mfma_f32_16x16x32_bf16 v[44:47], v[200:203], v[24:27], v[44:47]\n"
        "v_mfma_f32_16x16x32_bf16 v[40:43], v[204:207], v[32:35], 0\n"
        "v_mfma_f32_16x16x32_bf16 v[40:43], v[208:211], v[20:23], v[40:43]\n"
        "v_mfma_f32_16x16x32_bf16 v[40:43], v[212:215], v[24:27], v[40:43]\n"
        "v_cvt_pk_bf16_f32 v38, v48, v49\n"
        "v_cvt_pk_bf16_f32 v39, v50, v51\n"
        "ds_write2_b32 v161, v36, v38 offset0:0 offset1:1\n"
        "v_mfma_f32_16x16x32_bf16 v[60:63], v[216:219], v[32:35], 0\n"
        "v_mfma_f32_16x16x32_bf16 v[60:63], v[220:223], v[20:23], v[60:63]\n"
        "v_cvt_pk_bf16_f32 v48, v44, v45\n"
        "v_cvt_pk_bf16_f32 v49, v46, v47\n"
        "v_mfma_f32_16x16x32_bf16 v[60:63], v[224:227], v[24:27], v[60:63]\n"
        "v_cvt_pk_bf16_f32 v50, v40, v41\n"
        "v_cvt_pk_bf16_f32 v51, v42, v43\n"
        "v_mov_b32 v40, 0x3f803f80\n"
        "v_mov_b32 v41, 0x3f803f80\n"
        "v_mov_b32 v42, 0x3f803f80\n"
        "v_mov_b32 v43, 0x3f803f80\n"
        "v_mfma_f32_16x16x32_bf16 v[104:107], v[228:231], v[32:35], 0\n"
        "v_mfma_f32_16x16x32_bf16 v[104:107], v[232:235], v[20:23], v[104:107]\n"
        "v_cvt_pk_bf16_f32 v64, v60, v61\n"
        "v_cvt_pk_bf16_f32 v65, v62, v63\n"
        "v_mfma_f32_16x16x32_bf16 v[104:107], v[236:239], v[24:27], v[104:107]\n"
        "ds_write2_b32 v161, v37, v39 offset0:16 offset1:17\n"
        "ds_write2_b32 v161, v48, v50 offset0:8 offset1:9\n"
        "ds_write2_b32 v161, v49, v51 offset0:24 offset1:25\n"
        "s_nop 4\n"
        "v_cvt_pk_bf16_f32 v67, v106, v107\n"
        "v_cvt_pk_bf16_f32 v66, v104, v105\n"
        "v_mfma_f32_16x16x32_bf16 v[44:47], v[32:35], v[36:39], 0\n"
        "v_mfma_f32_16x16x32_bf16 v[44:47], v[20:23], v[48:51], v[44:47]\n"
        "ds_read_b64 v[160:161], v157\n"
        "v_mfma_f32_16x16x32_bf16 v[44:47], v[24:27], v[64:67], v[44:47]\n"
        "ds_read_b64 v[152:153], v162\n"
        "ds_read_b64 v[154:155], v162 offset:16\n"
        "ds_write_b128 v158, v[64:67] offset:128\n"
        "v_mfma_f32_16x16x32_bf16 v[60:63], v[40:43], v[32:35], 0\n"
        "ds_read_b64 v[156:157], v162 offset:32\n"
        "v_mfma_f32_16x16x32_bf16 v[60:63], v[40:43], v[20:23], v[60:63]\n"
        "v_mfma_f32_16x16x32_bf16 v[60:63], v[40:43], v[24:27], v[60:63]\n"
        "s_waitcnt lgkmcnt(3)\n"
        "v_mfma_f32_4x4x4_16b_bf16 v[48:51], v[152:153], v[160:161], 0\n"
        "s_waitcnt lgkmcnt(1)\n"
        "v_mfma_f32_4x4x4_16b_bf16 v[40:43], v[154:155], v[160:161], 0\n"
        "ds_read_b128 v[120:123], v0 offset:4112\n"
        "ds_read_b128 v[64:67], v0 offset:5136\n"
        "s_waitcnt lgkmcnt(2)\n"
        "v_mfma_f32_4x4x4_16b_bf16 v[36:39], v[156:157], v[160:161], 0\n"
        "s_mov_b32 vcc_lo, 0x22222222\n"
        "s_mov_b32 vcc_hi, 0\n"
        "v_cndmask_b32 v158, v44, v45, vcc\n"
        "v_mfma_f32_16x16x32_bf16 v[128:131], v[32:35], v[240:243], 0\n"
        "s_mov_b32 vcc_lo, 0x44444444\n"
        "s_mov_b32 vcc_hi, 0\n"
        "v_cndmask_b32 v158, v158, v46, vcc\n"
        "v_mfma_f32_16x16x32_bf16 v[128:131], v[20:23], v[244:247], v[128:131]\n"
        "ds_read_b128 v[124:127], v0 offset:6176\n"
        "v_mfma_f32_16x16x32_bf16 v[128:131], v[24:27], v[248:251], v[128:131]\n"
        "ds_read_b128 v[132:135], v0 offset:7200\n"
        "ds_read_b128 v[104:107], v0 offset:8224\n"
        "s_mov_b32 vcc_lo, 0x88888888\n"
        "s_mov_b32 vcc_hi, 0\n"
        "v_cndmask_b32 v158, v158, v47, vcc\n"
        "s_mov_b64 exec, 0x00f0000f\n"
        "ds_write2_b32 v159, v158, v60 offset0:0 offset1:1\n"
        "s_mov_b64 exec, -1\n"
        "s_waitcnt lgkmcnt(4)\n"
        "v_mfma_f32_16x16x32_bf16 v[44:47], v[32:35], v[252:255], 0\n"
        "v_cvt_pk_bf16_f32 v146, v128, v48\n"
        "v_cvt_pk_bf16_f32 v156, v129, v49\n"
        "v_mfma_f32_16x16x32_bf16 v[44:47], v[20:23], v[120:123], v[44:47]\n"
        "ds_read_b64 v[152:153], v162 offset:48\n"
        "v_cvt_pk_bf16_f32 v154, v130, v50\n"
        "v_mfma_f32_16x16x32_bf16 v[44:47], v[24:27], v[64:67], v[44:47]\n"
        "ds_read_b64 v[144:145], v162 offset:64\n"
        "v_cvt_pk_bf16_f32 v158, v131, v51\n"
        "s_waitcnt lgkmcnt(3)\n"
        "v_mfma_f32_16x16x32_bf16 v[64:67], v[32:35], v[124:127], 0\n"
        "ds_read_b64 v[138:139], v162 offset:80\n"
        "v_mfma_f32_16x16x32_bf16 v[64:67], v[20:23], v[132:135], v[64:67]\n"
        "ds_read_b128 v[60:63], v0 offset:9264\n"
        "v_mfma_f32_16x16x32_bf16 v[64:67], v[24:27], v[104:107], v[64:67]\n"
        "ds_read_b128 v[120:123], v0 offset:10288\n"
        "ds_read_b128 v[104:107], v0 offset:11312\n"
        "v_cvt_pk_bf16_f32 v147, v44, v40\n"
        "v_cvt_pk_bf16_f32 v157, v45, v41\n"
        "v_cvt_pk_bf16_f32 v155, v46, v42\n"
        "v_cvt_pk_bf16_f32 v159, v47, v43\n"
        "ds_write_b64 v163, v[146:147]\n"
        "s_waitcnt lgkmcnt(5)\n"
        "v_mfma_f32_4x4x4_16b_bf16 v[48:51], v[152:153], v[160:161], 0\n"
        "v_mfma_f32_4x4x4_16b_bf16 v[44:47], v[144:145], v[160:161], 0\n"
        "ds_read_b128 v[132:135], v0 offset:12352\n"
        "ds_read_b128 v[128:131], v0 offset:13376\n"
        "ds_read_b128 v[124:127], v0 offset:14400\n"
        "s_waitcnt lgkmcnt(7)\n"
        "v_mfma_f32_4x4x4_16b_bf16 v[40:43], v[138:139], v[160:161], 0\n"
        "v_cvt_pk_bf16_f32 v144, v64, v36\n"
        "v_cvt_pk_bf16_f32 v146, v65, v37\n"
        "v_cvt_pk_bf16_f32 v152, v66, v38\n"
        "v_cvt_pk_bf16_f32 v160, v67, v39\n"
        "s_waitcnt lgkmcnt(4)\n"
        "v_mfma_f32_16x16x32_bf16 v[136:139], v[32:35], v[60:63], 0\n"
        "v_mfma_f32_16x16x32_bf16 v[136:139], v[20:23], v[120:123], v[136:139]\n"
        "ds_read_b128 v[36:39], v0 offset:15440\n"
        "v_mfma_f32_16x16x32_bf16 v[136:139], v[24:27], v[104:107], v[136:139]\n"
        "ds_read_b128 v[60:63], v0 offset:16464\n"
        "ds_read_b128 v[64:67], v0 offset:17488\n"
        "ds_write_b64 v163, v[156:157] offset:392\n"
        "ds_write_b64 v163, v[154:155] offset:784\n"
        "s_waitcnt lgkmcnt(5)\n"
        "v_mfma_f32_16x16x32_bf16 v[104:107], v[32:35], v[132:135], 0\n"
        "v_mfma_f32_16x16x32_bf16 v[104:107], v[20:23], v[128:131], v[104:107]\n"
        "v_mfma_f32_16x16x32_bf16 v[104:107], v[24:27], v[124:127], v[104:107]\n"
        "v_cvt_pk_bf16_f32 v145, v136, v48\n"
        "v_cvt_pk_bf16_f32 v147, v137, v49\n"
        "v_cvt_pk_bf16_f32 v153, v138, v50\n"
        "v_cvt_pk_bf16_f32 v161, v139, v51\n"
        "s_waitcnt lgkmcnt(2)\n"
        "v_mfma_f32_16x16x32_bf16 v[48:51], v[32:35], v[36:39], 0\n"
        "v_mfma_f32_16x16x32_bf16 v[48:51], v[20:23], v[60:63], v[48:51]\n"
        "v_mfma_f32_16x16x32_bf16 v[48:51], v[24:27], v[64:67], v[48:51]\n"
        "v_cvt_pk_bf16_f32 v156, v104, v44\n"
        "v_cvt_pk_bf16_f32 v154, v105, v45\n"
        "v_cvt_pk_bf16_f32 v138, v106, v46\n"
        "v_cvt_pk_bf16_f32 v136, v107, v47\n"
        "ds_write_b64 v163, v[158:159] offset:1176\n"
        "ds_write_b64 v163, v[144:145] offset:8\n"
        "ds_write_b64 v163, v[146:147] offset:400\n"
        "ds_write_b64 v163, v[152:153] offset:792\n"
        "v_cvt_pk_bf16_f32 v157, v48, v40\n"
        "v_cvt_pk_bf16_f32 v155, v49, v41\n"
        "v_cvt_pk_bf16_f32 v139, v50, v42\n"
        "v_cvt_pk_bf16_f32 v137, v51, v43\n"
        "ds_write_b64 v163, v[160:161] offset:1184\n"
        "ds_write_b64 v163, v[156:157] offset:16\n"
        "ds_write_b64 v163, v[154:155] offset:408\n"
        "ds_write_b64 v163, v[138:139] offset:800\n"
        "ds_write_b64 v163, v[136:137] offset:1192\n"
        "s_waitcnt lgkmcnt(0)\n"
        "s_nop 6\n"
        "s_setprio 0\n"
        "s_setpc_b64 s[64:65]\n"
        "L49A_%=:\n"
        "s_nop 7\n"
        "v_mov_b32 v166, 21600\n"
        "L47A_%=:\n"
        "ds_read_b32 v167, v166 offset:64\n"
        "s_waitcnt lgkmcnt(0)\n"
        "v_readfirstlane_b32 s40, v167\n"
        "s_nop 3\n"
        "s_cmp_ge_i32 s40, 1\n"
        "s_cbranch_scc1 L48A_%=\n"
        "s_sleep 1\n"
        "s_sub_u32 s41, s41, 1\n"
        "s_cbranch_scc0 L47A_%=\n"
        "s_endpgm\n"
        "L48A_%=:\n"
        "v_mbcnt_lo_u32_b32 v167, -1, 0\n"
        "v_lshl_add_u32 v166, v167, 2, v166\n"
        "ds_read_b32 v167, v166 offset:68\n"
        "s_mov_b32 s14, s40\n"
        "s_mov_b32 s76, -1\n"
        "s_waitcnt lgkmcnt(0)\n"
        "v_readlane_b32 s22, v167, 0\n"
        "v_readlane_b32 s23, v167, 1\n"
        "s_nop 3\n"
        "s_add_u32 s22, s22, s17\n"
        "s_add_u32 s23, s23, s17\n"
        "s_nop 1\n"
        "s_branch L50A_%=\n"
        "L53A_%=:\n"
        "s_nop 7\n"
        "L51A_%=:\n"
        "ds_read_b32 v163, v0 offset:21604\n"
        "s_waitcnt lgkmcnt(0)\n"
        "v_readfirstlane_b32 s40, v163\n"
        "s_nop 3\n"
        "s_cmp_ge_i32 s40, s68\n"
        "s_cbranch_scc1 L52A_%=\n"
        "s_sleep 1\n"
        "s_sub_u32 s41, s41, 1\n"
        "s_cbranch_scc0 L51A_%=\n"
        "s_endpgm\n"
        "L52A_%=:\n"
        "s_nop 1\n"
        "s_branch L54A_%=\n"
        "L49B_%=:\n"
        "s_nop 7\n"
        "v_mov_b32 v166, 21600\n"
        "L47B_%=:\n"
        "ds_read_b32 v167, v166 offset:64\n"
        "s_waitcnt lgkmcnt(0)\n"
        "v_readfirstlane_b32 s40, v167\n"
        "s_nop 3\n"
        "s_cmp_ge_i32 s40, 1\n"
        "s_cbranch_scc1 L48B_%=\n"
        "s_sleep 1\n"
        "s_sub_u32 s41, s41, 1\n"
        "s_cbranch_scc0 L47B_%=\n"
        "s_endpgm\n"
        "L48B_%=:\n"
        "v_mbcnt_lo_u32_b32 v167, -1, 0\n"
        "v_lshl_add_u32 v166, v167, 2, v166\n"
        "ds_read_b32 v167, v166 offset:68\n"
        "s_mov_b32 s14, s40\n"
        "s_mov_b32 s76, -1\n"
        "s_waitcnt lgkmcnt(0)\n"
        "v_readlane_b32 s22, v167, 0\n"
        "v_readlane_b32 s23, v167, 1\n"
        "s_nop 3\n"
        "s_add_u32 s22, s22, s17\n"
        "s_add_u32 s23, s23, s17\n"
        "s_nop 1\n"
        "s_branch L50B_%=\n"
        "L53B_%=:\n"
        "s_nop 7\n"
        "L51B_%=:\n"
        "ds_read_b32 v163, v0 offset:21600\n"
        "s_waitcnt lgkmcnt(0)\n"
        "v_readfirstlane_b32 s40, v163\n"
        "s_nop 3\n"
        "s_cmp_ge_i32 s40, s68\n"
        "s_cbranch_scc1 L52B_%=\n"
        "s_sleep 1\n"
        "s_sub_u32 s41, s41, 1\n"
        "s_cbranch_scc0 L51B_%=\n"
        "s_endpgm\n"
        "L52B_%=:\n"
        "s_nop 1\n"
        "s_branch L54B_%=\n"
        "CREDOA_%=:\n"
        "s_waitcnt lgkmcnt(0)\n"
        "s_nop 15\n"
        "s_mov_b32 s36, 0x20000000\n"
        "ds_read_b128 v[88:91], v7 offset:0\n"
        "ds_read_b128 v[76:79], v7 offset:3328\n"
        "ds_read_b128 v[92:95], v7 offset:64\n"
        "ds_read_b128 v[80:83], v7 offset:3392\n"
        "ds_read_b128 v[100:103], v7 offset:128\n"
        "ds_read_b128 v[84:87], v7 offset:3456\n"
        "s_waitcnt lgkmcnt(0)\n"
        "s_branch MTOPA_%=\n"
        "CREDOB_%=:\n"
        "s_waitcnt lgkmcnt(0)\n"
        "s_nop 15\n"
        "s_mov_b32 s36, 0x20000000\n"
        "ds_read_b128 v[88:91], v7 offset:40064\n"
        "ds_read_b128 v[76:79], v7 offset:43392\n"
        "ds_read_b128 v[92:95], v7 offset:40128\n"
        "ds_read_b128 v[80:83], v7 offset:43456\n"
        "ds_read_b128 v[100:103], v7 offset:40192\n"
        "ds_read_b128 v[84:87], v7 offset:43520\n"
        "s_waitcnt lgkmcnt(0)\n"
        "s_branch MTOPB_%=\n"
        "MTOPA_%=:\n"
        "s_waitcnt lgkmcnt(5)\n"
        "v_mfma_f32_16x16x32_bf16 v[20:23], v[168:171], v[88:91], 0\n"
        "v_cvt_pk_bf16_f32 v132, v140, v164\n"
        "v_cvt_pk_bf16_f32 v133, v108, v68\n"
        "v_cvt_pk_bf16_f32 v134, v148, v72\n"
        "v_cvt_pk_bf16_f32 v136, v141, v165\n"
        "v_cvt_pk_bf16_f32 v137, v109, v69\n"
        "v_cvt_pk_bf16_f32 v138, v149, v73\n"
        "v_cvt_pk_bf16_f32 v140, v142, v166\n"
        "v_cvt_pk_bf16_f32 v141, v110, v70\n"
        "s_waitcnt lgkmcnt(3)\n"
        "v_mfma_f32_16x16x32_bf16 v[20:23], v[172:175], v[92:95], v[20:23]\n"
        "v_cvt_pk_bf16_f32 v142, v150, v74\n"
        "v_cvt_pk_bf16_f32 v144, v143, v167\n"
        "v_cvt_pk_bf16_f32 v145, v111, v71\n"
        "v_cvt_pk_bf16_f32 v146, v151, v75\n"
        "v_cvt_pk_bf16_f32 v148, v116, v28\n"
        "v_cvt_pk_bf16_f32 v149, v112, v96\n"
        "v_cvt_pk_bf16_f32 v150, v52, v56\n"
        "v_cvt_pk_bf16_f32 v152, v117, v29\n"
        "s_waitcnt lgkmcnt(1)\n"
        "v_mfma_f32_16x16x32_bf16 v[20:23], v[176:179], v[100:103], v[20:23]\n"
        "v_cvt_pk_bf16_f32 v153, v113, v97\n"
        "v_cvt_pk_bf16_f32 v154, v53, v57\n"
        "v_cvt_pk_bf16_f32 v156, v118, v30\n"
        "v_cvt_pk_bf16_f32 v157, v114, v98\n"
        "v_cvt_pk_bf16_f32 v158, v54, v58\n"
        "v_cvt_pk_bf16_f32 v160, v119, v31\n"
        "v_cvt_pk_bf16_f32 v161, v115, v99\n"
        "v_cvt_pk_bf16_f32 v162, v55, v59\n"
        "v_mfma_f32_16x16x32_bf16 v[24:27], v[188:191], v[100:103], 0\n"
        "v_cvt_pk_bf16_f32 v40, v20, v21\n"
        "v_cvt_pk_bf16_f32 v41, v22, v23\n"
        "v_mfma_f32_16x16x32_bf16 v[24:27], v[184:187], v[92:95], v[24:27]\n"
        "s_setprio 2\n"
        "v_mfma_f32_16x16x32_bf16 v[24:27], v[180:183], v[88:91], v[24:27]\n"
        "s_add_u32 s37, s36, 196224\n"
        "ds_read_b128 v[28:31], v16 offset:128\n"
        "ds_read_b32 v163, v10\n"
        "v_mfma_f32_16x16x32_bf16 v[32:35], v[192:195], v[88:91], 0\n"
        "ds_read_b128 v[20:23], v5\n"
        "v_mfma_f32_16x16x32_bf16 v[32:35], v[196:199], v[92:95], v[32:35]\n"
        "ds_read_b64 v[164:165], v11 offset:192\n"
        "v_mfma_f32_16x16x32_bf16 v[32:35], v[200:203], v[100:103], v[32:35]\n"
        "buffer_store_dwordx3 v[132:134], v12, s[8:11], s37 offen offset:384 sc0 sc1 nt\n"
        "v_cvt_pk_bf16_f32 v42, v24, v25\n"
        "v_cvt_pk_bf16_f32 v43, v26, v27\n"
        "s_waitcnt lgkmcnt(3)\n"
        "v_mfma_f32_16x16x32_bf16 v[48:51], v[28:31], v[100:103], 0\n"
        "ds_read_b128 v[56:59], v7 offset:6784\n"
        "ds_read_i16 v167, v0 offset:20584\n"
        "ds_read_i16 v159, v0 offset:20586\n"
        "v_cvt_pk_bf16_f32 v44, v32, v33\n"
        "v_cvt_pk_bf16_f32 v45, v34, v35\n"
        "s_waitcnt lgkmcnt(3)\n"
        "v_smfmac_f32_16x16x64_bf16 v[48:51], v[20:23], v[88:95], v4\n"
        "s_add_u32 s37, s36, 196224\n"
        "ds_read_b128 v[32:35], v7 offset:6656\n"
        "ds_read_b128 v[36:39], v7 offset:6720\n"
        "v_mfma_f32_16x16x32_bf16 v[52:55], v[28:31], v[84:87], 0\n"
        "v_smfmac_f32_16x16x64_bf16 v[52:55], v[20:23], v[76:83], v4\n"
        "s_waitcnt lgkmcnt(4)\n"
        "v_mfma_f32_16x16x32_bf16 v[24:27], v[28:31], v[56:59], 0\n"
        "buffer_store_dwordx3 v[136:138], v13, s[8:11], s37 offen offset:1152 sc0 sc1 nt\n"
        "v_add_f32_dpp v165, v50, v51 quad_perm:[0,1,2,3] row_mask:0x1 bank_mask:0xf\n"
        "s_waitcnt lgkmcnt(2)\n"
        "v_add_u32 v167, v9, v167\n"
        "v_add_f32_dpp v164, v48, v49 quad_perm:[0,1,2,3] row_mask:0x1 bank_mask:0xf\n"
        "v_add_u32 v159, v9, v159\n"
        "v_add_f32_dpp v165, v54, v55 quad_perm:[0,1,2,3] row_mask:0x2 bank_mask:0xf\n"
        "v_add_f32_dpp v164, v52, v53 quad_perm:[0,1,2,3] row_mask:0x2 bank_mask:0xf\n"
        "s_waitcnt lgkmcnt(0)\n"
        "v_smfmac_f32_16x16x64_bf16 v[24:27], v[20:23], v[32:39], v4\n"
        "v_mfma_f32_16x16x32_bf16 v[28:31], v[212:215], v[100:103], 0\n"
        "s_add_u32 s37, s36, 294336\n"
        "v_mfma_f32_16x16x32_bf16 v[28:31], v[208:211], v[92:95], v[28:31]\n"
        "v_mfma_f32_16x16x32_bf16 v[28:31], v[204:207], v[88:91], v[28:31]\n"
        "v_mfma_f32_16x16x32_bf16 v[20:23], v[216:219], v[88:91], 0\n"
        "buffer_store_dwordx3 v[140:142], v12, s[8:11], s37 offen offset:576 sc0 sc1 nt\n"
        "v_mfma_f32_16x16x32_bf16 v[20:23], v[220:223], v[92:95], v[20:23]\n"
        "v_mfma_f32_16x16x32_bf16 v[20:23], v[224:227], v[100:103], v[20:23]\n"
        "s_add_u32 s37, s36, 392448\n"
        "v_add_f32_dpp v165, v26, v27 quad_perm:[0,1,2,3] row_mask:0x4 bank_mask:0xf\n"
        "v_mul_f32 v166, s52, v165\n"
        "v_fma_f32 v166, -v166, v165, v163\n"
        "v_cvt_pk_bf16_f32 v46, v28, v29\n"
        "v_cvt_pk_bf16_f32 v47, v30, v31\n"
        "v_add_f32_e64 v166, s53, |v166|\n"
        "v_rsq_f32 v163, v166\n"
        "v_mfma_f32_16x16x32_bf16 v[52:55], v[236:239], v[100:103], 0\n"
        "s_mov_b32 exec_hi, 0xffff\n"
        "ds_write_b32 v10, v163\n"
        "s_mov_b32 exec_hi, -1\n"
        "v_cvt_pk_bf16_f32 v48, v20, v21\n"
        "v_cvt_pk_bf16_f32 v49, v22, v23\n"
        "ds_read_b32 v20, v167 offset:0\n"
        "v_add_f32_dpp v164, v24, v25 quad_perm:[0,1,2,3] row_mask:0x4 bank_mask:0xf\n"
        "v_mfma_f32_16x16x32_bf16 v[52:55], v[232:235], v[92:95], v[52:55]\n"
        "s_mov_b32 exec_hi, 0xffff\n"
        "v_add_u32 v10, 40064, v10\n"
        "s_mov_b32 exec_hi, -1\n"
        "ds_read_b32 v21, v167 offset:832\n"
        "ds_read_b32 v22, v167 offset:1664\n"
        "ds_read_b32 v23, v167 offset:2496\n"
        "ds_read_b32 v24, v167 offset:3328\n"
        "v_mul_f32 v164, v164, v163\n"
        "v_mfma_f32_16x16x32_bf16 v[52:55], v[228:231], v[88:91], v[52:55]\n"
        "buffer_store_dwordx3 v[144:146], v12, s[8:11], s37 offen sc0 sc1 nt\n"
        "ds_read_b32 v25, v167 offset:4160\n"
        "ds_read_b32 v26, v167 offset:4992\n"
        "ds_read_b32 v27, v167 offset:5824\n"
        "ds_read_b32 v28, v167 offset:6656\n"
        "ds_read_b32 v29, v167 offset:7488\n"
        "ds_read_b32 v30, v167 offset:8320\n"
        "ds_read_b32 v31, v167 offset:9152\n"
        "v_cvt_pk_bf16_f32 v50, v52, v53\n"
        "v_cvt_pk_bf16_f32 v51, v54, v55\n"
        "v_readlane_b32 s58, v163, 56\n"
        "v_mfma_f32_16x16x32_bf16 v[52:55], v[88:91], v[40:43], 0\n"
        "s_mov_b64 exec, s[56:57]\n"
        "v_mov_b32 v164, 0xff800000\n"
        "s_mov_b64 exec, -1\n"
        "ds_read_b32 v165, v159 offset:0\n"
        "ds_read_b32 v166, v159 offset:3328\n"
        "v_max_f32_dpp v155, v164, v164 row_ror:8 row_mask:0xf bank_mask:0xf\n"
        "v_mfma_f32_16x16x32_bf16 v[52:55], v[92:95], v[44:47], v[52:55]\n"
        "s_add_u32 s37, s36, 392448\n"
        "v_max_f32_dpp v155, v155, v155 row_ror:4 row_mask:0xf bank_mask:0xf\n"
        "ds_read_b32 v167, v159 offset:6656\n"
        "v_mfma_f32_16x16x32_bf16 v[52:55], v[100:103], v[48:51], v[52:55]\n"
        "v_max_f32_dpp v155, v155, v155 row_ror:2 row_mask:0xf bank_mask:0xf\n"
        "v_mfma_f32_16x16x32_bf16 v[96:99], v[84:87], v[48:51], 0\n"
        "v_mfma_f32_16x16x32_bf16 v[96:99], v[80:83], v[44:47], v[96:99]\n"
        "buffer_store_dwordx3 v[148:150], v15, s[8:11], s37 offen offset:768 sc0 sc1 nt\n"
        "v_max_f32_dpp v155, v155, v155 row_ror:1 row_mask:0xf bank_mask:0xf\n"
        "v_mfma_f32_16x16x32_bf16 v[96:99], v[76:79], v[40:43], v[96:99]\n"
        "ds_read_b128 v[60:63], v8 offset:128\n"
        "v_mfma_f32_16x16x32_bf16 v[68:71], v[32:35], v[40:43], 0\n"
        "s_add_u32 s37, s36, 490560\n"
        "ds_read2_b32 v[72:73], v6 offset0:0 offset1:2\n"
        "ds_read2_b32 v[74:75], v6 offset0:8 offset1:10\n"
        "s_waitcnt lgkmcnt(3)\n"
        "v_mul_f32 v52, v52, v20\n"
        "v_mul_f32 v53, v53, v21\n"
        "v_mul_f32 v54, v54, v22\n"
        "v_mul_f32 v55, v55, v23\n"
        "v_mul_f32 v96, v96, v24\n"
        "v_mfma_f32_16x16x32_bf16 v[68:71], v[36:39], v[44:47], v[68:71]\n"
        "v_max3_f32 v159, v52, v53, v54\n"
        "v_mul_f32 v97, v97, v25\n"
        "v_mul_f32 v98, v98, v26\n"
        "v_max3_f32 v159, v159, v55, v96\n"
        "v_mul_f32 v99, v99, v27\n"
        "v_max3_f32 v159, v159, v97, v98\n"
        "v_mfma_f32_16x16x32_bf16 v[68:71], v[56:59], v[48:51], v[68:71]\n"
        "s_waitcnt lgkmcnt(2)\n"
        "v_mfma_f32_16x16x32_bf16 v[108:111], v[60:63], v[48:51], 0\n"
        "buffer_store_dwordx3 v[152:154], v12, s[8:11], s37 offen offset:192 sc0 sc1 nt\n"
        "s_waitcnt lgkmcnt(0)\n"
        "v_smfmac_f32_16x16x64_bf16 v[108:111], v[72:75], v[40:47], v4\n"
        "v_mfma_f32_16x16x32_bf16 v[48:51], v[168:171], v[76:79], 0\n"
        "s_add_u32 s37, s36, 490560\n"
        "v_mfma_f32_16x16x32_bf16 v[48:51], v[172:175], v[80:83], v[48:51]\n"
        "v_mul_f32 v68, v68, v28\n"
        "v_mul_f32 v69, v69, v29\n"
        "v_mul_f32 v70, v70, v30\n"
        "v_max3_f32 v159, v159, v99, v68\n"
        "v_mul_f32 v71, v71, v31\n"
        "v_add_f32 v108, v108, v109\n"
        "v_mul_f32 v108, s58, v108\n"
        "v_max3_f32 v159, v159, v69, v70\n"
        "v_mfma_f32_16x16x32_bf16 v[48:51], v[176:179], v[84:87], v[48:51]\n"
        "s_mov_b64 exec, s[48:49]\n"
        "v_mov_b32 v108, 0xff800000\n"
        "s_mov_b64 exec, -1\n"
        "v_max3_f32 v159, v159, v71, v108\n"
        "v_mfma_f32_16x16x32_bf16 v[44:47], v[188:191], v[84:87], 0\n"
        "buffer_store_dwordx3 v[156:158], v14, s[8:11], s37 offen offset:960 sc0 sc1 nt\n"
        "v_permlane32_swap_b32 v159, v155\n"
        "v_max_f32 v159, v159, v155\n"
        "v_cvt_pk_bf16_f32 v40, v48, v49\n"
        "v_cvt_pk_bf16_f32 v41, v50, v51\n"
        "v_mov_b32 v158, v159\n"
        "v_mfma_f32_16x16x32_bf16 v[44:47], v[184:187], v[80:83], v[44:47]\n"
        "v_mfma_f32_16x16x32_bf16 v[44:47], v[180:183], v[76:79], v[44:47]\n"
        "s_add_u32 s37, s36, 588672\n"
        "v_permlane16_swap_b32 v159, v158\n"
        "v_max_f32 v159, v159, v158\n"
        "v_mov_b32 v155, v159\n"
        "v_mfma_f32_16x16x32_bf16 v[64:67], v[192:195], v[76:79], 0\n"
        "v_mfma_f32_16x16x32_bf16 v[64:67], v[196:199], v[80:83], v[64:67]\n"
        "v_permlane32_swap_b32 v159, v155\n"
        "v_mul_f32 v159, v159, v165\n"
        "v_cvt_pk_bf16_f32 v42, v44, v45\n"
        "v_cvt_pk_bf16_f32 v43, v46, v47\n"
        "v_fma_f32 v52, v52, v165, -v159\n"
        "v_fma_f32 v53, v53, v165, -v159\n"
        "v_fma_f32 v54, v54, v165, -v159\n"
        "v_mfma_f32_16x16x32_bf16 v[64:67], v[200:203], v[84:87], v[64:67]\n"
        "buffer_store_dwordx3 v[160:162], v12, s[8:11], s37 offen offset:384 sc0 sc1 nt\n"
        "v_exp_f32 v52, v52\n"
        "v_exp_f32 v53, v53\n"
        "v_exp_f32 v54, v54\n"
        "v_fma_f32 v55, v55, v165, -v159\n"
        "v_add_f32 v162, v52, v53\n"
        "v_exp_f32 v55, v55\n"
        "v_mfma_f32_16x16x32_bf16 v[48:51], v[212:215], v[84:87], 0\n"
        "v_cvt_pk_bf16_f32 v44, v64, v65\n"
        "v_cvt_pk_bf16_f32 v45, v66, v67\n"
        "v_fma_f32 v96, v96, v165, -v159\n"
        "v_add_f32 v162, v162, v54\n"
        "v_exp_f32 v96, v96\n"
        "v_fma_f32 v97, v97, v165, -v159\n"
        "v_add_f32 v162, v162, v55\n"
        "v_mfma_f32_16x16x32_bf16 v[48:51], v[208:211], v[80:83], v[48:51]\n"
        "v_exp_f32 v97, v97\n"
        "v_fma_f32 v98, v98, v165, -v159\n"
        "v_add_f32 v162, v162, v96\n"
        "v_exp_f32 v98, v98\n"
        "v_fma_f32 v99, v99, v165, -v159\n"
        "v_sub_f32 v164, v164, v155\n"
        "v_mfma_f32_16x16x32_bf16 v[48:51], v[204:207], v[76:79], v[48:51]\n"
        "v_add_f32 v162, v162, v97\n"
        "v_exp_f32 v99, v99\n"
        "v_fma_f32 v68, v68, v165, -v159\n"
        "v_mul_f32 v164, s58, v164\n"
        "v_add_f32 v162, v162, v98\n"
        "v_exp_f32 v68, v68\n"
        "v_mfma_f32_16x16x32_bf16 v[64:67], v[216:219], v[76:79], 0\n"
        "v_fma_f32 v69, v69, v165, -v159\n"
        "v_cvt_pk_bf16_f32 v46, v48, v49\n"
        "v_cvt_pk_bf16_f32 v47, v50, v51\n"
        "v_exp_f32 v164, v164\n"
        "v_add_f32 v162, v162, v99\n"
        "v_exp_f32 v69, v69\n"
        "v_mfma_f32_16x16x32_bf16 v[64:67], v[220:223], v[80:83], v[64:67]\n"
        "v_fma_f32 v70, v70, v165, -v159\n"
        "v_add_f32 v162, v162, v68\n"
        "v_exp_f32 v70, v70\n"
        "v_fma_f32 v71, v71, v165, -v159\n"
        "v_add_f32_dpp v161, v164, v164 row_ror:8 row_mask:0xf bank_mask:0xf\n"
        "v_add_f32 v162, v162, v69\n"
        "v_fma_f32 v108, v108, v165, -v159\n"
        "v_mfma_f32_16x16x32_bf16 v[64:67], v[224:227], v[84:87], v[64:67]\n"
        "v_exp_f32 v71, v71\n"
        "v_add_f32_dpp v161, v161, v161 row_ror:4 row_mask:0xf bank_mask:0xf\n"
        "v_add_f32 v162, v162, v70\n"
        "v_exp_f32 v108, v108\n"
        "v_add_f32_dpp v161, v161, v161 row_ror:2 row_mask:0xf bank_mask:0xf\n"
        "v_add_f32 v162, v162, v71\n"
        "v_mfma_f32_16x16x32_bf16 v[48:51], v[236:239], v[84:87], 0\n"
        "v_add_f32_dpp v161, v161, v161 row_ror:1 row_mask:0xf bank_mask:0xf\n"
        "v_cvt_pk_bf16_f32 v112, v64, v65\n"
        "v_cvt_pk_bf16_f32 v113, v66, v67\n"
        "v_add_f32 v162, v162, v108\n"
        "v_mul_f32 v52, v52, v20\n"
        "v_mul_f32 v53, v53, v21\n"
        "v_permlane32_swap_b32 v162, v161\n"
        "v_mfma_f32_16x16x32_bf16 v[48:51], v[232:235], v[80:83], v[48:51]\n"
        "v_add_f32 v162, v162, v161\n"
        "v_mov_b32 v165, v162\n"
        "v_mul_f32 v54, v54, v22\n"
        "v_mul_f32 v55, v55, v23\n"
        "v_permlane16_swap_b32 v162, v165\n"
        "v_add_f32 v162, v162, v165\n"
        "v_mul_f32 v96, v96, v24\n"
        "v_mfma_f32_16x16x32_bf16 v[48:51], v[228:231], v[76:79], v[48:51]\n"
        "v_rcp_f32 v162, v162\n"
        "v_mul_f32 v97, v97, v25\n"
        "v_mov_b32 v161, v162\n"
        "v_mul_f32 v98, v98, v26\n"
        "v_mul_f32 v99, v99, v27\n"
        "v_permlane32_swap_b32 v162, v161\n"
        "v_mfma_f32_16x16x32_bf16 v[64:67], v[88:91], v[40:43], 0\n"
        "v_mul_f32 v52, v52, v162\n"
        "v_cvt_pk_bf16_f32 v114, v48, v49\n"
        "v_cvt_pk_bf16_f32 v115, v50, v51\n"
        "v_mul_f32 v53, v53, v162\n"
        "v_mul_f32 v54, v54, v162\n"
        "v_mul_f32 v55, v55, v162\n"
        "v_mul_f32 v96, v96, v162\n"
        "v_mul_f32 v97, v97, v162\n"
        "v_mfma_f32_16x16x32_bf16 v[64:67], v[92:95], v[44:47], v[64:67]\n"
        "v_mul_f32 v98, v98, v162\n"
        "v_mul_f32 v99, v99, v162\n"
        "v_cvt_pk_bf16_f32 v48, v52, v53\n"
        "v_cvt_pk_bf16_f32 v49, v54, v55\n"
        "v_cvt_pk_bf16_f32 v50, v96, v97\n"
        "v_cvt_pk_bf16_f32 v51, v98, v99\n"
        "v_mul_f32 v68, v68, v28\n"
        "v_mul_f32 v69, v69, v29\n"
        "v_mfma_f32_16x16x32_bf16 v[64:67], v[100:103], v[112:115], v[64:67]\n"
        "v_mul_f32 v70, v70, v30\n"
        "v_mul_f32 v71, v71, v31\n"
        "v_mul_f32 v108, s58, v108\n"
        "v_mul_f32 v68, v68, v162\n"
        "v_mul_f32 v69, v69, v162\n"
        "v_mul_f32 v70, v70, v162\n"
        "v_mul_f32 v71, v71, v162\n"
        "v_fma_f32 v108, v108, v162, v19\n"
        "v_mfma_f32_16x16x32_bf16 v[104:107], v[84:87], v[112:115], 0\n"
        "v_mul_f32 v64, v64, v20\n"
        "v_mul_f32 v65, v65, v21\n"
        "v_mul_f32 v66, v66, v22\n"
        "v_mul_f32 v67, v67, v23\n"
        "v_max3_f32 v165, v64, v65, v66\n"
        "v_cvt_pk_bf16_f32 v52, v68, v69\n"
        "v_cvt_pk_bf16_f32 v54, v70, v71\n"
        "v_cvt_pk_bf16_f32 v53, v108, 0\n"
        "v_mfma_f32_16x16x32_bf16 v[104:107], v[80:83], v[44:47], v[104:107]\n"
        "v_mul_f32 v164, v164, v163\n"
        "v_mul_f32 v164, v164, v161\n"
        "v_cvt_pk_bf16_f32 v164, v164, 0\n"
        "ds_write_b16 v1, v164\n"
        "v_mfma_f32_16x16x32_bf16 v[104:107], v[76:79], v[40:43], v[104:107]\n"
        "v_mfma_f32_16x16x32_bf16 v[68:71], v[32:35], v[40:43], 0\n"
        "v_mfma_f32_16x16x32_bf16 v[68:71], v[36:39], v[44:47], v[68:71]\n"
        "v_mfma_f32_16x16x32_bf16 v[68:71], v[56:59], v[112:115], v[68:71]\n"
        "v_mfma_f32_16x16x32_bf16 v[96:99], v[60:63], v[112:115], 0\n"
        "v_smfmac_f32_16x16x64_bf16 v[96:99], v[72:75], v[40:47], v4\n"
        "v_mfma_f32_16x16x32_bf16 v[108:111], v[168:171], v[32:35], 0\n"
        "v_mfma_f32_16x16x32_bf16 v[108:111], v[172:175], v[36:39], v[108:111]\n"
        "v_mfma_f32_16x16x32_bf16 v[108:111], v[176:179], v[56:59], v[108:111]\n"
        "v_mul_f32 v104, v104, v24\n"
        "v_mul_f32 v105, v105, v25\n"
        "v_mul_f32 v106, v106, v26\n"
        "v_max3_f32 v165, v165, v67, v104\n"
        "v_mul_f32 v107, v107, v27\n"
        "v_add_f32 v96, v96, v97\n"
        "v_mul_f32 v68, v68, v28\n"
        "v_mul_f32 v96, s58, v96\n"
        "v_mfma_f32_16x16x32_bf16 v[44:47], v[188:191], v[56:59], 0\n"
        "s_mov_b64 exec, s[48:49]\n"
        "v_mov_b32 v96, 0xff800000\n"
        "s_mov_b64 exec, -1\n"
        "v_cvt_pk_bf16_f32 v40, v108, v109\n"
        "v_cvt_pk_bf16_f32 v41, v110, v111\n"
        "v_max3_f32 v165, v165, v105, v106\n"
        "v_mul_f32 v69, v69, v29\n"
        "v_mul_f32 v70, v70, v30\n"
        "v_max3_f32 v165, v165, v107, v68\n"
        "v_mfma_f32_16x16x32_bf16 v[44:47], v[184:187], v[36:39], v[44:47]\n"
        "v_mul_f32 v71, v71, v31\n"
        "v_max3_f32 v165, v165, v69, v70\n"
        "v_max3_f32 v165, v165, v71, v96\n"
        "v_mfma_f32_16x16x32_bf16 v[44:47], v[180:183], v[32:35], v[44:47]\n"
        "v_mfma_f32_16x16x32_bf16 v[112:115], v[192:195], v[32:35], 0\n"
        "v_mfma_f32_16x16x32_bf16 v[112:115], v[196:199], v[36:39], v[112:115]\n"
        "v_mfma_f32_16x16x32_bf16 v[112:115], v[200:203], v[56:59], v[112:115]\n"
        "v_mfma_f32_16x16x32_bf16 v[116:119], v[212:215], v[56:59], 0\n"
        "v_mfma_f32_16x16x32_bf16 v[116:119], v[208:211], v[36:39], v[116:119]\n"
        "v_mfma_f32_16x16x32_bf16 v[116:119], v[204:207], v[32:35], v[116:119]\n"
        "v_mfma_f32_16x16x32_bf16 v[108:111], v[216:219], v[32:35], 0\n"
        "v_mfma_f32_16x16x32_bf16 v[108:111], v[220:223], v[36:39], v[108:111]\n"
        "v_cvt_pk_bf16_f32 v42, v44, v45\n"
        "v_cvt_pk_bf16_f32 v43, v46, v47\n"
        "v_mfma_f32_16x16x32_bf16 v[108:111], v[224:227], v[56:59], v[108:111]\n"
        "v_cvt_pk_bf16_f32 v44, v112, v113\n"
        "v_cvt_pk_bf16_f32 v45, v114, v115\n"
        "v_mfma_f32_16x16x32_bf16 v[112:115], v[236:239], v[56:59], 0\n"
        "v_cvt_pk_bf16_f32 v46, v116, v117\n"
        "v_cvt_pk_bf16_f32 v47, v118, v119\n"
        "v_mfma_f32_16x16x32_bf16 v[112:115], v[232:235], v[36:39], v[112:115]\n"
        "v_mfma_f32_16x16x32_bf16 v[112:115], v[228:231], v[32:35], v[112:115]\n"
        "v_mfma_f32_16x16x32_bf16 v[124:127], v[88:91], v[40:43], 0\n"
        "v_cvt_pk_bf16_f32 v116, v108, v109\n"
        "v_cvt_pk_bf16_f32 v117, v110, v111\n"
        "v_mfma_f32_16x16x32_bf16 v[124:127], v[92:95], v[44:47], v[124:127]\n"
        "s_nop 3\n"
        "v_cvt_pk_bf16_f32 v118, v112, v113\n"
        "v_cvt_pk_bf16_f32 v119, v114, v115\n"
        "s_nop 1\n"
        "v_mfma_f32_16x16x32_bf16 v[124:127], v[100:103], v[116:119], v[124:127]\n"
        "v_mfma_f32_16x16x32_bf16 v[132:135], v[84:87], v[116:119], 0\n"
        "v_mfma_f32_16x16x32_bf16 v[132:135], v[80:83], v[44:47], v[132:135]\n"
        "v_mfma_f32_16x16x32_bf16 v[132:135], v[76:79], v[40:43], v[132:135]\n"
        "v_mfma_f32_16x16x32_bf16 v[112:115], v[32:35], v[40:43], 0\n"
        "v_mfma_f32_16x16x32_bf16 v[112:115], v[36:39], v[44:47], v[112:115]\n"
        "v_mfma_f32_16x16x32_bf16 v[112:115], v[56:59], v[116:119], v[112:115]\n"
        "v_mfma_f32_16x16x32_bf16 v[36:39], v[60:63], v[116:119], 0\n"
        "v_smfmac_f32_16x16x64_bf16 v[36:39], v[72:75], v[40:47], v4\n"
        "v_mul_f32 v124, v124, v20\n"
        "v_mul_f32 v125, v125, v21\n"
        "v_mul_f32 v126, v126, v22\n"
        "v_mul_f32 v127, v127, v23\n"
        "v_mul_f32 v132, v132, v24\n"
        "v_max3_f32 v163, v124, v125, v126\n"
        "v_mul_f32 v133, v133, v25\n"
        "v_mfma_f32_16x16x32_bf16 v[44:47], v[88:91], v[240:243], 0\n"
        "v_add_f32 v36, v36, v37\n"
        "v_mul_f32 v134, v134, v26\n"
        "v_max3_f32 v163, v163, v127, v132\n"
        "v_mul_f32 v135, v135, v27\n"
        "v_mul_f32 v112, v112, v28\n"
        "v_mul_f32 v36, s58, v36\n"
        "v_max3_f32 v163, v163, v133, v134\n"
        "v_mul_f32 v113, v113, v29\n"
        "v_mfma_f32_16x16x32_bf16 v[44:47], v[92:95], v[244:247], v[44:47]\n"
        "s_mov_b64 exec, s[48:49]\n"
        "v_mov_b32 v36, 0xff800000\n"
        "s_mov_b64 exec, -1\n"
        "v_mul_f32 v114, v114, v30\n"
        "v_max3_f32 v163, v163, v135, v112\n"
        "v_mul_f32 v115, v115, v31\n"
        "v_max3_f32 v163, v163, v113, v114\n"
        "v_max3_f32 v163, v163, v115, v36\n"
        "v_mfma_f32_16x16x32_bf16 v[44:47], v[100:103], v[248:251], v[44:47]\n"
        "ds_read_b128 v[72:75], v17 offset:0\n"
        "v_permlane32_swap_b32 v165, v163\n"
        "v_max_f32 v165, v165, v163\n"
        "v_mov_b32 v164, v165\n"
        "v_mfma_f32_16x16x32_bf16 v[40:43], v[84:87], v[248:251], 0\n"
        "ds_read_b128 v[108:111], v17 offset:64\n"
        "v_permlane16_swap_b32 v165, v164\n"
        "v_max_f32 v165, v165, v164\n"
        "v_cvt_pk_bf16_f32 v32, v44, v45\n"
        "v_cvt_pk_bf16_f32 v33, v46, v47\n"
        "v_mov_b32 v163, v165\n"
        "v_mfma_f32_16x16x32_bf16 v[40:43], v[80:83], v[244:247], v[40:43]\n"
        "ds_read_b128 v[116:119], v17 offset:128\n"
        "v_permlane32_swap_b32 v165, v163\n"
        "v_mul_f32 v165, v165, v166\n"
        "v_mul_f32 v163, v163, v167\n"
        "v_fma_f32 v64, v64, v166, -v165\n"
        "v_fma_f32 v65, v65, v166, -v165\n"
        "v_fma_f32 v124, v124, v167, -v163\n"
        "v_mfma_f32_16x16x32_bf16 v[40:43], v[76:79], v[240:243], v[40:43]\n"
        "s_waitcnt lgkmcnt(2)\n"
        "ds_read_b128 v[120:123], v0 offset:5136\n"
        "v_fma_f32 v125, v125, v167, -v163\n"
        "v_exp_f32 v64, v64\n"
        "v_exp_f32 v65, v65\n"
        "v_fma_f32 v66, v66, v166, -v165\n"
        "v_fma_f32 v126, v126, v167, -v163\n"
        "v_mfma_f32_16x16x32_bf16 v[44:47], v[72:75], v[240:243], 0\n"
        "s_waitcnt lgkmcnt(2)\n"
        "ds_read_b128 v[128:131], v0 offset:4112\n"
        "v_cvt_pk_bf16_f32 v34, v40, v41\n"
        "v_cvt_pk_bf16_f32 v35, v42, v43\n"
        "v_exp_f32 v124, v124\n"
        "v_exp_f32 v125, v125\n"
        "v_fma_f32 v67, v67, v166, -v165\n"
        "v_mfma_f32_16x16x32_bf16 v[44:47], v[108:111], v[244:247], v[44:47]\n"
        "s_waitcnt lgkmcnt(2)\n"
        "v_exp_f32 v66, v66\n"
        "v_exp_f32 v126, v126\n"
        "v_fma_f32 v127, v127, v167, -v163\n"
        "v_add_f32 v99, v64, v65\n"
        "v_exp_f32 v67, v67\n"
        "v_mfma_f32_16x16x32_bf16 v[44:47], v[116:119], v[248:251], v[44:47]\n"
        "s_waitcnt lgkmcnt(1)\n"
        "v_fma_f32 v104, v104, v166, -v165\n"
        "v_add_f32 v164, v124, v125\n"
        "v_exp_f32 v127, v127\n"
        "v_fma_f32 v132, v132, v167, -v163\n"
        "v_add_f32 v99, v99, v66\n"
        "v_exp_f32 v104, v104\n"
        "v_mfma_f32_16x16x32_bf16 v[136:139], v[116:119], v[120:123], 0\n"
        "s_waitcnt lgkmcnt(0)\n"
        "v_cvt_pk_bf16_f32 v40, v44, v45\n"
        "v_cvt_pk_bf16_f32 v44, v46, v47\n"
        "v_fma_f32 v105, v105, v166, -v165\n"
        "v_add_f32 v164, v164, v126\n"
        "v_exp_f32 v132, v132\n"
        "v_fma_f32 v133, v133, v167, -v163\n"
        "v_add_f32 v99, v99, v67\n"
        "v_mfma_f32_16x16x32_bf16 v[136:139], v[108:111], v[128:131], v[136:139]\n"
        "v_exp_f32 v105, v105\n"
        "v_fma_f32 v106, v106, v166, -v165\n"
        "v_add_f32 v164, v164, v127\n"
        "v_exp_f32 v133, v133\n"
        "v_fma_f32 v134, v134, v167, -v163\n"
        "v_add_f32 v99, v99, v104\n"
        "v_mfma_f32_16x16x32_bf16 v[136:139], v[72:75], v[252:255], v[136:139]\n"
        "v_exp_f32 v106, v106\n"
        "v_fma_f32 v107, v107, v166, -v165\n"
        "v_add_f32 v164, v164, v132\n"
        "v_exp_f32 v134, v134\n"
        "v_fma_f32 v135, v135, v167, -v163\n"
        "v_add_f32 v99, v99, v105\n"
        "v_mfma_f32_16x16x32_bf16 v[140:143], v[76:79], v[252:255], 0\n"
        "v_exp_f32 v107, v107\n"
        "v_cvt_pk_bf16_f32 v56, v136, v137\n"
        "v_cvt_pk_bf16_f32 v60, v138, v139\n"
        "v_fma_f32 v68, v68, v166, -v165\n"
        "v_add_f32 v164, v164, v133\n"
        "v_exp_f32 v135, v135\n"
        "v_mfma_f32_16x16x32_bf16 v[140:143], v[80:83], v[128:131], v[140:143]\n"
        "v_fma_f32 v112, v112, v167, -v163\n"
        "v_add_f32 v99, v99, v106\n"
        "v_exp_f32 v68, v68\n"
        "v_fma_f32 v69, v69, v166, -v165\n"
        "v_add_f32 v164, v164, v134\n"
        "v_exp_f32 v112, v112\n"
        "v_mfma_f32_16x16x32_bf16 v[140:143], v[84:87], v[120:123], v[140:143]\n"
        "ds_read_b128 v[148:151], v0 offset:6176\n"
        "v_fma_f32 v113, v113, v167, -v163\n"
        "v_add_f32 v99, v99, v107\n"
        "v_exp_f32 v69, v69\n"
        "v_fma_f32 v70, v70, v166, -v165\n"
        "v_add_f32 v164, v164, v135\n"
        "v_fma_f32 v114, v114, v167, -v163\n"
        "v_mfma_f32_16x16x32_bf16 v[152:155], v[100:103], v[120:123], 0\n"
        "ds_read_b128 v[144:147], v0 offset:7200\n"
        "v_cvt_pk_bf16_f32 v122, v140, v141\n"
        "v_cvt_pk_bf16_f32 v123, v142, v143\n"
        "v_exp_f32 v113, v113\n"
        "v_add_f32 v99, v99, v68\n"
        "v_exp_f32 v70, v70\n"
        "v_mfma_f32_16x16x32_bf16 v[152:155], v[92:95], v[128:131], v[152:155]\n"
        "ds_read_b128 v[140:143], v0 offset:8224\n"
        "v_fma_f32 v71, v71, v166, -v165\n"
        "v_add_f32 v164, v164, v112\n"
        "v_exp_f32 v114, v114\n"
        "v_fma_f32 v115, v115, v167, -v163\n"
        "v_add_f32 v99, v99, v69\n"
        "v_fma_f32 v96, v96, v166, -v165\n"
        "v_mfma_f32_16x16x32_bf16 v[152:155], v[88:91], v[252:255], v[152:155]\n"
        "s_waitcnt lgkmcnt(2)\n"
        "v_exp_f32 v71, v71\n"
        "v_add_f32 v164, v164, v113\n"
        "v_exp_f32 v115, v115\n"
        "v_fma_f32 v36, v36, v167, -v163\n"
        "v_add_f32 v99, v99, v70\n"
        "v_add_f32 v164, v164, v114\n"
        "v_mfma_f32_16x16x32_bf16 v[136:139], v[88:91], v[148:151], 0\n"
        "s_waitcnt lgkmcnt(1)\n"
        "v_cvt_pk_bf16_f32 v120, v152, v153\n"
        "v_cvt_pk_bf16_f32 v121, v154, v155\n"
        "v_exp_f32 v96, v96\n"
        "v_exp_f32 v36, v36\n"
        "v_add_f32 v99, v99, v71\n"
        "v_add_f32 v164, v164, v115\n"
        "v_mfma_f32_16x16x32_bf16 v[136:139], v[92:95], v[144:147], v[136:139]\n"
        "s_waitcnt lgkmcnt(0)\n"
        "v_add_f32 v99, v99, v96\n"
        "v_add_f32 v164, v164, v36\n"
        "v_mul_f32 v64, v64, v20\n"
        "v_mul_f32 v65, v65, v21\n"
        "v_permlane32_swap_b32 v99, v164\n"
        "v_add_f32 v99, v99, v164\n"
        "v_mov_b32 v167, v99\n"
        "v_mfma_f32_16x16x32_bf16 v[136:139], v[100:103], v[140:143], v[136:139]\n"
        "v_mul_f32 v66, v66, v22\n"
        "v_permlane16_swap_b32 v99, v167\n"
        "v_add_f32 v99, v99, v167\n"
        "v_rcp_f32 v99, v99\n"
        "v_mul_f32 v67, v67, v23\n"
        "v_mov_b32 v164, v99\n"
        "v_mfma_f32_16x16x32_bf16 v[160:163], v[84:87], v[140:143], 0\n"
        "v_mul_f32 v104, v104, v24\n"
        "v_cvt_pk_bf16_f32 v128, v136, v137\n"
        "v_cvt_pk_bf16_f32 v129, v138, v139\n"
        "v_permlane32_swap_b32 v99, v164\n"
        "v_mul_f32 v105, v105, v25\n"
        "v_mul_f32 v106, v106, v26\n"
        "v_mul_f32 v107, v107, v27\n"
        "v_mfma_f32_16x16x32_bf16 v[160:163], v[80:83], v[144:147], v[160:163]\n"
        "v_mul_f32 v64, v64, v99\n"
        "v_mul_f32 v65, v65, v99\n"
        "v_mul_f32 v66, v66, v99\n"
        "v_mul_f32 v67, v67, v99\n"
        "v_mul_f32 v104, v104, v99\n"
        "v_mul_f32 v105, v105, v99\n"
        "v_mul_f32 v106, v106, v99\n"
        "v_mul_f32 v107, v107, v99\n"
        "v_mfma_f32_16x16x32_bf16 v[160:163], v[76:79], v[148:151], v[160:163]\n"
        "ds_read_b128 v[152:155], v0 offset:11312\n"
        "v_cvt_pk_bf16_f32 v136, v64, v65\n"
        "v_cvt_pk_bf16_f32 v137, v66, v67\n"
        "v_cvt_pk_bf16_f32 v138, v104, v105\n"
        "v_cvt_pk_bf16_f32 v139, v106, v107\n"
        "v_mul_f32 v68, v68, v28\n"
        "v_mul_f32 v69, v69, v29\n"
        "v_mul_f32 v70, v70, v30\n"
        "v_mfma_f32_16x16x32_bf16 v[64:67], v[72:75], v[148:151], 0\n"
        "ds_read_b128 v[156:159], v0 offset:10288\n"
        "v_cvt_pk_bf16_f32 v130, v160, v161\n"
        "v_cvt_pk_bf16_f32 v131, v162, v163\n"
        "v_mul_f32 v71, v71, v31\n"
        "v_mul_f32 v96, s58, v96\n"
        "v_mul_f32 v68, v68, v99\n"
        "v_mul_f32 v69, v69, v99\n"
        "v_mul_f32 v70, v70, v99\n"
        "v_mfma_f32_16x16x32_bf16 v[64:67], v[108:111], v[144:147], v[64:67]\n"
        "ds_read_b128 v[148:151], v0 offset:9264\n"
        "v_mul_f32 v71, v71, v99\n"
        "v_fma_f32 v96, v96, v99, v19\n"
        "v_cvt_pk_bf16_f32 v104, v68, v69\n"
        "v_cvt_pk_bf16_f32 v106, v70, v71\n"
        "v_cvt_pk_bf16_f32 v105, v96, 0\n"
        "v_mul_f32 v124, v124, v20\n"
        "v_mul_f32 v125, v125, v21\n"
        "v_mfma_f32_16x16x32_bf16 v[64:67], v[116:119], v[140:143], v[64:67]\n"
        "s_waitcnt lgkmcnt(2)\n"
        "v_mul_f32 v126, v126, v22\n"
        "v_mul_f32 v127, v127, v23\n"
        "v_mul_f32 v132, v132, v24\n"
        "v_mul_f32 v133, v133, v25\n"
        "v_mul_f32 v134, v134, v26\n"
        "v_mul_f32 v135, v135, v27\n"
        "v_mul_f32 v124, v124, v164\n"
        "v_cvt_pk_bf16_f32 v20, v64, v65\n"
        "v_mfma_f32_16x16x32_bf16 v[68:71], v[116:119], v[152:155], 0\n"
        "s_waitcnt lgkmcnt(1)\n"
        "v_cvt_pk_bf16_f32 v24, v66, v67\n"
        "v_mul_f32 v125, v125, v164\n"
        "v_mul_f32 v126, v126, v164\n"
        "v_mul_f32 v127, v127, v164\n"
        "v_mul_f32 v132, v132, v164\n"
        "v_mul_f32 v133, v133, v164\n"
        "v_mul_f32 v134, v134, v164\n"
        "v_mul_f32 v135, v135, v164\n"
        "v_mfma_f32_16x16x32_bf16 v[68:71], v[108:111], v[156:159], v[68:71]\n"
        "s_waitcnt lgkmcnt(0)\n"
        "v_cvt_pk_bf16_f32 v64, v124, v125\n"
        "v_cvt_pk_bf16_f32 v65, v126, v127\n"
        "v_cvt_pk_bf16_f32 v66, v132, v133\n"
        "v_cvt_pk_bf16_f32 v67, v134, v135\n"
        "v_mul_f32 v112, v112, v28\n"
        "v_mul_f32 v113, v113, v29\n"
        "v_mul_f32 v114, v114, v30\n"
        "v_mul_f32 v115, v115, v31\n"
        "v_mfma_f32_16x16x32_bf16 v[68:71], v[72:75], v[148:151], v[68:71]\n"
        "v_mul_f32 v36, s58, v36\n"
        "v_mul_f32 v112, v112, v164\n"
        "v_mul_f32 v113, v113, v164\n"
        "v_mul_f32 v114, v114, v164\n"
        "v_mul_f32 v115, v115, v164\n"
        "v_fma_f32 v36, v36, v164, v19\n"
        "v_cvt_pk_bf16_f32 v144, v112, v113\n"
        "v_cvt_pk_bf16_f32 v146, v114, v115\n"
        "v_mfma_f32_16x16x32_bf16 v[28:31], v[76:79], v[148:151], 0\n"
        "v_cvt_pk_bf16_f32 v41, v68, v69\n"
        "v_cvt_pk_bf16_f32 v45, v70, v71\n"
        "v_cvt_pk_bf16_f32 v145, v36, 0\n"
        "v_mfma_f32_16x16x32_bf16 v[28:31], v[80:83], v[156:159], v[28:31]\n"
        "v_mfma_f32_16x16x32_bf16 v[28:31], v[84:87], v[152:155], v[28:31]\n"
        "ds_read_b128 v[96:99], v0 offset:12352\n"
        "v_mfma_f32_16x16x32_bf16 v[68:71], v[100:103], v[152:155], 0\n"
        "ds_read_b128 v[132:135], v0 offset:13376\n"
        "v_mfma_f32_16x16x32_bf16 v[68:71], v[92:95], v[156:159], v[68:71]\n"
        "ds_read_b128 v[112:115], v0 offset:14400\n"
        "v_mfma_f32_16x16x32_bf16 v[68:71], v[88:91], v[148:151], v[68:71]\n"
        "s_waitcnt lgkmcnt(2)\n"
        "v_mfma_f32_16x16x32_bf16 v[148:151], v[88:91], v[96:99], 0\n"
        "v_cvt_pk_bf16_f32 v38, v28, v29\n"
        "v_cvt_pk_bf16_f32 v39, v30, v31\n"
        "s_waitcnt lgkmcnt(1)\n"
        "v_mfma_f32_16x16x32_bf16 v[148:151], v[92:95], v[132:135], v[148:151]\n"
        "s_waitcnt lgkmcnt(0)\n"
        "v_mfma_f32_16x16x32_bf16 v[148:151], v[100:103], v[112:115], v[148:151]\n"
        "v_cvt_pk_bf16_f32 v36, v68, v69\n"
        "v_cvt_pk_bf16_f32 v37, v70, v71\n"
        "v_mfma_f32_16x16x32_bf16 v[152:155], v[84:87], v[112:115], 0\n"
        "v_mfma_f32_16x16x32_bf16 v[152:155], v[80:83], v[132:135], v[152:155]\n"
        "v_mfma_f32_16x16x32_bf16 v[152:155], v[76:79], v[96:99], v[152:155]\n"
        "ds_read_b128 v[68:71], v0 offset:17488\n"
        "v_mfma_f32_16x16x32_bf16 v[140:143], v[72:75], v[96:99], 0\n"
        "ds_read_b128 v[28:31], v0 offset:16464\n"
        "v_cvt_pk_bf16_f32 v124, v148, v149\n"
        "v_cvt_pk_bf16_f32 v125, v150, v151\n"
        "v_mfma_f32_16x16x32_bf16 v[140:143], v[108:111], v[132:135], v[140:143]\n"
        "ds_read_b128 v[96:99], v0 offset:15440\n"
        "v_mfma_f32_16x16x32_bf16 v[140:143], v[116:119], v[112:115], v[140:143]\n"
        "v_cvt_pk_bf16_f32 v126, v152, v153\n"
        "v_cvt_pk_bf16_f32 v127, v154, v155\n"
        "s_waitcnt lgkmcnt(2)\n"
        "v_mfma_f32_16x16x32_bf16 v[112:115], v[116:119], v[68:71], 0\n"
        "s_waitcnt lgkmcnt(1)\n"
        "v_mfma_f32_16x16x32_bf16 v[112:115], v[108:111], v[28:31], v[112:115]\n"
        "s_waitcnt lgkmcnt(0)\n"
        "v_mfma_f32_16x16x32_bf16 v[112:115], v[72:75], v[96:99], v[112:115]\n"
        "ds_read2_b32 v[46:47], v11 offset0:0 offset1:3\n"
        "v_cvt_pk_bf16_f32 v57, v140, v141\n"
        "v_cvt_pk_bf16_f32 v61, v142, v143\n"
        "v_mov_b32_dpp v55, v19 row_shr:4 row_mask:0xf bank_mask:0xf bound_ctrl:0\n"
        "v_mfma_f32_16x16x32_bf16 v[72:75], v[76:79], v[96:99], 0\n"
        "ds_read_b64_tr_b16 v[42:43], v9 offset:0\n"
        "v_mfma_f32_16x16x32_bf16 v[72:75], v[80:83], v[28:31], v[72:75]\n"
        "ds_read2_b32 v[62:63], v11 offset0:1 offset1:4\n"
        "v_cvt_pk_bf16_f32 v21, v112, v113\n"
        "v_cvt_pk_bf16_f32 v25, v114, v115\n"
        "v_mfma_f32_16x16x32_bf16 v[72:75], v[84:87], v[68:71], v[72:75]\n"
        "ds_read_b64_tr_b16 v[58:59], v9 offset:16\n"
        "v_mfma_f32_16x16x32_bf16 v[76:79], v[100:103], v[68:71], 0\n"
        "ds_read2_b32 v[26:27], v11 offset0:2 offset1:5\n"
        "v_mfma_f32_16x16x32_bf16 v[76:79], v[92:95], v[28:31], v[76:79]\n"
        "s_add_u32 s31, s31, 1\n"
        "ds_read_b64_tr_b16 v[22:23], v9 offset:32\n"
        "v_mfma_f32_16x16x32_bf16 v[76:79], v[88:91], v[96:99], v[76:79]\n"
        "s_mul_i32 s38, s31, 1344\n"
        "v_cvt_pk_bf16_f32 v134, v72, v73\n"
        "v_cvt_pk_bf16_f32 v135, v74, v75\n"
        "v_mfma_f32_16x16x32_bf16 v[28:31], v[48:51], v[32:35], 0\n"
        "s_waitcnt lgkmcnt(4)\n"
        "s_cmp_ge_u32 s31, s76\n"
        "v_smfmac_f32_16x16x64_bf16 v[28:31], v[52:55], v[40:47], v18\n"
        "s_cbranch_scc1 L49A_%=\n"
        "L50A_%=:\n"
        "v_cvt_pk_bf16_f32 v132, v76, v77\n"
        "v_cvt_pk_bf16_f32 v133, v78, v79\n"
        "v_mfma_f32_16x16x32_bf16 v[68:71], v[48:51], v[120:123], 0\n"
        "s_waitcnt lgkmcnt(2)\n"
        "s_cmp_ge_u32 s31, s71\n"
        "v_smfmac_f32_16x16x64_bf16 v[68:71], v[52:55], v[56:63], v18\n"
        "s_cselect_b32 s39, s70, s72\n"
        "ds_read_b64_tr_b16 v[42:43], v9 offset:48\n"
        "v_mfma_f32_16x16x32_bf16 v[80:83], v[48:51], v[128:131], 0\n"
        "s_waitcnt lgkmcnt(1)\n"
        "s_cmp_ge_u32 s31, 20\n"
        "v_smfmac_f32_16x16x64_bf16 v[80:83], v[52:55], v[20:27], v18\n"
        "s_cselect_b32 s39, s22, s39\n"
        "ds_read_b64_tr_b16 v[58:59], v9 offset:64\n"
        "v_cvt_pk_bf16_f32 v164, v28, v68\n"
        "v_cvt_pk_bf16_f32 v152, v29, v69\n"
        "v_cvt_pk_bf16_f32 v156, v30, v70\n"
        "v_cvt_pk_bf16_f32 v160, v31, v71\n"
        "v_mfma_f32_16x16x32_bf16 v[84:87], v[48:51], v[36:39], 0\n"
        "s_cmp_ge_u32 s31, 21\n"
        "s_waitcnt lgkmcnt(1)\n"
        "v_smfmac_f32_16x16x64_bf16 v[84:87], v[52:55], v[40:47], v18 abid:1\n"
        "s_cselect_b32 s39, s23, s39\n"
        "ds_read_b64_tr_b16 v[22:23], v9 offset:80\n"
        "v_mfma_f32_16x16x32_bf16 v[76:79], v[48:51], v[124:127], 0\n"
        "s_add_u32 s39, s39, s38\n"
        "ds_read_b128 v[68:71], v2\n"
        "v_mov_b64 v[28:29], 0\n"
        "v_mov_b64 v[30:31], 0\n"
        "s_waitcnt lgkmcnt(2)\n"
        "v_smfmac_f32_16x16x64_bf16 v[76:79], v[52:55], v[56:63], v18 abid:1\n"
        "s_mul_i32 s68, s31, 3\n"
        "ds_read_b128 v[72:75], v3\n"
        "v_cvt_pk_bf16_f32 v165, v80, v84\n"
        "v_cvt_pk_bf16_f32 v153, v81, v85\n"
        "v_cvt_pk_bf16_f32 v157, v82, v86\n"
        "v_cvt_pk_bf16_f32 v161, v83, v87\n"
        "v_mfma_f32_16x16x32_bf16 v[80:83], v[48:51], v[132:135], 0\n"
        "s_lshr_b32 s38, s31, 3\n"
        "v_mov_b64 v[48:49], 0\n"
        "v_mov_b64 v[50:51], 0\n"
        "s_waitcnt lgkmcnt(2)\n"
        "v_smfmac_f32_16x16x64_bf16 v[80:83], v[52:55], v[20:27], v18 abid:1\n"
        "s_add_u32 s38, s38, 11\n"
        "s_waitcnt lgkmcnt(1)\n"
        "v_mov_b32_dpp v107, v19 row_shr:4 row_mask:0xf bank_mask:0xf bound_ctrl:0\n"
        "v_smfmac_f32_16x16x64_bf16 v[28:31], v[68:71], v[32:39], v4\n"
        "s_bitcmp1_b32 s31, 0\n"
        "v_mov_b64 v[52:53], 0\n"
        "v_mov_b64 v[54:55], 0\n"
        "s_waitcnt lgkmcnt(0)\n"
        "v_smfmac_f32_16x16x64_bf16 v[28:31], v[72:75], v[40:47], v4 abid:1\n"
        "s_cselect_b32 s38, 3, s38\n"
        "s_add_u32 s37, s35, 0\n"
        "v_cvt_pk_bf16_f32 v166, v76, v80\n"
        "v_cvt_pk_bf16_f32 v154, v77, v81\n"
        "v_cvt_pk_bf16_f32 v158, v78, v82\n"
        "v_cvt_pk_bf16_f32 v162, v79, v83\n"
        "v_smfmac_f32_16x16x64_bf16 v[48:51], v[68:71], v[120:127], v4\n"
        "s_add_u32 s68, s68, s38\n"
        "v_add_f32 v28, v28, v29\n"
        "v_add_f32 v30, v30, v31\n"
        "v_smfmac_f32_16x16x64_bf16 v[48:51], v[72:75], v[56:63], v4 abid:1\n"
        "s_cmp_lt_u32 s31, s14\n"
        "ds_read_b64_tr_b16 v[42:43], v9 offset:3328\n"
        "v_smfmac_f32_16x16x64_bf16 v[52:55], v[68:71], v[128:135], v4\n"
        "buffer_store_dwordx3 v[164:166], v12, s[8:11], s37 offen sc0 sc1\n"
        "s_cselect_b32 s68, s68, 0\n"
        "v_smfmac_f32_16x16x64_bf16 v[52:55], v[72:75], v[20:27], v4 abid:1\n"
        "s_and_b32 s38, s31, 7\n"
        "ds_read_b64_tr_b16 v[58:59], v9 offset:3344\n"
        "v_add_f32 v48, v48, v49\n"
        "v_add_f32 v50, v50, v51\n"
        "v_cvt_pk_bf16_f32 v100, v28, v48\n"
        "v_mfma_f32_16x16x32_bf16 v[140:143], v[136:139], v[32:35], 0\n"
        "s_cselect_b32 s30, 1, -7\n"
        "s_waitcnt lgkmcnt(1)\n"
        "v_smfmac_f32_16x16x64_bf16 v[140:143], v[104:107], v[40:47], v18\n"
        "s_add_u32 s37, s35, 0\n"
        "s_mul_i32 s29, s30, 192\n"
        "ds_read_b64_tr_b16 v[22:23], v9 offset:3360\n"
        "v_add_f32 v52, v52, v53\n"
        "v_add_f32 v54, v54, v55\n"
        "v_cvt_pk_bf16_f32 v101, v52, v30\n"
        "v_cvt_pk_bf16_f32 v102, v50, v54\n"
        "v_mfma_f32_16x16x32_bf16 v[164:167], v[136:139], v[120:123], 0\n"
        "s_mul_i32 s30, s30, 392\n"
        "s_waitcnt lgkmcnt(1)\n"
        "v_smfmac_f32_16x16x64_bf16 v[164:167], v[104:107], v[56:63], v18\n"
        "s_cmp_eq_u32 s73, s18\n"
        "ds_read_b64_tr_b16 v[42:43], v9 offset:3376\n"
        "v_mfma_f32_16x16x32_bf16 v[108:111], v[136:139], v[128:131], 0\n"
        "buffer_store_dwordx3 v[152:154], v15, s[8:11], s37 offen offset:768 sc0 sc1 nt\n"
        "s_cselect_b32 s34, -7488, s75\n"
        "s_waitcnt lgkmcnt(1)\n"
        "v_smfmac_f32_16x16x64_bf16 v[108:111], v[104:107], v[20:27], v18\n"
        "s_add_u32 s73, s73, s34\n"
        "ds_read_b64_tr_b16 v[58:59], v9 offset:3392\n"
        "v_mov_b32_dpp v147, v19 row_shr:4 row_mask:0xf bank_mask:0xf bound_ctrl:0\n"
        "v_writelane_b32 v10, s73, 56\n"
        "v_add_u32 v11, s30, v11\n"
        "v_mfma_f32_16x16x32_bf16 v[68:71], v[136:139], v[36:39], 0\n"
        "s_mov_b64 exec, s[62:63]\n"
        "v_add_u32 v16, s29, v16\n"
        "v_add_u32 v8, s34, v8\n"
        "s_mov_b64 exec, -1\n"
        "s_mov_b64 exec, s[50:51]\n"
        "v_add_u32 v5, s29, v5\n"
        "v_add_u32 v6, s34, v6\n"
        "s_mov_b64 exec, -1\n"
        "s_waitcnt lgkmcnt(1)\n"
        "v_smfmac_f32_16x16x64_bf16 v[68:71], v[104:107], v[40:47], v18 abid:1\n"
        "s_add_u32 s37, s35, 98112\n"
        "ds_read_b64_tr_b16 v[22:23], v9 offset:3408\n"
        "v_mfma_f32_16x16x32_bf16 v[148:151], v[136:139], v[124:127], 0\n"
        "s_waitcnt lgkmcnt(1)\n"
        "v_smfmac_f32_16x16x64_bf16 v[148:151], v[104:107], v[56:63], v18 abid:1\n"
        "ds_read_b64_tr_b16 v[42:43], v9 offset:6656\n"
        "v_mfma_f32_16x16x32_bf16 v[72:75], v[136:139], v[132:135], 0\n"
        "buffer_store_dwordx3 v[156:158], v12, s[8:11], s37 offen offset:192 sc0 sc1 nt\n"
        "ds_read_b32 v163, v0 offset:21604\n"
        "s_waitcnt lgkmcnt(2)\n"
        "v_smfmac_f32_16x16x64_bf16 v[72:75], v[104:107], v[20:27], v18 abid:1\n"
        "ds_read_b64_tr_b16 v[58:59], v9 offset:6672\n"
        "v_mfma_f32_16x16x32_bf16 v[116:119], v[64:67], v[32:35], 0\n"
        "s_waitcnt lgkmcnt(2)\n"
        "v_smfmac_f32_16x16x64_bf16 v[116:119], v[144:147], v[40:47], v18\n"
        "s_add_u32 s37, s35, 98112\n"
        "ds_read_b64_tr_b16 v[22:23], v9 offset:6688\n"
        "v_mfma_f32_16x16x32_bf16 v[28:31], v[64:67], v[120:123], 0\n"
        "s_waitcnt lgkmcnt(1)\n"
        "v_smfmac_f32_16x16x64_bf16 v[28:31], v[144:147], v[56:63], v18\n"
        "ds_read_b64_tr_b16 v[42:43], v9 offset:6704\n"
        "v_readfirstlane_b32 s40, v163\n"
        "v_mfma_f32_16x16x32_bf16 v[112:115], v[64:67], v[128:131], 0\n"
        "buffer_store_dwordx3 v[160:162], v14, s[8:11], s37 offen offset:960 sc0 sc1 nt\n"
        "s_add_u32 s37, s35, 588672\n"
        "s_add_u32 s46, s37, 1344\n"
        "s_cmp_lt_i32 s40, s68\n"
        "s_waitcnt lgkmcnt(1)\n"
        "v_smfmac_f32_16x16x64_bf16 v[112:115], v[144:147], v[20:27], v18\n"
        "buffer_store_dwordx3 v[100:102], v12, s[44:47], s37 offen offset:1152 sc0 sc1 nt\n"
        "ds_read_b64_tr_b16 v[58:59], v9 offset:6720\n"
        "v_mfma_f32_16x16x32_bf16 v[96:99], v[64:67], v[36:39], 0\n"
        "s_waitcnt lgkmcnt(1)\n"
        "v_smfmac_f32_16x16x64_bf16 v[96:99], v[144:147], v[40:47], v18 abid:1\n"
        "ds_read_b64_tr_b16 v[22:23], v9 offset:6736\n"
        "s_mov_b64 exec, 1\n"
        "ds_inc_u32 v0, v1 offset:21608\n"
        "s_mov_b64 exec, -1\n"
        "s_cbranch_scc1 L53A_%=\n"
        "L54A_%=:\n"
        "ds_read_b128 v[88:91], v7 offset:40064\n"
        "ds_read_b128 v[76:79], v7 offset:43392\n"
        "ds_read_b128 v[92:95], v7 offset:40128\n"
        "ds_read_b128 v[80:83], v7 offset:43456\n"
        "v_mfma_f32_16x16x32_bf16 v[52:55], v[64:67], v[124:127], 0\n"
        "s_waitcnt lgkmcnt(6)\n"
        "ds_read_b128 v[100:103], v7 offset:40192\n"
        "ds_read_b128 v[84:87], v7 offset:43520\n"
        "s_mov_b32 s36, s35\n"
        "v_smfmac_f32_16x16x64_bf16 v[52:55], v[144:147], v[56:63], v18 abid:1\n"
        "s_mov_b32 s35, s39\n"
        "v_mfma_f32_16x16x32_bf16 v[56:59], v[64:67], v[132:135], 0\n"
        "s_waitcnt lgkmcnt(7)\n"
        "v_smfmac_f32_16x16x64_bf16 v[56:59], v[144:147], v[20:27], v18 abid:1\n"
        "s_setprio 0\n"
        "s_waitcnt lgkmcnt(0)\n"
        "s_nop 15\n"
        "s_branch CNEXTA_%=\n"
        "MTOPB_%=:\n"
        "s_waitcnt lgkmcnt(5)\n"
        "v_mfma_f32_16x16x32_bf16 v[20:23], v[168:171], v[88:91], 0\n"
        "v_cvt_pk_bf16_f32 v132, v140, v164\n"
        "v_cvt_pk_bf16_f32 v133, v108, v68\n"
        "v_cvt_pk_bf16_f32 v134, v148, v72\n"
        "v_cvt_pk_bf16_f32 v136, v141, v165\n"
        "v_cvt_pk_bf16_f32 v137, v109, v69\n"
        "v_cvt_pk_bf16_f32 v138, v149, v73\n"
        "v_cvt_pk_bf16_f32 v140, v142, v166\n"
        "v_cvt_pk_bf16_f32 v141, v110, v70\n"
        "s_waitcnt lgkmcnt(3)\n"
        "v_mfma_f32_16x16x32_bf16 v[20:23], v[172:175], v[92:95], v[20:23]\n"
        "v_cvt_pk_bf16_f32 v142, v150, v74\n"
        "v_cvt_pk_bf16_f32 v144, v143, v167\n"
        "v_cvt_pk_bf16_f32 v145, v111, v71\n"
        "v_cvt_pk_bf16_f32 v146, v151, v75\n"
        "v_cvt_pk_bf16_f32 v148, v116, v28\n"
        "v_cvt_pk_bf16_f32 v149, v112, v96\n"
        "v_cvt_pk_bf16_f32 v150, v52, v56\n"
        "v_cvt_pk_bf16_f32 v152, v117, v29\n"
        "s_waitcnt lgkmcnt(1)\n"
        "v_mfma_f32_16x16x32_bf16 v[20:23], v[176:179], v[100:103], v[20:23]\n"
        "v_cvt_pk_bf16_f32 v153, v113, v97\n"
        "v_cvt_pk_bf16_f32 v154, v53, v57\n"
        "v_cvt_pk_bf16_f32 v156, v118, v30\n"
        "v_cvt_pk_bf16_f32 v157, v114, v98\n"
        "v_cvt_pk_bf16_f32 v158, v54, v58\n"
        "v_cvt_pk_bf16_f32 v160, v119, v31\n"
        "v_cvt_pk_bf16_f32 v161, v115, v99\n"
        "v_cvt_pk_bf16_f32 v162, v55, v59\n"
        "v_mfma_f32_16x16x32_bf16 v[24:27], v[188:191], v[100:103], 0\n"
        "v_cvt_pk_bf16_f32 v40, v20, v21\n"
        "v_cvt_pk_bf16_f32 v41, v22, v23\n"
        "v_mfma_f32_16x16x32_bf16 v[24:27], v[184:187], v[92:95], v[24:27]\n"
        "s_setprio 2\n"
        "v_mfma_f32_16x16x32_bf16 v[24:27], v[180:183], v[88:91], v[24:27]\n"
        "s_add_u32 s37, s36, 196224\n"
        "ds_read_b128 v[28:31], v16 offset:128\n"
        "ds_read_b32 v163, v10\n"
        "v_mfma_f32_16x16x32_bf16 v[32:35], v[192:195], v[88:91], 0\n"
        "ds_read_b128 v[20:23], v5\n"
        "v_mfma_f32_16x16x32_bf16 v[32:35], v[196:199], v[92:95], v[32:35]\n"
        "ds_read_b64 v[164:165], v11 offset:192\n"
        "v_mfma_f32_16x16x32_bf16 v[32:35], v[200:203], v[100:103], v[32:35]\n"
        "buffer_store_dwordx3 v[132:134], v12, s[8:11], s37 offen offset:384 sc0 sc1 nt\n"
        "v_cvt_pk_bf16_f32 v42, v24, v25\n"
        "v_cvt_pk_bf16_f32 v43, v26, v27\n"
        "s_waitcnt lgkmcnt(3)\n"
        "v_mfma_f32_16x16x32_bf16 v[48:51], v[28:31], v[100:103], 0\n"
        "ds_read_b128 v[56:59], v7 offset:46848\n"
        "ds_read_i16 v167, v0 offset:20584\n"
        "ds_read_i16 v159, v0 offset:20586\n"
        "v_cvt_pk_bf16_f32 v44, v32, v33\n"
        "v_cvt_pk_bf16_f32 v45, v34, v35\n"
        "s_waitcnt lgkmcnt(3)\n"
        "v_smfmac_f32_16x16x64_bf16 v[48:51], v[20:23], v[88:95], v4\n"
        "s_add_u32 s37, s36, 196224\n"
        "ds_read_b128 v[32:35], v7 offset:46720\n"
        "ds_read_b128 v[36:39], v7 offset:46784\n"
        "v_mfma_f32_16x16x32_bf16 v[52:55], v[28:31], v[84:87], 0\n"
        "v_smfmac_f32_16x16x64_bf16 v[52:55], v[20:23], v[76:83], v4\n"
        "s_waitcnt lgkmcnt(4)\n"
        "v_mfma_f32_16x16x32_bf16 v[24:27], v[28:31], v[56:59], 0\n"
        "buffer_store_dwordx3 v[136:138], v13, s[8:11], s37 offen offset:1152 sc0 sc1 nt\n"
        "v_add_f32_dpp v165, v50, v51 quad_perm:[0,1,2,3] row_mask:0x1 bank_mask:0xf\n"
        "s_waitcnt lgkmcnt(2)\n"
        "v_add_u32 v167, v9, v167\n"
        "v_add_f32_dpp v164, v48, v49 quad_perm:[0,1,2,3] row_mask:0x1 bank_mask:0xf\n"
        "v_add_u32 v159, v9, v159\n"
        "v_add_f32_dpp v165, v54, v55 quad_perm:[0,1,2,3] row_mask:0x2 bank_mask:0xf\n"
        "v_add_f32_dpp v164, v52, v53 quad_perm:[0,1,2,3] row_mask:0x2 bank_mask:0xf\n"
        "s_waitcnt lgkmcnt(0)\n"
        "v_smfmac_f32_16x16x64_bf16 v[24:27], v[20:23], v[32:39], v4\n"
        "v_mfma_f32_16x16x32_bf16 v[28:31], v[212:215], v[100:103], 0\n"
        "s_add_u32 s37, s36, 294336\n"
        "v_mfma_f32_16x16x32_bf16 v[28:31], v[208:211], v[92:95], v[28:31]\n"
        "v_mfma_f32_16x16x32_bf16 v[28:31], v[204:207], v[88:91], v[28:31]\n"
        "v_mfma_f32_16x16x32_bf16 v[20:23], v[216:219], v[88:91], 0\n"
        "buffer_store_dwordx3 v[140:142], v12, s[8:11], s37 offen offset:576 sc0 sc1 nt\n"
        "v_mfma_f32_16x16x32_bf16 v[20:23], v[220:223], v[92:95], v[20:23]\n"
        "v_mfma_f32_16x16x32_bf16 v[20:23], v[224:227], v[100:103], v[20:23]\n"
        "s_add_u32 s37, s36, 392448\n"
        "v_add_f32_dpp v165, v26, v27 quad_perm:[0,1,2,3] row_mask:0x4 bank_mask:0xf\n"
        "v_mul_f32 v166, s52, v165\n"
        "v_fma_f32 v166, -v166, v165, v163\n"
        "v_cvt_pk_bf16_f32 v46, v28, v29\n"
        "v_cvt_pk_bf16_f32 v47, v30, v31\n"
        "v_add_f32_e64 v166, s53, |v166|\n"
        "v_rsq_f32 v163, v166\n"
        "v_mfma_f32_16x16x32_bf16 v[52:55], v[236:239], v[100:103], 0\n"
        "s_mov_b32 exec_hi, 0xffff\n"
        "ds_write_b32 v10, v163\n"
        "s_mov_b32 exec_hi, -1\n"
        "v_cvt_pk_bf16_f32 v48, v20, v21\n"
        "v_cvt_pk_bf16_f32 v49, v22, v23\n"
        "ds_read_b32 v20, v167 offset:40064\n"
        "v_add_f32_dpp v164, v24, v25 quad_perm:[0,1,2,3] row_mask:0x4 bank_mask:0xf\n"
        "v_mfma_f32_16x16x32_bf16 v[52:55], v[232:235], v[92:95], v[52:55]\n"
        "s_mov_b32 exec_hi, 0xffff\n"
        "v_add_u32 v10, -40064, v10\n"
        "s_mov_b32 exec_hi, -1\n"
        "ds_read_b32 v21, v167 offset:40896\n"
        "ds_read_b32 v22, v167 offset:41728\n"
        "ds_read_b32 v23, v167 offset:42560\n"
        "ds_read_b32 v24, v167 offset:43392\n"
        "v_mul_f32 v164, v164, v163\n"
        "v_mfma_f32_16x16x32_bf16 v[52:55], v[228:231], v[88:91], v[52:55]\n"
        "buffer_store_dwordx3 v[144:146], v12, s[8:11], s37 offen sc0 sc1 nt\n"
        "ds_read_b32 v25, v167 offset:44224\n"
        "ds_read_b32 v26, v167 offset:45056\n"
        "ds_read_b32 v27, v167 offset:45888\n"
        "ds_read_b32 v28, v167 offset:46720\n"
        "ds_read_b32 v29, v167 offset:47552\n"
        "ds_read_b32 v30, v167 offset:48384\n"
        "ds_read_b32 v31, v167 offset:49216\n"
        "v_cvt_pk_bf16_f32 v50, v52, v53\n"
        "v_cvt_pk_bf16_f32 v51, v54, v55\n"
        "v_readlane_b32 s58, v163, 56\n"
        "v_mfma_f32_16x16x32_bf16 v[52:55], v[88:91], v[40:43], 0\n"
        "s_mov_b64 exec, s[56:57]\n"
        "v_mov_b32 v164, 0xff800000\n"
        "s_mov_b64 exec, -1\n"
        "ds_read_b32 v165, v159 offset:40064\n"
        "ds_read_b32 v166, v159 offset:43392\n"
        "v_max_f32_dpp v155, v164, v164 row_ror:8 row_mask:0xf bank_mask:0xf\n"
        "v_mfma_f32_16x16x32_bf16 v[52:55], v[92:95], v[44:47], v[52:55]\n"
        "s_add_u32 s37, s36, 392448\n"
        "v_max_f32_dpp v155, v155, v155 row_ror:4 row_mask:0xf bank_mask:0xf\n"
        "ds_read_b32 v167, v159 offset:46720\n"
        "v_mfma_f32_16x16x32_bf16 v[52:55], v[100:103], v[48:51], v[52:55]\n"
        "v_max_f32_dpp v155, v155, v155 row_ror:2 row_mask:0xf bank_mask:0xf\n"
        "v_mfma_f32_16x16x32_bf16 v[96:99], v[84:87], v[48:51], 0\n"
        "v_mfma_f32_16x16x32_bf16 v[96:99], v[80:83], v[44:47], v[96:99]\n"
        "buffer_store_dwordx3 v[148:150], v15, s[8:11], s37 offen offset:768 sc0 sc1 nt\n"
        "v_max_f32_dpp v155, v155, v155 row_ror:1 row_mask:0xf bank_mask:0xf\n"
        "v_mfma_f32_16x16x32_bf16 v[96:99], v[76:79], v[40:43], v[96:99]\n"
        "ds_read_b128 v[60:63], v8 offset:128\n"
        "v_mfma_f32_16x16x32_bf16 v[68:71], v[32:35], v[40:43], 0\n"
        "s_add_u32 s37, s36, 490560\n"
        "ds_read2_b32 v[72:73], v6 offset0:0 offset1:2\n"
        "ds_read2_b32 v[74:75], v6 offset0:8 offset1:10\n"
        "s_waitcnt lgkmcnt(3)\n"
        "v_mul_f32 v52, v52, v20\n"
        "v_mul_f32 v53, v53, v21\n"
        "v_mul_f32 v54, v54, v22\n"
        "v_mul_f32 v55, v55, v23\n"
        "v_mul_f32 v96, v96, v24\n"
        "v_mfma_f32_16x16x32_bf16 v[68:71], v[36:39], v[44:47], v[68:71]\n"
        "v_max3_f32 v159, v52, v53, v54\n"
        "v_mul_f32 v97, v97, v25\n"
        "v_mul_f32 v98, v98, v26\n"
        "v_max3_f32 v159, v159, v55, v96\n"
        "v_mul_f32 v99, v99, v27\n"
        "v_max3_f32 v159, v159, v97, v98\n"
        "v_mfma_f32_16x16x32_bf16 v[68:71], v[56:59], v[48:51], v[68:71]\n"
        "s_waitcnt lgkmcnt(2)\n"
        "v_mfma_f32_16x16x32_bf16 v[108:111], v[60:63], v[48:51], 0\n"
        "buffer_store_dwordx3 v[152:154], v12, s[8:11], s37 offen offset:192 sc0 sc1 nt\n"
        "s_waitcnt lgkmcnt(0)\n"
        "v_smfmac_f32_16x16x64_bf16 v[108:111], v[72:75], v[40:47], v4\n"
        "v_mfma_f32_16x16x32_bf16 v[48:51], v[168:171], v[76:79], 0\n"
        "s_add_u32 s37, s36, 490560\n"
        "v_mfma_f32_16x16x32_bf16 v[48:51], v[172:175], v[80:83], v[48:51]\n"
        "v_mul_f32 v68, v68, v28\n"
        "v_mul_f32 v69, v69, v29\n"
        "v_mul_f32 v70, v70, v30\n"
        "v_max3_f32 v159, v159, v99, v68\n"
        "v_mul_f32 v71, v71, v31\n"
        "v_add_f32 v108, v108, v109\n"
        "v_mul_f32 v108, s58, v108\n"
        "v_max3_f32 v159, v159, v69, v70\n"
        "v_mfma_f32_16x16x32_bf16 v[48:51], v[176:179], v[84:87], v[48:51]\n"
        "s_mov_b64 exec, s[48:49]\n"
        "v_mov_b32 v108, 0xff800000\n"
        "s_mov_b64 exec, -1\n"
        "v_max3_f32 v159, v159, v71, v108\n"
        "v_mfma_f32_16x16x32_bf16 v[44:47], v[188:191], v[84:87], 0\n"
        "buffer_store_dwordx3 v[156:158], v14, s[8:11], s37 offen offset:960 sc0 sc1 nt\n"
        "v_permlane32_swap_b32 v159, v155\n"
        "v_max_f32 v159, v159, v155\n"
        "v_cvt_pk_bf16_f32 v40, v48, v49\n"
        "v_cvt_pk_bf16_f32 v41, v50, v51\n"
        "v_mov_b32 v158, v159\n"
        "v_mfma_f32_16x16x32_bf16 v[44:47], v[184:187], v[80:83], v[44:47]\n"
        "v_mfma_f32_16x16x32_bf16 v[44:47], v[180:183], v[76:79], v[44:47]\n"
        "s_add_u32 s37, s36, 588672\n"
        "v_permlane16_swap_b32 v159, v158\n"
        "v_max_f32 v159, v159, v158\n"
        "v_mov_b32 v155, v159\n"
        "v_mfma_f32_16x16x32_bf16 v[64:67], v[192:195], v[76:79], 0\n"
        "v_mfma_f32_16x16x32_bf16 v[64:67], v[196:199], v[80:83], v[64:67]\n"
        "v_permlane32_swap_b32 v159, v155\n"
        "v_mul_f32 v159, v159, v165\n"
        "v_cvt_pk_bf16_f32 v42, v44, v45\n"
        "v_cvt_pk_bf16_f32 v43, v46, v47\n"
        "v_fma_f32 v52, v52, v165, -v159\n"
        "v_fma_f32 v53, v53, v165, -v159\n"
        "v_fma_f32 v54, v54, v165, -v159\n"
        "v_mfma_f32_16x16x32_bf16 v[64:67], v[200:203], v[84:87], v[64:67]\n"
        "buffer_store_dwordx3 v[160:162], v12, s[8:11], s37 offen offset:384 sc0 sc1 nt\n"
        "v_exp_f32 v52, v52\n"
        "v_exp_f32 v53, v53\n"
        "v_exp_f32 v54, v54\n"
        "v_fma_f32 v55, v55, v165, -v159\n"
        "v_add_f32 v162, v52, v53\n"
        "v_exp_f32 v55, v55\n"
        "v_mfma_f32_16x16x32_bf16 v[48:51], v[212:215], v[84:87], 0\n"
        "v_cvt_pk_bf16_f32 v44, v64, v65\n"
        "v_cvt_pk_bf16_f32 v45, v66, v67\n"
        "v_fma_f32 v96, v96, v165, -v159\n"
        "v_add_f32 v162, v162, v54\n"
        "v_exp_f32 v96, v96\n"
        "v_fma_f32 v97, v97, v165, -v159\n"
        "v_add_f32 v162, v162, v55\n"
        "v_mfma_f32_16x16x32_bf16 v[48:51], v[208:211], v[80:83], v[48:51]\n"
        "v_exp_f32 v97, v97\n"
        "v_fma_f32 v98, v98, v165, -v159\n"
        "v_add_f32 v162, v162, v96\n"
        "v_exp_f32 v98, v98\n"
        "v_fma_f32 v99, v99, v165, -v159\n"
        "v_sub_f32 v164, v164, v155\n"
        "v_mfma_f32_16x16x32_bf16 v[48:51], v[204:207], v[76:79], v[48:51]\n"
        "v_add_f32 v162, v162, v97\n"
        "v_exp_f32 v99, v99\n"
        "v_fma_f32 v68, v68, v165, -v159\n"
        "v_mul_f32 v164, s58, v164\n"
        "v_add_f32 v162, v162, v98\n"
        "v_exp_f32 v68, v68\n"
        "v_mfma_f32_16x16x32_bf16 v[64:67], v[216:219], v[76:79], 0\n"
        "v_fma_f32 v69, v69, v165, -v159\n"
        "v_cvt_pk_bf16_f32 v46, v48, v49\n"
        "v_cvt_pk_bf16_f32 v47, v50, v51\n"
        "v_exp_f32 v164, v164\n"
        "v_add_f32 v162, v162, v99\n"
        "v_exp_f32 v69, v69\n"
        "v_mfma_f32_16x16x32_bf16 v[64:67], v[220:223], v[80:83], v[64:67]\n"
        "v_fma_f32 v70, v70, v165, -v159\n"
        "v_add_f32 v162, v162, v68\n"
        "v_exp_f32 v70, v70\n"
        "v_fma_f32 v71, v71, v165, -v159\n"
        "v_add_f32_dpp v161, v164, v164 row_ror:8 row_mask:0xf bank_mask:0xf\n"
        "v_add_f32 v162, v162, v69\n"
        "v_fma_f32 v108, v108, v165, -v159\n"
        "v_mfma_f32_16x16x32_bf16 v[64:67], v[224:227], v[84:87], v[64:67]\n"
        "v_exp_f32 v71, v71\n"
        "v_add_f32_dpp v161, v161, v161 row_ror:4 row_mask:0xf bank_mask:0xf\n"
        "v_add_f32 v162, v162, v70\n"
        "v_exp_f32 v108, v108\n"
        "v_add_f32_dpp v161, v161, v161 row_ror:2 row_mask:0xf bank_mask:0xf\n"
        "v_add_f32 v162, v162, v71\n"
        "v_mfma_f32_16x16x32_bf16 v[48:51], v[236:239], v[84:87], 0\n"
        "v_add_f32_dpp v161, v161, v161 row_ror:1 row_mask:0xf bank_mask:0xf\n"
        "v_cvt_pk_bf16_f32 v112, v64, v65\n"
        "v_cvt_pk_bf16_f32 v113, v66, v67\n"
        "v_add_f32 v162, v162, v108\n"
        "v_mul_f32 v52, v52, v20\n"
        "v_mul_f32 v53, v53, v21\n"
        "v_permlane32_swap_b32 v162, v161\n"
        "v_mfma_f32_16x16x32_bf16 v[48:51], v[232:235], v[80:83], v[48:51]\n"
        "v_add_f32 v162, v162, v161\n"
        "v_mov_b32 v165, v162\n"
        "v_mul_f32 v54, v54, v22\n"
        "v_mul_f32 v55, v55, v23\n"
        "v_permlane16_swap_b32 v162, v165\n"
        "v_add_f32 v162, v162, v165\n"
        "v_mul_f32 v96, v96, v24\n"
        "v_mfma_f32_16x16x32_bf16 v[48:51], v[228:231], v[76:79], v[48:51]\n"
        "v_rcp_f32 v162, v162\n"
        "v_mul_f32 v97, v97, v25\n"
        "v_mov_b32 v161, v162\n"
        "v_mul_f32 v98, v98, v26\n"
        "v_mul_f32 v99, v99, v27\n"
        "v_permlane32_swap_b32 v162, v161\n"
        "v_mfma_f32_16x16x32_bf16 v[64:67], v[88:91], v[40:43], 0\n"
        "v_mul_f32 v52, v52, v162\n"
        "v_cvt_pk_bf16_f32 v114, v48, v49\n"
        "v_cvt_pk_bf16_f32 v115, v50, v51\n"
        "v_mul_f32 v53, v53, v162\n"
        "v_mul_f32 v54, v54, v162\n"
        "v_mul_f32 v55, v55, v162\n"
        "v_mul_f32 v96, v96, v162\n"
        "v_mul_f32 v97, v97, v162\n"
        "v_mfma_f32_16x16x32_bf16 v[64:67], v[92:95], v[44:47], v[64:67]\n"
        "v_mul_f32 v98, v98, v162\n"
        "v_mul_f32 v99, v99, v162\n"
        "v_cvt_pk_bf16_f32 v48, v52, v53\n"
        "v_cvt_pk_bf16_f32 v49, v54, v55\n"
        "v_cvt_pk_bf16_f32 v50, v96, v97\n"
        "v_cvt_pk_bf16_f32 v51, v98, v99\n"
        "v_mul_f32 v68, v68, v28\n"
        "v_mul_f32 v69, v69, v29\n"
        "v_mfma_f32_16x16x32_bf16 v[64:67], v[100:103], v[112:115], v[64:67]\n"
        "v_mul_f32 v70, v70, v30\n"
        "v_mul_f32 v71, v71, v31\n"
        "v_mul_f32 v108, s58, v108\n"
        "v_mul_f32 v68, v68, v162\n"
        "v_mul_f32 v69, v69, v162\n"
        "v_mul_f32 v70, v70, v162\n"
        "v_mul_f32 v71, v71, v162\n"
        "v_fma_f32 v108, v108, v162, v19\n"
        "v_mfma_f32_16x16x32_bf16 v[104:107], v[84:87], v[112:115], 0\n"
        "v_mul_f32 v64, v64, v20\n"
        "v_mul_f32 v65, v65, v21\n"
        "v_mul_f32 v66, v66, v22\n"
        "v_mul_f32 v67, v67, v23\n"
        "v_max3_f32 v165, v64, v65, v66\n"
        "v_cvt_pk_bf16_f32 v52, v68, v69\n"
        "v_cvt_pk_bf16_f32 v54, v70, v71\n"
        "v_cvt_pk_bf16_f32 v53, v108, 0\n"
        "v_mfma_f32_16x16x32_bf16 v[104:107], v[80:83], v[44:47], v[104:107]\n"
        "v_mul_f32 v164, v164, v163\n"
        "v_mul_f32 v164, v164, v161\n"
        "v_cvt_pk_bf16_f32 v164, v164, 0\n"
        "ds_write_b16 v1, v164\n"
        "v_mfma_f32_16x16x32_bf16 v[104:107], v[76:79], v[40:43], v[104:107]\n"
        "v_mfma_f32_16x16x32_bf16 v[68:71], v[32:35], v[40:43], 0\n"
        "v_mfma_f32_16x16x32_bf16 v[68:71], v[36:39], v[44:47], v[68:71]\n"
        "v_mfma_f32_16x16x32_bf16 v[68:71], v[56:59], v[112:115], v[68:71]\n"
        "v_mfma_f32_16x16x32_bf16 v[96:99], v[60:63], v[112:115], 0\n"
        "v_smfmac_f32_16x16x64_bf16 v[96:99], v[72:75], v[40:47], v4\n"
        "v_mfma_f32_16x16x32_bf16 v[108:111], v[168:171], v[32:35], 0\n"
        "v_mfma_f32_16x16x32_bf16 v[108:111], v[172:175], v[36:39], v[108:111]\n"
        "v_mfma_f32_16x16x32_bf16 v[108:111], v[176:179], v[56:59], v[108:111]\n"
        "v_mul_f32 v104, v104, v24\n"
        "v_mul_f32 v105, v105, v25\n"
        "v_mul_f32 v106, v106, v26\n"
        "v_max3_f32 v165, v165, v67, v104\n"
        "v_mul_f32 v107, v107, v27\n"
        "v_add_f32 v96, v96, v97\n"
        "v_mul_f32 v68, v68, v28\n"
        "v_mul_f32 v96, s58, v96\n"
        "v_mfma_f32_16x16x32_bf16 v[44:47], v[188:191], v[56:59], 0\n"
        "s_mov_b64 exec, s[48:49]\n"
        "v_mov_b32 v96, 0xff800000\n"
        "s_mov_b64 exec, -1\n"
        "v_cvt_pk_bf16_f32 v40, v108, v109\n"
        "v_cvt_pk_bf16_f32 v41, v110, v111\n"
        "v_max3_f32 v165, v165, v105, v106\n"
        "v_mul_f32 v69, v69, v29\n"
        "v_mul_f32 v70, v70, v30\n"
        "v_max3_f32 v165, v165, v107, v68\n"
        "v_mfma_f32_16x16x32_bf16 v[44:47], v[184:187], v[36:39], v[44:47]\n"
        "v_mul_f32 v71, v71, v31\n"
        "v_max3_f32 v165, v165, v69, v70\n"
        "v_max3_f32 v165, v165, v71, v96\n"
        "v_mfma_f32_16x16x32_bf16 v[44:47], v[180:183], v[32:35], v[44:47]\n"
        "v_mfma_f32_16x16x32_bf16 v[112:115], v[192:195], v[32:35], 0\n"
        "v_mfma_f32_16x16x32_bf16 v[112:115], v[196:199], v[36:39], v[112:115]\n"
        "v_mfma_f32_16x16x32_bf16 v[112:115], v[200:203], v[56:59], v[112:115]\n"
        "v_mfma_f32_16x16x32_bf16 v[116:119], v[212:215], v[56:59], 0\n"
        "v_mfma_f32_16x16x32_bf16 v[116:119], v[208:211], v[36:39], v[116:119]\n"
        "v_mfma_f32_16x16x32_bf16 v[116:119], v[204:207], v[32:35], v[116:119]\n"
        "v_mfma_f32_16x16x32_bf16 v[108:111], v[216:219], v[32:35], 0\n"
        "v_mfma_f32_16x16x32_bf16 v[108:111], v[220:223], v[36:39], v[108:111]\n"
        "v_cvt_pk_bf16_f32 v42, v44, v45\n"
        "v_cvt_pk_bf16_f32 v43, v46, v47\n"
        "v_mfma_f32_16x16x32_bf16 v[108:111], v[224:227], v[56:59], v[108:111]\n"
        "v_cvt_pk_bf16_f32 v44, v112, v113\n"
        "v_cvt_pk_bf16_f32 v45, v114, v115\n"
        "v_mfma_f32_16x16x32_bf16 v[112:115], v[236:239], v[56:59], 0\n"
        "v_cvt_pk_bf16_f32 v46, v116, v117\n"
        "v_cvt_pk_bf16_f32 v47, v118, v119\n"
        "v_mfma_f32_16x16x32_bf16 v[112:115], v[232:235], v[36:39], v[112:115]\n"
        "v_mfma_f32_16x16x32_bf16 v[112:115], v[228:231], v[32:35], v[112:115]\n"
        "v_mfma_f32_16x16x32_bf16 v[124:127], v[88:91], v[40:43], 0\n"
        "v_cvt_pk_bf16_f32 v116, v108, v109\n"
        "v_cvt_pk_bf16_f32 v117, v110, v111\n"
        "v_mfma_f32_16x16x32_bf16 v[124:127], v[92:95], v[44:47], v[124:127]\n"
        "s_nop 3\n"
        "v_cvt_pk_bf16_f32 v118, v112, v113\n"
        "v_cvt_pk_bf16_f32 v119, v114, v115\n"
        "s_nop 1\n"
        "v_mfma_f32_16x16x32_bf16 v[124:127], v[100:103], v[116:119], v[124:127]\n"
        "v_mfma_f32_16x16x32_bf16 v[132:135], v[84:87], v[116:119], 0\n"
        "v_mfma_f32_16x16x32_bf16 v[132:135], v[80:83], v[44:47], v[132:135]\n"
        "v_mfma_f32_16x16x32_bf16 v[132:135], v[76:79], v[40:43], v[132:135]\n"
        "v_mfma_f32_16x16x32_bf16 v[112:115], v[32:35], v[40:43], 0\n"
        "v_mfma_f32_16x16x32_bf16 v[112:115], v[36:39], v[44:47], v[112:115]\n"
        "v_mfma_f32_16x16x32_bf16 v[112:115], v[56:59], v[116:119], v[112:115]\n"
        "v_mfma_f32_16x16x32_bf16 v[36:39], v[60:63], v[116:119], 0\n"
        "v_smfmac_f32_16x16x64_bf16 v[36:39], v[72:75], v[40:47], v4\n"
        "v_mul_f32 v124, v124, v20\n"
        "v_mul_f32 v125, v125, v21\n"
        "v_mul_f32 v126, v126, v22\n"
        "v_mul_f32 v127, v127, v23\n"
        "v_mul_f32 v132, v132, v24\n"
        "v_max3_f32 v163, v124, v125, v126\n"
        "v_mul_f32 v133, v133, v25\n"
        "v_mfma_f32_16x16x32_bf16 v[44:47], v[88:91], v[240:243], 0\n"
        "v_add_f32 v36, v36, v37\n"
        "v_mul_f32 v134, v134, v26\n"
        "v_max3_f32 v163, v163, v127, v132\n"
        "v_mul_f32 v135, v135, v27\n"
        "v_mul_f32 v112, v112, v28\n"
        "v_mul_f32 v36, s58, v36\n"
        "v_max3_f32 v163, v163, v133, v134\n"
        "v_mul_f32 v113, v113, v29\n"
        "v_mfma_f32_16x16x32_bf16 v[44:47], v[92:95], v[244:247], v[44:47]\n"
        "s_mov_b64 exec, s[48:49]\n"
        "v_mov_b32 v36, 0xff800000\n"
        "s_mov_b64 exec, -1\n"
        "v_mul_f32 v114, v114, v30\n"
        "v_max3_f32 v163, v163, v135, v112\n"
        "v_mul_f32 v115, v115, v31\n"
        "v_max3_f32 v163, v163, v113, v114\n"
        "v_max3_f32 v163, v163, v115, v36\n"
        "v_mfma_f32_16x16x32_bf16 v[44:47], v[100:103], v[248:251], v[44:47]\n"
        "ds_read_b128 v[72:75], v17 offset:40064\n"
        "v_permlane32_swap_b32 v165, v163\n"
        "v_max_f32 v165, v165, v163\n"
        "v_mov_b32 v164, v165\n"
        "v_mfma_f32_16x16x32_bf16 v[40:43], v[84:87], v[248:251], 0\n"
        "ds_read_b128 v[108:111], v17 offset:40128\n"
        "v_permlane16_swap_b32 v165, v164\n"
        "v_max_f32 v165, v165, v164\n"
        "v_cvt_pk_bf16_f32 v32, v44, v45\n"
        "v_cvt_pk_bf16_f32 v33, v46, v47\n"
        "v_mov_b32 v163, v165\n"
        "v_mfma_f32_16x16x32_bf16 v[40:43], v[80:83], v[244:247], v[40:43]\n"
        "ds_read_b128 v[116:119], v17 offset:40192\n"
        "v_permlane32_swap_b32 v165, v163\n"
        "v_mul_f32 v165, v165, v166\n"
        "v_mul_f32 v163, v163, v167\n"
        "v_fma_f32 v64, v64, v166, -v165\n"
        "v_fma_f32 v65, v65, v166, -v165\n"
        "v_fma_f32 v124, v124, v167, -v163\n"
        "v_mfma_f32_16x16x32_bf16 v[40:43], v[76:79], v[240:243], v[40:43]\n"
        "s_waitcnt lgkmcnt(2)\n"
        "ds_read_b128 v[120:123], v0 offset:5136\n"
        "v_fma_f32 v125, v125, v167, -v163\n"
        "v_exp_f32 v64, v64\n"
        "v_exp_f32 v65, v65\n"
        "v_fma_f32 v66, v66, v166, -v165\n"
        "v_fma_f32 v126, v126, v167, -v163\n"
        "v_mfma_f32_16x16x32_bf16 v[44:47], v[72:75], v[240:243], 0\n"
        "s_waitcnt lgkmcnt(2)\n"
        "ds_read_b128 v[128:131], v0 offset:4112\n"
        "v_cvt_pk_bf16_f32 v34, v40, v41\n"
        "v_cvt_pk_bf16_f32 v35, v42, v43\n"
        "v_exp_f32 v124, v124\n"
        "v_exp_f32 v125, v125\n"
        "v_fma_f32 v67, v67, v166, -v165\n"
        "v_mfma_f32_16x16x32_bf16 v[44:47], v[108:111], v[244:247], v[44:47]\n"
        "s_waitcnt lgkmcnt(2)\n"
        "v_exp_f32 v66, v66\n"
        "v_exp_f32 v126, v126\n"
        "v_fma_f32 v127, v127, v167, -v163\n"
        "v_add_f32 v99, v64, v65\n"
        "v_exp_f32 v67, v67\n"
        "v_mfma_f32_16x16x32_bf16 v[44:47], v[116:119], v[248:251], v[44:47]\n"
        "s_waitcnt lgkmcnt(1)\n"
        "v_fma_f32 v104, v104, v166, -v165\n"
        "v_add_f32 v164, v124, v125\n"
        "v_exp_f32 v127, v127\n"
        "v_fma_f32 v132, v132, v167, -v163\n"
        "v_add_f32 v99, v99, v66\n"
        "v_exp_f32 v104, v104\n"
        "v_mfma_f32_16x16x32_bf16 v[136:139], v[116:119], v[120:123], 0\n"
        "s_waitcnt lgkmcnt(0)\n"
        "v_cvt_pk_bf16_f32 v40, v44, v45\n"
        "v_cvt_pk_bf16_f32 v44, v46, v47\n"
        "v_fma_f32 v105, v105, v166, -v165\n"
        "v_add_f32 v164, v164, v126\n"
        "v_exp_f32 v132, v132\n"
        "v_fma_f32 v133, v133, v167, -v163\n"
        "v_add_f32 v99, v99, v67\n"
        "v_mfma_f32_16x16x32_bf16 v[136:139], v[108:111], v[128:131], v[136:139]\n"
        "v_exp_f32 v105, v105\n"
        "v_fma_f32 v106, v106, v166, -v165\n"
        "v_add_f32 v164, v164, v127\n"
        "v_exp_f32 v133, v133\n"
        "v_fma_f32 v134, v134, v167, -v163\n"
        "v_add_f32 v99, v99, v104\n"
        "v_mfma_f32_16x16x32_bf16 v[136:139], v[72:75], v[252:255], v[136:139]\n"
        "v_exp_f32 v106, v106\n"
        "v_fma_f32 v107, v107, v166, -v165\n"
        "v_add_f32 v164, v164, v132\n"
        "v_exp_f32 v134, v134\n"
        "v_fma_f32 v135, v135, v167, -v163\n"
        "v_add_f32 v99, v99, v105\n"
        "v_mfma_f32_16x16x32_bf16 v[140:143], v[76:79], v[252:255], 0\n"
        "v_exp_f32 v107, v107\n"
        "v_cvt_pk_bf16_f32 v56, v136, v137\n"
        "v_cvt_pk_bf16_f32 v60, v138, v139\n"
        "v_fma_f32 v68, v68, v166, -v165\n"
        "v_add_f32 v164, v164, v133\n"
        "v_exp_f32 v135, v135\n"
        "v_mfma_f32_16x16x32_bf16 v[140:143], v[80:83], v[128:131], v[140:143]\n"
        "v_fma_f32 v112, v112, v167, -v163\n"
        "v_add_f32 v99, v99, v106\n"
        "v_exp_f32 v68, v68\n"
        "v_fma_f32 v69, v69, v166, -v165\n"
        "v_add_f32 v164, v164, v134\n"
        "v_exp_f32 v112, v112\n"
        "v_mfma_f32_16x16x32_bf16 v[140:143], v[84:87], v[120:123], v[140:143]\n"
        "ds_read_b128 v[148:151], v0 offset:6176\n"
        "v_fma_f32 v113, v113, v167, -v163\n"
        "v_add_f32 v99, v99, v107\n"
        "v_exp_f32 v69, v69\n"
        "v_fma_f32 v70, v70, v166, -v165\n"
        "v_add_f32 v164, v164, v135\n"
        "v_fma_f32 v114, v114, v167, -v163\n"
        "v_mfma_f32_16x16x32_bf16 v[152:155], v[100:103], v[120:123], 0\n"
        "ds_read_b128 v[144:147], v0 offset:7200\n"
        "v_cvt_pk_bf16_f32 v122, v140, v141\n"
        "v_cvt_pk_bf16_f32 v123, v142, v143\n"
        "v_exp_f32 v113, v113\n"
        "v_add_f32 v99, v99, v68\n"
        "v_exp_f32 v70, v70\n"
        "v_mfma_f32_16x16x32_bf16 v[152:155], v[92:95], v[128:131], v[152:155]\n"
        "ds_read_b128 v[140:143], v0 offset:8224\n"
        "v_fma_f32 v71, v71, v166, -v165\n"
        "v_add_f32 v164, v164, v112\n"
        "v_exp_f32 v114, v114\n"
        "v_fma_f32 v115, v115, v167, -v163\n"
        "v_add_f32 v99, v99, v69\n"
        "v_fma_f32 v96, v96, v166, -v165\n"
        "v_mfma_f32_16x16x32_bf16 v[152:155], v[88:91], v[252:255], v[152:155]\n"
        "s_waitcnt lgkmcnt(2)\n"
        "v_exp_f32 v71, v71\n"
        "v_add_f32 v164, v164, v113\n"
        "v_exp_f32 v115, v115\n"
        "v_fma_f32 v36, v36, v167, -v163\n"
        "v_add_f32 v99, v99, v70\n"
        "v_add_f32 v164, v164, v114\n"
        "v_mfma_f32_16x16x32_bf16 v[136:139], v[88:91], v[148:151], 0\n"
        "s_waitcnt lgkmcnt(1)\n"
        "v_cvt_pk_bf16_f32 v120, v152, v153\n"
        "v_cvt_pk_bf16_f32 v121, v154, v155\n"
        "v_exp_f32 v96, v96\n"
        "v_exp_f32 v36, v36\n"
        "v_add_f32 v99, v99, v71\n"
        "v_add_f32 v164, v164, v115\n"
        "v_mfma_f32_16x16x32_bf16 v[136:139], v[92:95], v[144:147], v[136:139]\n"
        "s_waitcnt lgkmcnt(0)\n"
        "v_add_f32 v99, v99, v96\n"
        "v_add_f32 v164, v164, v36\n"
        "v_mul_f32 v64, v64, v20\n"
        "v_mul_f32 v65, v65, v21\n"
        "v_permlane32_swap_b32 v99, v164\n"
        "v_add_f32 v99, v99, v164\n"
        "v_mov_b32 v167, v99\n"
        "v_mfma_f32_16x16x32_bf16 v[136:139], v[100:103], v[140:143], v[136:139]\n"
        "v_mul_f32 v66, v66, v22\n"
        "v_permlane16_swap_b32 v99, v167\n"
        "v_add_f32 v99, v99, v167\n"
        "v_rcp_f32 v99, v99\n"
        "v_mul_f32 v67, v67, v23\n"
        "v_mov_b32 v164, v99\n"
        "v_mfma_f32_16x16x32_bf16 v[160:163], v[84:87], v[140:143], 0\n"
        "v_mul_f32 v104, v104, v24\n"
        "v_cvt_pk_bf16_f32 v128, v136, v137\n"
        "v_cvt_pk_bf16_f32 v129, v138, v139\n"
        "v_permlane32_swap_b32 v99, v164\n"
        "v_mul_f32 v105, v105, v25\n"
        "v_mul_f32 v106, v106, v26\n"
        "v_mul_f32 v107, v107, v27\n"
        "v_mfma_f32_16x16x32_bf16 v[160:163], v[80:83], v[144:147], v[160:163]\n"
        "v_mul_f32 v64, v64, v99\n"
        "v_mul_f32 v65, v65, v99\n"
        "v_mul_f32 v66, v66, v99\n"
        "v_mul_f32 v67, v67, v99\n"
        "v_mul_f32 v104, v104, v99\n"
        "v_mul_f32 v105, v105, v99\n"
        "v_mul_f32 v106, v106, v99\n"
        "v_mul_f32 v107, v107, v99\n"
        "v_mfma_f32_16x16x32_bf16 v[160:163], v[76:79], v[148:151], v[160:163]\n"
        "ds_read_b128 v[152:155], v0 offset:11312\n"
        "v_cvt_pk_bf16_f32 v136, v64, v65\n"
        "v_cvt_pk_bf16_f32 v137, v66, v67\n"
        "v_cvt_pk_bf16_f32 v138, v104, v105\n"
        "v_cvt_pk_bf16_f32 v139, v106, v107\n"
        "v_mul_f32 v68, v68, v28\n"
        "v_mul_f32 v69, v69, v29\n"
        "v_mul_f32 v70, v70, v30\n"
        "v_mfma_f32_16x16x32_bf16 v[64:67], v[72:75], v[148:151], 0\n"
        "ds_read_b128 v[156:159], v0 offset:10288\n"
        "v_cvt_pk_bf16_f32 v130, v160, v161\n"
        "v_cvt_pk_bf16_f32 v131, v162, v163\n"
        "v_mul_f32 v71, v71, v31\n"
        "v_mul_f32 v96, s58, v96\n"
        "v_mul_f32 v68, v68, v99\n"
        "v_mul_f32 v69, v69, v99\n"
        "v_mul_f32 v70, v70, v99\n"
        "v_mfma_f32_16x16x32_bf16 v[64:67], v[108:111], v[144:147], v[64:67]\n"
        "ds_read_b128 v[148:151], v0 offset:9264\n"
        "v_mul_f32 v71, v71, v99\n"
        "v_fma_f32 v96, v96, v99, v19\n"
        "v_cvt_pk_bf16_f32 v104, v68, v69\n"
        "v_cvt_pk_bf16_f32 v106, v70, v71\n"
        "v_cvt_pk_bf16_f32 v105, v96, 0\n"
        "v_mul_f32 v124, v124, v20\n"
        "v_mul_f32 v125, v125, v21\n"
        "v_mfma_f32_16x16x32_bf16 v[64:67], v[116:119], v[140:143], v[64:67]\n"
        "s_waitcnt lgkmcnt(2)\n"
        "v_mul_f32 v126, v126, v22\n"
        "v_mul_f32 v127, v127, v23\n"
        "v_mul_f32 v132, v132, v24\n"
        "v_mul_f32 v133, v133, v25\n"
        "v_mul_f32 v134, v134, v26\n"
        "v_mul_f32 v135, v135, v27\n"
        "v_mul_f32 v124, v124, v164\n"
        "v_cvt_pk_bf16_f32 v20, v64, v65\n"
        "v_mfma_f32_16x16x32_bf16 v[68:71], v[116:119], v[152:155], 0\n"
        "s_waitcnt lgkmcnt(1)\n"
        "v_cvt_pk_bf16_f32 v24, v66, v67\n"
        "v_mul_f32 v125, v125, v164\n"
        "v_mul_f32 v126, v126, v164\n"
        "v_mul_f32 v127, v127, v164\n"
        "v_mul_f32 v132, v132, v164\n"
        "v_mul_f32 v133, v133, v164\n"
        "v_mul_f32 v134, v134, v164\n"
        "v_mul_f32 v135, v135, v164\n"
        "v_mfma_f32_16x16x32_bf16 v[68:71], v[108:111], v[156:159], v[68:71]\n"
        "s_waitcnt lgkmcnt(0)\n"
        "v_cvt_pk_bf16_f32 v64, v124, v125\n"
        "v_cvt_pk_bf16_f32 v65, v126, v127\n"
        "v_cvt_pk_bf16_f32 v66, v132, v133\n"
        "v_cvt_pk_bf16_f32 v67, v134, v135\n"
        "v_mul_f32 v112, v112, v28\n"
        "v_mul_f32 v113, v113, v29\n"
        "v_mul_f32 v114, v114, v30\n"
        "v_mul_f32 v115, v115, v31\n"
        "v_mfma_f32_16x16x32_bf16 v[68:71], v[72:75], v[148:151], v[68:71]\n"
        "v_mul_f32 v36, s58, v36\n"
        "v_mul_f32 v112, v112, v164\n"
        "v_mul_f32 v113, v113, v164\n"
        "v_mul_f32 v114, v114, v164\n"
        "v_mul_f32 v115, v115, v164\n"
        "v_fma_f32 v36, v36, v164, v19\n"
        "v_cvt_pk_bf16_f32 v144, v112, v113\n"
        "v_cvt_pk_bf16_f32 v146, v114, v115\n"
        "v_mfma_f32_16x16x32_bf16 v[28:31], v[76:79], v[148:151], 0\n"
        "v_cvt_pk_bf16_f32 v41, v68, v69\n"
        "v_cvt_pk_bf16_f32 v45, v70, v71\n"
        "v_cvt_pk_bf16_f32 v145, v36, 0\n"
        "v_mfma_f32_16x16x32_bf16 v[28:31], v[80:83], v[156:159], v[28:31]\n"
        "v_mfma_f32_16x16x32_bf16 v[28:31], v[84:87], v[152:155], v[28:31]\n"
        "ds_read_b128 v[96:99], v0 offset:12352\n"
        "v_mfma_f32_16x16x32_bf16 v[68:71], v[100:103], v[152:155], 0\n"
        "ds_read_b128 v[132:135], v0 offset:13376\n"
        "v_mfma_f32_16x16x32_bf16 v[68:71], v[92:95], v[156:159], v[68:71]\n"
        "ds_read_b128 v[112:115], v0 offset:14400\n"
        "v_mfma_f32_16x16x32_bf16 v[68:71], v[88:91], v[148:151], v[68:71]\n"
        "s_waitcnt lgkmcnt(2)\n"
        "v_mfma_f32_16x16x32_bf16 v[148:151], v[88:91], v[96:99], 0\n"
        "v_cvt_pk_bf16_f32 v38, v28, v29\n"
        "v_cvt_pk_bf16_f32 v39, v30, v31\n"
        "s_waitcnt lgkmcnt(1)\n"
        "v_mfma_f32_16x16x32_bf16 v[148:151], v[92:95], v[132:135], v[148:151]\n"
        "s_waitcnt lgkmcnt(0)\n"
        "v_mfma_f32_16x16x32_bf16 v[148:151], v[100:103], v[112:115], v[148:151]\n"
        "v_cvt_pk_bf16_f32 v36, v68, v69\n"
        "v_cvt_pk_bf16_f32 v37, v70, v71\n"
        "v_mfma_f32_16x16x32_bf16 v[152:155], v[84:87], v[112:115], 0\n"
        "v_mfma_f32_16x16x32_bf16 v[152:155], v[80:83], v[132:135], v[152:155]\n"
        "v_mfma_f32_16x16x32_bf16 v[152:155], v[76:79], v[96:99], v[152:155]\n"
        "ds_read_b128 v[68:71], v0 offset:17488\n"
        "v_mfma_f32_16x16x32_bf16 v[140:143], v[72:75], v[96:99], 0\n"
        "ds_read_b128 v[28:31], v0 offset:16464\n"
        "v_cvt_pk_bf16_f32 v124, v148, v149\n"
        "v_cvt_pk_bf16_f32 v125, v150, v151\n"
        "v_mfma_f32_16x16x32_bf16 v[140:143], v[108:111], v[132:135], v[140:143]\n"
        "ds_read_b128 v[96:99], v0 offset:15440\n"
        "v_mfma_f32_16x16x32_bf16 v[140:143], v[116:119], v[112:115], v[140:143]\n"
        "v_cvt_pk_bf16_f32 v126, v152, v153\n"
        "v_cvt_pk_bf16_f32 v127, v154, v155\n"
        "s_waitcnt lgkmcnt(2)\n"
        "v_mfma_f32_16x16x32_bf16 v[112:115], v[116:119], v[68:71], 0\n"
        "s_waitcnt lgkmcnt(1)\n"
        "v_mfma_f32_16x16x32_bf16 v[112:115], v[108:111], v[28:31], v[112:115]\n"
        "s_waitcnt lgkmcnt(0)\n"
        "v_mfma_f32_16x16x32_bf16 v[112:115], v[72:75], v[96:99], v[112:115]\n"
        "ds_read2_b32 v[46:47], v11 offset0:0 offset1:3\n"
        "v_cvt_pk_bf16_f32 v57, v140, v141\n"
        "v_cvt_pk_bf16_f32 v61, v142, v143\n"
        "v_mov_b32_dpp v55, v19 row_shr:4 row_mask:0xf bank_mask:0xf bound_ctrl:0\n"
        "v_mfma_f32_16x16x32_bf16 v[72:75], v[76:79], v[96:99], 0\n"
        "ds_read_b64_tr_b16 v[42:43], v9 offset:40064\n"
        "v_mfma_f32_16x16x32_bf16 v[72:75], v[80:83], v[28:31], v[72:75]\n"
        "ds_read2_b32 v[62:63], v11 offset0:1 offset1:4\n"
        "v_cvt_pk_bf16_f32 v21, v112, v113\n"
        "v_cvt_pk_bf16_f32 v25, v114, v115\n"
        "v_mfma_f32_16x16x32_bf16 v[72:75], v[84:87], v[68:71], v[72:75]\n"
        "ds_read_b64_tr_b16 v[58:59], v9 offset:40080\n"
        "v_mfma_f32_16x16x32_bf16 v[76:79], v[100:103], v[68:71], 0\n"
        "ds_read2_b32 v[26:27], v11 offset0:2 offset1:5\n"
        "v_mfma_f32_16x16x32_bf16 v[76:79], v[92:95], v[28:31], v[76:79]\n"
        "s_add_u32 s31, s31, 1\n"
        "ds_read_b64_tr_b16 v[22:23], v9 offset:40096\n"
        "v_mfma_f32_16x16x32_bf16 v[76:79], v[88:91], v[96:99], v[76:79]\n"
        "s_mul_i32 s38, s31, 1344\n"
        "v_cvt_pk_bf16_f32 v134, v72, v73\n"
        "v_cvt_pk_bf16_f32 v135, v74, v75\n"
        "v_mfma_f32_16x16x32_bf16 v[28:31], v[48:51], v[32:35], 0\n"
        "s_waitcnt lgkmcnt(4)\n"
        "s_cmp_ge_u32 s31, s76\n"
        "v_smfmac_f32_16x16x64_bf16 v[28:31], v[52:55], v[40:47], v18\n"
        "s_cbranch_scc1 L49B_%=\n"
        "L50B_%=:\n"
        "v_cvt_pk_bf16_f32 v132, v76, v77\n"
        "v_cvt_pk_bf16_f32 v133, v78, v79\n"
        "v_mfma_f32_16x16x32_bf16 v[68:71], v[48:51], v[120:123], 0\n"
        "s_waitcnt lgkmcnt(2)\n"
        "s_cmp_ge_u32 s31, s71\n"
        "v_smfmac_f32_16x16x64_bf16 v[68:71], v[52:55], v[56:63], v18\n"
        "s_cselect_b32 s39, s70, s72\n"
        "ds_read_b64_tr_b16 v[42:43], v9 offset:40112\n"
        "v_mfma_f32_16x16x32_bf16 v[80:83], v[48:51], v[128:131], 0\n"
        "s_waitcnt lgkmcnt(1)\n"
        "s_cmp_ge_u32 s31, 20\n"
        "v_smfmac_f32_16x16x64_bf16 v[80:83], v[52:55], v[20:27], v18\n"
        "s_cselect_b32 s39, s22, s39\n"
        "ds_read_b64_tr_b16 v[58:59], v9 offset:40128\n"
        "v_cvt_pk_bf16_f32 v164, v28, v68\n"
        "v_cvt_pk_bf16_f32 v152, v29, v69\n"
        "v_cvt_pk_bf16_f32 v156, v30, v70\n"
        "v_cvt_pk_bf16_f32 v160, v31, v71\n"
        "v_mfma_f32_16x16x32_bf16 v[84:87], v[48:51], v[36:39], 0\n"
        "s_cmp_ge_u32 s31, 21\n"
        "s_waitcnt lgkmcnt(1)\n"
        "v_smfmac_f32_16x16x64_bf16 v[84:87], v[52:55], v[40:47], v18 abid:1\n"
        "s_cselect_b32 s39, s23, s39\n"
        "ds_read_b64_tr_b16 v[22:23], v9 offset:40144\n"
        "v_mfma_f32_16x16x32_bf16 v[76:79], v[48:51], v[124:127], 0\n"
        "s_add_u32 s39, s39, s38\n"
        "ds_read_b128 v[68:71], v2\n"
        "v_mov_b64 v[28:29], 0\n"
        "v_mov_b64 v[30:31], 0\n"
        "s_waitcnt lgkmcnt(2)\n"
        "v_smfmac_f32_16x16x64_bf16 v[76:79], v[52:55], v[56:63], v18 abid:1\n"
        "s_mul_i32 s68, s31, 3\n"
        "ds_read_b128 v[72:75], v3\n"
        "v_cvt_pk_bf16_f32 v165, v80, v84\n"
        "v_cvt_pk_bf16_f32 v153, v81, v85\n"
        "v_cvt_pk_bf16_f32 v157, v82, v86\n"
        "v_cvt_pk_bf16_f32 v161, v83, v87\n"
        "v_mfma_f32_16x16x32_bf16 v[80:83], v[48:51], v[132:135], 0\n"
        "s_lshr_b32 s38, s31, 3\n"
        "v_mov_b64 v[48:49], 0\n"
        "v_mov_b64 v[50:51], 0\n"
        "s_waitcnt lgkmcnt(2)\n"
        "v_smfmac_f32_16x16x64_bf16 v[80:83], v[52:55], v[20:27], v18 abid:1\n"
        "s_add_u32 s38, s38, 11\n"
        "s_waitcnt lgkmcnt(1)\n"
        "v_mov_b32_dpp v107, v19 row_shr:4 row_mask:0xf bank_mask:0xf bound_ctrl:0\n"
        "v_smfmac_f32_16x16x64_bf16 v[28:31], v[68:71], v[32:39], v4\n"
        "s_bitcmp1_b32 s31, 0\n"
        "v_mov_b64 v[52:53], 0\n"
        "v_mov_b64 v[54:55], 0\n"
        "s_waitcnt lgkmcnt(0)\n"
        "v_smfmac_f32_16x16x64_bf16 v[28:31], v[72:75], v[40:47], v4 abid:1\n"
        "s_cselect_b32 s38, 3, s38\n"
        "s_add_u32 s37, s35, 0\n"
        "v_cvt_pk_bf16_f32 v166, v76, v80\n"
        "v_cvt_pk_bf16_f32 v154, v77, v81\n"
        "v_cvt_pk_bf16_f32 v158, v78, v82\n"
        "v_cvt_pk_bf16_f32 v162, v79, v83\n"
        "v_smfmac_f32_16x16x64_bf16 v[48:51], v[68:71], v[120:127], v4\n"
        "s_add_u32 s68, s68, s38\n"
        "v_add_f32 v28, v28, v29\n"
        "v_add_f32 v30, v30, v31\n"
        "v_smfmac_f32_16x16x64_bf16 v[48:51], v[72:75], v[56:63], v4 abid:1\n"
        "s_cmp_lt_u32 s31, s14\n"
        "ds_read_b64_tr_b16 v[42:43], v9 offset:43392\n"
        "v_smfmac_f32_16x16x64_bf16 v[52:55], v[68:71], v[128:135], v4\n"
        "buffer_store_dwordx3 v[164:166], v12, s[8:11], s37 offen sc0 sc1\n"
        "s_cselect_b32 s68, s68, 0\n"
        "v_smfmac_f32_16x16x64_bf16 v[52:55], v[72:75], v[20:27], v4 abid:1\n"
        "s_and_b32 s38, s31, 7\n"
        "ds_read_b64_tr_b16 v[58:59], v9 offset:43408\n"
        "v_add_f32 v48, v48, v49\n"
        "v_add_f32 v50, v50, v51\n"
        "v_cvt_pk_bf16_f32 v100, v28, v48\n"
        "v_mfma_f32_16x16x32_bf16 v[140:143], v[136:139], v[32:35], 0\n"
        "s_cselect_b32 s30, 1, -7\n"
        "s_waitcnt lgkmcnt(1)\n"
        "v_smfmac_f32_16x16x64_bf16 v[140:143], v[104:107], v[40:47], v18\n"
        "s_add_u32 s37, s35, 0\n"
        "s_mul_i32 s29, s30, 192\n"
        "ds_read_b64_tr_b16 v[22:23], v9 offset:43424\n"
        "v_add_f32 v52, v52, v53\n"
        "v_add_f32 v54, v54, v55\n"
        "v_cvt_pk_bf16_f32 v101, v52, v30\n"
        "v_cvt_pk_bf16_f32 v102, v50, v54\n"
        "v_mfma_f32_16x16x32_bf16 v[164:167], v[136:139], v[120:123], 0\n"
        "s_mul_i32 s30, s30, 392\n"
        "s_waitcnt lgkmcnt(1)\n"
        "v_smfmac_f32_16x16x64_bf16 v[164:167], v[104:107], v[56:63], v18\n"
        "s_cmp_eq_u32 s73, s18\n"
        "ds_read_b64_tr_b16 v[42:43], v9 offset:43440\n"
        "v_mfma_f32_16x16x32_bf16 v[108:111], v[136:139], v[128:131], 0\n"
        "buffer_store_dwordx3 v[152:154], v15, s[8:11], s37 offen offset:768 sc0 sc1 nt\n"
        "s_cselect_b32 s34, -7488, s75\n"
        "s_waitcnt lgkmcnt(1)\n"
        "v_smfmac_f32_16x16x64_bf16 v[108:111], v[104:107], v[20:27], v18\n"
        "s_add_u32 s73, s73, s34\n"
        "ds_read_b64_tr_b16 v[58:59], v9 offset:43456\n"
        "v_mov_b32_dpp v147, v19 row_shr:4 row_mask:0xf bank_mask:0xf bound_ctrl:0\n"
        "v_writelane_b32 v10, s73, 56\n"
        "v_add_u32 v11, s30, v11\n"
        "v_mfma_f32_16x16x32_bf16 v[68:71], v[136:139], v[36:39], 0\n"
        "s_mov_b64 exec, s[62:63]\n"
        "v_add_u32 v16, s29, v16\n"
        "v_add_u32 v8, s34, v8\n"
        "s_mov_b64 exec, -1\n"
        "s_mov_b64 exec, s[50:51]\n"
        "v_add_u32 v5, s29, v5\n"
        "v_add_u32 v6, s34, v6\n"
        "s_mov_b64 exec, -1\n"
        "s_waitcnt lgkmcnt(1)\n"
        "v_smfmac_f32_16x16x64_bf16 v[68:71], v[104:107], v[40:47], v18 abid:1\n"
        "s_add_u32 s37, s35, 98112\n"
        "ds_read_b64_tr_b16 v[22:23], v9 offset:43472\n"
        "v_mfma_f32_16x16x32_bf16 v[148:151], v[136:139], v[124:127], 0\n"
        "s_waitcnt lgkmcnt(1)\n"
        "v_smfmac_f32_16x16x64_bf16 v[148:151], v[104:107], v[56:63], v18 abid:1\n"
        "ds_read_b64_tr_b16 v[42:43], v9 offset:46720\n"
        "v_mfma_f32_16x16x32_bf16 v[72:75], v[136:139], v[132:135], 0\n"
        "buffer_store_dwordx3 v[156:158], v12, s[8:11], s37 offen offset:192 sc0 sc1 nt\n"
        "ds_read_b32 v163, v0 offset:21600\n"
        "s_waitcnt lgkmcnt(2)\n"
        "v_smfmac_f32_16x16x64_bf16 v[72:75], v[104:107], v[20:27], v18 abid:1\n"
        "ds_read_b64_tr_b16 v[58:59], v9 offset:46736\n"
        "v_mfma_f32_16x16x32_bf16 v[116:119], v[64:67], v[32:35], 0\n"
        "s_waitcnt lgkmcnt(2)\n"
        "v_smfmac_f32_16x16x64_bf16 v[116:119], v[144:147], v[40:47], v18\n"
        "s_add_u32 s37, s35, 98112\n"
        "ds_read_b64_tr_b16 v[22:23], v9 offset:46752\n"
        "v_mfma_f32_16x16x32_bf16 v[28:31], v[64:67], v[120:123], 0\n"
        "s_waitcnt lgkmcnt(1)\n"
        "v_smfmac_f32_16x16x64_bf16 v[28:31], v[144:147], v[56:63], v18\n"
        "ds_read_b64_tr_b16 v[42:43], v9 offset:46768\n"
        "v_readfirstlane_b32 s40, v163\n"
        "v_mfma_f32_16x16x32_bf16 v[112:115], v[64:67], v[128:131], 0\n"
        "buffer_store_dwordx3 v[160:162], v14, s[8:11], s37 offen offset:960 sc0 sc1 nt\n"
        "s_add_u32 s37, s35, 588672\n"
        "s_add_u32 s46, s37, 1344\n"
        "s_cmp_lt_i32 s40, s68\n"
        "s_waitcnt lgkmcnt(1)\n"
        "v_smfmac_f32_16x16x64_bf16 v[112:115], v[144:147], v[20:27], v18\n"
        "buffer_store_dwordx3 v[100:102], v12, s[44:47], s37 offen offset:1152 sc0 sc1 nt\n"
        "ds_read_b64_tr_b16 v[58:59], v9 offset:46784\n"
        "v_mfma_f32_16x16x32_bf16 v[96:99], v[64:67], v[36:39], 0\n"
        "s_waitcnt lgkmcnt(1)\n"
        "v_smfmac_f32_16x16x64_bf16 v[96:99], v[144:147], v[40:47], v18 abid:1\n"
        "ds_read_b64_tr_b16 v[22:23], v9 offset:46800\n"
        "s_mov_b64 exec, 1\n"
        "ds_inc_u32 v0, v1 offset:21612\n"
        "s_mov_b64 exec, -1\n"
        "s_cbranch_scc1 L53B_%=\n"
        "L54B_%=:\n"
        "ds_read_b128 v[88:91], v7 offset:0\n"
        "ds_read_b128 v[76:79], v7 offset:3328\n"
        "ds_read_b128 v[92:95], v7 offset:64\n"
        "ds_read_b128 v[80:83], v7 offset:3392\n"
        "v_mfma_f32_16x16x32_bf16 v[52:55], v[64:67], v[124:127], 0\n"
        "s_waitcnt lgkmcnt(6)\n"
        "ds_read_b128 v[100:103], v7 offset:128\n"
        "ds_read_b128 v[84:87], v7 offset:3456\n"
        "s_mov_b32 s36, s35\n"
        "v_smfmac_f32_16x16x64_bf16 v[52:55], v[144:147], v[56:63], v18 abid:1\n"
        "s_mov_b32 s35, s39\n"
        "v_mfma_f32_16x16x32_bf16 v[56:59], v[64:67], v[132:135], 0\n"
        "s_waitcnt lgkmcnt(7)\n"
        "v_smfmac_f32_16x16x64_bf16 v[56:59], v[144:147], v[20:27], v18 abid:1\n"
        "s_setprio 0\n"
        "s_waitcnt lgkmcnt(0)\n"
        "s_nop 15\n"
        "s_branch CNEXTB_%=\n"
        :: "{v0}"(tid), "{s[0:1]}"(kap), "{s2}"(bid), "{s[4:5]}"(tab) : CLOBBERS);
}

extern "C" void kernel_launch(void* const* d_in, const int* in_sizes, int n_in, void* d_out, int out_size, void* d_ws, size_t ws_size, hipStream_t stream) {
    (void)in_sizes; (void)n_in; (void)out_size; (void)ws_size;
    (void)hipFuncSetAttribute((const void*)swin_kernel, hipFuncAttributeMaxDynamicSharedMemorySize, LDS_BYTES);
    swin_kernel<<<256, 512, LDS_BYTES, stream>>>((const char*)d_in[0], (const char*)d_in[1], (const char*)d_in[2], (const char*)d_in[3], (const char*)d_in[4], (char*)d_out, (const char*)d_ws);
}
```

```cpp
#define LTOK_ 208
#define FPAD 32
#define NPR 36
#define NWC 22
#define LK0 0
#define LK1 0
#define LK2 1
#define LK3 1
#include <hip/hip_runtime.h>
#include <hip/hip_bf16.h>

constexpr int TOKB = 192, ROWB = 511 * TOKB;
constexpr unsigned FRAMEB = 511u * ROWB;
constexpr int LTOK = LTOK_;
constexpr int LFRAME = 48 * LTOK + FPAD;
constexpr int LBUF = 4 * LFRAME;
constexpr int LSTASH = 10 * 4 * LTOK;
constexpr int LSTG = 6208;
constexpr int VST = 392, LEXT = 1536 + 8 * VST;
constexpr int WT = 3088;
constexpr int LDS_AUX = (54 - NPR) / 3 * WT, LDS_TAB = LDS_AUX + 1024, LDS_CNT = LDS_TAB + 2048, LDS_BUF = LDS_CNT + 128, LDS_STASH = LDS_BUF + 2 * LBUF,
              LDS_STG = LDS_STASH + LSTASH, LDS_SCRAP = LDS_STG + 4 * LSTG, LDS_EXT = LDS_SCRAP + 512, LDS_BYTES = 163840;
static_assert(NWC >= 18 && NPR % 3 == 0 && LDS_EXT + 4 * LEXT + 256 <= LDS_BYTES && NPR * 1024 <= LBUF, "LDS");
constexpr unsigned RAWM = 18432, RAW_T = LDS_BUF, RAW_TV = RAW_T + 2 * RAWM + 128, RAW_SV = (36 - NPR) / 3 * WT, RAW_SO = LDS_EXT, RAW_SQ = LDS_BUF + LBUF, RAW_SK = RAW_SQ + RAWM;
static_assert(RAW_SV + RAWM <= LDS_AUX && RAW_SO + RAWM <= LDS_BYTES && RAW_SK + RAWM <= LDS_STASH && RAW_TV + 2 * RAWM <= LDS_STASH && RAWM + 6 * WT <= 2 * RAWM + 128, "fold LDS");

constexpr int phi(int v) { return v ^ (v >> 1); }
struct Tab {
    unsigned img[3][64][4];
    unsigned p[4][7][64][4];
    unsigned c[4][6][64][4];
    unsigned d[4][7][64][4];
    static constexpr void job(unsigned (*q)[64][4], int lane, unsigned L, unsigned R, int mode, int tau, unsigned st0, unsigned st1) {
        const int i = lane & 15, g = lane >> 4;
        for (int a = 0; a < 2; ++a) {
            const unsigned ra = (8 * (i >> 2) + 4 * a + (i & 3)) * 192 + ((g ^ ((2 * (i >> 2) + a) & 3)) * 16);
            q[0][lane][a] = L + ra;
            q[0][lane][2 + a] = mode == 0 ? R + tau * 6144 + ra : mode == 2 ? R + tau * 6144 + (16 * a + i) * 192 + ((g ^ (i >> 2)) * 16)
                                          : R + tau * 64 + (8 * g + 4 * a + (i >> 2)) * 192 + (((i & 3) ^ ((2 * g + a) & 3)) * 16);
        }
        q[1][lane][0] = st0; q[1][lane][1] = st1; q[1][lane][2] = LDS_SCRAP + g * 32;
    }
    static constexpr unsigned scat(bool win, int lane, int tau, int tb) {
        const int i = lane & 15, c = 32 * tau + 8 * (i >> 2) + 4 * tb + (i & 3), r = c / 24, o = c % 24, q = win ? o / 6 : o & 3, t = win ? o % 6 : o >> 2;
        return (win ? RAW_SV : 3 * t < NPR - 18 ? LDS_BUF + 18 * 1024 : -(NPR - 18) / 3 * WT) + t * WT + ((lane & 48) + 4 * q + r) * 16;
    }
    constexpr Tab() : img{}, p{}, c{}, d{} {
        for (int e = 0; e < 48; ++e) img[0][e >> 2][e & 3] = 0x3f803f80u;
        img[0][12][0] = 0x7f800000u;
        for (int e = 0; e < 8; ++e) img[0][36 + 10 + e][e >> 1] = e & 1 ? 0x3f800000u : 0x3f80u;
        for (int k = 0; k < 4; ++k) for (int lane = 0; lane < 64; ++lane) {
            const int i = lane & 15, g = lane >> 4, rowv = 8 * (i >> 2) + (i & 3);
            unsigned vi[20] = {};
            {
                const int pp = i >> 2, t = i & 3;
                const int t1 = (k + 3 - LK1) & 3;
                if (t1 < 3) job(p[k], lane, RAW_TV, RAW_TV + RAWM, 1, t1, scat(false, lane, t1, 0), scat(false, lane, t1, 1));
                const unsigned wl = lane * 16, lm = pp == g ? ~0u : 0u, idp = LDS_AUX + 576 + (t == g ? 10 + pp : 0) * 16, sidx = 0xdddd;
                unsigned vA[3] = {};
                for (int m = 0; m < 3; ++m) {
                    const int n = 64 * m + lane, tf = n / 96, q = n % 96 / 12, s = (n % 12) ^ tf;
                    const unsigned cc = tf * FRAMEB + (3 * (s & 3) + (s >> 2)) * 16 + 2048 - 1024 * m;
                    vA[m] = cc + q * TOKB;
                    img[1][lane][m] = (cc + 6 * ROWB + 6 * TOKB) | ((unsigned)(2 * (q & 3) + (q >> 2)) << 28);
                }
                const unsigned vA0 = vA[0], vA1 = vA[1], vA2 = vA[2], vB0 = vA0 + (ROWB - 7 * TOKB), vB1 = vA1 + (ROWB - 7 * TOKB), vB2 = vA2 + (ROWB - 7 * TOKB);
                const unsigned dA = LDS_BUF + t * LFRAME + pp * LTOK + g * 16, rA = LDS_BUF + t * LFRAME + (pp == g ? pp : 4 + g) * LTOK + 192;
                img[1][lane][3] = (2 * pp * (4 * LTOK) + t * LTOK + g * 16) | (((pp == g ? 2 * pp : 2 * g + 1) * (4 * LTOK) + t * LTOK + 192) << 16);
                const unsigned cd0 = k < 3 ? dA + k * 8 * LTOK : LDS_STASH + (img[1][lane][3] & 0xffffu), cr0 = k < 3 ? rA + k * 8 * LTOK : LDS_STASH + (img[1][lane][3] >> 16);
                const unsigned xr = LDS_STG + k * LSTG + (t >> 1) * 3104 + ((t & 1) * 8 + pp) * TOKB + (g ^ (t & 1)) * 16;
                const unsigned axp = LDS_AUX + (t ? 384 : 0);
                const unsigned pf = g * FRAMEB + (i >> 1) * TOKB + (i & 1) * 188 + 2048;
                vi[0] = xr; vi[1] = lm; vi[2] = rA; vi[3] = dA; vi[4] = vA0; vi[5] = vA1; vi[6] = vA2; vi[7] = vB0; vi[8] = vB1; vi[9] = vB2; vi[10] = sidx; vi[11] = cd0; vi[12] = cr0; vi[13] = wl; vi[14] = idp; vi[15] = pf; vi[16] = axp;
                for (int e = 0; e < 20; ++e) p[k][2 + (e >> 2)][lane][e & 3] = vi[e];
            }
            {
                const int t0 = (k + 3 - LK0) & 3, t2 = (k + 3 - LK2) & 3, t3 = (k + 3 - LK3) & 3;
                const unsigned s0 = LDS_BUF + t0 * 6144 + lane * 16, s3 = LDS_BUF + LBUF + t3 * 6144 + lane * 16;
                if (t0 < 3) job(c[k], lane, RAW_T, RAW_T + RAWM, 2, t0, s0, s0 + 3072); else job(c[k], lane, RAW_T, RAW_T + RAWM, 1, 0, LDS_SCRAP + rowv * 4, 0);
                if (t2 < 3) job(c[k] + 4, lane, RAW_SV, RAW_SO, 1, t2, scat(true, lane, t2, 0), scat(true, lane, t2, 1));
                if (t3 < 3) job(d[k], lane, RAW_SQ, RAW_SK, 0, t3, s3, s3 + 3072); else job(d[k], lane, RAW_SQ, RAW_SK, 1, 0, LDS_SCRAP + rowv * 4, 0);
                for (int m = 0; m < 3; ++m) {
                    const int n = 64 * m + lane, row = n / 12, pc = ((n % 12) ^ ((row >> 2) & 3)) * 16;
                    c[k][3][lane][m] = (24 * (row & 3) + 12 * ((row >> 2) & 1) + 2 * (row >> 3)) * 576 + pc;
                    c[k][2][lane][m] = row * 192 + pc;
                }
                const int dg = 16 * (2 * (g & 1) + (g >> 1)), pix = 4 * (i & 3) + phi(i >> 2);
                const unsigned wl = lane * 16, fr = LDS_BUF + k * LFRAME, ext = LDS_EXT + k * LEXT;
                const unsigned ta = fr + pix * LTOK + dg, zr = LDS_AUX + 384 + g * 16, t30 = i & 3 ? zr : LDS_STASH + k * LTOK + dg;
                const unsigned xa = fr + (4 * (i >> 2) + phi(g)) * LTOK + 96 * (i & 1) + 8 * ((i >> 1) & 1);
                const int sg = 8 * ((i >> 1) & 1) + 4 * (i >> 3) + 2 * ((i >> 2) & 1) + (i & 1);
                const unsigned tv = fr + (32 + 4 * (sg & 3) + phi(sg >> 2)) * LTOK + dg, c1f = (i >> 2) == 2 * g ? 0x3f800000u : 0u;
                const unsigned sidx = g < 2 ? 0x0e0e0404u | ((i >> 2) == 2 * g ? (i & 3) * 0x00100010u : (i >> 2) == 2 * g + 1 ? (i & 3) * 0x40004000u : 0u) : 0xeeee4444u;
                const unsigned ga0 = phi(g) * TOKB + 48 * (i & 3) + 12 * (i >> 2), wr = ROWB - 7 * TOKB;
                const unsigned ga1 = ga0 + (phi(g) >= 1 ? wr : 0), ga2 = ga0 + (phi(g) >= 2 ? wr : 0), ga3 = ga0 + (phi(g) >= 3 ? wr : 0);
                const unsigned pb = LDS_SCRAP + k * 128, pr0 = i < 4 && (i >> 1) == (g >> 1) ? pb + (i & 1) * 32 + (g & 1) * 16 : zr, pr1 = i == 0 || i == 2 ? pb + 64 + g * 16 : zr;
                const unsigned pw = g < 2 ? pb + ((i >> 1) & 1) * 32 + (i >> 3) * 16 + ((i >> 2) & 1) * 8 + g * 4 + (i & 1) * 2
                                          : pb + 64 + (i >> 2) * 16 + (g & 1) * 4 + ((i >> 1) & 1) * 8 + (i & 1) * 2 + (lane == 57 ? 6 : 0);
                const unsigned six2 = (i & 1 ? 0xeeeeu : 0x4444u) | ((i & 2 ? 0xeeee0000u : 0x44440000u) & (g < 2 ? 0x0f0f0000u : 0xffff0000u));
                const unsigned exs0 = i & 2 ? LDS_AUX + g * 16 : ext + (i & 1) * 64 + g * 16, t3s0 = i & 2 ? zr : LDS_STASH + k * LTOK + 64 * (g >> 1) + 16 * (g & 1) + 4 * (i & 1);
                const unsigned ex0 = i & 1 ? zr : i & 2 ? LDS_AUX + g * 16 : ext + g * 16, exv0 = ext + 1536 + i * 24;
                const unsigned rla0 = lane < 48 ? fr + (16 * g + pix) * LTOK + 192 : lane == 56 ? LDS_STASH + k * LTOK + 192 : LDS_AUX + 192;
                img[2][lane][0] = ((i & 7) * (4 * LTOK) + dg) | ((((4 * g + (i & 3)) & 7) * (4 * LTOK) + 96 * ((i >> 2) & 1) + 8 * (i >> 3)) << 16);
                img[2][lane][1] = ((i & 7) * 192u + g * 16) | ((1536u + i * 24 + (g & 1) * 4 * VST) << 16);
                img[2][lane][2] = ((fr + phi(g) * LTOK + 192 - xa) & 0xffffu) | ((fr + pix * LTOK + 192 - xa) << 16);
                img[2][lane][3] = (LDS_AUX + 576 + (10 + (i & 3)) * 16) | ((1536u + (i & 7) * VST + 384) << 16);
                vi[0] = wl; vi[1] = pw; vi[2] = pr0; vi[3] = pr1; vi[4] = six2; vi[5] = exs0; vi[6] = t3s0; vi[7] = ta; vi[8] = t30; vi[9] = xa; vi[10] = rla0; vi[11] = exv0; vi[12] = ga0; vi[13] = ga1; vi[14] = ga2; vi[15] = ga3; vi[16] = ex0; vi[17] = tv; vi[18] = sidx; vi[19] = c1f;
                for (int e = 0; e < 20; ++e) d[k][2 + (e >> 2)][lane][e & 3] = vi[e];
            }
        }
    }
};
static __device__ const Tab TAB __attribute__((aligned(128))) = Tab();

#define CLOBBERS "v1", "v2", "v3", "v4", "v5", "v6", "v7", "v8", "v9", "v10", "v11", "v12", "v13", "v14", "v15", "v16", "v17", "v18", "v19", "v20", "v21", "v22", "v23", "v24", "v25", "v26", "v27", "v28", "v29", "v30", "v31", "v32", "v33", "v34", "v35", "v36", "v37", "v38", "v39", "v40", "v41", "v42", "v43", "v44", "v45", "v46", "v47", "v48", "v49", "v50", "v51", "v52", "v53", "v54", "v55", "v56", "v57", "v58", "v59", "v60", "v61", "v62", "v63", "v64", "v65", "v66", "v67", "v68", "v69", "v70", "v71", "v72", "v73", "v74", "v75", "v76", "v77", "v78", "v79", "v80", "v81", "v82", "v83", "v84", "v85", "v86", "v87", "v88", "v89", "v90", "v91", "v92", "v93", "v94", "v95", "v96", "v97", "v98", "v99", "v100", "v101", "v102", "v103", "v104", "v105", "v106", "v107", "v108", "v109", "v110", "v111", "v112", "v113", "v114", "v115", "v116", "v117", "v118", "v119", "v120", "v121", "v122", "v123", "v124", "v125", "v126", "v127", "v128", "v129", "v130", "v131", "v132", "v133", "v134", "v135", "v136", "v137", "v138", "v139", "v140", "v141", "v142", "v143", "v144", "v145", "v146", "v147", "v148", "v149", "v150", "v151", "v152", "v153", "v154", "v155", "v156", "v157", "v158", "v159", "v160", "v161", "v162", "v163", "v164", "v165", "v166", "v167", "v168", "v169", "v170", "v171", "v172", "v173", "v174", "v175", "v176", "v177", "v178", "v179", "v180", "v181", "v182", "v183", "v184", "v185", "v186", "v187", "v188", "v189", "v190", "v191", "v192", "v193", "v194", "v195", "v196", "v197", "v198", "v199", "v200", "v201", "v202", "v203", "v204", "v205", "v206", "v207", "v208", "v209", "v210", "v211", "v212", "v213", "v214", "v215", "v216", "v217", "v218", "v219", "v220", "v221", "v222", "v223", "v224", "v225", "v226", "v227", "v228", "v229", "v230", "v231", "v232", "v233", "v234", "v235", "v236", "v237", "v238", "v239", "v240", "v241", "v242", "v243", "v244", "v245", "v246", "v247", "v248", "v249", "v250", "v251", "v252", "v253", "v254", "v255", "s3", "s6", "s7", "s8", "s9", "s10", "s11", "s12", "s13", "s14", "s15", "s16", "s17", "s18", "s19", "s20", "s21", "s22", "s23", "s24", "s25", "s26", "s27", "s28", "s29", "s30", "s31", "s34", "s35", "s36", "s37", "s38", "s39", "s40", "s41", "s42", "s43", "s44", "s45", "s46", "s47", "s48", "s49", "s50", "s51", "s52", "s53", "s54", "s55", "s56", "s57", "s58", "s59", "s60", "s61", "s62", "s63", "s64", "s65", "s66", "s67", "s68", "s69", "s70", "s71", "s72", "s73", "s74", "s75", "s76", "s77", "vcc", "scc", "memory"
__global__ __launch_bounds__(512, 2) void swin_kernel(const char* x, const char* wqkv_t, const char* wo_t, const char* wqkv_s, const char* wo_s, char* out, const char* ws) {
    const unsigned long long kap = (unsigned long long)__builtin_amdgcn_kernarg_segment_ptr(), tab = (unsigned long long)&TAB;
    const unsigned tid = threadIdx.x, bid = blockIdx.x;
    asm volatile(
        "s_load_dwordx8 s[44:51], s[0:1], 0x0\n"
        "s_load_dwordx4 s[52:55], s[0:1], 0x20\n"
        "s_load_dwordx2 s[56:57], s[0:1], 0x30\n"
        "v_readfirstlane_b32 s3, v0\n"
        "v_and_b32 v56, 63, v0\n"
        "v_lshlrev_b32 v57, 4, v56\n"
        "s_bfe_u32 s3, s3, 0x30006\n"
        "s_and_b32 s6, s3, 3\n"
        "s_mul_i32 s64, s3, 9216\n"
        "s_add_u32 s64, s64, 21728\n"
        "s_mov_b32 s27, 0x20000\n"
        "s_cmp_lt_u32 s3, 4\n"
        "s_cbranch_scc0 PPRO_%=\n"
        "s_mul_i32 s7, s6, 6144\n"
        "s_add_u32 s7, s7, 33792\n"
        "s_add_u32 s22, s4, s7\n"
        "s_addc_u32 s23, s5, 0\n"
        "global_load_dwordx4 v[240:243], v57, s[22:23] offset:-2048\n"
        "global_load_dwordx4 v[244:247], v57, s[22:23] offset:-1024\n"
        "global_load_dwordx4 v[36:39], v57, s[22:23] offset:1024\n"
        "global_load_dwordx4 v[48:51], v57, s[22:23] offset:2048\n"
        "global_load_dwordx4 v[52:55], v57, s[22:23] offset:3072\n"
        "s_mov_b32 s65, 13824\n"
        "v_add_u32 v24, 0, v56\n"
        "v_mul_u32_u24 v21, 171, v24\n"
        "v_lshrrev_b32 v21, 11, v21\n"
        "v_mad_i32_i24 v24, v21, -12, v24\n"
        "v_bfe_u32 v58, v21, 2, 2\n"
        "v_xor_b32 v58, v24, v58\n"
        "v_and_b32 v27, 7, v21\n"
        "v_mul_u32_u24 v27, 576, v27\n"
        "v_lshl_add_u32 v58, v58, 4, v27\n"
        "v_add_u32 v25, 64, v56\n"
        "v_mul_u32_u24 v22, 171, v25\n"
        "v_lshrrev_b32 v22, 11, v22\n"
        "v_mad_i32_i24 v25, v22, -12, v25\n"
        "v_bfe_u32 v59, v22, 2, 2\n"
        "v_xor_b32 v59, v25, v59\n"
        "v_and_b32 v27, 7, v22\n"
        "v_mul_u32_u24 v27, 576, v27\n"
        "v_lshl_add_u32 v59, v59, 4, v27\n"
        "v_lshrrev_b32 v27, 3, v22\n"
        "v_mad_u32_u24 v59, v27, s65, v59\n"
        "v_add_u32 v26, 128, v56\n"
        "v_mul_u32_u24 v23, 171, v26\n"
        "v_lshrrev_b32 v23, 11, v23\n"
        "v_mad_i32_i24 v26, v23, -12, v26\n"
        "v_bfe_u32 v60, v23, 2, 2\n"
        "v_xor_b32 v60, v26, v60\n"
        "v_and_b32 v27, 7, v23\n"
        "v_mul_u32_u24 v27, 576, v27\n"
        "v_lshl_add_u32 v60, v60, 4, v27\n"
        "v_add_u32 v60, s65, v60\n"
        "s_lshr_b32 s61, s6, 1\n"
        "s_mul_i32 s61, s61, 192\n"
        "s_mov_b32 s62, 27648\n"
        "s_mov_b32 s63, 4608\n"
        "s_add_u32 s7, s62, s63\n"
        "s_lshl_b32 s29, s63, 1\n"
        "s_add_u32 s60, s7, s63\n"
        "s_bitcmp1_b32 s6, 0\n"
        "s_cselect_b32 s28, s7, 0\n"
        "s_cselect_b32 s29, s29, s62\n"
        "s_cselect_b32 s60, s60, s63\n"
        "s_add_u32 s28, s28, s61\n"
        "s_add_u32 s29, s29, s61\n"
        "s_add_u32 s60, s60, s61\n"
        "s_mov_b32 s26, 55296\n"
        "s_waitcnt lgkmcnt(0)\n"
        "s_mov_b32 s24, s46\n"
        "s_and_b32 s25, s47, 0xffff\n"
        "s_add_u32 m0, s64, 0\n"
        "s_nop 0\n"
        "buffer_load_dwordx4 v58, s[24:27], s28 offen lds\n"
        "s_add_u32 m0, s64, 1024\n"
        "s_nop 0\n"
        "buffer_load_dwordx4 v59, s[24:27], s28 offen lds\n"
        "s_add_u32 m0, s64, 2048\n"
        "s_nop 0\n"
        "buffer_load_dwordx4 v60, s[24:27], s28 offen lds\n"
        "s_add_u32 m0, s64, 3072\n"
        "s_nop 0\n"
        "buffer_load_dwordx4 v58, s[24:27], s29 offen lds\n"
        "s_add_u32 m0, s64, 4096\n"
        "s_nop 0\n"
        "buffer_load_dwordx4 v59, s[24:27], s29 offen lds\n"
        "s_add_u32 m0, s64, 5120\n"
        "s_nop 0\n"
        "buffer_load_dwordx4 v60, s[24:27], s29 offen lds\n"
        "s_add_u32 m0, s64, 6144\n"
        "s_nop 0\n"
        "buffer_load_dwordx4 v58, s[24:27], s60 offen lds\n"
        "s_add_u32 m0, s64, 7168\n"
        "s_nop 0\n"
        "buffer_load_dwordx4 v59, s[24:27], s60 offen lds\n"
        "s_add_u32 m0, s64, 8192\n"
        "s_nop 0\n"
        "buffer_load_dwordx4 v60, s[24:27], s60 offen lds\n"
        "s_barrier\n"
        "s_setprio 1\n"
        "v_mov_b32 v250, 21600\n"
        "s_mov_b32 s41, 0x400000\n"
        "s_and_b32 s7, s2, 7\n"
        "s_lshl_b32 s7, s7, 5\n"
        "s_lshr_b32 s14, s2, 3\n"
        "s_add_u32 s7, s7, s14\n"
        "s_and_b32 s16, s7, 255\n"
        "s_sub_u32 s7, s7, s16\n"
        "s_mul_i32 s7, s7, 20\n"
        "s_add_u32 s7, s7, s16\n"
        "s_mov_b32 s14, 22\n"
        "s_mul_i32 s15, s7, 57457\n"
        "s_lshr_b32 s15, s15, 22\n"
        "s_mov_b32 s16, s7\n"
        "s_mul_i32 s15, s15, 588672\n"
        "s_mul_i32 s7, s16, 1344\n"
        "s_add_u32 s15, s15, s7\n"
        "s_mov_b32 s8, s54\n"
        "s_and_b32 s9, s55, 0xffff\n"
        "s_mov_b32 s10, 200540928\n"
        "s_mov_b32 s11, 0x20000\n"
        "s_mov_b64 s[12:13], s[54:55]\n"
        "s_mul_i32 s17, s6, 50135232\n"
        "s_mul_i32 s18, s6, 208\n"
        "s_add_u32 s18, s18, 109536\n"
        "s_mul_i32 s19, s6, 4672\n"
        "s_add_u32 s19, s19, 135520\n"
        "s_mul_i32 s7, s6, 7168\n"
        "s_add_u32 s7, s7, 59392\n"
        "s_add_u32 s22, s4, s7\n"
        "s_addc_u32 s23, s5, 0\n"
        "s_waitcnt vmcnt(0)\n"
        "s_mov_b64 exec, 1\n"
        "ds_inc_u32 v250, v250 offset:16\n"
        "s_mov_b64 exec, -1\n"
        "L67_%=:\n"
        "ds_read_b32 v251, v250 offset:16\n"
        "s_waitcnt lgkmcnt(0)\n"
        "v_readfirstlane_b32 s40, v251\n"
        "s_nop 3\n"
        "s_cmp_ge_i32 s40, 4\n"
        "s_cbranch_scc1 L68_%=\n"
        "s_nop 0\n"
        "s_sub_u32 s41, s41, 1\n"
        "s_cbranch_scc0 L67_%=\n"
        "s_endpgm\n"
        "L68_%=:\n"
        "s_cmp_eq_u32 s6, 0\n"
        "s_cbranch_scc1 L69_%=\n"
        "s_mov_b32 s34, 0x4162ccfd\n"
        "s_mov_b32 s35, 21624\n"
        "s_mov_b32 s36, 21624\n"
        "s_mov_b32 s37, 21628\n"
        "s_call_b64 s[30:31], FOLDM_%=\n"
        "s_branch L70_%=\n"
        "L69_%=:\n"
        "s_mov_b32 s34, 0xbe173353\n"
        "s_mov_b32 s35, 21656\n"
        "s_mov_b32 s36, 21656\n"
        "s_mov_b32 s37, 21624\n"
        "s_call_b64 s[30:31], FOLDR_%=\n"
        "s_cmp_lg_u32 s2, 0\n"
        "s_cbranch_scc1 L66_%=\n"
        "s_mov_b64 s[24:25], s[56:57]\n"
        "s_mov_b64 exec, 1\n"
        "v_mov_b32 v27, 0\n"
        "v_mov_b32 v28, 0\n"
        "global_atomic_swap v252, v27, v28, s[24:25] offset:0 sc0\n"
        "s_mov_b64 exec, -1\n"
        "s_waitcnt vmcnt(0)\n"
        "s_mov_b64 exec, 1\n"
        "v_mov_b32 v27, 0\n"
        "v_mov_b32 v28, 1\n"
        "global_atomic_add v252, v27, v28, s[24:25] offset:256 sc0\n"
        "s_mov_b64 exec, -1\n"
        "L66_%=:\n"
        "L70_%=:\n"
        "s_setprio 2\n"
        "s_waitcnt vmcnt(0)\n"
        "v_mov_b64 v[240:241], v[48:49]\n"
        "v_mov_b64 v[242:243], v[50:51]\n"
        "v_mov_b64 v[244:245], v[52:53]\n"
        "v_mov_b64 v[246:247], v[54:55]\n"
        "L71_%=:\n"
        "ds_read_b32 v251, v250 offset:32\n"
        "s_waitcnt lgkmcnt(0)\n"
        "v_readfirstlane_b32 s40, v251\n"
        "s_nop 3\n"
        "s_cmp_ge_i32 s40, 4\n"
        "s_cbranch_scc1 L72_%=\n"
        "s_nop 0\n"
        "s_sub_u32 s41, s41, 1\n"
        "s_cbranch_scc0 L71_%=\n"
        "s_endpgm\n"
        "L72_%=:\n"
        "s_cmp_eq_u32 s6, 1\n"
        "s_cbranch_scc1 L73_%=\n"
        "s_mov_b32 s34, 0x411cc471\n"
        "s_mov_b32 s35, 21640\n"
        "s_mov_b32 s36, 21640\n"
        "s_mov_b32 s37, 21652\n"
        "s_call_b64 s[30:31], FOLDW_%=\n"
        "s_branch L74_%=\n"
        "L73_%=:\n"
        "s_mov_b64 exec, 1\n"
        "ds_inc_u32 v250, v250 offset:40\n"
        "s_mov_b64 exec, -1\n"
        "s_mov_b32 s24, s50\n"
        "s_and_b32 s25, s51, 0xffff\n"
        "s_mov_b32 s26, 55296\n"
        "s_mov_b32 m0, 61792\n"
        "s_mov_b32 s28, 0\n"
        "buffer_load_dwordx4 v36, s[24:27], s28 offen lds\n"
        "s_mov_b32 m0, 62816\n"
        "s_nop 0\n"
        "buffer_load_dwordx4 v37, s[24:27], s28 offen lds\n"
        "s_mov_b32 m0, 63840\n"
        "s_nop 0\n"
        "buffer_load_dwordx4 v38, s[24:27], s28 offen lds\n"
        "s_mov_b32 m0, 64864\n"
        "s_mov_b32 s28, 576\n"
        "buffer_load_dwordx4 v36, s[24:27], s28 offen lds\n"
        "s_mov_b32 m0, 65888\n"
        "s_nop 0\n"
        "buffer_load_dwordx4 v37, s[24:27], s28 offen lds\n"
        "s_mov_b32 m0, 66912\n"
        "s_nop 0\n"
        "buffer_load_dwordx4 v38, s[24:27], s28 offen lds\n"
        "s_mov_b32 m0, 67936\n"
        "s_mov_b32 s28, 2304\n"
        "buffer_load_dwordx4 v36, s[24:27], s28 offen lds\n"
        "s_mov_b32 m0, 68960\n"
        "s_nop 0\n"
        "buffer_load_dwordx4 v37, s[24:27], s28 offen lds\n"
        "s_mov_b32 m0, 69984\n"
        "s_nop 0\n"
        "buffer_load_dwordx4 v38, s[24:27], s28 offen lds\n"
        "s_mov_b32 m0, 71008\n"
        "s_mov_b32 s28, 2880\n"
        "buffer_load_dwordx4 v36, s[24:27], s28 offen lds\n"
        "s_mov_b32 m0, 72032\n"
        "s_nop 0\n"
        "buffer_load_dwordx4 v37, s[24:27], s28 offen lds\n"
        "s_mov_b32 m0, 73056\n"
        "s_nop 0\n"
        "buffer_load_dwordx4 v38, s[24:27], s28 offen lds\n"
        "s_mov_b32 m0, 74080\n"
        "s_mov_b32 s28, 4608\n"
        "buffer_load_dwordx4 v36, s[24:27], s28 offen lds\n"
        "s_mov_b32 m0, 75104\n"
        "s_nop 0\n"
        "buffer_load_dwordx4 v37, s[24:27], s28 offen lds\n"
        "s_mov_b32 m0, 76128\n"
        "s_nop 0\n"
        "buffer_load_dwordx4 v38, s[24:27], s28 offen lds\n"
        "s_mov_b32 m0, 77152\n"
        "s_mov_b32 s28, 5184\n"
        "buffer_load_dwordx4 v36, s[24:27], s28 offen lds\n"
        "s_mov_b32 m0, 78176\n"
        "s_nop 0\n"
        "buffer_load_dwordx4 v37, s[24:27], s28 offen lds\n"
        "s_mov_b32 m0, 79200\n"
        "s_nop 0\n"
        "buffer_load_dwordx4 v38, s[24:27], s28 offen lds\n"
        "s_mov_b32 s24, s50\n"
        "s_and_b32 s25, s51, 0xffff\n"
        "s_mov_b32 s26, 55296\n"
        "s_mov_b32 m0, 80224\n"
        "s_mov_b32 s28, 192\n"
        "buffer_load_dwordx4 v36, s[24:27], s28 offen lds\n"
        "s_mov_b32 m0, 81248\n"
        "s_nop 0\n"
        "buffer_load_dwordx4 v37, s[24:27], s28 offen lds\n"
        "s_mov_b32 m0, 82272\n"
        "s_nop 0\n"
        "buffer_load_dwordx4 v38, s[24:27], s28 offen lds\n"
        "s_mov_b32 m0, 83296\n"
        "s_mov_b32 s28, 768\n"
        "buffer_load_dwordx4 v36, s[24:27], s28 offen lds\n"
        "s_mov_b32 m0, 84320\n"
        "s_nop 0\n"
        "buffer_load_dwordx4 v37, s[24:27], s28 offen lds\n"
        "s_mov_b32 m0, 85344\n"
        "s_nop 0\n"
        "buffer_load_dwordx4 v38, s[24:27], s28 offen lds\n"
        "s_mov_b32 m0, 86368\n"
        "s_mov_b32 s28, 2496\n"
        "buffer_load_dwordx4 v36, s[24:27], s28 offen lds\n"
        "s_mov_b32 m0, 87392\n"
        "s_nop 0\n"
        "buffer_load_dwordx4 v37, s[24:27], s28 offen lds\n"
        "s_mov_b32 m0, 88416\n"
        "s_nop 0\n"
        "buffer_load_dwordx4 v38, s[24:27], s28 offen lds\n"
        "s_mov_b32 m0, 89440\n"
        "s_mov_b32 s28, 3072\n"
        "buffer_load_dwordx4 v36, s[24:27], s28 offen lds\n"
        "s_mov_b32 m0, 90464\n"
        "s_nop 0\n"
        "buffer_load_dwordx4 v37, s[24:27], s28 offen lds\n"
        "s_mov_b32 m0, 91488\n"
        "s_nop 0\n"
        "buffer_load_dwordx4 v38, s[24:27], s28 offen lds\n"
        "s_mov_b32 m0, 92512\n"
        "s_mov_b32 s28, 4800\n"
        "buffer_load_dwordx4 v36, s[24:27], s28 offen lds\n"
        "s_mov_b32 m0, 93536\n"
        "s_nop 0\n"
        "buffer_load_dwordx4 v37, s[24:27], s28 offen lds\n"
        "s_mov_b32 m0, 94560\n"
        "s_nop 0\n"
        "buffer_load_dwordx4 v38, s[24:27], s28 offen lds\n"
        "s_mov_b32 m0, 95584\n"
        "s_mov_b32 s28, 5376\n"
        "buffer_load_dwordx4 v36, s[24:27], s28 offen lds\n"
        "s_mov_b32 m0, 96608\n"
        "s_nop 0\n"
        "buffer_load_dwordx4 v37, s[24:27], s28 offen lds\n"
        "s_mov_b32 m0, 97632\n"
        "s_nop 0\n"
        "buffer_load_dwordx4 v38, s[24:27], s28 offen lds\n"
        "global_load_dwordx4 v[40:43], v57, s[22:23] offset:-3072\n"
        "global_load_dwordx4 v[44:47], v57, s[22:23] offset:-2048\n"
        "global_load_dwordx4 v[0:3], v57, s[22:23] offset:-1024\n"
        "global_load_dwordx4 v[4:7], v57, s[22:23] offset:0\n"
        "global_load_dwordx4 v[8:11], v57, s[22:23] offset:1024\n"
        "global_load_dwordx4 v[12:15], v57, s[22:23] offset:2048\n"
        "global_load_dwordx4 v[16:19], v57, s[22:23] offset:3072\n"
        "L74_%=:\n"
        "s_setprio 2\n"
        "s_waitcnt vmcnt(0)\n"
        "v_mov_b64 v[240:241], v[40:41]\n"
        "v_mov_b64 v[242:243], v[42:43]\n"
        "v_mov_b64 v[244:245], v[44:45]\n"
        "v_mov_b64 v[246:247], v[46:47]\n"
        "s_cmp_eq_u32 s6, 1\n"
        "s_cbranch_scc1 L79_%=\n"
        "s_mov_b32 s34, 0x41622adf\n"
        "s_mov_b32 s35, 21648\n"
        "s_mov_b32 s36, 21648\n"
        "s_mov_b32 s37, 21652\n"
        "L75_%=:\n"
        "ds_read_b32 v251, v250 offset:44\n"
        "s_waitcnt lgkmcnt(0)\n"
        "v_readfirstlane_b32 s40, v251\n"
        "s_nop 3\n"
        "s_cmp_ge_i32 s40, 1\n"
        "s_cbranch_scc1 L76_%=\n"
        "s_nop 0\n"
        "s_sub_u32 s41, s41, 1\n"
        "s_cbranch_scc0 L75_%=\n"
        "s_endpgm\n"
        "L76_%=:\n"
        "s_call_b64 s[30:31], FOLDM_%=\n"
        "s_branch L80_%=\n"
        "L79_%=:\n"
        "s_mov_b64 exec, 1\n"
        "ds_inc_u32 v250, v250 offset:44\n"
        "s_mov_b64 exec, -1\n"
        "L77_%=:\n"
        "ds_read_b32 v251, v250 offset:40\n"
        "s_waitcnt lgkmcnt(0)\n"
        "v_readfirstlane_b32 s40, v251\n"
        "s_nop 3\n"
        "s_cmp_ge_i32 s40, 4\n"
        "s_cbranch_scc1 L78_%=\n"
        "s_nop 0\n"
        "s_sub_u32 s41, s41, 1\n"
        "s_cbranch_scc0 L77_%=\n"
        "s_endpgm\n"
        "L78_%=:\n"
        "s_mov_b32 s34, 0xbe16c740\n"
        "s_mov_b32 s35, 21656\n"
        "s_mov_b32 s36, 21656\n"
        "s_mov_b32 s37, 21648\n"
        "s_call_b64 s[30:31], FOLDR_%=\n"
        "L80_%=:\n"
        "s_branch CONS_%=\n"
        "FOLDM_%=:\n"
        "ds_read_b128 v[88:91], v242\n"
        "ds_read_b128 v[92:95], v240\n"
        "ds_read_b128 v[180:183], v242 offset:64\n"
        "ds_read_b128 v[172:175], v240 offset:64\n"
        "ds_read_b128 v[184:187], v242 offset:128\n"
        "ds_read_b128 v[164:167], v240 offset:128\n"
        "ds_read_b128 v[168:171], v241\n"
        "ds_read_b128 v[100:103], v241 offset:64\n"
        "ds_read_b128 v[104:107], v241 offset:128\n"
        "ds_read_b128 v[140:143], v240 offset:6144\n"
        "ds_read_b128 v[132:135], v240 offset:6208\n"
        "ds_read_b128 v[96:99], v240 offset:6272\n"
        "ds_read_b128 v[136:139], v241 offset:6144\n"
        "ds_read_b128 v[128:131], v241 offset:6208\n"
        "ds_read_b128 v[108:111], v241 offset:6272\n"
        "s_waitcnt lgkmcnt(13)\n"
        "v_mfma_f32_16x16x32_bf16 v[64:67], v[92:95], v[88:91], 0\n"
        "s_waitcnt lgkmcnt(11)\n"
        "ds_read_b128 v[152:155], v240 offset:12288\n"
        "v_mfma_f32_16x16x32_bf16 v[64:67], v[172:175], v[180:183], v[64:67]\n"
        "s_waitcnt lgkmcnt(8)\n"
        "ds_read_b128 v[160:163], v240 offset:12352\n"
        "v_mfma_f32_16x16x32_bf16 v[64:67], v[164:167], v[184:187], v[64:67]\n"
        "ds_read_b128 v[112:115], v240 offset:12416\n"
        "v_mfma_f32_16x16x32_bf16 v[68:71], v[168:171], v[88:91], 0\n"
        "ds_read_b128 v[116:119], v241 offset:12288\n"
        "v_mfma_f32_16x16x32_bf16 v[68:71], v[100:103], v[180:183], v[68:71]\n"
        "s_waitcnt lgkmcnt(4)\n"
        "ds_read_b128 v[120:123], v241 offset:12352\n"
        "v_mfma_f32_16x16x32_bf16 v[68:71], v[104:107], v[184:187], v[68:71]\n"
        "ds_read_b128 v[124:127], v241 offset:12416\n"
        "v_mfma_f32_16x16x32_bf16 v[72:75], v[140:143], v[88:91], 0\n"
        "ds_read_b128 v[144:147], v243\n"
        "v_mfma_f32_16x16x32_bf16 v[72:75], v[132:135], v[180:183], v[72:75]\n"
        "ds_read_b128 v[148:151], v243 offset:64\n"
        "v_mfma_f32_16x16x32_bf16 v[72:75], v[96:99], v[184:187], v[72:75]\n"
        "ds_read_b128 v[156:159], v243 offset:128\n"
        "v_mfma_f32_16x16x32_bf16 v[76:79], v[136:139], v[88:91], 0\n"
        "v_add_f32 v176, v64, v68\n"
        "v_add_f32 v177, v65, v69\n"
        "v_add_f32 v178, v66, v70\n"
        "v_add_f32 v179, v67, v71\n"
        "v_mfma_f32_16x16x32_bf16 v[76:79], v[128:131], v[180:183], v[76:79]\n"
        "v_mfma_f32_16x16x32_bf16 v[76:79], v[108:111], v[184:187], v[76:79]\n"
        "s_waitcnt lgkmcnt(3)\n"
        "v_add_f32 v176, v176, v72\n"
        "v_add_f32 v177, v177, v73\n"
        "v_add_f32 v178, v178, v74\n"
        "v_add_f32 v179, v179, v75\n"
        "v_mfma_f32_16x16x32_bf16 v[80:83], v[152:155], v[88:91], 0\n"
        "v_mfma_f32_16x16x32_bf16 v[80:83], v[160:163], v[180:183], v[80:83]\n"
        "v_mfma_f32_16x16x32_bf16 v[80:83], v[112:115], v[184:187], v[80:83]\n"
        "v_add_f32 v176, v176, v76\n"
        "v_add_f32 v177, v177, v77\n"
        "v_add_f32 v178, v178, v78\n"
        "v_add_f32 v179, v179, v79\n"
        "v_mfma_f32_16x16x32_bf16 v[84:87], v[116:119], v[88:91], 0\n"
        "v_mfma_f32_16x16x32_bf16 v[84:87], v[120:123], v[180:183], v[84:87]\n"
        "v_mfma_f32_16x16x32_bf16 v[84:87], v[124:127], v[184:187], v[84:87]\n"
        "s_waitcnt lgkmcnt(0)\n"
        "v_add_f32 v176, v176, v80\n"
        "v_add_f32 v177, v177, v81\n"
        "v_add_f32 v178, v178, v82\n"
        "v_add_f32 v179, v179, v83\n"
        "v_mov_b32 v239, s35\n"
        "s_waitcnt vmcnt(6)\n"
        "s_mov_b64 exec, 1\n"
        "ds_inc_u32 v239, v239 offset:0\n"
        "s_mov_b64 exec, -1\n"
        "v_add_f32 v176, v176, v84\n"
        "v_add_f32 v177, v177, v85\n"
        "v_add_f32 v178, v178, v86\n"
        "v_add_f32 v179, v179, v87\n"
        "v_add_f32 v176, v176, v177\n"
        "v_add_f32 v178, v178, v179\n"
        "v_add_f32 v176, v176, v178\n"
        "v_mov_b32 v177, v176\n"
        "v_mfma_f32_16x16x32_bf16 v[88:91], v[92:95], v[144:147], 0\n"
        "v_mfma_f32_16x16x32_bf16 v[88:91], v[172:175], v[148:151], v[88:91]\n"
        "v_permlane32_swap_b32 v176, v177\n"
        "v_add_f32 v176, v176, v177\n"
        "v_mov_b32 v177, v176\n"
        "v_mfma_f32_16x16x32_bf16 v[88:91], v[164:167], v[156:159], v[88:91]\n"
        "v_mfma_f32_16x16x32_bf16 v[92:95], v[168:171], v[144:147], 0\n"
        "v_permlane16_swap_b32 v176, v177\n"
        "v_add_f32 v176, v176, v177\n"
        "v_mul_f32 v176, 0xbc2aaaab, v176\n"
        "v_mul_f32 v176, s34, v176\n"
        "v_fma_f32 v64, v64, s34, v176\n"
        "v_fma_f32 v65, v65, s34, v176\n"
        "v_fma_f32 v66, v66, s34, v176\n"
        "v_fma_f32 v67, v67, s34, v176\n"
        "v_fma_f32 v68, v68, s34, v176\n"
        "v_fma_f32 v69, v69, s34, v176\n"
        "v_fma_f32 v70, v70, s34, v176\n"
        "v_fma_f32 v71, v71, s34, v176\n"
        "v_fma_f32 v72, v72, s34, v176\n"
        "v_fma_f32 v73, v73, s34, v176\n"
        "v_fma_f32 v74, v74, s34, v176\n"
        "v_fma_f32 v75, v75, s34, v176\n"
        "v_fma_f32 v76, v76, s34, v176\n"
        "v_fma_f32 v77, v77, s34, v176\n"
        "v_fma_f32 v78, v78, s34, v176\n"
        "v_fma_f32 v79, v79, s34, v176\n"
        "v_fma_f32 v80, v80, s34, v176\n"
        "v_fma_f32 v81, v81, s34, v176\n"
        "v_fma_f32 v82, v82, s34, v176\n"
        "v_fma_f32 v83, v83, s34, v176\n"
        "v_fma_f32 v84, v84, s34, v176\n"
        "v_fma_f32 v85, v85, s34, v176\n"
        "v_fma_f32 v86, v86, s34, v176\n"
        "v_fma_f32 v87, v87, s34, v176\n"
        "v_mfma_f32_16x16x32_bf16 v[92:95], v[100:103], v[148:151], v[92:95]\n"
        "v_mfma_f32_16x16x32_bf16 v[92:95], v[104:107], v[156:159], v[92:95]\n"
        "v_mfma_f32_16x16x32_bf16 v[100:103], v[140:143], v[144:147], 0\n"
        "v_mfma_f32_16x16x32_bf16 v[100:103], v[132:135], v[148:151], v[100:103]\n"
        "v_mfma_f32_16x16x32_bf16 v[100:103], v[96:99], v[156:159], v[100:103]\n"
        "v_mfma_f32_16x16x32_bf16 v[104:107], v[136:139], v[144:147], 0\n"
        "v_mfma_f32_16x16x32_bf16 v[104:107], v[128:131], v[148:151], v[104:107]\n"
        "v_mfma_f32_16x16x32_bf16 v[104:107], v[108:111], v[156:159], v[104:107]\n"
        "v_mfma_f32_16x16x32_bf16 v[96:99], v[152:155], v[144:147], 0\n"
        "v_mfma_f32_16x16x32_bf16 v[96:99], v[160:163], v[148:151], v[96:99]\n"
        "v_add_f32 v128, v88, v92\n"
        "v_add_f32 v129, v89, v93\n"
        "v_add_f32 v130, v90, v94\n"
        "v_add_f32 v131, v91, v95\n"
        "v_add_f32 v128, v128, v100\n"
        "v_add_f32 v129, v129, v101\n"
        "v_add_f32 v130, v130, v102\n"
        "v_add_f32 v131, v131, v103\n"
        "v_add_f32 v128, v128, v104\n"
        "v_add_f32 v129, v129, v105\n"
        "v_add_f32 v130, v130, v106\n"
        "v_add_f32 v131, v131, v107\n"
        "v_mfma_f32_16x16x32_bf16 v[96:99], v[112:115], v[156:159], v[96:99]\n"
        "v_mfma_f32_16x16x32_bf16 v[108:111], v[116:119], v[144:147], 0\n"
        "v_mfma_f32_16x16x32_bf16 v[108:111], v[120:123], v[148:151], v[108:111]\n"
        "v_mfma_f32_16x16x32_bf16 v[108:111], v[124:127], v[156:159], v[108:111]\n"
        "v_mov_b32 v239, s37\n"
        "s_nop 3\n"
        "v_add_f32 v128, v128, v96\n"
        "v_add_f32 v129, v129, v97\n"
        "v_add_f32 v130, v130, v98\n"
        "v_add_f32 v131, v131, v99\n"
        "v_add_f32 v128, v128, v108\n"
        "v_add_f32 v129, v129, v109\n"
        "v_add_f32 v130, v130, v110\n"
        "v_add_f32 v131, v131, v111\n"
        "v_add_f32 v128, v128, v129\n"
        "v_add_f32 v130, v130, v131\n"
        "v_add_f32 v128, v128, v130\n"
        "v_mov_b32 v129, v128\n"
        "s_nop 1\n"
        "v_permlane32_swap_b32 v128, v129\n"
        "v_add_f32 v128, v128, v129\n"
        "v_mov_b32 v129, v128\n"
        "s_nop 1\n"
        "v_permlane16_swap_b32 v128, v129\n"
        "v_add_f32 v128, v128, v129\n"
        "v_mul_f32 v128, 0xbc2aaaab, v128\n"
        "v_mul_f32 v128, s34, v128\n"
        "v_fma_f32 v88, v88, s34, v128\n"
        "v_fma_f32 v89, v89, s34, v128\n"
        "v_fma_f32 v90, v90, s34, v128\n"
        "v_fma_f32 v91, v91, s34, v128\n"
        "v_fma_f32 v92, v92, s34, v128\n"
        "v_fma_f32 v93, v93, s34, v128\n"
        "v_fma_f32 v94, v94, s34, v128\n"
        "v_fma_f32 v95, v95, s34, v128\n"
        "v_fma_f32 v100, v100, s34, v128\n"
        "v_fma_f32 v101, v101, s34, v128\n"
        "v_fma_f32 v102, v102, s34, v128\n"
        "v_fma_f32 v103, v103, s34, v128\n"
        "v_fma_f32 v104, v104, s34, v128\n"
        "v_fma_f32 v105, v105, s34, v128\n"
        "v_fma_f32 v106, v106, s34, v128\n"
        "v_fma_f32 v107, v107, s34, v128\n"
        "v_fma_f32 v96, v96, s34, v128\n"
        "v_fma_f32 v97, v97, s34, v128\n"
        "v_fma_f32 v98, v98, s34, v128\n"
        "v_fma_f32 v99, v99, s34, v128\n"
        "v_fma_f32 v108, v108, s34, v128\n"
        "v_fma_f32 v109, v109, s34, v128\n"
        "v_fma_f32 v110, v110, s34, v128\n"
        "v_fma_f32 v111, v111, s34, v128\n"
        "v_mov_b32 v237, s36\n"
        "L62_%=:\n"
        "ds_read_b32 v238, v237 offset:0\n"
        "s_waitcnt lgkmcnt(0)\n"
        "v_readfirstlane_b32 s40, v238\n"
        "s_nop 3\n"
        "s_cmp_ge_i32 s40, 4\n"
        "s_cbranch_scc1 L63_%=\n"
        "s_nop 0\n"
        "s_sub_u32 s41, s41, 1\n"
        "s_cbranch_scc0 L62_%=\n"
        "s_endpgm\n"
        "L63_%=:\n"
        "ds_read_b128 v[132:135], v246\n"
        "ds_read_b128 v[128:131], v246 offset:16\n"
        "ds_read_b128 v[124:127], v246 offset:128\n"
        "ds_read_b128 v[120:123], v246 offset:144\n"
        "ds_read_b128 v[116:119], v246 offset:256\n"
        "ds_read_b128 v[112:115], v246 offset:272\n"
        "s_waitcnt lgkmcnt(5)\n"
        "v_add_f32 v64, v64, v132\n"
        "v_add_f32 v65, v65, v133\n"
        "v_add_f32 v66, v66, v134\n"
        "v_add_f32 v67, v67, v135\n"
        "s_waitcnt lgkmcnt(0)\n"
        "v_add_f32 v68, v68, v128\n"
        "v_add_f32 v69, v69, v129\n"
        "v_add_f32 v70, v70, v130\n"
        "v_add_f32 v71, v71, v131\n"
        "v_add_f32 v88, v88, v132\n"
        "v_add_f32 v89, v89, v133\n"
        "v_add_f32 v90, v90, v134\n"
        "v_add_f32 v91, v91, v135\n"
        "v_add_f32 v92, v92, v128\n"
        "v_add_f32 v93, v93, v129\n"
        "v_add_f32 v94, v94, v130\n"
        "v_add_f32 v95, v95, v131\n"
        "v_add_f32 v72, v72, v124\n"
        "v_add_f32 v73, v73, v125\n"
        "v_add_f32 v74, v74, v126\n"
        "v_add_f32 v75, v75, v127\n"
        "v_add_f32 v76, v76, v120\n"
        "v_add_f32 v77, v77, v121\n"
        "v_add_f32 v78, v78, v122\n"
        "v_add_f32 v79, v79, v123\n"
        "v_add_f32 v100, v100, v124\n"
        "v_add_f32 v101, v101, v125\n"
        "v_add_f32 v102, v102, v126\n"
        "v_add_f32 v103, v103, v127\n"
        "v_add_f32 v104, v104, v120\n"
        "v_add_f32 v105, v105, v121\n"
        "v_add_f32 v106, v106, v122\n"
        "v_add_f32 v107, v107, v123\n"
        "v_add_f32 v80, v80, v116\n"
        "v_add_f32 v81, v81, v117\n"
        "v_add_f32 v82, v82, v118\n"
        "v_add_f32 v83, v83, v119\n"
        "v_add_f32 v84, v84, v112\n"
        "v_add_f32 v85, v85, v113\n"
        "v_add_f32 v86, v86, v114\n"
        "v_add_f32 v87, v87, v115\n"
        "v_add_f32 v96, v96, v116\n"
        "v_add_f32 v97, v97, v117\n"
        "v_add_f32 v98, v98, v118\n"
        "v_add_f32 v99, v99, v119\n"
        "v_add_f32 v108, v108, v112\n"
        "v_add_f32 v109, v109, v113\n"
        "v_add_f32 v110, v110, v114\n"
        "v_add_f32 v111, v111, v115\n"
        "v_cvt_pk_bf16_f32 v112, v64, v65\n"
        "v_cvt_pk_bf16_f32 v113, v66, v67\n"
        "v_cvt_pk_bf16_f32 v114, v68, v69\n"
        "v_cvt_pk_bf16_f32 v115, v70, v71\n"
        "v_cvt_pk_bf16_f32 v64, v88, v89\n"
        "v_cvt_pk_bf16_f32 v65, v90, v91\n"
        "v_cvt_pk_bf16_f32 v66, v92, v93\n"
        "v_cvt_pk_bf16_f32 v67, v94, v95\n"
        "v_cvt_pk_bf16_f32 v68, v72, v73\n"
        "v_cvt_pk_bf16_f32 v69, v74, v75\n"
        "v_cvt_pk_bf16_f32 v70, v76, v77\n"
        "v_cvt_pk_bf16_f32 v71, v78, v79\n"
        "v_cvt_pk_bf16_f32 v72, v100, v101\n"
        "v_cvt_pk_bf16_f32 v73, v102, v103\n"
        "v_cvt_pk_bf16_f32 v74, v104, v105\n"
        "v_cvt_pk_bf16_f32 v75, v106, v107\n"
        "v_cvt_pk_bf16_f32 v76, v80, v81\n"
        "v_cvt_pk_bf16_f32 v77, v82, v83\n"
        "v_cvt_pk_bf16_f32 v78, v84, v85\n"
        "v_cvt_pk_bf16_f32 v79, v86, v87\n"
        "v_cvt_pk_bf16_f32 v80, v96, v97\n"
        "v_cvt_pk_bf16_f32 v81, v98, v99\n"
        "v_cvt_pk_bf16_f32 v82, v108, v109\n"
        "v_cvt_pk_bf16_f32 v83, v110, v111\n"
        "ds_write_b128 v244, v[112:115]\n"
        "ds_write_b128 v245, v[64:67]\n"
        "ds_write_b128 v244, v[68:71] offset:1024\n"
        "ds_write_b128 v245, v[72:75] offset:1024\n"
        "ds_write_b128 v244, v[76:79] offset:2048\n"
        "ds_write_b128 v245, v[80:83] offset:2048\n"
        "s_mov_b64 exec, 1\n"
        "ds_inc_u32 v239, v239 offset:0\n"
        "s_mov_b64 exec, -1\n"
        "s_setpc_b64 s[30:31]\n"
        "FOLDR_%=:\n"
        "ds_read_b64_tr_b16 v[84:85], v242 offset:0\n"
        "ds_read_b64_tr_b16 v[86:87], v243 offset:0\n"
        "ds_read_b64_tr_b16 v[92:93], v242 offset:6144\n"
        "ds_read_b64_tr_b16 v[94:95], v243 offset:6144\n"
        "ds_read_b64_tr_b16 v[80:81], v242 offset:12288\n"
        "ds_read_b64_tr_b16 v[82:83], v243 offset:12288\n"
        "ds_read_b64_tr_b16 v[76:77], v242 offset:8\n"
        "ds_read_b64_tr_b16 v[78:79], v243 offset:8\n"
        "ds_read_b64_tr_b16 v[68:69], v242 offset:6152\n"
        "ds_read_b64_tr_b16 v[70:71], v243 offset:6152\n"
        "ds_read_b64_tr_b16 v[72:73], v242 offset:12296\n"
        "ds_read_b64_tr_b16 v[74:75], v243 offset:12296\n"
        "v_mov_b32 v64, 0x3f803f80\n"
        "v_mov_b32 v65, 0x3f803f80\n"
        "v_mov_b32 v66, 0x3f803f80\n"
        "v_mov_b32 v67, 0x3f803f80\n"
        "ds_read_b64_tr_b16 v[88:89], v242 offset:64\n"
        "ds_read_b64_tr_b16 v[90:91], v243 offset:64\n"
        "s_waitcnt lgkmcnt(12)\n"
        "v_mfma_f32_16x16x32_bf16 v[104:107], v[84:87], v[64:67], 0\n"
        "s_waitcnt lgkmcnt(10)\n"
        "v_mfma_f32_16x16x32_bf16 v[104:107], v[92:95], v[64:67], v[104:107]\n"
        "s_waitcnt lgkmcnt(6)\n"
        "ds_read_b64_tr_b16 v[96:97], v242 offset:6208\n"
        "ds_read_b64_tr_b16 v[98:99], v243 offset:6208\n"
        "v_mfma_f32_16x16x32_bf16 v[104:107], v[80:83], v[64:67], v[104:107]\n"
        "ds_read_b64_tr_b16 v[92:93], v242 offset:12352\n"
        "ds_read_b64_tr_b16 v[94:95], v243 offset:12352\n"
        "v_mfma_f32_16x16x32_bf16 v[100:103], v[76:79], v[64:67], 0\n"
        "s_waitcnt lgkmcnt(6)\n"
        "ds_read_b64_tr_b16 v[80:81], v242 offset:72\n"
        "ds_read_b64_tr_b16 v[82:83], v243 offset:72\n"
        "v_mfma_f32_16x16x32_bf16 v[100:103], v[68:71], v[64:67], v[100:103]\n"
        "ds_read_b64_tr_b16 v[76:77], v242 offset:6216\n"
        "ds_read_b64_tr_b16 v[78:79], v243 offset:6216\n"
        "v_cvt_pk_bf16_f32 v68, v104, v105\n"
        "v_cvt_pk_bf16_f32 v69, v106, v107\n"
        "v_mfma_f32_16x16x32_bf16 v[100:103], v[72:75], v[64:67], v[100:103]\n"
        "s_waitcnt lgkmcnt(8)\n"
        "ds_read_b64_tr_b16 v[84:85], v242 offset:12360\n"
        "ds_read_b64_tr_b16 v[86:87], v243 offset:12360\n"
        "v_mfma_f32_16x16x32_bf16 v[104:107], v[88:91], v[64:67], 0\n"
        "ds_read_b64_tr_b16 v[72:73], v242 offset:128\n"
        "ds_read_b64_tr_b16 v[74:75], v243 offset:128\n"
        "s_waitcnt lgkmcnt(10)\n"
        "v_mfma_f32_16x16x32_bf16 v[104:107], v[96:99], v[64:67], v[104:107]\n"
        "ds_read_b64_tr_b16 v[88:89], v242 offset:6272\n"
        "ds_read_b64_tr_b16 v[90:91], v243 offset:6272\n"
        "v_cvt_pk_bf16_f32 v70, v100, v101\n"
        "v_cvt_pk_bf16_f32 v71, v102, v103\n"
        "s_waitcnt lgkmcnt(9)\n"
        "v_mfma_f32_16x16x32_bf16 v[104:107], v[92:95], v[64:67], v[104:107]\n"
        "s_waitcnt lgkmcnt(8)\n"
        "ds_read_b64_tr_b16 v[100:101], v242 offset:12416\n"
        "ds_read_b64_tr_b16 v[102:103], v243 offset:12416\n"
        "v_mfma_f32_16x16x32_bf16 v[108:111], v[80:83], v[64:67], 0\n"
        "s_waitcnt lgkmcnt(8)\n"
        "ds_read_b64_tr_b16 v[92:93], v242 offset:136\n"
        "ds_read_b64_tr_b16 v[94:95], v243 offset:136\n"
        "v_mfma_f32_16x16x32_bf16 v[108:111], v[76:79], v[64:67], v[108:111]\n"
        "ds_read_b64_tr_b16 v[96:97], v242 offset:6280\n"
        "ds_read_b64_tr_b16 v[98:99], v243 offset:6280\n"
        "v_cvt_pk_bf16_f32 v80, v104, v105\n"
        "v_cvt_pk_bf16_f32 v81, v106, v107\n"
        "s_waitcnt lgkmcnt(8)\n"
        "v_mfma_f32_16x16x32_bf16 v[108:111], v[84:87], v[64:67], v[108:111]\n"
        "ds_read_b64_tr_b16 v[76:77], v242 offset:12424\n"
        "ds_read_b64_tr_b16 v[78:79], v243 offset:12424\n"
        "v_mfma_f32_16x16x32_bf16 v[116:119], v[72:75], v[64:67], 0\n"
        "s_waitcnt lgkmcnt(8)\n"
        "ds_read_b128 v[72:75], v240\n"
        "v_mfma_f32_16x16x32_bf16 v[116:119], v[88:91], v[64:67], v[116:119]\n"
        "ds_read_b128 v[104:107], v241\n"
        "s_waitcnt lgkmcnt(8)\n"
        "v_mfma_f32_16x16x32_bf16 v[116:119], v[100:103], v[64:67], v[116:119]\n"
        "ds_read_b128 v[84:87], v240 offset:6144\n"
        "v_cvt_pk_bf16_f32 v82, v108, v109\n"
        "v_cvt_pk_bf16_f32 v83, v110, v111\n"
        "s_waitcnt lgkmcnt(7)\n"
        "v_mfma_f32_16x16x32_bf16 v[112:115], v[92:95], v[64:67], 0\n"
        "s_waitcnt lgkmcnt(5)\n"
        "ds_read_b128 v[92:95], v241 offset:6144\n"
        "v_mfma_f32_16x16x32_bf16 v[112:115], v[96:99], v[64:67], v[112:115]\n"
        "ds_read_b128 v[100:103], v240 offset:12288\n"
        "v_cvt_pk_bf16_f32 v88, v116, v117\n"
        "v_cvt_pk_bf16_f32 v89, v118, v119\n"
        "s_waitcnt lgkmcnt(5)\n"
        "v_mfma_f32_16x16x32_bf16 v[112:115], v[76:79], v[64:67], v[112:115]\n"
        "s_waitcnt lgkmcnt(4)\n"
        "ds_read_b128 v[116:119], v241 offset:12288\n"
        "v_mfma_f32_16x16x32_bf16 v[64:67], v[68:71], v[72:75], 0\n"
        "s_waitcnt lgkmcnt(4)\n"
        "ds_read_b128 v[120:123], v240 offset:64\n"
        "v_mfma_f32_16x16x32_bf16 v[72:75], v[68:71], v[104:107], 0\n"
        "ds_read_b128 v[108:111], v241 offset:64\n"
        "s_waitcnt lgkmcnt(5)\n"
        "v_mfma_f32_16x16x32_bf16 v[76:79], v[68:71], v[84:87], 0\n"
        "ds_read_b128 v[104:107], v240 offset:6208\n"
        "v_cvt_pk_bf16_f32 v90, v112, v113\n"
        "v_cvt_pk_bf16_f32 v91, v114, v115\n"
        "s_waitcnt lgkmcnt(5)\n"
        "v_mfma_f32_16x16x32_bf16 v[84:87], v[68:71], v[92:95], 0\n"
        "ds_read_b128 v[124:127], v241 offset:6208\n"
        "s_waitcnt lgkmcnt(5)\n"
        "v_mfma_f32_16x16x32_bf16 v[92:95], v[68:71], v[100:103], 0\n"
        "ds_read_b128 v[112:115], v240 offset:12352\n"
        "s_waitcnt lgkmcnt(5)\n"
        "v_mfma_f32_16x16x32_bf16 v[96:99], v[68:71], v[116:119], 0\n"
        "ds_read_b128 v[100:103], v241 offset:12352\n"
        "s_waitcnt lgkmcnt(5)\n"
        "v_mfma_f32_16x16x32_bf16 v[64:67], v[80:83], v[120:123], v[64:67]\n"
        "ds_read_b128 v[68:71], v240 offset:128\n"
        "s_waitcnt lgkmcnt(5)\n"
        "v_mfma_f32_16x16x32_bf16 v[72:75], v[80:83], v[108:111], v[72:75]\n"
        "ds_read_b128 v[116:119], v241 offset:128\n"
        "s_waitcnt lgkmcnt(5)\n"
        "v_mfma_f32_16x16x32_bf16 v[76:79], v[80:83], v[104:107], v[76:79]\n"
        "ds_read_b128 v[108:111], v240 offset:6272\n"
        "s_waitcnt lgkmcnt(5)\n"
        "v_mfma_f32_16x16x32_bf16 v[84:87], v[80:83], v[124:127], v[84:87]\n"
        "ds_read_b128 v[104:107], v241 offset:6272\n"
        "s_waitcnt lgkmcnt(5)\n"
        "v_mfma_f32_16x16x32_bf16 v[92:95], v[80:83], v[112:115], v[92:95]\n"
        "ds_read_b128 v[120:123], v240 offset:12416\n"
        "s_waitcnt lgkmcnt(5)\n"
        "v_mfma_f32_16x16x32_bf16 v[96:99], v[80:83], v[100:103], v[96:99]\n"
        "ds_read_b128 v[112:115], v241 offset:12416\n"
        "s_waitcnt lgkmcnt(5)\n"
        "v_mfma_f32_16x16x32_bf16 v[64:67], v[88:91], v[68:71], v[64:67]\n"
        "s_waitcnt lgkmcnt(4)\n"
        "v_mfma_f32_16x16x32_bf16 v[72:75], v[88:91], v[116:119], v[72:75]\n"
        "v_mov_b32 v239, s35\n"
        "s_waitcnt lgkmcnt(3)\n"
        "v_mfma_f32_16x16x32_bf16 v[76:79], v[88:91], v[108:111], v[76:79]\n"
        "s_waitcnt vmcnt(6)\n"
        "s_mov_b64 exec, 1\n"
        "ds_inc_u32 v239, v239 offset:0\n"
        "s_mov_b64 exec, -1\n"
        "s_waitcnt lgkmcnt(3)\n"
        "v_mfma_f32_16x16x32_bf16 v[84:87], v[88:91], v[104:107], v[84:87]\n"
        "v_add_f32 v238, v64, v72\n"
        "s_waitcnt lgkmcnt(2)\n"
        "v_mfma_f32_16x16x32_bf16 v[92:95], v[88:91], v[120:123], v[92:95]\n"
        "v_add_f32 v238, v238, v76\n"
        "s_waitcnt lgkmcnt(1)\n"
        "v_mfma_f32_16x16x32_bf16 v[96:99], v[88:91], v[112:115], v[96:99]\n"
        "v_mov_b32 v239, s37\n"
        "s_nop 0\n"
        "v_add_f32 v238, v238, v84\n"
        "s_nop 1\n"
        "v_add_f32 v238, v238, v92\n"
        "s_nop 1\n"
        "v_add_f32 v238, v238, v96\n"
        "s_nop 1\n"
        "v_add_f32_dpp v238, v238, v238 row_ror:8 row_mask:0xf bank_mask:0xf\n"
        "s_nop 1\n"
        "v_add_f32_dpp v238, v238, v238 row_ror:4 row_mask:0xf bank_mask:0xf\n"
        "s_nop 1\n"
        "v_add_f32_dpp v238, v238, v238 row_ror:2 row_mask:0xf bank_mask:0xf\n"
        "s_nop 1\n"
        "v_add_f32_dpp v238, v238, v238 row_ror:1 row_mask:0xf bank_mask:0xf\n"
        "v_mul_f32 v238, 0xbc2aaaab, v238\n"
        "v_mul_f32 v238, s34, v238\n"
        "v_fma_f32 v64, v64, s34, v238\n"
        "v_fma_f32 v72, v72, s34, v238\n"
        "v_fma_f32 v76, v76, s34, v238\n"
        "v_fma_f32 v84, v84, s34, v238\n"
        "v_fma_f32 v92, v92, s34, v238\n"
        "v_fma_f32 v96, v96, s34, v238\n"
        "ds_write_b32 v244, v64\n"
        "ds_write_b32 v244, v72 offset:16\n"
        "ds_write_b32 v244, v76 offset:128\n"
        "ds_write_b32 v244, v84 offset:144\n"
        "ds_write_b32 v244, v92 offset:256\n"
        "ds_write_b32 v244, v96 offset:272\n"
        "s_mov_b64 exec, 1\n"
        "ds_inc_u32 v239, v239 offset:0\n"
        "s_mov_b64 exec, -1\n"
        "s_setpc_b64 s[30:31]\n"
        "FOLDW_%=:\n"
        "ds_read_b64_tr_b16 v[64:65], v242 offset:0\n"
        "ds_read_b64_tr_b16 v[66:67], v243 offset:0\n"
        "ds_read_b128 v[172:175], v240\n"
        "ds_read_b64_tr_b16 v[180:181], v242 offset:6144\n"
        "ds_read_b64_tr_b16 v[182:183], v243 offset:6144\n"
        "ds_read_b128 v[120:123], v240 offset:64\n"
        "ds_read_b64_tr_b16 v[184:185], v242 offset:12288\n"
        "ds_read_b64_tr_b16 v[186:187], v243 offset:12288\n"
        "ds_read_b128 v[168:171], v240 offset:128\n"
        "ds_read_b128 v[160:163], v241\n"
        "ds_read_b128 v[148:151], v241 offset:64\n"
        "ds_read_b128 v[132:135], v241 offset:128\n"
        "ds_read_b128 v[124:127], v240 offset:6144\n"
        "ds_read_b128 v[104:107], v240 offset:6208\n"
        "ds_read_b128 v[92:95], v240 offset:6272\n"
        "ds_read_b128 v[96:99], v241 offset:6144\n"
        "ds_read_b128 v[88:91], v241 offset:6208\n"
        "ds_read_b128 v[84:87], v241 offset:6272\n"
        "s_waitcnt lgkmcnt(15)\n"
        "v_mfma_f32_16x16x32_bf16 v[136:139], v[172:175], v[64:67], 0\n"
        "ds_read_b128 v[100:103], v240 offset:12288\n"
        "s_waitcnt lgkmcnt(12)\n"
        "v_mfma_f32_16x16x32_bf16 v[136:139], v[120:123], v[180:183], v[136:139]\n"
        "s_waitcnt lgkmcnt(10)\n"
        "ds_read_b128 v[108:111], v240 offset:12352\n"
        "v_mfma_f32_16x16x32_bf16 v[136:139], v[168:171], v[184:187], v[136:139]\n"
        "s_waitcnt lgkmcnt(7)\n"
        "ds_read_b128 v[68:71], v240 offset:12416\n"
        "v_mfma_f32_16x16x32_bf16 v[140:143], v[160:163], v[64:67], 0\n"
        "ds_read_b128 v[72:75], v241 offset:12288\n"
        "v_mfma_f32_16x16x32_bf16 v[140:143], v[148:151], v[180:183], v[140:143]\n"
        "ds_read_b128 v[76:79], v241 offset:12352\n"
        "v_mfma_f32_16x16x32_bf16 v[140:143], v[132:135], v[184:187], v[140:143]\n"
        "ds_read_b128 v[80:83], v241 offset:12416\n"
        "v_mfma_f32_16x16x32_bf16 v[144:147], v[124:127], v[64:67], 0\n"
        "s_waitcnt lgkmcnt(6)\n"
        "ds_read_b64_tr_b16 v[112:113], v242 offset:8\n"
        "ds_read_b64_tr_b16 v[114:115], v243 offset:8\n"
        "v_mfma_f32_16x16x32_bf16 v[144:147], v[104:107], v[180:183], v[144:147]\n"
        "ds_read_b64_tr_b16 v[116:117], v242 offset:6152\n"
        "ds_read_b64_tr_b16 v[118:119], v243 offset:6152\n"
        "v_add_f32 v164, v136, v140\n"
        "v_add_f32 v165, v137, v141\n"
        "v_add_f32 v166, v138, v142\n"
        "v_add_f32 v167, v139, v143\n"
        "v_mfma_f32_16x16x32_bf16 v[144:147], v[92:95], v[184:187], v[144:147]\n"
        "ds_read_b64_tr_b16 v[128:129], v242 offset:12296\n"
        "ds_read_b64_tr_b16 v[130:131], v243 offset:12296\n"
        "v_mfma_f32_16x16x32_bf16 v[152:155], v[96:99], v[64:67], 0\n"
        "v_mfma_f32_16x16x32_bf16 v[152:155], v[88:91], v[180:183], v[152:155]\n"
        "v_mfma_f32_16x16x32_bf16 v[152:155], v[84:87], v[184:187], v[152:155]\n"
        "s_waitcnt lgkmcnt(6)\n"
        "v_mfma_f32_16x16x32_bf16 v[156:159], v[100:103], v[64:67], 0\n"
        "v_mfma_f32_16x16x32_bf16 v[156:159], v[108:111], v[180:183], v[156:159]\n"
        "v_add_f32 v164, v164, v144\n"
        "v_add_f32 v165, v165, v145\n"
        "v_add_f32 v166, v166, v146\n"
        "v_add_f32 v167, v167, v147\n"
        "v_mfma_f32_16x16x32_bf16 v[156:159], v[68:71], v[184:187], v[156:159]\n"
        "v_add_f32 v164, v164, v152\n"
        "v_add_f32 v165, v165, v153\n"
        "v_add_f32 v166, v166, v154\n"
        "v_add_f32 v167, v167, v155\n"
        "v_mfma_f32_16x16x32_bf16 v[176:179], v[72:75], v[64:67], 0\n"
        "v_mfma_f32_16x16x32_bf16 v[176:179], v[76:79], v[180:183], v[176:179]\n"
        "v_mfma_f32_16x16x32_bf16 v[176:179], v[80:83], v[184:187], v[176:179]\n"
        "s_waitcnt lgkmcnt(0)\n"
        "v_mov_b32 v239, s35\n"
        "v_add_f32 v164, v164, v156\n"
        "v_add_f32 v165, v165, v157\n"
        "v_add_f32 v166, v166, v158\n"
        "v_add_f32 v167, v167, v159\n"
        "s_waitcnt vmcnt(6)\n"
        "s_mov_b64 exec, 1\n"
        "ds_inc_u32 v239, v239 offset:0\n"
        "s_mov_b64 exec, -1\n"
        "v_add_f32 v164, v164, v176\n"
        "v_add_f32 v165, v165, v177\n"
        "v_mfma_f32_16x16x32_bf16 v[64:67], v[172:175], v[112:115], 0\n"
        "v_add_f32 v166, v166, v178\n"
        "v_add_f32 v167, v167, v179\n"
        "v_add_f32 v164, v164, v165\n"
        "v_add_f32 v166, v166, v167\n"
        "v_add_f32 v164, v164, v166\n"
        "v_mov_b32 v165, v164\n"
        "v_mfma_f32_16x16x32_bf16 v[64:67], v[120:123], v[116:119], v[64:67]\n"
        "v_mfma_f32_16x16x32_bf16 v[64:67], v[168:171], v[128:131], v[64:67]\n"
        "v_permlane32_swap_b32 v164, v165\n"
        "v_add_f32 v164, v164, v165\n"
        "v_mov_b32 v165, v164\n"
        "v_mfma_f32_16x16x32_bf16 v[120:123], v[160:163], v[112:115], 0\n"
        "global_load_dwordx4 v[40:43], v57, s[22:23] offset:-3072\n"
        "v_permlane16_swap_b32 v164, v165\n"
        "v_add_f32 v164, v164, v165\n"
        "v_mul_f32 v164, 0xbc2aaaab, v164\n"
        "v_mul_f32 v164, s34, v164\n"
        "v_fma_f32 v136, v136, s34, v164\n"
        "v_fma_f32 v137, v137, s34, v164\n"
        "v_fma_f32 v138, v138, s34, v164\n"
        "v_fma_f32 v139, v139, s34, v164\n"
        "v_fma_f32 v140, v140, s34, v164\n"
        "v_fma_f32 v141, v141, s34, v164\n"
        "v_mfma_f32_16x16x32_bf16 v[120:123], v[148:151], v[116:119], v[120:123]\n"
        "v_fma_f32 v142, v142, s34, v164\n"
        "v_fma_f32 v143, v143, s34, v164\n"
        "v_fma_f32 v144, v144, s34, v164\n"
        "v_fma_f32 v145, v145, s34, v164\n"
        "v_fma_f32 v146, v146, s34, v164\n"
        "v_fma_f32 v147, v147, s34, v164\n"
        "v_fma_f32 v152, v152, s34, v164\n"
        "v_fma_f32 v153, v153, s34, v164\n"
        "v_fma_f32 v154, v154, s34, v164\n"
        "v_fma_f32 v155, v155, s34, v164\n"
        "v_fma_f32 v156, v156, s34, v164\n"
        "v_fma_f32 v157, v157, s34, v164\n"
        "v_fma_f32 v158, v158, s34, v164\n"
        "v_fma_f32 v159, v159, s34, v164\n"
        "v_mfma_f32_16x16x32_bf16 v[120:123], v[132:135], v[128:131], v[120:123]\n"
        "global_load_dwordx4 v[44:47], v57, s[22:23] offset:-2048\n"
        "v_fma_f32 v176, v176, s34, v164\n"
        "v_fma_f32 v177, v177, s34, v164\n"
        "v_fma_f32 v178, v178, s34, v164\n"
        "v_fma_f32 v179, v179, s34, v164\n"
        "v_cvt_pk_bf16_f32 v132, v136, v137\n"
        "v_cvt_pk_bf16_f32 v133, v138, v139\n"
        "v_cvt_pk_bf16_f32 v134, v140, v141\n"
        "v_add_f32 v148, v64, v120\n"
        "v_add_f32 v149, v65, v121\n"
        "v_add_f32 v150, v66, v122\n"
        "v_add_f32 v151, v67, v123\n"
        "v_cvt_pk_bf16_f32 v135, v142, v143\n"
        "v_mfma_f32_16x16x32_bf16 v[140:143], v[124:127], v[112:115], 0\n"
        "v_cvt_pk_bf16_f32 v136, v144, v145\n"
        "v_cvt_pk_bf16_f32 v137, v146, v147\n"
        "v_cvt_pk_bf16_f32 v138, v152, v153\n"
        "v_cvt_pk_bf16_f32 v139, v154, v155\n"
        "v_cvt_pk_bf16_f32 v124, v156, v157\n"
        "v_cvt_pk_bf16_f32 v125, v158, v159\n"
        "v_cvt_pk_bf16_f32 v126, v176, v177\n"
        "v_cvt_pk_bf16_f32 v127, v178, v179\n"
        "v_mfma_f32_16x16x32_bf16 v[140:143], v[104:107], v[116:119], v[140:143]\n"
        "global_load_dwordx4 v[0:3], v57, s[22:23] offset:-1024\n"
        "v_mfma_f32_16x16x32_bf16 v[140:143], v[92:95], v[128:131], v[140:143]\n"
        "v_mfma_f32_16x16x32_bf16 v[104:107], v[96:99], v[112:115], 0\n"
        "global_load_dwordx4 v[4:7], v57, s[22:23] offset:0\n"
        "v_mfma_f32_16x16x32_bf16 v[104:107], v[88:91], v[116:119], v[104:107]\n"
        "v_mfma_f32_16x16x32_bf16 v[104:107], v[84:87], v[128:131], v[104:107]\n"
        "global_load_dwordx4 v[8:11], v57, s[22:23] offset:1024\n"
        "v_mfma_f32_16x16x32_bf16 v[84:87], v[100:103], v[112:115], 0\n"
        "v_mfma_f32_16x16x32_bf16 v[84:87], v[108:111], v[116:119], v[84:87]\n"
        "global_load_dwordx4 v[12:15], v57, s[22:23] offset:2048\n"
        "v_add_f32 v148, v148, v140\n"
        "v_add_f32 v149, v149, v141\n"
        "v_add_f32 v150, v150, v142\n"
        "v_add_f32 v151, v151, v143\n"
        "v_add_f32 v148, v148, v104\n"
        "v_add_f32 v149, v149, v105\n"
        "v_add_f32 v150, v150, v106\n"
        "v_add_f32 v151, v151, v107\n"
        "v_mfma_f32_16x16x32_bf16 v[84:87], v[68:71], v[128:131], v[84:87]\n"
        "v_mfma_f32_16x16x32_bf16 v[88:91], v[72:75], v[112:115], 0\n"
        "global_load_dwordx4 v[16:19], v57, s[22:23] offset:3072\n"
        "v_mfma_f32_16x16x32_bf16 v[88:91], v[76:79], v[116:119], v[88:91]\n"
        "v_mfma_f32_16x16x32_bf16 v[88:91], v[80:83], v[128:131], v[88:91]\n"
        "v_mov_b32 v239, s37\n"
        "s_nop 2\n"
        "v_add_f32 v148, v148, v84\n"
        "v_add_f32 v149, v149, v85\n"
        "v_add_f32 v150, v150, v86\n"
        "v_add_f32 v151, v151, v87\n"
        "v_add_f32 v148, v148, v88\n"
        "v_add_f32 v149, v149, v89\n"
        "v_add_f32 v150, v150, v90\n"
        "v_add_f32 v151, v151, v91\n"
        "v_add_f32 v148, v148, v149\n"
        "v_add_f32 v150, v150, v151\n"
        "v_add_f32 v148, v148, v150\n"
        "v_mov_b32 v149, v148\n"
        "s_nop 1\n"
        "v_permlane32_swap_b32 v148, v149\n"
        "v_add_f32 v148, v148, v149\n"
        "v_mov_b32 v149, v148\n"
        "s_nop 1\n"
        "v_permlane16_swap_b32 v148, v149\n"
        "v_add_f32 v148, v148, v149\n"
        "v_mul_f32 v148, 0xbc2aaaab, v148\n"
        "v_mul_f32 v148, s34, v148\n"
        "v_fma_f32 v64, v64, s34, v148\n"
        "v_fma_f32 v65, v65, s34, v148\n"
        "v_fma_f32 v66, v66, s34, v148\n"
        "v_fma_f32 v67, v67, s34, v148\n"
        "v_fma_f32 v120, v120, s34, v148\n"
        "v_fma_f32 v121, v121, s34, v148\n"
        "v_fma_f32 v122, v122, s34, v148\n"
        "v_fma_f32 v123, v123, s34, v148\n"
        "v_fma_f32 v140, v140, s34, v148\n"
        "v_fma_f32 v141, v141, s34, v148\n"
        "v_fma_f32 v142, v142, s34, v148\n"
        "v_fma_f32 v143, v143, s34, v148\n"
        "v_fma_f32 v104, v104, s34, v148\n"
        "v_fma_f32 v105, v105, s34, v148\n"
        "v_fma_f32 v106, v106, s34, v148\n"
        "v_fma_f32 v107, v107, s34, v148\n"
        "v_fma_f32 v84, v84, s34, v148\n"
        "v_fma_f32 v85, v85, s34, v148\n"
        "v_fma_f32 v86, v86, s34, v148\n"
        "v_fma_f32 v87, v87, s34, v148\n"
        "v_fma_f32 v88, v88, s34, v148\n"
        "v_fma_f32 v89, v89, s34, v148\n"
        "v_fma_f32 v90, v90, s34, v148\n"
        "v_fma_f32 v91, v91, s34, v148\n"
        "v_cvt_pk_bf16_f32 v68, v64, v65\n"
        "v_cvt_pk_bf16_f32 v69, v66, v67\n"
        "v_cvt_pk_bf16_f32 v70, v120, v121\n"
        "v_cvt_pk_bf16_f32 v71, v122, v123\n"
        "v_cvt_pk_bf16_f32 v64, v140, v141\n"
        "v_cvt_pk_bf16_f32 v65, v142, v143\n"
        "v_cvt_pk_bf16_f32 v66, v104, v105\n"
        "v_cvt_pk_bf16_f32 v67, v106, v107\n"
        "v_cvt_pk_bf16_f32 v72, v84, v85\n"
        "v_cvt_pk_bf16_f32 v73, v86, v87\n"
        "v_cvt_pk_bf16_f32 v74, v88, v89\n"
        "v_cvt_pk_bf16_f32 v75, v90, v91\n"
        "s_nop 1\n"
        "v_mov_b32 v238, s36\n"
        "L64_%=:\n"
        "ds_read_b32 v237, v238 offset:0\n"
        "s_waitcnt lgkmcnt(0)\n"
        "v_readfirstlane_b32 s40, v237\n"
        "s_nop 3\n"
        "s_cmp_ge_i32 s40, 4\n"
        "s_cbranch_scc1 L65_%=\n"
        "s_nop 0\n"
        "s_sub_u32 s41, s41, 1\n"
        "s_cbranch_scc0 L64_%=\n"
        "s_endpgm\n"
        "L65_%=:\n"
        "ds_write_b128 v244, v[132:135]\n"
        "ds_write_b128 v245, v[68:71]\n"
        "ds_write_b128 v244, v[136:139] offset:1024\n"
        "ds_write_b128 v245, v[64:67] offset:1024\n"
        "ds_write_b128 v244, v[124:127] offset:2048\n"
        "ds_write_b128 v245, v[72:75] offset:2048\n"
        "s_mov_b64 exec, 1\n"
        "ds_inc_u32 v239, v239 offset:0\n"
        "s_mov_b64 exec, -1\n"
        "s_setpc_b64 s[30:31]\n"
        "PPRO_%=:\n"
        "s_add_u32 s64, s64, 128\n"
        "s_cmp_lg_u32 s6, 0\n"
        "s_cbranch_scc1 L82_%=\n"
        "s_mov_b32 exec_hi, 0\n"
        "v_lshlrev_b32 v27, 2, v56\n"
        "v_add_u32 v27, 21600, v27\n"
        "v_mov_b32 v28, 0\n"
        "ds_write_b32 v27, v28\n"
        "s_mov_b64 exec, 0x1fffff\n"
        "s_bfe_u32 s7, s2, 0x50003\n"
        "s_mul_i32 s7, s7, 2656\n"
        "v_lshlrev_b32 v27, 7, v56\n"
        "v_add_u32 v27, s7, v27\n"
        "global_load_dword v252, v27, s[4:5]\n"
        "s_mov_b64 exec, -1\n"
        "L82_%=:\n"
        "s_cmp_lt_u32 s6, 2\n"
        "s_cselect_b32 s65, 3, 1\n"
        "s_cselect_b32 s61, 384, 0\n"
        "s_cselect_b32 s62, 9, 1\n"
        "s_cselect_b32 s63, 3, 4\n"
        "s_cselect_b32 s26, 3, 1\n"
        "s_cselect_b32 s34, 9, 1\n"
        "s_cselect_b32 s70, 72, 1\n"
        "s_cselect_b32 s71, 36, 4\n"
        "s_cselect_b32 s72, 6, 8\n"
        "s_mul_i32 s70, s70, 192\n"
        "s_mul_i32 s71, s71, 192\n"
        "s_mul_i32 s72, s72, 192\n"
        "s_mul_i32 s65, s65, 192\n"
        "s_mul_i32 s62, s62, 3072\n"
        "s_mul_i32 s63, s63, 1536\n"
        "s_mul_i32 s26, s26, 18432\n"
        "s_mul_i32 s34, s34, 1536\n"
        "s_add_u32 s7, s62, s63\n"
        "s_lshl_b32 s29, s63, 1\n"
        "s_add_u32 s60, s7, s63\n"
        "s_bitcmp1_b32 s6, 0\n"
        "s_cselect_b32 s28, s7, 0\n"
        "s_cselect_b32 s29, s29, s62\n"
        "s_cselect_b32 s60, s60, s63\n"
        "s_add_u32 s28, s28, s61\n"
        "s_add_u32 s29, s29, s61\n"
        "s_add_u32 s60, s60, s61\n"
        "v_add_u32 v24, 0, v56\n"
        "v_mul_u32_u24 v21, 171, v24\n"
        "v_lshrrev_b32 v21, 11, v21\n"
        "v_mad_i32_i24 v24, v21, -12, v24\n"
        "v_bfe_u32 v58, v21, 2, 2\n"
        "v_xor_b32 v58, v24, v58\n"
        "v_and_b32 v27, 3, v21\n"
        "v_mul_u32_u24 v27, s70, v27\n"
        "v_lshl_add_u32 v32, v58, 4, v27\n"
        "v_bfe_u32 v27, v21, 2, 1\n"
        "v_mad_u32_u24 v32, v27, s71, v32\n"
        "v_lshrrev_b32 v27, 3, v21\n"
        "v_mad_u32_u24 v32, v27, s72, v32\n"
        "v_and_b32 v27, 7, v21\n"
        "v_mul_u32_u24 v27, s65, v27\n"
        "v_lshl_add_u32 v58, v58, 4, v27\n"
        "v_add_u32 v25, 64, v56\n"
        "v_mul_u32_u24 v22, 171, v25\n"
        "v_lshrrev_b32 v22, 11, v22\n"
        "v_mad_i32_i24 v25, v22, -12, v25\n"
        "v_bfe_u32 v59, v22, 2, 2\n"
        "v_xor_b32 v59, v25, v59\n"
        "v_and_b32 v27, 3, v22\n"
        "v_mul_u32_u24 v27, s70, v27\n"
        "v_lshl_add_u32 v33, v59, 4, v27\n"
        "v_bfe_u32 v27, v22, 2, 1\n"
        "v_mad_u32_u24 v33, v27, s71, v33\n"
        "v_lshrrev_b32 v27, 3, v22\n"
        "v_mad_u32_u24 v33, v27, s72, v33\n"
        "v_and_b32 v27, 7, v22\n"
        "v_mul_u32_u24 v27, s65, v27\n"
        "v_lshl_add_u32 v59, v59, 4, v27\n"
        "v_lshrrev_b32 v27, 3, v22\n"
        "v_mad_u32_u24 v59, v27, s34, v59\n"
        "v_add_u32 v26, 128, v56\n"
        "v_mul_u32_u24 v23, 171, v26\n"
        "v_lshrrev_b32 v23, 11, v23\n"
        "v_mad_i32_i24 v26, v23, -12, v26\n"
        "v_bfe_u32 v60, v23, 2, 2\n"
        "v_xor_b32 v60, v26, v60\n"
        "v_and_b32 v27, 3, v23\n"
        "v_mul_u32_u24 v27, s70, v27\n"
        "v_lshl_add_u32 v34, v60, 4, v27\n"
        "v_bfe_u32 v27, v23, 2, 1\n"
        "v_mad_u32_u24 v34, v27, s71, v34\n"
        "v_lshrrev_b32 v27, 3, v23\n"
        "v_mad_u32_u24 v34, v27, s72, v34\n"
        "v_and_b32 v27, 7, v23\n"
        "v_mul_u32_u24 v27, s65, v27\n"
        "v_lshl_add_u32 v60, v60, 4, v27\n"
        "v_add_u32 v60, s34, v60\n"
        "s_and_b32 s7, s2, 7\n"
        "s_lshl_b32 s7, s7, 5\n"
        "s_lshr_b32 s14, s2, 3\n"
        "s_add_u32 s7, s7, s14\n"
        "s_and_b32 s16, s7, 255\n"
        "s_sub_u32 s7, s7, s16\n"
        "s_mul_i32 s7, s7, 20\n"
        "s_add_u32 s7, s7, s16\n"
        "s_mov_b32 s14, 22\n"
        "s_mul_i32 s15, s7, 57457\n"
        "s_lshr_b32 s15, s15, 22\n"
        "s_mov_b32 s16, s7\n"
        "s_mul_i32 s15, s15, 588672\n"
        "s_mul_i32 s7, s16, 1344\n"
        "s_add_u32 s15, s15, s7\n"
        "s_mul_i32 s74, s6, 98112\n"
        "s_add_u32 s74, s74, s15\n"
        "s_mov_b32 s36, 96768\n"
        "s_mov_b32 s40, 599187\n"
        "s_cmp_eq_u32 s6, 3\n"
        "s_cselect_b32 s7, s16, s6\n"
        "s_cselect_b32 s36, 588672, s36\n"
        "s_cselect_b32 s74, 589824, s74\n"
        "s_cselect_b32 s37, 512, 1\n"
        "s_cselect_b32 s38, 256, 4\n"
        "s_cselect_b32 s39, 7, 1\n"
        "s_cselect_b32 s40, 57457, s40\n"
        "s_mul_i32 s39, s39, 192\n"
        "s_mov_b32 s35, 50135232\n"
        "v_mov_b32 v29, 2048\n"
        "v_bfe_u32 v27, v21, 2, 1\n"
        "v_and_b32 v21, 3, v21\n"
        "v_mul_u32_u24 v21, s37, v21\n"
        "v_mad_u32_u24 v21, v27, s38, v21\n"
        "v_add_u32 v21, s7, v21\n"
        "v_and_b32 v27, 3, v24\n"
        "v_mad_u32_u24 v29, v27, 48, v29\n"
        "v_lshrrev_b32 v27, 2, v24\n"
        "v_lshl_add_u32 v29, v27, 4, v29\n"
        "v_mul_u32_u24 v27, s40, v21\n"
        "v_mul_u32_u24 v21, s39, v21\n"
        "v_lshrrev_b32 v27, 22, v27\n"
        "v_mul_u32_u24 v27, s36, v27\n"
        "v_add3_u32 v29, v29, v21, v27\n"
        "v_lshrrev_b32 v27, 3, v22\n"
        "v_xor_b32 v25, v25, v27\n"
        "v_mul_lo_u32 v30, v27, s35\n"
        "v_add_u32 v30, 1024, v30\n"
        "v_bfe_u32 v27, v22, 2, 1\n"
        "v_and_b32 v22, 3, v22\n"
        "v_mul_u32_u24 v22, s37, v22\n"
        "v_mad_u32_u24 v22, v27, s38, v22\n"
        "v_add_u32 v22, s7, v22\n"
        "v_and_b32 v27, 3, v25\n"
        "v_mad_u32_u24 v30, v27, 48, v30\n"
        "v_lshrrev_b32 v27, 2, v25\n"
        "v_lshl_add_u32 v30, v27, 4, v30\n"
        "v_mul_u32_u24 v27, s40, v22\n"
        "v_mul_u32_u24 v22, s39, v22\n"
        "v_lshrrev_b32 v27, 22, v27\n"
        "v_mul_u32_u24 v27, s36, v27\n"
        "v_add3_u32 v30, v30, v22, v27\n"
        "v_mov_b32 v31, 50135232\n"
        "v_xor_b32 v26, 1, v26\n"
        "v_bfe_u32 v27, v23, 2, 1\n"
        "v_and_b32 v23, 3, v23\n"
        "v_mul_u32_u24 v23, s37, v23\n"
        "v_mad_u32_u24 v23, v27, s38, v23\n"
        "v_add_u32 v23, s7, v23\n"
        "v_and_b32 v27, 3, v26\n"
        "v_mad_u32_u24 v31, v27, 48, v31\n"
        "v_lshrrev_b32 v27, 2, v26\n"
        "v_lshl_add_u32 v31, v27, 4, v31\n"
        "v_mul_u32_u24 v27, s40, v23\n"
        "v_mul_u32_u24 v23, s39, v23\n"
        "v_lshrrev_b32 v27, 22, v27\n"
        "v_mul_u32_u24 v27, s36, v27\n"
        "v_add3_u32 v31, v31, v23, v27\n"
        "s_mul_i32 s17, s6, 6208\n"
        "s_add_u32 s17, s17, 110176\n"
        "s_mov_b32 s18, s6\n"
        "s_add_u32 s75, s74, 100270464\n"
        "v_mov_b32 v250, 21600\n"
        "s_mov_b32 s41, 0x400000\n"
        "s_waitcnt lgkmcnt(0)\n"
        "s_cmp_lg_u32 s6, 0\n"
        "s_cbranch_scc1 L83_%=\n"
        "s_bitcmp1_b32 s2, 7\n"
        "s_cselect_b64 s[8:9], s[50:51], s[46:47]\n"
        "s_cselect_b64 s[10:11], s[52:53], s[48:49]\n"
        "s_bfe_u32 s7, s2, 0x40003\n"
        "s_cmp_ge_u32 s7, 12\n"
        "s_cselect_b64 s[8:9], s[10:11], s[8:9]\n"
        "s_cselect_b32 s10, 12, 0\n"
        "s_sub_u32 s7, s7, s10\n"
        "s_mul_i32 s7, s7, 4608\n"
        "v_lshlrev_b32 v27, 7, v56\n"
        "v_add_u32 v27, s7, v27\n"
        "s_mov_b32 exec_hi, 0xf\n"
        "global_load_dword v252, v27, s[8:9]\n"
        "s_mov_b32 exec_hi, -1\n"
        "s_mov_b64 s[24:25], s[56:57]\n"
        "s_mov_b64 exec, 1\n"
        "v_mov_b32 v27, 0\n"
        "global_load_dword v253, v27, s[24:25] offset:256 sc1\n"
        "s_mov_b64 exec, -1\n"
        "L83_%=:\n"
        "s_cmp_lt_u32 s6, 2\n"
        "s_cselect_b64 s[24:25], s[46:47], s[48:49]\n"
        "s_and_b32 s25, s25, 0xffff\n"
        "s_sub_u32 s8, s44, 2048\n"
        "s_subb_u32 s9, s45, 0\n"
        "s_and_b32 s9, s9, 0xffff\n"
        "s_mov_b32 s10, 200542976\n"
        "s_mov_b32 s11, 0x20000\n"
        "s_mul_i32 s7, s6, 7168\n"
        "s_add_u32 s7, s7, 6144\n"
        "s_add_u32 s22, s4, s7\n"
        "s_addc_u32 s23, s5, 0\n"
        "s_sub_u32 s7, s6, 0\n"
        "s_and_b32 s7, s7, 3\n"
        "s_min_u32 s7, s7, 2\n"
        "s_lshl_b32 s7, s7, 10\n"
        "s_add_u32 s73, s7, 18528\n"
        "v_add_u32 v27, s7, v57\n"
        "s_barrier\n"
        "s_mov_b32 m0, s73\n"
        "s_nop 0\n"
        "global_load_lds_dwordx4 v27, s[4:5]\n"
        "global_load_dwordx4 v[240:243], v57, s[22:23] offset:-3072\n"
        "global_load_dwordx4 v[244:247], v57, s[22:23] offset:-2048\n"
        "s_add_u32 m0, s64, 0\n"
        "s_nop 0\n"
        "buffer_load_dwordx4 v58, s[24:27], s28 offen lds\n"
        "s_add_u32 m0, s64, 1024\n"
        "s_nop 0\n"
        "buffer_load_dwordx4 v59, s[24:27], s28 offen lds\n"
        "s_add_u32 m0, s64, 2048\n"
        "s_nop 0\n"
        "buffer_load_dwordx4 v60, s[24:27], s28 offen lds\n"
        "s_add_u32 m0, s64, 3072\n"
        "s_nop 0\n"
        "buffer_load_dwordx4 v58, s[24:27], s29 offen lds\n"
        "s_add_u32 m0, s64, 4096\n"
        "s_nop 0\n"
        "buffer_load_dwordx4 v59, s[24:27], s29 offen lds\n"
        "s_add_u32 m0, s64, 5120\n"
        "s_nop 0\n"
        "buffer_load_dwordx4 v60, s[24:27], s29 offen lds\n"
        "s_add_u32 m0, s64, 6144\n"
        "s_nop 0\n"
        "buffer_load_dwordx4 v58, s[24:27], s60 offen lds\n"
        "s_add_u32 m0, s64, 7168\n"
        "s_nop 0\n"
        "buffer_load_dwordx4 v59, s[24:27], s60 offen lds\n"
        "s_add_u32 m0, s64, 8192\n"
        "s_nop 0\n"
        "buffer_load_dwordx4 v60, s[24:27], s60 offen lds\n"
        "s_cmp_lt_u32 s6, 2\n"
        "s_cselect_b32 s62, 3, 16\n"
        "s_cselect_b32 s63, 12, 32\n"
        "s_cselect_b64 s[24:25], s[50:51], s[52:53]\n"
        "s_cselect_b32 s64, 0, 117088\n"
        "s_mul_i32 s62, s62, 192\n"
        "s_mul_i32 s63, s63, 192\n"
        "s_and_b32 s25, s25, 0xffff\n"
        "s_mul_i32 s7, s6, 9216\n"
        "s_add_u32 s64, s64, s7\n"
        "s_add_u32 s64, s64, 0\n"
        "s_add_u32 s7, s62, s63\n"
        "s_lshl_b32 s29, s63, 1\n"
        "s_add_u32 s60, s7, s63\n"
        "s_bitcmp1_b32 s6, 0\n"
        "s_cselect_b32 s28, s7, 0\n"
        "s_cselect_b32 s29, s29, s62\n"
        "s_cselect_b32 s60, s60, s63\n"
        "s_add_u32 s28, s28, s61\n"
        "s_add_u32 s29, s29, s61\n"
        "s_add_u32 s60, s60, s61\n"
        "s_add_u32 m0, s64, 0\n"
        "s_nop 0\n"
        "buffer_load_dwordx4 v32, s[24:27], s28 offen lds\n"
        "s_add_u32 m0, s64, 1024\n"
        "s_nop 0\n"
        "buffer_load_dwordx4 v33, s[24:27], s28 offen lds\n"
        "s_add_u32 m0, s64, 2048\n"
        "s_nop 0\n"
        "buffer_load_dwordx4 v34, s[24:27], s28 offen lds\n"
        "s_add_u32 m0, s64, 3072\n"
        "s_nop 0\n"
        "buffer_load_dwordx4 v32, s[24:27], s29 offen lds\n"
        "s_add_u32 m0, s64, 4096\n"
        "s_nop 0\n"
        "buffer_load_dwordx4 v33, s[24:27], s29 offen lds\n"
        "s_add_u32 m0, s64, 5120\n"
        "s_nop 0\n"
        "buffer_load_dwordx4 v34, s[24:27], s29 offen lds\n"
        "s_add_u32 m0, s64, 6144\n"
        "s_nop 0\n"
        "buffer_load_dwordx4 v32, s[24:27], s60 offen lds\n"
        "s_add_u32 m0, s64, 7168\n"
        "s_nop 0\n"
        "buffer_load_dwordx4 v33, s[24:27], s60 offen lds\n"
        "s_add_u32 m0, s64, 8192\n"
        "s_nop 0\n"
        "buffer_load_dwordx4 v34, s[24:27], s60 offen lds\n"
        "s_setprio 3\n"
        "s_waitcnt vmcnt(9)\n"
        "s_mov_b64 exec, 1\n"
        "ds_inc_u32 v250, v250 offset:20\n"
        "s_mov_b64 exec, -1\n"
        "L84_%=:\n"
        "ds_read_b32 v251, v250 offset:20\n"
        "s_waitcnt lgkmcnt(0)\n"
        "v_readfirstlane_b32 s40, v251\n"
        "s_nop 3\n"
        "s_cmp_ge_i32 s40, 4\n"
        "s_cbranch_scc1 L85_%=\n"
        "s_nop 0\n"
        "s_sub_u32 s41, s41, 1\n"
        "s_cbranch_scc0 L84_%=\n"
        "s_endpgm\n"
        "L85_%=:\n"
        "s_add_u32 m0, s17, 0\n"
        "s_nop 0\n"
        "buffer_load_dwordx4 v29, s[8:11], s74 offen lds\n"
        "buffer_load_dwordx4 v30, s[8:11], s74 offen offset:1024 lds\n"
        "buffer_load_dwordx4 v31, s[8:11], s74 offen offset:2048 lds\n"
        "s_add_u32 m0, s17, 3104\n"
        "s_nop 0\n"
        "buffer_load_dwordx4 v29, s[8:11], s75 offen lds\n"
        "buffer_load_dwordx4 v30, s[8:11], s75 offen offset:1024 lds\n"
        "buffer_load_dwordx4 v31, s[8:11], s75 offen offset:2048 lds\n"
        "s_cmp_eq_u32 s6, 0\n"
        "s_cbranch_scc1 L86_%=\n"
        "s_mov_b32 s34, 0x411cc471\n"
        "s_mov_b32 s35, 21632\n"
        "s_mov_b32 s36, 21624\n"
        "s_mov_b32 s37, 21628\n"
        "s_call_b64 s[30:31], FOLDW_%=\n"
        "s_branch L87_%=\n"
        "L86_%=:\n"
        "s_waitcnt vmcnt(6)\n"
        "s_mov_b64 exec, 1\n"
        "ds_inc_u32 v250, v250 offset:32\n"
        "s_mov_b64 exec, -1\n"
        "global_load_dwordx4 v[40:43], v57, s[22:23] offset:-3072\n"
        "global_load_dwordx4 v[44:47], v57, s[22:23] offset:-2048\n"
        "global_load_dwordx4 v[0:3], v57, s[22:23] offset:-1024\n"
        "global_load_dwordx4 v[4:7], v57, s[22:23] offset:0\n"
        "global_load_dwordx4 v[8:11], v57, s[22:23] offset:1024\n"
        "global_load_dwordx4 v[12:15], v57, s[22:23] offset:2048\n"
        "global_load_dwordx4 v[16:19], v57, s[22:23] offset:3072\n"
        "L87_%=:\n"
        "s_mov_b64 s[24:25], s[56:57]\n"
        "s_setprio 3\n"
        "s_mov_b32 s44, 0xf0000f\n"
        "s_mov_b32 s45, 0xf0000f00\n"
        "s_mov_b32 s46, 0xff000ff\n"
        "s_mov_b32 s47, 0xf00fff00\n"
        "s_mov_b32 s48, 0x22222222\n"
        "s_mov_b32 s49, 0x22222222\n"
        "s_mov_b32 s50, 0x44444444\n"
        "s_mov_b32 s51, 0x44444444\n"
        "s_mov_b32 s52, 0x88888888\n"
        "s_mov_b32 s53, 0x88888888\n"
        "s_mov_b64 vcc, s[44:45]\n"
        "s_mov_b32 s28, 0x3c2aaaab\n"
        "s_mov_b32 s29, 0x3a7ba882\n"
        "s_mov_b32 s30, 0\n"
        "s_mov_b32 s34, 0\n"
        "s_cmp_eq_u32 s18, 3\n"
        "s_cselect_b32 s31, 6, s18\n"
        "s_mov_b32 s36, s18\n"
        "s_mov_b32 s37, s18\n"
        "s_add_u32 s71, s17, 3104\n"
        "s_mov_b32 s68, 4\n"
        "s_mov_b32 s74, 16\n"
        "s_mov_b32 s72, -1\n"
        "s_mov_b32 s41, 0x400000\n"
        "L23_%=:\n"
        "ds_read_b32 v251, v250 offset:28\n"
        "s_waitcnt lgkmcnt(0)\n"
        "v_readfirstlane_b32 s40, v251\n"
        "s_nop 3\n"
        "s_cmp_ge_i32 s40, 6\n"
        "s_cbranch_scc1 L24_%=\n"
        "s_nop 0\n"
        "s_sub_u32 s41, s41, 1\n"
        "s_cbranch_scc0 L23_%=\n"
        "s_endpgm\n"
        "L24_%=:\n"
        "s_waitcnt vmcnt(0)\n"
        "v_readfirstlane_b32 s26, v253\n"
        "ds_read_b128 v[16:19], v16\n"
        "ds_read_b64 v[94:95], v14\n"
        "ds_read_b64 v[86:87], v14\n"
        "ds_read_b64 v[28:29], v14\n"
        "ds_read_b64 v[90:91], v14\n"
        "v_mov_b32 v102, 21600\n"
        "v_add_u32 v20, 21728, v13\n"
        "ds_read_b128 v[112:115], v20 offset:0\n"
        "ds_read_b128 v[116:119], v20 offset:1024\n"
        "ds_read_b128 v[120:123], v20 offset:2048\n"
        "ds_read_b128 v[124:127], v20 offset:3072\n"
        "ds_read_b128 v[128:131], v20 offset:4096\n"
        "ds_read_b128 v[132:135], v20 offset:5120\n"
        "ds_read_b128 v[136:139], v20 offset:6144\n"
        "ds_read_b128 v[140:143], v20 offset:7168\n"
        "ds_read_b128 v[144:147], v20 offset:8192\n"
        "ds_read_b128 v[148:151], v20 offset:9216\n"
        "ds_read_b128 v[152:155], v20 offset:10240\n"
        "ds_read_b128 v[156:159], v20 offset:11264\n"
        "ds_read_b128 v[160:163], v20 offset:12288\n"
        "ds_read_b128 v[164:167], v20 offset:13312\n"
        "ds_read_b128 v[168:171], v20 offset:14336\n"
        "ds_read_b128 v[172:175], v20 offset:15360\n"
        "ds_read_b128 v[176:179], v20 offset:16384\n"
        "ds_read_b128 v[180:183], v20 offset:17408\n"
        "ds_read_b128 v[184:187], v20 offset:18432\n"
        "ds_read_b128 v[188:191], v20 offset:19456\n"
        "ds_read_b128 v[192:195], v20 offset:20480\n"
        "ds_read_b128 v[196:199], v20 offset:21520\n"
        "ds_read_b128 v[200:203], v20 offset:22544\n"
        "ds_read_b128 v[204:207], v20 offset:23568\n"
        "ds_read_b128 v[208:211], v20 offset:24608\n"
        "ds_read_b128 v[212:215], v20 offset:25632\n"
        "ds_read_b128 v[216:219], v20 offset:26656\n"
        "ds_read_b128 v[220:223], v20 offset:27696\n"
        "ds_read_b128 v[224:227], v20 offset:28720\n"
        "ds_read_b128 v[228:231], v20 offset:29744\n"
        "ds_read_b128 v[232:235], v20 offset:30784\n"
        "ds_read_b128 v[236:239], v20 offset:31808\n"
        "ds_read_b128 v[240:243], v20 offset:32832\n"
        "ds_read_b128 v[244:247], v20 offset:33872\n"
        "ds_read_b128 v[248:251], v20 offset:34896\n"
        "ds_read_b128 v[252:255], v20 offset:35920\n"
        "ds_read_b128 v[60:63], v0 offset:0\n"
        "ds_read_b128 v[66:69], v0 offset:64\n"
        "ds_read_b128 v[72:75], v0 offset:128\n"
        "ds_read_b128 v[42:45], v0 offset:768\n"
        "ds_read_b128 v[48:51], v0 offset:832\n"
        "ds_read_b128 v[54:57], v0 offset:896\n"
        "s_add_u32 s37, s37, 4\n"
        "s_cmp_ge_u32 s37, 49\n"
        "s_cselect_b32 s69, 49, 0\n"
        "s_sub_u32 s37, s37, s69\n"
        "s_sub_u32 s69, s37, 4\n"
        "s_cmp_lt_u32 s69, 4\n"
        "s_subb_u32 s36, s36, -4\n"
        "s_cmp_ge_u32 s36, 6\n"
        "s_cselect_b32 s69, 6, 0\n"
        "s_addc_u32 s34, s34, 0\n"
        "s_sub_u32 s36, s36, s69\n"
        "s_cmp_ge_u32 s34, s74\n"
        "s_cbranch_scc0 L27_%=\n"
        "s_nop 1\n"
        "v_mov_b32 v22, 21600\n"
        "L25_%=:\n"
        "ds_read_b32 v23, v22 offset:64\n"
        "s_waitcnt lgkmcnt(0)\n"
        "v_readfirstlane_b32 s40, v23\n"
        "s_nop 3\n"
        "s_cmp_ge_i32 s40, 1\n"
        "s_cbranch_scc1 L26_%=\n"
        "s_sleep 1\n"
        "s_sub_u32 s41, s41, 1\n"
        "s_cbranch_scc0 L25_%=\n"
        "s_endpgm\n"
        "L26_%=:\n"
        "v_mbcnt_lo_u32_b32 v23, -1, 0\n"
        "v_lshl_add_u32 v22, v23, 2, v22\n"
        "ds_read_b32 v23, v22 offset:68\n"
        "s_mov_b32 s14, s40\n"
        "s_mov_b32 s74, -1\n"
        "s_waitcnt lgkmcnt(0)\n"
        "v_readlane_b32 s22, v23, 0\n"
        "v_readlane_b32 s23, v23, 1\n"
        "s_nop 3\n"
        "L27_%=:\n"
        "s_mul_i32 s38, s34, 256\n"
        "s_add_u32 s38, s38, s16\n"
        "s_cmp_ge_u32 s34, 20\n"
        "s_cselect_b32 s38, s22, s38\n"
        "s_cmp_ge_u32 s34, 21\n"
        "s_cselect_b32 s38, s23, s38\n"
        "s_mul_hi_u32 s70, s38, 58835169\n"
        "s_mul_i32 s38, s38, 1344\n"
        "s_mul_i32 s70, s70, 588672\n"
        "s_add_u32 s38, s38, s70\n"
        "s_mul_i32 s69, s36, 98304\n"
        "s_add_u32 s38, s38, s69\n"
        "s_mov_b32 s35, s36\n"
        "s_mul_i32 s69, s36, -12\n"
        "s_add_u32 s69, s69, 60\n"
        "s_lshl_b64 s[54:55], -1, s69\n"
        "s_lshl_b64 s[56:57], s[54:55], 24\n"
        "s_ashr_i64 s[60:61], s[54:55], 8\n"
        "s_mov_b32 s58, s61\n"
        "s_mov_b32 s59, s56\n"
        "v_cndmask_b32_e64 v15, v4, v7, s[56:57]\n"
        "v_cndmask_b32_e64 v100, v5, v8, s[58:59]\n"
        "v_cndmask_b32_e64 v103, v6, v9, s[60:61]\n"
        "s_cmp_eq_u32 s37, 3\n"
        "s_cbranch_scc0 L28_%=\n"
        "s_nop 1\n"
        "ds_read_b32 v15, v13 offset:19552\n"
        "ds_read_b32 v100, v13 offset:19556\n"
        "ds_read_b32 v103, v13 offset:19560\n"
        "s_mov_b32 s35, 6\n"
        "s_mov_b32 s38, 0\n"
        "s_mov_b32 s69, 1344\n"
        "s_waitcnt lgkmcnt(0)\n"
        "v_lshrrev_b32 v22, 28, v15\n"
        "v_add_u32 v22, s34, v22\n"
        "v_and_b32 v15, 0x0fffffff, v15\n"
        "v_mul_u32_u24 v23, 256, v22\n"
        "v_add_u32 v23, s16, v23\n"
        "v_cmpx_le_u32 vcc, 20, v22\n"
        "v_mov_b32 v23, s22\n"
        "s_mov_b64 exec, -1\n"
        "v_cmpx_le_u32 vcc, 21, v22\n"
        "v_mov_b32 v23, s23\n"
        "s_mov_b64 exec, -1\n"
        "v_mad_u32_u24 v15, v23, s69, v15\n"
        "v_mul_u32_u24 v23, 57457, v23\n"
        "v_lshrrev_b32 v23, 22, v23\n"
        "v_mul_u32_u24 v23, 588672, v23\n"
        "v_add_u32 v15, v15, v23\n"
        "v_lshrrev_b32 v22, 28, v100\n"
        "v_add_u32 v22, s34, v22\n"
        "v_and_b32 v100, 0x0fffffff, v100\n"
        "v_mul_u32_u24 v23, 256, v22\n"
        "v_add_u32 v23, s16, v23\n"
        "v_cmpx_le_u32 vcc, 20, v22\n"
        "v_mov_b32 v23, s22\n"
        "s_mov_b64 exec, -1\n"
        "v_cmpx_le_u32 vcc, 21, v22\n"
        "v_mov_b32 v23, s23\n"
        "s_mov_b64 exec, -1\n"
        "v_mad_u32_u24 v100, v23, s69, v100\n"
        "v_mul_u32_u24 v23, 57457, v23\n"
        "v_lshrrev_b32 v23, 22, v23\n"
        "v_mul_u32_u24 v23, 588672, v23\n"
        "v_add_u32 v100, v100, v23\n"
        "v_lshrrev_b32 v22, 28, v103\n"
        "v_add_u32 v22, s34, v22\n"
        "v_and_b32 v103, 0x0fffffff, v103\n"
        "v_mul_u32_u24 v23, 256, v22\n"
        "v_add_u32 v23, s16, v23\n"
        "v_cmpx_le_u32 vcc, 20, v22\n"
        "v_mov_b32 v23, s22\n"
        "s_mov_b64 exec, -1\n"
        "v_cmpx_le_u32 vcc, 21, v22\n"
        "v_mov_b32 v23, s23\n"
        "s_mov_b64 exec, -1\n"
        "v_mad_u32_u24 v103, v23, s69, v103\n"
        "v_mul_u32_u24 v23, 57457, v23\n"
        "v_lshrrev_b32 v23, 22, v23\n"
        "v_mul_u32_u24 v23, 588672, v23\n"
        "v_add_u32 v103, v103, v23\n"
        "s_nop 4\n"
        "s_mov_b64 vcc, s[44:45]\n"
        "L28_%=:\n"
        "s_cmp_lt_u32 s34, s14\n"
        "s_cselect_b32 s38, s38, 0x80000000\n"
        "s_add_u32 s39, s38, 100270464\n"
        "s_waitcnt lgkmcnt(0)\n"
        "s_mov_b64 exec, 1\n"
        "ds_inc_u32 v102, v10 offset:8\n"
        "s_mov_b64 exec, -1\n"
        "s_cmp_eq_u32 s18, 0\n"
        "s_cselect_b32 s73, -20, 8\n"
        "s_setprio 1\n"
        ".p2align 6\n"
        "s_nop 0\n"
        "s_nop 0\n"
        "s_nop 0\n"
        "s_nop 0\n"
        "PTOP_%=:\n"
        "s_waitcnt lgkmcnt(5)\n"
        "v_mfma_f32_16x16x32_bf16 v[34:37], v[112:115], v[60:63], 0\n"
        "s_mov_b32 m0, s17\n"
        "v_cndmask_b32_e64 v88, v82, v83, s[48:49]\n"
        "s_nop 1\n"
        "v_cndmask_b32_e64 v89, v76, v77, s[48:49]\n"
        "s_waitcnt lgkmcnt(3)\n"
        "v_mfma_f32_16x16x32_bf16 v[34:37], v[116:119], v[66:69], v[34:37]\n"
        "ds_read_b32 v101, v102 offset:8\n"
        "v_cndmask_b32_e64 v88, v88, v84, s[50:51]\n"
        "s_waitcnt lgkmcnt(2)\n"
        "v_mfma_f32_16x16x32_bf16 v[34:37], v[120:123], v[72:75], v[34:37]\n"
        "s_waitcnt lgkmcnt(1)\n"
        "buffer_load_dwordx4 v15, s[8:11], s38 offen lds\n"
        "v_mfma_f32_16x16x32_bf16 v[38:41], v[132:135], v[72:75], 0\n"
        "v_cndmask_b32_e64 v89, v89, v78, s[50:51]\n"
        "v_cndmask_b32_e64 v88, v88, v85, s[52:53]\n"
        "v_mfma_f32_16x16x32_bf16 v[38:41], v[128:131], v[66:69], v[38:41]\n"
        "s_setprio 3 \n"
        "v_cndmask_b32_e64 v89, v89, v79, s[52:53]\n"
        "v_mfma_f32_16x16x32_bf16 v[38:41], v[124:127], v[60:63], v[38:41]\n"
        "buffer_load_dwordx4 v100, s[8:11], s38 offen offset:1024 lds\n"
        "v_mfma_f32_16x16x32_bf16 v[78:81], v[136:139], v[60:63], 0\n"
        "s_waitcnt lgkmcnt(0)\n"
        "v_readfirstlane_b32 s40, v101\n"
        "v_cndmask_b32_dpp v88, v89, v88, vcc row_ror:4 row_mask:0xf bank_mask:0xf\n"
        "v_mfma_f32_16x16x32_bf16 v[78:81], v[140:143], v[66:69], v[78:81]\n"
        "s_cmp_lt_i32 s40, s68\n"
        "v_mfma_f32_16x16x32_bf16 v[78:81], v[144:147], v[72:75], v[78:81]\n"
        "s_cbranch_scc1 L3_%=\n"
        "L4_%=:\n"
        "ds_write_b128 v11, v[104:107]\n"
        "s_mov_b64 exec, s[46:47]\n"
        "ds_write_b32 v12, v88\n"
        "s_mov_b64 exec, -1\n"
        "v_mfma_f32_16x16x32_bf16 v[82:85], v[156:159], v[72:75], 0\n"
        "v_mfma_f32_16x16x32_bf16 v[82:85], v[152:155], v[66:69], v[82:85]\n"
        "v_mfma_f32_16x16x32_bf16 v[82:85], v[148:151], v[60:63], v[82:85]\n"
        "buffer_load_dwordx4 v103, s[8:11], s38 offen offset:2048 lds\n"
        "s_mov_b32 m0, s71\n"
        "ds_write_b128 v11, v[20:23] offset:832\n"
        "s_cmp_eq_u32 s72, s30\n"
        "v_mfma_f32_16x16x32_bf16 v[104:107], v[160:163], v[60:63], 0\n"
        "s_setprio 3\n"
        "s_cbranch_scc1 L5_%=\n"
        "s_setprio 1\n"
        "L5_%=:\n"
        "v_mfma_f32_16x16x32_bf16 v[104:107], v[164:167], v[66:69], v[104:107]\n"
        "v_mfma_f32_16x16x32_bf16 v[104:107], v[168:171], v[72:75], v[104:107]\n"
        "buffer_load_dwordx4 v15, s[8:11], s39 offen lds\n"
        "ds_write_b128 v11, v[108:111] offset:64\n"
        "v_mfma_f32_16x16x32_bf16 v[108:111], v[180:183], v[72:75], 0\n"
        "v_mfma_f32_16x16x32_bf16 v[108:111], v[176:179], v[66:69], v[108:111]\n"
        "v_mfma_f32_16x16x32_bf16 v[108:111], v[172:175], v[60:63], v[108:111]\n"
        "buffer_load_dwordx4 v100, s[8:11], s39 offen offset:1024 lds\n"
        "ds_write_b128 v11, v[24:27] offset:896\n"
        "v_mfma_f32_16x16x32_bf16 v[20:23], v[60:63], v[60:63], 0\n"
        "v_mfma_f32_16x16x32_bf16 v[20:23], v[66:69], v[66:69], v[20:23]\n"
        "v_mfma_f32_16x16x32_bf16 v[20:23], v[72:75], v[72:75], v[20:23]\n"
        "buffer_load_dwordx4 v103, s[8:11], s39 offen offset:2048 lds\n"
        "ds_write_b128 v11, v[96:99] offset:128\n"
        "v_mov_b64 v[96:97], 0\n"
        "v_mov_b64 v[98:99], 0\n"
        "v_mfma_f32_16x16x32_bf16 v[24:27], v[42:45], v[42:45], 0\n"
        "v_mfma_f32_16x16x32_bf16 v[24:27], v[48:51], v[48:51], v[24:27]\n"
        "v_mfma_f32_16x16x32_bf16 v[24:27], v[54:57], v[54:57], v[24:27]\n"
        "ds_write_b128 v11, v[30:33] offset:960\n"
        "v_smfmac_f32_16x16x64_bf16 v[96:99], v[60:63], v[34:41], v10\n"
        "v_cndmask_b32_e64 v93, v20, v21, s[48:49]\n"
        "v_smfmac_f32_16x16x64_bf16 v[96:99], v[66:69], v[78:85], v10\n"
        "v_cndmask_b32_e64 v93, v93, v22, s[50:51]\n"
        "v_smfmac_f32_16x16x64_bf16 v[96:99], v[72:75], v[104:111], v10\n"
        "s_mov_b64 exec, 2\n"
        "ds_inc_u32 v102, v10 offset:0\n"
        "s_mov_b64 exec, -1\n"
        "v_mfma_f32_16x16x32_bf16 v[34:37], v[112:115], v[42:45], 0\n"
        "v_cndmask_b32_e64 v111, v24, v25, s[48:49]\n"
        "v_cndmask_b32_e64 v111, v111, v26, s[50:51]\n"
        "v_mfma_f32_16x16x32_bf16 v[34:37], v[116:119], v[48:51], v[34:37]\n"
        "s_and_b32 s69, s30, 1\n"
        "v_cndmask_b32_e64 v93, v93, v23, s[52:53]\n"
        "v_cndmask_b32_e64 v111, v111, v27, s[52:53]\n"
        "v_mfma_f32_16x16x32_bf16 v[34:37], v[120:123], v[54:57], v[34:37]\n"
        "s_lshl2_add_u32 s70, s69, 21600\n"
        "v_mov_b32 v102, s70\n"
        "v_cndmask_b32_dpp v93, v111, v93, vcc row_ror:4 row_mask:0xf bank_mask:0xf\n"
        "v_mfma_f32_16x16x32_bf16 v[38:41], v[132:135], v[54:57], 0\n"
        "s_mul_i32 s69, s69, 40064\n"
        "s_mul_i32 s70, s31, 1664\n"
        "v_mfma_f32_16x16x32_bf16 v[38:41], v[128:131], v[48:51], v[38:41]\n"
        "s_add_u32 s69, s69, s70\n"
        "v_add_u32 v11, s69, v3\n"
        "v_mfma_f32_16x16x32_bf16 v[38:41], v[124:127], v[42:45], v[38:41]\n"
        "v_add_u32 v12, s69, v2\n"
        "s_lshr_b32 s68, s30, 1\n"
        "v_mfma_f32_16x16x32_bf16 v[104:107], v[136:139], v[42:45], 0\n"
        "s_lshl2_add_u32 s68, s68, 4\n"
        "s_cmp_eq_u32 s31, 6\n"
        "s_cbranch_scc1 L8_%=\n"
        "L9_%=:\n"
        "v_mfma_f32_16x16x32_bf16 v[104:107], v[140:143], v[48:51], v[104:107]\n"
        "s_mov_b32 s72, s30\n"
        "s_mov_b32 s30, s34\n"
        "v_mfma_f32_16x16x32_bf16 v[104:107], v[144:147], v[54:57], v[104:107]\n"
        "s_mov_b32 s31, s35\n"
        "s_add_u32 s37, s37, 4\n"
        "v_mfma_f32_16x16x32_bf16 v[108:111], v[156:159], v[54:57], 0\n"
        "s_cmp_ge_u32 s37, 49\n"
        "s_cselect_b32 s69, 49, 0\n"
        "v_mfma_f32_16x16x32_bf16 v[108:111], v[152:155], v[48:51], v[108:111]\n"
        "s_sub_u32 s37, s37, s69\n"
        "s_sub_u32 s69, s37, 4\n"
        "s_cmp_lt_u32 s69, 4\n"
        "v_mfma_f32_16x16x32_bf16 v[108:111], v[148:151], v[42:45], v[108:111]\n"
        "s_subb_u32 s36, s36, -4\n"
        "s_cmp_ge_u32 s36, 6\n"
        "v_mfma_f32_16x16x32_bf16 v[76:79], v[160:163], v[42:45], 0\n"
        "s_cselect_b32 s69, 6, 0\n"
        "s_addc_u32 s34, s34, 0\n"
        "v_mfma_f32_16x16x32_bf16 v[76:79], v[164:167], v[48:51], v[76:79]\n"
        "s_sub_u32 s36, s36, s69\n"
        "s_cmp_ge_u32 s34, s74\n"
        "v_mfma_f32_16x16x32_bf16 v[76:79], v[168:171], v[54:57], v[76:79]\n"
        "s_cbranch_scc1 L10_%=\n"
        "L11_%=:\n"
        "s_mul_i32 s38, s34, 256\n"
        "v_mfma_f32_16x16x32_bf16 v[80:83], v[180:183], v[54:57], 0\n"
        "s_add_u32 s38, s38, s16\n"
        "s_cmp_ge_u32 s34, 20\n"
        "s_cselect_b32 s38, s22, s38\n"
        "v_mfma_f32_16x16x32_bf16 v[80:83], v[176:179], v[48:51], v[80:83]\n"
        "s_cmp_ge_u32 s34, 21\n"
        "s_cselect_b32 s38, s23, s38\n"
        "v_mfma_f32_16x16x32_bf16 v[80:83], v[172:175], v[42:45], v[80:83]\n"
        "s_mul_hi_u32 s70, s38, 58835169\n"
        "s_mul_i32 s38, s38, 1344\n"
        "v_mfma_f32_16x16x32_bf16 v[20:23], v[16:19], v[60:63], 0\n"
        "s_mul_i32 s70, s70, 588672\n"
        "s_add_u32 s38, s38, s70\n"
        "v_mfma_f32_16x16x32_bf16 v[20:23], v[16:19], v[66:69], v[20:23]\n"
        "s_mul_i32 s69, s36, 98304\n"
        "s_add_u32 s38, s38, s69\n"
        "s_mov_b32 s35, s36\n"
        "v_mfma_f32_16x16x32_bf16 v[20:23], v[16:19], v[72:75], v[20:23]\n"
        "s_mul_i32 s69, s36, -12\n"
        "v_mov_b64 v[30:31], 0\n"
        "v_mov_b64 v[32:33], 0\n"
        "v_mfma_f32_16x16x32_bf16 v[24:27], v[16:19], v[42:45], 0\n"
        "s_add_u32 s69, s69, 60\n"
        "s_lshl_b64 s[54:55], -1, s69\n"
        "s_lshl_b64 s[56:57], s[54:55], 24\n"
        "v_mfma_f32_16x16x32_bf16 v[24:27], v[16:19], v[48:51], v[24:27]\n"
        "s_ashr_i64 s[60:61], s[54:55], 8\n"
        "s_mov_b32 s58, s61\n"
        "v_mfma_f32_16x16x32_bf16 v[24:27], v[16:19], v[54:57], v[24:27]\n"
        "s_mov_b32 s59, s56\n"
        "v_cndmask_b32_e64 v15, v4, v7, s[56:57]\n"
        "v_smfmac_f32_16x16x64_bf16 v[30:33], v[42:45], v[34:41], v10\n"
        "v_cndmask_b32_e64 v100, v5, v8, s[58:59]\n"
        "v_smfmac_f32_16x16x64_bf16 v[30:33], v[48:51], v[104:111], v10\n"
        "v_cndmask_b32_e64 v103, v6, v9, s[60:61]\n"
        "v_smfmac_f32_16x16x64_bf16 v[30:33], v[54:57], v[76:83], v10\n"
        "s_cmp_eq_u32 s37, 3\n"
        "s_cbranch_scc1 L12_%=\n"
        "L13_%=:\n"
        "v_cndmask_b32_dpp v110, v24, v20, vcc row_ror:4 row_mask:0xf bank_mask:0xf\n"
        "v_mfma_f32_16x16x32_bf16 v[36:39], v[54:57], v[192:195], 0\n"
        "s_cmp_lt_u32 s34, s14\n"
        "v_mul_f32 v111, s28, v110\n"
        "v_fma_f32 v111, -v111, v110, v93\n"
        "v_mfma_f32_16x16x32_bf16 v[36:39], v[48:51], v[188:191], v[36:39]\n"
        "s_cselect_b32 s38, s38, 0x80000000\n"
        "s_add_u32 s39, s38, 100270464\n"
        "v_add_f32_e64 v111, s29, |v111|\n"
        "v_mfma_f32_16x16x32_bf16 v[36:39], v[42:45], v[184:187], v[36:39]\n"
        "v_rsq_f32 v84, v111\n"
        "v_cndmask_b32_dpp v104, v30, v96, vcc row_ror:4 row_mask:0xf bank_mask:0xf\n"
        "v_mfma_f32_16x16x32_bf16 v[24:27], v[72:75], v[192:195], 0\n"
        "v_cndmask_b32_e64 v84, 0, v84, s[46:47]\n"
        "v_cndmask_b32_dpp v105, v31, v97, vcc row_ror:4 row_mask:0xf bank_mask:0xf\n"
        "v_mfma_f32_16x16x32_bf16 v[24:27], v[66:69], v[188:191], v[24:27]\n"
        "v_cndmask_b32_dpp v85, v32, v98, vcc row_ror:4 row_mask:0xf bank_mask:0xf\n"
        "v_mul_f32_dpp v104, v84, v104 quad_perm:[0,0,0,0] row_mask:0xf bank_mask:0xf\n"
        "v_mfma_f32_16x16x32_bf16 v[24:27], v[60:63], v[184:187], v[24:27]\n"
        "v_mul_f32_dpp v105, v84, v105 quad_perm:[1,1,1,1] row_mask:0xf bank_mask:0xf\n"
        "v_mul_f32_dpp v85, v84, v85 quad_perm:[2,2,2,2] row_mask:0xf bank_mask:0xf\n"
        "v_mfma_f32_16x16x32_bf16 v[20:23], v[60:63], v[220:223], 0\n"
        "v_cndmask_b32_dpp v106, v33, v99, vcc row_ror:4 row_mask:0xf bank_mask:0xf\n"
        "v_max3_f32 v107, v104, v105, v85\n"
        "v_mfma_f32_16x16x32_bf16 v[20:23], v[66:69], v[224:227], v[20:23]\n"
        "v_mul_f32_dpp v106, v84, v106 quad_perm:[3,3,3,3] row_mask:0xf bank_mask:0xf\n"
        "v_max_f32 v107, v107, v106\n"
        "v_mfma_f32_16x16x32_bf16 v[20:23], v[72:75], v[228:231], v[20:23]\n"
        "v_mul_f32 v107, v107, v84\n"
        "v_fma_f32 v104, v104, v84, -v107\n"
        "v_fma_f32 v105, v105, v84, -v107\n"
        "v_mfma_f32_16x16x32_bf16 v[32:35], v[42:45], v[220:223], 0\n"
        "v_fma_f32 v85, v85, v84, -v107\n"
        "v_fma_f32 v106, v106, v84, -v107\n"
        "v_mfma_f32_16x16x32_bf16 v[32:35], v[48:51], v[224:227], v[32:35]\n"
        "v_exp_f32 v104, v104\n"
        "v_mfma_f32_16x16x32_bf16 v[32:35], v[54:57], v[228:231], v[32:35]\n"
        "v_exp_f32 v105, v105\n"
        "v_mfma_f32_16x16x32_bf16 v[108:111], v[54:57], v[204:207], 0\n"
        "v_exp_f32 v85, v85\n"
        "v_mfma_f32_16x16x32_bf16 v[108:111], v[48:51], v[200:203], v[108:111]\n"
        "v_exp_f32 v106, v106\n"
        "v_mfma_f32_16x16x32_bf16 v[108:111], v[42:45], v[196:199], v[108:111]\n"
        "v_add_f32 v107, v104, v105\n"
        "v_add_f32 v101, v85, v106\n"
        "v_add_f32 v107, v107, v101\n"
        "v_mfma_f32_16x16x32_bf16 v[80:83], v[72:75], v[204:207], 0\n"
        "v_rcp_f32 v107, v107\n"
        "v_mfma_f32_16x16x32_bf16 v[80:83], v[66:69], v[200:203], v[80:83]\n"
        "v_mul_f32_dpp v104, v84, v104 quad_perm:[0,0,0,0] row_mask:0xf bank_mask:0xf\n"
        "v_mul_f32_dpp v105, v84, v105 quad_perm:[1,1,1,1] row_mask:0xf bank_mask:0xf\n"
        "v_mfma_f32_16x16x32_bf16 v[80:83], v[60:63], v[196:199], v[80:83]\n"
        "v_mul_f32_dpp v85, v84, v85 quad_perm:[2,2,2,2] row_mask:0xf bank_mask:0xf\n"
        "v_mul_f32_dpp v106, v84, v106 quad_perm:[3,3,3,3] row_mask:0xf bank_mask:0xf\n"
        "v_mul_f32 v104, v104, v107\n"
        "v_mfma_f32_16x16x32_bf16 v[76:79], v[60:63], v[232:235], 0\n"
        "v_mul_f32 v105, v105, v107\n"
        "v_mul_f32 v85, v85, v107\n"
        "v_mfma_f32_16x16x32_bf16 v[76:79], v[66:69], v[236:239], v[76:79]\n"
        "v_mul_f32 v106, v106, v107\n"
        "v_cvt_pk_bf16_f32 v99, v104, v105\n"
        "v_mfma_f32_16x16x32_bf16 v[76:79], v[72:75], v[240:243], v[76:79]\n"
        "v_cvt_pk_bf16_f32 v101, v85, v106\n"
        "v_and_b32 v58, v1, v99\n"
        "v_and_b32 v59, v1, v101\n"
        "v_mfma_f32_16x16x32_bf16 v[104:107], v[42:45], v[232:235], 0\n"
        "v_cvt_pk_bf16_f32 v92, v24, v25\n"
        "v_cvt_pk_bf16_f32 v93, v26, v27\n"
        "v_mfma_f32_16x16x32_bf16 v[104:107], v[48:51], v[236:239], v[104:107]\n"
        "v_and_b32_dpp v40, v99, v1 row_ror:12 row_mask:0xf bank_mask:0xf\n"
        "v_and_b32_dpp v41, v101, v1 row_ror:12 row_mask:0xf bank_mask:0xf\n"
        "v_mfma_f32_16x16x32_bf16 v[104:107], v[54:57], v[240:243], v[104:107]\n"
        "v_cvt_pk_bf16_f32 v84, v36, v37\n"
        "v_cvt_pk_bf16_f32 v85, v38, v39\n"
        "v_mov_b64 v[70:71], v[58:59]\n"
        "v_mfma_f32_16x16x32_bf16 v[36:39], v[92:95], v[58:61], 0\n"
        "v_cvt_pk_bf16_f32 v96, v20, v21\n"
        "v_cvt_pk_bf16_f32 v97, v22, v23\n"
        "v_mfma_f32_16x16x32_bf16 v[22:25], v[84:87], v[40:43], 0\n"
        "v_mov_b64 v[52:53], v[40:41]\n"
        "v_cvt_pk_bf16_f32 v88, v32, v33\n"
        "v_cvt_pk_bf16_f32 v89, v34, v35\n"
        "v_mfma_f32_16x16x32_bf16 v[32:35], v[94:97], v[68:71], 0\n"
        "v_mov_b64 v[64:65], v[58:59]\n"
        "v_cvt_pk_bf16_f32 v30, v80, v81\n"
        "v_mfma_f32_16x16x32_bf16 v[96:99], v[86:89], v[50:53], 0\n"
        "v_cvt_pk_bf16_f32 v31, v82, v83\n"
        "v_mov_b64 v[46:47], v[40:41]\n"
        "v_mfma_f32_16x16x32_bf16 v[80:83], v[54:57], v[216:219], 0\n"
        "v_cvt_pk_bf16_f32 v92, v108, v109\n"
        "v_cvt_pk_bf16_f32 v93, v110, v111\n"
        "v_mfma_f32_16x16x32_bf16 v[80:83], v[48:51], v[212:215], v[80:83]\n"
        "v_cvt_pk_bf16_f32 v26, v76, v77\n"
        "v_cvt_pk_bf16_f32 v27, v78, v79\n"
        "v_mfma_f32_16x16x32_bf16 v[80:83], v[42:45], v[208:211], v[80:83]\n"
        "v_cvt_pk_bf16_f32 v88, v104, v105\n"
        "v_cvt_pk_bf16_f32 v89, v106, v107\n"
        "v_mfma_f32_16x16x32_bf16 v[108:111], v[72:75], v[216:219], 0\n"
        "v_mov_b64 v[76:77], v[58:59]\n"
        "v_mov_b64 v[58:59], v[40:41]\n"
        "v_mfma_f32_16x16x32_bf16 v[108:111], v[66:69], v[212:215], v[108:111]\n"
        "v_cvt_pk_bf16_f32 v104, v36, v37\n"
        "v_cvt_pk_bf16_f32 v105, v38, v39\n"
        "v_mfma_f32_16x16x32_bf16 v[108:111], v[60:63], v[208:211], v[108:111]\n"
        "v_cvt_pk_bf16_f32 v84, v80, v81\n"
        "v_cvt_pk_bf16_f32 v85, v82, v83\n"
        "v_mfma_f32_16x16x32_bf16 v[80:83], v[28:31], v[62:65], 0\n"
        "v_cvt_pk_bf16_f32 v106, v32, v33\n"
        "v_cvt_pk_bf16_f32 v107, v34, v35\n"
        "v_mfma_f32_16x16x32_bf16 v[32:35], v[90:93], v[44:47], 0\n"
        "v_cvt_pk_bf16_f32 v20, v22, v23\n"
        "v_cvt_pk_bf16_f32 v21, v24, v25\n"
        "v_mfma_f32_16x16x32_bf16 v[38:41], v[60:63], v[244:247], 0\n"
        "v_cvt_pk_bf16_f32 v92, v108, v109\n"
        "v_cvt_pk_bf16_f32 v93, v110, v111\n"
        "v_mfma_f32_16x16x32_bf16 v[38:41], v[66:69], v[248:251], v[38:41]\n"
        "v_cvt_pk_bf16_f32 v108, v80, v81\n"
        "v_cvt_pk_bf16_f32 v109, v82, v83\n"
        "v_mfma_f32_16x16x32_bf16 v[38:41], v[72:75], v[252:255], v[38:41]\n"
        "v_cvt_pk_bf16_f32 v22, v96, v97\n"
        "v_cvt_pk_bf16_f32 v23, v98, v99\n"
        "v_mfma_f32_16x16x32_bf16 v[80:83], v[42:45], v[244:247], 0\n"
        "v_cvt_pk_bf16_f32 v24, v32, v33\n"
        "v_cvt_pk_bf16_f32 v25, v34, v35\n"
        "v_mfma_f32_16x16x32_bf16 v[80:83], v[48:51], v[248:251], v[80:83]\n"
        "v_mfma_f32_16x16x32_bf16 v[80:83], v[54:57], v[252:255], v[80:83]\n"
        "v_mfma_f32_16x16x32_bf16 v[34:37], v[26:29], v[70:73], 0\n"
        "v_cvt_pk_bf16_f32 v96, v38, v39\n"
        "v_cvt_pk_bf16_f32 v97, v40, v41\n"
        "v_mfma_f32_16x16x32_bf16 v[38:41], v[88:91], v[52:55], 0\n"
        "v_mfma_f32_16x16x32_bf16 v[30:33], v[92:95], v[64:67], 0\n"
        "s_waitcnt vmcnt(0)\n"
        "ds_read_b128 v[60:63], v0\n"
        "ds_read_b128 v[42:45], v0 offset:768\n"
        "ds_read_b128 v[66:69], v0 offset:64\n"
        "v_cvt_pk_bf16_f32 v88, v80, v81\n"
        "v_mfma_f32_16x16x32_bf16 v[78:81], v[84:87], v[46:49], 0\n"
        "s_add_u32 s73, s73, 1\n"
        "ds_read_b128 v[48:51], v0 offset:832\n"
        "v_cvt_pk_bf16_f32 v89, v82, v83\n"
        "v_cvt_pk_bf16_f32 v110, v34, v35\n"
        "v_cvt_pk_bf16_f32 v111, v36, v37\n"
        "v_mfma_f32_16x16x32_bf16 v[82:85], v[94:97], v[74:77], 0\n"
        "s_cmp_lt_u32 s73, 4\n"
        "ds_read_b128 v[72:75], v0 offset:128\n"
        "v_cvt_pk_bf16_f32 v96, v30, v31\n"
        "v_cvt_pk_bf16_f32 v97, v32, v33\n"
        "v_cvt_pk_bf16_f32 v26, v38, v39\n"
        "v_mfma_f32_16x16x32_bf16 v[34:37], v[86:89], v[56:59], 0\n"
        "s_cbranch_scc1 L19_%=\n"
        "L20_%=:\n"
        "ds_read_b128 v[54:57], v0 offset:896\n"
        "v_cvt_pk_bf16_f32 v98, v82, v83\n"
        "v_cvt_pk_bf16_f32 v99, v84, v85\n"
        "v_cvt_pk_bf16_f32 v27, v40, v41\n"
        "v_mfma_f32_16x16x32_bf16 v[82:85], v[104:107], v[104:107], 0\n"
        "v_cvt_pk_bf16_f32 v30, v78, v79\n"
        "v_cvt_pk_bf16_f32 v31, v80, v81\n"
        "v_mfma_f32_16x16x32_bf16 v[82:85], v[108:111], v[108:111], v[82:85]\n"
        "v_cvt_pk_bf16_f32 v32, v34, v35\n"
        "v_cvt_pk_bf16_f32 v33, v36, v37\n"
        "v_mfma_f32_16x16x32_bf16 v[82:85], v[96:99], v[96:99], v[82:85]\n"
        "v_mfma_f32_16x16x32_bf16 v[76:79], v[20:23], v[20:23], 0\n"
        "v_mfma_f32_16x16x32_bf16 v[76:79], v[24:27], v[24:27], v[76:79]\n"
        "v_mfma_f32_16x16x32_bf16 v[76:79], v[30:33], v[30:33], v[76:79]\n"
        "s_cmp_lt_u32 s30, s14\n"
        "s_cbranch_scc1 PTOP_%=\n"
        "s_waitcnt lgkmcnt(0)\n"
        "s_nop 15\n"
        "L21_%=:\n"
        "ds_read_b32 v255, v102 offset:8\n"
        "s_waitcnt lgkmcnt(0)\n"
        "v_readfirstlane_b32 s40, v255\n"
        "s_nop 3\n"
        "s_cmp_ge_i32 s40, s68\n"
        "s_cbranch_scc1 L22_%=\n"
        "s_sleep 1\n"
        "s_sub_u32 s41, s41, 1\n"
        "s_cbranch_scc0 L21_%=\n"
        "s_endpgm\n"
        "L22_%=:\n"
        "v_cndmask_b32_e64 v255, v82, v83, s[48:49]\n"
        "v_cndmask_b32_e64 v254, v76, v77, s[48:49]\n"
        "v_cndmask_b32_e64 v255, v255, v84, s[50:51]\n"
        "v_cndmask_b32_e64 v254, v254, v78, s[50:51]\n"
        "v_cndmask_b32_e64 v255, v255, v85, s[52:53]\n"
        "v_cndmask_b32_e64 v254, v254, v79, s[52:53]\n"
        "ds_write_b128 v11, v[104:107]\n"
        "ds_write_b128 v11, v[20:23] offset:832\n"
        "v_cndmask_b32_dpp v255, v254, v255, vcc row_ror:4 row_mask:0xf bank_mask:0xf\n"
        "ds_write_b128 v11, v[108:111] offset:64\n"
        "ds_write_b128 v11, v[24:27] offset:896\n"
        "ds_write_b128 v11, v[96:99] offset:128\n"
        "ds_write_b128 v11, v[30:33] offset:960\n"
        "s_mov_b64 exec, s[46:47]\n"
        "ds_write_b32 v12, v255\n"
        "s_mov_b64 exec, -1\n"
        "s_mov_b64 exec, 2\n"
        "ds_inc_u32 v102, v10 offset:0\n"
        "s_mov_b64 exec, -1\n"
        "s_or_b32 s40, s18, s2\n"
        "s_cbranch_scc1 L31_%=\n"
        "L30_%=:\n"
        "s_mov_b64 exec, 1\n"
        "v_mov_b32 v20, 0\n"
        "v_mov_b32 v21, 1\n"
        "global_atomic_add v22, v20, v21, s[24:25] offset:256 sc0\n"
        "s_mov_b64 exec, -1\n"
        "s_mov_b64 exec, 1\n"
        "v_mov_b32 v20, 0\n"
        "v_mov_b32 v21, 0\n"
        "global_atomic_add v22, v20, v21, s[24:25] offset:0 sc0\n"
        "s_mov_b64 exec, -1\n"
        "s_waitcnt vmcnt(0)\n"
        "v_readfirstlane_b32 s40, v22\n"
        "s_nop 3\n"
        "s_cmp_ge_u32 s40, 512\n"
        "s_cbranch_scc1 L31_%=\n"
        "s_sleep 8\n"
        "s_sub_u32 s41, s41, 64\n"
        "s_cbranch_scc0 L30_%=\n"
        "L31_%=:\n"
        "s_endpgm\n"
        "L3_%=:\n"
        "s_nop 7\n"
        "L1_%=:\n"
        "ds_read_b32 v101, v102 offset:8\n"
        "s_waitcnt lgkmcnt(0)\n"
        "v_readfirstlane_b32 s40, v101\n"
        "s_nop 3\n"
        "s_cmp_ge_i32 s40, s68\n"
        "s_cbranch_scc1 L2_%=\n"
        "s_sleep 1\n"
        "s_sub_u32 s41, s41, 1\n"
        "s_cbranch_scc0 L1_%=\n"
        "s_endpgm\n"
        "L2_%=:\n"
        "s_nop 1\n"
        "s_branch L4_%=\n"
        "L8_%=:\n"
        "s_nop 7\n"
        "ds_read_u16 v11, v13 offset:19564\n"
        "ds_read_u16 v12, v13 offset:19566\n"
        "s_cmp_ge_u32 s30, 10\n"
        "s_cselect_b32 s69, 10, 0\n"
        "s_cmp_ge_u32 s30, 20\n"
        "s_cselect_b32 s69, 20, s69\n"
        "s_sub_u32 s69, s30, s69\n"
        "s_mul_i32 s69, s69, 832\n"
        "s_waitcnt lgkmcnt(0)\n"
        "v_add_u32 v11, s69, v11\n"
        "v_subrev_u32 v101, 8320, v11\n"
        "v_min_u32 v11, v11, v101\n"
        "v_add_u32 v11, 101856, v11\n"
        "v_add_u32 v12, s69, v12\n"
        "v_subrev_u32 v101, 8320, v12\n"
        "v_min_u32 v12, v12, v101\n"
        "v_add_u32 v12, 101856, v12\n"
        "s_mov_b32 s69, 21660\n"
        "v_writelane_b32 v102, s69, 1\n"
        "s_nop 1\n"
        "s_branch L9_%=\n"
        "L10_%=:\n"
        "s_nop 7\n"
        "v_mov_b32 v101, 21600\n"
        "L6_%=:\n"
        "ds_read_b32 v92, v101 offset:64\n"
        "s_waitcnt lgkmcnt(0)\n"
        "v_readfirstlane_b32 s40, v92\n"
        "s_nop 3\n"
        "s_cmp_ge_i32 s40, 1\n"
        "s_cbranch_scc1 L7_%=\n"
        "s_sleep 1\n"
        "s_sub_u32 s41, s41, 1\n"
        "s_cbranch_scc0 L6_%=\n"
        "s_endpgm\n"
        "L7_%=:\n"
        "v_mbcnt_lo_u32_b32 v92, -1, 0\n"
        "v_lshl_add_u32 v101, v92, 2, v101\n"
        "ds_read_b32 v92, v101 offset:68\n"
        "s_mov_b32 s14, s40\n"
        "s_mov_b32 s74, -1\n"
        "s_waitcnt lgkmcnt(0)\n"
        "v_readlane_b32 s22, v92, 0\n"
        "v_readlane_b32 s23, v92, 1\n"
        "s_nop 3\n"
        "s_nop 1\n"
        "s_branch L11_%=\n"
        "L12_%=:\n"
        "s_nop 7\n"
        "ds_read_b32 v15, v13 offset:19552\n"
        "ds_read_b32 v100, v13 offset:19556\n"
        "ds_read_b32 v103, v13 offset:19560\n"
        "s_mov_b32 s35, 6\n"
        "s_mov_b32 s38, 0\n"
        "s_mov_b32 s69, 1344\n"
        "s_waitcnt lgkmcnt(0)\n"
        "v_lshrrev_b32 v101, 28, v15\n"
        "v_add_u32 v101, s34, v101\n"
        "v_and_b32 v15, 0x0fffffff, v15\n"
        "v_mul_u32_u24 v92, 256, v101\n"
        "v_add_u32 v92, s16, v92\n"
        "v_cmpx_le_u32 vcc, 20, v101\n"
        "v_mov_b32 v92, s22\n"
        "s_mov_b64 exec, -1\n"
        "v_cmpx_le_u32 vcc, 21, v101\n"
        "v_mov_b32 v92, s23\n"
        "s_mov_b64 exec, -1\n"
        "v_mad_u32_u24 v15, v92, s69, v15\n"
        "v_mul_u32_u24 v92, 57457, v92\n"
        "v_lshrrev_b32 v92, 22, v92\n"
        "v_mul_u32_u24 v92, 588672, v92\n"
        "v_add_u32 v15, v15, v92\n"
        "v_lshrrev_b32 v101, 28, v100\n"
        "v_add_u32 v101, s34, v101\n"
        "v_and_b32 v100, 0x0fffffff, v100\n"
        "v_mul_u32_u24 v92, 256, v101\n"
        "v_add_u32 v92, s16, v92\n"
        "v_cmpx_le_u32 vcc, 20, v101\n"
        "v_mov_b32 v92, s22\n"
        "s_mov_b64 exec, -1\n"
        "v_cmpx_le_u32 vcc, 21, v101\n"
        "v_mov_b32 v92, s23\n"
        "s_mov_b64 exec, -1\n"
        "v_mad_u32_u24 v100, v92, s69, v100\n"
        "v_mul_u32_u24 v92, 57457, v92\n"
        "v_lshrrev_b32 v92, 22, v92\n"
        "v_mul_u32_u24 v92, 588672, v92\n"
        "v_add_u32 v100, v100, v92\n"
        "v_lshrrev_b32 v101, 28, v103\n"
        "v_add_u32 v101, s34, v101\n"
        "v_and_b32 v103, 0x0fffffff, v103\n"
        "v_mul_u32_u24 v92, 256, v101\n"
        "v_add_u32 v92, s16, v92\n"
        "v_cmpx_le_u32 vcc, 20, v101\n"
        "v_mov_b32 v92, s22\n"
        "s_mov_b64 exec, -1\n"
        "v_cmpx_le_u32 vcc, 21, v101\n"
        "v_mov_b32 v92, s23\n"
        "s_mov_b64 exec, -1\n"
        "v_mad_u32_u24 v103, v92, s69, v103\n"
        "v_mul_u32_u24 v92, 57457, v92\n"
        "v_lshrrev_b32 v92, 22, v92\n"
        "v_mul_u32_u24 v92, 588672, v92\n"
        "v_add_u32 v103, v103, v92\n"
        "s_nop 4\n"
        "s_mov_b64 vcc, s[44:45]\n"
        "s_nop 1\n"
        "s_branch L13_%=\n"
        "L19_%=:\n"
        "s_nop 7\n"
        "v_readfirstlane_b32 s62, v14\n"
        "s_cmp_eq_u32 s73, 1\n"
        "s_cbranch_scc1 L14_%=\n"
        "s_cmp_ge_u32 s73, 2\n"
        "s_cbranch_scc1 L15_%=\n"
        "s_mov_b32 s27, 20\n"
        "s_cmp_lg_u32 s2, 0\n"
        "s_cbranch_scc1 L18_%=\n"
        "s_not_b32 s26, s26\n"
        "s_mov_b64 exec, 1\n"
        "v_mov_b32 v101, 0\n"
        "v_mov_b32 v92, 1\n"
        "global_atomic_add v14, v101, v92, s[24:25] offset:256 sc0\n"
        "s_mov_b64 exec, -1\n"
        "s_branch L16_%=\n"
        "L18_%=:\n"
        "s_mov_b64 exec, 1\n"
        "v_mov_b32 v101, 0\n"
        "global_load_dword v14, v101, s[24:25] offset:256 sc1\n"
        "s_mov_b64 exec, -1\n"
        "s_branch L16_%=\n"
        "L14_%=:\n"
        "s_cmp_lg_u32 s62, s26\n"
        "s_cbranch_scc1 L17_%=\n"
        "s_sleep 8\n"
        "s_mov_b64 exec, 1\n"
        "v_mov_b32 v101, 0\n"
        "global_load_dword v14, v101, s[24:25] offset:256 sc1\n"
        "s_mov_b64 exec, -1\n"
        "s_waitcnt vmcnt(0)\n"
        "v_readfirstlane_b32 s62, v14\n"
        "s_sub_u32 s41, s41, 64\n"
        "s_cbranch_scc0 L14_%=\n"
        "s_endpgm\n"
        "L15_%=:\n"
        "s_cmp_lt_u32 s62, 209\n"
        "s_addc_u32 s27, s27, 0\n"
        "s_min_u32 s62, s62, 208\n"
        "s_add_u32 s62, s62, 5120\n"
        "s_lshl2_add_u32 s64, s73, 21600\n"
        "v_mov_b32 v101, s64\n"
        "v_mov_b32 v92, s62\n"
        "ds_write_b32 v101, v92 offset:60\n"
        "s_waitcnt lgkmcnt(0)\n"
        "s_cmp_lt_u32 s73, 3\n"
        "s_cbranch_scc1 L17_%=\n"
        "v_mov_b32 v92, s27\n"
        "ds_write_b32 v101, v92 offset:52\n"
        "s_waitcnt lgkmcnt(0)\n"
        "s_branch L16_%=\n"
        "L17_%=:\n"
        "s_mov_b64 exec, 1\n"
        "v_mov_b32 v101, 0\n"
        "v_mov_b32 v92, 1\n"
        "global_atomic_add v14, v101, v92, s[24:25] offset:0 sc0\n"
        "s_mov_b64 exec, -1\n"
        "L16_%=:\n"
        "s_nop 1\n"
        "s_branch L20_%=\n"
        "CONS_%=:\n"
        "s_mov_b32 s41, 0x400000\n"
        "s_mov_b32 s48, 0xffffffff\n"
        "s_mov_b32 s49, 0xffff0000\n"
        "s_mov_b32 s50, 0x33333333\n"
        "s_mov_b32 s51, 0x33333333\n"
        "s_mov_b32 s52, 0x3c2aaaab\n"
        "s_mov_b32 s53, 0x3a7ba882\n"
        "s_mov_b32 s56, 0x0\n"
        "s_mov_b32 s57, 0xfeff0000\n"
        "s_mov_b32 s62, 0x11111111\n"
        "s_mov_b32 s63, 0x11111111\n"
        "s_mov_b64 s[44:45], s[12:13]\n"
        "s_and_b32 s45, s45, 0xffff\n"
        "s_mov_b32 s47, 0x20000\n"
        "s_mov_b32 s70, 832\n"
        "s_mov_b32 s71, 17\n"
        "s_sub_u32 s69, s18, 7488\n"
        "s_mov_b32 s28, 0xffff0000\n"
        "s_mov_b32 s31, 0\n"
        "s_add_u32 s35, s15, s17\n"
        "s_mov_b32 s36, 0x20000000\n"
        "L58_%=:\n"
        "ds_read_b32 v251, v250 offset:52\n"
        "s_waitcnt lgkmcnt(0)\n"
        "v_readfirstlane_b32 s40, v251\n"
        "s_nop 3\n"
        "s_cmp_ge_i32 s40, 6\n"
        "s_cbranch_scc1 L59_%=\n"
        "s_nop 0\n"
        "s_sub_u32 s41, s41, 1\n"
        "s_cbranch_scc0 L58_%=\n"
        "s_endpgm\n"
        "L59_%=:\n"
        "s_waitcnt vmcnt(0)\n"
        "s_setprio 2\n"
        "v_add_u32 v24, 2, v1\n"
        "v_mov_b32 v25, 0x3f80\n"
        "s_mov_b32 exec_lo, 0\n"
        "s_mov_b32 exec_hi, 0x1000000\n"
        "ds_write_b16 v24, v25\n"
        "s_mov_b64 exec, -1\n"
        "v_add_u32 v24, 61792, v0\n"
        "ds_read_b128 v[168:171], v24 offset:0\n"
        "ds_read_b128 v[172:175], v24 offset:1024\n"
        "ds_read_b128 v[176:179], v24 offset:2048\n"
        "ds_read_b128 v[180:183], v24 offset:3072\n"
        "ds_read_b128 v[184:187], v24 offset:4096\n"
        "ds_read_b128 v[188:191], v24 offset:5120\n"
        "ds_read_b128 v[192:195], v24 offset:6144\n"
        "ds_read_b128 v[196:199], v24 offset:7168\n"
        "ds_read_b128 v[200:203], v24 offset:8192\n"
        "ds_read_b128 v[204:207], v24 offset:9216\n"
        "ds_read_b128 v[208:211], v24 offset:10240\n"
        "ds_read_b128 v[212:215], v24 offset:11264\n"
        "ds_read_b128 v[216:219], v24 offset:12288\n"
        "ds_read_b128 v[220:223], v24 offset:13312\n"
        "ds_read_b128 v[224:227], v24 offset:14336\n"
        "ds_read_b128 v[228:231], v24 offset:15360\n"
        "ds_read_b128 v[232:235], v24 offset:16384\n"
        "ds_read_b128 v[236:239], v24 offset:17408\n"
        "ds_read_b128 v[240:243], v0 offset:0\n"
        "ds_read_b128 v[244:247], v0 offset:1024\n"
        "ds_read_b128 v[248:251], v0 offset:2048\n"
        "ds_read_b128 v[252:255], v0 offset:3088\n"
        "v_mov_b32 v25, 21600\n"
        "s_waitcnt lgkmcnt(0)\n"
        "s_mov_b64 exec, 1\n"
        "ds_inc_u32 v25, v1 offset:12\n"
        "s_mov_b64 exec, -1\n"
        "s_call_b64 s[64:65], CGRQ_%=\n"
        "L60_%=:\n"
        "ds_read_b32 v24, v0 offset:21600\n"
        "s_waitcnt lgkmcnt(0)\n"
        "v_readfirstlane_b32 s40, v24\n"
        "s_nop 3\n"
        "s_cmp_ge_i32 s40, 10\n"
        "s_cbranch_scc1 L61_%=\n"
        "s_sleep 1\n"
        "s_sub_u32 s41, s41, 1\n"
        "s_cbranch_scc0 L60_%=\n"
        "s_endpgm\n"
        "L61_%=:\n"
        "ds_read_b128 v[124:127], v7 offset:0\n"
        "ds_read_b128 v[132:135], v7 offset:3328\n"
        "ds_read_b128 v[128:131], v7 offset:64\n"
        "ds_read_b128 v[136:139], v7 offset:3392\n"
        "ds_read_b128 v[160:163], v7 offset:128\n"
        "ds_read_b128 v[52:55], v7 offset:3456\n"
        "s_waitcnt lgkmcnt(0)\n"
        "s_branch CTOP_%=\n"
        ".p2align 6\n"
        "s_nop 0\n"
        "s_nop 0\n"
        "s_nop 0\n"
        "s_nop 0\n"
        "s_nop 0\n"
        "s_nop 0\n"
        "s_nop 0\n"
        "s_nop 0\n"
        "s_nop 0\n"
        "s_nop 0\n"
        "s_nop 0\n"
        "s_nop 0\n"
        "s_nop 0\n"
        "s_nop 0\n"
        "s_nop 0\n"
        "CFULL_%=:\n"
        "s_call_b64 s[64:65], CGRP_%=\n"
        "CTOP_%=:\n"
        "s_waitcnt lgkmcnt(5)\n"
        "v_mfma_f32_16x16x32_bf16 v[24:27], v[168:171], v[124:127], 0\n"
        "v_cvt_pk_bf16_f32 v156, v152, v56\n"
        "v_cvt_pk_bf16_f32 v157, v72, v20\n"
        "s_waitcnt lgkmcnt(3)\n"
        "v_mfma_f32_16x16x32_bf16 v[24:27], v[172:175], v[128:131], v[24:27]\n"
        "ds_read_b128 v[44:47], v16 offset:128\n"
        "v_cvt_pk_bf16_f32 v158, v84, v60\n"
        "s_waitcnt lgkmcnt(2)\n"
        "v_mfma_f32_16x16x32_bf16 v[24:27], v[176:179], v[160:163], v[24:27]\n"
        "ds_read_b128 v[48:51], v5\n"
        "v_cvt_pk_bf16_f32 v164, v153, v57\n"
        "v_mfma_f32_16x16x32_bf16 v[32:35], v[188:191], v[160:163], 0\n"
        "v_cvt_pk_bf16_f32 v165, v73, v21\n"
        "v_cvt_pk_bf16_f32 v166, v85, v61\n"
        "v_mfma_f32_16x16x32_bf16 v[32:35], v[184:187], v[128:131], v[32:35]\n"
        "v_cvt_pk_bf16_f32 v146, v154, v58\n"
        "v_cvt_pk_bf16_f32 v147, v74, v22\n"
        "v_mfma_f32_16x16x32_bf16 v[32:35], v[180:183], v[124:127], v[32:35]\n"
        "s_add_u32 s37, s36, 196224\n"
        "ds_read_b128 v[92:95], v7 offset:6656\n"
        "ds_read_b128 v[96:99], v7 offset:6720\n"
        "ds_read_b128 v[116:119], v7 offset:6784\n"
        "v_mfma_f32_16x16x32_bf16 v[76:79], v[192:195], v[124:127], 0\n"
        "v_cvt_pk_bf16_f32 v80, v24, v25\n"
        "v_cvt_pk_bf16_f32 v81, v26, v27\n"
        "v_mfma_f32_16x16x32_bf16 v[76:79], v[196:199], v[128:131], v[76:79]\n"
        "v_cvt_pk_bf16_f32 v82, v32, v33\n"
        "v_cvt_pk_bf16_f32 v83, v34, v35\n"
        "v_mfma_f32_16x16x32_bf16 v[76:79], v[200:203], v[160:163], v[76:79]\n"
        "buffer_store_dwordx3 v[156:158], v12, s[8:11], s37 offen offset:384 sc0 sc1 nt\n"
        "v_cvt_pk_bf16_f32 v148, v86, v62\n"
        "v_cvt_pk_bf16_f32 v150, v155, v59\n"
        "v_cvt_pk_bf16_f32 v151, v75, v23\n"
        "v_cvt_pk_bf16_f32 v152, v87, v63\n"
        "s_waitcnt lgkmcnt(3)\n"
        "v_mfma_f32_16x16x32_bf16 v[32:35], v[44:47], v[160:163], 0\n"
        "v_cvt_pk_bf16_f32 v72, v68, v36\n"
        "v_cvt_pk_bf16_f32 v84, v76, v77\n"
        "v_smfmac_f32_16x16x64_bf16 v[32:35], v[48:51], v[124:131], v4\n"
        "s_add_u32 s37, s36, 196224\n"
        "v_cvt_pk_bf16_f32 v85, v78, v79\n"
        "v_mfma_f32_16x16x32_bf16 v[24:27], v[44:47], v[52:55], 0\n"
        "v_cvt_pk_bf16_f32 v73, v40, v64\n"
        "v_cvt_pk_bf16_f32 v74, v104, v88\n"
        "v_smfmac_f32_16x16x64_bf16 v[24:27], v[48:51], v[132:139], v4\n"
        "s_waitcnt lgkmcnt(0)\n"
        "s_setprio 2\n"
        "v_cvt_pk_bf16_f32 v156, v69, v37\n"
        "v_cvt_pk_bf16_f32 v157, v41, v65\n"
        "v_mfma_f32_16x16x32_bf16 v[100:103], v[44:47], v[116:119], 0\n"
        "v_cvt_pk_bf16_f32 v158, v105, v89\n"
        "v_cvt_pk_bf16_f32 v76, v70, v38\n"
        "v_smfmac_f32_16x16x64_bf16 v[100:103], v[48:51], v[92:99], v4\n"
        "buffer_store_dwordx3 v[164:166], v13, s[8:11], s37 offen offset:1152 sc0 sc1 nt\n"
        "v_cvt_pk_bf16_f32 v77, v42, v66\n"
        "v_cvt_pk_bf16_f32 v78, v106, v90\n"
        "v_cvt_pk_bf16_f32 v88, v71, v39\n"
        "v_cvt_pk_bf16_f32 v89, v43, v67\n"
        "v_cvt_pk_bf16_f32 v90, v107, v91\n"
        "v_mfma_f32_16x16x32_bf16 v[20:23], v[212:215], v[160:163], 0\n"
        "s_add_u32 s37, s36, 294336\n"
        "ds_read_b64 v[154:155], v11 offset:192\n"
        "v_mfma_f32_16x16x32_bf16 v[20:23], v[208:211], v[128:131], v[20:23]\n"
        "ds_read_b32 v153, v10\n"
        "v_mfma_f32_16x16x32_bf16 v[20:23], v[204:207], v[124:127], v[20:23]\n"
        "v_mfma_f32_16x16x32_bf16 v[28:31], v[216:219], v[124:127], 0\n"
        "v_mfma_f32_16x16x32_bf16 v[28:31], v[220:223], v[128:131], v[28:31]\n"
        "v_mfma_f32_16x16x32_bf16 v[28:31], v[224:227], v[160:163], v[28:31]\n"
        "buffer_store_dwordx3 v[146:148], v12, s[8:11], s37 offen offset:576 sc0 sc1 nt\n"
        "s_add_u32 s37, s36, 392448\n"
        "s_waitcnt lgkmcnt(1)\n"
        "v_add_f32_dpp v155, v34, v35 quad_perm:[0,1,2,3] row_mask:0x1 bank_mask:0xf\n"
        "v_add_f32_dpp v154, v32, v33 quad_perm:[0,1,2,3] row_mask:0x1 bank_mask:0xf\n"
        "v_cvt_pk_bf16_f32 v86, v20, v21\n"
        "v_mfma_f32_16x16x32_bf16 v[44:47], v[236:239], v[160:163], 0\n"
        "v_cvt_pk_bf16_f32 v87, v22, v23\n"
        "v_cvt_pk_bf16_f32 v20, v28, v29\n"
        "v_mfma_f32_16x16x32_bf16 v[44:47], v[232:235], v[128:131], v[44:47]\n"
        "v_cvt_pk_bf16_f32 v21, v30, v31\n"
        "v_add_f32_dpp v155, v26, v27 quad_perm:[0,1,2,3] row_mask:0x2 bank_mask:0xf\n"
        "v_mfma_f32_16x16x32_bf16 v[44:47], v[228:231], v[124:127], v[44:47]\n"
        "buffer_store_dwordx3 v[150:152], v12, s[8:11], s37 offen sc0 sc1 nt\n"
        "v_add_f32_dpp v154, v24, v25 quad_perm:[0,1,2,3] row_mask:0x2 bank_mask:0xf\n"
        "v_add_f32_dpp v155, v102, v103 quad_perm:[0,1,2,3] row_mask:0x4 bank_mask:0xf\n"
        "v_mul_f32 v165, s52, v155\n"
        "v_add_f32_dpp v154, v100, v101 quad_perm:[0,1,2,3] row_mask:0x4 bank_mask:0xf\n"
        "s_waitcnt lgkmcnt(0)\n"
        "v_fma_f32 v165, -v165, v155, v153\n"
        "v_add_f32_e64 v165, s53, |v165|\n"
        "v_cvt_pk_bf16_f32 v23, v46, v47\n"
        "v_cvt_pk_bf16_f32 v22, v44, v45\n"
        "v_mfma_f32_16x16x32_bf16 v[100:103], v[124:127], v[80:83], 0\n"
        "v_rsq_f32 v153, v165\n"
        "v_mfma_f32_16x16x32_bf16 v[100:103], v[128:131], v[84:87], v[100:103]\n"
        "s_add_u32 s37, s36, 392448\n"
        "ds_read_b128 v[104:107], v8 offset:128\n"
        "v_and_b32 v56, s28, v153\n"
        "v_mfma_f32_16x16x32_bf16 v[100:103], v[160:163], v[20:23], v[100:103]\n"
        "ds_read2_b32 v[108:109], v6 offset0:0 offset1:2\n"
        "ds_read2_b32 v[110:111], v6 offset0:8 offset1:10\n"
        "v_mfma_f32_16x16x32_bf16 v[144:147], v[52:55], v[20:23], 0\n"
        "v_and_b32 v164, s28, v153\n"
        "v_sub_f32 v167, v153, v56\n"
        "v_mfma_f32_16x16x32_bf16 v[144:147], v[136:139], v[84:87], v[144:147]\n"
        "v_cvt_pk_bf16_f32 v57, v167, v56\n"
        "v_cvt_pk_bf16_f32 v165, v164, v167\n"
        "v_mfma_f32_16x16x32_bf16 v[144:147], v[132:135], v[80:83], v[144:147]\n"
        "buffer_store_dwordx3 v[72:74], v15, s[8:11], s37 offen offset:768 sc0 sc1 nt\n"
        "v_mul_f32 v154, v154, v153\n"
        "v_readlane_b32 s58, v153, 56\n"
        "s_mov_b64 exec, s[56:57]\n"
        "v_mov_b32 v154, 0xff800000\n"
        "s_mov_b64 exec, -1\n"
        "v_mfma_f32_16x16x4_4b_bf16 v[32:47], v[56:57], v[164:165], 0 blgp:4\n"
        "v_mul_f32 v154, s58, v154\n"
        "v_exp_f32 v154, v154\n"
        "v_mfma_f32_16x16x32_bf16 v[24:27], v[92:95], v[80:83], 0\n"
        "s_add_u32 s37, s36, 490560\n"
        "v_add_f32_dpp v167, v154, v154 row_ror:8 row_mask:0xf bank_mask:0xf\n"
        "v_mul_f32 v154, v154, v153\n"
        "v_mfma_f32_16x16x32_bf16 v[24:27], v[96:99], v[84:87], v[24:27]\n"
        "v_add_f32_dpp v167, v167, v167 row_ror:4 row_mask:0xf bank_mask:0xf\n"
        "v_mfma_f32_16x16x32_bf16 v[24:27], v[116:119], v[20:23], v[24:27]\n"
        "s_waitcnt lgkmcnt(0)\n"
        "v_add_f32_dpp v167, v167, v167 row_ror:2 row_mask:0xf bank_mask:0xf\n"
        "v_mfma_f32_16x16x32_bf16 v[148:151], v[104:107], v[20:23], 0\n"
        "v_mul_f32 v100, v100, v32\n"
        "v_mul_f32 v101, v101, v33\n"
        "v_smfmac_f32_16x16x64_bf16 v[148:151], v[108:111], v[80:87], v4\n"
        "buffer_store_dwordx3 v[156:158], v12, s[8:11], s37 offen offset:192 sc0 sc1 nt\n"
        "v_mul_f32 v102, v102, v34\n"
        "v_mul_f32 v103, v103, v35\n"
        "v_exp_f32 v100, v100\n"
        "v_mul_f32 v144, v144, v36\n"
        "v_mfma_f32_16x16x32_bf16 v[20:23], v[168:171], v[132:135], 0\n"
        "s_add_u32 s37, s36, 490560\n"
        "v_exp_f32 v101, v101\n"
        "v_mfma_f32_16x16x32_bf16 v[20:23], v[172:175], v[136:139], v[20:23]\n"
        "v_exp_f32 v102, v102\n"
        "v_mfma_f32_16x16x32_bf16 v[20:23], v[176:179], v[52:55], v[20:23]\n"
        "v_exp_f32 v103, v103\n"
        "v_add_f32 v151, v100, v101\n"
        "v_mfma_f32_16x16x32_bf16 v[28:31], v[188:191], v[52:55], 0\n"
        "v_add_f32 v151, v151, v102\n"
        "v_add_f32 v151, v151, v103\n"
        "v_mfma_f32_16x16x32_bf16 v[28:31], v[184:187], v[136:139], v[28:31]\n"
        "v_mul_f32 v145, v145, v37\n"
        "v_mul_f32 v146, v146, v38\n"
        "v_mfma_f32_16x16x32_bf16 v[28:31], v[180:183], v[132:135], v[28:31]\n"
        "buffer_store_dwordx3 v[76:78], v14, s[8:11], s37 offen offset:960 sc0 sc1 nt\n"
        "v_cvt_pk_bf16_f32 v68, v20, v21\n"
        "v_cvt_pk_bf16_f32 v69, v22, v23\n"
        "v_mul_f32 v147, v147, v39\n"
        "v_exp_f32 v144, v144\n"
        "v_mfma_f32_16x16x32_bf16 v[20:23], v[192:195], v[132:135], 0\n"
        "s_add_u32 s37, s36, 588672\n"
        "v_exp_f32 v145, v145\n"
        "v_mfma_f32_16x16x32_bf16 v[20:23], v[196:199], v[136:139], v[20:23]\n"
        "v_cvt_pk_bf16_f32 v70, v28, v29\n"
        "v_cvt_pk_bf16_f32 v71, v30, v31\n"
        "v_mfma_f32_16x16x32_bf16 v[20:23], v[200:203], v[52:55], v[20:23]\n"
        "buffer_store_dwordx3 v[88:90], v12, s[8:11], s37 offen offset:384 sc0 sc1 nt\n"
        "v_exp_f32 v146, v146\n"
        "v_exp_f32 v147, v147\n"
        "v_add_f32 v151, v151, v144\n"
        "v_add_f32 v151, v151, v145\n"
        "v_mfma_f32_16x16x32_bf16 v[28:31], v[212:215], v[52:55], 0\n"
        "v_add_f32 v151, v151, v146\n"
        "v_add_f32 v151, v151, v147\n"
        "v_mfma_f32_16x16x32_bf16 v[28:31], v[208:211], v[136:139], v[28:31]\n"
        "v_cvt_pk_bf16_f32 v72, v20, v21\n"
        "v_cvt_pk_bf16_f32 v73, v22, v23\n"
        "v_mfma_f32_16x16x32_bf16 v[28:31], v[204:207], v[132:135], v[28:31]\n"
        "v_mul_f32 v24, v24, v40\n"
        "v_mul_f32 v25, v25, v41\n"
        "v_mul_f32 v26, v26, v42\n"
        "v_mfma_f32_16x16x32_bf16 v[20:23], v[216:219], v[132:135], 0\n"
        "v_mul_f32 v27, v27, v43\n"
        "v_add_f32 v148, v148, v149\n"
        "v_mfma_f32_16x16x32_bf16 v[20:23], v[220:223], v[136:139], v[20:23]\n"
        "v_exp_f32 v24, v24\n"
        "v_mfma_f32_16x16x32_bf16 v[20:23], v[224:227], v[52:55], v[20:23]\n"
        "v_cvt_pk_bf16_f32 v74, v28, v29\n"
        "v_cvt_pk_bf16_f32 v75, v30, v31\n"
        "v_add_f32 v151, v151, v24\n"
        "v_mfma_f32_16x16x32_bf16 v[64:67], v[236:239], v[52:55], 0\n"
        "v_exp_f32 v25, v25\n"
        "v_mfma_f32_16x16x32_bf16 v[64:67], v[232:235], v[136:139], v[64:67]\n"
        "v_exp_f32 v26, v26\n"
        "v_mfma_f32_16x16x32_bf16 v[64:67], v[228:231], v[132:135], v[64:67]\n"
        "v_cvt_pk_bf16_f32 v48, v20, v21\n"
        "v_cvt_pk_bf16_f32 v49, v22, v23\n"
        "v_mfma_f32_16x16x4_4b_bf16 v[28:43], v[56:57], v[164:165], 0 blgp:5\n"
        "v_exp_f32 v27, v27\n"
        "v_add_f32 v151, v151, v25\n"
        "v_add_f32 v151, v151, v26\n"
        "v_mul_f32 v148, v148, v44\n"
        "v_exp_f32 v148, v148\n"
        "v_cvt_pk_bf16_f32 v50, v64, v65\n"
        "v_cvt_pk_bf16_f32 v51, v66, v67\n"
        "v_mfma_f32_16x16x32_bf16 v[58:61], v[124:127], v[68:71], 0\n"
        "v_add_f32 v151, v151, v27\n"
        "v_add_f32_dpp v167, v167, v167 row_ror:1 row_mask:0xf bank_mask:0xf\n"
        "v_mfma_f32_16x16x32_bf16 v[58:61], v[128:131], v[72:75], v[58:61]\n"
        "v_add_f32_dpp v151, v148, v151 quad_perm:[0,1,2,3] row_mask:0x4 bank_mask:0xf\n"
        "v_mfma_f32_16x16x32_bf16 v[58:61], v[160:163], v[48:51], v[58:61]\n"
        "v_mfma_f32_16x16x32_bf16 v[112:115], v[52:55], v[48:51], 0\n"
        "v_permlane32_swap_b32 v151, v167\n"
        "v_mfma_f32_16x16x32_bf16 v[112:115], v[136:139], v[72:75], v[112:115]\n"
        "v_add_f32 v151, v151, v167\n"
        "v_mov_b32 v167, v151\n"
        "v_mfma_f32_16x16x32_bf16 v[112:115], v[132:135], v[68:71], v[112:115]\n"
        "v_mfma_f32_16x16x32_bf16 v[20:23], v[92:95], v[68:71], 0\n"
        "v_permlane16_swap_b32 v151, v167\n"
        "v_mfma_f32_16x16x32_bf16 v[20:23], v[96:99], v[72:75], v[20:23]\n"
        "v_add_f32 v151, v151, v167\n"
        "v_mul_f32 v58, v58, v28\n"
        "v_mfma_f32_16x16x32_bf16 v[20:23], v[116:119], v[48:51], v[20:23]\n"
        "v_rcp_f32 v123, v151\n"
        "v_mul_f32 v59, v59, v29\n"
        "v_and_b32 v158, s28, v123\n"
        "v_mfma_f32_16x16x32_bf16 v[62:65], v[104:107], v[48:51], 0\n"
        "v_sub_f32 v152, v123, v158\n"
        "v_cvt_pk_bf16_f32 v159, v158, v152\n"
        "v_smfmac_f32_16x16x64_bf16 v[62:65], v[108:111], v[68:75], v4\n"
        "v_min3_f32 v167, v151, v123, v153\n"
        "v_mfma_f32_16x16x32_bf16 v[48:51], v[168:171], v[92:95], 0\n"
        "v_mul_f32 v60, v60, v30\n"
        "v_mul_f32 v61, v61, v31\n"
        "v_mfma_f32_16x16x32_bf16 v[48:51], v[172:175], v[96:99], v[48:51]\n"
        "v_exp_f32 v58, v58\n"
        "v_mfma_f32_16x16x32_bf16 v[48:51], v[176:179], v[116:119], v[48:51]\n"
        "v_exp_f32 v59, v59\n"
        "v_exp_f32 v60, v60\n"
        "v_mfma_f32_16x16x32_bf16 v[68:71], v[188:191], v[116:119], 0\n"
        "v_exp_f32 v61, v61\n"
        "v_mfma_f32_16x16x32_bf16 v[68:71], v[184:187], v[96:99], v[68:71]\n"
        "v_add_f32 v155, v58, v59\n"
        "v_add_f32 v155, v155, v60\n"
        "v_mfma_f32_16x16x32_bf16 v[68:71], v[180:183], v[92:95], v[68:71]\n"
        "v_cvt_pk_bf16_f32 v44, v48, v49\n"
        "v_cvt_pk_bf16_f32 v45, v50, v51\n"
        "v_add_f32 v155, v155, v61\n"
        "v_mfma_f32_16x16x32_bf16 v[28:31], v[192:195], v[92:95], 0\n"
        "v_mul_f32 v112, v112, v32\n"
        "v_mul_f32 v113, v113, v33\n"
        "v_mfma_f32_16x16x32_bf16 v[28:31], v[196:199], v[96:99], v[28:31]\n"
        "v_mul_f32 v114, v114, v34\n"
        "v_cvt_pk_bf16_f32 v46, v68, v69\n"
        "v_mfma_f32_16x16x32_bf16 v[28:31], v[200:203], v[116:119], v[28:31]\n"
        "v_cvt_pk_bf16_f32 v47, v70, v71\n"
        "v_mul_f32 v115, v115, v35\n"
        "v_exp_f32 v112, v112\n"
        "v_mfma_f32_16x16x32_bf16 v[68:71], v[212:215], v[116:119], 0\n"
        "v_exp_f32 v113, v113\n"
        "v_mfma_f32_16x16x32_bf16 v[68:71], v[208:211], v[96:99], v[68:71]\n"
        "v_exp_f32 v114, v114\n"
        "v_mfma_f32_16x16x32_bf16 v[68:71], v[204:207], v[92:95], v[68:71]\n"
        "v_cvt_pk_bf16_f32 v48, v28, v29\n"
        "v_cvt_pk_bf16_f32 v49, v30, v31\n"
        "v_add_f32 v155, v155, v112\n"
        "v_mfma_f32_16x16x32_bf16 v[64:67], v[216:219], v[92:95], 0\n"
        "v_exp_f32 v115, v115\n"
        "v_mfma_f32_16x16x32_bf16 v[64:67], v[220:223], v[96:99], v[64:67]\n"
        "v_add_f32 v155, v155, v113\n"
        "v_add_f32 v155, v155, v114\n"
        "v_mfma_f32_16x16x32_bf16 v[64:67], v[224:227], v[116:119], v[64:67]\n"
        "v_cvt_pk_bf16_f32 v50, v68, v69\n"
        "v_cvt_pk_bf16_f32 v51, v70, v71\n"
        "v_add_f32 v155, v155, v115\n"
        "v_mul_f32 v20, v20, v36\n"
        "v_mfma_f32_16x16x32_bf16 v[28:31], v[236:239], v[116:119], 0\n"
        "v_mul_f32 v21, v21, v37\n"
        "v_mul_f32 v22, v22, v38\n"
        "v_mfma_f32_16x16x32_bf16 v[28:31], v[232:235], v[96:99], v[28:31]\n"
        "v_cvt_pk_bf16_f32 v150, v64, v65\n"
        "v_cvt_pk_bf16_f32 v151, v66, v67\n"
        "v_mfma_f32_16x16x32_bf16 v[28:31], v[228:231], v[92:95], v[28:31]\n"
        "v_exp_f32 v20, v20\n"
        "v_mfma_f32_16x16x4_4b_bf16 v[76:91], v[56:57], v[158:159], 0 blgp:4\n"
        "v_exp_f32 v21, v21\n"
        "v_mul_f32 v23, v23, v39\n"
        "v_exp_f32 v22, v22\n"
        "v_exp_f32 v23, v23\n"
        "v_add_f32 v155, v155, v20\n"
        "v_add_f32 v155, v155, v21\n"
        "v_cvt_pk_bf16_f32 v152, v28, v29\n"
        "v_cvt_pk_bf16_f32 v153, v30, v31\n"
        "v_mfma_f32_16x16x32_bf16 v[36:39], v[124:127], v[44:47], 0\n"
        "v_add_f32 v155, v155, v22\n"
        "v_add_f32 v155, v155, v23\n"
        "v_mfma_f32_16x16x32_bf16 v[36:39], v[128:131], v[48:51], v[36:39]\n"
        "v_mul_f32 v100, v100, v76\n"
        "v_mul_f32 v101, v101, v77\n"
        "v_mfma_f32_16x16x32_bf16 v[36:39], v[160:163], v[150:153], v[36:39]\n"
        "v_mul_f32 v102, v102, v78\n"
        "v_mul_f32 v103, v103, v79\n"
        "v_mul_f32 v144, v144, v80\n"
        "v_mul_f32 v145, v145, v81\n"
        "v_mfma_f32_16x16x32_bf16 v[140:143], v[52:55], v[150:153], 0\n"
        "v_mul_f32 v146, v146, v82\n"
        "v_mul_f32 v147, v147, v83\n"
        "v_mfma_f32_16x16x32_bf16 v[140:143], v[136:139], v[48:51], v[140:143]\n"
        "v_mul_f32 v24, v24, v84\n"
        "v_mul_f32 v25, v25, v85\n"
        "v_mfma_f32_16x16x32_bf16 v[140:143], v[132:135], v[44:47], v[140:143]\n"
        "v_add_f32 v62, v62, v63\n"
        "v_mul_f32 v62, v62, v40\n"
        "v_mul_f32 v26, v26, v86\n"
        "v_mfma_f32_16x16x4_4b_bf16 v[64:79], v[56:57], v[164:165], 0 blgp:6\n"
        "v_exp_f32 v62, v62\n"
        "v_mul_f32 v27, v27, v87\n"
        "v_fma_f32 v148, v148, v88, v19\n"
        "v_add_f32_dpp v155, v62, v155 quad_perm:[0,1,2,3] row_mask:0x4 bank_mask:0xf\n"
        "v_cvt_pk_bf16_f32 v40, v100, v101\n"
        "v_cvt_pk_bf16_f32 v41, v102, v103\n"
        "v_cvt_pk_bf16_f32 v42, v144, v145\n"
        "v_cvt_pk_bf16_f32 v43, v146, v147\n"
        "v_cvt_pk_bf16_f32 v164, v24, v25\n"
        "v_mfma_f32_16x16x32_bf16 v[156:159], v[92:95], v[44:47], 0\n"
        "v_cvt_pk_bf16_f32 v166, v26, v27\n"
        "v_cvt_pk_bf16_f32 v165, v148, 0\n"
        "v_mfma_f32_16x16x32_bf16 v[156:159], v[96:99], v[48:51], v[156:159]\n"
        "v_mul_f32 v36, v36, v64\n"
        "v_mul_f32 v37, v37, v65\n"
        "v_mfma_f32_16x16x32_bf16 v[156:159], v[116:119], v[150:153], v[156:159]\n"
        "v_mul_f32 v38, v38, v66\n"
        "v_mul_f32 v39, v39, v67\n"
        "v_mul_f32 v140, v140, v68\n"
        "v_mfma_f32_16x16x32_bf16 v[92:95], v[104:107], v[150:153], 0\n"
        "ds_read_b128 v[88:91], v17 offset:0\n"
        "v_mul_f32 v141, v141, v69\n"
        "v_smfmac_f32_16x16x64_bf16 v[92:95], v[108:111], v[44:51], v4\n"
        "ds_read_b128 v[84:87], v17 offset:64\n"
        "ds_read_b128 v[150:153], v17 offset:128\n"
        "v_mfma_f32_16x16x32_bf16 v[48:51], v[124:127], v[240:243], 0\n"
        "v_exp_f32 v36, v36\n"
        "v_mfma_f32_16x16x32_bf16 v[48:51], v[128:131], v[244:247], v[48:51]\n"
        "ds_read_b128 v[80:83], v0 offset:5136\n"
        "v_mul_f32 v142, v142, v70\n"
        "v_mfma_f32_16x16x32_bf16 v[48:51], v[160:163], v[248:251], v[48:51]\n"
        "ds_read_b128 v[32:35], v0 offset:4112\n"
        "v_mul_f32 v143, v143, v71\n"
        "v_mfma_f32_16x16x32_bf16 v[24:27], v[52:55], v[248:251], 0\n"
        "v_exp_f32 v37, v37\n"
        "v_mfma_f32_16x16x32_bf16 v[24:27], v[136:139], v[244:247], v[24:27]\n"
        "v_exp_f32 v38, v38\n"
        "v_mfma_f32_16x16x32_bf16 v[24:27], v[132:135], v[240:243], v[24:27]\n"
        "s_waitcnt lgkmcnt(2)\n"
        "v_cvt_pk_bf16_f32 v44, v48, v49\n"
        "v_cvt_pk_bf16_f32 v45, v50, v51\n"
        "v_add_f32 v145, v36, v37\n"
        "v_mfma_f32_16x16x32_bf16 v[104:107], v[88:91], v[240:243], 0\n"
        "v_exp_f32 v39, v39\n"
        "v_mfma_f32_16x16x32_bf16 v[104:107], v[84:87], v[244:247], v[104:107]\n"
        "v_add_f32 v145, v145, v38\n"
        "v_cvt_pk_bf16_f32 v46, v24, v25\n"
        "v_mfma_f32_16x16x32_bf16 v[104:107], v[150:153], v[248:251], v[104:107]\n"
        "s_waitcnt lgkmcnt(0)\n"
        "v_cvt_pk_bf16_f32 v47, v26, v27\n"
        "v_add_f32 v145, v145, v39\n"
        "v_exp_f32 v140, v140\n"
        "v_mfma_f32_16x16x32_bf16 v[64:67], v[150:153], v[80:83], 0\n"
        "v_exp_f32 v141, v141\n"
        "v_mfma_f32_16x16x32_bf16 v[64:67], v[84:87], v[32:35], v[64:67]\n"
        "ds_read_b128 v[146:149], v0 offset:6176\n"
        "v_cvt_pk_bf16_f32 v116, v104, v105\n"
        "v_mfma_f32_16x16x32_bf16 v[64:67], v[88:91], v[252:255], v[64:67]\n"
        "ds_read_b128 v[24:27], v0 offset:7200\n"
        "ds_read_b128 v[108:111], v0 offset:8224\n"
        "v_mfma_f32_16x16x32_bf16 v[100:103], v[132:135], v[252:255], 0\n"
        "v_cvt_pk_bf16_f32 v120, v106, v107\n"
        "v_add_f32 v145, v145, v140\n"
        "v_mfma_f32_16x16x32_bf16 v[100:103], v[136:139], v[32:35], v[100:103]\n"
        "v_exp_f32 v142, v142\n"
        "v_mfma_f32_16x16x32_bf16 v[100:103], v[52:55], v[80:83], v[100:103]\n"
        "v_cvt_pk_bf16_f32 v94, v64, v65\n"
        "v_cvt_pk_bf16_f32 v98, v66, v67\n"
        "v_add_f32 v145, v145, v141\n"
        "v_mfma_f32_16x16x32_bf16 v[68:71], v[160:163], v[80:83], 0\n"
        "v_exp_f32 v143, v143\n"
        "v_mfma_f32_16x16x32_bf16 v[68:71], v[128:131], v[32:35], v[68:71]\n"
        "v_add_f32 v145, v145, v142\n"
        "v_add_f32 v145, v145, v143\n"
        "v_mfma_f32_16x16x32_bf16 v[68:71], v[124:127], v[252:255], v[68:71]\n"
        "s_waitcnt lgkmcnt(0)\n"
        "v_cvt_pk_bf16_f32 v106, v100, v101\n"
        "v_cvt_pk_bf16_f32 v107, v102, v103\n"
        "v_mul_f32 v156, v156, v72\n"
        "v_mul_f32 v157, v157, v73\n"
        "v_mfma_f32_16x16x32_bf16 v[32:35], v[124:127], v[146:149], 0\n"
        "v_mul_f32 v158, v158, v74\n"
        "v_mul_f32 v159, v159, v75\n"
        "v_mfma_f32_16x16x32_bf16 v[32:35], v[128:131], v[24:27], v[32:35]\n"
        "ds_read_b128 v[48:51], v0 offset:11312\n"
        "v_cvt_pk_bf16_f32 v104, v68, v69\n"
        "v_mfma_f32_16x16x32_bf16 v[32:35], v[160:163], v[108:111], v[32:35]\n"
        "ds_read_b128 v[80:83], v0 offset:10288\n"
        "ds_read_b128 v[64:67], v0 offset:9264\n"
        "v_mfma_f32_16x16x32_bf16 v[100:103], v[52:55], v[108:111], 0\n"
        "v_cvt_pk_bf16_f32 v105, v70, v71\n"
        "v_add_f32 v92, v92, v93\n"
        "v_mfma_f32_16x16x32_bf16 v[100:103], v[136:139], v[24:27], v[100:103]\n"
        "v_exp_f32 v156, v156\n"
        "v_mfma_f32_16x16x32_bf16 v[100:103], v[132:135], v[146:149], v[100:103]\n"
        "v_cvt_pk_bf16_f32 v28, v32, v33\n"
        "v_cvt_pk_bf16_f32 v29, v34, v35\n"
        "v_add_f32 v145, v145, v156\n"
        "v_mfma_f32_16x16x32_bf16 v[70:73], v[88:91], v[146:149], 0\n"
        "v_exp_f32 v157, v157\n"
        "v_mfma_f32_16x16x32_bf16 v[70:73], v[84:87], v[24:27], v[70:73]\n"
        "v_exp_f32 v158, v158\n"
        "v_mfma_f32_16x16x32_bf16 v[70:73], v[150:153], v[108:111], v[70:73]\n"
        "s_waitcnt lgkmcnt(0)\n"
        "v_cvt_pk_bf16_f32 v30, v100, v101\n"
        "v_cvt_pk_bf16_f32 v31, v102, v103\n"
        "v_exp_f32 v159, v159\n"
        "v_mfma_f32_16x16x32_bf16 v[32:35], v[150:153], v[48:51], 0\n"
        "v_add_f32 v145, v145, v157\n"
        "v_add_f32 v145, v145, v158\n"
        "v_mfma_f32_16x16x32_bf16 v[32:35], v[84:87], v[80:83], v[32:35]\n"
        "ds_read_b128 v[100:103], v0 offset:12352\n"
        "v_cvt_pk_bf16_f32 v144, v70, v71\n"
        "v_mfma_f32_16x16x32_bf16 v[32:35], v[88:91], v[64:67], v[32:35]\n"
        "ds_read_b128 v[108:111], v0 offset:13376\n"
        "ds_read_b128 v[24:27], v0 offset:14400\n"
        "v_mfma_f32_16x16x32_bf16 v[68:71], v[132:135], v[64:67], 0\n"
        "v_cvt_pk_bf16_f32 v148, v72, v73\n"
        "v_mul_f32 v92, v92, v76\n"
        "v_mfma_f32_16x16x32_bf16 v[68:71], v[136:139], v[80:83], v[68:71]\n"
        "v_add_f32 v145, v145, v159\n"
        "v_readlane_b32 s59, v123, 32\n"
        "v_mfma_f32_16x16x32_bf16 v[68:71], v[52:55], v[48:51], v[68:71]\n"
        "v_cvt_pk_bf16_f32 v117, v32, v33\n"
        "v_cvt_pk_bf16_f32 v121, v34, v35\n"
        "v_exp_f32 v92, v92\n"
        "v_mfma_f32_16x16x32_bf16 v[72:75], v[160:163], v[48:51], 0\n"
        "v_mul_f32 v154, s59, v154\n"
        "v_add_f32_dpp v145, v92, v145 quad_perm:[0,1,2,3] row_mask:0x4 bank_mask:0xf\n"
        "v_mfma_f32_16x16x32_bf16 v[72:75], v[128:131], v[80:83], v[72:75]\n"
        "v_cvt_pk_bf16_f32 v154, v154, 0\n"
        "v_cvt_pk_bf16_f32 v50, v68, v69\n"
        "v_mfma_f32_16x16x32_bf16 v[72:75], v[124:127], v[64:67], v[72:75]\n"
        "s_waitcnt lgkmcnt(0)\n"
        "v_cvt_pk_bf16_f32 v51, v70, v71\n"
        "v_permlane32_swap_b32 v155, v145\n"
        "v_mfma_f32_16x16x32_bf16 v[80:83], v[124:127], v[100:103], 0\n"
        "v_add_f32 v155, v155, v145\n"
        "v_mov_b32 v149, v155\n"
        "v_mfma_f32_16x16x32_bf16 v[80:83], v[128:131], v[108:111], v[80:83]\n"
        "ds_read_b128 v[32:35], v0 offset:17488\n"
        "v_cvt_pk_bf16_f32 v48, v72, v73\n"
        "v_mfma_f32_16x16x32_bf16 v[80:83], v[160:163], v[24:27], v[80:83]\n"
        "ds_read_b128 v[68:71], v0 offset:16464\n"
        "ds_read_b128 v[64:67], v0 offset:15440\n"
        "v_mfma_f32_16x16x32_bf16 v[76:79], v[52:55], v[24:27], 0\n"
        "v_cvt_pk_bf16_f32 v49, v74, v75\n"
        "v_mfma_f32_16x16x32_bf16 v[76:79], v[136:139], v[108:111], v[76:79]\n"
        "v_permlane16_swap_b32 v155, v149\n"
        "v_mfma_f32_16x16x32_bf16 v[76:79], v[132:135], v[100:103], v[76:79]\n"
        "v_add_f32 v155, v155, v149\n"
        "v_rcp_f32 v147, v155\n"
        "v_mfma_f32_16x16x32_bf16 v[72:75], v[88:91], v[100:103], 0\n"
        "ds_read2_b32 v[122:123], v11 offset0:0 offset1:3\n"
        "v_min3_f32 v167, v167, v155, v147\n"
        "v_mfma_f32_16x16x32_bf16 v[72:75], v[84:87], v[108:111], v[72:75]\n"
        "ds_read_b64_tr_b16 v[118:119], v9 offset:0\n"
        "v_and_b32 v102, s28, v147\n"
        "v_mfma_f32_16x16x32_bf16 v[72:75], v[150:153], v[24:27], v[72:75]\n"
        "s_waitcnt lgkmcnt(2)\n"
        "ds_read2_b32 v[100:101], v11 offset0:1 offset1:4\n"
        "v_sub_f32 v149, v147, v102\n"
        "v_cvt_pk_bf16_f32 v103, v102, v149\n"
        "v_cvt_pk_bf16_f32 v108, v80, v81\n"
        "v_mfma_f32_16x16x32_bf16 v[24:27], v[150:153], v[32:35], 0\n"
        "ds_read_b64_tr_b16 v[96:97], v9 offset:16\n"
        "v_cvt_pk_bf16_f32 v109, v82, v83\n"
        "v_mfma_f32_16x16x32_bf16 v[24:27], v[84:87], v[68:71], v[24:27]\n"
        "ds_read2_b32 v[150:151], v11 offset0:2 offset1:5\n"
        "v_cvt_pk_bf16_f32 v110, v76, v77\n"
        "v_mfma_f32_16x16x32_bf16 v[24:27], v[88:91], v[64:67], v[24:27]\n"
        "ds_read_b64_tr_b16 v[146:147], v9 offset:32\n"
        "v_cvt_pk_bf16_f32 v111, v78, v79\n"
        "v_cvt_pk_bf16_f32 v95, v72, v73\n"
        "v_mfma_f32_16x16x4_4b_bf16 v[76:91], v[56:57], v[102:103], 0 blgp:4\n"
        "v_cvt_pk_bf16_f32 v99, v74, v75\n"
        "v_cmp_nle_f32 vcc, 0x2b800000, v167\n"
        "v_mov_b32_dpp v167, v19 row_shr:4 row_mask:0xf bank_mask:0xf bound_ctrl:0\n"
        "ds_write_b16 v1, v154\n"
        "v_cvt_pk_bf16_f32 v145, v24, v25\n"
        "v_cvt_pk_bf16_f32 v149, v26, v27\n"
        "s_andn2_b64 vcc, vcc, s[56:57]\n"
        "s_cbranch_scc1 CREDOA_%=\n"
        "v_mfma_f32_16x16x32_bf16 v[24:27], v[132:135], v[64:67], 0\n"
        "s_mov_b32 exec_hi, 0xffff\n"
        "v_add_u32 v10, 40064, v10\n"
        "s_mov_b32 exec_hi, -1\n"
        "v_mfma_f32_16x16x32_bf16 v[24:27], v[136:139], v[68:71], v[24:27]\n"
        "v_mul_f32 v58, v58, v76\n"
        "v_mul_f32 v59, v59, v77\n"
        "v_mfma_f32_16x16x32_bf16 v[24:27], v[52:55], v[32:35], v[24:27]\n"
        "s_add_u32 s31, s31, 1\n"
        "v_mul_f32 v60, v60, v78\n"
        "v_mul_f32 v61, v61, v79\n"
        "v_mul_f32 v112, v112, v80\n"
        "v_mul_f32 v113, v113, v81\n"
        "v_mfma_f32_16x16x32_bf16 v[52:55], v[160:163], v[32:35], 0\n"
        "s_cmp_ge_u32 s31, s71\n"
        "v_mul_f32 v114, v114, v82\n"
        "v_mul_f32 v115, v115, v83\n"
        "v_mfma_f32_16x16x32_bf16 v[52:55], v[128:131], v[68:71], v[52:55]\n"
        "s_cbranch_scc1 L34A_%=\n"
        "L35A_%=:\n"
        "v_mul_f32 v20, v20, v84\n"
        "v_cvt_pk_bf16_f32 v34, v24, v25\n"
        "v_mfma_f32_16x16x32_bf16 v[52:55], v[124:127], v[64:67], v[52:55]\n"
        "s_add_u32 s16, s16, 256\n"
        "v_cvt_pk_bf16_f32 v35, v26, v27\n"
        "v_mul_f32 v21, v21, v85\n"
        "v_mfma_f32_16x16x32_bf16 v[124:127], v[40:43], v[44:47], 0\n"
        "s_waitcnt lgkmcnt(0)\n"
        "v_mul_f32 v22, v22, v86\n"
        "v_mul_f32 v23, v23, v87\n"
        "v_smfmac_f32_16x16x64_bf16 v[124:127], v[164:167], v[116:123], v18\n"
        "s_cmp_ge_u32 s31, 20\n"
        "s_cselect_b32 s39, s22, s16\n"
        "ds_read_b64_tr_b16 v[118:119], v9 offset:48\n"
        "v_mfma_f32_16x16x4_4b_bf16 v[72:87], v[56:57], v[102:103], 0 blgp:6\n"
        "v_cvt_pk_bf16_f32 v32, v52, v53\n"
        "v_cvt_pk_bf16_f32 v33, v54, v55\n"
        "v_fma_f32 v62, v62, v88, v19\n"
        "v_cvt_pk_bf16_f32 v68, v58, v59\n"
        "v_cvt_pk_bf16_f32 v69, v60, v61\n"
        "v_cvt_pk_bf16_f32 v70, v112, v113\n"
        "v_cvt_pk_bf16_f32 v71, v114, v115\n"
        "v_cvt_pk_bf16_f32 v24, v20, v21\n"
        "v_cvt_pk_bf16_f32 v26, v22, v23\n"
        "v_mfma_f32_16x16x32_bf16 v[88:91], v[40:43], v[104:107], 0\n"
        "s_cmp_ge_u32 s31, 21\n"
        "v_cvt_pk_bf16_f32 v25, v62, 0\n"
        "v_mul_f32 v36, v36, v72\n"
        "v_smfmac_f32_16x16x64_bf16 v[88:91], v[164:167], v[94:101], v18\n"
        "s_cselect_b32 s39, s23, s39\n"
        "ds_read_b64_tr_b16 v[96:97], v9 offset:64\n"
        "v_mfma_f32_16x16x32_bf16 v[56:59], v[40:43], v[28:31], 0\n"
        "s_mul_hi_u32 s38, s39, 58835169\n"
        "ds_read_b128 v[128:131], v2\n"
        "v_mul_f32 v37, v37, v73\n"
        "v_smfmac_f32_16x16x64_bf16 v[56:59], v[164:167], v[144:151], v18\n"
        "s_mul_i32 s39, s39, 1344\n"
        "ds_read_b64_tr_b16 v[146:147], v9 offset:80\n"
        "ds_read_b128 v[52:55], v3\n"
        "v_mfma_f32_16x16x32_bf16 v[112:115], v[40:43], v[48:51], 0\n"
        "s_waitcnt lgkmcnt(4)\n"
        "v_mul_f32 v38, v38, v74\n"
        "v_mul_f32 v39, v39, v75\n"
        "v_smfmac_f32_16x16x64_bf16 v[112:115], v[164:167], v[116:123], v18 abid:1\n"
        "s_mul_i32 s38, s38, 588672\n"
        "s_add_u32 s39, s39, s38\n"
        "v_mul_f32 v140, v140, v76\n"
        "v_cvt_pk_bf16_f32 v152, v124, v88\n"
        "v_mul_f32 v141, v141, v77\n"
        "v_mfma_f32_16x16x32_bf16 v[72:75], v[40:43], v[108:111], 0\n"
        "s_add_u32 s39, s39, s17\n"
        "v_mov_b64 v[60:61], 0\n"
        "s_waitcnt lgkmcnt(3)\n"
        "v_smfmac_f32_16x16x64_bf16 v[72:75], v[164:167], v[94:101], v18 abid:1\n"
        "s_mul_i32 s68, s31, 3\n"
        "v_mov_b64 v[62:63], 0\n"
        "v_cvt_pk_bf16_f32 v153, v56, v112\n"
        "v_mfma_f32_16x16x32_bf16 v[64:67], v[40:43], v[32:35], 0\n"
        "s_waitcnt lgkmcnt(1)\n"
        "v_mov_b64 v[20:21], 0\n"
        "v_mov_b64 v[22:23], 0\n"
        "v_smfmac_f32_16x16x64_bf16 v[64:67], v[164:167], v[144:151], v18 abid:1\n"
        "s_waitcnt lgkmcnt(0)\n"
        "s_bitcmp1_b32 s31, 0\n"
        "v_mov_b32_dpp v27, v19 row_shr:4 row_mask:0xf bank_mask:0xf bound_ctrl:0\n"
        "s_cselect_b32 s38, 3, 10\n"
        "v_smfmac_f32_16x16x64_bf16 v[60:63], v[128:131], v[44:51], v4\n"
        "s_add_u32 s68, s68, s38\n"
        "v_mov_b64 v[40:41], 0\n"
        "v_smfmac_f32_16x16x64_bf16 v[60:63], v[52:55], v[116:123], v4 abid:1\n"
        "s_add_u32 s37, s35, 0\n"
        "s_cmp_lt_u32 s31, s14\n"
        "ds_read_b64_tr_b16 v[118:119], v9 offset:3328\n"
        "v_mov_b64 v[42:43], 0\n"
        "v_smfmac_f32_16x16x64_bf16 v[20:23], v[128:131], v[104:111], v4\n"
        "s_cselect_b32 s68, s68, 0\n"
        "v_cvt_pk_bf16_f32 v154, v72, v64\n"
        "v_smfmac_f32_16x16x64_bf16 v[20:23], v[52:55], v[94:101], v4 abid:1\n"
        "s_and_b32 s38, s31, 7\n"
        "ds_read_b64_tr_b16 v[96:97], v9 offset:3344\n"
        "v_cvt_pk_bf16_f32 v160, v125, v89\n"
        "v_smfmac_f32_16x16x64_bf16 v[40:43], v[128:131], v[28:35], v4\n"
        "s_cselect_b32 s30, 1, -7\n"
        "v_cvt_pk_bf16_f32 v88, v126, v90\n"
        "v_smfmac_f32_16x16x64_bf16 v[40:43], v[52:55], v[144:151], v4 abid:1\n"
        "buffer_store_dwordx3 v[152:154], v12, s[8:11], s37 offen sc0 sc1 nt\n"
        "ds_read_b64_tr_b16 v[146:147], v9 offset:3360\n"
        "v_cvt_pk_bf16_f32 v164, v127, v91\n"
        "v_cvt_pk_bf16_f32 v161, v57, v113\n"
        "v_cvt_pk_bf16_f32 v89, v58, v114\n"
        "v_cvt_pk_bf16_f32 v165, v59, v115\n"
        "v_cvt_pk_bf16_f32 v162, v73, v65\n"
        "v_mfma_f32_16x16x32_bf16 v[152:155], v[68:71], v[44:47], 0\n"
        "s_waitcnt lgkmcnt(2)\n"
        "v_cvt_pk_bf16_f32 v90, v74, v66\n"
        "v_cvt_pk_bf16_f32 v166, v75, v67\n"
        "v_smfmac_f32_16x16x64_bf16 v[152:155], v[24:27], v[116:123], v18\n"
        "s_mul_i32 s29, s30, 192\n"
        "s_mul_i32 s30, s30, 392\n"
        "ds_read_b64_tr_b16 v[118:119], v9 offset:3376\n"
        "v_add_f32 v60, v60, v61\n"
        "v_add_f32 v62, v62, v63\n"
        "v_add_f32 v20, v20, v21\n"
        "v_mfma_f32_16x16x32_bf16 v[56:59], v[68:71], v[104:107], 0\n"
        "s_waitcnt lgkmcnt(2)\n"
        "v_add_f32 v22, v22, v23\n"
        "v_cvt_pk_bf16_f32 v112, v60, v20\n"
        "v_smfmac_f32_16x16x64_bf16 v[56:59], v[24:27], v[94:101], v18\n"
        "s_add_u32 s37, s35, 0\n"
        "s_cmp_eq_u32 s69, s18\n"
        "ds_read_b64_tr_b16 v[96:97], v9 offset:3392\n"
        "v_add_f32 v40, v40, v41\n"
        "v_add_f32 v42, v42, v43\n"
        "v_cvt_pk_bf16_f32 v113, v40, v62\n"
        "v_mfma_f32_16x16x32_bf16 v[72:75], v[68:71], v[28:31], 0\n"
        "s_waitcnt lgkmcnt(2)\n"
        "v_cvt_pk_bf16_f32 v114, v22, v42\n"
        "v_mul_f32 v142, v142, v78\n"
        "v_smfmac_f32_16x16x64_bf16 v[72:75], v[24:27], v[144:151], v18\n"
        "buffer_store_dwordx3 v[160:162], v15, s[8:11], s37 offen offset:768 sc0 sc1 nt\n"
        "ds_read_b64_tr_b16 v[146:147], v9 offset:3408\n"
        "s_cselect_b32 s34, -7488, s70\n"
        "v_mul_f32 v143, v143, v79\n"
        "v_mul_f32 v156, v156, v80\n"
        "v_mul_f32 v157, v157, v81\n"
        "v_mul_f32 v158, v158, v82\n"
        "v_mfma_f32_16x16x32_bf16 v[20:23], v[68:71], v[48:51], 0\n"
        "s_waitcnt lgkmcnt(2)\n"
        "s_add_u32 s69, s69, s34\n"
        "v_mul_f32 v159, v159, v83\n"
        "v_smfmac_f32_16x16x64_bf16 v[20:23], v[24:27], v[116:123], v18 abid:1\n"
        "s_add_u32 s37, s35, 98112\n"
        "ds_read_b64_tr_b16 v[118:119], v9 offset:6656\n"
        "v_fma_f32 v92, v92, v84, v19\n"
        "v_mfma_f32_16x16x32_bf16 v[84:87], v[68:71], v[108:111], 0\n"
        "s_waitcnt lgkmcnt(2)\n"
        "v_cvt_pk_bf16_f32 v80, v36, v37\n"
        "v_cvt_pk_bf16_f32 v81, v38, v39\n"
        "v_smfmac_f32_16x16x64_bf16 v[84:87], v[24:27], v[94:101], v18 abid:1\n"
        "ds_read_b64_tr_b16 v[96:97], v9 offset:6672\n"
        "v_cvt_pk_bf16_f32 v82, v140, v141\n"
        "v_mfma_f32_16x16x32_bf16 v[60:63], v[68:71], v[32:35], 0\n"
        "s_waitcnt lgkmcnt(2)\n"
        "v_cvt_pk_bf16_f32 v83, v142, v143\n"
        "v_cvt_pk_bf16_f32 v76, v156, v157\n"
        "v_smfmac_f32_16x16x64_bf16 v[60:63], v[24:27], v[144:151], v18 abid:1\n"
        "buffer_store_dwordx3 v[88:90], v12, s[8:11], s37 offen offset:192 sc0 sc1 nt\n"
        "ds_read_b64_tr_b16 v[146:147], v9 offset:6688\n"
        "ds_read_b32 v167, v0 offset:21604\n"
        "v_cvt_pk_bf16_f32 v78, v158, v159\n"
        "v_cvt_pk_bf16_f32 v77, v92, 0\n"
        "v_mov_b32_dpp v79, v19 row_shr:4 row_mask:0xf bank_mask:0xf bound_ctrl:0\n"
        "v_mfma_f32_16x16x32_bf16 v[68:71], v[80:83], v[44:47], 0\n"
        "s_waitcnt lgkmcnt(3)\n"
        "v_writelane_b32 v10, s69, 56\n"
        "v_add_u32 v11, s30, v11\n"
        "v_smfmac_f32_16x16x64_bf16 v[68:71], v[76:79], v[116:123], v18\n"
        "s_add_u32 s37, s35, 98112\n"
        "ds_read_b64_tr_b16 v[118:119], v9 offset:6704\n"
        "v_mfma_f32_16x16x32_bf16 v[36:39], v[80:83], v[104:107], 0\n"
        "s_waitcnt lgkmcnt(3)\n"
        "v_smfmac_f32_16x16x64_bf16 v[36:39], v[76:79], v[94:101], v18\n"
        "ds_read_b64_tr_b16 v[96:97], v9 offset:6720\n"
        "v_mfma_f32_16x16x32_bf16 v[40:43], v[80:83], v[28:31], 0\n"
        "s_waitcnt lgkmcnt(3)\n"
        "s_waitcnt lgkmcnt(2)\n"
        "v_readfirstlane_b32 s40, v167\n"
        "v_smfmac_f32_16x16x64_bf16 v[40:43], v[76:79], v[144:151], v18\n"
        "buffer_store_dwordx3 v[164:166], v14, s[8:11], s37 offen offset:960 sc0 sc1 nt\n"
        "ds_read_b64_tr_b16 v[146:147], v9 offset:6736\n"
        "s_add_u32 s37, s35, 588672\n"
        "s_add_u32 s46, s37, 1344\n"
        "s_cmp_lt_i32 s40, s68\n"
        "s_mov_b64 exec, 1\n"
        "ds_inc_u32 v0, v1 offset:21608\n"
        "s_mov_b64 exec, -1\n"
        "s_cbranch_scc1 L38A_%=\n"
        "L39A_%=:\n"
        "ds_read_b128 v[124:127], v7 offset:40064\n"
        "v_mfma_f32_16x16x32_bf16 v[64:67], v[80:83], v[48:51], 0\n"
        "s_waitcnt lgkmcnt(4)\n"
        "ds_read_b128 v[132:135], v7 offset:43392\n"
        "v_smfmac_f32_16x16x64_bf16 v[64:67], v[76:79], v[116:123], v18 abid:1\n"
        "buffer_store_dwordx3 v[112:114], v12, s[44:47], s37 offen offset:1152 sc0 sc1 nt\n"
        "ds_read_b128 v[128:131], v7 offset:40128\n"
        "ds_read_b128 v[136:139], v7 offset:43456\n"
        "ds_read_b128 v[160:163], v7 offset:40192\n"
        "ds_read_b128 v[52:55], v7 offset:43520\n"
        "v_mfma_f32_16x16x32_bf16 v[104:107], v[80:83], v[108:111], 0\n"
        "s_waitcnt lgkmcnt(7)\n"
        "s_mov_b32 s36, s35\n"
        "v_smfmac_f32_16x16x64_bf16 v[104:107], v[76:79], v[94:101], v18 abid:1\n"
        "s_mov_b64 exec, s[62:63]\n"
        "v_add_u32 v16, s29, v16\n"
        "v_add_u32 v8, s34, v8\n"
        "s_mov_b64 exec, -1\n"
        "v_mfma_f32_16x16x32_bf16 v[88:91], v[80:83], v[32:35], 0\n"
        "s_mov_b32 s35, s39\n"
        "v_smfmac_f32_16x16x64_bf16 v[88:91], v[76:79], v[144:151], v18 abid:1\n"
        "s_mov_b64 exec, s[50:51]\n"
        "v_add_u32 v5, s29, v5\n"
        "v_add_u32 v6, s34, v6\n"
        "s_mov_b64 exec, -1\n"
        "s_setprio 0\n"
        "CNEXTA_%=:\n"
        "s_cmp_ge_u32 s31, s14\n"
        "s_cbranch_scc1 CEND_%=\n"
        "s_waitcnt lgkmcnt(5)\n"
        "v_mfma_f32_16x16x32_bf16 v[24:27], v[168:171], v[124:127], 0\n"
        "v_cvt_pk_bf16_f32 v156, v152, v56\n"
        "v_cvt_pk_bf16_f32 v157, v72, v20\n"
        "s_waitcnt lgkmcnt(3)\n"
        "v_mfma_f32_16x16x32_bf16 v[24:27], v[172:175], v[128:131], v[24:27]\n"
        "ds_read_b128 v[44:47], v16 offset:128\n"
        "v_cvt_pk_bf16_f32 v158, v84, v60\n"
        "s_waitcnt lgkmcnt(2)\n"
        "v_mfma_f32_16x16x32_bf16 v[24:27], v[176:179], v[160:163], v[24:27]\n"
        "ds_read_b128 v[48:51], v5\n"
        "v_cvt_pk_bf16_f32 v164, v153, v57\n"
        "v_mfma_f32_16x16x32_bf16 v[32:35], v[188:191], v[160:163], 0\n"
        "v_cvt_pk_bf16_f32 v165, v73, v21\n"
        "v_cvt_pk_bf16_f32 v166, v85, v61\n"
        "v_mfma_f32_16x16x32_bf16 v[32:35], v[184:187], v[128:131], v[32:35]\n"
        "v_cvt_pk_bf16_f32 v146, v154, v58\n"
        "v_cvt_pk_bf16_f32 v147, v74, v22\n"
        "v_mfma_f32_16x16x32_bf16 v[32:35], v[180:183], v[124:127], v[32:35]\n"
        "s_add_u32 s37, s36, 196224\n"
        "ds_read_b128 v[92:95], v7 offset:46720\n"
        "ds_read_b128 v[96:99], v7 offset:46784\n"
        "ds_read_b128 v[116:119], v7 offset:46848\n"
        "v_mfma_f32_16x16x32_bf16 v[76:79], v[192:195], v[124:127], 0\n"
        "v_cvt_pk_bf16_f32 v80, v24, v25\n"
        "v_cvt_pk_bf16_f32 v81, v26, v27\n"
        "v_mfma_f32_16x16x32_bf16 v[76:79], v[196:199], v[128:131], v[76:79]\n"
        "v_cvt_pk_bf16_f32 v82, v32, v33\n"
        "v_cvt_pk_bf16_f32 v83, v34, v35\n"
        "v_mfma_f32_16x16x32_bf16 v[76:79], v[200:203], v[160:163], v[76:79]\n"
        "buffer_store_dwordx3 v[156:158], v12, s[8:11], s37 offen offset:384 sc0 sc1 nt\n"
        "v_cvt_pk_bf16_f32 v148, v86, v62\n"
        "v_cvt_pk_bf16_f32 v150, v155, v59\n"
        "v_cvt_pk_bf16_f32 v151, v75, v23\n"
        "v_cvt_pk_bf16_f32 v152, v87, v63\n"
        "s_waitcnt lgkmcnt(3)\n"
        "v_mfma_f32_16x16x32_bf16 v[32:35], v[44:47], v[160:163], 0\n"
        "v_cvt_pk_bf16_f32 v72, v68, v36\n"
        "v_cvt_pk_bf16_f32 v84, v76, v77\n"
        "v_smfmac_f32_16x16x64_bf16 v[32:35], v[48:51], v[124:131], v4\n"
        "s_add_u32 s37, s36, 196224\n"
        "v_cvt_pk_bf16_f32 v85, v78, v79\n"
        "v_mfma_f32_16x16x32_bf16 v[24:27], v[44:47], v[52:55], 0\n"
        "v_cvt_pk_bf16_f32 v73, v40, v64\n"
        "v_cvt_pk_bf16_f32 v74, v104, v88\n"
        "v_smfmac_f32_16x16x64_bf16 v[24:27], v[48:51], v[132:139], v4\n"
        "s_waitcnt lgkmcnt(0)\n"
        "s_setprio 2\n"
        "v_cvt_pk_bf16_f32 v156, v69, v37\n"
        "v_cvt_pk_bf16_f32 v157, v41, v65\n"
        "v_mfma_f32_16x16x32_bf16 v[100:103], v[44:47], v[116:119], 0\n"
        "v_cvt_pk_bf16_f32 v158, v105, v89\n"
        "v_cvt_pk_bf16_f32 v76, v70, v38\n"
        "v_smfmac_f32_16x16x64_bf16 v[100:103], v[48:51], v[92:99], v4\n"
        "buffer_store_dwordx3 v[164:166], v13, s[8:11], s37 offen offset:1152 sc0 sc1 nt\n"
        "v_cvt_pk_bf16_f32 v77, v42, v66\n"
        "v_cvt_pk_bf16_f32 v78, v106, v90\n"
        "v_cvt_pk_bf16_f32 v88, v71, v39\n"
        "v_cvt_pk_bf16_f32 v89, v43, v67\n"
        "v_cvt_pk_bf16_f32 v90, v107, v91\n"
        "v_mfma_f32_16x16x32_bf16 v[20:23], v[212:215], v[160:163], 0\n"
        "s_add_u32 s37, s36, 294336\n"
        "ds_read_b64 v[154:155], v11 offset:192\n"
        "v_mfma_f32_16x16x32_bf16 v[20:23], v[208:211], v[128:131], v[20:23]\n"
        "ds_read_b32 v153, v10\n"
        "v_mfma_f32_16x16x32_bf16 v[20:23], v[204:207], v[124:127], v[20:23]\n"
        "v_mfma_f32_16x16x32_bf16 v[28:31], v[216:219], v[124:127], 0\n"
        "v_mfma_f32_16x16x32_bf16 v[28:31], v[220:223], v[128:131], v[28:31]\n"
        "v_mfma_f32_16x16x32_bf16 v[28:31], v[224:227], v[160:163], v[28:31]\n"
        "buffer_store_dwordx3 v[146:148], v12, s[8:11], s37 offen offset:576 sc0 sc1 nt\n"
        "s_add_u32 s37, s36, 392448\n"
        "s_waitcnt lgkmcnt(1)\n"
        "v_add_f32_dpp v155, v34, v35 quad_perm:[0,1,2,3] row_mask:0x1 bank_mask:0xf\n"
        "v_add_f32_dpp v154, v32, v33 quad_perm:[0,1,2,3] row_mask:0x1 bank_mask:0xf\n"
        "v_cvt_pk_bf16_f32 v86, v20, v21\n"
        "v_mfma_f32_16x16x32_bf16 v[44:47], v[236:239], v[160:163], 0\n"
        "v_cvt_pk_bf16_f32 v87, v22, v23\n"
        "v_cvt_pk_bf16_f32 v20, v28, v29\n"
        "v_mfma_f32_16x16x32_bf16 v[44:47], v[232:235], v[128:131], v[44:47]\n"
        "v_cvt_pk_bf16_f32 v21, v30, v31\n"
        "v_add_f32_dpp v155, v26, v27 quad_perm:[0,1,2,3] row_mask:0x2 bank_mask:0xf\n"
        "v_mfma_f32_16x16x32_bf16 v[44:47], v[228:231], v[124:127], v[44:47]\n"
        "buffer_store_dwordx3 v[150:152], v12, s[8:11], s37 offen sc0 sc1 nt\n"
        "v_add_f32_dpp v154, v24, v25 quad_perm:[0,1,2,3] row_mask:0x2 bank_mask:0xf\n"
        "v_add_f32_dpp v155, v102, v103 quad_perm:[0,1,2,3] row_mask:0x4 bank_mask:0xf\n"
        "v_mul_f32 v165, s52, v155\n"
        "v_add_f32_dpp v154, v100, v101 quad_perm:[0,1,2,3] row_mask:0x4 bank_mask:0xf\n"
        "s_waitcnt lgkmcnt(0)\n"
        "v_fma_f32 v165, -v165, v155, v153\n"
        "v_add_f32_e64 v165, s53, |v165|\n"
        "v_cvt_pk_bf16_f32 v23, v46, v47\n"
        "v_cvt_pk_bf16_f32 v22, v44, v45\n"
        "v_mfma_f32_16x16x32_bf16 v[100:103], v[124:127], v[80:83], 0\n"
        "v_rsq_f32 v153, v165\n"
        "v_mfma_f32_16x16x32_bf16 v[100:103], v[128:131], v[84:87], v[100:103]\n"
        "s_add_u32 s37, s36, 392448\n"
        "ds_read_b128 v[104:107], v8 offset:128\n"
        "v_and_b32 v56, s28, v153\n"
        "v_mfma_f32_16x16x32_bf16 v[100:103], v[160:163], v[20:23], v[100:103]\n"
        "ds_read2_b32 v[108:109], v6 offset0:0 offset1:2\n"
        "ds_read2_b32 v[110:111], v6 offset0:8 offset1:10\n"
        "v_mfma_f32_16x16x32_bf16 v[144:147], v[52:55], v[20:23], 0\n"
        "v_and_b32 v164, s28, v153\n"
        "v_sub_f32 v167, v153, v56\n"
        "v_mfma_f32_16x16x32_bf16 v[144:147], v[136:139], v[84:87], v[144:147]\n"
        "v_cvt_pk_bf16_f32 v57, v167, v56\n"
        "v_cvt_pk_bf16_f32 v165, v164, v167\n"
        "v_mfma_f32_16x16x32_bf16 v[144:147], v[132:135], v[80:83], v[144:147]\n"
        "buffer_store_dwordx3 v[72:74], v15, s[8:11], s37 offen offset:768 sc0 sc1 nt\n"
        "v_mul_f32 v154, v154, v153\n"
        "v_readlane_b32 s58, v153, 56\n"
        "s_mov_b64 exec, s[56:57]\n"
        "v_mov_b32 v154, 0xff800000\n"
        "s_mov_b64 exec, -1\n"
        "v_mfma_f32_16x16x4_4b_bf16 v[32:47], v[56:57], v[164:165], 0 blgp:4\n"
        "v_mul_f32 v154, s58, v154\n"
        "v_exp_f32 v154, v154\n"
        "v_mfma_f32_16x16x32_bf16 v[24:27], v[92:95], v[80:83], 0\n"
        "s_add_u32 s37, s36, 490560\n"
        "v_add_f32_dpp v167, v154, v154 row_ror:8 row_mask:0xf bank_mask:0xf\n"
        "v_mul_f32 v154, v154, v153\n"
        "v_mfma_f32_16x16x32_bf16 v[24:27], v[96:99], v[84:87], v[24:27]\n"
        "v_add_f32_dpp v167, v167, v167 row_ror:4 row_mask:0xf bank_mask:0xf\n"
        "v_mfma_f32_16x16x32_bf16 v[24:27], v[116:119], v[20:23], v[24:27]\n"
        "s_waitcnt lgkmcnt(0)\n"
        "v_add_f32_dpp v167, v167, v167 row_ror:2 row_mask:0xf bank_mask:0xf\n"
        "v_mfma_f32_16x16x32_bf16 v[148:151], v[104:107], v[20:23], 0\n"
        "v_mul_f32 v100, v100, v32\n"
        "v_mul_f32 v101, v101, v33\n"
        "v_smfmac_f32_16x16x64_bf16 v[148:151], v[108:111], v[80:87], v4\n"
        "buffer_store_dwordx3 v[156:158], v12, s[8:11], s37 offen offset:192 sc0 sc1 nt\n"
        "v_mul_f32 v102, v102, v34\n"
        "v_mul_f32 v103, v103, v35\n"
        "v_exp_f32 v100, v100\n"
        "v_mul_f32 v144, v144, v36\n"
        "v_mfma_f32_16x16x32_bf16 v[20:23], v[168:171], v[132:135], 0\n"
        "s_add_u32 s37, s36, 490560\n"
        "v_exp_f32 v101, v101\n"
        "v_mfma_f32_16x16x32_bf16 v[20:23], v[172:175], v[136:139], v[20:23]\n"
        "v_exp_f32 v102, v102\n"
        "v_mfma_f32_16x16x32_bf16 v[20:23], v[176:179], v[52:55], v[20:23]\n"
        "v_exp_f32 v103, v103\n"
        "v_add_f32 v151, v100, v101\n"
        "v_mfma_f32_16x16x32_bf16 v[28:31], v[188:191], v[52:55], 0\n"
        "v_add_f32 v151, v151, v102\n"
        "v_add_f32 v151, v151, v103\n"
        "v_mfma_f32_16x16x32_bf16 v[28:31], v[184:187], v[136:139], v[28:31]\n"
        "v_mul_f32 v145, v145, v37\n"
        "v_mul_f32 v146, v146, v38\n"
        "v_mfma_f32_16x16x32_bf16 v[28:31], v[180:183], v[132:135], v[28:31]\n"
        "buffer_store_dwordx3 v[76:78], v14, s[8:11], s37 offen offset:960 sc0 sc1 nt\n"
        "v_cvt_pk_bf16_f32 v68, v20, v21\n"
        "v_cvt_pk_bf16_f32 v69, v22, v23\n"
        "v_mul_f32 v147, v147, v39\n"
        "v_exp_f32 v144, v144\n"
        "v_mfma_f32_16x16x32_bf16 v[20:23], v[192:195], v[132:135], 0\n"
        "s_add_u32 s37, s36, 588672\n"
        "v_exp_f32 v145, v145\n"
        "v_mfma_f32_16x16x32_bf16 v[20:23], v[196:199], v[136:139], v[20:23]\n"
        "v_cvt_pk_bf16_f32 v70, v28, v29\n"
        "v_cvt_pk_bf16_f32 v71, v30, v31\n"
        "v_mfma_f32_16x16x32_bf16 v[20:23], v[200:203], v[52:55], v[20:23]\n"
        "buffer_store_dwordx3 v[88:90], v12, s[8:11], s37 offen offset:384 sc0 sc1 nt\n"
        "v_exp_f32 v146, v146\n"
        "v_exp_f32 v147, v147\n"
        "v_add_f32 v151, v151, v144\n"
        "v_add_f32 v151, v151, v145\n"
        "v_mfma_f32_16x16x32_bf16 v[28:31], v[212:215], v[52:55], 0\n"
        "v_add_f32 v151, v151, v146\n"
        "v_add_f32 v151, v151, v147\n"
        "v_mfma_f32_16x16x32_bf16 v[28:31], v[208:211], v[136:139], v[28:31]\n"
        "v_cvt_pk_bf16_f32 v72, v20, v21\n"
        "v_cvt_pk_bf16_f32 v73, v22, v23\n"
        "v_mfma_f32_16x16x32_bf16 v[28:31], v[204:207], v[132:135], v[28:31]\n"
        "v_mul_f32 v24, v24, v40\n"
        "v_mul_f32 v25, v25, v41\n"
        "v_mul_f32 v26, v26, v42\n"
        "v_mfma_f32_16x16x32_bf16 v[20:23], v[216:219], v[132:135], 0\n"
        "v_mul_f32 v27, v27, v43\n"
        "v_add_f32 v148, v148, v149\n"
        "v_mfma_f32_16x16x32_bf16 v[20:23], v[220:223], v[136:139], v[20:23]\n"
        "v_exp_f32 v24, v24\n"
        "v_mfma_f32_16x16x32_bf16 v[20:23], v[224:227], v[52:55], v[20:23]\n"
        "v_cvt_pk_bf16_f32 v74, v28, v29\n"
        "v_cvt_pk_bf16_f32 v75, v30, v31\n"
        "v_add_f32 v151, v151, v24\n"
        "v_mfma_f32_16x16x32_bf16 v[64:67], v[236:239], v[52:55], 0\n"
        "v_exp_f32 v25, v25\n"
        "v_mfma_f32_16x16x32_bf16 v[64:67], v[232:235], v[136:139], v[64:67]\n"
        "v_exp_f32 v26, v26\n"
        "v_mfma_f32_16x16x32_bf16 v[64:67], v[228:231], v[132:135], v[64:67]\n"
        "v_cvt_pk_bf16_f32 v48, v20, v21\n"
        "v_cvt_pk_bf16_f32 v49, v22, v23\n"
        "v_mfma_f32_16x16x4_4b_bf16 v[28:43], v[56:57], v[164:165], 0 blgp:5\n"
        "v_exp_f32 v27, v27\n"
        "v_add_f32 v151, v151, v25\n"
        "v_add_f32 v151, v151, v26\n"
        "v_mul_f32 v148, v148, v44\n"
        "v_exp_f32 v148, v148\n"
        "v_cvt_pk_bf16_f32 v50, v64, v65\n"
        "v_cvt_pk_bf16_f32 v51, v66, v67\n"
        "v_mfma_f32_16x16x32_bf16 v[58:61], v[124:127], v[68:71], 0\n"
        "v_add_f32 v151, v151, v27\n"
        "v_add_f32_dpp v167, v167, v167 row_ror:1 row_mask:0xf bank_mask:0xf\n"
        "v_mfma_f32_16x16x32_bf16 v[58:61], v[128:131], v[72:75], v[58:61]\n"
        "v_add_f32_dpp v151, v148, v151 quad_perm:[0,1,2,3] row_mask:0x4 bank_mask:0xf\n"
        "v_mfma_f32_16x16x32_bf16 v[58:61], v[160:163], v[48:51], v[58:61]\n"
        "v_mfma_f32_16x16x32_bf16 v[112:115], v[52:55], v[48:51], 0\n"
        "v_permlane32_swap_b32 v151, v167\n"
        "v_mfma_f32_16x16x32_bf16 v[112:115], v[136:139], v[72:75], v[112:115]\n"
        "v_add_f32 v151, v151, v167\n"
        "v_mov_b32 v167, v151\n"
        "v_mfma_f32_16x16x32_bf16 v[112:115], v[132:135], v[68:71], v[112:115]\n"
        "v_mfma_f32_16x16x32_bf16 v[20:23], v[92:95], v[68:71], 0\n"
        "v_permlane16_swap_b32 v151, v167\n"
        "v_mfma_f32_16x16x32_bf16 v[20:23], v[96:99], v[72:75], v[20:23]\n"
        "v_add_f32 v151, v151, v167\n"
        "v_mul_f32 v58, v58, v28\n"
        "v_mfma_f32_16x16x32_bf16 v[20:23], v[116:119], v[48:51], v[20:23]\n"
        "v_rcp_f32 v123, v151\n"
        "v_mul_f32 v59, v59, v29\n"
        "v_and_b32 v158, s28, v123\n"
        "v_mfma_f32_16x16x32_bf16 v[62:65], v[104:107], v[48:51], 0\n"
        "v_sub_f32 v152, v123, v158\n"
        "v_cvt_pk_bf16_f32 v159, v158, v152\n"
        "v_smfmac_f32_16x16x64_bf16 v[62:65], v[108:111], v[68:75], v4\n"
        "v_min3_f32 v167, v151, v123, v153\n"
        "v_mfma_f32_16x16x32_bf16 v[48:51], v[168:171], v[92:95], 0\n"
        "v_mul_f32 v60, v60, v30\n"
        "v_mul_f32 v61, v61, v31\n"
        "v_mfma_f32_16x16x32_bf16 v[48:51], v[172:175], v[96:99], v[48:51]\n"
        "v_exp_f32 v58, v58\n"
        "v_mfma_f32_16x16x32_bf16 v[48:51], v[176:179], v[116:119], v[48:51]\n"
        "v_exp_f32 v59, v59\n"
        "v_exp_f32 v60, v60\n"
        "v_mfma_f32_16x16x32_bf16 v[68:71], v[188:191], v[116:119], 0\n"
        "v_exp_f32 v61, v61\n"
        "v_mfma_f32_16x16x32_bf16 v[68:71], v[184:187], v[96:99], v[68:71]\n"
        "v_add_f32 v155, v58, v59\n"
        "v_add_f32 v155, v155, v60\n"
        "v_mfma_f32_16x16x32_bf16 v[68:71], v[180:183], v[92:95], v[68:71]\n"
        "v_cvt_pk_bf16_f32 v44, v48, v49\n"
        "v_cvt_pk_bf16_f32 v45, v50, v51\n"
        "v_add_f32 v155, v155, v61\n"
        "v_mfma_f32_16x16x32_bf16 v[28:31], v[192:195], v[92:95], 0\n"
        "v_mul_f32 v112, v112, v32\n"
        "v_mul_f32 v113, v113, v33\n"
        "v_mfma_f32_16x16x32_bf16 v[28:31], v[196:199], v[96:99], v[28:31]\n"
        "v_mul_f32 v114, v114, v34\n"
        "v_cvt_pk_bf16_f32 v46, v68, v69\n"
        "v_mfma_f32_16x16x32_bf16 v[28:31], v[200:203], v[116:119], v[28:31]\n"
        "v_cvt_pk_bf16_f32 v47, v70, v71\n"
        "v_mul_f32 v115, v115, v35\n"
        "v_exp_f32 v112, v112\n"
        "v_mfma_f32_16x16x32_bf16 v[68:71], v[212:215], v[116:119], 0\n"
        "v_exp_f32 v113, v113\n"
        "v_mfma_f32_16x16x32_bf16 v[68:71], v[208:211], v[96:99], v[68:71]\n"
        "v_exp_f32 v114, v114\n"
        "v_mfma_f32_16x16x32_bf16 v[68:71], v[204:207], v[92:95], v[68:71]\n"
        "v_cvt_pk_bf16_f32 v48, v28, v29\n"
        "v_cvt_pk_bf16_f32 v49, v30, v31\n"
        "v_add_f32 v155, v155, v112\n"
        "v_mfma_f32_16x16x32_bf16 v[64:67], v[216:219], v[92:95], 0\n"
        "v_exp_f32 v115, v115\n"
        "v_mfma_f32_16x16x32_bf16 v[64:67], v[220:223], v[96:99], v[64:67]\n"
        "v_add_f32 v155, v155, v113\n"
        "v_add_f32 v155, v155, v114\n"
        "v_mfma_f32_16x16x32_bf16 v[64:67], v[224:227], v[116:119], v[64:67]\n"
        "v_cvt_pk_bf16_f32 v50, v68, v69\n"
        "v_cvt_pk_bf16_f32 v51, v70, v71\n"
        "v_add_f32 v155, v155, v115\n"
        "v_mul_f32 v20, v20, v36\n"
        "v_mfma_f32_16x16x32_bf16 v[28:31], v[236:239], v[116:119], 0\n"
        "v_mul_f32 v21, v21, v37\n"
        "v_mul_f32 v22, v22, v38\n"
        "v_mfma_f32_16x16x32_bf16 v[28:31], v[232:235], v[96:99], v[28:31]\n"
        "v_cvt_pk_bf16_f32 v150, v64, v65\n"
        "v_cvt_pk_bf16_f32 v151, v66, v67\n"
        "v_mfma_f32_16x16x32_bf16 v[28:31], v[228:231], v[92:95], v[28:31]\n"
        "v_exp_f32 v20, v20\n"
        "v_mfma_f32_16x16x4_4b_bf16 v[76:91], v[56:57], v[158:159], 0 blgp:4\n"
        "v_exp_f32 v21, v21\n"
        "v_mul_f32 v23, v23, v39\n"
        "v_exp_f32 v22, v22\n"
        "v_exp_f32 v23, v23\n"
        "v_add_f32 v155, v155, v20\n"
        "v_add_f32 v155, v155, v21\n"
        "v_cvt_pk_bf16_f32 v152, v28, v29\n"
        "v_cvt_pk_bf16_f32 v153, v30, v31\n"
        "v_mfma_f32_16x16x32_bf16 v[36:39], v[124:127], v[44:47], 0\n"
        "v_add_f32 v155, v155, v22\n"
        "v_add_f32 v155, v155, v23\n"
        "v_mfma_f32_16x16x32_bf16 v[36:39], v[128:131], v[48:51], v[36:39]\n"
        "v_mul_f32 v100, v100, v76\n"
        "v_mul_f32 v101, v101, v77\n"
        "v_mfma_f32_16x16x32_bf16 v[36:39], v[160:163], v[150:153], v[36:39]\n"
        "v_mul_f32 v102, v102, v78\n"
        "v_mul_f32 v103, v103, v79\n"
        "v_mul_f32 v144, v144, v80\n"
        "v_mul_f32 v145, v145, v81\n"
        "v_mfma_f32_16x16x32_bf16 v[140:143], v[52:55], v[150:153], 0\n"
        "v_mul_f32 v146, v146, v82\n"
        "v_mul_f32 v147, v147, v83\n"
        "v_mfma_f32_16x16x32_bf16 v[140:143], v[136:139], v[48:51], v[140:143]\n"
        "v_mul_f32 v24, v24, v84\n"
        "v_mul_f32 v25, v25, v85\n"
        "v_mfma_f32_16x16x32_bf16 v[140:143], v[132:135], v[44:47], v[140:143]\n"
        "v_add_f32 v62, v62, v63\n"
        "v_mul_f32 v62, v62, v40\n"
        "v_mul_f32 v26, v26, v86\n"
        "v_mfma_f32_16x16x4_4b_bf16 v[64:79], v[56:57], v[164:165], 0 blgp:6\n"
        "v_exp_f32 v62, v62\n"
        "v_mul_f32 v27, v27, v87\n"
        "v_fma_f32 v148, v148, v88, v19\n"
        "v_add_f32_dpp v155, v62, v155 quad_perm:[0,1,2,3] row_mask:0x4 bank_mask:0xf\n"
        "v_cvt_pk_bf16_f32 v40, v100, v101\n"
        "v_cvt_pk_bf16_f32 v41, v102, v103\n"
        "v_cvt_pk_bf16_f32 v42, v144, v145\n"
        "v_cvt_pk_bf16_f32 v43, v146, v147\n"
        "v_cvt_pk_bf16_f32 v164, v24, v25\n"
        "v_mfma_f32_16x16x32_bf16 v[156:159], v[92:95], v[44:47], 0\n"
        "v_cvt_pk_bf16_f32 v166, v26, v27\n"
        "v_cvt_pk_bf16_f32 v165, v148, 0\n"
        "v_mfma_f32_16x16x32_bf16 v[156:159], v[96:99], v[48:51], v[156:159]\n"
        "v_mul_f32 v36, v36, v64\n"
        "v_mul_f32 v37, v37, v65\n"
        "v_mfma_f32_16x16x32_bf16 v[156:159], v[116:119], v[150:153], v[156:159]\n"
        "v_mul_f32 v38, v38, v66\n"
        "v_mul_f32 v39, v39, v67\n"
        "v_mul_f32 v140, v140, v68\n"
        "v_mfma_f32_16x16x32_bf16 v[92:95], v[104:107], v[150:153], 0\n"
        "ds_read_b128 v[88:91], v17 offset:40064\n"
        "v_mul_f32 v141, v141, v69\n"
        "v_smfmac_f32_16x16x64_bf16 v[92:95], v[108:111], v[44:51], v4\n"
        "ds_read_b128 v[84:87], v17 offset:40128\n"
        "ds_read_b128 v[150:153], v17 offset:40192\n"
        "v_mfma_f32_16x16x32_bf16 v[48:51], v[124:127], v[240:243], 0\n"
        "v_exp_f32 v36, v36\n"
        "v_mfma_f32_16x16x32_bf16 v[48:51], v[128:131], v[244:247], v[48:51]\n"
        "ds_read_b128 v[80:83], v0 offset:5136\n"
        "v_mul_f32 v142, v142, v70\n"
        "v_mfma_f32_16x16x32_bf16 v[48:51], v[160:163], v[248:251], v[48:51]\n"
        "ds_read_b128 v[32:35], v0 offset:4112\n"
        "v_mul_f32 v143, v143, v71\n"
        "v_mfma_f32_16x16x32_bf16 v[24:27], v[52:55], v[248:251], 0\n"
        "v_exp_f32 v37, v37\n"
        "v_mfma_f32_16x16x32_bf16 v[24:27], v[136:139], v[244:247], v[24:27]\n"
        "v_exp_f32 v38, v38\n"
        "v_mfma_f32_16x16x32_bf16 v[24:27], v[132:135], v[240:243], v[24:27]\n"
        "s_waitcnt lgkmcnt(2)\n"
        "v_cvt_pk_bf16_f32 v44, v48, v49\n"
        "v_cvt_pk_bf16_f32 v45, v50, v51\n"
        "v_add_f32 v145, v36, v37\n"
        "v_mfma_f32_16x16x32_bf16 v[104:107], v[88:91], v[240:243], 0\n"
        "v_exp_f32 v39, v39\n"
        "v_mfma_f32_16x16x32_bf16 v[104:107], v[84:87], v[244:247], v[104:107]\n"
        "v_add_f32 v145, v145, v38\n"
        "v_cvt_pk_bf16_f32 v46, v24, v25\n"
        "v_mfma_f32_16x16x32_bf16 v[104:107], v[150:153], v[248:251], v[104:107]\n"
        "s_waitcnt lgkmcnt(0)\n"
        "v_cvt_pk_bf16_f32 v47, v26, v27\n"
        "v_add_f32 v145, v145, v39\n"
        "v_exp_f32 v140, v140\n"
        "v_mfma_f32_16x16x32_bf16 v[64:67], v[150:153], v[80:83], 0\n"
        "v_exp_f32 v141, v141\n"
        "v_mfma_f32_16x16x32_bf16 v[64:67], v[84:87], v[32:35], v[64:67]\n"
        "ds_read_b128 v[146:149], v0 offset:6176\n"
        "v_cvt_pk_bf16_f32 v116, v104, v105\n"
        "v_mfma_f32_16x16x32_bf16 v[64:67], v[88:91], v[252:255], v[64:67]\n"
        "ds_read_b128 v[24:27], v0 offset:7200\n"
        "ds_read_b128 v[108:111], v0 offset:8224\n"
        "v_mfma_f32_16x16x32_bf16 v[100:103], v[132:135], v[252:255], 0\n"
        "v_cvt_pk_bf16_f32 v120, v106, v107\n"
        "v_add_f32 v145, v145, v140\n"
        "v_mfma_f32_16x16x32_bf16 v[100:103], v[136:139], v[32:35], v[100:103]\n"
        "v_exp_f32 v142, v142\n"
        "v_mfma_f32_16x16x32_bf16 v[100:103], v[52:55], v[80:83], v[100:103]\n"
        "v_cvt_pk_bf16_f32 v94, v64, v65\n"
        "v_cvt_pk_bf16_f32 v98, v66, v67\n"
        "v_add_f32 v145, v145, v141\n"
        "v_mfma_f32_16x16x32_bf16 v[68:71], v[160:163], v[80:83], 0\n"
        "v_exp_f32 v143, v143\n"
        "v_mfma_f32_16x16x32_bf16 v[68:71], v[128:131], v[32:35], v[68:71]\n"
        "v_add_f32 v145, v145, v142\n"
        "v_add_f32 v145, v145, v143\n"
        "v_mfma_f32_16x16x32_bf16 v[68:71], v[124:127], v[252:255], v[68:71]\n"
        "s_waitcnt lgkmcnt(0)\n"
        "v_cvt_pk_bf16_f32 v106, v100, v101\n"
        "v_cvt_pk_bf16_f32 v107, v102, v103\n"
        "v_mul_f32 v156, v156, v72\n"
        "v_mul_f32 v157, v157, v73\n"
        "v_mfma_f32_16x16x32_bf16 v[32:35], v[124:127], v[146:149], 0\n"
        "v_mul_f32 v158, v158, v74\n"
        "v_mul_f32 v159, v159, v75\n"
        "v_mfma_f32_16x16x32_bf16 v[32:35], v[128:131], v[24:27], v[32:35]\n"
        "ds_read_b128 v[48:51], v0 offset:11312\n"
        "v_cvt_pk_bf16_f32 v104, v68, v69\n"
        "v_mfma_f32_16x16x32_bf16 v[32:35], v[160:163], v[108:111], v[32:35]\n"
        "ds_read_b128 v[80:83], v0 offset:10288\n"
        "ds_read_b128 v[64:67], v0 offset:9264\n"
        "v_mfma_f32_16x16x32_bf16 v[100:103], v[52:55], v[108:111], 0\n"
        "v_cvt_pk_bf16_f32 v105, v70, v71\n"
        "v_add_f32 v92, v92, v93\n"
        "v_mfma_f32_16x16x32_bf16 v[100:103], v[136:139], v[24:27], v[100:103]\n"
        "v_exp_f32 v156, v156\n"
        "v_mfma_f32_16x16x32_bf16 v[100:103], v[132:135], v[146:149], v[100:103]\n"
        "v_cvt_pk_bf16_f32 v28, v32, v33\n"
        "v_cvt_pk_bf16_f32 v29, v34, v35\n"
        "v_add_f32 v145, v145, v156\n"
        "v_mfma_f32_16x16x32_bf16 v[70:73], v[88:91], v[146:149], 0\n"
        "v_exp_f32 v157, v157\n"
        "v_mfma_f32_16x16x32_bf16 v[70:73], v[84:87], v[24:27], v[70:73]\n"
        "v_exp_f32 v158, v158\n"
        "v_mfma_f32_16x16x32_bf16 v[70:73], v[150:153], v[108:111], v[70:73]\n"
        "s_waitcnt lgkmcnt(0)\n"
        "v_cvt_pk_bf16_f32 v30, v100, v101\n"
        "v_cvt_pk_bf16_f32 v31, v102, v103\n"
        "v_exp_f32 v159, v159\n"
        "v_mfma_f32_16x16x32_bf16 v[32:35], v[150:153], v[48:51], 0\n"
        "v_add_f32 v145, v145, v157\n"
        "v_add_f32 v145, v145, v158\n"
        "v_mfma_f32_16x16x32_bf16 v[32:35], v[84:87], v[80:83], v[32:35]\n"
        "ds_read_b128 v[100:103], v0 offset:12352\n"
        "v_cvt_pk_bf16_f32 v144, v70, v71\n"
        "v_mfma_f32_16x16x32_bf16 v[32:35], v[88:91], v[64:67], v[32:35]\n"
        "ds_read_b128 v[108:111], v0 offset:13376\n"
        "ds_read_b128 v[24:27], v0 offset:14400\n"
        "v_mfma_f32_16x16x32_bf16 v[68:71], v[132:135], v[64:67], 0\n"
        "v_cvt_pk_bf16_f32 v148, v72, v73\n"
        "v_mul_f32 v92, v92, v76\n"
        "v_mfma_f32_16x16x32_bf16 v[68:71], v[136:139], v[80:83], v[68:71]\n"
        "v_add_f32 v145, v145, v159\n"
        "v_readlane_b32 s59, v123, 32\n"
        "v_mfma_f32_16x16x32_bf16 v[68:71], v[52:55], v[48:51], v[68:71]\n"
        "v_cvt_pk_bf16_f32 v117, v32, v33\n"
        "v_cvt_pk_bf16_f32 v121, v34, v35\n"
        "v_exp_f32 v92, v92\n"
        "v_mfma_f32_16x16x32_bf16 v[72:75], v[160:163], v[48:51], 0\n"
        "v_mul_f32 v154, s59, v154\n"
        "v_add_f32_dpp v145, v92, v145 quad_perm:[0,1,2,3] row_mask:0x4 bank_mask:0xf\n"
        "v_mfma_f32_16x16x32_bf16 v[72:75], v[128:131], v[80:83], v[72:75]\n"
        "v_cvt_pk_bf16_f32 v154, v154, 0\n"
        "v_cvt_pk_bf16_f32 v50, v68, v69\n"
        "v_mfma_f32_16x16x32_bf16 v[72:75], v[124:127], v[64:67], v[72:75]\n"
        "s_waitcnt lgkmcnt(0)\n"
        "v_cvt_pk_bf16_f32 v51, v70, v71\n"
        "v_permlane32_swap_b32 v155, v145\n"
        "v_mfma_f32_16x16x32_bf16 v[80:83], v[124:127], v[100:103], 0\n"
        "v_add_f32 v155, v155, v145\n"
        "v_mov_b32 v149, v155\n"
        "v_mfma_f32_16x16x32_bf16 v[80:83], v[128:131], v[108:111], v[80:83]\n"
        "ds_read_b128 v[32:35], v0 offset:17488\n"
        "v_cvt_pk_bf16_f32 v48, v72, v73\n"
        "v_mfma_f32_16x16x32_bf16 v[80:83], v[160:163], v[24:27], v[80:83]\n"
        "ds_read_b128 v[68:71], v0 offset:16464\n"
        "ds_read_b128 v[64:67], v0 offset:15440\n"
        "v_mfma_f32_16x16x32_bf16 v[76:79], v[52:55], v[24:27], 0\n"
        "v_cvt_pk_bf16_f32 v49, v74, v75\n"
        "v_mfma_f32_16x16x32_bf16 v[76:79], v[136:139], v[108:111], v[76:79]\n"
        "v_permlane16_swap_b32 v155, v149\n"
        "v_mfma_f32_16x16x32_bf16 v[76:79], v[132:135], v[100:103], v[76:79]\n"
        "v_add_f32 v155, v155, v149\n"
        "v_rcp_f32 v147, v155\n"
        "v_mfma_f32_16x16x32_bf16 v[72:75], v[88:91], v[100:103], 0\n"
        "ds_read2_b32 v[122:123], v11 offset0:0 offset1:3\n"
        "v_min3_f32 v167, v167, v155, v147\n"
        "v_mfma_f32_16x16x32_bf16 v[72:75], v[84:87], v[108:111], v[72:75]\n"
        "ds_read_b64_tr_b16 v[118:119], v9 offset:40064\n"
        "v_and_b32 v102, s28, v147\n"
        "v_mfma_f32_16x16x32_bf16 v[72:75], v[150:153], v[24:27], v[72:75]\n"
        "s_waitcnt lgkmcnt(2)\n"
        "ds_read2_b32 v[100:101], v11 offset0:1 offset1:4\n"
        "v_sub_f32 v149, v147, v102\n"
        "v_cvt_pk_bf16_f32 v103, v102, v149\n"
        "v_cvt_pk_bf16_f32 v108, v80, v81\n"
        "v_mfma_f32_16x16x32_bf16 v[24:27], v[150:153], v[32:35], 0\n"
        "ds_read_b64_tr_b16 v[96:97], v9 offset:40080\n"
        "v_cvt_pk_bf16_f32 v109, v82, v83\n"
        "v_mfma_f32_16x16x32_bf16 v[24:27], v[84:87], v[68:71], v[24:27]\n"
        "ds_read2_b32 v[150:151], v11 offset0:2 offset1:5\n"
        "v_cvt_pk_bf16_f32 v110, v76, v77\n"
        "v_mfma_f32_16x16x32_bf16 v[24:27], v[88:91], v[64:67], v[24:27]\n"
        "ds_read_b64_tr_b16 v[146:147], v9 offset:40096\n"
        "v_cvt_pk_bf16_f32 v111, v78, v79\n"
        "v_cvt_pk_bf16_f32 v95, v72, v73\n"
        "v_mfma_f32_16x16x4_4b_bf16 v[76:91], v[56:57], v[102:103], 0 blgp:4\n"
        "v_cvt_pk_bf16_f32 v99, v74, v75\n"
        "v_cmp_nle_f32 vcc, 0x2b800000, v167\n"
        "v_mov_b32_dpp v167, v19 row_shr:4 row_mask:0xf bank_mask:0xf bound_ctrl:0\n"
        "ds_write_b16 v1, v154\n"
        "v_cvt_pk_bf16_f32 v145, v24, v25\n"
        "v_cvt_pk_bf16_f32 v149, v26, v27\n"
        "s_andn2_b64 vcc, vcc, s[56:57]\n"
        "s_cbranch_scc1 CREDOB_%=\n"
        "v_mfma_f32_16x16x32_bf16 v[24:27], v[132:135], v[64:67], 0\n"
        "s_mov_b32 exec_hi, 0xffff\n"
        "v_add_u32 v10, -40064, v10\n"
        "s_mov_b32 exec_hi, -1\n"
        "v_mfma_f32_16x16x32_bf16 v[24:27], v[136:139], v[68:71], v[24:27]\n"
        "v_mul_f32 v58, v58, v76\n"
        "v_mul_f32 v59, v59, v77\n"
        "v_mfma_f32_16x16x32_bf16 v[24:27], v[52:55], v[32:35], v[24:27]\n"
        "s_add_u32 s31, s31, 1\n"
        "v_mul_f32 v60, v60, v78\n"
        "v_mul_f32 v61, v61, v79\n"
        "v_mul_f32 v112, v112, v80\n"
        "v_mul_f32 v113, v113, v81\n"
        "v_mfma_f32_16x16x32_bf16 v[52:55], v[160:163], v[32:35], 0\n"
        "s_cmp_ge_u32 s31, s71\n"
        "v_mul_f32 v114, v114, v82\n"
        "v_mul_f32 v115, v115, v83\n"
        "v_mfma_f32_16x16x32_bf16 v[52:55], v[128:131], v[68:71], v[52:55]\n"
        "s_cbranch_scc1 L34B_%=\n"
        "L35B_%=:\n"
        "v_mul_f32 v20, v20, v84\n"
        "v_cvt_pk_bf16_f32 v34, v24, v25\n"
        "v_mfma_f32_16x16x32_bf16 v[52:55], v[124:127], v[64:67], v[52:55]\n"
        "s_add_u32 s16, s16, 256\n"
        "v_cvt_pk_bf16_f32 v35, v26, v27\n"
        "v_mul_f32 v21, v21, v85\n"
        "v_mfma_f32_16x16x32_bf16 v[124:127], v[40:43], v[44:47], 0\n"
        "s_waitcnt lgkmcnt(0)\n"
        "v_mul_f32 v22, v22, v86\n"
        "v_mul_f32 v23, v23, v87\n"
        "v_smfmac_f32_16x16x64_bf16 v[124:127], v[164:167], v[116:123], v18\n"
        "s_cmp_ge_u32 s31, 20\n"
        "s_cselect_b32 s39, s22, s16\n"
        "ds_read_b64_tr_b16 v[118:119], v9 offset:40112\n"
        "v_mfma_f32_16x16x4_4b_bf16 v[72:87], v[56:57], v[102:103], 0 blgp:6\n"
        "v_cvt_pk_bf16_f32 v32, v52, v53\n"
        "v_cvt_pk_bf16_f32 v33, v54, v55\n"
        "v_fma_f32 v62, v62, v88, v19\n"
        "v_cvt_pk_bf16_f32 v68, v58, v59\n"
        "v_cvt_pk_bf16_f32 v69, v60, v61\n"
        "v_cvt_pk_bf16_f32 v70, v112, v113\n"
        "v_cvt_pk_bf16_f32 v71, v114, v115\n"
        "v_cvt_pk_bf16_f32 v24, v20, v21\n"
        "v_cvt_pk_bf16_f32 v26, v22, v23\n"
        "v_mfma_f32_16x16x32_bf16 v[88:91], v[40:43], v[104:107], 0\n"
        "s_cmp_ge_u32 s31, 21\n"
        "v_cvt_pk_bf16_f32 v25, v62, 0\n"
        "v_mul_f32 v36, v36, v72\n"
        "v_smfmac_f32_16x16x64_bf16 v[88:91], v[164:167], v[94:101], v18\n"
        "s_cselect_b32 s39, s23, s39\n"
        "ds_read_b64_tr_b16 v[96:97], v9 offset:40128\n"
        "v_mfma_f32_16x16x32_bf16 v[56:59], v[40:43], v[28:31], 0\n"
        "s_mul_hi_u32 s38, s39, 58835169\n"
        "ds_read_b128 v[128:131], v2\n"
        "v_mul_f32 v37, v37, v73\n"
        "v_smfmac_f32_16x16x64_bf16 v[56:59], v[164:167], v[144:151], v18\n"
        "s_mul_i32 s39, s39, 1344\n"
        "ds_read_b64_tr_b16 v[146:147], v9 offset:40144\n"
        "ds_read_b128 v[52:55], v3\n"
        "v_mfma_f32_16x16x32_bf16 v[112:115], v[40:43], v[48:51], 0\n"
        "s_waitcnt lgkmcnt(4)\n"
        "v_mul_f32 v38, v38, v74\n"
        "v_mul_f32 v39, v39, v75\n"
        "v_smfmac_f32_16x16x64_bf16 v[112:115], v[164:167], v[116:123], v18 abid:1\n"
        "s_mul_i32 s38, s38, 588672\n"
        "s_add_u32 s39, s39, s38\n"
        "v_mul_f32 v140, v140, v76\n"
        "v_cvt_pk_bf16_f32 v152, v124, v88\n"
        "v_mul_f32 v141, v141, v77\n"
        "v_mfma_f32_16x16x32_bf16 v[72:75], v[40:43], v[108:111], 0\n"
        "s_add_u32 s39, s39, s17\n"
        "v_mov_b64 v[60:61], 0\n"
        "s_waitcnt lgkmcnt(3)\n"
        "v_smfmac_f32_16x16x64_bf16 v[72:75], v[164:167], v[94:101], v18 abid:1\n"
        "s_mul_i32 s68, s31, 3\n"
        "v_mov_b64 v[62:63], 0\n"
        "v_cvt_pk_bf16_f32 v153, v56, v112\n"
        "v_mfma_f32_16x16x32_bf16 v[64:67], v[40:43], v[32:35], 0\n"
        "s_waitcnt lgkmcnt(1)\n"
        "v_mov_b64 v[20:21], 0\n"
        "v_mov_b64 v[22:23], 0\n"
        "v_smfmac_f32_16x16x64_bf16 v[64:67], v[164:167], v[144:151], v18 abid:1\n"
        "s_waitcnt lgkmcnt(0)\n"
        "s_bitcmp1_b32 s31, 0\n"
        "v_mov_b32_dpp v27, v19 row_shr:4 row_mask:0xf bank_mask:0xf bound_ctrl:0\n"
        "s_cselect_b32 s38, 3, 10\n"
        "v_smfmac_f32_16x16x64_bf16 v[60:63], v[128:131], v[44:51], v4\n"
        "s_add_u32 s68, s68, s38\n"
        "v_mov_b64 v[40:41], 0\n"
        "v_smfmac_f32_16x16x64_bf16 v[60:63], v[52:55], v[116:123], v4 abid:1\n"
        "s_add_u32 s37, s35, 0\n"
        "s_cmp_lt_u32 s31, s14\n"
        "ds_read_b64_tr_b16 v[118:119], v9 offset:43392\n"
        "v_mov_b64 v[42:43], 0\n"
        "v_smfmac_f32_16x16x64_bf16 v[20:23], v[128:131], v[104:111], v4\n"
        "s_cselect_b32 s68, s68, 0\n"
        "v_cvt_pk_bf16_f32 v154, v72, v64\n"
        "v_smfmac_f32_16x16x64_bf16 v[20:23], v[52:55], v[94:101], v4 abid:1\n"
        "s_and_b32 s38, s31, 7\n"
        "ds_read_b64_tr_b16 v[96:97], v9 offset:43408\n"
        "v_cvt_pk_bf16_f32 v160, v125, v89\n"
        "v_smfmac_f32_16x16x64_bf16 v[40:43], v[128:131], v[28:35], v4\n"
        "s_cselect_b32 s30, 1, -7\n"
        "v_cvt_pk_bf16_f32 v88, v126, v90\n"
        "v_smfmac_f32_16x16x64_bf16 v[40:43], v[52:55], v[144:151], v4 abid:1\n"
        "buffer_store_dwordx3 v[152:154], v12, s[8:11], s37 offen sc0 sc1 nt\n"
        "ds_read_b64_tr_b16 v[146:147], v9 offset:43424\n"
        "v_cvt_pk_bf16_f32 v164, v127, v91\n"
        "v_cvt_pk_bf16_f32 v161, v57, v113\n"
        "v_cvt_pk_bf16_f32 v89, v58, v114\n"
        "v_cvt_pk_bf16_f32 v165, v59, v115\n"
        "v_cvt_pk_bf16_f32 v162, v73, v65\n"
        "v_mfma_f32_16x16x32_bf16 v[152:155], v[68:71], v[44:47], 0\n"
        "s_waitcnt lgkmcnt(2)\n"
        "v_cvt_pk_bf16_f32 v90, v74, v66\n"
        "v_cvt_pk_bf16_f32 v166, v75, v67\n"
        "v_smfmac_f32_16x16x64_bf16 v[152:155], v[24:27], v[116:123], v18\n"
        "s_mul_i32 s29, s30, 192\n"
        "s_mul_i32 s30, s30, 392\n"
        "ds_read_b64_tr_b16 v[118:119], v9 offset:43440\n"
        "v_add_f32 v60, v60, v61\n"
        "v_add_f32 v62, v62, v63\n"
        "v_add_f32 v20, v20, v21\n"
        "v_mfma_f32_16x16x32_bf16 v[56:59], v[68:71], v[104:107], 0\n"
        "s_waitcnt lgkmcnt(2)\n"
        "v_add_f32 v22, v22, v23\n"
        "v_cvt_pk_bf16_f32 v112, v60, v20\n"
        "v_smfmac_f32_16x16x64_bf16 v[56:59], v[24:27], v[94:101], v18\n"
        "s_add_u32 s37, s35, 0\n"
        "s_cmp_eq_u32 s69, s18\n"
        "ds_read_b64_tr_b16 v[96:97], v9 offset:43456\n"
        "v_add_f32 v40, v40, v41\n"
        "v_add_f32 v42, v42, v43\n"
        "v_cvt_pk_bf16_f32 v113, v40, v62\n"
        "v_mfma_f32_16x16x32_bf16 v[72:75], v[68:71], v[28:31], 0\n"
        "s_waitcnt lgkmcnt(2)\n"
        "v_cvt_pk_bf16_f32 v114, v22, v42\n"
        "v_mul_f32 v142, v142, v78\n"
        "v_smfmac_f32_16x16x64_bf16 v[72:75], v[24:27], v[144:151], v18\n"
        "buffer_store_dwordx3 v[160:162], v15, s[8:11], s37 offen offset:768 sc0 sc1 nt\n"
        "ds_read_b64_tr_b16 v[146:147], v9 offset:43472\n"
        "s_cselect_b32 s34, -7488, s70\n"
        "v_mul_f32 v143, v143, v79\n"
        "v_mul_f32 v156, v156, v80\n"
        "v_mul_f32 v157, v157, v81\n"
        "v_mul_f32 v158, v158, v82\n"
        "v_mfma_f32_16x16x32_bf16 v[20:23], v[68:71], v[48:51], 0\n"
        "s_waitcnt lgkmcnt(2)\n"
        "s_add_u32 s69, s69, s34\n"
        "v_mul_f32 v159, v159, v83\n"
        "v_smfmac_f32_16x16x64_bf16 v[20:23], v[24:27], v[116:123], v18 abid:1\n"
        "s_add_u32 s37, s35, 98112\n"
        "ds_read_b64_tr_b16 v[118:119], v9 offset:46720\n"
        "v_fma_f32 v92, v92, v84, v19\n"
        "v_mfma_f32_16x16x32_bf16 v[84:87], v[68:71], v[108:111], 0\n"
        "s_waitcnt lgkmcnt(2)\n"
        "v_cvt_pk_bf16_f32 v80, v36, v37\n"
        "v_cvt_pk_bf16_f32 v81, v38, v39\n"
        "v_smfmac_f32_16x16x64_bf16 v[84:87], v[24:27], v[94:101], v18 abid:1\n"
        "ds_read_b64_tr_b16 v[96:97], v9 offset:46736\n"
        "v_cvt_pk_bf16_f32 v82, v140, v141\n"
        "v_mfma_f32_16x16x32_bf16 v[60:63], v[68:71], v[32:35], 0\n"
        "s_waitcnt lgkmcnt(2)\n"
        "v_cvt_pk_bf16_f32 v83, v142, v143\n"
        "v_cvt_pk_bf16_f32 v76, v156, v157\n"
        "v_smfmac_f32_16x16x64_bf16 v[60:63], v[24:27], v[144:151], v18 abid:1\n"
        "buffer_store_dwordx3 v[88:90], v12, s[8:11], s37 offen offset:192 sc0 sc1 nt\n"
        "ds_read_b64_tr_b16 v[146:147], v9 offset:46752\n"
        "ds_read_b32 v167, v0 offset:21600\n"
        "v_cvt_pk_bf16_f32 v78, v158, v159\n"
        "v_cvt_pk_bf16_f32 v77, v92, 0\n"
        "v_mov_b32_dpp v79, v19 row_shr:4 row_mask:0xf bank_mask:0xf bound_ctrl:0\n"
        "v_mfma_f32_16x16x32_bf16 v[68:71], v[80:83], v[44:47], 0\n"
        "s_waitcnt lgkmcnt(3)\n"
        "v_writelane_b32 v10, s69, 56\n"
        "v_add_u32 v11, s30, v11\n"
        "v_smfmac_f32_16x16x64_bf16 v[68:71], v[76:79], v[116:123], v18\n"
        "s_add_u32 s37, s35, 98112\n"
        "ds_read_b64_tr_b16 v[118:119], v9 offset:46768\n"
        "v_mfma_f32_16x16x32_bf16 v[36:39], v[80:83], v[104:107], 0\n"
        "s_waitcnt lgkmcnt(3)\n"
        "v_smfmac_f32_16x16x64_bf16 v[36:39], v[76:79], v[94:101], v18\n"
        "ds_read_b64_tr_b16 v[96:97], v9 offset:46784\n"
        "v_mfma_f32_16x16x32_bf16 v[40:43], v[80:83], v[28:31], 0\n"
        "s_waitcnt lgkmcnt(3)\n"
        "s_waitcnt lgkmcnt(2)\n"
        "v_readfirstlane_b32 s40, v167\n"
        "v_smfmac_f32_16x16x64_bf16 v[40:43], v[76:79], v[144:151], v18\n"
        "buffer_store_dwordx3 v[164:166], v14, s[8:11], s37 offen offset:960 sc0 sc1 nt\n"
        "ds_read_b64_tr_b16 v[146:147], v9 offset:46800\n"
        "s_add_u32 s37, s35, 588672\n"
        "s_add_u32 s46, s37, 1344\n"
        "s_cmp_lt_i32 s40, s68\n"
        "s_mov_b64 exec, 1\n"
        "ds_inc_u32 v0, v1 offset:21612\n"
        "s_mov_b64 exec, -1\n"
        "s_cbranch_scc1 L38B_%=\n"
        "L39B_%=:\n"
        "ds_read_b128 v[124:127], v7 offset:0\n"
        "v_mfma_f32_16x16x32_bf16 v[64:67], v[80:83], v[48:51], 0\n"
        "s_waitcnt lgkmcnt(4)\n"
        "ds_read_b128 v[132:135], v7 offset:3328\n"
        "v_smfmac_f32_16x16x64_bf16 v[64:67], v[76:79], v[116:123], v18 abid:1\n"
        "buffer_store_dwordx3 v[112:114], v12, s[44:47], s37 offen offset:1152 sc0 sc1 nt\n"
        "ds_read_b128 v[128:131], v7 offset:64\n"
        "ds_read_b128 v[136:139], v7 offset:3392\n"
        "ds_read_b128 v[160:163], v7 offset:128\n"
        "ds_read_b128 v[52:55], v7 offset:3456\n"
        "v_mfma_f32_16x16x32_bf16 v[104:107], v[80:83], v[108:111], 0\n"
        "s_waitcnt lgkmcnt(7)\n"
        "s_mov_b32 s36, s35\n"
        "v_smfmac_f32_16x16x64_bf16 v[104:107], v[76:79], v[94:101], v18 abid:1\n"
        "s_mov_b64 exec, s[62:63]\n"
        "v_add_u32 v16, s29, v16\n"
        "v_add_u32 v8, s34, v8\n"
        "s_mov_b64 exec, -1\n"
        "v_mfma_f32_16x16x32_bf16 v[88:91], v[80:83], v[32:35], 0\n"
        "s_mov_b32 s35, s39\n"
        "v_smfmac_f32_16x16x64_bf16 v[88:91], v[76:79], v[144:151], v18 abid:1\n"
        "s_mov_b64 exec, s[50:51]\n"
        "v_add_u32 v5, s29, v5\n"
        "v_add_u32 v6, s34, v6\n"
        "s_mov_b64 exec, -1\n"
        "s_setprio 0\n"
        "CNEXTB_%=:\n"
        "s_cmp_ge_u32 s31, s14\n"
        "s_cbranch_scc1 CEND_%=\n"
        "s_cmp_gt_i32 s29, 0\n"
        "s_cbranch_scc1 CTOP_%=\n"
        "s_branch CFULL_%=\n"
        "CEND_%=:\n"
        "s_waitcnt lgkmcnt(0)\n"
        "s_nop 15\n"
        "v_cvt_pk_bf16_f32 v248, v152, v56\n"
        "v_cvt_pk_bf16_f32 v249, v72, v20\n"
        "v_cvt_pk_bf16_f32 v250, v84, v60\n"
        "s_add_u32 s37, s36, 196224\n"
        "buffer_store_dwordx3 v[248:250], v12, s[8:11], s37 offen offset:384 sc0 sc1 nt\n"
        "v_cvt_pk_bf16_f32 v252, v153, v57\n"
        "v_cvt_pk_bf16_f32 v253, v73, v21\n"
        "v_cvt_pk_bf16_f32 v254, v85, v61\n"
        "s_add_u32 s37, s36, 196224\n"
        "buffer_store_dwordx3 v[252:254], v13, s[8:11], s37 offen offset:1152 sc0 sc1 nt\n"
        "v_cvt_pk_bf16_f32 v248, v154, v58\n"
        "v_cvt_pk_bf16_f32 v249, v74, v22\n"
        "v_cvt_pk_bf16_f32 v250, v86, v62\n"
        "s_add_u32 s37, s36, 294336\n"
        "buffer_store_dwordx3 v[248:250], v12, s[8:11], s37 offen offset:576 sc0 sc1 nt\n"
        "v_cvt_pk_bf16_f32 v252, v155, v59\n"
        "v_cvt_pk_bf16_f32 v253, v75, v23\n"
        "v_cvt_pk_bf16_f32 v254, v87, v63\n"
        "s_add_u32 s37, s36, 392448\n"
        "buffer_store_dwordx3 v[252:254], v12, s[8:11], s37 offen sc0 sc1 nt\n"
        "v_cvt_pk_bf16_f32 v248, v68, v36\n"
        "v_cvt_pk_bf16_f32 v249, v40, v64\n"
        "v_cvt_pk_bf16_f32 v250, v104, v88\n"
        "s_add_u32 s37, s36, 392448\n"
        "v_cvt_pk_bf16_f32 v252, v69, v37\n"
        "v_cvt_pk_bf16_f32 v253, v41, v65\n"
        "v_cvt_pk_bf16_f32 v254, v105, v89\n"
        "buffer_store_dwordx3 v[248:250], v15, s[8:11], s37 offen offset:768 sc0 sc1 nt\n"
        "s_add_u32 s37, s36, 490560\n"
        "v_cvt_pk_bf16_f32 v246, v70, v38\n"
        "v_cvt_pk_bf16_f32 v247, v42, v66\n"
        "v_cvt_pk_bf16_f32 v248, v106, v90\n"
        "buffer_store_dwordx3 v[252:254], v12, s[8:11], s37 offen offset:192 sc0 sc1 nt\n"
        "s_add_u32 s37, s36, 490560\n"
        "buffer_store_dwordx3 v[246:248], v14, s[8:11], s37 offen offset:960 sc0 sc1 nt\n"
        "s_add_u32 s37, s36, 588672\n"
        "v_cvt_pk_bf16_f32 v252, v71, v39\n"
        "v_cvt_pk_bf16_f32 v253, v43, v67\n"
        "v_cvt_pk_bf16_f32 v254, v107, v91\n"
        "buffer_store_dwordx3 v[252:254], v12, s[8:11], s37 offen offset:384 sc0 sc1 nt\n"
        "s_nop 1\n"
        "s_endpgm\n"
        "L34A_%=:\n"
        "s_nop 7\n"
        "v_mov_b32 v153, 21600\n"
        "L32A_%=:\n"
        "ds_read_b32 v163, v153 offset:64\n"
        "s_waitcnt lgkmcnt(0)\n"
        "v_readfirstlane_b32 s40, v163\n"
        "s_nop 3\n"
        "s_cmp_ge_i32 s40, 1\n"
        "s_cbranch_scc1 L33A_%=\n"
        "s_sleep 1\n"
        "s_sub_u32 s41, s41, 1\n"
        "s_cbranch_scc0 L32A_%=\n"
        "s_endpgm\n"
        "L33A_%=:\n"
        "v_mbcnt_lo_u32_b32 v163, -1, 0\n"
        "v_lshl_add_u32 v153, v163, 2, v153\n"
        "ds_read_b32 v163, v153 offset:68\n"
        "s_mov_b32 s14, s40\n"
        "s_mov_b32 s71, -1\n"
        "s_waitcnt lgkmcnt(0)\n"
        "v_readlane_b32 s22, v163, 0\n"
        "v_readlane_b32 s23, v163, 1\n"
        "s_nop 3\n"
        "s_nop 1\n"
        "s_branch L35A_%=\n"
        "L38A_%=:\n"
        "s_nop 7\n"
        "L36A_%=:\n"
        "ds_read_b32 v167, v0 offset:21604\n"
        "s_waitcnt lgkmcnt(0)\n"
        "v_readfirstlane_b32 s40, v167\n"
        "s_nop 3\n"
        "s_cmp_ge_i32 s40, s68\n"
        "s_cbranch_scc1 L37A_%=\n"
        "s_sleep 1\n"
        "s_sub_u32 s41, s41, 1\n"
        "s_cbranch_scc0 L36A_%=\n"
        "s_endpgm\n"
        "L37A_%=:\n"
        "s_nop 1\n"
        "s_branch L39A_%=\n"
        "L34B_%=:\n"
        "s_nop 7\n"
        "v_mov_b32 v153, 21600\n"
        "L32B_%=:\n"
        "ds_read_b32 v163, v153 offset:64\n"
        "s_waitcnt lgkmcnt(0)\n"
        "v_readfirstlane_b32 s40, v163\n"
        "s_nop 3\n"
        "s_cmp_ge_i32 s40, 1\n"
        "s_cbranch_scc1 L33B_%=\n"
        "s_sleep 1\n"
        "s_sub_u32 s41, s41, 1\n"
        "s_cbranch_scc0 L32B_%=\n"
        "s_endpgm\n"
        "L33B_%=:\n"
        "v_mbcnt_lo_u32_b32 v163, -1, 0\n"
        "v_lshl_add_u32 v153, v163, 2, v153\n"
        "ds_read_b32 v163, v153 offset:68\n"
        "s_mov_b32 s14, s40\n"
        "s_mov_b32 s71, -1\n"
        "s_waitcnt lgkmcnt(0)\n"
        "v_readlane_b32 s22, v163, 0\n"
        "v_readlane_b32 s23, v163, 1\n"
        "s_nop 3\n"
        "s_nop 1\n"
        "s_branch L35B_%=\n"
        "L38B_%=:\n"
        "s_nop 7\n"
        "L36B_%=:\n"
        "ds_read_b32 v167, v0 offset:21600\n"
        "s_waitcnt lgkmcnt(0)\n"
        "v_readfirstlane_b32 s40, v167\n"
        "s_nop 3\n"
        "s_cmp_ge_i32 s40, s68\n"
        "s_cbranch_scc1 L37B_%=\n"
        "s_sleep 1\n"
        "s_sub_u32 s41, s41, 1\n"
        "s_cbranch_scc0 L36B_%=\n"
        "s_endpgm\n"
        "L37B_%=:\n"
        "s_nop 1\n"
        "s_branch L39B_%=\n"
        "CGRP_%=:\n"
        "s_setprio 3\n"
        "CGRQ_%=:\n"
        "s_waitcnt lgkmcnt(0)\n"
        "s_nop 6\n"
        "ds_read_u16 v167, v0 offset:20576\n"
        "s_lshr_b32 s61, s31, 3\n"
        "s_add_u32 s61, s61, 1\n"
        "L40_%=:\n"
        "ds_read_b32 v166, v0 offset:21660\n"
        "s_waitcnt lgkmcnt(0)\n"
        "v_readfirstlane_b32 s40, v166\n"
        "s_nop 3\n"
        "s_cmp_ge_i32 s40, s61\n"
        "s_cbranch_scc1 L41_%=\n"
        "s_sleep 1\n"
        "s_sub_u32 s41, s41, 1\n"
        "s_cbranch_scc0 L40_%=\n"
        "s_endpgm\n"
        "L41_%=:\n"
        "s_sub_u32 s60, s69, 102048\n"
        "v_add_u32 v167, s60, v167\n"
        "v_subrev_u32 v165, 8320, v167\n"
        "v_min_u32 v167, v167, v165\n"
        "v_add_u32 v167, 101856, v167\n"
        "ds_read_b128 v[28:31], v167 offset:64\n"
        "ds_read_b128 v[24:27], v167 offset:128\n"
        "ds_read_b128 v[32:35], v167\n"
        "s_waitcnt lgkmcnt(1)\n"
        "s_waitcnt lgkmcnt(0)\n"
        "v_mfma_f32_16x16x32_bf16 v[44:47], v[168:171], v[32:35], 0\n"
        "ds_read_u16 v162, v0 offset:20578\n"
        "v_lshrrev_b32 v159, 8, v0\n"
        "v_mfma_f32_16x16x32_bf16 v[44:47], v[172:175], v[28:31], v[44:47]\n"
        "ds_read_u16 v163, v0 offset:20580\n"
        "v_mfma_f32_16x16x32_bf16 v[44:47], v[176:179], v[24:27], v[44:47]\n"
        "ds_read_u16 v167, v0 offset:20582\n"
        "ds_read_u16 v158, v0 offset:20588\n"
        "ds_read_u16 v166, v0 offset:20590\n"
        "v_mfma_f32_16x16x32_bf16 v[52:55], v[180:183], v[32:35], 0\n"
        "v_mfma_f32_16x16x32_bf16 v[52:55], v[184:187], v[28:31], v[52:55]\n"
        "v_mfma_f32_16x16x32_bf16 v[52:55], v[188:191], v[24:27], v[52:55]\n"
        "v_mfma_f32_16x16x32_bf16 v[48:51], v[192:195], v[32:35], 0\n"
        "s_waitcnt lgkmcnt(4)\n"
        "v_add_u32 v162, s60, v162\n"
        "v_subrev_u32 v165, 8320, v162\n"
        "v_mfma_f32_16x16x32_bf16 v[48:51], v[196:199], v[28:31], v[48:51]\n"
        "v_min_u32 v162, v162, v165\n"
        "v_add_u32 v162, 101856, v162\n"
        "v_mfma_f32_16x16x32_bf16 v[48:51], v[200:203], v[24:27], v[48:51]\n"
        "ds_read_b64 v[160:161], v162\n"
        "v_cvt_pk_bf16_f32 v76, v44, v45\n"
        "v_cvt_pk_bf16_f32 v77, v46, v47\n"
        "v_cvt_pk_bf16_f32 v78, v52, v53\n"
        "v_cvt_pk_bf16_f32 v79, v54, v55\n"
        "s_waitcnt lgkmcnt(1)\n"
        "v_add_u32 v163, s19, v163\n"
        "v_mfma_f32_16x16x32_bf16 v[44:47], v[204:207], v[32:35], 0\n"
        "v_cvt_pk_bf16_f32 v80, v48, v49\n"
        "v_cvt_pk_bf16_f32 v81, v50, v51\n"
        "v_mfma_f32_16x16x32_bf16 v[44:47], v[208:211], v[28:31], v[44:47]\n"
        "ds_read_b64 v[164:165], v158\n"
        "v_mad_i32_i24 v159, v159, -8, v163\n"
        "v_mfma_f32_16x16x32_bf16 v[44:47], v[212:215], v[24:27], v[44:47]\n"
        "ds_read_b64 v[156:157], v162 offset:16\n"
        "v_add_u32 v166, s19, v166\n"
        "v_add_u32 v167, s19, v167\n"
        "v_mfma_f32_16x16x32_bf16 v[52:55], v[216:219], v[32:35], 0\n"
        "ds_read_b64 v[150:151], v162 offset:32\n"
        "v_mfma_f32_16x16x32_bf16 v[52:55], v[220:223], v[28:31], v[52:55]\n"
        "v_mfma_f32_16x16x32_bf16 v[52:55], v[224:227], v[24:27], v[52:55]\n"
        "ds_write2_b32 v159, v76, v78 offset0:0 offset1:1\n"
        "v_cvt_pk_bf16_f32 v82, v44, v45\n"
        "v_cvt_pk_bf16_f32 v83, v46, v47\n"
        "v_mfma_f32_16x16x32_bf16 v[44:47], v[228:231], v[32:35], 0\n"
        "v_mfma_f32_16x16x32_bf16 v[44:47], v[232:235], v[28:31], v[44:47]\n"
        "v_mfma_f32_16x16x32_bf16 v[44:47], v[236:239], v[24:27], v[44:47]\n"
        "s_waitcnt lgkmcnt(2)\n"
        "ds_write2_b32 v159, v77, v79 offset0:16 offset1:17\n"
        "v_cvt_pk_bf16_f32 v48, v52, v53\n"
        "v_cvt_pk_bf16_f32 v49, v54, v55\n"
        "v_mfma_f32_4x4x4_16b_bf16 v[96:99], v[160:161], v[164:165], 0\n"
        "v_mfma_f32_4x4x4_16b_bf16 v[92:95], v[156:157], v[164:165], 0\n"
        "s_waitcnt lgkmcnt(2)\n"
        "v_mfma_f32_4x4x4_16b_bf16 v[52:55], v[150:151], v[164:165], 0\n"
        "v_mov_b32 v100, 0x3f803f80\n"
        "v_mov_b32 v101, 0x3f803f80\n"
        "v_mov_b32 v102, 0x3f803f80\n"
        "v_mov_b32 v103, 0x3f803f80\n"
        "v_cvt_pk_bf16_f32 v50, v44, v45\n"
        "v_mfma_f32_16x16x32_bf16 v[124:127], v[32:35], v[240:243], 0\n"
        "v_cvt_pk_bf16_f32 v51, v46, v47\n"
        "v_mfma_f32_16x16x32_bf16 v[124:127], v[28:31], v[244:247], v[124:127]\n"
        "ds_read_b128 v[108:111], v0 offset:4112\n"
        "v_mfma_f32_16x16x32_bf16 v[124:127], v[24:27], v[248:251], v[124:127]\n"
        "ds_read_b128 v[112:115], v0 offset:5136\n"
        "ds_write2_b32 v159, v80, v82 offset0:8 offset1:9\n"
        "v_mfma_f32_16x16x32_bf16 v[44:47], v[100:103], v[32:35], 0\n"
        "v_mfma_f32_16x16x32_bf16 v[44:47], v[100:103], v[28:31], v[44:47]\n"
        "ds_read_b128 v[120:123], v0 offset:6176\n"
        "v_mfma_f32_16x16x32_bf16 v[44:47], v[100:103], v[24:27], v[44:47]\n"
        "ds_read_b128 v[116:119], v0 offset:7200\n"
        "ds_read_b128 v[100:103], v0 offset:8224\n"
        "ds_read_b64 v[160:161], v162 offset:48\n"
        "v_cvt_pk_bf16_f32 v158, v124, v96\n"
        "v_cvt_pk_bf16_f32 v156, v125, v97\n"
        "v_cvt_pk_bf16_f32 v150, v126, v98\n"
        "v_cvt_pk_bf16_f32 v148, v127, v99\n"
        "v_mfma_f32_16x16x32_bf16 v[128:131], v[32:35], v[76:79], 0\n"
        "v_mfma_f32_16x16x32_bf16 v[128:131], v[28:31], v[80:83], v[128:131]\n"
        "ds_read_b64 v[146:147], v162 offset:64\n"
        "v_mfma_f32_16x16x32_bf16 v[128:131], v[24:27], v[48:51], v[128:131]\n"
        "s_waitcnt lgkmcnt(5)\n"
        "ds_write2_b32 v159, v81, v83 offset0:24 offset1:25\n"
        "v_mfma_f32_16x16x32_bf16 v[76:79], v[32:35], v[252:255], 0\n"
        "ds_read_b64 v[144:145], v162 offset:80\n"
        "v_mfma_f32_16x16x32_bf16 v[76:79], v[28:31], v[108:111], v[76:79]\n"
        "ds_read_b128 v[96:99], v0 offset:9264\n"
        "v_mfma_f32_16x16x32_bf16 v[76:79], v[24:27], v[112:115], v[76:79]\n"
        "s_waitcnt lgkmcnt(4)\n"
        "ds_read_b128 v[108:111], v0 offset:10288\n"
        "ds_read_b128 v[112:115], v0 offset:11312\n"
        "s_mov_b32 vcc_lo, 0x22222222\n"
        "v_cndmask_b32 v141, v128, v129, vcc\n"
        "s_mov_b32 vcc_lo, 0x44444444\n"
        "v_cndmask_b32 v141, v141, v130, vcc\n"
        "v_mfma_f32_16x16x32_bf16 v[124:127], v[32:35], v[120:123], 0\n"
        "s_mov_b32 vcc_lo, 0x88888888\n"
        "v_cndmask_b32 v141, v141, v131, vcc\n"
        "v_cvt_pk_bf16_f32 v159, v76, v92\n"
        "v_mfma_f32_16x16x32_bf16 v[124:127], v[28:31], v[116:119], v[124:127]\n"
        "ds_read_b128 v[132:135], v0 offset:12352\n"
        "v_cvt_pk_bf16_f32 v157, v77, v93\n"
        "v_mfma_f32_16x16x32_bf16 v[124:127], v[24:27], v[100:103], v[124:127]\n"
        "ds_read_b128 v[136:139], v0 offset:13376\n"
        "ds_read_b128 v[128:131], v0 offset:14400\n"
        "v_cvt_pk_bf16_f32 v151, v78, v94\n"
        "v_cvt_pk_bf16_f32 v149, v79, v95\n"
        "v_mfma_f32_4x4x4_16b_bf16 v[116:119], v[160:161], v[164:165], 0\n"
        "s_waitcnt lgkmcnt(5)\n"
        "v_mfma_f32_4x4x4_16b_bf16 v[80:83], v[146:147], v[164:165], 0\n"
        "ds_read_b128 v[120:123], v0 offset:15440\n"
        "ds_read_b128 v[100:103], v0 offset:16464\n"
        "ds_read_b128 v[92:95], v0 offset:17488\n"
        "v_mfma_f32_4x4x4_16b_bf16 v[76:79], v[144:145], v[164:165], 0\n"
        "v_cvt_pk_bf16_f32 v146, v124, v52\n"
        "v_cvt_pk_bf16_f32 v164, v125, v53\n"
        "v_cvt_pk_bf16_f32 v144, v126, v54\n"
        "v_cvt_pk_bf16_f32 v142, v127, v55\n"
        "s_waitcnt lgkmcnt(6)\n"
        "v_mfma_f32_16x16x32_bf16 v[52:55], v[32:35], v[96:99], 0\n"
        "v_mfma_f32_16x16x32_bf16 v[52:55], v[28:31], v[108:111], v[52:55]\n"
        "v_mfma_f32_16x16x32_bf16 v[52:55], v[24:27], v[112:115], v[52:55]\n"
        "s_waitcnt lgkmcnt(3)\n"
        "ds_write_b128 v163, v[48:51] offset:128\n"
        "v_mfma_f32_16x16x32_bf16 v[48:51], v[32:35], v[132:135], 0\n"
        "ds_read_b128 v[124:127], v7\n"
        "v_mfma_f32_16x16x32_bf16 v[48:51], v[28:31], v[136:139], v[48:51]\n"
        "ds_read_b128 v[132:135], v7 offset:3328\n"
        "v_mfma_f32_16x16x32_bf16 v[48:51], v[24:27], v[128:131], v[48:51]\n"
        "s_waitcnt lgkmcnt(3)\n"
        "ds_read_b128 v[128:131], v7 offset:64\n"
        "v_cvt_pk_bf16_f32 v147, v52, v116\n"
        "v_cvt_pk_bf16_f32 v165, v53, v117\n"
        "v_cvt_pk_bf16_f32 v145, v54, v118\n"
        "v_cvt_pk_bf16_f32 v143, v55, v119\n"
        "v_mfma_f32_16x16x32_bf16 v[96:99], v[32:35], v[120:123], 0\n"
        "ds_read_b128 v[136:139], v7 offset:3392\n"
        "v_cvt_pk_bf16_f32 v140, v48, v80\n"
        "v_mfma_f32_16x16x32_bf16 v[96:99], v[28:31], v[100:103], v[96:99]\n"
        "ds_read_b128 v[160:163], v7 offset:128\n"
        "v_cvt_pk_bf16_f32 v122, v49, v81\n"
        "v_mfma_f32_16x16x32_bf16 v[96:99], v[24:27], v[92:95], v[96:99]\n"
        "s_mov_b64 exec, 0x00f0000f\n"
        "ds_write2_b32 v166, v141, v44 offset0:0 offset1:1\n"
        "s_mov_b64 exec, -1\n"
        "ds_read_b128 v[52:55], v7 offset:3456\n"
        "v_cvt_pk_bf16_f32 v120, v50, v82\n"
        "v_cvt_pk_bf16_f32 v118, v51, v83\n"
        "s_mov_b32 exec_hi, 0\n"
        "ds_write2_b64 v167, v[158:159], v[156:157] offset0:0 offset1:49\n"
        "ds_write2_b64 v167, v[150:151], v[148:149] offset0:98 offset1:147\n"
        "s_mov_b32 exec_hi, -1\n"
        "v_cvt_pk_bf16_f32 v141, v96, v76\n"
        "v_cvt_pk_bf16_f32 v123, v97, v77\n"
        "v_cvt_pk_bf16_f32 v121, v98, v78\n"
        "v_cvt_pk_bf16_f32 v119, v99, v79\n"
        "s_mov_b32 exec_hi, 0\n"
        "ds_write2_b64 v167, v[146:147], v[164:165] offset0:1 offset1:50\n"
        "ds_write2_b64 v167, v[144:145], v[142:143] offset0:99 offset1:148\n"
        "s_mov_b32 exec_hi, -1\n"
        "s_mov_b32 exec_hi, 0\n"
        "ds_write2_b64 v167, v[140:141], v[122:123] offset0:2 offset1:51\n"
        "ds_write2_b64 v167, v[120:121], v[118:119] offset0:100 offset1:149\n"
        "s_mov_b32 exec_hi, -1\n"
        "s_waitcnt lgkmcnt(0)\n"
        "s_nop 6\n"
        "s_setprio 0\n"
        "s_setpc_b64 s[64:65]\n"
        "L52A_%=:\n"
        "s_nop 7\n"
        "v_mov_b32 v167, 21600\n"
        "L50A_%=:\n"
        "ds_read_b32 v166, v167 offset:64\n"
        "s_waitcnt lgkmcnt(0)\n"
        "v_readfirstlane_b32 s40, v166\n"
        "s_nop 3\n"
        "s_cmp_ge_i32 s40, 1\n"
        "s_cbranch_scc1 L51A_%=\n"
        "s_sleep 1\n"
        "s_sub_u32 s41, s41, 1\n"
        "s_cbranch_scc0 L50A_%=\n"
        "s_endpgm\n"
        "L51A_%=:\n"
        "v_mbcnt_lo_u32_b32 v166, -1, 0\n"
        "v_lshl_add_u32 v167, v166, 2, v167\n"
        "ds_read_b32 v166, v167 offset:68\n"
        "s_mov_b32 s14, s40\n"
        "s_mov_b32 s71, -1\n"
        "s_waitcnt lgkmcnt(0)\n"
        "v_readlane_b32 s22, v166, 0\n"
        "v_readlane_b32 s23, v166, 1\n"
        "s_nop 3\n"
        "s_nop 1\n"
        "s_branch L53A_%=\n"
        "L56A_%=:\n"
        "s_nop 7\n"
        "L54A_%=:\n"
        "ds_read_b32 v167, v0 offset:21604\n"
        "s_waitcnt lgkmcnt(0)\n"
        "v_readfirstlane_b32 s40, v167\n"
        "s_nop 3\n"
        "s_cmp_ge_i32 s40, s68\n"
        "s_cbranch_scc1 L55A_%=\n"
        "s_sleep 1\n"
        "s_sub_u32 s41, s41, 1\n"
        "s_cbranch_scc0 L54A_%=\n"
        "s_endpgm\n"
        "L55A_%=:\n"
        "s_nop 1\n"
        "s_branch L57A_%=\n"
        "L52B_%=:\n"
        "s_nop 7\n"
        "v_mov_b32 v167, 21600\n"
        "L50B_%=:\n"
        "ds_read_b32 v166, v167 offset:64\n"
        "s_waitcnt lgkmcnt(0)\n"
        "v_readfirstlane_b32 s40, v166\n"
        "s_nop 3\n"
        "s_cmp_ge_i32 s40, 1\n"
        "s_cbranch_scc1 L51B_%=\n"
        "s_sleep 1\n"
        "s_sub_u32 s41, s41, 1\n"
        "s_cbranch_scc0 L50B_%=\n"
        "s_endpgm\n"
        "L51B_%=:\n"
        "v_mbcnt_lo_u32_b32 v166, -1, 0\n"
        "v_lshl_add_u32 v167, v166, 2, v167\n"
        "ds_read_b32 v166, v167 offset:68\n"
        "s_mov_b32 s14, s40\n"
        "s_mov_b32 s71, -1\n"
        "s_waitcnt lgkmcnt(0)\n"
        "v_readlane_b32 s22, v166, 0\n"
        "v_readlane_b32 s23, v166, 1\n"
        "s_nop 3\n"
        "s_nop 1\n"
        "s_branch L53B_%=\n"
        "L56B_%=:\n"
        "s_nop 7\n"
        "L54B_%=:\n"
        "ds_read_b32 v167, v0 offset:21600\n"
        "s_waitcnt lgkmcnt(0)\n"
        "v_readfirstlane_b32 s40, v167\n"
        "s_nop 3\n"
        "s_cmp_ge_i32 s40, s68\n"
        "s_cbranch_scc1 L55B_%=\n"
        "s_sleep 1\n"
        "s_sub_u32 s41, s41, 1\n"
        "s_cbranch_scc0 L54B_%=\n"
        "s_endpgm\n"
        "L55B_%=:\n"
        "s_nop 1\n"
        "s_branch L57B_%=\n"
        "CREDOA_%=:\n"
        "s_waitcnt lgkmcnt(0)\n"
        "s_nop 15\n"
        "s_mov_b32 s36, 0x20000000\n"
        "ds_read_b128 v[124:127], v7 offset:0\n"
        "ds_read_b128 v[132:135], v7 offset:3328\n"
        "ds_read_b128 v[128:131], v7 offset:64\n"
        "ds_read_b128 v[136:139], v7 offset:3392\n"
        "ds_read_b128 v[160:163], v7 offset:128\n"
        "ds_read_b128 v[52:55], v7 offset:3456\n"
        "s_waitcnt lgkmcnt(0)\n"
        "s_branch MTOPA_%=\n"
        "CREDOB_%=:\n"
        "s_waitcnt lgkmcnt(0)\n"
        "s_nop 15\n"
        "s_mov_b32 s36, 0x20000000\n"
        "ds_read_b128 v[124:127], v7 offset:40064\n"
        "ds_read_b128 v[132:135], v7 offset:43392\n"
        "ds_read_b128 v[128:131], v7 offset:40128\n"
        "ds_read_b128 v[136:139], v7 offset:43456\n"
        "ds_read_b128 v[160:163], v7 offset:40192\n"
        "ds_read_b128 v[52:55], v7 offset:43520\n"
        "s_waitcnt lgkmcnt(0)\n"
        "s_branch MTOPB_%=\n"
        "MTOPA_%=:\n"
        "s_waitcnt lgkmcnt(5)\n"
        "v_mfma_f32_16x16x32_bf16 v[24:27], v[168:171], v[124:127], 0\n"
        "v_cvt_pk_bf16_f32 v112, v152, v56\n"
        "v_cvt_pk_bf16_f32 v113, v72, v20\n"
        "v_cvt_pk_bf16_f32 v114, v84, v60\n"
        "v_cvt_pk_bf16_f32 v116, v153, v57\n"
        "v_cvt_pk_bf16_f32 v117, v73, v21\n"
        "v_cvt_pk_bf16_f32 v118, v85, v61\n"
        "v_cvt_pk_bf16_f32 v120, v154, v58\n"
        "v_cvt_pk_bf16_f32 v121, v74, v22\n"
        "s_waitcnt lgkmcnt(3)\n"
        "v_mfma_f32_16x16x32_bf16 v[24:27], v[172:175], v[128:131], v[24:27]\n"
        "v_cvt_pk_bf16_f32 v122, v86, v62\n"
        "v_cvt_pk_bf16_f32 v140, v155, v59\n"
        "v_cvt_pk_bf16_f32 v141, v75, v23\n"
        "v_cvt_pk_bf16_f32 v142, v87, v63\n"
        "v_cvt_pk_bf16_f32 v144, v68, v36\n"
        "v_cvt_pk_bf16_f32 v145, v40, v64\n"
        "v_cvt_pk_bf16_f32 v146, v104, v88\n"
        "v_cvt_pk_bf16_f32 v148, v69, v37\n"
        "s_waitcnt lgkmcnt(1)\n"
        "v_mfma_f32_16x16x32_bf16 v[24:27], v[176:179], v[160:163], v[24:27]\n"
        "v_cvt_pk_bf16_f32 v149, v41, v65\n"
        "v_cvt_pk_bf16_f32 v150, v105, v89\n"
        "v_cvt_pk_bf16_f32 v152, v70, v38\n"
        "v_cvt_pk_bf16_f32 v153, v42, v66\n"
        "v_cvt_pk_bf16_f32 v154, v106, v90\n"
        "v_cvt_pk_bf16_f32 v156, v71, v39\n"
        "v_cvt_pk_bf16_f32 v157, v43, v67\n"
        "v_cvt_pk_bf16_f32 v158, v107, v91\n"
        "v_mfma_f32_16x16x32_bf16 v[32:35], v[188:191], v[160:163], 0\n"
        "v_cvt_pk_bf16_f32 v40, v24, v25\n"
        "v_cvt_pk_bf16_f32 v41, v26, v27\n"
        "v_mfma_f32_16x16x32_bf16 v[32:35], v[184:187], v[128:131], v[32:35]\n"
        "v_mfma_f32_16x16x32_bf16 v[32:35], v[180:183], v[124:127], v[32:35]\n"
        "s_add_u32 s37, s36, 196224\n"
        "ds_read_b128 v[28:31], v16 offset:128\n"
        "ds_read_b32 v159, v10\n"
        "v_mfma_f32_16x16x32_bf16 v[24:27], v[192:195], v[124:127], 0\n"
        "ds_read_b128 v[20:23], v5\n"
        "v_mfma_f32_16x16x32_bf16 v[24:27], v[196:199], v[128:131], v[24:27]\n"
        "ds_read_b64 v[164:165], v11 offset:192\n"
        "v_mfma_f32_16x16x32_bf16 v[24:27], v[200:203], v[160:163], v[24:27]\n"
        "buffer_store_dwordx3 v[112:114], v12, s[8:11], s37 offen offset:384 sc0 sc1 nt\n"
        "v_cvt_pk_bf16_f32 v42, v32, v33\n"
        "v_cvt_pk_bf16_f32 v43, v34, v35\n"
        "s_waitcnt lgkmcnt(3)\n"
        "v_mfma_f32_16x16x32_bf16 v[48:51], v[28:31], v[160:163], 0\n"
        "ds_read_b128 v[60:63], v7 offset:6784\n"
        "ds_read_i16 v167, v0 offset:20584\n"
        "ds_read_i16 v155, v0 offset:20586\n"
        "v_cvt_pk_bf16_f32 v44, v24, v25\n"
        "v_cvt_pk_bf16_f32 v45, v26, v27\n"
        "s_waitcnt lgkmcnt(3)\n"
        "v_smfmac_f32_16x16x64_bf16 v[48:51], v[20:23], v[124:131], v4\n"
        "s_add_u32 s37, s36, 196224\n"
        "ds_read_b128 v[32:35], v7 offset:6656\n"
        "ds_read_b128 v[36:39], v7 offset:6720\n"
        "v_mfma_f32_16x16x32_bf16 v[56:59], v[28:31], v[52:55], 0\n"
        "v_smfmac_f32_16x16x64_bf16 v[56:59], v[20:23], v[132:139], v4\n"
        "s_setprio 2\n"
        "s_waitcnt lgkmcnt(4)\n"
        "v_mfma_f32_16x16x32_bf16 v[24:27], v[28:31], v[60:63], 0\n"
        "buffer_store_dwordx3 v[116:118], v13, s[8:11], s37 offen offset:1152 sc0 sc1 nt\n"
        "v_add_f32_dpp v165, v50, v51 quad_perm:[0,1,2,3] row_mask:0x1 bank_mask:0xf\n"
        "s_waitcnt lgkmcnt(2)\n"
        "v_add_u32 v167, v9, v167\n"
        "v_add_f32_dpp v164, v48, v49 quad_perm:[0,1,2,3] row_mask:0x1 bank_mask:0xf\n"
        "v_add_f32_dpp v165, v58, v59 quad_perm:[0,1,2,3] row_mask:0x2 bank_mask:0xf\n"
        "v_add_u32 v155, v9, v155\n"
        "v_add_f32_dpp v164, v56, v57 quad_perm:[0,1,2,3] row_mask:0x2 bank_mask:0xf\n"
        "s_waitcnt lgkmcnt(0)\n"
        "v_smfmac_f32_16x16x64_bf16 v[24:27], v[20:23], v[32:39], v4\n"
        "v_mfma_f32_16x16x32_bf16 v[28:31], v[212:215], v[160:163], 0\n"
        "s_add_u32 s37, s36, 294336\n"
        "v_mfma_f32_16x16x32_bf16 v[28:31], v[208:211], v[128:131], v[28:31]\n"
        "v_mfma_f32_16x16x32_bf16 v[28:31], v[204:207], v[124:127], v[28:31]\n"
        "v_mfma_f32_16x16x32_bf16 v[20:23], v[216:219], v[124:127], 0\n"
        "buffer_store_dwordx3 v[120:122], v12, s[8:11], s37 offen offset:576 sc0 sc1 nt\n"
        "v_mfma_f32_16x16x32_bf16 v[20:23], v[220:223], v[128:131], v[20:23]\n"
        "v_mfma_f32_16x16x32_bf16 v[20:23], v[224:227], v[160:163], v[20:23]\n"
        "s_add_u32 s37, s36, 392448\n"
        "v_add_f32_dpp v165, v26, v27 quad_perm:[0,1,2,3] row_mask:0x4 bank_mask:0xf\n"
        "v_mul_f32 v166, s52, v165\n"
        "v_fma_f32 v166, -v166, v165, v159\n"
        "v_cvt_pk_bf16_f32 v46, v28, v29\n"
        "v_cvt_pk_bf16_f32 v47, v30, v31\n"
        "v_add_f32_e64 v166, s53, |v166|\n"
        "v_rsq_f32 v159, v166\n"
        "v_mfma_f32_16x16x32_bf16 v[56:59], v[236:239], v[160:163], 0\n"
        "s_mov_b32 exec_hi, 0xffff\n"
        "ds_write_b32 v10, v159\n"
        "s_mov_b32 exec_hi, -1\n"
        "v_cvt_pk_bf16_f32 v48, v20, v21\n"
        "v_cvt_pk_bf16_f32 v49, v22, v23\n"
        "ds_read_b32 v20, v167 offset:0\n"
        "v_add_f32_dpp v164, v24, v25 quad_perm:[0,1,2,3] row_mask:0x4 bank_mask:0xf\n"
        "v_mfma_f32_16x16x32_bf16 v[56:59], v[232:235], v[128:131], v[56:59]\n"
        "s_mov_b32 exec_hi, 0xffff\n"
        "v_add_u32 v10, 40064, v10\n"
        "s_mov_b32 exec_hi, -1\n"
        "ds_read_b32 v21, v167 offset:832\n"
        "ds_read_b32 v22, v167 offset:1664\n"
        "ds_read_b32 v23, v167 offset:2496\n"
        "ds_read_b32 v24, v167 offset:3328\n"
        "v_mul_f32 v164, v164, v159\n"
        "v_mfma_f32_16x16x32_bf16 v[56:59], v[228:231], v[124:127], v[56:59]\n"
        "buffer_store_dwordx3 v[140:142], v12, s[8:11], s37 offen sc0 sc1 nt\n"
        "ds_read_b32 v25, v167 offset:4160\n"
        "ds_read_b32 v26, v167 offset:4992\n"
        "ds_read_b32 v27, v167 offset:5824\n"
        "ds_read_b32 v28, v167 offset:6656\n"
        "ds_read_b32 v29, v167 offset:7488\n"
        "ds_read_b32 v30, v167 offset:8320\n"
        "ds_read_b32 v31, v167 offset:9152\n"
        "v_cvt_pk_bf16_f32 v50, v56, v57\n"
        "v_cvt_pk_bf16_f32 v51, v58, v59\n"
        "v_readlane_b32 s58, v159, 56\n"
        "v_mfma_f32_16x16x32_bf16 v[56:59], v[124:127], v[40:43], 0\n"
        "s_mov_b64 exec, s[56:57]\n"
        "v_mov_b32 v164, 0xff800000\n"
        "s_mov_b64 exec, -1\n"
        "ds_read_b32 v165, v155 offset:0\n"
        "ds_read_b32 v166, v155 offset:3328\n"
        "v_max_f32_dpp v151, v164, v164 row_ror:8 row_mask:0xf bank_mask:0xf\n"
        "v_mfma_f32_16x16x32_bf16 v[56:59], v[128:131], v[44:47], v[56:59]\n"
        "s_add_u32 s37, s36, 392448\n"
        "v_max_f32_dpp v151, v151, v151 row_ror:4 row_mask:0xf bank_mask:0xf\n"
        "ds_read_b32 v167, v155 offset:6656\n"
        "v_mfma_f32_16x16x32_bf16 v[56:59], v[160:163], v[48:51], v[56:59]\n"
        "v_max_f32_dpp v151, v151, v151 row_ror:2 row_mask:0xf bank_mask:0xf\n"
        "v_mfma_f32_16x16x32_bf16 v[72:75], v[52:55], v[48:51], 0\n"
        "v_mfma_f32_16x16x32_bf16 v[72:75], v[136:139], v[44:47], v[72:75]\n"
        "buffer_store_dwordx3 v[144:146], v15, s[8:11], s37 offen offset:768 sc0 sc1 nt\n"
        "v_max_f32_dpp v151, v151, v151 row_ror:1 row_mask:0xf bank_mask:0xf\n"
        "v_mfma_f32_16x16x32_bf16 v[72:75], v[132:135], v[40:43], v[72:75]\n"
        "ds_read_b128 v[88:91], v8 offset:128\n"
        "v_mfma_f32_16x16x32_bf16 v[68:71], v[32:35], v[40:43], 0\n"
        "s_add_u32 s37, s36, 490560\n"
        "ds_read2_b32 v[92:93], v6 offset0:0 offset1:2\n"
        "ds_read2_b32 v[94:95], v6 offset0:8 offset1:10\n"
        "s_waitcnt lgkmcnt(3)\n"
        "v_mul_f32 v56, v56, v20\n"
        "v_mul_f32 v57, v57, v21\n"
        "v_mul_f32 v58, v58, v22\n"
        "v_mul_f32 v59, v59, v23\n"
        "v_mul_f32 v72, v72, v24\n"
        "v_mfma_f32_16x16x32_bf16 v[68:71], v[36:39], v[44:47], v[68:71]\n"
        "v_max3_f32 v155, v56, v57, v58\n"
        "v_mul_f32 v73, v73, v25\n"
        "v_mul_f32 v74, v74, v26\n"
        "v_max3_f32 v155, v155, v59, v72\n"
        "v_mul_f32 v75, v75, v27\n"
        "v_max3_f32 v155, v155, v73, v74\n"
        "v_mfma_f32_16x16x32_bf16 v[68:71], v[60:63], v[48:51], v[68:71]\n"
        "s_waitcnt lgkmcnt(2)\n"
        "v_mfma_f32_16x16x32_bf16 v[76:79], v[88:91], v[48:51], 0\n"
        "buffer_store_dwordx3 v[148:150], v12, s[8:11], s37 offen offset:192 sc0 sc1 nt\n"
        "s_waitcnt lgkmcnt(0)\n"
        "v_smfmac_f32_16x16x64_bf16 v[76:79], v[92:95], v[40:47], v4\n"
        "v_mfma_f32_16x16x32_bf16 v[48:51], v[168:171], v[132:135], 0\n"
        "s_add_u32 s37, s36, 490560\n"
        "v_mfma_f32_16x16x32_bf16 v[48:51], v[172:175], v[136:139], v[48:51]\n"
        "v_mul_f32 v68, v68, v28\n"
        "v_mul_f32 v69, v69, v29\n"
        "v_mul_f32 v70, v70, v30\n"
        "v_max3_f32 v155, v155, v75, v68\n"
        "v_mul_f32 v71, v71, v31\n"
        "v_add_f32 v76, v76, v77\n"
        "v_mul_f32 v76, s58, v76\n"
        "v_max3_f32 v155, v155, v69, v70\n"
        "v_mfma_f32_16x16x32_bf16 v[48:51], v[176:179], v[52:55], v[48:51]\n"
        "s_mov_b64 exec, s[48:49]\n"
        "v_mov_b32 v76, 0xff800000\n"
        "s_mov_b64 exec, -1\n"
        "v_max3_f32 v155, v155, v71, v76\n"
        "v_mfma_f32_16x16x32_bf16 v[44:47], v[188:191], v[52:55], 0\n"
        "buffer_store_dwordx3 v[152:154], v14, s[8:11], s37 offen offset:960 sc0 sc1 nt\n"
        "v_permlane32_swap_b32 v155, v151\n"
        "v_max_f32 v155, v155, v151\n"
        "v_cvt_pk_bf16_f32 v40, v48, v49\n"
        "v_cvt_pk_bf16_f32 v41, v50, v51\n"
        "v_mov_b32 v154, v155\n"
        "v_mfma_f32_16x16x32_bf16 v[44:47], v[184:187], v[136:139], v[44:47]\n"
        "v_mfma_f32_16x16x32_bf16 v[44:47], v[180:183], v[132:135], v[44:47]\n"
        "s_add_u32 s37, s36, 588672\n"
        "v_permlane16_swap_b32 v155, v154\n"
        "v_max_f32 v155, v155, v154\n"
        "v_mov_b32 v151, v155\n"
        "v_mfma_f32_16x16x32_bf16 v[64:67], v[192:195], v[132:135], 0\n"
        "v_mfma_f32_16x16x32_bf16 v[64:67], v[196:199], v[136:139], v[64:67]\n"
        "v_permlane32_swap_b32 v155, v151\n"
        "v_mul_f32 v155, v155, v165\n"
        "v_cvt_pk_bf16_f32 v42, v44, v45\n"
        "v_cvt_pk_bf16_f32 v43, v46, v47\n"
        "v_fma_f32 v56, v56, v165, -v155\n"
        "v_fma_f32 v57, v57, v165, -v155\n"
        "v_fma_f32 v58, v58, v165, -v155\n"
        "v_mfma_f32_16x16x32_bf16 v[64:67], v[200:203], v[52:55], v[64:67]\n"
        "buffer_store_dwordx3 v[156:158], v12, s[8:11], s37 offen offset:384 sc0 sc1 nt\n"
        "v_exp_f32 v56, v56\n"
        "v_exp_f32 v57, v57\n"
        "v_exp_f32 v58, v58\n"
        "v_fma_f32 v59, v59, v165, -v155\n"
        "v_add_f32 v158, v56, v57\n"
        "v_exp_f32 v59, v59\n"
        "v_mfma_f32_16x16x32_bf16 v[48:51], v[212:215], v[52:55], 0\n"
        "v_cvt_pk_bf16_f32 v44, v64, v65\n"
        "v_cvt_pk_bf16_f32 v45, v66, v67\n"
        "v_fma_f32 v72, v72, v165, -v155\n"
        "v_add_f32 v158, v158, v58\n"
        "v_exp_f32 v72, v72\n"
        "v_fma_f32 v73, v73, v165, -v155\n"
        "v_add_f32 v158, v158, v59\n"
        "v_mfma_f32_16x16x32_bf16 v[48:51], v[208:211], v[136:139], v[48:51]\n"
        "v_exp_f32 v73, v73\n"
        "v_fma_f32 v74, v74, v165, -v155\n"
        "v_add_f32 v158, v158, v72\n"
        "v_exp_f32 v74, v74\n"
        "v_fma_f32 v75, v75, v165, -v155\n"
        "v_sub_f32 v164, v164, v151\n"
        "v_mfma_f32_16x16x32_bf16 v[48:51], v[204:207], v[132:135], v[48:51]\n"
        "v_add_f32 v158, v158, v73\n"
        "v_exp_f32 v75, v75\n"
        "v_fma_f32 v68, v68, v165, -v155\n"
        "v_mul_f32 v164, s58, v164\n"
        "v_add_f32 v158, v158, v74\n"
        "v_exp_f32 v68, v68\n"
        "v_mfma_f32_16x16x32_bf16 v[64:67], v[216:219], v[132:135], 0\n"
        "v_fma_f32 v69, v69, v165, -v155\n"
        "v_cvt_pk_bf16_f32 v46, v48, v49\n"
        "v_cvt_pk_bf16_f32 v47, v50, v51\n"
        "v_exp_f32 v164, v164\n"
        "v_add_f32 v158, v158, v75\n"
        "v_exp_f32 v69, v69\n"
        "v_mfma_f32_16x16x32_bf16 v[64:67], v[220:223], v[136:139], v[64:67]\n"
        "v_fma_f32 v70, v70, v165, -v155\n"
        "v_add_f32 v158, v158, v68\n"
        "v_exp_f32 v70, v70\n"
        "v_fma_f32 v71, v71, v165, -v155\n"
        "v_add_f32_dpp v157, v164, v164 row_ror:8 row_mask:0xf bank_mask:0xf\n"
        "v_add_f32 v158, v158, v69\n"
        "v_fma_f32 v76, v76, v165, -v155\n"
        "v_mfma_f32_16x16x32_bf16 v[64:67], v[224:227], v[52:55], v[64:67]\n"
        "v_exp_f32 v71, v71\n"
        "v_add_f32_dpp v157, v157, v157 row_ror:4 row_mask:0xf bank_mask:0xf\n"
        "v_add_f32 v158, v158, v70\n"
        "v_exp_f32 v76, v76\n"
        "v_add_f32_dpp v157, v157, v157 row_ror:2 row_mask:0xf bank_mask:0xf\n"
        "v_add_f32 v158, v158, v71\n"
        "v_mfma_f32_16x16x32_bf16 v[48:51], v[236:239], v[52:55], 0\n"
        "v_add_f32_dpp v157, v157, v157 row_ror:1 row_mask:0xf bank_mask:0xf\n"
        "v_cvt_pk_bf16_f32 v80, v64, v65\n"
        "v_cvt_pk_bf16_f32 v81, v66, v67\n"
        "v_add_f32 v158, v158, v76\n"
        "v_mul_f32 v56, v56, v20\n"
        "v_mul_f32 v57, v57, v21\n"
        "v_permlane32_swap_b32 v158, v157\n"
        "v_mfma_f32_16x16x32_bf16 v[48:51], v[232:235], v[136:139], v[48:51]\n"
        "v_add_f32 v158, v158, v157\n"
        "v_mov_b32 v165, v158\n"
        "v_mul_f32 v58, v58, v22\n"
        "v_mul_f32 v59, v59, v23\n"
        "v_permlane16_swap_b32 v158, v165\n"
        "v_add_f32 v158, v158, v165\n"
        "v_mul_f32 v72, v72, v24\n"
        "v_mfma_f32_16x16x32_bf16 v[48:51], v[228:231], v[132:135], v[48:51]\n"
        "v_rcp_f32 v158, v158\n"
        "v_mul_f32 v73, v73, v25\n"
        "v_mov_b32 v157, v158\n"
        "v_mul_f32 v74, v74, v26\n"
        "v_mul_f32 v75, v75, v27\n"
        "v_permlane32_swap_b32 v158, v157\n"
        "v_mfma_f32_16x16x32_bf16 v[64:67], v[124:127], v[40:43], 0\n"
        "v_mul_f32 v56, v56, v158\n"
        "v_cvt_pk_bf16_f32 v82, v48, v49\n"
        "v_cvt_pk_bf16_f32 v83, v50, v51\n"
        "v_mul_f32 v57, v57, v158\n"
        "v_mul_f32 v58, v58, v158\n"
        "v_mul_f32 v59, v59, v158\n"
        "v_mul_f32 v72, v72, v158\n"
        "v_mul_f32 v73, v73, v158\n"
        "v_mfma_f32_16x16x32_bf16 v[64:67], v[128:131], v[44:47], v[64:67]\n"
        "v_mul_f32 v74, v74, v158\n"
        "v_mul_f32 v75, v75, v158\n"
        "v_cvt_pk_bf16_f32 v48, v56, v57\n"
        "v_cvt_pk_bf16_f32 v49, v58, v59\n"
        "v_cvt_pk_bf16_f32 v50, v72, v73\n"
        "v_cvt_pk_bf16_f32 v51, v74, v75\n"
        "v_mul_f32 v68, v68, v28\n"
        "v_mul_f32 v69, v69, v29\n"
        "v_mfma_f32_16x16x32_bf16 v[64:67], v[160:163], v[80:83], v[64:67]\n"
        "v_mul_f32 v70, v70, v30\n"
        "v_mul_f32 v71, v71, v31\n"
        "v_mul_f32 v76, s58, v76\n"
        "v_mul_f32 v68, v68, v158\n"
        "v_mul_f32 v69, v69, v158\n"
        "v_mul_f32 v70, v70, v158\n"
        "v_mul_f32 v71, v71, v158\n"
        "v_fma_f32 v76, v76, v158, v19\n"
        "v_mfma_f32_16x16x32_bf16 v[84:87], v[52:55], v[80:83], 0\n"
        "v_mul_f32 v64, v64, v20\n"
        "v_mul_f32 v65, v65, v21\n"
        "v_mul_f32 v66, v66, v22\n"
        "v_mul_f32 v67, v67, v23\n"
        "v_max3_f32 v165, v64, v65, v66\n"
        "v_cvt_pk_bf16_f32 v56, v68, v69\n"
        "v_cvt_pk_bf16_f32 v58, v70, v71\n"
        "v_cvt_pk_bf16_f32 v57, v76, 0\n"
        "v_mfma_f32_16x16x32_bf16 v[84:87], v[136:139], v[44:47], v[84:87]\n"
        "v_mul_f32 v164, v164, v159\n"
        "v_mul_f32 v164, v164, v157\n"
        "v_cvt_pk_bf16_f32 v164, v164, 0\n"
        "ds_write_b16 v1, v164\n"
        "v_mfma_f32_16x16x32_bf16 v[84:87], v[132:135], v[40:43], v[84:87]\n"
        "v_mfma_f32_16x16x32_bf16 v[68:71], v[32:35], v[40:43], 0\n"
        "v_mfma_f32_16x16x32_bf16 v[68:71], v[36:39], v[44:47], v[68:71]\n"
        "v_mfma_f32_16x16x32_bf16 v[68:71], v[60:63], v[80:83], v[68:71]\n"
        "v_mfma_f32_16x16x32_bf16 v[72:75], v[88:91], v[80:83], 0\n"
        "v_smfmac_f32_16x16x64_bf16 v[72:75], v[92:95], v[40:47], v4\n"
        "v_mfma_f32_16x16x32_bf16 v[80:83], v[168:171], v[32:35], 0\n"
        "v_mfma_f32_16x16x32_bf16 v[80:83], v[172:175], v[36:39], v[80:83]\n"
        "v_mfma_f32_16x16x32_bf16 v[80:83], v[176:179], v[60:63], v[80:83]\n"
        "v_mul_f32 v84, v84, v24\n"
        "v_mul_f32 v85, v85, v25\n"
        "v_mul_f32 v86, v86, v26\n"
        "v_max3_f32 v165, v165, v67, v84\n"
        "v_mul_f32 v87, v87, v27\n"
        "v_add_f32 v72, v72, v73\n"
        "v_mul_f32 v68, v68, v28\n"
        "v_mul_f32 v72, s58, v72\n"
        "v_mfma_f32_16x16x32_bf16 v[40:43], v[188:191], v[60:63], 0\n"
        "s_mov_b64 exec, s[48:49]\n"
        "v_mov_b32 v72, 0xff800000\n"
        "s_mov_b64 exec, -1\n"
        "v_cvt_pk_bf16_f32 v76, v80, v81\n"
        "v_cvt_pk_bf16_f32 v77, v82, v83\n"
        "v_max3_f32 v165, v165, v85, v86\n"
        "v_mul_f32 v69, v69, v29\n"
        "v_mul_f32 v70, v70, v30\n"
        "v_max3_f32 v165, v165, v87, v68\n"
        "v_mfma_f32_16x16x32_bf16 v[40:43], v[184:187], v[36:39], v[40:43]\n"
        "v_mul_f32 v71, v71, v31\n"
        "v_max3_f32 v165, v165, v69, v70\n"
        "v_max3_f32 v165, v165, v71, v72\n"
        "v_mfma_f32_16x16x32_bf16 v[40:43], v[180:183], v[32:35], v[40:43]\n"
        "v_mfma_f32_16x16x32_bf16 v[100:103], v[192:195], v[32:35], 0\n"
        "v_mfma_f32_16x16x32_bf16 v[100:103], v[196:199], v[36:39], v[100:103]\n"
        "v_mfma_f32_16x16x32_bf16 v[100:103], v[200:203], v[60:63], v[100:103]\n"
        "v_mfma_f32_16x16x32_bf16 v[96:99], v[212:215], v[60:63], 0\n"
        "v_mfma_f32_16x16x32_bf16 v[96:99], v[208:211], v[36:39], v[96:99]\n"
        "v_mfma_f32_16x16x32_bf16 v[96:99], v[204:207], v[32:35], v[96:99]\n"
        "v_mfma_f32_16x16x32_bf16 v[44:47], v[216:219], v[32:35], 0\n"
        "v_mfma_f32_16x16x32_bf16 v[44:47], v[220:223], v[36:39], v[44:47]\n"
        "v_cvt_pk_bf16_f32 v78, v40, v41\n"
        "v_cvt_pk_bf16_f32 v79, v42, v43\n"
        "v_mfma_f32_16x16x32_bf16 v[44:47], v[224:227], v[60:63], v[44:47]\n"
        "v_cvt_pk_bf16_f32 v80, v100, v101\n"
        "v_cvt_pk_bf16_f32 v81, v102, v103\n"
        "v_mfma_f32_16x16x32_bf16 v[40:43], v[236:239], v[60:63], 0\n"
        "v_cvt_pk_bf16_f32 v82, v96, v97\n"
        "v_cvt_pk_bf16_f32 v83, v98, v99\n"
        "v_mfma_f32_16x16x32_bf16 v[40:43], v[232:235], v[36:39], v[40:43]\n"
        "v_mfma_f32_16x16x32_bf16 v[40:43], v[228:231], v[32:35], v[40:43]\n"
        "v_mfma_f32_16x16x32_bf16 v[108:111], v[124:127], v[76:79], 0\n"
        "v_cvt_pk_bf16_f32 v96, v44, v45\n"
        "v_cvt_pk_bf16_f32 v97, v46, v47\n"
        "v_mfma_f32_16x16x32_bf16 v[108:111], v[128:131], v[80:83], v[108:111]\n"
        "s_nop 3\n"
        "v_cvt_pk_bf16_f32 v98, v40, v41\n"
        "v_cvt_pk_bf16_f32 v99, v42, v43\n"
        "s_nop 1\n"
        "v_mfma_f32_16x16x32_bf16 v[108:111], v[160:163], v[96:99], v[108:111]\n"
        "v_mfma_f32_16x16x32_bf16 v[140:143], v[52:55], v[96:99], 0\n"
        "v_mfma_f32_16x16x32_bf16 v[140:143], v[136:139], v[80:83], v[140:143]\n"
        "v_mfma_f32_16x16x32_bf16 v[140:143], v[132:135], v[76:79], v[140:143]\n"
        "v_mfma_f32_16x16x32_bf16 v[100:103], v[32:35], v[76:79], 0\n"
        "v_mfma_f32_16x16x32_bf16 v[100:103], v[36:39], v[80:83], v[100:103]\n"
        "v_mfma_f32_16x16x32_bf16 v[100:103], v[60:63], v[96:99], v[100:103]\n"
        "v_mfma_f32_16x16x32_bf16 v[44:47], v[88:91], v[96:99], 0\n"
        "v_smfmac_f32_16x16x64_bf16 v[44:47], v[92:95], v[76:83], v4\n"
        "v_mul_f32 v108, v108, v20\n"
        "v_mul_f32 v109, v109, v21\n"
        "v_mul_f32 v110, v110, v22\n"
        "v_mul_f32 v111, v111, v23\n"
        "v_mul_f32 v140, v140, v24\n"
        "v_max3_f32 v159, v108, v109, v110\n"
        "v_mul_f32 v141, v141, v25\n"
        "v_mfma_f32_16x16x32_bf16 v[60:63], v[124:127], v[240:243], 0\n"
        "v_add_f32 v44, v44, v45\n"
        "v_mul_f32 v142, v142, v26\n"
        "v_max3_f32 v159, v159, v111, v140\n"
        "v_mul_f32 v143, v143, v27\n"
        "v_mul_f32 v100, v100, v28\n"
        "v_mul_f32 v44, s58, v44\n"
        "v_max3_f32 v159, v159, v141, v142\n"
        "v_mul_f32 v101, v101, v29\n"
        "v_mfma_f32_16x16x32_bf16 v[60:63], v[128:131], v[244:247], v[60:63]\n"
        "s_mov_b64 exec, s[48:49]\n"
        "v_mov_b32 v44, 0xff800000\n"
        "s_mov_b64 exec, -1\n"
        "v_mul_f32 v102, v102, v30\n"
        "v_max3_f32 v159, v159, v143, v100\n"
        "v_mul_f32 v103, v103, v31\n"
        "v_max3_f32 v159, v159, v101, v102\n"
        "v_max3_f32 v159, v159, v103, v44\n"
        "v_mfma_f32_16x16x32_bf16 v[60:63], v[160:163], v[248:251], v[60:63]\n"
        "ds_read_b128 v[32:35], v17 offset:0\n"
        "v_permlane32_swap_b32 v165, v159\n"
        "v_max_f32 v165, v165, v159\n"
        "v_mov_b32 v164, v165\n"
        "v_mfma_f32_16x16x32_bf16 v[76:79], v[52:55], v[248:251], 0\n"
        "ds_read_b128 v[36:39], v17 offset:64\n"
        "v_permlane16_swap_b32 v165, v164\n"
        "v_max_f32 v165, v165, v164\n"
        "v_cvt_pk_bf16_f32 v40, v60, v61\n"
        "v_cvt_pk_bf16_f32 v41, v62, v63\n"
        "v_mov_b32 v159, v165\n"
        "v_mfma_f32_16x16x32_bf16 v[76:79], v[136:139], v[244:247], v[76:79]\n"
        "ds_read_b128 v[60:63], v17 offset:128\n"
        "v_permlane32_swap_b32 v165, v159\n"
        "v_mul_f32 v165, v165, v166\n"
        "v_mul_f32 v159, v159, v167\n"
        "v_fma_f32 v64, v64, v166, -v165\n"
        "v_fma_f32 v65, v65, v166, -v165\n"
        "v_fma_f32 v108, v108, v167, -v159\n"
        "v_mfma_f32_16x16x32_bf16 v[76:79], v[132:135], v[240:243], v[76:79]\n"
        "s_waitcnt lgkmcnt(2)\n"
        "ds_read_b128 v[96:99], v0 offset:5136\n"
        "v_fma_f32 v109, v109, v167, -v159\n"
        "v_exp_f32 v64, v64\n"
        "v_exp_f32 v65, v65\n"
        "v_fma_f32 v66, v66, v166, -v165\n"
        "v_fma_f32 v110, v110, v167, -v159\n"
        "v_mfma_f32_16x16x32_bf16 v[80:83], v[32:35], v[240:243], 0\n"
        "s_waitcnt lgkmcnt(2)\n"
        "ds_read_b128 v[104:107], v0 offset:4112\n"
        "v_cvt_pk_bf16_f32 v42, v76, v77\n"
        "v_cvt_pk_bf16_f32 v43, v78, v79\n"
        "v_exp_f32 v108, v108\n"
        "v_exp_f32 v109, v109\n"
        "v_fma_f32 v67, v67, v166, -v165\n"
        "v_mfma_f32_16x16x32_bf16 v[80:83], v[36:39], v[244:247], v[80:83]\n"
        "s_waitcnt lgkmcnt(2)\n"
        "v_exp_f32 v66, v66\n"
        "v_exp_f32 v110, v110\n"
        "v_fma_f32 v111, v111, v167, -v159\n"
        "v_add_f32 v95, v64, v65\n"
        "v_exp_f32 v67, v67\n"
        "v_mfma_f32_16x16x32_bf16 v[80:83], v[60:63], v[248:251], v[80:83]\n"
        "s_waitcnt lgkmcnt(1)\n"
        "v_fma_f32 v84, v84, v166, -v165\n"
        "v_add_f32 v164, v108, v109\n"
        "v_exp_f32 v111, v111\n"
        "v_fma_f32 v140, v140, v167, -v159\n"
        "v_add_f32 v95, v95, v66\n"
        "v_exp_f32 v84, v84\n"
        "v_mfma_f32_16x16x32_bf16 v[112:115], v[60:63], v[96:99], 0\n"
        "s_waitcnt lgkmcnt(0)\n"
        "v_cvt_pk_bf16_f32 v76, v80, v81\n"
        "v_cvt_pk_bf16_f32 v80, v82, v83\n"
        "v_fma_f32 v85, v85, v166, -v165\n"
        "v_add_f32 v164, v164, v110\n"
        "v_exp_f32 v140, v140\n"
        "v_fma_f32 v141, v141, v167, -v159\n"
        "v_add_f32 v95, v95, v67\n"
        "v_mfma_f32_16x16x32_bf16 v[112:115], v[36:39], v[104:107], v[112:115]\n"
        "v_exp_f32 v85, v85\n"
        "v_fma_f32 v86, v86, v166, -v165\n"
        "v_add_f32 v164, v164, v111\n"
        "v_exp_f32 v141, v141\n"
        "v_fma_f32 v142, v142, v167, -v159\n"
        "v_add_f32 v95, v95, v84\n"
        "v_mfma_f32_16x16x32_bf16 v[112:115], v[32:35], v[252:255], v[112:115]\n"
        "v_exp_f32 v86, v86\n"
        "v_fma_f32 v87, v87, v166, -v165\n"
        "v_add_f32 v164, v164, v140\n"
        "v_exp_f32 v142, v142\n"
        "v_fma_f32 v143, v143, v167, -v159\n"
        "v_add_f32 v95, v95, v85\n"
        "v_mfma_f32_16x16x32_bf16 v[120:123], v[132:135], v[252:255], 0\n"
        "v_exp_f32 v87, v87\n"
        "v_cvt_pk_bf16_f32 v88, v112, v113\n"
        "v_cvt_pk_bf16_f32 v92, v114, v115\n"
        "v_fma_f32 v68, v68, v166, -v165\n"
        "v_add_f32 v164, v164, v141\n"
        "v_exp_f32 v143, v143\n"
        "v_mfma_f32_16x16x32_bf16 v[120:123], v[136:139], v[104:107], v[120:123]\n"
        "v_fma_f32 v100, v100, v167, -v159\n"
        "v_add_f32 v95, v95, v86\n"
        "v_exp_f32 v68, v68\n"
        "v_fma_f32 v69, v69, v166, -v165\n"
        "v_add_f32 v164, v164, v142\n"
        "v_exp_f32 v100, v100\n"
        "v_mfma_f32_16x16x32_bf16 v[120:123], v[52:55], v[96:99], v[120:123]\n"
        "ds_read_b128 v[144:147], v0 offset:6176\n"
        "v_fma_f32 v101, v101, v167, -v159\n"
        "v_add_f32 v95, v95, v87\n"
        "v_exp_f32 v69, v69\n"
        "v_fma_f32 v70, v70, v166, -v165\n"
        "v_add_f32 v164, v164, v143\n"
        "v_fma_f32 v102, v102, v167, -v159\n"
        "v_mfma_f32_16x16x32_bf16 v[148:151], v[160:163], v[96:99], 0\n"
        "ds_read_b128 v[116:119], v0 offset:7200\n"
        "v_cvt_pk_bf16_f32 v98, v120, v121\n"
        "v_cvt_pk_bf16_f32 v99, v122, v123\n"
        "v_exp_f32 v101, v101\n"
        "v_add_f32 v95, v95, v68\n"
        "v_exp_f32 v70, v70\n"
        "v_mfma_f32_16x16x32_bf16 v[148:151], v[128:131], v[104:107], v[148:151]\n"
        "ds_read_b128 v[112:115], v0 offset:8224\n"
        "v_fma_f32 v71, v71, v166, -v165\n"
        "v_add_f32 v164, v164, v100\n"
        "v_exp_f32 v102, v102\n"
        "v_fma_f32 v103, v103, v167, -v159\n"
        "v_add_f32 v95, v95, v69\n"
        "v_fma_f32 v72, v72, v166, -v165\n"
        "v_mfma_f32_16x16x32_bf16 v[148:151], v[124:127], v[252:255], v[148:151]\n"
        "s_waitcnt lgkmcnt(2)\n"
        "v_exp_f32 v71, v71\n"
        "v_add_f32 v164, v164, v101\n"
        "v_exp_f32 v103, v103\n"
        "v_fma_f32 v44, v44, v167, -v159\n"
        "v_add_f32 v95, v95, v70\n"
        "v_add_f32 v164, v164, v102\n"
        "v_mfma_f32_16x16x32_bf16 v[120:123], v[124:127], v[144:147], 0\n"
        "s_waitcnt lgkmcnt(1)\n"
        "v_cvt_pk_bf16_f32 v96, v148, v149\n"
        "v_cvt_pk_bf16_f32 v97, v150, v151\n"
        "v_exp_f32 v72, v72\n"
        "v_exp_f32 v44, v44\n"
        "v_add_f32 v95, v95, v71\n"
        "v_add_f32 v164, v164, v103\n"
        "v_mfma_f32_16x16x32_bf16 v[120:123], v[128:131], v[116:119], v[120:123]\n"
        "s_waitcnt lgkmcnt(0)\n"
        "v_add_f32 v95, v95, v72\n"
        "v_add_f32 v164, v164, v44\n"
        "v_mul_f32 v64, v64, v20\n"
        "v_mul_f32 v65, v65, v21\n"
        "v_permlane32_swap_b32 v95, v164\n"
        "v_add_f32 v95, v95, v164\n"
        "v_mov_b32 v167, v95\n"
        "v_mfma_f32_16x16x32_bf16 v[120:123], v[160:163], v[112:115], v[120:123]\n"
        "v_mul_f32 v66, v66, v22\n"
        "v_permlane16_swap_b32 v95, v167\n"
        "v_add_f32 v95, v95, v167\n"
        "v_rcp_f32 v95, v95\n"
        "v_mul_f32 v67, v67, v23\n"
        "v_mov_b32 v164, v95\n"
        "v_mfma_f32_16x16x32_bf16 v[156:159], v[52:55], v[112:115], 0\n"
        "v_mul_f32 v84, v84, v24\n"
        "v_cvt_pk_bf16_f32 v104, v120, v121\n"
        "v_cvt_pk_bf16_f32 v105, v122, v123\n"
        "v_permlane32_swap_b32 v95, v164\n"
        "v_mul_f32 v85, v85, v25\n"
        "v_mul_f32 v86, v86, v26\n"
        "v_mul_f32 v87, v87, v27\n"
        "v_mfma_f32_16x16x32_bf16 v[156:159], v[136:139], v[116:119], v[156:159]\n"
        "v_mul_f32 v64, v64, v95\n"
        "v_mul_f32 v65, v65, v95\n"
        "v_mul_f32 v66, v66, v95\n"
        "v_mul_f32 v67, v67, v95\n"
        "v_mul_f32 v84, v84, v95\n"
        "v_mul_f32 v85, v85, v95\n"
        "v_mul_f32 v86, v86, v95\n"
        "v_mul_f32 v87, v87, v95\n"
        "v_mfma_f32_16x16x32_bf16 v[156:159], v[132:135], v[144:147], v[156:159]\n"
        "ds_read_b128 v[148:151], v0 offset:11312\n"
        "v_cvt_pk_bf16_f32 v120, v64, v65\n"
        "v_cvt_pk_bf16_f32 v121, v66, v67\n"
        "v_cvt_pk_bf16_f32 v122, v84, v85\n"
        "v_cvt_pk_bf16_f32 v123, v86, v87\n"
        "v_mul_f32 v68, v68, v28\n"
        "v_mul_f32 v69, v69, v29\n"
        "v_mul_f32 v70, v70, v30\n"
        "v_mfma_f32_16x16x32_bf16 v[152:155], v[32:35], v[144:147], 0\n"
        "ds_read_b128 v[84:87], v0 offset:10288\n"
        "v_cvt_pk_bf16_f32 v106, v156, v157\n"
        "v_cvt_pk_bf16_f32 v107, v158, v159\n"
        "v_mul_f32 v71, v71, v31\n"
        "v_mul_f32 v72, s58, v72\n"
        "v_mul_f32 v68, v68, v95\n"
        "v_mul_f32 v69, v69, v95\n"
        "v_mul_f32 v70, v70, v95\n"
        "v_mfma_f32_16x16x32_bf16 v[152:155], v[36:39], v[116:119], v[152:155]\n"
        "ds_read_b128 v[144:147], v0 offset:9264\n"
        "v_mul_f32 v71, v71, v95\n"
        "v_fma_f32 v72, v72, v95, v19\n"
        "v_cvt_pk_bf16_f32 v64, v68, v69\n"
        "v_cvt_pk_bf16_f32 v66, v70, v71\n"
        "v_cvt_pk_bf16_f32 v65, v72, 0\n"
        "v_mul_f32 v108, v108, v20\n"
        "v_mul_f32 v109, v109, v21\n"
        "v_mfma_f32_16x16x32_bf16 v[152:155], v[60:63], v[112:115], v[152:155]\n"
        "s_waitcnt lgkmcnt(2)\n"
        "v_mul_f32 v110, v110, v22\n"
        "v_mul_f32 v111, v111, v23\n"
        "v_mul_f32 v140, v140, v24\n"
        "v_mul_f32 v141, v141, v25\n"
        "v_mul_f32 v142, v142, v26\n"
        "v_mul_f32 v143, v143, v27\n"
        "v_mul_f32 v108, v108, v164\n"
        "v_cvt_pk_bf16_f32 v112, v152, v153\n"
        "v_mfma_f32_16x16x32_bf16 v[20:23], v[60:63], v[148:151], 0\n"
        "s_waitcnt lgkmcnt(1)\n"
        "v_cvt_pk_bf16_f32 v116, v154, v155\n"
        "v_mul_f32 v109, v109, v164\n"
        "v_mul_f32 v110, v110, v164\n"
        "v_mul_f32 v111, v111, v164\n"
        "v_mul_f32 v140, v140, v164\n"
        "v_mul_f32 v141, v141, v164\n"
        "v_mul_f32 v142, v142, v164\n"
        "v_mul_f32 v143, v143, v164\n"
        "v_mfma_f32_16x16x32_bf16 v[20:23], v[36:39], v[84:87], v[20:23]\n"
        "s_waitcnt lgkmcnt(0)\n"
        "v_cvt_pk_bf16_f32 v24, v108, v109\n"
        "v_cvt_pk_bf16_f32 v25, v110, v111\n"
        "v_cvt_pk_bf16_f32 v26, v140, v141\n"
        "v_cvt_pk_bf16_f32 v27, v142, v143\n"
        "v_mul_f32 v100, v100, v28\n"
        "v_mul_f32 v101, v101, v29\n"
        "v_mul_f32 v102, v102, v30\n"
        "v_mul_f32 v103, v103, v31\n"
        "v_mfma_f32_16x16x32_bf16 v[20:23], v[32:35], v[144:147], v[20:23]\n"
        "v_mul_f32 v44, s58, v44\n"
        "v_mul_f32 v100, v100, v164\n"
        "v_mul_f32 v101, v101, v164\n"
        "v_mul_f32 v102, v102, v164\n"
        "v_mul_f32 v103, v103, v164\n"
        "v_fma_f32 v44, v44, v164, v19\n"
        "v_cvt_pk_bf16_f32 v28, v100, v101\n"
        "v_cvt_pk_bf16_f32 v30, v102, v103\n"
        "v_mfma_f32_16x16x32_bf16 v[68:71], v[132:135], v[144:147], 0\n"
        "v_cvt_pk_bf16_f32 v77, v20, v21\n"
        "v_cvt_pk_bf16_f32 v81, v22, v23\n"
        "v_cvt_pk_bf16_f32 v29, v44, 0\n"
        "v_mfma_f32_16x16x32_bf16 v[68:71], v[136:139], v[84:87], v[68:71]\n"
        "v_mfma_f32_16x16x32_bf16 v[68:71], v[52:55], v[148:151], v[68:71]\n"
        "ds_read_b128 v[20:23], v0 offset:12352\n"
        "v_mfma_f32_16x16x32_bf16 v[72:75], v[160:163], v[148:151], 0\n"
        "ds_read_b128 v[108:111], v0 offset:13376\n"
        "v_mfma_f32_16x16x32_bf16 v[72:75], v[128:131], v[84:87], v[72:75]\n"
        "ds_read_b128 v[140:143], v0 offset:14400\n"
        "v_mfma_f32_16x16x32_bf16 v[72:75], v[124:127], v[144:147], v[72:75]\n"
        "s_waitcnt lgkmcnt(2)\n"
        "v_mfma_f32_16x16x32_bf16 v[144:147], v[124:127], v[20:23], 0\n"
        "v_cvt_pk_bf16_f32 v46, v68, v69\n"
        "v_cvt_pk_bf16_f32 v47, v70, v71\n"
        "s_waitcnt lgkmcnt(1)\n"
        "v_mfma_f32_16x16x32_bf16 v[144:147], v[128:131], v[108:111], v[144:147]\n"
        "s_waitcnt lgkmcnt(0)\n"
        "v_mfma_f32_16x16x32_bf16 v[144:147], v[160:163], v[140:143], v[144:147]\n"
        "v_cvt_pk_bf16_f32 v44, v72, v73\n"
        "v_cvt_pk_bf16_f32 v45, v74, v75\n"
        "v_mfma_f32_16x16x32_bf16 v[148:151], v[52:55], v[140:143], 0\n"
        "v_mfma_f32_16x16x32_bf16 v[148:151], v[136:139], v[108:111], v[148:151]\n"
        "v_mfma_f32_16x16x32_bf16 v[148:151], v[132:135], v[20:23], v[148:151]\n"
        "ds_read_b128 v[72:75], v0 offset:17488\n"
        "v_mfma_f32_16x16x32_bf16 v[84:87], v[32:35], v[20:23], 0\n"
        "ds_read_b128 v[68:71], v0 offset:16464\n"
        "v_cvt_pk_bf16_f32 v100, v144, v145\n"
        "v_cvt_pk_bf16_f32 v101, v146, v147\n"
        "v_mfma_f32_16x16x32_bf16 v[84:87], v[36:39], v[108:111], v[84:87]\n"
        "ds_read_b128 v[20:23], v0 offset:15440\n"
        "v_mfma_f32_16x16x32_bf16 v[84:87], v[60:63], v[140:143], v[84:87]\n"
        "v_cvt_pk_bf16_f32 v102, v148, v149\n"
        "v_cvt_pk_bf16_f32 v103, v150, v151\n"
        "s_waitcnt lgkmcnt(2)\n"
        "v_mfma_f32_16x16x32_bf16 v[108:111], v[60:63], v[72:75], 0\n"
        "s_waitcnt lgkmcnt(1)\n"
        "v_mfma_f32_16x16x32_bf16 v[108:111], v[36:39], v[68:71], v[108:111]\n"
        "s_waitcnt lgkmcnt(0)\n"
        "v_mfma_f32_16x16x32_bf16 v[108:111], v[32:35], v[20:23], v[108:111]\n"
        "ds_read2_b32 v[82:83], v11 offset0:0 offset1:3\n"
        "v_cvt_pk_bf16_f32 v89, v84, v85\n"
        "v_cvt_pk_bf16_f32 v93, v86, v87\n"
        "v_mov_b32_dpp v59, v19 row_shr:4 row_mask:0xf bank_mask:0xf bound_ctrl:0\n"
        "v_mfma_f32_16x16x32_bf16 v[36:39], v[132:135], v[20:23], 0\n"
        "ds_read_b64_tr_b16 v[78:79], v9 offset:0\n"
        "v_mfma_f32_16x16x32_bf16 v[36:39], v[136:139], v[68:71], v[36:39]\n"
        "ds_read2_b32 v[94:95], v11 offset0:1 offset1:4\n"
        "v_cvt_pk_bf16_f32 v113, v108, v109\n"
        "v_cvt_pk_bf16_f32 v117, v110, v111\n"
        "v_mfma_f32_16x16x32_bf16 v[36:39], v[52:55], v[72:75], v[36:39]\n"
        "s_add_u32 s31, s31, 1\n"
        "ds_read_b64_tr_b16 v[90:91], v9 offset:16\n"
        "v_mfma_f32_16x16x32_bf16 v[32:35], v[160:163], v[72:75], 0\n"
        "s_cmp_ge_u32 s31, s71\n"
        "ds_read2_b32 v[118:119], v11 offset0:2 offset1:5\n"
        "v_mfma_f32_16x16x32_bf16 v[32:35], v[128:131], v[68:71], v[32:35]\n"
        "s_cbranch_scc1 L52A_%=\n"
        "L53A_%=:\n"
        "ds_read_b64_tr_b16 v[114:115], v9 offset:32\n"
        "v_cvt_pk_bf16_f32 v110, v36, v37\n"
        "v_cvt_pk_bf16_f32 v111, v38, v39\n"
        "v_mfma_f32_16x16x32_bf16 v[32:35], v[124:127], v[20:23], v[32:35]\n"
        "s_add_u32 s16, s16, 256\n"
        "v_mfma_f32_16x16x32_bf16 v[20:23], v[48:51], v[40:43], 0\n"
        "s_waitcnt lgkmcnt(4)\n"
        "s_cmp_ge_u32 s31, 20\n"
        "v_smfmac_f32_16x16x64_bf16 v[20:23], v[56:59], v[76:83], v18\n"
        "s_cselect_b32 s39, s22, s16\n"
        "v_mfma_f32_16x16x32_bf16 v[36:39], v[48:51], v[96:99], 0\n"
        "s_waitcnt lgkmcnt(2)\n"
        "v_cvt_pk_bf16_f32 v108, v32, v33\n"
        "v_cvt_pk_bf16_f32 v109, v34, v35\n"
        "s_cmp_ge_u32 s31, 21\n"
        "v_smfmac_f32_16x16x64_bf16 v[36:39], v[56:59], v[88:95], v18\n"
        "s_cselect_b32 s39, s23, s39\n"
        "ds_read_b64_tr_b16 v[78:79], v9 offset:48\n"
        "v_mfma_f32_16x16x32_bf16 v[32:35], v[48:51], v[104:107], 0\n"
        "s_waitcnt lgkmcnt(1)\n"
        "s_mul_hi_u32 s38, s39, 58835169\n"
        "v_smfmac_f32_16x16x64_bf16 v[32:35], v[56:59], v[112:119], v18\n"
        "s_mul_i32 s39, s39, 1344\n"
        "ds_read_b64_tr_b16 v[90:91], v9 offset:64\n"
        "v_cvt_pk_bf16_f32 v152, v20, v36\n"
        "v_cvt_pk_bf16_f32 v156, v21, v37\n"
        "v_cvt_pk_bf16_f32 v160, v22, v38\n"
        "v_cvt_pk_bf16_f32 v164, v23, v39\n"
        "v_mfma_f32_16x16x32_bf16 v[68:71], v[48:51], v[44:47], 0\n"
        "s_mul_i32 s38, s38, 588672\n"
        "s_waitcnt lgkmcnt(1)\n"
        "v_smfmac_f32_16x16x64_bf16 v[68:71], v[56:59], v[76:83], v18 abid:1\n"
        "s_add_u32 s39, s39, s38\n"
        "ds_read_b64_tr_b16 v[114:115], v9 offset:80\n"
        "v_mfma_f32_16x16x32_bf16 v[60:63], v[48:51], v[100:103], 0\n"
        "s_add_u32 s39, s39, s17\n"
        "ds_read_b128 v[36:39], v2\n"
        "v_mov_b64 v[20:21], 0\n"
        "v_mov_b64 v[22:23], 0\n"
        "s_waitcnt lgkmcnt(2)\n"
        "v_smfmac_f32_16x16x64_bf16 v[60:63], v[56:59], v[88:95], v18 abid:1\n"
        "s_mul_i32 s68, s31, 3\n"
        "ds_read_b128 v[52:55], v3\n"
        "v_cvt_pk_bf16_f32 v153, v32, v68\n"
        "v_cvt_pk_bf16_f32 v157, v33, v69\n"
        "v_cvt_pk_bf16_f32 v161, v34, v70\n"
        "v_cvt_pk_bf16_f32 v165, v35, v71\n"
        "v_mfma_f32_16x16x32_bf16 v[68:71], v[48:51], v[108:111], 0\n"
        "s_bitcmp1_b32 s31, 0\n"
        "v_mov_b64 v[32:33], 0\n"
        "v_mov_b64 v[34:35], 0\n"
        "s_waitcnt lgkmcnt(2)\n"
        "v_smfmac_f32_16x16x64_bf16 v[68:71], v[56:59], v[112:119], v18 abid:1\n"
        "s_cselect_b32 s38, 3, 10\n"
        "s_waitcnt lgkmcnt(1)\n"
        "v_mov_b32_dpp v67, v19 row_shr:4 row_mask:0xf bank_mask:0xf bound_ctrl:0\n"
        "v_smfmac_f32_16x16x64_bf16 v[20:23], v[36:39], v[40:47], v4\n"
        "s_add_u32 s68, s68, s38\n"
        "v_mov_b64 v[48:49], 0\n"
        "v_mov_b64 v[50:51], 0\n"
        "s_waitcnt lgkmcnt(0)\n"
        "v_smfmac_f32_16x16x64_bf16 v[20:23], v[52:55], v[76:83], v4 abid:1\n"
        "s_add_u32 s37, s35, 0\n"
        "s_cmp_lt_u32 s31, s14\n"
        "v_cvt_pk_bf16_f32 v154, v60, v68\n"
        "v_cvt_pk_bf16_f32 v158, v61, v69\n"
        "v_cvt_pk_bf16_f32 v162, v62, v70\n"
        "v_cvt_pk_bf16_f32 v166, v63, v71\n"
        "v_smfmac_f32_16x16x64_bf16 v[32:35], v[36:39], v[96:103], v4\n"
        "s_cselect_b32 s68, s68, 0\n"
        "v_add_f32 v20, v20, v21\n"
        "v_add_f32 v22, v22, v23\n"
        "v_smfmac_f32_16x16x64_bf16 v[32:35], v[52:55], v[88:95], v4 abid:1\n"
        "s_and_b32 s38, s31, 7\n"
        "ds_read_b64_tr_b16 v[78:79], v9 offset:3328\n"
        "v_smfmac_f32_16x16x64_bf16 v[48:51], v[36:39], v[104:111], v4\n"
        "buffer_store_dwordx3 v[152:154], v12, s[8:11], s37 offen sc0 sc1 nt\n"
        "s_cselect_b32 s30, 1, -7\n"
        "v_smfmac_f32_16x16x64_bf16 v[48:51], v[52:55], v[112:119], v4 abid:1\n"
        "s_mul_i32 s29, s30, 192\n"
        "ds_read_b64_tr_b16 v[90:91], v9 offset:3344\n"
        "v_add_f32 v32, v32, v33\n"
        "v_add_f32 v34, v34, v35\n"
        "v_cvt_pk_bf16_f32 v148, v20, v32\n"
        "v_mfma_f32_16x16x32_bf16 v[152:155], v[120:123], v[40:43], 0\n"
        "s_mul_i32 s30, s30, 392\n"
        "s_waitcnt lgkmcnt(1)\n"
        "v_smfmac_f32_16x16x64_bf16 v[152:155], v[64:67], v[76:83], v18\n"
        "s_add_u32 s37, s35, 0\n"
        "s_cmp_eq_u32 s69, s18\n"
        "ds_read_b64_tr_b16 v[114:115], v9 offset:3360\n"
        "v_add_f32 v48, v48, v49\n"
        "v_add_f32 v50, v50, v51\n"
        "v_cvt_pk_bf16_f32 v149, v48, v22\n"
        "v_cvt_pk_bf16_f32 v150, v34, v50\n"
        "v_mfma_f32_16x16x32_bf16 v[56:59], v[120:123], v[96:99], 0\n"
        "s_cselect_b32 s34, -7488, s70\n"
        "s_waitcnt lgkmcnt(1)\n"
        "v_smfmac_f32_16x16x64_bf16 v[56:59], v[64:67], v[88:95], v18\n"
        "s_add_u32 s69, s69, s34\n"
        "ds_read_b64_tr_b16 v[78:79], v9 offset:3376\n"
        "v_writelane_b32 v10, s69, 56\n"
        "v_add_u32 v11, s30, v11\n"
        "s_mov_b64 exec, s[62:63]\n"
        "v_add_u32 v16, s29, v16\n"
        "v_add_u32 v8, s34, v8\n"
        "s_mov_b64 exec, -1\n"
        "v_mfma_f32_16x16x32_bf16 v[72:75], v[120:123], v[104:107], 0\n"
        "buffer_store_dwordx3 v[156:158], v15, s[8:11], s37 offen offset:768 sc0 sc1 nt\n"
        "s_mov_b64 exec, s[50:51]\n"
        "v_add_u32 v5, s29, v5\n"
        "v_add_u32 v6, s34, v6\n"
        "s_mov_b64 exec, -1\n"
        "s_waitcnt lgkmcnt(1)\n"
        "v_smfmac_f32_16x16x64_bf16 v[72:75], v[64:67], v[112:119], v18\n"
        "ds_read_b64_tr_b16 v[90:91], v9 offset:3392\n"
        "v_mov_b32_dpp v31, v19 row_shr:4 row_mask:0xf bank_mask:0xf bound_ctrl:0\n"
        "v_mfma_f32_16x16x32_bf16 v[20:23], v[120:123], v[44:47], 0\n"
        "s_waitcnt lgkmcnt(1)\n"
        "v_smfmac_f32_16x16x64_bf16 v[20:23], v[64:67], v[76:83], v18 abid:1\n"
        "s_add_u32 s37, s35, 98112\n"
        "ds_read_b64_tr_b16 v[114:115], v9 offset:3408\n"
        "v_mfma_f32_16x16x32_bf16 v[84:87], v[120:123], v[100:103], 0\n"
        "s_waitcnt lgkmcnt(1)\n"
        "v_smfmac_f32_16x16x64_bf16 v[84:87], v[64:67], v[88:95], v18 abid:1\n"
        "ds_read_b64_tr_b16 v[78:79], v9 offset:6656\n"
        "v_mfma_f32_16x16x32_bf16 v[60:63], v[120:123], v[108:111], 0\n"
        "buffer_store_dwordx3 v[160:162], v12, s[8:11], s37 offen offset:192 sc0 sc1 nt\n"
        "ds_read_b32 v167, v0 offset:21604\n"
        "s_waitcnt lgkmcnt(2)\n"
        "v_smfmac_f32_16x16x64_bf16 v[60:63], v[64:67], v[112:119], v18 abid:1\n"
        "ds_read_b64_tr_b16 v[90:91], v9 offset:6672\n"
        "v_mfma_f32_16x16x32_bf16 v[68:71], v[24:27], v[40:43], 0\n"
        "s_waitcnt lgkmcnt(2)\n"
        "v_smfmac_f32_16x16x64_bf16 v[68:71], v[28:31], v[76:83], v18\n"
        "s_add_u32 s37, s35, 98112\n"
        "ds_read_b64_tr_b16 v[114:115], v9 offset:6688\n"
        "v_mfma_f32_16x16x32_bf16 v[36:39], v[24:27], v[96:99], 0\n"
        "s_waitcnt lgkmcnt(1)\n"
        "v_smfmac_f32_16x16x64_bf16 v[36:39], v[28:31], v[88:95], v18\n"
        "ds_read_b64_tr_b16 v[78:79], v9 offset:6704\n"
        "v_readfirstlane_b32 s40, v167\n"
        "v_mfma_f32_16x16x32_bf16 v[40:43], v[24:27], v[104:107], 0\n"
        "buffer_store_dwordx3 v[164:166], v14, s[8:11], s37 offen offset:960 sc0 sc1 nt\n"
        "s_add_u32 s37, s35, 588672\n"
        "s_add_u32 s46, s37, 1344\n"
        "s_cmp_lt_i32 s40, s68\n"
        "s_waitcnt lgkmcnt(1)\n"
        "v_smfmac_f32_16x16x64_bf16 v[40:43], v[28:31], v[112:119], v18\n"
        "buffer_store_dwordx3 v[148:150], v12, s[44:47], s37 offen offset:1152 sc0 sc1 nt\n"
        "ds_read_b64_tr_b16 v[90:91], v9 offset:6720\n"
        "v_mfma_f32_16x16x32_bf16 v[64:67], v[24:27], v[44:47], 0\n"
        "s_waitcnt lgkmcnt(1)\n"
        "v_smfmac_f32_16x16x64_bf16 v[64:67], v[28:31], v[76:83], v18 abid:1\n"
        "ds_read_b64_tr_b16 v[114:115], v9 offset:6736\n"
        "s_mov_b64 exec, 1\n"
        "ds_inc_u32 v0, v1 offset:21608\n"
        "s_mov_b64 exec, -1\n"
        "s_cbranch_scc1 L56A_%=\n"
        "L57A_%=:\n"
        "ds_read_b128 v[124:127], v7 offset:40064\n"
        "ds_read_b128 v[132:135], v7 offset:43392\n"
        "ds_read_b128 v[128:131], v7 offset:40128\n"
        "ds_read_b128 v[136:139], v7 offset:43456\n"
        "v_mfma_f32_16x16x32_bf16 v[104:107], v[24:27], v[100:103], 0\n"
        "s_waitcnt lgkmcnt(6)\n"
        "ds_read_b128 v[160:163], v7 offset:40192\n"
        "ds_read_b128 v[52:55], v7 offset:43520\n"
        "s_mov_b32 s36, s35\n"
        "v_smfmac_f32_16x16x64_bf16 v[104:107], v[28:31], v[88:95], v18 abid:1\n"
        "s_mov_b32 s35, s39\n"
        "v_mfma_f32_16x16x32_bf16 v[88:91], v[24:27], v[108:111], 0\n"
        "s_waitcnt lgkmcnt(7)\n"
        "v_smfmac_f32_16x16x64_bf16 v[88:91], v[28:31], v[112:119], v18 abid:1\n"
        "s_setprio 0\n"
        "s_waitcnt lgkmcnt(0)\n"
        "s_nop 15\n"
        "s_branch CNEXTA_%=\n"
        "MTOPB_%=:\n"
        "s_waitcnt lgkmcnt(5)\n"
        "v_mfma_f32_16x16x32_bf16 v[24:27], v[168:171], v[124:127], 0\n"
        "v_cvt_pk_bf16_f32 v112, v152, v56\n"
        "v_cvt_pk_bf16_f32 v113, v72, v20\n"
        "v_cvt_pk_bf16_f32 v114, v84, v60\n"
        "v_cvt_pk_bf16_f32 v116, v153, v57\n"
        "v_cvt_pk_bf16_f32 v117, v73, v21\n"
        "v_cvt_pk_bf16_f32 v118, v85, v61\n"
        "v_cvt_pk_bf16_f32 v120, v154, v58\n"
        "v_cvt_pk_bf16_f32 v121, v74, v22\n"
        "s_waitcnt lgkmcnt(3)\n"
        "v_mfma_f32_16x16x32_bf16 v[24:27], v[172:175], v[128:131], v[24:27]\n"
        "v_cvt_pk_bf16_f32 v122, v86, v62\n"
        "v_cvt_pk_bf16_f32 v140, v155, v59\n"
        "v_cvt_pk_bf16_f32 v141, v75, v23\n"
        "v_cvt_pk_bf16_f32 v142, v87, v63\n"
        "v_cvt_pk_bf16_f32 v144, v68, v36\n"
        "v_cvt_pk_bf16_f32 v145, v40, v64\n"
        "v_cvt_pk_bf16_f32 v146, v104, v88\n"
        "v_cvt_pk_bf16_f32 v148, v69, v37\n"
        "s_waitcnt lgkmcnt(1)\n"
        "v_mfma_f32_16x16x32_bf16 v[24:27], v[176:179], v[160:163], v[24:27]\n"
        "v_cvt_pk_bf16_f32 v149, v41, v65\n"
        "v_cvt_pk_bf16_f32 v150, v105, v89\n"
        "v_cvt_pk_bf16_f32 v152, v70, v38\n"
        "v_cvt_pk_bf16_f32 v153, v42, v66\n"
        "v_cvt_pk_bf16_f32 v154, v106, v90\n"
        "v_cvt_pk_bf16_f32 v156, v71, v39\n"
        "v_cvt_pk_bf16_f32 v157, v43, v67\n"
        "v_cvt_pk_bf16_f32 v158, v107, v91\n"
        "v_mfma_f32_16x16x32_bf16 v[32:35], v[188:191], v[160:163], 0\n"
        "v_cvt_pk_bf16_f32 v40, v24, v25\n"
        "v_cvt_pk_bf16_f32 v41, v26, v27\n"
        "v_mfma_f32_16x16x32_bf16 v[32:35], v[184:187], v[128:131], v[32:35]\n"
        "v_mfma_f32_16x16x32_bf16 v[32:35], v[180:183], v[124:127], v[32:35]\n"
        "s_add_u32 s37, s36, 196224\n"
        "ds_read_b128 v[28:31], v16 offset:128\n"
        "ds_read_b32 v159, v10\n"
        "v_mfma_f32_16x16x32_bf16 v[24:27], v[192:195], v[124:127], 0\n"
        "ds_read_b128 v[20:23], v5\n"
        "v_mfma_f32_16x16x32_bf16 v[24:27], v[196:199], v[128:131], v[24:27]\n"
        "ds_read_b64 v[164:165], v11 offset:192\n"
        "v_mfma_f32_16x16x32_bf16 v[24:27], v[200:203], v[160:163], v[24:27]\n"
        "buffer_store_dwordx3 v[112:114], v12, s[8:11], s37 offen offset:384 sc0 sc1 nt\n"
        "v_cvt_pk_bf16_f32 v42, v32, v33\n"
        "v_cvt_pk_bf16_f32 v43, v34, v35\n"
        "s_waitcnt lgkmcnt(3)\n"
        "v_mfma_f32_16x16x32_bf16 v[48:51], v[28:31], v[160:163], 0\n"
        "ds_read_b128 v[60:63], v7 offset:46848\n"
        "ds_read_i16 v167, v0 offset:20584\n"
        "ds_read_i16 v155, v0 offset:20586\n"
        "v_cvt_pk_bf16_f32 v44, v24, v25\n"
        "v_cvt_pk_bf16_f32 v45, v26, v27\n"
        "s_waitcnt lgkmcnt(3)\n"
        "v_smfmac_f32_16x16x64_bf16 v[48:51], v[20:23], v[124:131], v4\n"
        "s_add_u32 s37, s36, 196224\n"
        "ds_read_b128 v[32:35], v7 offset:46720\n"
        "ds_read_b128 v[36:39], v7 offset:46784\n"
        "v_mfma_f32_16x16x32_bf16 v[56:59], v[28:31], v[52:55], 0\n"
        "v_smfmac_f32_16x16x64_bf16 v[56:59], v[20:23], v[132:139], v4\n"
        "s_setprio 2\n"
        "s_waitcnt lgkmcnt(4)\n"
        "v_mfma_f32_16x16x32_bf16 v[24:27], v[28:31], v[60:63], 0\n"
        "buffer_store_dwordx3 v[116:118], v13, s[8:11], s37 offen offset:1152 sc0 sc1 nt\n"
        "v_add_f32_dpp v165, v50, v51 quad_perm:[0,1,2,3] row_mask:0x1 bank_mask:0xf\n"
        "s_waitcnt lgkmcnt(2)\n"
        "v_add_u32 v167, v9, v167\n"
        "v_add_f32_dpp v164, v48, v49 quad_perm:[0,1,2,3] row_mask:0x1 bank_mask:0xf\n"
        "v_add_f32_dpp v165, v58, v59 quad_perm:[0,1,2,3] row_mask:0x2 bank_mask:0xf\n"
        "v_add_u32 v155, v9, v155\n"
        "v_add_f32_dpp v164, v56, v57 quad_perm:[0,1,2,3] row_mask:0x2 bank_mask:0xf\n"
        "s_waitcnt lgkmcnt(0)\n"
        "v_smfmac_f32_16x16x64_bf16 v[24:27], v[20:23], v[32:39], v4\n"
        "v_mfma_f32_16x16x32_bf16 v[28:31], v[212:215], v[160:163], 0\n"
        "s_add_u32 s37, s36, 294336\n"
        "v_mfma_f32_16x16x32_bf16 v[28:31], v[208:211], v[128:131], v[28:31]\n"
        "v_mfma_f32_16x16x32_bf16 v[28:31], v[204:207], v[124:127], v[28:31]\n"
        "v_mfma_f32_16x16x32_bf16 v[20:23], v[216:219], v[124:127], 0\n"
        "buffer_store_dwordx3 v[120:122], v12, s[8:11], s37 offen offset:576 sc0 sc1 nt\n"
        "v_mfma_f32_16x16x32_bf16 v[20:23], v[220:223], v[128:131], v[20:23]\n"
        "v_mfma_f32_16x16x32_bf16 v[20:23], v[224:227], v[160:163], v[20:23]\n"
        "s_add_u32 s37, s36, 392448\n"
        "v_add_f32_dpp v165, v26, v27 quad_perm:[0,1,2,3] row_mask:0x4 bank_mask:0xf\n"
        "v_mul_f32 v166, s52, v165\n"
        "v_fma_f32 v166, -v166, v165, v159\n"
        "v_cvt_pk_bf16_f32 v46, v28, v29\n"
        "v_cvt_pk_bf16_f32 v47, v30, v31\n"
        "v_add_f32_e64 v166, s53, |v166|\n"
        "v_rsq_f32 v159, v166\n"
        "v_mfma_f32_16x16x32_bf16 v[56:59], v[236:239], v[160:163], 0\n"
        "s_mov_b32 exec_hi, 0xffff\n"
        "ds_write_b32 v10, v159\n"
        "s_mov_b32 exec_hi, -1\n"
        "v_cvt_pk_bf16_f32 v48, v20, v21\n"
        "v_cvt_pk_bf16_f32 v49, v22, v23\n"
        "ds_read_b32 v20, v167 offset:40064\n"
        "v_add_f32_dpp v164, v24, v25 quad_perm:[0,1,2,3] row_mask:0x4 bank_mask:0xf\n"
        "v_mfma_f32_16x16x32_bf16 v[56:59], v[232:235], v[128:131], v[56:59]\n"
        "s_mov_b32 exec_hi, 0xffff\n"
        "v_add_u32 v10, -40064, v10\n"
        "s_mov_b32 exec_hi, -1\n"
        "ds_read_b32 v21, v167 offset:40896\n"
        "ds_read_b32 v22, v167 offset:41728\n"
        "ds_read_b32 v23, v167 offset:42560\n"
        "ds_read_b32 v24, v167 offset:43392\n"
        "v_mul_f32 v164, v164, v159\n"
        "v_mfma_f32_16x16x32_bf16 v[56:59], v[228:231], v[124:127], v[56:59]\n"
        "buffer_store_dwordx3 v[140:142], v12, s[8:11], s37 offen sc0 sc1 nt\n"
        "ds_read_b32 v25, v167 offset:44224\n"
        "ds_read_b32 v26, v167 offset:45056\n"
        "ds_read_b32 v27, v167 offset:45888\n"
        "ds_read_b32 v28, v167 offset:46720\n"
        "ds_read_b32 v29, v167 offset:47552\n"
        "ds_read_b32 v30, v167 offset:48384\n"
        "ds_read_b32 v31, v167 offset:49216\n"
        "v_cvt_pk_bf16_f32 v50, v56, v57\n"
        "v_cvt_pk_bf16_f32 v51, v58, v59\n"
        "v_readlane_b32 s58, v159, 56\n"
        "v_mfma_f32_16x16x32_bf16 v[56:59], v[124:127], v[40:43], 0\n"
        "s_mov_b64 exec, s[56:57]\n"
        "v_mov_b32 v164, 0xff800000\n"
        "s_mov_b64 exec, -1\n"
        "ds_read_b32 v165, v155 offset:40064\n"
        "ds_read_b32 v166, v155 offset:43392\n"
        "v_max_f32_dpp v151, v164, v164 row_ror:8 row_mask:0xf bank_mask:0xf\n"
        "v_mfma_f32_16x16x32_bf16 v[56:59], v[128:131], v[44:47], v[56:59]\n"
        "s_add_u32 s37, s36, 392448\n"
        "v_max_f32_dpp v151, v151, v151 row_ror:4 row_mask:0xf bank_mask:0xf\n"
        "ds_read_b32 v167, v155 offset:46720\n"
        "v_mfma_f32_16x16x32_bf16 v[56:59], v[160:163], v[48:51], v[56:59]\n"
        "v_max_f32_dpp v151, v151, v151 row_ror:2 row_mask:0xf bank_mask:0xf\n"
        "v_mfma_f32_16x16x32_bf16 v[72:75], v[52:55], v[48:51], 0\n"
        "v_mfma_f32_16x16x32_bf16 v[72:75], v[136:139], v[44:47], v[72:75]\n"
        "buffer_store_dwordx3 v[144:146], v15, s[8:11], s37 offen offset:768 sc0 sc1 nt\n"
        "v_max_f32_dpp v151, v151, v151 row_ror:1 row_mask:0xf bank_mask:0xf\n"
        "v_mfma_f32_16x16x32_bf16 v[72:75], v[132:135], v[40:43], v[72:75]\n"
        "ds_read_b128 v[88:91], v8 offset:128\n"
        "v_mfma_f32_16x16x32_bf16 v[68:71], v[32:35], v[40:43], 0\n"
        "s_add_u32 s37, s36, 490560\n"
        "ds_read2_b32 v[92:93], v6 offset0:0 offset1:2\n"
        "ds_read2_b32 v[94:95], v6 offset0:8 offset1:10\n"
        "s_waitcnt lgkmcnt(3)\n"
        "v_mul_f32 v56, v56, v20\n"
        "v_mul_f32 v57, v57, v21\n"
        "v_mul_f32 v58, v58, v22\n"
        "v_mul_f32 v59, v59, v23\n"
        "v_mul_f32 v72, v72, v24\n"
        "v_mfma_f32_16x16x32_bf16 v[68:71], v[36:39], v[44:47], v[68:71]\n"
        "v_max3_f32 v155, v56, v57, v58\n"
        "v_mul_f32 v73, v73, v25\n"
        "v_mul_f32 v74, v74, v26\n"
        "v_max3_f32 v155, v155, v59, v72\n"
        "v_mul_f32 v75, v75, v27\n"
        "v_max3_f32 v155, v155, v73, v74\n"
        "v_mfma_f32_16x16x32_bf16 v[68:71], v[60:63], v[48:51], v[68:71]\n"
        "s_waitcnt lgkmcnt(2)\n"
        "v_mfma_f32_16x16x32_bf16 v[76:79], v[88:91], v[48:51], 0\n"
        "buffer_store_dwordx3 v[148:150], v12, s[8:11], s37 offen offset:192 sc0 sc1 nt\n"
        "s_waitcnt lgkmcnt(0)\n"
        "v_smfmac_f32_16x16x64_bf16 v[76:79], v[92:95], v[40:47], v4\n"
        "v_mfma_f32_16x16x32_bf16 v[48:51], v[168:171], v[132:135], 0\n"
        "s_add_u32 s37, s36, 490560\n"
        "v_mfma_f32_16x16x32_bf16 v[48:51], v[172:175], v[136:139], v[48:51]\n"
        "v_mul_f32 v68, v68, v28\n"
        "v_mul_f32 v69, v69, v29\n"
        "v_mul_f32 v70, v70, v30\n"
        "v_max3_f32 v155, v155, v75, v68\n"
        "v_mul_f32 v71, v71, v31\n"
        "v_add_f32 v76, v76, v77\n"
        "v_mul_f32 v76, s58, v76\n"
        "v_max3_f32 v155, v155, v69, v70\n"
        "v_mfma_f32_16x16x32_bf16 v[48:51], v[176:179], v[52:55], v[48:51]\n"
        "s_mov_b64 exec, s[48:49]\n"
        "v_mov_b32 v76, 0xff800000\n"
        "s_mov_b64 exec, -1\n"
        "v_max3_f32 v155, v155, v71, v76\n"
        "v_mfma_f32_16x16x32_bf16 v[44:47], v[188:191], v[52:55], 0\n"
        "buffer_store_dwordx3 v[152:154], v14, s[8:11], s37 offen offset:960 sc0 sc1 nt\n"
        "v_permlane32_swap_b32 v155, v151\n"
        "v_max_f32 v155, v155, v151\n"
        "v_cvt_pk_bf16_f32 v40, v48, v49\n"
        "v_cvt_pk_bf16_f32 v41, v50, v51\n"
        "v_mov_b32 v154, v155\n"
        "v_mfma_f32_16x16x32_bf16 v[44:47], v[184:187], v[136:139], v[44:47]\n"
        "v_mfma_f32_16x16x32_bf16 v[44:47], v[180:183], v[132:135], v[44:47]\n"
        "s_add_u32 s37, s36, 588672\n"
        "v_permlane16_swap_b32 v155, v154\n"
        "v_max_f32 v155, v155, v154\n"
        "v_mov_b32 v151, v155\n"
        "v_mfma_f32_16x16x32_bf16 v[64:67], v[192:195], v[132:135], 0\n"
        "v_mfma_f32_16x16x32_bf16 v[64:67], v[196:199], v[136:139], v[64:67]\n"
        "v_permlane32_swap_b32 v155, v151\n"
        "v_mul_f32 v155, v155, v165\n"
        "v_cvt_pk_bf16_f32 v42, v44, v45\n"
        "v_cvt_pk_bf16_f32 v43, v46, v47\n"
        "v_fma_f32 v56, v56, v165, -v155\n"
        "v_fma_f32 v57, v57, v165, -v155\n"
        "v_fma_f32 v58, v58, v165, -v155\n"
        "v_mfma_f32_16x16x32_bf16 v[64:67], v[200:203], v[52:55], v[64:67]\n"
        "buffer_store_dwordx3 v[156:158], v12, s[8:11], s37 offen offset:384 sc0 sc1 nt\n"
        "v_exp_f32 v56, v56\n"
        "v_exp_f32 v57, v57\n"
        "v_exp_f32 v58, v58\n"
        "v_fma_f32 v59, v59, v165, -v155\n"
        "v_add_f32 v158, v56, v57\n"
        "v_exp_f32 v59, v59\n"
        "v_mfma_f32_16x16x32_bf16 v[48:51], v[212:215], v[52:55], 0\n"
        "v_cvt_pk_bf16_f32 v44, v64, v65\n"
        "v_cvt_pk_bf16_f32 v45, v66, v67\n"
        "v_fma_f32 v72, v72, v165, -v155\n"
        "v_add_f32 v158, v158, v58\n"
        "v_exp_f32 v72, v72\n"
        "v_fma_f32 v73, v73, v165, -v155\n"
        "v_add_f32 v158, v158, v59\n"
        "v_mfma_f32_16x16x32_bf16 v[48:51], v[208:211], v[136:139], v[48:51]\n"
        "v_exp_f32 v73, v73\n"
        "v_fma_f32 v74, v74, v165, -v155\n"
        "v_add_f32 v158, v158, v72\n"
        "v_exp_f32 v74, v74\n"
        "v_fma_f32 v75, v75, v165, -v155\n"
        "v_sub_f32 v164, v164, v151\n"
        "v_mfma_f32_16x16x32_bf16 v[48:51], v[204:207], v[132:135], v[48:51]\n"
        "v_add_f32 v158, v158, v73\n"
        "v_exp_f32 v75, v75\n"
        "v_fma_f32 v68, v68, v165, -v155\n"
        "v_mul_f32 v164, s58, v164\n"
        "v_add_f32 v158, v158, v74\n"
        "v_exp_f32 v68, v68\n"
        "v_mfma_f32_16x16x32_bf16 v[64:67], v[216:219], v[132:135], 0\n"
        "v_fma_f32 v69, v69, v165, -v155\n"
        "v_cvt_pk_bf16_f32 v46, v48, v49\n"
        "v_cvt_pk_bf16_f32 v47, v50, v51\n"
        "v_exp_f32 v164, v164\n"
        "v_add_f32 v158, v158, v75\n"
        "v_exp_f32 v69, v69\n"
        "v_mfma_f32_16x16x32_bf16 v[64:67], v[220:223], v[136:139], v[64:67]\n"
        "v_fma_f32 v70, v70, v165, -v155\n"
        "v_add_f32 v158, v158, v68\n"
        "v_exp_f32 v70, v70\n"
        "v_fma_f32 v71, v71, v165, -v155\n"
        "v_add_f32_dpp v157, v164, v164 row_ror:8 row_mask:0xf bank_mask:0xf\n"
        "v_add_f32 v158, v158, v69\n"
        "v_fma_f32 v76, v76, v165, -v155\n"
        "v_mfma_f32_16x16x32_bf16 v[64:67], v[224:227], v[52:55], v[64:67]\n"
        "v_exp_f32 v71, v71\n"
        "v_add_f32_dpp v157, v157, v157 row_ror:4 row_mask:0xf bank_mask:0xf\n"
        "v_add_f32 v158, v158, v70\n"
        "v_exp_f32 v76, v76\n"
        "v_add_f32_dpp v157, v157, v157 row_ror:2 row_mask:0xf bank_mask:0xf\n"
        "v_add_f32 v158, v158, v71\n"
        "v_mfma_f32_16x16x32_bf16 v[48:51], v[236:239], v[52:55], 0\n"
        "v_add_f32_dpp v157, v157, v157 row_ror:1 row_mask:0xf bank_mask:0xf\n"
        "v_cvt_pk_bf16_f32 v80, v64, v65\n"
        "v_cvt_pk_bf16_f32 v81, v66, v67\n"
        "v_add_f32 v158, v158, v76\n"
        "v_mul_f32 v56, v56, v20\n"
        "v_mul_f32 v57, v57, v21\n"
        "v_permlane32_swap_b32 v158, v157\n"
        "v_mfma_f32_16x16x32_bf16 v[48:51], v[232:235], v[136:139], v[48:51]\n"
        "v_add_f32 v158, v158, v157\n"
        "v_mov_b32 v165, v158\n"
        "v_mul_f32 v58, v58, v22\n"
        "v_mul_f32 v59, v59, v23\n"
        "v_permlane16_swap_b32 v158, v165\n"
        "v_add_f32 v158, v158, v165\n"
        "v_mul_f32 v72, v72, v24\n"
        "v_mfma_f32_16x16x32_bf16 v[48:51], v[228:231], v[132:135], v[48:51]\n"
        "v_rcp_f32 v158, v158\n"
        "v_mul_f32 v73, v73, v25\n"
        "v_mov_b32 v157, v158\n"
        "v_mul_f32 v74, v74, v26\n"
        "v_mul_f32 v75, v75, v27\n"
        "v_permlane32_swap_b32 v158, v157\n"
        "v_mfma_f32_16x16x32_bf16 v[64:67], v[124:127], v[40:43], 0\n"
        "v_mul_f32 v56, v56, v158\n"
        "v_cvt_pk_bf16_f32 v82, v48, v49\n"
        "v_cvt_pk_bf16_f32 v83, v50, v51\n"
        "v_mul_f32 v57, v57, v158\n"
        "v_mul_f32 v58, v58, v158\n"
        "v_mul_f32 v59, v59, v158\n"
        "v_mul_f32 v72, v72, v158\n"
        "v_mul_f32 v73, v73, v158\n"
        "v_mfma_f32_16x16x32_bf16 v[64:67], v[128:131], v[44:47], v[64:67]\n"
        "v_mul_f32 v74, v74, v158\n"
        "v_mul_f32 v75, v75, v158\n"
        "v_cvt_pk_bf16_f32 v48, v56, v57\n"
        "v_cvt_pk_bf16_f32 v49, v58, v59\n"
        "v_cvt_pk_bf16_f32 v50, v72, v73\n"
        "v_cvt_pk_bf16_f32 v51, v74, v75\n"
        "v_mul_f32 v68, v68, v28\n"
        "v_mul_f32 v69, v69, v29\n"
        "v_mfma_f32_16x16x32_bf16 v[64:67], v[160:163], v[80:83], v[64:67]\n"
        "v_mul_f32 v70, v70, v30\n"
        "v_mul_f32 v71, v71, v31\n"
        "v_mul_f32 v76, s58, v76\n"
        "v_mul_f32 v68, v68, v158\n"
        "v_mul_f32 v69, v69, v158\n"
        "v_mul_f32 v70, v70, v158\n"
        "v_mul_f32 v71, v71, v158\n"
        "v_fma_f32 v76, v76, v158, v19\n"
        "v_mfma_f32_16x16x32_bf16 v[84:87], v[52:55], v[80:83], 0\n"
        "v_mul_f32 v64, v64, v20\n"
        "v_mul_f32 v65, v65, v21\n"
        "v_mul_f32 v66, v66, v22\n"
        "v_mul_f32 v67, v67, v23\n"
        "v_max3_f32 v165, v64, v65, v66\n"
        "v_cvt_pk_bf16_f32 v56, v68, v69\n"
        "v_cvt_pk_bf16_f32 v58, v70, v71\n"
        "v_cvt_pk_bf16_f32 v57, v76, 0\n"
        "v_mfma_f32_16x16x32_bf16 v[84:87], v[136:139], v[44:47], v[84:87]\n"
        "v_mul_f32 v164, v164, v159\n"
        "v_mul_f32 v164, v164, v157\n"
        "v_cvt_pk_bf16_f32 v164, v164, 0\n"
        "ds_write_b16 v1, v164\n"
        "v_mfma_f32_16x16x32_bf16 v[84:87], v[132:135], v[40:43], v[84:87]\n"
        "v_mfma_f32_16x16x32_bf16 v[68:71], v[32:35], v[40:43], 0\n"
        "v_mfma_f32_16x16x32_bf16 v[68:71], v[36:39], v[44:47], v[68:71]\n"
        "v_mfma_f32_16x16x32_bf16 v[68:71], v[60:63], v[80:83], v[68:71]\n"
        "v_mfma_f32_16x16x32_bf16 v[72:75], v[88:91], v[80:83], 0\n"
        "v_smfmac_f32_16x16x64_bf16 v[72:75], v[92:95], v[40:47], v4\n"
        "v_mfma_f32_16x16x32_bf16 v[80:83], v[168:171], v[32:35], 0\n"
        "v_mfma_f32_16x16x32_bf16 v[80:83], v[172:175], v[36:39], v[80:83]\n"
        "v_mfma_f32_16x16x32_bf16 v[80:83], v[176:179], v[60:63], v[80:83]\n"
        "v_mul_f32 v84, v84, v24\n"
        "v_mul_f32 v85, v85, v25\n"
        "v_mul_f32 v86, v86, v26\n"
        "v_max3_f32 v165, v165, v67, v84\n"
        "v_mul_f32 v87, v87, v27\n"
        "v_add_f32 v72, v72, v73\n"
        "v_mul_f32 v68, v68, v28\n"
        "v_mul_f32 v72, s58, v72\n"
        "v_mfma_f32_16x16x32_bf16 v[40:43], v[188:191], v[60:63], 0\n"
        "s_mov_b64 exec, s[48:49]\n"
        "v_mov_b32 v72, 0xff800000\n"
        "s_mov_b64 exec, -1\n"
        "v_cvt_pk_bf16_f32 v76, v80, v81\n"
        "v_cvt_pk_bf16_f32 v77, v82, v83\n"
        "v_max3_f32 v165, v165, v85, v86\n"
        "v_mul_f32 v69, v69, v29\n"
        "v_mul_f32 v70, v70, v30\n"
        "v_max3_f32 v165, v165, v87, v68\n"
        "v_mfma_f32_16x16x32_bf16 v[40:43], v[184:187], v[36:39], v[40:43]\n"
        "v_mul_f32 v71, v71, v31\n"
        "v_max3_f32 v165, v165, v69, v70\n"
        "v_max3_f32 v165, v165, v71, v72\n"
        "v_mfma_f32_16x16x32_bf16 v[40:43], v[180:183], v[32:35], v[40:43]\n"
        "v_mfma_f32_16x16x32_bf16 v[100:103], v[192:195], v[32:35], 0\n"
        "v_mfma_f32_16x16x32_bf16 v[100:103], v[196:199], v[36:39], v[100:103]\n"
        "v_mfma_f32_16x16x32_bf16 v[100:103], v[200:203], v[60:63], v[100:103]\n"
        "v_mfma_f32_16x16x32_bf16 v[96:99], v[212:215], v[60:63], 0\n"
        "v_mfma_f32_16x16x32_bf16 v[96:99], v[208:211], v[36:39], v[96:99]\n"
        "v_mfma_f32_16x16x32_bf16 v[96:99], v[204:207], v[32:35], v[96:99]\n"
        "v_mfma_f32_16x16x32_bf16 v[44:47], v[216:219], v[32:35], 0\n"
        "v_mfma_f32_16x16x32_bf16 v[44:47], v[220:223], v[36:39], v[44:47]\n"
        "v_cvt_pk_bf16_f32 v78, v40, v41\n"
        "v_cvt_pk_bf16_f32 v79, v42, v43\n"
        "v_mfma_f32_16x16x32_bf16 v[44:47], v[224:227], v[60:63], v[44:47]\n"
        "v_cvt_pk_bf16_f32 v80, v100, v101\n"
        "v_cvt_pk_bf16_f32 v81, v102, v103\n"
        "v_mfma_f32_16x16x32_bf16 v[40:43], v[236:239], v[60:63], 0\n"
        "v_cvt_pk_bf16_f32 v82, v96, v97\n"
        "v_cvt_pk_bf16_f32 v83, v98, v99\n"
        "v_mfma_f32_16x16x32_bf16 v[40:43], v[232:235], v[36:39], v[40:43]\n"
        "v_mfma_f32_16x16x32_bf16 v[40:43], v[228:231], v[32:35], v[40:43]\n"
        "v_mfma_f32_16x16x32_bf16 v[108:111], v[124:127], v[76:79], 0\n"
        "v_cvt_pk_bf16_f32 v96, v44, v45\n"
        "v_cvt_pk_bf16_f32 v97, v46, v47\n"
        "v_mfma_f32_16x16x32_bf16 v[108:111], v[128:131], v[80:83], v[108:111]\n"
        "s_nop 3\n"
        "v_cvt_pk_bf16_f32 v98, v40, v41\n"
        "v_cvt_pk_bf16_f32 v99, v42, v43\n"
        "s_nop 1\n"
        "v_mfma_f32_16x16x32_bf16 v[108:111], v[160:163], v[96:99], v[108:111]\n"
        "v_mfma_f32_16x16x32_bf16 v[140:143], v[52:55], v[96:99], 0\n"
        "v_mfma_f32_16x16x32_bf16 v[140:143], v[136:139], v[80:83], v[140:143]\n"
        "v_mfma_f32_16x16x32_bf16 v[140:143], v[132:135], v[76:79], v[140:143]\n"
        "v_mfma_f32_16x16x32_bf16 v[100:103], v[32:35], v[76:79], 0\n"
        "v_mfma_f32_16x16x32_bf16 v[100:103], v[36:39], v[80:83], v[100:103]\n"
        "v_mfma_f32_16x16x32_bf16 v[100:103], v[60:63], v[96:99], v[100:103]\n"
        "v_mfma_f32_16x16x32_bf16 v[44:47], v[88:91], v[96:99], 0\n"
        "v_smfmac_f32_16x16x64_bf16 v[44:47], v[92:95], v[76:83], v4\n"
        "v_mul_f32 v108, v108, v20\n"
        "v_mul_f32 v109, v109, v21\n"
        "v_mul_f32 v110, v110, v22\n"
        "v_mul_f32 v111, v111, v23\n"
        "v_mul_f32 v140, v140, v24\n"
        "v_max3_f32 v159, v108, v109, v110\n"
        "v_mul_f32 v141, v141, v25\n"
        "v_mfma_f32_16x16x32_bf16 v[60:63], v[124:127], v[240:243], 0\n"
        "v_add_f32 v44, v44, v45\n"
        "v_mul_f32 v142, v142, v26\n"
        "v_max3_f32 v159, v159, v111, v140\n"
        "v_mul_f32 v143, v143, v27\n"
        "v_mul_f32 v100, v100, v28\n"
        "v_mul_f32 v44, s58, v44\n"
        "v_max3_f32 v159, v159, v141, v142\n"
        "v_mul_f32 v101, v101, v29\n"
        "v_mfma_f32_16x16x32_bf16 v[60:63], v[128:131], v[244:247], v[60:63]\n"
        "s_mov_b64 exec, s[48:49]\n"
        "v_mov_b32 v44, 0xff800000\n"
        "s_mov_b64 exec, -1\n"
        "v_mul_f32 v102, v102, v30\n"
        "v_max3_f32 v159, v159, v143, v100\n"
        "v_mul_f32 v103, v103, v31\n"
        "v_max3_f32 v159, v159, v101, v102\n"
        "v_max3_f32 v159, v159, v103, v44\n"
        "v_mfma_f32_16x16x32_bf16 v[60:63], v[160:163], v[248:251], v[60:63]\n"
        "ds_read_b128 v[32:35], v17 offset:40064\n"
        "v_permlane32_swap_b32 v165, v159\n"
        "v_max_f32 v165, v165, v159\n"
        "v_mov_b32 v164, v165\n"
        "v_mfma_f32_16x16x32_bf16 v[76:79], v[52:55], v[248:251], 0\n"
        "ds_read_b128 v[36:39], v17 offset:40128\n"
        "v_permlane16_swap_b32 v165, v164\n"
        "v_max_f32 v165, v165, v164\n"
        "v_cvt_pk_bf16_f32 v40, v60, v61\n"
        "v_cvt_pk_bf16_f32 v41, v62, v63\n"
        "v_mov_b32 v159, v165\n"
        "v_mfma_f32_16x16x32_bf16 v[76:79], v[136:139], v[244:247], v[76:79]\n"
        "ds_read_b128 v[60:63], v17 offset:40192\n"
        "v_permlane32_swap_b32 v165, v159\n"
        "v_mul_f32 v165, v165, v166\n"
        "v_mul_f32 v159, v159, v167\n"
        "v_fma_f32 v64, v64, v166, -v165\n"
        "v_fma_f32 v65, v65, v166, -v165\n"
        "v_fma_f32 v108, v108, v167, -v159\n"
        "v_mfma_f32_16x16x32_bf16 v[76:79], v[132:135], v[240:243], v[76:79]\n"
        "s_waitcnt lgkmcnt(2)\n"
        "ds_read_b128 v[96:99], v0 offset:5136\n"
        "v_fma_f32 v109, v109, v167, -v159\n"
        "v_exp_f32 v64, v64\n"
        "v_exp_f32 v65, v65\n"
        "v_fma_f32 v66, v66, v166, -v165\n"
        "v_fma_f32 v110, v110, v167, -v159\n"
        "v_mfma_f32_16x16x32_bf16 v[80:83], v[32:35], v[240:243], 0\n"
        "s_waitcnt lgkmcnt(2)\n"
        "ds_read_b128 v[104:107], v0 offset:4112\n"
        "v_cvt_pk_bf16_f32 v42, v76, v77\n"
        "v_cvt_pk_bf16_f32 v43, v78, v79\n"
        "v_exp_f32 v108, v108\n"
        "v_exp_f32 v109, v109\n"
        "v_fma_f32 v67, v67, v166, -v165\n"
        "v_mfma_f32_16x16x32_bf16 v[80:83], v[36:39], v[244:247], v[80:83]\n"
        "s_waitcnt lgkmcnt(2)\n"
        "v_exp_f32 v66, v66\n"
        "v_exp_f32 v110, v110\n"
        "v_fma_f32 v111, v111, v167, -v159\n"
        "v_add_f32 v95, v64, v65\n"
        "v_exp_f32 v67, v67\n"
        "v_mfma_f32_16x16x32_bf16 v[80:83], v[60:63], v[248:251], v[80:83]\n"
        "s_waitcnt lgkmcnt(1)\n"
        "v_fma_f32 v84, v84, v166, -v165\n"
        "v_add_f32 v164, v108, v109\n"
        "v_exp_f32 v111, v111\n"
        "v_fma_f32 v140, v140, v167, -v159\n"
        "v_add_f32 v95, v95, v66\n"
        "v_exp_f32 v84, v84\n"
        "v_mfma_f32_16x16x32_bf16 v[112:115], v[60:63], v[96:99], 0\n"
        "s_waitcnt lgkmcnt(0)\n"
        "v_cvt_pk_bf16_f32 v76, v80, v81\n"
        "v_cvt_pk_bf16_f32 v80, v82, v83\n"
        "v_fma_f32 v85, v85, v166, -v165\n"
        "v_add_f32 v164, v164, v110\n"
        "v_exp_f32 v140, v140\n"
        "v_fma_f32 v141, v141, v167, -v159\n"
        "v_add_f32 v95, v95, v67\n"
        "v_mfma_f32_16x16x32_bf16 v[112:115], v[36:39], v[104:107], v[112:115]\n"
        "v_exp_f32 v85, v85\n"
        "v_fma_f32 v86, v86, v166, -v165\n"
        "v_add_f32 v164, v164, v111\n"
        "v_exp_f32 v141, v141\n"
        "v_fma_f32 v142, v142, v167, -v159\n"
        "v_add_f32 v95, v95, v84\n"
        "v_mfma_f32_16x16x32_bf16 v[112:115], v[32:35], v[252:255], v[112:115]\n"
        "v_exp_f32 v86, v86\n"
        "v_fma_f32 v87, v87, v166, -v165\n"
        "v_add_f32 v164, v164, v140\n"
        "v_exp_f32 v142, v142\n"
        "v_fma_f32 v143, v143, v167, -v159\n"
        "v_add_f32 v95, v95, v85\n"
        "v_mfma_f32_16x16x32_bf16 v[120:123], v[132:135], v[252:255], 0\n"
        "v_exp_f32 v87, v87\n"
        "v_cvt_pk_bf16_f32 v88, v112, v113\n"
        "v_cvt_pk_bf16_f32 v92, v114, v115\n"
        "v_fma_f32 v68, v68, v166, -v165\n"
        "v_add_f32 v164, v164, v141\n"
        "v_exp_f32 v143, v143\n"
        "v_mfma_f32_16x16x32_bf16 v[120:123], v[136:139], v[104:107], v[120:123]\n"
        "v_fma_f32 v100, v100, v167, -v159\n"
        "v_add_f32 v95, v95, v86\n"
        "v_exp_f32 v68, v68\n"
        "v_fma_f32 v69, v69, v166, -v165\n"
        "v_add_f32 v164, v164, v142\n"
        "v_exp_f32 v100, v100\n"
        "v_mfma_f32_16x16x32_bf16 v[120:123], v[52:55], v[96:99], v[120:123]\n"
        "ds_read_b128 v[144:147], v0 offset:6176\n"
        "v_fma_f32 v101, v101, v167, -v159\n"
        "v_add_f32 v95, v95, v87\n"
        "v_exp_f32 v69, v69\n"
        "v_fma_f32 v70, v70, v166, -v165\n"
        "v_add_f32 v164, v164, v143\n"
        "v_fma_f32 v102, v102, v167, -v159\n"
        "v_mfma_f32_16x16x32_bf16 v[148:151], v[160:163], v[96:99], 0\n"
        "ds_read_b128 v[116:119], v0 offset:7200\n"
        "v_cvt_pk_bf16_f32 v98, v120, v121\n"
        "v_cvt_pk_bf16_f32 v99, v122, v123\n"
        "v_exp_f32 v101, v101\n"
        "v_add_f32 v95, v95, v68\n"
        "v_exp_f32 v70, v70\n"
        "v_mfma_f32_16x16x32_bf16 v[148:151], v[128:131], v[104:107], v[148:151]\n"
        "ds_read_b128 v[112:115], v0 offset:8224\n"
        "v_fma_f32 v71, v71, v166, -v165\n"
        "v_add_f32 v164, v164, v100\n"
        "v_exp_f32 v102, v102\n"
        "v_fma_f32 v103, v103, v167, -v159\n"
        "v_add_f32 v95, v95, v69\n"
        "v_fma_f32 v72, v72, v166, -v165\n"
        "v_mfma_f32_16x16x32_bf16 v[148:151], v[124:127], v[252:255], v[148:151]\n"
        "s_waitcnt lgkmcnt(2)\n"
        "v_exp_f32 v71, v71\n"
        "v_add_f32 v164, v164, v101\n"
        "v_exp_f32 v103, v103\n"
        "v_fma_f32 v44, v44, v167, -v159\n"
        "v_add_f32 v95, v95, v70\n"
        "v_add_f32 v164, v164, v102\n"
        "v_mfma_f32_16x16x32_bf16 v[120:123], v[124:127], v[144:147], 0\n"
        "s_waitcnt lgkmcnt(1)\n"
        "v_cvt_pk_bf16_f32 v96, v148, v149\n"
        "v_cvt_pk_bf16_f32 v97, v150, v151\n"
        "v_exp_f32 v72, v72\n"
        "v_exp_f32 v44, v44\n"
        "v_add_f32 v95, v95, v71\n"
        "v_add_f32 v164, v164, v103\n"
        "v_mfma_f32_16x16x32_bf16 v[120:123], v[128:131], v[116:119], v[120:123]\n"
        "s_waitcnt lgkmcnt(0)\n"
        "v_add_f32 v95, v95, v72\n"
        "v_add_f32 v164, v164, v44\n"
        "v_mul_f32 v64, v64, v20\n"
        "v_mul_f32 v65, v65, v21\n"
        "v_permlane32_swap_b32 v95, v164\n"
        "v_add_f32 v95, v95, v164\n"
        "v_mov_b32 v167, v95\n"
        "v_mfma_f32_16x16x32_bf16 v[120:123], v[160:163], v[112:115], v[120:123]\n"
        "v_mul_f32 v66, v66, v22\n"
        "v_permlane16_swap_b32 v95, v167\n"
        "v_add_f32 v95, v95, v167\n"
        "v_rcp_f32 v95, v95\n"
        "v_mul_f32 v67, v67, v23\n"
        "v_mov_b32 v164, v95\n"
        "v_mfma_f32_16x16x32_bf16 v[156:159], v[52:55], v[112:115], 0\n"
        "v_mul_f32 v84, v84, v24\n"
        "v_cvt_pk_bf16_f32 v104, v120, v121\n"
        "v_cvt_pk_bf16_f32 v105, v122, v123\n"
        "v_permlane32_swap_b32 v95, v164\n"
        "v_mul_f32 v85, v85, v25\n"
        "v_mul_f32 v86, v86, v26\n"
        "v_mul_f32 v87, v87, v27\n"
        "v_mfma_f32_16x16x32_bf16 v[156:159], v[136:139], v[116:119], v[156:159]\n"
        "v_mul_f32 v64, v64, v95\n"
        "v_mul_f32 v65, v65, v95\n"
        "v_mul_f32 v66, v66, v95\n"
        "v_mul_f32 v67, v67, v95\n"
        "v_mul_f32 v84, v84, v95\n"
        "v_mul_f32 v85, v85, v95\n"
        "v_mul_f32 v86, v86, v95\n"
        "v_mul_f32 v87, v87, v95\n"
        "v_mfma_f32_16x16x32_bf16 v[156:159], v[132:135], v[144:147], v[156:159]\n"
        "ds_read_b128 v[148:151], v0 offset:11312\n"
        "v_cvt_pk_bf16_f32 v120, v64, v65\n"
        "v_cvt_pk_bf16_f32 v121, v66, v67\n"
        "v_cvt_pk_bf16_f32 v122, v84, v85\n"
        "v_cvt_pk_bf16_f32 v123, v86, v87\n"
        "v_mul_f32 v68, v68, v28\n"
        "v_mul_f32 v69, v69, v29\n"
        "v_mul_f32 v70, v70, v30\n"
        "v_mfma_f32_16x16x32_bf16 v[152:155], v[32:35], v[144:147], 0\n"
        "ds_read_b128 v[84:87], v0 offset:10288\n"
        "v_cvt_pk_bf16_f32 v106, v156, v157\n"
        "v_cvt_pk_bf16_f32 v107, v158, v159\n"
        "v_mul_f32 v71, v71, v31\n"
        "v_mul_f32 v72, s58, v72\n"
        "v_mul_f32 v68, v68, v95\n"
        "v_mul_f32 v69, v69, v95\n"
        "v_mul_f32 v70, v70, v95\n"
        "v_mfma_f32_16x16x32_bf16 v[152:155], v[36:39], v[116:119], v[152:155]\n"
        "ds_read_b128 v[144:147], v0 offset:9264\n"
        "v_mul_f32 v71, v71, v95\n"
        "v_fma_f32 v72, v72, v95, v19\n"
        "v_cvt_pk_bf16_f32 v64, v68, v69\n"
        "v_cvt_pk_bf16_f32 v66, v70, v71\n"
        "v_cvt_pk_bf16_f32 v65, v72, 0\n"
        "v_mul_f32 v108, v108, v20\n"
        "v_mul_f32 v109, v109, v21\n"
        "v_mfma_f32_16x16x32_bf16 v[152:155], v[60:63], v[112:115], v[152:155]\n"
        "s_waitcnt lgkmcnt(2)\n"
        "v_mul_f32 v110, v110, v22\n"
        "v_mul_f32 v111, v111, v23\n"
        "v_mul_f32 v140, v140, v24\n"
        "v_mul_f32 v141, v141, v25\n"
        "v_mul_f32 v142, v142, v26\n"
        "v_mul_f32 v143, v143, v27\n"
        "v_mul_f32 v108, v108, v164\n"
        "v_cvt_pk_bf16_f32 v112, v152, v153\n"
        "v_mfma_f32_16x16x32_bf16 v[20:23], v[60:63], v[148:151], 0\n"
        "s_waitcnt lgkmcnt(1)\n"
        "v_cvt_pk_bf16_f32 v116, v154, v155\n"
        "v_mul_f32 v109, v109, v164\n"
        "v_mul_f32 v110, v110, v164\n"
        "v_mul_f32 v111, v111, v164\n"
        "v_mul_f32 v140, v140, v164\n"
        "v_mul_f32 v141, v141, v164\n"
        "v_mul_f32 v142, v142, v164\n"
        "v_mul_f32 v143, v143, v164\n"
        "v_mfma_f32_16x16x32_bf16 v[20:23], v[36:39], v[84:87], v[20:23]\n"
        "s_waitcnt lgkmcnt(0)\n"
        "v_cvt_pk_bf16_f32 v24, v108, v109\n"
        "v_cvt_pk_bf16_f32 v25, v110, v111\n"
        "v_cvt_pk_bf16_f32 v26, v140, v141\n"
        "v_cvt_pk_bf16_f32 v27, v142, v143\n"
        "v_mul_f32 v100, v100, v28\n"
        "v_mul_f32 v101, v101, v29\n"
        "v_mul_f32 v102, v102, v30\n"
        "v_mul_f32 v103, v103, v31\n"
        "v_mfma_f32_16x16x32_bf16 v[20:23], v[32:35], v[144:147], v[20:23]\n"
        "v_mul_f32 v44, s58, v44\n"
        "v_mul_f32 v100, v100, v164\n"
        "v_mul_f32 v101, v101, v164\n"
        "v_mul_f32 v102, v102, v164\n"
        "v_mul_f32 v103, v103, v164\n"
        "v_fma_f32 v44, v44, v164, v19\n"
        "v_cvt_pk_bf16_f32 v28, v100, v101\n"
        "v_cvt_pk_bf16_f32 v30, v102, v103\n"
        "v_mfma_f32_16x16x32_bf16 v[68:71], v[132:135], v[144:147], 0\n"
        "v_cvt_pk_bf16_f32 v77, v20, v21\n"
        "v_cvt_pk_bf16_f32 v81, v22, v23\n"
        "v_cvt_pk_bf16_f32 v29, v44, 0\n"
        "v_mfma_f32_16x16x32_bf16 v[68:71], v[136:139], v[84:87], v[68:71]\n"
        "v_mfma_f32_16x16x32_bf16 v[68:71], v[52:55], v[148:151], v[68:71]\n"
        "ds_read_b128 v[20:23], v0 offset:12352\n"
        "v_mfma_f32_16x16x32_bf16 v[72:75], v[160:163], v[148:151], 0\n"
        "ds_read_b128 v[108:111], v0 offset:13376\n"
        "v_mfma_f32_16x16x32_bf16 v[72:75], v[128:131], v[84:87], v[72:75]\n"
        "ds_read_b128 v[140:143], v0 offset:14400\n"
        "v_mfma_f32_16x16x32_bf16 v[72:75], v[124:127], v[144:147], v[72:75]\n"
        "s_waitcnt lgkmcnt(2)\n"
        "v_mfma_f32_16x16x32_bf16 v[144:147], v[124:127], v[20:23], 0\n"
        "v_cvt_pk_bf16_f32 v46, v68, v69\n"
        "v_cvt_pk_bf16_f32 v47, v70, v71\n"
        "s_waitcnt lgkmcnt(1)\n"
        "v_mfma_f32_16x16x32_bf16 v[144:147], v[128:131], v[108:111], v[144:147]\n"
        "s_waitcnt lgkmcnt(0)\n"
        "v_mfma_f32_16x16x32_bf16 v[144:147], v[160:163], v[140:143], v[144:147]\n"
        "v_cvt_pk_bf16_f32 v44, v72, v73\n"
        "v_cvt_pk_bf16_f32 v45, v74, v75\n"
        "v_mfma_f32_16x16x32_bf16 v[148:151], v[52:55], v[140:143], 0\n"
        "v_mfma_f32_16x16x32_bf16 v[148:151], v[136:139], v[108:111], v[148:151]\n"
        "v_mfma_f32_16x16x32_bf16 v[148:151], v[132:135], v[20:23], v[148:151]\n"
        "ds_read_b128 v[72:75], v0 offset:17488\n"
        "v_mfma_f32_16x16x32_bf16 v[84:87], v[32:35], v[20:23], 0\n"
        "ds_read_b128 v[68:71], v0 offset:16464\n"
        "v_cvt_pk_bf16_f32 v100, v144, v145\n"
        "v_cvt_pk_bf16_f32 v101, v146, v147\n"
        "v_mfma_f32_16x16x32_bf16 v[84:87], v[36:39], v[108:111], v[84:87]\n"
        "ds_read_b128 v[20:23], v0 offset:15440\n"
        "v_mfma_f32_16x16x32_bf16 v[84:87], v[60:63], v[140:143], v[84:87]\n"
        "v_cvt_pk_bf16_f32 v102, v148, v149\n"
        "v_cvt_pk_bf16_f32 v103, v150, v151\n"
        "s_waitcnt lgkmcnt(2)\n"
        "v_mfma_f32_16x16x32_bf16 v[108:111], v[60:63], v[72:75], 0\n"
        "s_waitcnt lgkmcnt(1)\n"
        "v_mfma_f32_16x16x32_bf16 v[108:111], v[36:39], v[68:71], v[108:111]\n"
        "s_waitcnt lgkmcnt(0)\n"
        "v_mfma_f32_16x16x32_bf16 v[108:111], v[32:35], v[20:23], v[108:111]\n"
        "ds_read2_b32 v[82:83], v11 offset0:0 offset1:3\n"
        "v_cvt_pk_bf16_f32 v89, v84, v85\n"
        "v_cvt_pk_bf16_f32 v93, v86, v87\n"
        "v_mov_b32_dpp v59, v19 row_shr:4 row_mask:0xf bank_mask:0xf bound_ctrl:0\n"
        "v_mfma_f32_16x16x32_bf16 v[36:39], v[132:135], v[20:23], 0\n"
        "ds_read_b64_tr_b16 v[78:79], v9 offset:40064\n"
        "v_mfma_f32_16x16x32_bf16 v[36:39], v[136:139], v[68:71], v[36:39]\n"
        "ds_read2_b32 v[94:95], v11 offset0:1 offset1:4\n"
        "v_cvt_pk_bf16_f32 v113, v108, v109\n"
        "v_cvt_pk_bf16_f32 v117, v110, v111\n"
        "v_mfma_f32_16x16x32_bf16 v[36:39], v[52:55], v[72:75], v[36:39]\n"
        "s_add_u32 s31, s31, 1\n"
        "ds_read_b64_tr_b16 v[90:91], v9 offset:40080\n"
        "v_mfma_f32_16x16x32_bf16 v[32:35], v[160:163], v[72:75], 0\n"
        "s_cmp_ge_u32 s31, s71\n"
        "ds_read2_b32 v[118:119], v11 offset0:2 offset1:5\n"
        "v_mfma_f32_16x16x32_bf16 v[32:35], v[128:131], v[68:71], v[32:35]\n"
        "s_cbranch_scc1 L52B_%=\n"
        "L53B_%=:\n"
        "ds_read_b64_tr_b16 v[114:115], v9 offset:40096\n"
        "v_cvt_pk_bf16_f32 v110, v36, v37\n"
        "v_cvt_pk_bf16_f32 v111, v38, v39\n"
        "v_mfma_f32_16x16x32_bf16 v[32:35], v[124:127], v[20:23], v[32:35]\n"
        "s_add_u32 s16, s16, 256\n"
        "v_mfma_f32_16x16x32_bf16 v[20:23], v[48:51], v[40:43], 0\n"
        "s_waitcnt lgkmcnt(4)\n"
        "s_cmp_ge_u32 s31, 20\n"
        "v_smfmac_f32_16x16x64_bf16 v[20:23], v[56:59], v[76:83], v18\n"
        "s_cselect_b32 s39, s22, s16\n"
        "v_mfma_f32_16x16x32_bf16 v[36:39], v[48:51], v[96:99], 0\n"
        "s_waitcnt lgkmcnt(2)\n"
        "v_cvt_pk_bf16_f32 v108, v32, v33\n"
        "v_cvt_pk_bf16_f32 v109, v34, v35\n"
        "s_cmp_ge_u32 s31, 21\n"
        "v_smfmac_f32_16x16x64_bf16 v[36:39], v[56:59], v[88:95], v18\n"
        "s_cselect_b32 s39, s23, s39\n"
        "ds_read_b64_tr_b16 v[78:79], v9 offset:40112\n"
        "v_mfma_f32_16x16x32_bf16 v[32:35], v[48:51], v[104:107], 0\n"
        "s_waitcnt lgkmcnt(1)\n"
        "s_mul_hi_u32 s38, s39, 58835169\n"
        "v_smfmac_f32_16x16x64_bf16 v[32:35], v[56:59], v[112:119], v18\n"
        "s_mul_i32 s39, s39, 1344\n"
        "ds_read_b64_tr_b16 v[90:91], v9 offset:40128\n"
        "v_cvt_pk_bf16_f32 v152, v20, v36\n"
        "v_cvt_pk_bf16_f32 v156, v21, v37\n"
        "v_cvt_pk_bf16_f32 v160, v22, v38\n"
        "v_cvt_pk_bf16_f32 v164, v23, v39\n"
        "v_mfma_f32_16x16x32_bf16 v[68:71], v[48:51], v[44:47], 0\n"
        "s_mul_i32 s38, s38, 588672\n"
        "s_waitcnt lgkmcnt(1)\n"
        "v_smfmac_f32_16x16x64_bf16 v[68:71], v[56:59], v[76:83], v18 abid:1\n"
        "s_add_u32 s39, s39, s38\n"
        "ds_read_b64_tr_b16 v[114:115], v9 offset:40144\n"
        "v_mfma_f32_16x16x32_bf16 v[60:63], v[48:51], v[100:103], 0\n"
        "s_add_u32 s39, s39, s17\n"
        "ds_read_b128 v[36:39], v2\n"
        "v_mov_b64 v[20:21], 0\n"
        "v_mov_b64 v[22:23], 0\n"
        "s_waitcnt lgkmcnt(2)\n"
        "v_smfmac_f32_16x16x64_bf16 v[60:63], v[56:59], v[88:95], v18 abid:1\n"
        "s_mul_i32 s68, s31, 3\n"
        "ds_read_b128 v[52:55], v3\n"
        "v_cvt_pk_bf16_f32 v153, v32, v68\n"
        "v_cvt_pk_bf16_f32 v157, v33, v69\n"
        "v_cvt_pk_bf16_f32 v161, v34, v70\n"
        "v_cvt_pk_bf16_f32 v165, v35, v71\n"
        "v_mfma_f32_16x16x32_bf16 v[68:71], v[48:51], v[108:111], 0\n"
        "s_bitcmp1_b32 s31, 0\n"
        "v_mov_b64 v[32:33], 0\n"
        "v_mov_b64 v[34:35], 0\n"
        "s_waitcnt lgkmcnt(2)\n"
        "v_smfmac_f32_16x16x64_bf16 v[68:71], v[56:59], v[112:119], v18 abid:1\n"
        "s_cselect_b32 s38, 3, 10\n"
        "s_waitcnt lgkmcnt(1)\n"
        "v_mov_b32_dpp v67, v19 row_shr:4 row_mask:0xf bank_mask:0xf bound_ctrl:0\n"
        "v_smfmac_f32_16x16x64_bf16 v[20:23], v[36:39], v[40:47], v4\n"
        "s_add_u32 s68, s68, s38\n"
        "v_mov_b64 v[48:49], 0\n"
        "v_mov_b64 v[50:51], 0\n"
        "s_waitcnt lgkmcnt(0)\n"
        "v_smfmac_f32_16x16x64_bf16 v[20:23], v[52:55], v[76:83], v4 abid:1\n"
        "s_add_u32 s37, s35, 0\n"
        "s_cmp_lt_u32 s31, s14\n"
        "v_cvt_pk_bf16_f32 v154, v60, v68\n"
        "v_cvt_pk_bf16_f32 v158, v61, v69\n"
        "v_cvt_pk_bf16_f32 v162, v62, v70\n"
        "v_cvt_pk_bf16_f32 v166, v63, v71\n"
        "v_smfmac_f32_16x16x64_bf16 v[32:35], v[36:39], v[96:103], v4\n"
        "s_cselect_b32 s68, s68, 0\n"
        "v_add_f32 v20, v20, v21\n"
        "v_add_f32 v22, v22, v23\n"
        "v_smfmac_f32_16x16x64_bf16 v[32:35], v[52:55], v[88:95], v4 abid:1\n"
        "s_and_b32 s38, s31, 7\n"
        "ds_read_b64_tr_b16 v[78:79], v9 offset:43392\n"
        "v_smfmac_f32_16x16x64_bf16 v[48:51], v[36:39], v[104:111], v4\n"
        "buffer_store_dwordx3 v[152:154], v12, s[8:11], s37 offen sc0 sc1 nt\n"
        "s_cselect_b32 s30, 1, -7\n"
        "v_smfmac_f32_16x16x64_bf16 v[48:51], v[52:55], v[112:119], v4 abid:1\n"
        "s_mul_i32 s29, s30, 192\n"
        "ds_read_b64_tr_b16 v[90:91], v9 offset:43408\n"
        "v_add_f32 v32, v32, v33\n"
        "v_add_f32 v34, v34, v35\n"
        "v_cvt_pk_bf16_f32 v148, v20, v32\n"
        "v_mfma_f32_16x16x32_bf16 v[152:155], v[120:123], v[40:43], 0\n"
        "s_mul_i32 s30, s30, 392\n"
        "s_waitcnt lgkmcnt(1)\n"
        "v_smfmac_f32_16x16x64_bf16 v[152:155], v[64:67], v[76:83], v18\n"
        "s_add_u32 s37, s35, 0\n"
        "s_cmp_eq_u32 s69, s18\n"
        "ds_read_b64_tr_b16 v[114:115], v9 offset:43424\n"
        "v_add_f32 v48, v48, v49\n"
        "v_add_f32 v50, v50, v51\n"
        "v_cvt_pk_bf16_f32 v149, v48, v22\n"
        "v_cvt_pk_bf16_f32 v150, v34, v50\n"
        "v_mfma_f32_16x16x32_bf16 v[56:59], v[120:123], v[96:99], 0\n"
        "s_cselect_b32 s34, -7488, s70\n"
        "s_waitcnt lgkmcnt(1)\n"
        "v_smfmac_f32_16x16x64_bf16 v[56:59], v[64:67], v[88:95], v18\n"
        "s_add_u32 s69, s69, s34\n"
        "ds_read_b64_tr_b16 v[78:79], v9 offset:43440\n"
        "v_writelane_b32 v10, s69, 56\n"
        "v_add_u32 v11, s30, v11\n"
        "s_mov_b64 exec, s[62:63]\n"
        "v_add_u32 v16, s29, v16\n"
        "v_add_u32 v8, s34, v8\n"
        "s_mov_b64 exec, -1\n"
        "v_mfma_f32_16x16x32_bf16 v[72:75], v[120:123], v[104:107], 0\n"
        "buffer_store_dwordx3 v[156:158], v15, s[8:11], s37 offen offset:768 sc0 sc1 nt\n"
        "s_mov_b64 exec, s[50:51]\n"
        "v_add_u32 v5, s29, v5\n"
        "v_add_u32 v6, s34, v6\n"
        "s_mov_b64 exec, -1\n"
        "s_waitcnt lgkmcnt(1)\n"
        "v_smfmac_f32_16x16x64_bf16 v[72:75], v[64:67], v[112:119], v18\n"
        "ds_read_b64_tr_b16 v[90:91], v9 offset:43456\n"
        "v_mov_b32_dpp v31, v19 row_shr:4 row_mask:0xf bank_mask:0xf bound_ctrl:0\n"
        "v_mfma_f32_16x16x32_bf16 v[20:23], v[120:123], v[44:47], 0\n"
        "s_waitcnt lgkmcnt(1)\n"
        "v_smfmac_f32_16x16x64_bf16 v[20:23], v[64:67], v[76:83], v18 abid:1\n"
        "s_add_u32 s37, s35, 98112\n"
        "ds_read_b64_tr_b16 v[114:115], v9 offset:43472\n"
        "v_mfma_f32_16x16x32_bf16 v[84:87], v[120:123], v[100:103], 0\n"
        "s_waitcnt lgkmcnt(1)\n"
        "v_smfmac_f32_16x16x64_bf16 v[84:87], v[64:67], v[88:95], v18 abid:1\n"
        "ds_read_b64_tr_b16 v[78:79], v9 offset:46720\n"
        "v_mfma_f32_16x16x32_bf16 v[60:63], v[120:123], v[108:111], 0\n"
        "buffer_store_dwordx3 v[160:162], v12, s[8:11], s37 offen offset:192 sc0 sc1 nt\n"
        "ds_read_b32 v167, v0 offset:21600\n"
        "s_waitcnt lgkmcnt(2)\n"
        "v_smfmac_f32_16x16x64_bf16 v[60:63], v[64:67], v[112:119], v18 abid:1\n"
        "ds_read_b64_tr_b16 v[90:91], v9 offset:46736\n"
        "v_mfma_f32_16x16x32_bf16 v[68:71], v[24:27], v[40:43], 0\n"
        "s_waitcnt lgkmcnt(2)\n"
        "v_smfmac_f32_16x16x64_bf16 v[68:71], v[28:31], v[76:83], v18\n"
        "s_add_u32 s37, s35, 98112\n"
        "ds_read_b64_tr_b16 v[114:115], v9 offset:46752\n"
        "v_mfma_f32_16x16x32_bf16 v[36:39], v[24:27], v[96:99], 0\n"
        "s_waitcnt lgkmcnt(1)\n"
        "v_smfmac_f32_16x16x64_bf16 v[36:39], v[28:31], v[88:95], v18\n"
        "ds_read_b64_tr_b16 v[78:79], v9 offset:46768\n"
        "v_readfirstlane_b32 s40, v167\n"
        "v_mfma_f32_16x16x32_bf16 v[40:43], v[24:27], v[104:107], 0\n"
        "buffer_store_dwordx3 v[164:166], v14, s[8:11], s37 offen offset:960 sc0 sc1 nt\n"
        "s_add_u32 s37, s35, 588672\n"
        "s_add_u32 s46, s37, 1344\n"
        "s_cmp_lt_i32 s40, s68\n"
        "s_waitcnt lgkmcnt(1)\n"
        "v_smfmac_f32_16x16x64_bf16 v[40:43], v[28:31], v[112:119], v18\n"
        "buffer_store_dwordx3 v[148:150], v12, s[44:47], s37 offen offset:1152 sc0 sc1 nt\n"
        "ds_read_b64_tr_b16 v[90:91], v9 offset:46784\n"
        "v_mfma_f32_16x16x32_bf16 v[64:67], v[24:27], v[44:47], 0\n"
        "s_waitcnt lgkmcnt(1)\n"
        "v_smfmac_f32_16x16x64_bf16 v[64:67], v[28:31], v[76:83], v18 abid:1\n"
        "ds_read_b64_tr_b16 v[114:115], v9 offset:46800\n"
        "s_mov_b64 exec, 1\n"
        "ds_inc_u32 v0, v1 offset:21612\n"
        "s_mov_b64 exec, -1\n"
        "s_cbranch_scc1 L56B_%=\n"
        "L57B_%=:\n"
        "ds_read_b128 v[124:127], v7 offset:0\n"
        "ds_read_b128 v[132:135], v7 offset:3328\n"
        "ds_read_b128 v[128:131], v7 offset:64\n"
        "ds_read_b128 v[136:139], v7 offset:3392\n"
        "v_mfma_f32_16x16x32_bf16 v[104:107], v[24:27], v[100:103], 0\n"
        "s_waitcnt lgkmcnt(6)\n"
        "ds_read_b128 v[160:163], v7 offset:128\n"
        "ds_read_b128 v[52:55], v7 offset:3456\n"
        "s_mov_b32 s36, s35\n"
        "v_smfmac_f32_16x16x64_bf16 v[104:107], v[28:31], v[88:95], v18 abid:1\n"
        "s_mov_b32 s35, s39\n"
        "v_mfma_f32_16x16x32_bf16 v[88:91], v[24:27], v[108:111], 0\n"
        "s_waitcnt lgkmcnt(7)\n"
        "v_smfmac_f32_16x16x64_bf16 v[88:91], v[28:31], v[112:119], v18 abid:1\n"
        "s_setprio 0\n"
        "s_waitcnt lgkmcnt(0)\n"
        "s_nop 15\n"
        "s_branch CNEXTB_%=\n"
        :: "{v0}"(tid), "{s[0:1]}"(kap), "{s2}"(bid), "{s[4:5]}"(tab) : CLOBBERS);
}

extern "C" void kernel_launch(void* const* d_in, const int* in_sizes, int n_in, void* d_out, int out_size, void* d_ws, size_t ws_size, hipStream_t stream) {
    (void)in_sizes; (void)n_in; (void)out_size; (void)ws_size;
    (void)hipFuncSetAttribute((const void*)swin_kernel, hipFuncAttributeMaxDynamicSharedMemorySize, LDS_BYTES);
    swin_kernel<<<256, 512, LDS_BYTES, stream>>>((const char*)d_in[0], (const char*)d_in[1], (const char*)d_in[2], (const char*)d_in[3], (const char*)d_in[4], (char*)d_out, (const char*)d_ws);
}
```

```cpp
#define LTOK_ 208
#define FPAD 32
#define NPR 36
#define NWC 22
#define ASP 0
#define LK0 0
#define LK1 0
#define LK2 1
#define LK3 1
#include <hip/hip_runtime.h>
#include <hip/hip_bf16.h>

constexpr int TOKB = 192, ROWB = 511 * TOKB;
constexpr unsigned FRAMEB = 511u * ROWB;
constexpr int LTOK = LTOK_;
constexpr int LFRAME = 48 * LTOK + FPAD;
constexpr int LBUF = 4 * LFRAME;
constexpr int LSTASH = 10 * 4 * LTOK;
constexpr int LSTG = 6208;
constexpr int VST = 392, LEXT = 1536 + 8 * VST;
constexpr int WT = 3088;
constexpr int LDS_AUX = (54 - NPR) / 3 * WT, LDS_TAB = LDS_AUX + 1024, LDS_CNT = LDS_TAB + 2048, LDS_BUF = LDS_CNT + 128, LDS_STASH = LDS_BUF + 2 * LBUF,
              LDS_STG = LDS_STASH + LSTASH, LDS_SCRAP = LDS_STG + 4 * LSTG, LDS_EXT = LDS_SCRAP + 512, LDS_BYTES = 163840;
static_assert(NWC >= 18 && NPR % 3 == 0 && LDS_EXT + 4 * LEXT + 256 <= LDS_BYTES && NPR * 1024 <= LBUF, "LDS");
constexpr unsigned RAWM = 18432, RAW_T = LDS_BUF, RAW_TV = RAW_T + 2 * RAWM + 128, RAW_SV = (36 - NPR) / 3 * WT, RAW_SO = LDS_EXT, RAW_SQ = LDS_BUF + LBUF, RAW_SK = RAW_SQ + RAWM;
static_assert(RAW_SV + RAWM <= LDS_AUX && RAW_SO + RAWM <= LDS_BYTES && RAW_SK + RAWM <= LDS_STASH && RAW_TV + 2 * RAWM <= LDS_STASH && RAWM + 6 * WT <= 2 * RAWM + 128, "fold LDS");

constexpr int phi(int v) { return v ^ (v >> 1); }
struct Tab {
    unsigned img[3][64][4];
    unsigned p[4][7][64][4];
    unsigned c[4][6][64][4];
    unsigned d[4][7][64][4];
    static constexpr void job(unsigned (*q)[64][4], int lane, unsigned L, unsigned R, int mode, int tau, unsigned st0, unsigned st1) {
        const int i = lane & 15, g = lane >> 4;
        for (int a = 0; a < 2; ++a) {
            const unsigned ra = (8 * (i >> 2) + 4 * a + (i & 3)) * 192 + ((g ^ ((2 * (i >> 2) + a) & 3)) * 16);
            q[0][lane][a] = L + ra;
            q[0][lane][2 + a] = mode == 0 ? R + tau * 6144 + ra : mode == 2 ? R + tau * 6144 + (16 * a + i) * 192 + ((g ^ (i >> 2)) * 16)
                                          : R + tau * 64 + (8 * g + 4 * a + (i >> 2)) * 192 + (((i & 3) ^ ((2 * g + a) & 3)) * 16);
        }
        q[1][lane][0] = st0; q[1][lane][1] = st1; q[1][lane][2] = LDS_SCRAP + g * 32;
    }
    static constexpr unsigned scat(bool win, int lane, int tau, int tb) {
        const int i = lane & 15, c = 32 * tau + 8 * (i >> 2) + 4 * tb + (i & 3), r = c / 24, o = c % 24, q = win ? o / 6 : o & 3, t = win ? o % 6 : o >> 2;
        return (win ? RAW_SV : 3 * t < NPR - 18 ? LDS_BUF + 18 * 1024 : -(NPR - 18) / 3 * WT) + t * WT + ((lane & 48) + 4 * q + r) * 16;
    }
    constexpr Tab() : img{}, p{}, c{}, d{} {
        for (int e = 0; e < 48; ++e) img[0][e >> 2][e & 3] = 0x3f803f80u;
        img[0][12][0] = 0x7f800000u;
        for (int e = 0; e < 8; ++e) img[0][36 + 10 + e][e >> 1] = e & 1 ? 0x3f800000u : 0x3f80u;
        for (int k = 0; k < 4; ++k) for (int lane = 0; lane < 64; ++lane) {
            const int i = lane & 15, g = lane >> 4, rowv = 8 * (i >> 2) + (i & 3);
            unsigned vi[20] = {};
            {
                const int pp = i >> 2, t = i & 3;
                const int t1 = (k + 3 - LK1) & 3;
                if (t1 < 3) job(p[k], lane, RAW_TV, RAW_TV + RAWM, 1, t1, scat(false, lane, t1, 0), scat(false, lane, t1, 1));
                const unsigned wl = lane * 16, lm = pp == g ? ~0u : 0u, idp = LDS_AUX + 576 + (t == g ? 10 + pp : 0) * 16, sidx = i & 1 ? 0xeeeeddddu : 0x4444ddddu;
                unsigned vA[3] = {};
                for (int m = 0; m < 3; ++m) {
                    const int n = 64 * m + lane, tf = n / 96, q = n % 96 / 12, s = (n % 12) ^ tf;
                    const unsigned cc = tf * FRAMEB + (3 * (s & 3) + (s >> 2)) * 16 + 2048 - 1024 * m;
                    vA[m] = cc + q * TOKB;
                    img[1][lane][m] = (cc + 6 * ROWB + 6 * TOKB) | ((unsigned)(2 * (q & 3) + (q >> 2)) << 28);
                }
                const unsigned vA0 = vA[0], vA1 = vA[1], vA2 = vA[2], vB0 = vA0 + (ROWB - 7 * TOKB), vB1 = vA1 + (ROWB - 7 * TOKB), vB2 = vA2 + (ROWB - 7 * TOKB);
                const unsigned dA = LDS_BUF + t * LFRAME + pp * LTOK + g * 16, rA = LDS_BUF + t * LFRAME + (pp == g ? pp : 4 + g) * LTOK + 192;
                img[1][lane][3] = (2 * pp * (4 * LTOK) + t * LTOK + g * 16) | (((pp == g ? 2 * pp : 2 * g + 1) * (4 * LTOK) + t * LTOK + 192) << 16);
                const unsigned cd0 = k < 3 ? dA + k * 8 * LTOK : LDS_STASH + (img[1][lane][3] & 0xffffu), cr0 = k < 3 ? rA + k * 8 * LTOK : LDS_STASH + (img[1][lane][3] >> 16);
                const unsigned xr = LDS_STG + k * LSTG + (t >> 1) * 3104 + ((t & 1) * 8 + pp) * TOKB + (g ^ (t & 1)) * 16;
                const unsigned axp = LDS_AUX + (t > ASP ? 384 : 0);
                const unsigned pf = g * FRAMEB + (i >> 1) * TOKB + (i & 1) * 188 + 2048;
                vi[0] = xr; vi[1] = lm; vi[2] = rA; vi[3] = dA; vi[4] = vA0; vi[5] = vA1; vi[6] = vA2; vi[7] = vB0; vi[8] = vB1; vi[9] = vB2; vi[10] = sidx; vi[11] = cd0; vi[12] = cr0; vi[13] = wl; vi[14] = idp; vi[15] = pf; vi[16] = axp;
                for (int e = 0; e < 20; ++e) p[k][2 + (e >> 2)][lane][e & 3] = vi[e];
            }
            {
                const int t0 = (k + 3 - LK0) & 3, t2 = (k + 3 - LK2) & 3, t3 = (k + 3 - LK3) & 3;
                const unsigned s0 = LDS_BUF + t0 * 6144 + lane * 16, s3 = LDS_BUF + LBUF + t3 * 6144 + lane * 16;
                if (t0 < 3) job(c[k], lane, RAW_T, RAW_T + RAWM, 2, t0, s0, s0 + 3072); else job(c[k], lane, RAW_T, RAW_T + RAWM, 1, 0, LDS_SCRAP + rowv * 4, 0);
                if (t2 < 3) job(c[k] + 4, lane, RAW_SV, RAW_SO, 1, t2, scat(true, lane, t2, 0), scat(true, lane, t2, 1));
                if (t3 < 3) job(d[k], lane, RAW_SQ, RAW_SK, 0, t3, s3, s3 + 3072); else job(d[k], lane, RAW_SQ, RAW_SK, 1, 0, LDS_SCRAP + rowv * 4, 0);
                for (int m = 0; m < 3; ++m) {
                    const int n = 64 * m + lane, row = n / 12, pc = ((n % 12) ^ ((row >> 2) & 3)) * 16;
                    c[k][3][lane][m] = (24 * (row & 3) + 12 * ((row >> 2) & 1) + 2 * (row >> 3)) * 576 + pc;
                    c[k][2][lane][m] = row * 192 + pc;
                }
                const int dg = 16 * (2 * (g & 1) + (g >> 1)), pix = 4 * (i & 3) + phi(i >> 2);
                const unsigned wl = lane * 16, fr = LDS_BUF + k * LFRAME, ext = LDS_EXT + k * LEXT;
                const unsigned ta = fr + pix * LTOK + dg, zr = LDS_AUX + 384 + g * 16, t30 = i & 3 ? zr : LDS_STASH + k * LTOK + dg;
                const unsigned xa = fr + (4 * (i >> 2) + phi(g)) * LTOK + 96 * (i & 1) + 8 * ((i >> 1) & 1);
                const int sg = 8 * ((i >> 1) & 1) + 4 * (i >> 3) + 2 * ((i >> 2) & 1) + (i & 1);
                const unsigned tv = fr + (32 + 4 * (sg & 3) + phi(sg >> 2)) * LTOK + dg, c1f = (i >> 2) == 2 * g ? 0x3f800000u : 0u;
                const unsigned sidx = g < 2 ? 0x0e0e0404u | ((i >> 2) == 2 * g ? (i & 3) * 0x00100010u : (i >> 2) == 2 * g + 1 ? (i & 3) * 0x40004000u : 0u) : 0xeeee4444u;
                const unsigned ga0 = phi(g) * TOKB + 48 * (i & 3) + 12 * (i >> 2), wr = ROWB - 7 * TOKB;
                const unsigned ga1 = ga0 + (phi(g) >= 1 ? wr : 0), ga2 = ga0 + (phi(g) >= 2 ? wr : 0), ga3 = ga0 + (phi(g) >= 3 ? wr : 0);
                const unsigned pb = LDS_SCRAP + k * 128, pr0 = i < 4 && (i >> 1) == (g >> 1) ? pb + (i & 1) * 32 + (g & 1) * 16 : zr, pr1 = i == 0 || i == 2 ? pb + 64 + g * 16 : zr;
                const unsigned pw = g < 2 ? pb + ((i >> 1) & 1) * 32 + (i >> 3) * 16 + ((i >> 2) & 1) * 8 + g * 4 + (i & 1) * 2
                                          : pb + 64 + (i >> 2) * 16 + (g & 1) * 4 + ((i >> 1) & 1) * 8 + (i & 1) * 2 + (lane == 57 ? 6 : 0);
                const unsigned six2 = (i & 1 ? 0xeeeeu : 0x4444u) | ((i & 2 ? 0xeeee0000u : 0x44440000u) & (g < 2 ? 0x0f0f0000u : 0xffff0000u));
                const unsigned exs0 = i & 2 ? LDS_AUX + g * 16 : ext + (i & 1) * 64 + g * 16, t3s0 = i & 2 ? zr : LDS_STASH + k * LTOK + 64 * (g >> 1) + 16 * (g & 1) + 4 * (i & 1);
                const unsigned ex0 = i & 1 ? zr : i & 2 ? LDS_AUX + g * 16 : ext + g * 16, exv0 = ext + 1536 + i * 24;
                const unsigned rla0 = lane < 48 ? fr + (16 * g + pix) * LTOK + 192 : lane == 56 ? LDS_STASH + k * LTOK + 192 : LDS_AUX + 192;
                img[2][lane][0] = ((i & 7) * (4 * LTOK) + dg) | ((((4 * g + (i & 3)) & 7) * (4 * LTOK) + 96 * ((i >> 2) & 1) + 8 * (i >> 3)) << 16);
                img[2][lane][1] = ((i & 7) * 192u + g * 16) | ((1536u + i * 24 + (g & 1) * 4 * VST) << 16);
                img[2][lane][2] = ((fr + phi(g) * LTOK + 192 - xa) & 0xffffu) | ((fr + pix * LTOK + 192 - xa) << 16);
                img[2][lane][3] = (LDS_AUX + 576 + (10 + (i & 3)) * 16) | ((1536u + (i & 7) * VST + 384) << 16);
                vi[0] = wl; vi[1] = pw; vi[2] = pr0; vi[3] = pr1; vi[4] = six2; vi[5] = exs0; vi[6] = t3s0; vi[7] = ta; vi[8] = t30; vi[9] = xa; vi[10] = rla0; vi[11] = exv0; vi[12] = ga0; vi[13] = ga1; vi[14] = ga2; vi[15] = ga3; vi[16] = ex0; vi[17] = tv; vi[18] = sidx; vi[19] = c1f;
                for (int e = 0; e < 20; ++e) d[k][2 + (e >> 2)][lane][e & 3] = vi[e];
            }
        }
    }
};
static __device__ const Tab TAB __attribute__((aligned(128))) = Tab();

#define CLOBBERS "v1", "v2", "v3", "v4", "v5", "v6", "v7", "v8", "v9", "v10", "v11", "v12", "v13", "v14", "v15", "v16", "v17", "v18", "v19", "v20", "v21", "v22", "v23", "v24", "v25", "v26", "v27", "v28", "v29", "v30", "v31", "v32", "v33", "v34", "v35", "v36", "v37", "v38", "v39", "v40", "v41", "v42", "v43", "v44", "v45", "v46", "v47", "v48", "v49", "v50", "v51", "v52", "v53", "v54", "v55", "v56", "v57", "v58", "v59", "v60", "v61", "v62", "v63", "v64", "v65", "v66", "v67", "v68", "v69", "v70", "v71", "v72", "v73", "v74", "v75", "v76", "v77", "v78", "v79", "v80", "v81", "v82", "v83", "v84", "v85", "v86", "v87", "v88", "v89", "v90", "v91", "v92", "v93", "v94", "v95", "v96", "v97", "v98", "v99", "v100", "v101", "v102", "v103", "v104", "v105", "v106", "v107", "v108", "v109", "v110", "v111", "v112", "v113", "v114", "v115", "v116", "v117", "v118", "v119", "v120", "v121", "v122", "v123", "v124", "v125", "v126", "v127", "v128", "v129", "v130", "v131", "v132", "v133", "v134", "v135", "v136", "v137", "v138", "v139", "v140", "v141", "v142", "v143", "v144", "v145", "v146", "v147", "v148", "v149", "v150", "v151", "v152", "v153", "v154", "v155", "v156", "v157", "v158", "v159", "v160", "v161", "v162", "v163", "v164", "v165", "v166", "v167", "v168", "v169", "v170", "v171", "v172", "v173", "v174", "v175", "v176", "v177", "v178", "v179", "v180", "v181", "v182", "v183", "v184", "v185", "v186", "v187", "v188", "v189", "v190", "v191", "v192", "v193", "v194", "v195", "v196", "v197", "v198", "v199", "v200", "v201", "v202", "v203", "v204", "v205", "v206", "v207", "v208", "v209", "v210", "v211", "v212", "v213", "v214", "v215", "v216", "v217", "v218", "v219", "v220", "v221", "v222", "v223", "v224", "v225", "v226", "v227", "v228", "v229", "v230", "v231", "v232", "v233", "v234", "v235", "v236", "v237", "v238", "v239", "v240", "v241", "v242", "v243", "v244", "v245", "v246", "v247", "v248", "v249", "v250", "v251", "v252", "v253", "v254", "v255", "s3", "s6", "s7", "s8", "s9", "s10", "s11", "s12", "s13", "s14", "s15", "s16", "s17", "s18", "s19", "s20", "s21", "s22", "s23", "s24", "s25", "s26", "s27", "s28", "s29", "s30", "s31", "s34", "s35", "s36", "s37", "s38", "s39", "s40", "s41", "s42", "s43", "s44", "s45", "s46", "s47", "s48", "s49", "s50", "s51", "s52", "s53", "s54", "s55", "s56", "s57", "s58", "s59", "s60", "s61", "s62", "s63", "s64", "s65", "s66", "s67", "s68", "s69", "s70", "s71", "s72", "s73", "s74", "s75", "s76", "s77", "vcc", "scc", "memory"
__global__ __launch_bounds__(512, 2) void swin_kernel(const char* x, const char* wqkv_t, const char* wo_t, const char* wqkv_s, const char* wo_s, char* out, const char* ws) {
    const unsigned long long kap = (unsigned long long)__builtin_amdgcn_kernarg_segment_ptr(), tab = (unsigned long long)&TAB;
    const unsigned tid = threadIdx.x, bid = blockIdx.x;
    asm volatile(
        "s_load_dwordx8 s[44:51], s[0:1], 0x0\n"
        "s_load_dwordx4 s[52:55], s[0:1], 0x20\n"
        "s_load_dwordx2 s[56:57], s[0:1], 0x30\n"
        "v_readfirstlane_b32 s3, v0\n"
        "v_and_b32 v56, 63, v0\n"
        "v_lshlrev_b32 v57, 4, v56\n"
        "s_bfe_u32 s3, s3, 0x30006\n"
        "s_and_b32 s6, s3, 3\n"
        "s_mul_i32 s64, s3, 9216\n"
        "s_add_u32 s64, s64, 21728\n"
        "s_mov_b32 s27, 0x20000\n"
        "s_cmp_lt_u32 s3, 4\n"
        "s_cbranch_scc0 PPRO_%=\n"
        "s_mul_i32 s7, s6, 6144\n"
        "s_add_u32 s7, s7, 33792\n"
        "s_add_u32 s22, s4, s7\n"
        "s_addc_u32 s23, s5, 0\n"
        "global_load_dwordx4 v[240:243], v57, s[22:23] offset:-2048\n"
        "global_load_dwordx4 v[244:247], v57, s[22:23] offset:-1024\n"
        "global_load_dwordx4 v[36:39], v57, s[22:23] offset:1024\n"
        "global_load_dwordx4 v[48:51], v57, s[22:23] offset:2048\n"
        "global_load_dwordx4 v[52:55], v57, s[22:23] offset:3072\n"
        "s_mov_b32 s65, 13824\n"
        "v_add_u32 v24, 0, v56\n"
        "v_mul_u32_u24 v21, 171, v24\n"
        "v_lshrrev_b32 v21, 11, v21\n"
        "v_mad_i32_i24 v24, v21, -12, v24\n"
        "v_bfe_u32 v58, v21, 2, 2\n"
        "v_xor_b32 v58, v24, v58\n"
        "v_and_b32 v27, 7, v21\n"
        "v_mul_u32_u24 v27, 576, v27\n"
        "v_lshl_add_u32 v58, v58, 4, v27\n"
        "v_add_u32 v25, 64, v56\n"
        "v_mul_u32_u24 v22, 171, v25\n"
        "v_lshrrev_b32 v22, 11, v22\n"
        "v_mad_i32_i24 v25, v22, -12, v25\n"
        "v_bfe_u32 v59, v22, 2, 2\n"
        "v_xor_b32 v59, v25, v59\n"
        "v_and_b32 v27, 7, v22\n"
        "v_mul_u32_u24 v27, 576, v27\n"
        "v_lshl_add_u32 v59, v59, 4, v27\n"
        "v_lshrrev_b32 v27, 3, v22\n"
        "v_mad_u32_u24 v59, v27, s65, v59\n"
        "v_add_u32 v26, 128, v56\n"
        "v_mul_u32_u24 v23, 171, v26\n"
        "v_lshrrev_b32 v23, 11, v23\n"
        "v_mad_i32_i24 v26, v23, -12, v26\n"
        "v_bfe_u32 v60, v23, 2, 2\n"
        "v_xor_b32 v60, v26, v60\n"
        "v_and_b32 v27, 7, v23\n"
        "v_mul_u32_u24 v27, 576, v27\n"
        "v_lshl_add_u32 v60, v60, 4, v27\n"
        "v_add_u32 v60, s65, v60\n"
        "s_lshr_b32 s61, s6, 1\n"
        "s_mul_i32 s61, s61, 192\n"
        "s_mov_b32 s62, 27648\n"
        "s_mov_b32 s63, 4608\n"
        "s_add_u32 s7, s62, s63\n"
        "s_lshl_b32 s29, s63, 1\n"
        "s_add_u32 s60, s7, s63\n"
        "s_bitcmp1_b32 s6, 0\n"
        "s_cselect_b32 s28, s7, 0\n"
        "s_cselect_b32 s29, s29, s62\n"
        "s_cselect_b32 s60, s60, s63\n"
        "s_add_u32 s28, s28, s61\n"
        "s_add_u32 s29, s29, s61\n"
        "s_add_u32 s60, s60, s61\n"
        "s_mov_b32 s26, 55296\n"
        "s_waitcnt lgkmcnt(0)\n"
        "s_mov_b32 s24, s46\n"
        "s_and_b32 s25, s47, 0xffff\n"
        "s_add_u32 m0, s64, 0\n"
        "s_nop 0\n"
        "buffer_load_dwordx4 v58, s[24:27], s28 offen lds\n"
        "s_add_u32 m0, s64, 1024\n"
        "s_nop 0\n"
        "buffer_load_dwordx4 v59, s[24:27], s28 offen lds\n"
        "s_add_u32 m0, s64, 2048\n"
        "s_nop 0\n"
        "buffer_load_dwordx4 v60, s[24:27], s28 offen lds\n"
        "s_add_u32 m0, s64, 3072\n"
        "s_nop 0\n"
        "buffer_load_dwordx4 v58, s[24:27], s29 offen lds\n"
        "s_add_u32 m0, s64, 4096\n"
        "s_nop 0\n"
        "buffer_load_dwordx4 v59, s[24:27], s29 offen lds\n"
        "s_add_u32 m0, s64, 5120\n"
        "s_nop 0\n"
        "buffer_load_dwordx4 v60, s[24:27], s29 offen lds\n"
        "s_add_u32 m0, s64, 6144\n"
        "s_nop 0\n"
        "buffer_load_dwordx4 v58, s[24:27], s60 offen lds\n"
        "s_add_u32 m0, s64, 7168\n"
        "s_nop 0\n"
        "buffer_load_dwordx4 v59, s[24:27], s60 offen lds\n"
        "s_add_u32 m0, s64, 8192\n"
        "s_nop 0\n"
        "buffer_load_dwordx4 v60, s[24:27], s60 offen lds\n"
        "s_barrier\n"
        "s_setprio 1\n"
        "v_mov_b32 v250, 21600\n"
        "s_mov_b32 s41, 0x400000\n"
        "s_and_b32 s7, s2, 7\n"
        "s_lshl_b32 s7, s7, 5\n"
        "s_lshr_b32 s14, s2, 3\n"
        "s_add_u32 s7, s7, s14\n"
        "s_and_b32 s16, s7, 255\n"
        "s_sub_u32 s7, s7, s16\n"
        "s_mul_i32 s7, s7, 20\n"
        "s_add_u32 s7, s7, s16\n"
        "s_mov_b32 s14, 22\n"
        "s_mul_i32 s15, s7, 57457\n"
        "s_lshr_b32 s15, s15, 22\n"
        "s_mov_b32 s16, s7\n"
        "s_mul_i32 s15, s15, 588672\n"
        "s_mul_i32 s7, s16, 1344\n"
        "s_add_u32 s15, s15, s7\n"
        "s_mov_b32 s8, s54\n"
        "s_and_b32 s9, s55, 0xffff\n"
        "s_mov_b32 s10, 200540928\n"
        "s_mov_b32 s11, 0x20000\n"
        "s_mov_b64 s[12:13], s[54:55]\n"
        "s_mul_i32 s17, s6, 50135232\n"
        "s_mul_i32 s18, s6, 208\n"
        "s_add_u32 s18, s18, 109536\n"
        "s_mul_i32 s19, s6, 4672\n"
        "s_add_u32 s19, s19, 135520\n"
        "s_mul_i32 s7, s6, 7168\n"
        "s_add_u32 s7, s7, 59392\n"
        "s_add_u32 s22, s4, s7\n"
        "s_addc_u32 s23, s5, 0\n"
        "s_waitcnt vmcnt(0)\n"
        "s_mov_b64 exec, 1\n"
        "ds_inc_u32 v250, v250 offset:16\n"
        "s_mov_b64 exec, -1\n"
        "L67_%=:\n"
        "ds_read_b32 v251, v250 offset:16\n"
        "s_waitcnt lgkmcnt(0)\n"
        "v_readfirstlane_b32 s40, v251\n"
        "s_nop 3\n"
        "s_cmp_ge_i32 s40, 4\n"
        "s_cbranch_scc1 L68_%=\n"
        "s_nop 0\n"
        "s_sub_u32 s41, s41, 1\n"
        "s_cbranch_scc0 L67_%=\n"
        "s_endpgm\n"
        "L68_%=:\n"
        "s_cmp_eq_u32 s6, 0\n"
        "s_cbranch_scc1 L69_%=\n"
        "s_mov_b32 s34, 0x4162ccfd\n"
        "s_mov_b32 s35, 21624\n"
        "s_mov_b32 s36, 21624\n"
        "s_mov_b32 s37, 21628\n"
        "s_call_b64 s[30:31], FOLDM_%=\n"
        "s_branch L70_%=\n"
        "L69_%=:\n"
        "s_mov_b32 s34, 0xbe173353\n"
        "s_mov_b32 s35, 21656\n"
        "s_mov_b32 s36, 21656\n"
        "s_mov_b32 s37, 21624\n"
        "s_call_b64 s[30:31], FOLDR_%=\n"
        "s_cmp_lg_u32 s2, 0\n"
        "s_cbranch_scc1 L66_%=\n"
        "s_mov_b64 s[24:25], s[56:57]\n"
        "s_mov_b64 exec, 1\n"
        "v_mov_b32 v27, 0\n"
        "v_mov_b32 v28, 0\n"
        "global_atomic_swap v252, v27, v28, s[24:25] offset:0 sc0\n"
        "s_mov_b64 exec, -1\n"
        "s_waitcnt vmcnt(0)\n"
        "s_mov_b64 exec, 1\n"
        "v_mov_b32 v27, 0\n"
        "v_mov_b32 v28, 1\n"
        "global_atomic_add v252, v27, v28, s[24:25] offset:256 sc0\n"
        "s_mov_b64 exec, -1\n"
        "L66_%=:\n"
        "L70_%=:\n"
        "s_setprio 2\n"
        "s_waitcnt vmcnt(0)\n"
        "v_mov_b64 v[240:241], v[48:49]\n"
        "v_mov_b64 v[242:243], v[50:51]\n"
        "v_mov_b64 v[244:245], v[52:53]\n"
        "v_mov_b64 v[246:247], v[54:55]\n"
        "L71_%=:\n"
        "ds_read_b32 v251, v250 offset:32\n"
        "s_waitcnt lgkmcnt(0)\n"
        "v_readfirstlane_b32 s40, v251\n"
        "s_nop 3\n"
        "s_cmp_ge_i32 s40, 4\n"
        "s_cbranch_scc1 L72_%=\n"
        "s_nop 0\n"
        "s_sub_u32 s41, s41, 1\n"
        "s_cbranch_scc0 L71_%=\n"
        "s_endpgm\n"
        "L72_%=:\n"
        "s_cmp_eq_u32 s6, 1\n"
        "s_cbranch_scc1 L73_%=\n"
        "s_mov_b32 s34, 0x411cc471\n"
        "s_mov_b32 s35, 21640\n"
        "s_mov_b32 s36, 21640\n"
        "s_mov_b32 s37, 21652\n"
        "s_call_b64 s[30:31], FOLDW_%=\n"
        "s_branch L74_%=\n"
        "L73_%=:\n"
        "s_mov_b64 exec, 1\n"
        "ds_inc_u32 v250, v250 offset:40\n"
        "s_mov_b64 exec, -1\n"
        "s_mov_b32 s24, s50\n"
        "s_and_b32 s25, s51, 0xffff\n"
        "s_mov_b32 s26, 55296\n"
        "s_mov_b32 m0, 61792\n"
        "s_mov_b32 s28, 0\n"
        "buffer_load_dwordx4 v36, s[24:27], s28 offen lds\n"
        "s_mov_b32 m0, 62816\n"
        "s_nop 0\n"
        "buffer_load_dwordx4 v37, s[24:27], s28 offen lds\n"
        "s_mov_b32 m0, 63840\n"
        "s_nop 0\n"
        "buffer_load_dwordx4 v38, s[24:27], s28 offen lds\n"
        "s_mov_b32 m0, 64864\n"
        "s_mov_b32 s28, 576\n"
        "buffer_load_dwordx4 v36, s[24:27], s28 offen lds\n"
        "s_mov_b32 m0, 65888\n"
        "s_nop 0\n"
        "buffer_load_dwordx4 v37, s[24:27], s28 offen lds\n"
        "s_mov_b32 m0, 66912\n"
        "s_nop 0\n"
        "buffer_load_dwordx4 v38, s[24:27], s28 offen lds\n"
        "s_mov_b32 m0, 67936\n"
        "s_mov_b32 s28, 2304\n"
        "buffer_load_dwordx4 v36, s[24:27], s28 offen lds\n"
        "s_mov_b32 m0, 68960\n"
        "s_nop 0\n"
        "buffer_load_dwordx4 v37, s[24:27], s28 offen lds\n"
        "s_mov_b32 m0, 69984\n"
        "s_nop 0\n"
        "buffer_load_dwordx4 v38, s[24:27], s28 offen lds\n"
        "s_mov_b32 m0, 71008\n"
        "s_mov_b32 s28, 2880\n"
        "buffer_load_dwordx4 v36, s[24:27], s28 offen lds\n"
        "s_mov_b32 m0, 72032\n"
        "s_nop 0\n"
        "buffer_load_dwordx4 v37, s[24:27], s28 offen lds\n"
        "s_mov_b32 m0, 73056\n"
        "s_nop 0\n"
        "buffer_load_dwordx4 v38, s[24:27], s28 offen lds\n"
        "s_mov_b32 m0, 74080\n"
        "s_mov_b32 s28, 4608\n"
        "buffer_load_dwordx4 v36, s[24:27], s28 offen lds\n"
        "s_mov_b32 m0, 75104\n"
        "s_nop 0\n"
        "buffer_load_dwordx4 v37, s[24:27], s28 offen lds\n"
        "s_mov_b32 m0, 76128\n"
        "s_nop 0\n"
        "buffer_load_dwordx4 v38, s[24:27], s28 offen lds\n"
        "s_mov_b32 m0, 77152\n"
        "s_mov_b32 s28, 5184\n"
        "buffer_load_dwordx4 v36, s[24:27], s28 offen lds\n"
        "s_mov_b32 m0, 78176\n"
        "s_nop 0\n"
        "buffer_load_dwordx4 v37, s[24:27], s28 offen lds\n"
        "s_mov_b32 m0, 79200\n"
        "s_nop 0\n"
        "buffer_load_dwordx4 v38, s[24:27], s28 offen lds\n"
        "s_mov_b32 s24, s50\n"
        "s_and_b32 s25, s51, 0xffff\n"
        "s_mov_b32 s26, 55296\n"
        "s_mov_b32 m0, 80224\n"
        "s_mov_b32 s28, 192\n"
        "buffer_load_dwordx4 v36, s[24:27], s28 offen lds\n"
        "s_mov_b32 m0, 81248\n"
        "s_nop 0\n"
        "buffer_load_dwordx4 v37, s[24:27], s28 offen lds\n"
        "s_mov_b32 m0, 82272\n"
        "s_nop 0\n"
        "buffer_load_dwordx4 v38, s[24:27], s28 offen lds\n"
        "s_mov_b32 m0, 83296\n"
        "s_mov_b32 s28, 768\n"
        "buffer_load_dwordx4 v36, s[24:27], s28 offen lds\n"
        "s_mov_b32 m0, 84320\n"
        "s_nop 0\n"
        "buffer_load_dwordx4 v37, s[24:27], s28 offen lds\n"
        "s_mov_b32 m0, 85344\n"
        "s_nop 0\n"
        "buffer_load_dwordx4 v38, s[24:27], s28 offen lds\n"
        "s_mov_b32 m0, 86368\n"
        "s_mov_b32 s28, 2496\n"
        "buffer_load_dwordx4 v36, s[24:27], s28 offen lds\n"
        "s_mov_b32 m0, 87392\n"
        "s_nop 0\n"
        "buffer_load_dwordx4 v37, s[24:27], s28 offen lds\n"
        "s_mov_b32 m0, 88416\n"
        "s_nop 0\n"
        "buffer_load_dwordx4 v38, s[24:27], s28 offen lds\n"
        "s_mov_b32 m0, 89440\n"
        "s_mov_b32 s28, 3072\n"
        "buffer_load_dwordx4 v36, s[24:27], s28 offen lds\n"
        "s_mov_b32 m0, 90464\n"
        "s_nop 0\n"
        "buffer_load_dwordx4 v37, s[24:27], s28 offen lds\n"
        "s_mov_b32 m0, 91488\n"
        "s_nop 0\n"
        "buffer_load_dwordx4 v38, s[24:27], s28 offen lds\n"
        "s_mov_b32 m0, 92512\n"
        "s_mov_b32 s28, 4800\n"
        "buffer_load_dwordx4 v36, s[24:27], s28 offen lds\n"
        "s_mov_b32 m0, 93536\n"
        "s_nop 0\n"
        "buffer_load_dwordx4 v37, s[24:27], s28 offen lds\n"
        "s_mov_b32 m0, 94560\n"
        "s_nop 0\n"
        "buffer_load_dwordx4 v38, s[24:27], s28 offen lds\n"
        "s_mov_b32 m0, 95584\n"
        "s_mov_b32 s28, 5376\n"
        "buffer_load_dwordx4 v36, s[24:27], s28 offen lds\n"
        "s_mov_b32 m0, 96608\n"
        "s_nop 0\n"
        "buffer_load_dwordx4 v37, s[24:27], s28 offen lds\n"
        "s_mov_b32 m0, 97632\n"
        "s_nop 0\n"
        "buffer_load_dwordx4 v38, s[24:27], s28 offen lds\n"
        "global_load_dwordx4 v[40:43], v57, s[22:23] offset:-3072\n"
        "global_load_dwordx4 v[44:47], v57, s[22:23] offset:-2048\n"
        "global_load_dwordx4 v[0:3], v57, s[22:23] offset:-1024\n"
        "global_load_dwordx4 v[4:7], v57, s[22:23] offset:0\n"
        "global_load_dwordx4 v[8:11], v57, s[22:23] offset:1024\n"
        "global_load_dwordx4 v[12:15], v57, s[22:23] offset:2048\n"
        "global_load_dwordx4 v[16:19], v57, s[22:23] offset:3072\n"
        "L74_%=:\n"
        "s_setprio 2\n"
        "s_waitcnt vmcnt(0)\n"
        "v_mov_b64 v[240:241], v[40:41]\n"
        "v_mov_b64 v[242:243], v[42:43]\n"
        "v_mov_b64 v[244:245], v[44:45]\n"
        "v_mov_b64 v[246:247], v[46:47]\n"
        "s_cmp_eq_u32 s6, 1\n"
        "s_cbranch_scc1 L79_%=\n"
        "s_mov_b32 s34, 0x41622adf\n"
        "s_mov_b32 s35, 21648\n"
        "s_mov_b32 s36, 21648\n"
        "s_mov_b32 s37, 21652\n"
        "L75_%=:\n"
        "ds_read_b32 v251, v250 offset:44\n"
        "s_waitcnt lgkmcnt(0)\n"
        "v_readfirstlane_b32 s40, v251\n"
        "s_nop 3\n"
        "s_cmp_ge_i32 s40, 1\n"
        "s_cbranch_scc1 L76_%=\n"
        "s_nop 0\n"
        "s_sub_u32 s41, s41, 1\n"
        "s_cbranch_scc0 L75_%=\n"
        "s_endpgm\n"
        "L76_%=:\n"
        "s_call_b64 s[30:31], FOLDM_%=\n"
        "s_branch L80_%=\n"
        "L79_%=:\n"
        "s_mov_b64 exec, 1\n"
        "ds_inc_u32 v250, v250 offset:44\n"
        "s_mov_b64 exec, -1\n"
        "L77_%=:\n"
        "ds_read_b32 v251, v250 offset:40\n"
        "s_waitcnt lgkmcnt(0)\n"
        "v_readfirstlane_b32 s40, v251\n"
        "s_nop 3\n"
        "s_cmp_ge_i32 s40, 4\n"
        "s_cbranch_scc1 L78_%=\n"
        "s_nop 0\n"
        "s_sub_u32 s41, s41, 1\n"
        "s_cbranch_scc0 L77_%=\n"
        "s_endpgm\n"
        "L78_%=:\n"
        "s_mov_b32 s34, 0xbe16c740\n"
        "s_mov_b32 s35, 21656\n"
        "s_mov_b32 s36, 21656\n"
        "s_mov_b32 s37, 21648\n"
        "s_call_b64 s[30:31], FOLDR_%=\n"
        "L80_%=:\n"
        "s_branch CONS_%=\n"
        "FOLDM_%=:\n"
        "ds_read_b128 v[88:91], v242\n"
        "ds_read_b128 v[92:95], v240\n"
        "ds_read_b128 v[180:183], v242 offset:64\n"
        "ds_read_b128 v[172:175], v240 offset:64\n"
        "ds_read_b128 v[184:187], v242 offset:128\n"
        "ds_read_b128 v[164:167], v240 offset:128\n"
        "ds_read_b128 v[168:171], v241\n"
        "ds_read_b128 v[100:103], v241 offset:64\n"
        "ds_read_b128 v[104:107], v241 offset:128\n"
        "ds_read_b128 v[140:143], v240 offset:6144\n"
        "ds_read_b128 v[132:135], v240 offset:6208\n"
        "ds_read_b128 v[96:99], v240 offset:6272\n"
        "ds_read_b128 v[136:139], v241 offset:6144\n"
        "ds_read_b128 v[128:131], v241 offset:6208\n"
        "ds_read_b128 v[108:111], v241 offset:6272\n"
        "s_waitcnt lgkmcnt(13)\n"
        "v_mfma_f32_16x16x32_bf16 v[64:67], v[92:95], v[88:91], 0\n"
        "s_waitcnt lgkmcnt(11)\n"
        "ds_read_b128 v[152:155], v240 offset:12288\n"
        "v_mfma_f32_16x16x32_bf16 v[64:67], v[172:175], v[180:183], v[64:67]\n"
        "s_waitcnt lgkmcnt(8)\n"
        "ds_read_b128 v[160:163], v240 offset:12352\n"
        "v_mfma_f32_16x16x32_bf16 v[64:67], v[164:167], v[184:187], v[64:67]\n"
        "ds_read_b128 v[112:115], v240 offset:12416\n"
        "v_mfma_f32_16x16x32_bf16 v[68:71], v[168:171], v[88:91], 0\n"
        "ds_read_b128 v[116:119], v241 offset:12288\n"
        "v_mfma_f32_16x16x32_bf16 v[68:71], v[100:103], v[180:183], v[68:71]\n"
        "s_waitcnt lgkmcnt(4)\n"
        "ds_read_b128 v[120:123], v241 offset:12352\n"
        "v_mfma_f32_16x16x32_bf16 v[68:71], v[104:107], v[184:187], v[68:71]\n"
        "ds_read_b128 v[124:127], v241 offset:12416\n"
        "v_mfma_f32_16x16x32_bf16 v[72:75], v[140:143], v[88:91], 0\n"
        "ds_read_b128 v[144:147], v243\n"
        "v_mfma_f32_16x16x32_bf16 v[72:75], v[132:135], v[180:183], v[72:75]\n"
        "ds_read_b128 v[148:151], v243 offset:64\n"
        "v_mfma_f32_16x16x32_bf16 v[72:75], v[96:99], v[184:187], v[72:75]\n"
        "ds_read_b128 v[156:159], v243 offset:128\n"
        "v_mfma_f32_16x16x32_bf16 v[76:79], v[136:139], v[88:91], 0\n"
        "v_add_f32 v176, v64, v68\n"
        "v_add_f32 v177, v65, v69\n"
        "v_add_f32 v178, v66, v70\n"
        "v_add_f32 v179, v67, v71\n"
        "v_mfma_f32_16x16x32_bf16 v[76:79], v[128:131], v[180:183], v[76:79]\n"
        "v_mfma_f32_16x16x32_bf16 v[76:79], v[108:111], v[184:187], v[76:79]\n"
        "s_waitcnt lgkmcnt(3)\n"
        "v_add_f32 v176, v176, v72\n"
        "v_add_f32 v177, v177, v73\n"
        "v_add_f32 v178, v178, v74\n"
        "v_add_f32 v179, v179, v75\n"
        "v_mfma_f32_16x16x32_bf16 v[80:83], v[152:155], v[88:91], 0\n"
        "v_mfma_f32_16x16x32_bf16 v[80:83], v[160:163], v[180:183], v[80:83]\n"
        "v_mfma_f32_16x16x32_bf16 v[80:83], v[112:115], v[184:187], v[80:83]\n"
        "v_add_f32 v176, v176, v76\n"
        "v_add_f32 v177, v177, v77\n"
        "v_add_f32 v178, v178, v78\n"
        "v_add_f32 v179, v179, v79\n"
        "v_mfma_f32_16x16x32_bf16 v[84:87], v[116:119], v[88:91], 0\n"
        "v_mfma_f32_16x16x32_bf16 v[84:87], v[120:123], v[180:183], v[84:87]\n"
        "v_mfma_f32_16x16x32_bf16 v[84:87], v[124:127], v[184:187], v[84:87]\n"
        "s_waitcnt lgkmcnt(0)\n"
        "v_add_f32 v176, v176, v80\n"
        "v_add_f32 v177, v177, v81\n"
        "v_add_f32 v178, v178, v82\n"
        "v_add_f32 v179, v179, v83\n"
        "v_mov_b32 v239, s35\n"
        "s_waitcnt vmcnt(6)\n"
        "s_mov_b64 exec, 1\n"
        "ds_inc_u32 v239, v239 offset:0\n"
        "s_mov_b64 exec, -1\n"
        "v_add_f32 v176, v176, v84\n"
        "v_add_f32 v177, v177, v85\n"
        "v_add_f32 v178, v178, v86\n"
        "v_add_f32 v179, v179, v87\n"
        "v_add_f32 v176, v176, v177\n"
        "v_add_f32 v178, v178, v179\n"
        "v_add_f32 v176, v176, v178\n"
        "v_mov_b32 v177, v176\n"
        "v_mfma_f32_16x16x32_bf16 v[88:91], v[92:95], v[144:147], 0\n"
        "v_mfma_f32_16x16x32_bf16 v[88:91], v[172:175], v[148:151], v[88:91]\n"
        "v_permlane32_swap_b32 v176, v177\n"
        "v_add_f32 v176, v176, v177\n"
        "v_mov_b32 v177, v176\n"
        "v_mfma_f32_16x16x32_bf16 v[88:91], v[164:167], v[156:159], v[88:91]\n"
        "v_mfma_f32_16x16x32_bf16 v[92:95], v[168:171], v[144:147], 0\n"
        "v_permlane16_swap_b32 v176, v177\n"
        "v_add_f32 v176, v176, v177\n"
        "v_mul_f32 v176, 0xbc2aaaab, v176\n"
        "v_mul_f32 v176, s34, v176\n"
        "v_fma_f32 v64, v64, s34, v176\n"
        "v_fma_f32 v65, v65, s34, v176\n"
        "v_fma_f32 v66, v66, s34, v176\n"
        "v_fma_f32 v67, v67, s34, v176\n"
        "v_fma_f32 v68, v68, s34, v176\n"
        "v_fma_f32 v69, v69, s34, v176\n"
        "v_fma_f32 v70, v70, s34, v176\n"
        "v_fma_f32 v71, v71, s34, v176\n"
        "v_fma_f32 v72, v72, s34, v176\n"
        "v_fma_f32 v73, v73, s34, v176\n"
        "v_fma_f32 v74, v74, s34, v176\n"
        "v_fma_f32 v75, v75, s34, v176\n"
        "v_fma_f32 v76, v76, s34, v176\n"
        "v_fma_f32 v77, v77, s34, v176\n"
        "v_fma_f32 v78, v78, s34, v176\n"
        "v_fma_f32 v79, v79, s34, v176\n"
        "v_fma_f32 v80, v80, s34, v176\n"
        "v_fma_f32 v81, v81, s34, v176\n"
        "v_fma_f32 v82, v82, s34, v176\n"
        "v_fma_f32 v83, v83, s34, v176\n"
        "v_fma_f32 v84, v84, s34, v176\n"
        "v_fma_f32 v85, v85, s34, v176\n"
        "v_fma_f32 v86, v86, s34, v176\n"
        "v_fma_f32 v87, v87, s34, v176\n"
        "v_mfma_f32_16x16x32_bf16 v[92:95], v[100:103], v[148:151], v[92:95]\n"
        "v_mfma_f32_16x16x32_bf16 v[92:95], v[104:107], v[156:159], v[92:95]\n"
        "v_mfma_f32_16x16x32_bf16 v[100:103], v[140:143], v[144:147], 0\n"
        "v_mfma_f32_16x16x32_bf16 v[100:103], v[132:135], v[148:151], v[100:103]\n"
        "v_mfma_f32_16x16x32_bf16 v[100:103], v[96:99], v[156:159], v[100:103]\n"
        "v_mfma_f32_16x16x32_bf16 v[104:107], v[136:139], v[144:147], 0\n"
        "v_mfma_f32_16x16x32_bf16 v[104:107], v[128:131], v[148:151], v[104:107]\n"
        "v_mfma_f32_16x16x32_bf16 v[104:107], v[108:111], v[156:159], v[104:107]\n"
        "v_mfma_f32_16x16x32_bf16 v[96:99], v[152:155], v[144:147], 0\n"
        "v_mfma_f32_16x16x32_bf16 v[96:99], v[160:163], v[148:151], v[96:99]\n"
        "v_add_f32 v128, v88, v92\n"
        "v_add_f32 v129, v89, v93\n"
        "v_add_f32 v130, v90, v94\n"
        "v_add_f32 v131, v91, v95\n"
        "v_add_f32 v128, v128, v100\n"
        "v_add_f32 v129, v129, v101\n"
        "v_add_f32 v130, v130, v102\n"
        "v_add_f32 v131, v131, v103\n"
        "v_add_f32 v128, v128, v104\n"
        "v_add_f32 v129, v129, v105\n"
        "v_add_f32 v130, v130, v106\n"
        "v_add_f32 v131, v131, v107\n"
        "v_mfma_f32_16x16x32_bf16 v[96:99], v[112:115], v[156:159], v[96:99]\n"
        "v_mfma_f32_16x16x32_bf16 v[108:111], v[116:119], v[144:147], 0\n"
        "v_mfma_f32_16x16x32_bf16 v[108:111], v[120:123], v[148:151], v[108:111]\n"
        "v_mfma_f32_16x16x32_bf16 v[108:111], v[124:127], v[156:159], v[108:111]\n"
        "v_mov_b32 v239, s37\n"
        "s_nop 3\n"
        "v_add_f32 v128, v128, v96\n"
        "v_add_f32 v129, v129, v97\n"
        "v_add_f32 v130, v130, v98\n"
        "v_add_f32 v131, v131, v99\n"
        "v_add_f32 v128, v128, v108\n"
        "v_add_f32 v129, v129, v109\n"
        "v_add_f32 v130, v130, v110\n"
        "v_add_f32 v131, v131, v111\n"
        "v_add_f32 v128, v128, v129\n"
        "v_add_f32 v130, v130, v131\n"
        "v_add_f32 v128, v128, v130\n"
        "v_mov_b32 v129, v128\n"
        "s_nop 1\n"
        "v_permlane32_swap_b32 v128, v129\n"
        "v_add_f32 v128, v128, v129\n"
        "v_mov_b32 v129, v128\n"
        "s_nop 1\n"
        "v_permlane16_swap_b32 v128, v129\n"
        "v_add_f32 v128, v128, v129\n"
        "v_mul_f32 v128, 0xbc2aaaab, v128\n"
        "v_mul_f32 v128, s34, v128\n"
        "v_fma_f32 v88, v88, s34, v128\n"
        "v_fma_f32 v89, v89, s34, v128\n"
        "v_fma_f32 v90, v90, s34, v128\n"
        "v_fma_f32 v91, v91, s34, v128\n"
        "v_fma_f32 v92, v92, s34, v128\n"
        "v_fma_f32 v93, v93, s34, v128\n"
        "v_fma_f32 v94, v94, s34, v128\n"
        "v_fma_f32 v95, v95, s34, v128\n"
        "v_fma_f32 v100, v100, s34, v128\n"
        "v_fma_f32 v101, v101, s34, v128\n"
        "v_fma_f32 v102, v102, s34, v128\n"
        "v_fma_f32 v103, v103, s34, v128\n"
        "v_fma_f32 v104, v104, s34, v128\n"
        "v_fma_f32 v105, v105, s34, v128\n"
        "v_fma_f32 v106, v106, s34, v128\n"
        "v_fma_f32 v107, v107, s34, v128\n"
        "v_fma_f32 v96, v96, s34, v128\n"
        "v_fma_f32 v97, v97, s34, v128\n"
        "v_fma_f32 v98, v98, s34, v128\n"
        "v_fma_f32 v99, v99, s34, v128\n"
        "v_fma_f32 v108, v108, s34, v128\n"
        "v_fma_f32 v109, v109, s34, v128\n"
        "v_fma_f32 v110, v110, s34, v128\n"
        "v_fma_f32 v111, v111, s34, v128\n"
        "v_mov_b32 v237, s36\n"
        "L62_%=:\n"
        "ds_read_b32 v238, v237 offset:0\n"
        "s_waitcnt lgkmcnt(0)\n"
        "v_readfirstlane_b32 s40, v238\n"
        "s_nop 3\n"
        "s_cmp_ge_i32 s40, 4\n"
        "s_cbranch_scc1 L63_%=\n"
        "s_nop 0\n"
        "s_sub_u32 s41, s41, 1\n"
        "s_cbranch_scc0 L62_%=\n"
        "s_endpgm\n"
        "L63_%=:\n"
        "ds_read_b128 v[132:135], v246\n"
        "ds_read_b128 v[128:131], v246 offset:16\n"
        "ds_read_b128 v[124:127], v246 offset:128\n"
        "ds_read_b128 v[120:123], v246 offset:144\n"
        "ds_read_b128 v[116:119], v246 offset:256\n"
        "ds_read_b128 v[112:115], v246 offset:272\n"
        "s_waitcnt lgkmcnt(5)\n"
        "v_add_f32 v64, v64, v132\n"
        "v_add_f32 v65, v65, v133\n"
        "v_add_f32 v66, v66, v134\n"
        "v_add_f32 v67, v67, v135\n"
        "s_waitcnt lgkmcnt(0)\n"
        "v_add_f32 v68, v68, v128\n"
        "v_add_f32 v69, v69, v129\n"
        "v_add_f32 v70, v70, v130\n"
        "v_add_f32 v71, v71, v131\n"
        "v_add_f32 v88, v88, v132\n"
        "v_add_f32 v89, v89, v133\n"
        "v_add_f32 v90, v90, v134\n"
        "v_add_f32 v91, v91, v135\n"
        "v_add_f32 v92, v92, v128\n"
        "v_add_f32 v93, v93, v129\n"
        "v_add_f32 v94, v94, v130\n"
        "v_add_f32 v95, v95, v131\n"
        "v_add_f32 v72, v72, v124\n"
        "v_add_f32 v73, v73, v125\n"
        "v_add_f32 v74, v74, v126\n"
        "v_add_f32 v75, v75, v127\n"
        "v_add_f32 v76, v76, v120\n"
        "v_add_f32 v77, v77, v121\n"
        "v_add_f32 v78, v78, v122\n"
        "v_add_f32 v79, v79, v123\n"
        "v_add_f32 v100, v100, v124\n"
        "v_add_f32 v101, v101, v125\n"
        "v_add_f32 v102, v102, v126\n"
        "v_add_f32 v103, v103, v127\n"
        "v_add_f32 v104, v104, v120\n"
        "v_add_f32 v105, v105, v121\n"
        "v_add_f32 v106, v106, v122\n"
        "v_add_f32 v107, v107, v123\n"
        "v_add_f32 v80, v80, v116\n"
        "v_add_f32 v81, v81, v117\n"
        "v_add_f32 v82, v82, v118\n"
        "v_add_f32 v83, v83, v119\n"
        "v_add_f32 v84, v84, v112\n"
        "v_add_f32 v85, v85, v113\n"
        "v_add_f32 v86, v86, v114\n"
        "v_add_f32 v87, v87, v115\n"
        "v_add_f32 v96, v96, v116\n"
        "v_add_f32 v97, v97, v117\n"
        "v_add_f32 v98, v98, v118\n"
        "v_add_f32 v99, v99, v119\n"
        "v_add_f32 v108, v108, v112\n"
        "v_add_f32 v109, v109, v113\n"
        "v_add_f32 v110, v110, v114\n"
        "v_add_f32 v111, v111, v115\n"
        "v_cvt_pk_bf16_f32 v112, v64, v65\n"
        "v_cvt_pk_bf16_f32 v113, v66, v67\n"
        "v_cvt_pk_bf16_f32 v114, v68, v69\n"
        "v_cvt_pk_bf16_f32 v115, v70, v71\n"
        "v_cvt_pk_bf16_f32 v64, v88, v89\n"
        "v_cvt_pk_bf16_f32 v65, v90, v91\n"
        "v_cvt_pk_bf16_f32 v66, v92, v93\n"
        "v_cvt_pk_bf16_f32 v67, v94, v95\n"
        "v_cvt_pk_bf16_f32 v68, v72, v73\n"
        "v_cvt_pk_bf16_f32 v69, v74, v75\n"
        "v_cvt_pk_bf16_f32 v70, v76, v77\n"
        "v_cvt_pk_bf16_f32 v71, v78, v79\n"
        "v_cvt_pk_bf16_f32 v72, v100, v101\n"
        "v_cvt_pk_bf16_f32 v73, v102, v103\n"
        "v_cvt_pk_bf16_f32 v74, v104, v105\n"
        "v_cvt_pk_bf16_f32 v75, v106, v107\n"
        "v_cvt_pk_bf16_f32 v76, v80, v81\n"
        "v_cvt_pk_bf16_f32 v77, v82, v83\n"
        "v_cvt_pk_bf16_f32 v78, v84, v85\n"
        "v_cvt_pk_bf16_f32 v79, v86, v87\n"
        "v_cvt_pk_bf16_f32 v80, v96, v97\n"
        "v_cvt_pk_bf16_f32 v81, v98, v99\n"
        "v_cvt_pk_bf16_f32 v82, v108, v109\n"
        "v_cvt_pk_bf16_f32 v83, v110, v111\n"
        "ds_write_b128 v244, v[112:115]\n"
        "ds_write_b128 v245, v[64:67]\n"
        "ds_write_b128 v244, v[68:71] offset:1024\n"
        "ds_write_b128 v245, v[72:75] offset:1024\n"
        "ds_write_b128 v244, v[76:79] offset:2048\n"
        "ds_write_b128 v245, v[80:83] offset:2048\n"
        "s_mov_b64 exec, 1\n"
        "ds_inc_u32 v239, v239 offset:0\n"
        "s_mov_b64 exec, -1\n"
        "s_setpc_b64 s[30:31]\n"
        "FOLDR_%=:\n"
        "ds_read_b64_tr_b16 v[84:85], v242 offset:0\n"
        "ds_read_b64_tr_b16 v[86:87], v243 offset:0\n"
        "ds_read_b64_tr_b16 v[92:93], v242 offset:6144\n"
        "ds_read_b64_tr_b16 v[94:95], v243 offset:6144\n"
        "ds_read_b64_tr_b16 v[80:81], v242 offset:12288\n"
        "ds_read_b64_tr_b16 v[82:83], v243 offset:12288\n"
        "ds_read_b64_tr_b16 v[76:77], v242 offset:8\n"
        "ds_read_b64_tr_b16 v[78:79], v243 offset:8\n"
        "ds_read_b64_tr_b16 v[68:69], v242 offset:6152\n"
        "ds_read_b64_tr_b16 v[70:71], v243 offset:6152\n"
        "ds_read_b64_tr_b16 v[72:73], v242 offset:12296\n"
        "ds_read_b64_tr_b16 v[74:75], v243 offset:12296\n"
        "v_mov_b32 v64, 0x3f803f80\n"
        "v_mov_b32 v65, 0x3f803f80\n"
        "v_mov_b32 v66, 0x3f803f80\n"
        "v_mov_b32 v67, 0x3f803f80\n"
        "ds_read_b64_tr_b16 v[88:89], v242 offset:64\n"
        "ds_read_b64_tr_b16 v[90:91], v243 offset:64\n"
        "s_waitcnt lgkmcnt(12)\n"
        "v_mfma_f32_16x16x32_bf16 v[104:107], v[84:87], v[64:67], 0\n"
        "s_waitcnt lgkmcnt(10)\n"
        "v_mfma_f32_16x16x32_bf16 v[104:107], v[92:95], v[64:67], v[104:107]\n"
        "s_waitcnt lgkmcnt(6)\n"
        "ds_read_b64_tr_b16 v[96:97], v242 offset:6208\n"
        "ds_read_b64_tr_b16 v[98:99], v243 offset:6208\n"
        "v_mfma_f32_16x16x32_bf16 v[104:107], v[80:83], v[64:67], v[104:107]\n"
        "ds_read_b64_tr_b16 v[92:93], v242 offset:12352\n"
        "ds_read_b64_tr_b16 v[94:95], v243 offset:12352\n"
        "v_mfma_f32_16x16x32_bf16 v[100:103], v[76:79], v[64:67], 0\n"
        "s_waitcnt lgkmcnt(6)\n"
        "ds_read_b64_tr_b16 v[80:81], v242 offset:72\n"
        "ds_read_b64_tr_b16 v[82:83], v243 offset:72\n"
        "v_mfma_f32_16x16x32_bf16 v[100:103], v[68:71], v[64:67], v[100:103]\n"
        "ds_read_b64_tr_b16 v[76:77], v242 offset:6216\n"
        "ds_read_b64_tr_b16 v[78:79], v243 offset:6216\n"
        "v_cvt_pk_bf16_f32 v68, v104, v105\n"
        "v_cvt_pk_bf16_f32 v69, v106, v107\n"
        "v_mfma_f32_16x16x32_bf16 v[100:103], v[72:75], v[64:67], v[100:103]\n"
        "s_waitcnt lgkmcnt(8)\n"
        "ds_read_b64_tr_b16 v[84:85], v242 offset:12360\n"
        "ds_read_b64_tr_b16 v[86:87], v243 offset:12360\n"
        "v_mfma_f32_16x16x32_bf16 v[104:107], v[88:91], v[64:67], 0\n"
        "ds_read_b64_tr_b16 v[72:73], v242 offset:128\n"
        "ds_read_b64_tr_b16 v[74:75], v243 offset:128\n"
        "s_waitcnt lgkmcnt(10)\n"
        "v_mfma_f32_16x16x32_bf16 v[104:107], v[96:99], v[64:67], v[104:107]\n"
        "ds_read_b64_tr_b16 v[88:89], v242 offset:6272\n"
        "ds_read_b64_tr_b16 v[90:91], v243 offset:6272\n"
        "v_cvt_pk_bf16_f32 v70, v100, v101\n"
        "v_cvt_pk_bf16_f32 v71, v102, v103\n"
        "s_waitcnt lgkmcnt(9)\n"
        "v_mfma_f32_16x16x32_bf16 v[104:107], v[92:95], v[64:67], v[104:107]\n"
        "s_waitcnt lgkmcnt(8)\n"
        "ds_read_b64_tr_b16 v[100:101], v242 offset:12416\n"
        "ds_read_b64_tr_b16 v[102:103], v243 offset:12416\n"
        "v_mfma_f32_16x16x32_bf16 v[108:111], v[80:83], v[64:67], 0\n"
        "s_waitcnt lgkmcnt(8)\n"
        "ds_read_b64_tr_b16 v[92:93], v242 offset:136\n"
        "ds_read_b64_tr_b16 v[94:95], v243 offset:136\n"
        "v_mfma_f32_16x16x32_bf16 v[108:111], v[76:79], v[64:67], v[108:111]\n"
        "ds_read_b64_tr_b16 v[96:97], v242 offset:6280\n"
        "ds_read_b64_tr_b16 v[98:99], v243 offset:6280\n"
        "v_cvt_pk_bf16_f32 v80, v104, v105\n"
        "v_cvt_pk_bf16_f32 v81, v106, v107\n"
        "s_waitcnt lgkmcnt(8)\n"
        "v_mfma_f32_16x16x32_bf16 v[108:111], v[84:87], v[64:67], v[108:111]\n"
        "ds_read_b64_tr_b16 v[76:77], v242 offset:12424\n"
        "ds_read_b64_tr_b16 v[78:79], v243 offset:12424\n"
        "v_mfma_f32_16x16x32_bf16 v[116:119], v[72:75], v[64:67], 0\n"
        "s_waitcnt lgkmcnt(8)\n"
        "ds_read_b128 v[72:75], v240\n"
        "v_mfma_f32_16x16x32_bf16 v[116:119], v[88:91], v[64:67], v[116:119]\n"
        "ds_read_b128 v[104:107], v241\n"
        "s_waitcnt lgkmcnt(8)\n"
        "v_mfma_f32_16x16x32_bf16 v[116:119], v[100:103], v[64:67], v[116:119]\n"
        "ds_read_b128 v[84:87], v240 offset:6144\n"
        "v_cvt_pk_bf16_f32 v82, v108, v109\n"
        "v_cvt_pk_bf16_f32 v83, v110, v111\n"
        "s_waitcnt lgkmcnt(7)\n"
        "v_mfma_f32_16x16x32_bf16 v[112:115], v[92:95], v[64:67], 0\n"
        "s_waitcnt lgkmcnt(5)\n"
        "ds_read_b128 v[92:95], v241 offset:6144\n"
        "v_mfma_f32_16x16x32_bf16 v[112:115], v[96:99], v[64:67], v[112:115]\n"
        "ds_read_b128 v[100:103], v240 offset:12288\n"
        "v_cvt_pk_bf16_f32 v88, v116, v117\n"
        "v_cvt_pk_bf16_f32 v89, v118, v119\n"
        "s_waitcnt lgkmcnt(5)\n"
        "v_mfma_f32_16x16x32_bf16 v[112:115], v[76:79], v[64:67], v[112:115]\n"
        "s_waitcnt lgkmcnt(4)\n"
        "ds_read_b128 v[116:119], v241 offset:12288\n"
        "v_mfma_f32_16x16x32_bf16 v[64:67], v[68:71], v[72:75], 0\n"
        "s_waitcnt lgkmcnt(4)\n"
        "ds_read_b128 v[120:123], v240 offset:64\n"
        "v_mfma_f32_16x16x32_bf16 v[72:75], v[68:71], v[104:107], 0\n"
        "ds_read_b128 v[108:111], v241 offset:64\n"
        "s_waitcnt lgkmcnt(5)\n"
        "v_mfma_f32_16x16x32_bf16 v[76:79], v[68:71], v[84:87], 0\n"
        "ds_read_b128 v[104:107], v240 offset:6208\n"
        "v_cvt_pk_bf16_f32 v90, v112, v113\n"
        "v_cvt_pk_bf16_f32 v91, v114, v115\n"
        "s_waitcnt lgkmcnt(5)\n"
        "v_mfma_f32_16x16x32_bf16 v[84:87], v[68:71], v[92:95], 0\n"
        "ds_read_b128 v[124:127], v241 offset:6208\n"
        "s_waitcnt lgkmcnt(5)\n"
        "v_mfma_f32_16x16x32_bf16 v[92:95], v[68:71], v[100:103], 0\n"
        "ds_read_b128 v[112:115], v240 offset:12352\n"
        "s_waitcnt lgkmcnt(5)\n"
        "v_mfma_f32_16x16x32_bf16 v[96:99], v[68:71], v[116:119], 0\n"
        "ds_read_b128 v[100:103], v241 offset:12352\n"
        "s_waitcnt lgkmcnt(5)\n"
        "v_mfma_f32_16x16x32_bf16 v[64:67], v[80:83], v[120:123], v[64:67]\n"
        "ds_read_b128 v[68:71], v240 offset:128\n"
        "s_waitcnt lgkmcnt(5)\n"
        "v_mfma_f32_16x16x32_bf16 v[72:75], v[80:83], v[108:111], v[72:75]\n"
        "ds_read_b128 v[116:119], v241 offset:128\n"
        "s_waitcnt lgkmcnt(5)\n"
        "v_mfma_f32_16x16x32_bf16 v[76:79], v[80:83], v[104:107], v[76:79]\n"
        "ds_read_b128 v[108:111], v240 offset:6272\n"
        "s_waitcnt lgkmcnt(5)\n"
        "v_mfma_f32_16x16x32_bf16 v[84:87], v[80:83], v[124:127], v[84:87]\n"
        "ds_read_b128 v[104:107], v241 offset:6272\n"
        "s_waitcnt lgkmcnt(5)\n"
        "v_mfma_f32_16x16x32_bf16 v[92:95], v[80:83], v[112:115], v[92:95]\n"
        "ds_read_b128 v[120:123], v240 offset:12416\n"
        "s_waitcnt lgkmcnt(5)\n"
        "v_mfma_f32_16x16x32_bf16 v[96:99], v[80:83], v[100:103], v[96:99]\n"
        "ds_read_b128 v[112:115], v241 offset:12416\n"
        "s_waitcnt lgkmcnt(5)\n"
        "v_mfma_f32_16x16x32_bf16 v[64:67], v[88:91], v[68:71], v[64:67]\n"
        "s_waitcnt lgkmcnt(4)\n"
        "v_mfma_f32_16x16x32_bf16 v[72:75], v[88:91], v[116:119], v[72:75]\n"
        "v_mov_b32 v239, s35\n"
        "s_waitcnt lgkmcnt(3)\n"
        "v_mfma_f32_16x16x32_bf16 v[76:79], v[88:91], v[108:111], v[76:79]\n"
        "s_waitcnt vmcnt(6)\n"
        "s_mov_b64 exec, 1\n"
        "ds_inc_u32 v239, v239 offset:0\n"
        "s_mov_b64 exec, -1\n"
        "s_waitcnt lgkmcnt(3)\n"
        "v_mfma_f32_16x16x32_bf16 v[84:87], v[88:91], v[104:107], v[84:87]\n"
        "v_add_f32 v238, v64, v72\n"
        "s_waitcnt lgkmcnt(2)\n"
        "v_mfma_f32_16x16x32_bf16 v[92:95], v[88:91], v[120:123], v[92:95]\n"
        "v_add_f32 v238, v238, v76\n"
        "s_waitcnt lgkmcnt(1)\n"
        "v_mfma_f32_16x16x32_bf16 v[96:99], v[88:91], v[112:115], v[96:99]\n"
        "v_mov_b32 v239, s37\n"
        "s_nop 0\n"
        "v_add_f32 v238, v238, v84\n"
        "s_nop 1\n"
        "v_add_f32 v238, v238, v92\n"
        "s_nop 1\n"
        "v_add_f32 v238, v238, v96\n"
        "s_nop 1\n"
        "v_add_f32_dpp v238, v238, v238 row_ror:8 row_mask:0xf bank_mask:0xf\n"
        "s_nop 1\n"
        "v_add_f32_dpp v238, v238, v238 row_ror:4 row_mask:0xf bank_mask:0xf\n"
        "s_nop 1\n"
        "v_add_f32_dpp v238, v238, v238 row_ror:2 row_mask:0xf bank_mask:0xf\n"
        "s_nop 1\n"
        "v_add_f32_dpp v238, v238, v238 row_ror:1 row_mask:0xf bank_mask:0xf\n"
        "v_mul_f32 v238, 0xbc2aaaab, v238\n"
        "v_mul_f32 v238, s34, v238\n"
        "v_fma_f32 v64, v64, s34, v238\n"
        "v_fma_f32 v72, v72, s34, v238\n"
        "v_fma_f32 v76, v76, s34, v238\n"
        "v_fma_f32 v84, v84, s34, v238\n"
        "v_fma_f32 v92, v92, s34, v238\n"
        "v_fma_f32 v96, v96, s34, v238\n"
        "ds_write_b32 v244, v64\n"
        "ds_write_b32 v244, v72 offset:16\n"
        "ds_write_b32 v244, v76 offset:128\n"
        "ds_write_b32 v244, v84 offset:144\n"
        "ds_write_b32 v244, v92 offset:256\n"
        "ds_write_b32 v244, v96 offset:272\n"
        "s_mov_b64 exec, 1\n"
        "ds_inc_u32 v239, v239 offset:0\n"
        "s_mov_b64 exec, -1\n"
        "s_setpc_b64 s[30:31]\n"
        "FOLDW_%=:\n"
        "ds_read_b64_tr_b16 v[64:65], v242 offset:0\n"
        "ds_read_b64_tr_b16 v[66:67], v243 offset:0\n"
        "ds_read_b128 v[172:175], v240\n"
        "ds_read_b64_tr_b16 v[180:181], v242 offset:6144\n"
        "ds_read_b64_tr_b16 v[182:183], v243 offset:6144\n"
        "ds_read_b128 v[120:123], v240 offset:64\n"
        "ds_read_b64_tr_b16 v[184:185], v242 offset:12288\n"
        "ds_read_b64_tr_b16 v[186:187], v243 offset:12288\n"
        "ds_read_b128 v[168:171], v240 offset:128\n"
        "ds_read_b128 v[160:163], v241\n"
        "ds_read_b128 v[148:151], v241 offset:64\n"
        "ds_read_b128 v[132:135], v241 offset:128\n"
        "ds_read_b128 v[124:127], v240 offset:6144\n"
        "ds_read_b128 v[104:107], v240 offset:6208\n"
        "ds_read_b128 v[92:95], v240 offset:6272\n"
        "ds_read_b128 v[96:99], v241 offset:6144\n"
        "ds_read_b128 v[88:91], v241 offset:6208\n"
        "ds_read_b128 v[84:87], v241 offset:6272\n"
        "s_waitcnt lgkmcnt(15)\n"
        "v_mfma_f32_16x16x32_bf16 v[136:139], v[172:175], v[64:67], 0\n"
        "ds_read_b128 v[100:103], v240 offset:12288\n"
        "s_waitcnt lgkmcnt(12)\n"
        "v_mfma_f32_16x16x32_bf16 v[136:139], v[120:123], v[180:183], v[136:139]\n"
        "s_waitcnt lgkmcnt(10)\n"
        "ds_read_b128 v[108:111], v240 offset:12352\n"
        "v_mfma_f32_16x16x32_bf16 v[136:139], v[168:171], v[184:187], v[136:139]\n"
        "s_waitcnt lgkmcnt(7)\n"
        "ds_read_b128 v[68:71], v240 offset:12416\n"
        "v_mfma_f32_16x16x32_bf16 v[140:143], v[160:163], v[64:67], 0\n"
        "ds_read_b128 v[72:75], v241 offset:12288\n"
        "v_mfma_f32_16x16x32_bf16 v[140:143], v[148:151], v[180:183], v[140:143]\n"
        "ds_read_b128 v[76:79], v241 offset:12352\n"
        "v_mfma_f32_16x16x32_bf16 v[140:143], v[132:135], v[184:187], v[140:143]\n"
        "ds_read_b128 v[80:83], v241 offset:12416\n"
        "v_mfma_f32_16x16x32_bf16 v[144:147], v[124:127], v[64:67], 0\n"
        "s_waitcnt lgkmcnt(6)\n"
        "ds_read_b64_tr_b16 v[112:113], v242 offset:8\n"
        "ds_read_b64_tr_b16 v[114:115], v243 offset:8\n"
        "v_mfma_f32_16x16x32_bf16 v[144:147], v[104:107], v[180:183], v[144:147]\n"
        "ds_read_b64_tr_b16 v[116:117], v242 offset:6152\n"
        "ds_read_b64_tr_b16 v[118:119], v243 offset:6152\n"
        "v_add_f32 v164, v136, v140\n"
        "v_add_f32 v165, v137, v141\n"
        "v_add_f32 v166, v138, v142\n"
        "v_add_f32 v167, v139, v143\n"
        "v_mfma_f32_16x16x32_bf16 v[144:147], v[92:95], v[184:187], v[144:147]\n"
        "ds_read_b64_tr_b16 v[128:129], v242 offset:12296\n"
        "ds_read_b64_tr_b16 v[130:131], v243 offset:12296\n"
        "v_mfma_f32_16x16x32_bf16 v[152:155], v[96:99], v[64:67], 0\n"
        "v_mfma_f32_16x16x32_bf16 v[152:155], v[88:91], v[180:183], v[152:155]\n"
        "v_mfma_f32_16x16x32_bf16 v[152:155], v[84:87], v[184:187], v[152:155]\n"
        "s_waitcnt lgkmcnt(6)\n"
        "v_mfma_f32_16x16x32_bf16 v[156:159], v[100:103], v[64:67], 0\n"
        "v_mfma_f32_16x16x32_bf16 v[156:159], v[108:111], v[180:183], v[156:159]\n"
        "v_add_f32 v164, v164, v144\n"
        "v_add_f32 v165, v165, v145\n"
        "v_add_f32 v166, v166, v146\n"
        "v_add_f32 v167, v167, v147\n"
        "v_mfma_f32_16x16x32_bf16 v[156:159], v[68:71], v[184:187], v[156:159]\n"
        "v_add_f32 v164, v164, v152\n"
        "v_add_f32 v165, v165, v153\n"
        "v_add_f32 v166, v166, v154\n"
        "v_add_f32 v167, v167, v155\n"
        "v_mfma_f32_16x16x32_bf16 v[176:179], v[72:75], v[64:67], 0\n"
        "v_mfma_f32_16x16x32_bf16 v[176:179], v[76:79], v[180:183], v[176:179]\n"
        "v_mfma_f32_16x16x32_bf16 v[176:179], v[80:83], v[184:187], v[176:179]\n"
        "s_waitcnt lgkmcnt(0)\n"
        "v_mov_b32 v239, s35\n"
        "v_add_f32 v164, v164, v156\n"
        "v_add_f32 v165, v165, v157\n"
        "v_add_f32 v166, v166, v158\n"
        "v_add_f32 v167, v167, v159\n"
        "s_waitcnt vmcnt(6)\n"
        "s_mov_b64 exec, 1\n"
        "ds_inc_u32 v239, v239 offset:0\n"
        "s_mov_b64 exec, -1\n"
        "v_add_f32 v164, v164, v176\n"
        "v_add_f32 v165, v165, v177\n"
        "v_mfma_f32_16x16x32_bf16 v[64:67], v[172:175], v[112:115], 0\n"
        "v_add_f32 v166, v166, v178\n"
        "v_add_f32 v167, v167, v179\n"
        "v_add_f32 v164, v164, v165\n"
        "v_add_f32 v166, v166, v167\n"
        "v_add_f32 v164, v164, v166\n"
        "v_mov_b32 v165, v164\n"
        "v_mfma_f32_16x16x32_bf16 v[64:67], v[120:123], v[116:119], v[64:67]\n"
        "v_mfma_f32_16x16x32_bf16 v[64:67], v[168:171], v[128:131], v[64:67]\n"
        "v_permlane32_swap_b32 v164, v165\n"
        "v_add_f32 v164, v164, v165\n"
        "v_mov_b32 v165, v164\n"
        "v_mfma_f32_16x16x32_bf16 v[120:123], v[160:163], v[112:115], 0\n"
        "global_load_dwordx4 v[40:43], v57, s[22:23] offset:-3072\n"
        "v_permlane16_swap_b32 v164, v165\n"
        "v_add_f32 v164, v164, v165\n"
        "v_mul_f32 v164, 0xbc2aaaab, v164\n"
        "v_mul_f32 v164, s34, v164\n"
        "v_fma_f32 v136, v136, s34, v164\n"
        "v_fma_f32 v137, v137, s34, v164\n"
        "v_fma_f32 v138, v138, s34, v164\n"
        "v_fma_f32 v139, v139, s34, v164\n"
        "v_fma_f32 v140, v140, s34, v164\n"
        "v_fma_f32 v141, v141, s34, v164\n"
        "v_mfma_f32_16x16x32_bf16 v[120:123], v[148:151], v[116:119], v[120:123]\n"
        "v_fma_f32 v142, v142, s34, v164\n"
        "v_fma_f32 v143, v143, s34, v164\n"
        "v_fma_f32 v144, v144, s34, v164\n"
        "v_fma_f32 v145, v145, s34, v164\n"
        "v_fma_f32 v146, v146, s34, v164\n"
        "v_fma_f32 v147, v147, s34, v164\n"
        "v_fma_f32 v152, v152, s34, v164\n"
        "v_fma_f32 v153, v153, s34, v164\n"
        "v_fma_f32 v154, v154, s34, v164\n"
        "v_fma_f32 v155, v155, s34, v164\n"
        "v_fma_f32 v156, v156, s34, v164\n"
        "v_fma_f32 v157, v157, s34, v164\n"
        "v_fma_f32 v158, v158, s34, v164\n"
        "v_fma_f32 v159, v159, s34, v164\n"
        "v_mfma_f32_16x16x32_bf16 v[120:123], v[132:135], v[128:131], v[120:123]\n"
        "global_load_dwordx4 v[44:47], v57, s[22:23] offset:-2048\n"
        "v_fma_f32 v176, v176, s34, v164\n"
        "v_fma_f32 v177, v177, s34, v164\n"
        "v_fma_f32 v178, v178, s34, v164\n"
        "v_fma_f32 v179, v179, s34, v164\n"
        "v_cvt_pk_bf16_f32 v132, v136, v137\n"
        "v_cvt_pk_bf16_f32 v133, v138, v139\n"
        "v_cvt_pk_bf16_f32 v134, v140, v141\n"
        "v_add_f32 v148, v64, v120\n"
        "v_add_f32 v149, v65, v121\n"
        "v_add_f32 v150, v66, v122\n"
        "v_add_f32 v151, v67, v123\n"
        "v_cvt_pk_bf16_f32 v135, v142, v143\n"
        "v_mfma_f32_16x16x32_bf16 v[140:143], v[124:127], v[112:115], 0\n"
        "v_cvt_pk_bf16_f32 v136, v144, v145\n"
        "v_cvt_pk_bf16_f32 v137, v146, v147\n"
        "v_cvt_pk_bf16_f32 v138, v152, v153\n"
        "v_cvt_pk_bf16_f32 v139, v154, v155\n"
        "v_cvt_pk_bf16_f32 v124, v156, v157\n"
        "v_cvt_pk_bf16_f32 v125, v158, v159\n"
        "v_cvt_pk_bf16_f32 v126, v176, v177\n"
        "v_cvt_pk_bf16_f32 v127, v178, v179\n"
        "v_mfma_f32_16x16x32_bf16 v[140:143], v[104:107], v[116:119], v[140:143]\n"
        "global_load_dwordx4 v[0:3], v57, s[22:23] offset:-1024\n"
        "v_mfma_f32_16x16x32_bf16 v[140:143], v[92:95], v[128:131], v[140:143]\n"
        "v_mfma_f32_16x16x32_bf16 v[104:107], v[96:99], v[112:115], 0\n"
        "global_load_dwordx4 v[4:7], v57, s[22:23] offset:0\n"
        "v_mfma_f32_16x16x32_bf16 v[104:107], v[88:91], v[116:119], v[104:107]\n"
        "v_mfma_f32_16x16x32_bf16 v[104:107], v[84:87], v[128:131], v[104:107]\n"
        "global_load_dwordx4 v[8:11], v57, s[22:23] offset:1024\n"
        "v_mfma_f32_16x16x32_bf16 v[84:87], v[100:103], v[112:115], 0\n"
        "v_mfma_f32_16x16x32_bf16 v[84:87], v[108:111], v[116:119], v[84:87]\n"
        "global_load_dwordx4 v[12:15], v57, s[22:23] offset:2048\n"
        "v_add_f32 v148, v148, v140\n"
        "v_add_f32 v149, v149, v141\n"
        "v_add_f32 v150, v150, v142\n"
        "v_add_f32 v151, v151, v143\n"
        "v_add_f32 v148, v148, v104\n"
        "v_add_f32 v149, v149, v105\n"
        "v_add_f32 v150, v150, v106\n"
        "v_add_f32 v151, v151, v107\n"
        "v_mfma_f32_16x16x32_bf16 v[84:87], v[68:71], v[128:131], v[84:87]\n"
        "v_mfma_f32_16x16x32_bf16 v[88:91], v[72:75], v[112:115], 0\n"
        "global_load_dwordx4 v[16:19], v57, s[22:23] offset:3072\n"
        "v_mfma_f32_16x16x32_bf16 v[88:91], v[76:79], v[116:119], v[88:91]\n"
        "v_mfma_f32_16x16x32_bf16 v[88:91], v[80:83], v[128:131], v[88:91]\n"
        "v_mov_b32 v239, s37\n"
        "s_nop 2\n"
        "v_add_f32 v148, v148, v84\n"
        "v_add_f32 v149, v149, v85\n"
        "v_add_f32 v150, v150, v86\n"
        "v_add_f32 v151, v151, v87\n"
        "v_add_f32 v148, v148, v88\n"
        "v_add_f32 v149, v149, v89\n"
        "v_add_f32 v150, v150, v90\n"
        "v_add_f32 v151, v151, v91\n"
        "v_add_f32 v148, v148, v149\n"
        "v_add_f32 v150, v150, v151\n"
        "v_add_f32 v148, v148, v150\n"
        "v_mov_b32 v149, v148\n"
        "s_nop 1\n"
        "v_permlane32_swap_b32 v148, v149\n"
        "v_add_f32 v148, v148, v149\n"
        "v_mov_b32 v149, v148\n"
        "s_nop 1\n"
        "v_permlane16_swap_b32 v148, v149\n"
        "v_add_f32 v148, v148, v149\n"
        "v_mul_f32 v148, 0xbc2aaaab, v148\n"
        "v_mul_f32 v148, s34, v148\n"
        "v_fma_f32 v64, v64, s34, v148\n"
        "v_fma_f32 v65, v65, s34, v148\n"
        "v_fma_f32 v66, v66, s34, v148\n"
        "v_fma_f32 v67, v67, s34, v148\n"
        "v_fma_f32 v120, v120, s34, v148\n"
        "v_fma_f32 v121, v121, s34, v148\n"
        "v_fma_f32 v122, v122, s34, v148\n"
        "v_fma_f32 v123, v123, s34, v148\n"
        "v_fma_f32 v140, v140, s34, v148\n"
        "v_fma_f32 v141, v141, s34, v148\n"
        "v_fma_f32 v142, v142, s34, v148\n"
        "v_fma_f32 v143, v143, s34, v148\n"
        "v_fma_f32 v104, v104, s34, v148\n"
        "v_fma_f32 v105, v105, s34, v148\n"
        "v_fma_f32 v106, v106, s34, v148\n"
        "v_fma_f32 v107, v107, s34, v148\n"
        "v_fma_f32 v84, v84, s34, v148\n"
        "v_fma_f32 v85, v85, s34, v148\n"
        "v_fma_f32 v86, v86, s34, v148\n"
        "v_fma_f32 v87, v87, s34, v148\n"
        "v_fma_f32 v88, v88, s34, v148\n"
        "v_fma_f32 v89, v89, s34, v148\n"
        "v_fma_f32 v90, v90, s34, v148\n"
        "v_fma_f32 v91, v91, s34, v148\n"
        "v_cvt_pk_bf16_f32 v68, v64, v65\n"
        "v_cvt_pk_bf16_f32 v69, v66, v67\n"
        "v_cvt_pk_bf16_f32 v70, v120, v121\n"
        "v_cvt_pk_bf16_f32 v71, v122, v123\n"
        "v_cvt_pk_bf16_f32 v64, v140, v141\n"
        "v_cvt_pk_bf16_f32 v65, v142, v143\n"
        "v_cvt_pk_bf16_f32 v66, v104, v105\n"
        "v_cvt_pk_bf16_f32 v67, v106, v107\n"
        "v_cvt_pk_bf16_f32 v72, v84, v85\n"
        "v_cvt_pk_bf16_f32 v73, v86, v87\n"
        "v_cvt_pk_bf16_f32 v74, v88, v89\n"
        "v_cvt_pk_bf16_f32 v75, v90, v91\n"
        "s_nop 1\n"
        "v_mov_b32 v238, s36\n"
        "L64_%=:\n"
        "ds_read_b32 v237, v238 offset:0\n"
        "s_waitcnt lgkmcnt(0)\n"
        "v_readfirstlane_b32 s40, v237\n"
        "s_nop 3\n"
        "s_cmp_ge_i32 s40, 4\n"
        "s_cbranch_scc1 L65_%=\n"
        "s_nop 0\n"
        "s_sub_u32 s41, s41, 1\n"
        "s_cbranch_scc0 L64_%=\n"
        "s_endpgm\n"
        "L65_%=:\n"
        "ds_write_b128 v244, v[132:135]\n"
        "ds_write_b128 v244, v[136:139] offset:1024\n"
        "ds_write_b128 v244, v[124:127] offset:2048\n"
        "ds_write_b128 v245, v[68:71]\n"
        "ds_write_b128 v245, v[64:67] offset:1024\n"
        "ds_write_b128 v245, v[72:75] offset:2048\n"
        "s_mov_b64 exec, 1\n"
        "ds_inc_u32 v239, v239 offset:0\n"
        "s_mov_b64 exec, -1\n"
        "s_setpc_b64 s[30:31]\n"
        "PPRO_%=:\n"
        "s_add_u32 s64, s64, 128\n"
        "s_cmp_lg_u32 s6, 0\n"
        "s_cbranch_scc1 L82_%=\n"
        "s_mov_b32 exec_hi, 0\n"
        "v_lshlrev_b32 v27, 2, v56\n"
        "v_add_u32 v27, 21600, v27\n"
        "v_mov_b32 v28, 0\n"
        "ds_write_b32 v27, v28\n"
        "s_mov_b64 exec, 0x1fffff\n"
        "s_bfe_u32 s7, s2, 0x50003\n"
        "s_mul_i32 s7, s7, 2656\n"
        "v_lshlrev_b32 v27, 7, v56\n"
        "v_add_u32 v27, s7, v27\n"
        "global_load_dword v252, v27, s[4:5]\n"
        "s_mov_b64 exec, -1\n"
        "L82_%=:\n"
        "s_cmp_lt_u32 s6, 2\n"
        "s_cselect_b32 s65, 3, 1\n"
        "s_cselect_b32 s61, 384, 0\n"
        "s_cselect_b32 s62, 9, 1\n"
        "s_cselect_b32 s63, 3, 4\n"
        "s_cselect_b32 s26, 3, 1\n"
        "s_cselect_b32 s34, 9, 1\n"
        "s_cselect_b32 s70, 72, 1\n"
        "s_cselect_b32 s71, 36, 4\n"
        "s_cselect_b32 s72, 6, 8\n"
        "s_mul_i32 s70, s70, 192\n"
        "s_mul_i32 s71, s71, 192\n"
        "s_mul_i32 s72, s72, 192\n"
        "s_mul_i32 s65, s65, 192\n"
        "s_mul_i32 s62, s62, 3072\n"
        "s_mul_i32 s63, s63, 1536\n"
        "s_mul_i32 s26, s26, 18432\n"
        "s_mul_i32 s34, s34, 1536\n"
        "s_add_u32 s7, s62, s63\n"
        "s_lshl_b32 s29, s63, 1\n"
        "s_add_u32 s60, s7, s63\n"
        "s_bitcmp1_b32 s6, 0\n"
        "s_cselect_b32 s28, s7, 0\n"
        "s_cselect_b32 s29, s29, s62\n"
        "s_cselect_b32 s60, s60, s63\n"
        "s_add_u32 s28, s28, s61\n"
        "s_add_u32 s29, s29, s61\n"
        "s_add_u32 s60, s60, s61\n"
        "v_add_u32 v24, 0, v56\n"
        "v_mul_u32_u24 v21, 171, v24\n"
        "v_lshrrev_b32 v21, 11, v21\n"
        "v_mad_i32_i24 v24, v21, -12, v24\n"
        "v_bfe_u32 v58, v21, 2, 2\n"
        "v_xor_b32 v58, v24, v58\n"
        "v_and_b32 v27, 3, v21\n"
        "v_mul_u32_u24 v27, s70, v27\n"
        "v_lshl_add_u32 v32, v58, 4, v27\n"
        "v_bfe_u32 v27, v21, 2, 1\n"
        "v_mad_u32_u24 v32, v27, s71, v32\n"
        "v_lshrrev_b32 v27, 3, v21\n"
        "v_mad_u32_u24 v32, v27, s72, v32\n"
        "v_and_b32 v27, 7, v21\n"
        "v_mul_u32_u24 v27, s65, v27\n"
        "v_lshl_add_u32 v58, v58, 4, v27\n"
        "v_add_u32 v25, 64, v56\n"
        "v_mul_u32_u24 v22, 171, v25\n"
        "v_lshrrev_b32 v22, 11, v22\n"
        "v_mad_i32_i24 v25, v22, -12, v25\n"
        "v_bfe_u32 v59, v22, 2, 2\n"
        "v_xor_b32 v59, v25, v59\n"
        "v_and_b32 v27, 3, v22\n"
        "v_mul_u32_u24 v27, s70, v27\n"
        "v_lshl_add_u32 v33, v59, 4, v27\n"
        "v_bfe_u32 v27, v22, 2, 1\n"
        "v_mad_u32_u24 v33, v27, s71, v33\n"
        "v_lshrrev_b32 v27, 3, v22\n"
        "v_mad_u32_u24 v33, v27, s72, v33\n"
        "v_and_b32 v27, 7, v22\n"
        "v_mul_u32_u24 v27, s65, v27\n"
        "v_lshl_add_u32 v59, v59, 4, v27\n"
        "v_lshrrev_b32 v27, 3, v22\n"
        "v_mad_u32_u24 v59, v27, s34, v59\n"
        "v_add_u32 v26, 128, v56\n"
        "v_mul_u32_u24 v23, 171, v26\n"
        "v_lshrrev_b32 v23, 11, v23\n"
        "v_mad_i32_i24 v26, v23, -12, v26\n"
        "v_bfe_u32 v60, v23, 2, 2\n"
        "v_xor_b32 v60, v26, v60\n"
        "v_and_b32 v27, 3, v23\n"
        "v_mul_u32_u24 v27, s70, v27\n"
        "v_lshl_add_u32 v34, v60, 4, v27\n"
        "v_bfe_u32 v27, v23, 2, 1\n"
        "v_mad_u32_u24 v34, v27, s71, v34\n"
        "v_lshrrev_b32 v27, 3, v23\n"
        "v_mad_u32_u24 v34, v27, s72, v34\n"
        "v_and_b32 v27, 7, v23\n"
        "v_mul_u32_u24 v27, s65, v27\n"
        "v_lshl_add_u32 v60, v60, 4, v27\n"
        "v_add_u32 v60, s34, v60\n"
        "s_and_b32 s7, s2, 7\n"
        "s_lshl_b32 s7, s7, 5\n"
        "s_lshr_b32 s14, s2, 3\n"
        "s_add_u32 s7, s7, s14\n"
        "s_and_b32 s16, s7, 255\n"
        "s_sub_u32 s7, s7, s16\n"
        "s_mul_i32 s7, s7, 20\n"
        "s_add_u32 s7, s7, s16\n"
        "s_mov_b32 s14, 22\n"
        "s_mul_i32 s15, s7, 57457\n"
        "s_lshr_b32 s15, s15, 22\n"
        "s_mov_b32 s16, s7\n"
        "s_mul_i32 s15, s15, 588672\n"
        "s_mul_i32 s7, s16, 1344\n"
        "s_add_u32 s15, s15, s7\n"
        "s_mul_i32 s74, s6, 98112\n"
        "s_add_u32 s74, s74, s15\n"
        "s_mov_b32 s36, 96768\n"
        "s_mov_b32 s40, 599187\n"
        "s_cmp_eq_u32 s6, 3\n"
        "s_cselect_b32 s7, s16, s6\n"
        "s_cselect_b32 s36, 588672, s36\n"
        "s_cselect_b32 s74, 589824, s74\n"
        "s_cselect_b32 s37, 512, 1\n"
        "s_cselect_b32 s38, 256, 4\n"
        "s_cselect_b32 s39, 7, 1\n"
        "s_cselect_b32 s40, 57457, s40\n"
        "s_mul_i32 s39, s39, 192\n"
        "s_mov_b32 s35, 50135232\n"
        "v_mov_b32 v29, 2048\n"
        "v_bfe_u32 v27, v21, 2, 1\n"
        "v_and_b32 v21, 3, v21\n"
        "v_mul_u32_u24 v21, s37, v21\n"
        "v_mad_u32_u24 v21, v27, s38, v21\n"
        "v_add_u32 v21, s7, v21\n"
        "v_and_b32 v27, 3, v24\n"
        "v_mad_u32_u24 v29, v27, 48, v29\n"
        "v_lshrrev_b32 v27, 2, v24\n"
        "v_lshl_add_u32 v29, v27, 4, v29\n"
        "v_mul_u32_u24 v27, s40, v21\n"
        "v_mul_u32_u24 v21, s39, v21\n"
        "v_lshrrev_b32 v27, 22, v27\n"
        "v_mul_u32_u24 v27, s36, v27\n"
        "v_add3_u32 v29, v29, v21, v27\n"
        "v_lshrrev_b32 v27, 3, v22\n"
        "v_xor_b32 v25, v25, v27\n"
        "v_mul_lo_u32 v30, v27, s35\n"
        "v_add_u32 v30, 1024, v30\n"
        "v_bfe_u32 v27, v22, 2, 1\n"
        "v_and_b32 v22, 3, v22\n"
        "v_mul_u32_u24 v22, s37, v22\n"
        "v_mad_u32_u24 v22, v27, s38, v22\n"
        "v_add_u32 v22, s7, v22\n"
        "v_and_b32 v27, 3, v25\n"
        "v_mad_u32_u24 v30, v27, 48, v30\n"
        "v_lshrrev_b32 v27, 2, v25\n"
        "v_lshl_add_u32 v30, v27, 4, v30\n"
        "v_mul_u32_u24 v27, s40, v22\n"
        "v_mul_u32_u24 v22, s39, v22\n"
        "v_lshrrev_b32 v27, 22, v27\n"
        "v_mul_u32_u24 v27, s36, v27\n"
        "v_add3_u32 v30, v30, v22, v27\n"
        "v_mov_b32 v31, 50135232\n"
        "v_xor_b32 v26, 1, v26\n"
        "v_bfe_u32 v27, v23, 2, 1\n"
        "v_and_b32 v23, 3, v23\n"
        "v_mul_u32_u24 v23, s37, v23\n"
        "v_mad_u32_u24 v23, v27, s38, v23\n"
        "v_add_u32 v23, s7, v23\n"
        "v_and_b32 v27, 3, v26\n"
        "v_mad_u32_u24 v31, v27, 48, v31\n"
        "v_lshrrev_b32 v27, 2, v26\n"
        "v_lshl_add_u32 v31, v27, 4, v31\n"
        "v_mul_u32_u24 v27, s40, v23\n"
        "v_mul_u32_u24 v23, s39, v23\n"
        "v_lshrrev_b32 v27, 22, v27\n"
        "v_mul_u32_u24 v27, s36, v27\n"
        "v_add3_u32 v31, v31, v23, v27\n"
        "s_mul_i32 s17, s6, 6208\n"
        "s_add_u32 s17, s17, 110176\n"
        "s_mov_b32 s18, s6\n"
        "s_add_u32 s75, s74, 100270464\n"
        "v_mov_b32 v250, 21600\n"
        "s_mov_b32 s41, 0x400000\n"
        "s_waitcnt lgkmcnt(0)\n"
        "s_cmp_lg_u32 s6, 0\n"
        "s_cbranch_scc1 L83_%=\n"
        "s_bitcmp1_b32 s2, 7\n"
        "s_cselect_b64 s[8:9], s[50:51], s[46:47]\n"
        "s_cselect_b64 s[10:11], s[52:53], s[48:49]\n"
        "s_bfe_u32 s7, s2, 0x40003\n"
        "s_cmp_ge_u32 s7, 12\n"
        "s_cselect_b64 s[8:9], s[10:11], s[8:9]\n"
        "s_cselect_b32 s10, 12, 0\n"
        "s_sub_u32 s7, s7, s10\n"
        "s_mul_i32 s7, s7, 4608\n"
        "v_lshlrev_b32 v27, 7, v56\n"
        "v_add_u32 v27, s7, v27\n"
        "s_mov_b32 exec_hi, 0xf\n"
        "global_load_dword v252, v27, s[8:9]\n"
        "s_mov_b32 exec_hi, -1\n"
        "s_mov_b64 s[24:25], s[56:57]\n"
        "s_mov_b64 exec, 1\n"
        "v_mov_b32 v27, 0\n"
        "global_load_dword v253, v27, s[24:25] offset:256 sc1\n"
        "s_mov_b64 exec, -1\n"
        "L83_%=:\n"
        "s_cmp_lt_u32 s6, 2\n"
        "s_cselect_b64 s[24:25], s[46:47], s[48:49]\n"
        "s_and_b32 s25, s25, 0xffff\n"
        "s_sub_u32 s8, s44, 2048\n"
        "s_subb_u32 s9, s45, 0\n"
        "s_and_b32 s9, s9, 0xffff\n"
        "s_mov_b32 s10, 200542976\n"
        "s_mov_b32 s11, 0x20000\n"
        "s_mul_i32 s7, s6, 7168\n"
        "s_add_u32 s7, s7, 6144\n"
        "s_add_u32 s22, s4, s7\n"
        "s_addc_u32 s23, s5, 0\n"
        "s_sub_u32 s7, s6, 0\n"
        "s_and_b32 s7, s7, 3\n"
        "s_min_u32 s7, s7, 2\n"
        "s_lshl_b32 s7, s7, 10\n"
        "s_add_u32 s73, s7, 18528\n"
        "v_add_u32 v27, s7, v57\n"
        "s_barrier\n"
        "s_mov_b32 m0, s73\n"
        "s_nop 0\n"
        "global_load_lds_dwordx4 v27, s[4:5]\n"
        "global_load_dwordx4 v[240:243], v57, s[22:23] offset:-3072\n"
        "global_load_dwordx4 v[244:247], v57, s[22:23] offset:-2048\n"
        "s_add_u32 m0, s64, 0\n"
        "s_nop 0\n"
        "buffer_load_dwordx4 v58, s[24:27], s28 offen lds\n"
        "s_add_u32 m0, s64, 1024\n"
        "s_nop 0\n"
        "buffer_load_dwordx4 v59, s[24:27], s28 offen lds\n"
        "s_add_u32 m0, s64, 2048\n"
        "s_nop 0\n"
        "buffer_load_dwordx4 v60, s[24:27], s28 offen lds\n"
        "s_add_u32 m0, s64, 3072\n"
        "s_nop 0\n"
        "buffer_load_dwordx4 v58, s[24:27], s29 offen lds\n"
        "s_add_u32 m0, s64, 4096\n"
        "s_nop 0\n"
        "buffer_load_dwordx4 v59, s[24:27], s29 offen lds\n"
        "s_add_u32 m0, s64, 5120\n"
        "s_nop 0\n"
        "buffer_load_dwordx4 v60, s[24:27], s29 offen lds\n"
        "s_add_u32 m0, s64, 6144\n"
        "s_nop 0\n"
        "buffer_load_dwordx4 v58, s[24:27], s60 offen lds\n"
        "s_add_u32 m0, s64, 7168\n"
        "s_nop 0\n"
        "buffer_load_dwordx4 v59, s[24:27], s60 offen lds\n"
        "s_add_u32 m0, s64, 8192\n"
        "s_nop 0\n"
        "buffer_load_dwordx4 v60, s[24:27], s60 offen lds\n"
        "s_cmp_lt_u32 s6, 2\n"
        "s_cselect_b32 s62, 3, 16\n"
        "s_cselect_b32 s63, 12, 32\n"
        "s_cselect_b64 s[24:25], s[50:51], s[52:53]\n"
        "s_cselect_b32 s64, 0, 117088\n"
        "s_mul_i32 s62, s62, 192\n"
        "s_mul_i32 s63, s63, 192\n"
        "s_and_b32 s25, s25, 0xffff\n"
        "s_mul_i32 s7, s6, 9216\n"
        "s_add_u32 s64, s64, s7\n"
        "s_add_u32 s64, s64, 0\n"
        "s_add_u32 s7, s62, s63\n"
        "s_lshl_b32 s29, s63, 1\n"
        "s_add_u32 s60, s7, s63\n"
        "s_bitcmp1_b32 s6, 0\n"
        "s_cselect_b32 s28, s7, 0\n"
        "s_cselect_b32 s29, s29, s62\n"
        "s_cselect_b32 s60, s60, s63\n"
        "s_add_u32 s28, s28, s61\n"
        "s_add_u32 s29, s29, s61\n"
        "s_add_u32 s60, s60, s61\n"
        "s_add_u32 m0, s64, 0\n"
        "s_nop 0\n"
        "buffer_load_dwordx4 v32, s[24:27], s28 offen lds\n"
        "s_add_u32 m0, s64, 1024\n"
        "s_nop 0\n"
        "buffer_load_dwordx4 v33, s[24:27], s28 offen lds\n"
        "s_add_u32 m0, s64, 2048\n"
        "s_nop 0\n"
        "buffer_load_dwordx4 v34, s[24:27], s28 offen lds\n"
        "s_add_u32 m0, s64, 3072\n"
        "s_nop 0\n"
        "buffer_load_dwordx4 v32, s[24:27], s29 offen lds\n"
        "s_add_u32 m0, s64, 4096\n"
        "s_nop 0\n"
        "buffer_load_dwordx4 v33, s[24:27], s29 offen lds\n"
        "s_add_u32 m0, s64, 5120\n"
        "s_nop 0\n"
        "buffer_load_dwordx4 v34, s[24:27], s29 offen lds\n"
        "s_add_u32 m0, s64, 6144\n"
        "s_nop 0\n"
        "buffer_load_dwordx4 v32, s[24:27], s60 offen lds\n"
        "s_add_u32 m0, s64, 7168\n"
        "s_nop 0\n"
        "buffer_load_dwordx4 v33, s[24:27], s60 offen lds\n"
        "s_add_u32 m0, s64, 8192\n"
        "s_nop 0\n"
        "buffer_load_dwordx4 v34, s[24:27], s60 offen lds\n"
        "s_setprio 3\n"
        "s_waitcnt vmcnt(9)\n"
        "s_mov_b64 exec, 1\n"
        "ds_inc_u32 v250, v250 offset:20\n"
        "s_mov_b64 exec, -1\n"
        "L84_%=:\n"
        "ds_read_b32 v251, v250 offset:20\n"
        "s_waitcnt lgkmcnt(0)\n"
        "v_readfirstlane_b32 s40, v251\n"
        "s_nop 3\n"
        "s_cmp_ge_i32 s40, 4\n"
        "s_cbranch_scc1 L85_%=\n"
        "s_nop 0\n"
        "s_sub_u32 s41, s41, 1\n"
        "s_cbranch_scc0 L84_%=\n"
        "s_endpgm\n"
        "L85_%=:\n"
        "s_add_u32 m0, s17, 0\n"
        "s_nop 0\n"
        "buffer_load_dwordx4 v29, s[8:11], s74 offen lds\n"
        "buffer_load_dwordx4 v30, s[8:11], s74 offen offset:1024 lds\n"
        "buffer_load_dwordx4 v31, s[8:11], s74 offen offset:2048 lds\n"
        "s_add_u32 m0, s17, 3104\n"
        "s_nop 0\n"
        "buffer_load_dwordx4 v29, s[8:11], s75 offen lds\n"
        "buffer_load_dwordx4 v30, s[8:11], s75 offen offset:1024 lds\n"
        "buffer_load_dwordx4 v31, s[8:11], s75 offen offset:2048 lds\n"
        "s_cmp_eq_u32 s6, 0\n"
        "s_cbranch_scc1 L86_%=\n"
        "s_mov_b32 s34, 0x411cc471\n"
        "s_mov_b32 s35, 21632\n"
        "s_mov_b32 s36, 21624\n"
        "s_mov_b32 s37, 21628\n"
        "s_call_b64 s[30:31], FOLDW_%=\n"
        "s_branch L87_%=\n"
        "L86_%=:\n"
        "s_waitcnt vmcnt(6)\n"
        "s_mov_b64 exec, 1\n"
        "ds_inc_u32 v250, v250 offset:32\n"
        "s_mov_b64 exec, -1\n"
        "global_load_dwordx4 v[40:43], v57, s[22:23] offset:-3072\n"
        "global_load_dwordx4 v[44:47], v57, s[22:23] offset:-2048\n"
        "global_load_dwordx4 v[0:3], v57, s[22:23] offset:-1024\n"
        "global_load_dwordx4 v[4:7], v57, s[22:23] offset:0\n"
        "global_load_dwordx4 v[8:11], v57, s[22:23] offset:1024\n"
        "global_load_dwordx4 v[12:15], v57, s[22:23] offset:2048\n"
        "global_load_dwordx4 v[16:19], v57, s[22:23] offset:3072\n"
        "L87_%=:\n"
        "s_mov_b64 s[24:25], s[56:57]\n"
        "s_setprio 3\n"
        "s_mov_b32 s44, 0xf0000f\n"
        "s_mov_b32 s45, 0xf0000f00\n"
        "s_mov_b32 s46, 0xff000ff\n"
        "s_mov_b32 s47, 0xf00fff00\n"
        "s_mov_b32 s48, 0x22222222\n"
        "s_mov_b32 s49, 0x22222222\n"
        "s_mov_b32 s50, 0x44444444\n"
        "s_mov_b32 s51, 0x44444444\n"
        "s_mov_b32 s52, 0x88888888\n"
        "s_mov_b32 s53, 0x88888888\n"
        "s_mov_b64 vcc, s[44:45]\n"
        "s_mov_b32 s28, 0x3c2aaaab\n"
        "s_mov_b32 s29, 0x3a7ba882\n"
        "s_mov_b32 s30, 0\n"
        "s_mov_b32 s34, 0\n"
        "s_cmp_eq_u32 s18, 3\n"
        "s_cselect_b32 s31, 6, s18\n"
        "s_mov_b32 s36, s18\n"
        "s_mov_b32 s37, s18\n"
        "s_add_u32 s71, s17, 3104\n"
        "s_mov_b32 s68, 4\n"
        "s_mov_b32 s74, 16\n"
        "s_mov_b32 s72, -1\n"
        "s_mov_b32 s41, 0x400000\n"
        "L23_%=:\n"
        "ds_read_b32 v251, v250 offset:28\n"
        "s_waitcnt lgkmcnt(0)\n"
        "v_readfirstlane_b32 s40, v251\n"
        "s_nop 3\n"
        "s_cmp_ge_i32 s40, 6\n"
        "s_cbranch_scc1 L24_%=\n"
        "s_nop 0\n"
        "s_sub_u32 s41, s41, 1\n"
        "s_cbranch_scc0 L23_%=\n"
        "s_endpgm\n"
        "L24_%=:\n"
        "s_waitcnt vmcnt(0)\n"
        "v_readfirstlane_b32 s26, v253\n"
        "ds_read_b128 v[16:19], v16\n"
        "ds_read_b64 v[94:95], v14\n"
        "ds_read_b64 v[86:87], v14\n"
        "ds_read_b64 v[28:29], v14\n"
        "ds_read_b64 v[90:91], v14\n"
        "v_mov_b32 v102, 21600\n"
        "v_add_u32 v20, 21728, v13\n"
        "ds_read_b128 v[112:115], v20 offset:0\n"
        "ds_read_b128 v[116:119], v20 offset:1024\n"
        "ds_read_b128 v[120:123], v20 offset:2048\n"
        "ds_read_b128 v[124:127], v20 offset:3072\n"
        "ds_read_b128 v[128:131], v20 offset:4096\n"
        "ds_read_b128 v[132:135], v20 offset:5120\n"
        "ds_read_b128 v[136:139], v20 offset:6144\n"
        "ds_read_b128 v[140:143], v20 offset:7168\n"
        "ds_read_b128 v[144:147], v20 offset:8192\n"
        "ds_read_b128 v[148:151], v20 offset:9216\n"
        "ds_read_b128 v[152:155], v20 offset:10240\n"
        "ds_read_b128 v[156:159], v20 offset:11264\n"
        "ds_read_b128 v[160:163], v20 offset:12288\n"
        "ds_read_b128 v[164:167], v20 offset:13312\n"
        "ds_read_b128 v[168:171], v20 offset:14336\n"
        "ds_read_b128 v[172:175], v20 offset:15360\n"
        "ds_read_b128 v[176:179], v20 offset:16384\n"
        "ds_read_b128 v[180:183], v20 offset:17408\n"
        "ds_read_b128 v[184:187], v20 offset:18432\n"
        "ds_read_b128 v[188:191], v20 offset:19456\n"
        "ds_read_b128 v[192:195], v20 offset:20480\n"
        "ds_read_b128 v[196:199], v20 offset:21520\n"
        "ds_read_b128 v[200:203], v20 offset:22544\n"
        "ds_read_b128 v[204:207], v20 offset:23568\n"
        "ds_read_b128 v[208:211], v20 offset:24608\n"
        "ds_read_b128 v[212:215], v20 offset:25632\n"
        "ds_read_b128 v[216:219], v20 offset:26656\n"
        "ds_read_b128 v[220:223], v20 offset:27696\n"
        "ds_read_b128 v[224:227], v20 offset:28720\n"
        "ds_read_b128 v[228:231], v20 offset:29744\n"
        "ds_read_b128 v[232:235], v20 offset:30784\n"
        "ds_read_b128 v[236:239], v20 offset:31808\n"
        "ds_read_b128 v[240:243], v20 offset:32832\n"
        "ds_read_b128 v[244:247], v20 offset:33872\n"
        "ds_read_b128 v[248:251], v20 offset:34896\n"
        "ds_read_b128 v[252:255], v20 offset:35920\n"
        "ds_read_b128 v[60:63], v0 offset:0\n"
        "ds_read_b128 v[66:69], v0 offset:64\n"
        "ds_read_b128 v[72:75], v0 offset:128\n"
        "ds_read_b128 v[42:45], v0 offset:768\n"
        "ds_read_b128 v[48:51], v0 offset:832\n"
        "ds_read_b128 v[54:57], v0 offset:896\n"
        "s_add_u32 s37, s37, 4\n"
        "s_cmp_ge_u32 s37, 49\n"
        "s_cselect_b32 s69, 49, 0\n"
        "s_sub_u32 s37, s37, s69\n"
        "s_sub_u32 s69, s37, 4\n"
        "s_cmp_lt_u32 s69, 4\n"
        "s_subb_u32 s36, s36, -4\n"
        "s_cmp_ge_u32 s36, 6\n"
        "s_cselect_b32 s69, 6, 0\n"
        "s_addc_u32 s34, s34, 0\n"
        "s_sub_u32 s36, s36, s69\n"
        "s_cmp_ge_u32 s34, s74\n"
        "s_cbranch_scc0 L27_%=\n"
        "s_nop 1\n"
        "v_mov_b32 v22, 21600\n"
        "L25_%=:\n"
        "ds_read_b32 v23, v22 offset:64\n"
        "s_waitcnt lgkmcnt(0)\n"
        "v_readfirstlane_b32 s40, v23\n"
        "s_nop 3\n"
        "s_cmp_ge_i32 s40, 1\n"
        "s_cbranch_scc1 L26_%=\n"
        "s_sleep 1\n"
        "s_sub_u32 s41, s41, 1\n"
        "s_cbranch_scc0 L25_%=\n"
        "s_endpgm\n"
        "L26_%=:\n"
        "v_mbcnt_lo_u32_b32 v23, -1, 0\n"
        "v_lshl_add_u32 v22, v23, 2, v22\n"
        "ds_read_b32 v23, v22 offset:68\n"
        "s_mov_b32 s14, s40\n"
        "s_mov_b32 s74, -1\n"
        "s_waitcnt lgkmcnt(0)\n"
        "v_readlane_b32 s22, v23, 0\n"
        "v_readlane_b32 s23, v23, 1\n"
        "s_nop 3\n"
        "L27_%=:\n"
        "s_mul_i32 s38, s34, 256\n"
        "s_add_u32 s38, s38, s16\n"
        "s_cmp_ge_u32 s34, 20\n"
        "s_cselect_b32 s38, s22, s38\n"
        "s_cmp_ge_u32 s34, 21\n"
        "s_cselect_b32 s38, s23, s38\n"
        "s_mul_hi_u32 s70, s38, 58835169\n"
        "s_mul_i32 s38, s38, 1344\n"
        "s_mul_i32 s70, s70, 588672\n"
        "s_add_u32 s38, s38, s70\n"
        "s_mul_i32 s69, s36, 98304\n"
        "s_add_u32 s38, s38, s69\n"
        "s_mov_b32 s35, s36\n"
        "s_mul_i32 s69, s36, -12\n"
        "s_add_u32 s69, s69, 60\n"
        "s_lshl_b64 s[54:55], -1, s69\n"
        "s_lshl_b64 s[56:57], s[54:55], 24\n"
        "s_ashr_i64 s[60:61], s[54:55], 8\n"
        "s_mov_b32 s58, s61\n"
        "s_mov_b32 s59, s56\n"
        "v_cndmask_b32_e64 v15, v4, v7, s[56:57]\n"
        "v_cndmask_b32_e64 v100, v5, v8, s[58:59]\n"
        "v_cndmask_b32_e64 v103, v6, v9, s[60:61]\n"
        "s_cmp_eq_u32 s37, 3\n"
        "s_cbranch_scc0 L28_%=\n"
        "s_nop 1\n"
        "ds_read_b32 v15, v13 offset:19552\n"
        "ds_read_b32 v100, v13 offset:19556\n"
        "ds_read_b32 v103, v13 offset:19560\n"
        "s_mov_b32 s35, 6\n"
        "s_mov_b32 s38, 0\n"
        "s_mov_b32 s69, 1344\n"
        "s_waitcnt lgkmcnt(0)\n"
        "v_lshrrev_b32 v22, 28, v15\n"
        "v_add_u32 v22, s34, v22\n"
        "v_and_b32 v15, 0x0fffffff, v15\n"
        "v_mul_u32_u24 v23, 256, v22\n"
        "v_add_u32 v23, s16, v23\n"
        "v_cmpx_le_u32 vcc, 20, v22\n"
        "v_mov_b32 v23, s22\n"
        "s_mov_b64 exec, -1\n"
        "v_cmpx_le_u32 vcc, 21, v22\n"
        "v_mov_b32 v23, s23\n"
        "s_mov_b64 exec, -1\n"
        "v_mad_u32_u24 v15, v23, s69, v15\n"
        "v_mul_u32_u24 v23, 57457, v23\n"
        "v_lshrrev_b32 v23, 22, v23\n"
        "v_mul_u32_u24 v23, 588672, v23\n"
        "v_add_u32 v15, v15, v23\n"
        "v_lshrrev_b32 v22, 28, v100\n"
        "v_add_u32 v22, s34, v22\n"
        "v_and_b32 v100, 0x0fffffff, v100\n"
        "v_mul_u32_u24 v23, 256, v22\n"
        "v_add_u32 v23, s16, v23\n"
        "v_cmpx_le_u32 vcc, 20, v22\n"
        "v_mov_b32 v23, s22\n"
        "s_mov_b64 exec, -1\n"
        "v_cmpx_le_u32 vcc, 21, v22\n"
        "v_mov_b32 v23, s23\n"
        "s_mov_b64 exec, -1\n"
        "v_mad_u32_u24 v100, v23, s69, v100\n"
        "v_mul_u32_u24 v23, 57457, v23\n"
        "v_lshrrev_b32 v23, 22, v23\n"
        "v_mul_u32_u24 v23, 588672, v23\n"
        "v_add_u32 v100, v100, v23\n"
        "v_lshrrev_b32 v22, 28, v103\n"
        "v_add_u32 v22, s34, v22\n"
        "v_and_b32 v103, 0x0fffffff, v103\n"
        "v_mul_u32_u24 v23, 256, v22\n"
        "v_add_u32 v23, s16, v23\n"
        "v_cmpx_le_u32 vcc, 20, v22\n"
        "v_mov_b32 v23, s22\n"
        "s_mov_b64 exec, -1\n"
        "v_cmpx_le_u32 vcc, 21, v22\n"
        "v_mov_b32 v23, s23\n"
        "s_mov_b64 exec, -1\n"
        "v_mad_u32_u24 v103, v23, s69, v103\n"
        "v_mul_u32_u24 v23, 57457, v23\n"
        "v_lshrrev_b32 v23, 22, v23\n"
        "v_mul_u32_u24 v23, 588672, v23\n"
        "v_add_u32 v103, v103, v23\n"
        "s_nop 4\n"
        "s_mov_b64 vcc, s[44:45]\n"
        "L28_%=:\n"
        "s_cmp_lt_u32 s34, s14\n"
        "s_cselect_b32 s38, s38, 0x80000000\n"
        "s_add_u32 s39, s38, 100270464\n"
        "s_waitcnt lgkmcnt(0)\n"
        "s_mov_b64 exec, 1\n"
        "ds_inc_u32 v102, v0 offset:8\n"
        "s_mov_b64 exec, -1\n"
        "s_cmp_eq_u32 s18, 0\n"
        "s_cselect_b32 s73, -20, 8\n"
        "s_setprio 1\n"
        "PFST_%=:\n"
        "v_mfma_f32_16x16x32_bf16 v[34:37], v[112:115], v[60:63], 0\n"
        "s_mov_b32 m0, s17\n"
        "v_mfma_f32_16x16x32_bf16 v[34:37], v[116:119], v[66:69], v[34:37]\n"
        "v_mfma_f32_16x16x32_bf16 v[34:37], v[120:123], v[72:75], v[34:37]\n"
        "buffer_load_dwordx4 v15, s[8:11], s38 offen lds\n"
        "v_mfma_f32_16x16x32_bf16 v[38:41], v[132:135], v[72:75], 0\n"
        "v_mfma_f32_16x16x32_bf16 v[38:41], v[128:131], v[66:69], v[38:41]\n"
        "s_setprio 3\n"
        "v_mfma_f32_16x16x32_bf16 v[38:41], v[124:127], v[60:63], v[38:41]\n"
        "buffer_load_dwordx4 v100, s[8:11], s38 offen offset:1024 lds\n"
        "v_mfma_f32_16x16x32_bf16 v[78:81], v[136:139], v[60:63], 0\n"
        "v_mfma_f32_16x16x32_bf16 v[78:81], v[140:143], v[66:69], v[78:81]\n"
        "v_mfma_f32_16x16x32_bf16 v[78:81], v[144:147], v[72:75], v[78:81]\n"
        "v_mfma_f32_16x16x32_bf16 v[82:85], v[156:159], v[72:75], 0\n"
        "v_mfma_f32_16x16x32_bf16 v[82:85], v[152:155], v[66:69], v[82:85]\n"
        "v_mfma_f32_16x16x32_bf16 v[82:85], v[148:151], v[60:63], v[82:85]\n"
        "buffer_load_dwordx4 v103, s[8:11], s38 offen offset:2048 lds\n"
        "s_mov_b32 m0, s71\n"
        "s_cmp_eq_u32 s72, s30\n"
        "v_mfma_f32_16x16x32_bf16 v[104:107], v[160:163], v[60:63], 0\n"
        "s_setprio 3\n"
        "s_cbranch_scc1 F5_%=\n"
        "s_setprio 1\n"
        "F5_%=:\n"
        "v_mfma_f32_16x16x32_bf16 v[104:107], v[164:167], v[66:69], v[104:107]\n"
        "v_mfma_f32_16x16x32_bf16 v[104:107], v[168:171], v[72:75], v[104:107]\n"
        "buffer_load_dwordx4 v15, s[8:11], s39 offen lds\n"
        "v_mfma_f32_16x16x32_bf16 v[108:111], v[180:183], v[72:75], 0\n"
        "v_mfma_f32_16x16x32_bf16 v[108:111], v[176:179], v[66:69], v[108:111]\n"
        "v_mfma_f32_16x16x32_bf16 v[108:111], v[172:175], v[60:63], v[108:111]\n"
        "buffer_load_dwordx4 v100, s[8:11], s39 offen offset:1024 lds\n"
        "v_mfma_f32_16x16x32_bf16 v[20:23], v[60:63], v[60:63], 0\n"
        "v_mfma_f32_16x16x32_bf16 v[20:23], v[66:69], v[66:69], v[20:23]\n"
        "v_mfma_f32_16x16x32_bf16 v[20:23], v[72:75], v[72:75], v[20:23]\n"
        "buffer_load_dwordx4 v103, s[8:11], s39 offen offset:2048 lds\n"
        "v_mov_b64 v[96:97], 0\n"
        "v_mov_b64 v[98:99], 0\n"
        "v_mfma_f32_16x16x32_bf16 v[24:27], v[42:45], v[42:45], 0\n"
        "v_mfma_f32_16x16x32_bf16 v[24:27], v[48:51], v[48:51], v[24:27]\n"
        "v_mfma_f32_16x16x32_bf16 v[24:27], v[54:57], v[54:57], v[24:27]\n"
        "v_smfmac_f32_16x16x64_bf16 v[96:99], v[60:63], v[34:41], v10\n"
        "s_nop 0\n"
        "v_cndmask_b32_e64 v93, v20, v21, s[48:49]\n"
        "v_smfmac_f32_16x16x64_bf16 v[96:99], v[66:69], v[78:85], v10\n"
        "v_cndmask_b32_e64 v93, v93, v22, s[50:51]\n"
        "v_smfmac_f32_16x16x64_bf16 v[96:99], v[72:75], v[104:111], v10\n"
        "v_mfma_f32_16x16x32_bf16 v[34:37], v[112:115], v[42:45], 0\n"
        "s_nop 0\n"
        "v_cndmask_b32_e64 v111, v24, v25, s[48:49]\n"
        "v_cndmask_b32_e64 v111, v111, v26, s[50:51]\n"
        "v_mfma_f32_16x16x32_bf16 v[34:37], v[116:119], v[48:51], v[34:37]\n"
        "s_and_b32 s69, s30, 1\n"
        "v_cndmask_b32_e64 v93, v93, v23, s[52:53]\n"
        "v_cndmask_b32_e64 v111, v111, v27, s[52:53]\n"
        "v_mfma_f32_16x16x32_bf16 v[34:37], v[120:123], v[54:57], v[34:37]\n"
        "s_lshl2_add_u32 s70, s69, 21600\n"
        "v_mov_b32 v102, s70\n"
        "v_cndmask_b32_dpp v93, v111, v93, vcc row_ror:4 row_mask:0xf bank_mask:0xf\n"
        "v_mfma_f32_16x16x32_bf16 v[38:41], v[132:135], v[54:57], 0\n"
        "s_mul_i32 s69, s69, 40064\n"
        "s_mul_i32 s70, s31, 1664\n"
        "v_mfma_f32_16x16x32_bf16 v[38:41], v[128:131], v[48:51], v[38:41]\n"
        "s_add_u32 s69, s69, s70\n"
        "v_add_u32 v11, s69, v3\n"
        "v_mfma_f32_16x16x32_bf16 v[38:41], v[124:127], v[42:45], v[38:41]\n"
        "v_add_u32 v12, s69, v2\n"
        "s_lshr_b32 s68, s30, 1\n"
        "v_mfma_f32_16x16x32_bf16 v[104:107], v[136:139], v[42:45], 0\n"
        "s_lshl2_add_u32 s68, s68, 4\n"
        "s_cmp_eq_u32 s31, 6\n"
        "s_cbranch_scc1 F8_%=\n"
        "F9_%=:\n"
        "v_mfma_f32_16x16x32_bf16 v[104:107], v[140:143], v[48:51], v[104:107]\n"
        "s_mov_b32 s72, s30\n"
        "s_mov_b32 s30, s34\n"
        "v_mfma_f32_16x16x32_bf16 v[104:107], v[144:147], v[54:57], v[104:107]\n"
        "s_mov_b32 s31, s35\n"
        "s_add_u32 s37, s37, 4\n"
        "v_mfma_f32_16x16x32_bf16 v[108:111], v[156:159], v[54:57], 0\n"
        "s_cmp_ge_u32 s37, 49\n"
        "s_cselect_b32 s69, 49, 0\n"
        "v_mfma_f32_16x16x32_bf16 v[108:111], v[152:155], v[48:51], v[108:111]\n"
        "s_sub_u32 s37, s37, s69\n"
        "s_sub_u32 s69, s37, 4\n"
        "s_cmp_lt_u32 s69, 4\n"
        "v_mfma_f32_16x16x32_bf16 v[108:111], v[148:151], v[42:45], v[108:111]\n"
        "s_subb_u32 s36, s36, -4\n"
        "s_cmp_ge_u32 s36, 6\n"
        "v_mfma_f32_16x16x32_bf16 v[76:79], v[160:163], v[42:45], 0\n"
        "s_cselect_b32 s69, 6, 0\n"
        "s_addc_u32 s34, s34, 0\n"
        "v_mfma_f32_16x16x32_bf16 v[76:79], v[164:167], v[48:51], v[76:79]\n"
        "s_sub_u32 s36, s36, s69\n"
        "s_cmp_ge_u32 s34, s74\n"
        "v_mfma_f32_16x16x32_bf16 v[76:79], v[168:171], v[54:57], v[76:79]\n"
        "s_cbranch_scc1 F10_%=\n"
        "F11_%=:\n"
        "s_mul_i32 s38, s34, 256\n"
        "v_mfma_f32_16x16x32_bf16 v[80:83], v[180:183], v[54:57], 0\n"
        "s_add_u32 s38, s38, s16\n"
        "s_cmp_ge_u32 s34, 20\n"
        "s_cselect_b32 s38, s22, s38\n"
        "v_mfma_f32_16x16x32_bf16 v[80:83], v[176:179], v[48:51], v[80:83]\n"
        "s_cmp_ge_u32 s34, 21\n"
        "s_cselect_b32 s38, s23, s38\n"
        "v_mfma_f32_16x16x32_bf16 v[80:83], v[172:175], v[42:45], v[80:83]\n"
        "s_mul_hi_u32 s70, s38, 58835169\n"
        "s_mul_i32 s38, s38, 1344\n"
        "v_mfma_f32_16x16x32_bf16 v[20:23], v[16:19], v[60:63], 0\n"
        "s_mul_i32 s70, s70, 588672\n"
        "s_add_u32 s38, s38, s70\n"
        "v_mfma_f32_16x16x32_bf16 v[20:23], v[16:19], v[66:69], v[20:23]\n"
        "s_mul_i32 s69, s36, 98304\n"
        "s_add_u32 s38, s38, s69\n"
        "s_mov_b32 s35, s36\n"
        "v_mfma_f32_16x16x32_bf16 v[20:23], v[16:19], v[72:75], v[20:23]\n"
        "s_mul_i32 s69, s36, -12\n"
        "v_mov_b64 v[30:31], 0\n"
        "v_mov_b64 v[32:33], 0\n"
        "v_mfma_f32_16x16x32_bf16 v[24:27], v[16:19], v[42:45], 0\n"
        "s_add_u32 s69, s69, 60\n"
        "s_lshl_b64 s[54:55], -1, s69\n"
        "s_lshl_b64 s[56:57], s[54:55], 24\n"
        "v_mfma_f32_16x16x32_bf16 v[24:27], v[16:19], v[48:51], v[24:27]\n"
        "s_ashr_i64 s[60:61], s[54:55], 8\n"
        "s_mov_b32 s58, s61\n"
        "v_mfma_f32_16x16x32_bf16 v[24:27], v[16:19], v[54:57], v[24:27]\n"
        "s_mov_b32 s59, s56\n"
        "v_cndmask_b32_e64 v15, v4, v7, s[56:57]\n"
        "v_smfmac_f32_16x16x64_bf16 v[30:33], v[42:45], v[34:41], v10\n"
        "v_cndmask_b32_e64 v100, v5, v8, s[58:59]\n"
        "v_smfmac_f32_16x16x64_bf16 v[30:33], v[48:51], v[104:111], v10\n"
        "v_cndmask_b32_e64 v103, v6, v9, s[60:61]\n"
        "v_smfmac_f32_16x16x64_bf16 v[30:33], v[54:57], v[76:83], v10\n"
        "s_cmp_eq_u32 s37, 3\n"
        "s_cbranch_scc1 F12_%=\n"
        "F13_%=:\n"
        "v_cndmask_b32_dpp v110, v24, v20, vcc row_ror:4 row_mask:0xf bank_mask:0xf\n"
        "v_mfma_f32_16x16x32_bf16 v[36:39], v[54:57], v[192:195], 0\n"
        "s_cmp_lt_u32 s34, s14\n"
        "v_mul_f32 v111, s28, v110\n"
        "v_fma_f32 v111, -v111, v110, v93\n"
        "v_mfma_f32_16x16x32_bf16 v[36:39], v[48:51], v[188:191], v[36:39]\n"
        "s_cselect_b32 s38, s38, 0x80000000\n"
        "s_add_u32 s39, s38, 100270464\n"
        "v_add_f32_e64 v111, s29, |v111|\n"
        "v_mfma_f32_16x16x32_bf16 v[36:39], v[42:45], v[184:187], v[36:39]\n"
        "v_rsq_f32 v84, v111\n"
        "v_cndmask_b32_dpp v104, v30, v96, vcc row_ror:4 row_mask:0xf bank_mask:0xf\n"
        "v_mfma_f32_16x16x32_bf16 v[24:27], v[72:75], v[192:195], 0\n"
        "v_cndmask_b32_e64 v84, 0, v84, s[46:47]\n"
        "v_cndmask_b32_dpp v105, v31, v97, vcc row_ror:4 row_mask:0xf bank_mask:0xf\n"
        "v_mfma_f32_16x16x32_bf16 v[24:27], v[66:69], v[188:191], v[24:27]\n"
        "v_cndmask_b32_dpp v85, v32, v98, vcc row_ror:4 row_mask:0xf bank_mask:0xf\n"
        "v_mul_f32_dpp v104, v84, v104 quad_perm:[0,0,0,0] row_mask:0xf bank_mask:0xf\n"
        "v_mfma_f32_16x16x32_bf16 v[24:27], v[60:63], v[184:187], v[24:27]\n"
        "v_mul_f32_dpp v105, v84, v105 quad_perm:[1,1,1,1] row_mask:0xf bank_mask:0xf\n"
        "v_mul_f32_dpp v85, v84, v85 quad_perm:[2,2,2,2] row_mask:0xf bank_mask:0xf\n"
        "v_mfma_f32_16x16x32_bf16 v[20:23], v[60:63], v[220:223], 0\n"
        "v_cndmask_b32_dpp v106, v33, v99, vcc row_ror:4 row_mask:0xf bank_mask:0xf\n"
        "v_max3_f32 v107, v104, v105, v85\n"
        "v_mfma_f32_16x16x32_bf16 v[20:23], v[66:69], v[224:227], v[20:23]\n"
        "v_mul_f32_dpp v106, v84, v106 quad_perm:[3,3,3,3] row_mask:0xf bank_mask:0xf\n"
        "v_max_f32 v107, v107, v106\n"
        "v_mfma_f32_16x16x32_bf16 v[20:23], v[72:75], v[228:231], v[20:23]\n"
        "v_mul_f32 v107, v107, v84\n"
        "v_fma_f32 v104, v104, v84, -v107\n"
        "v_fma_f32 v105, v105, v84, -v107\n"
        "v_mfma_f32_16x16x32_bf16 v[32:35], v[42:45], v[220:223], 0\n"
        "v_fma_f32 v85, v85, v84, -v107\n"
        "v_fma_f32 v106, v106, v84, -v107\n"
        "v_mfma_f32_16x16x32_bf16 v[32:35], v[48:51], v[224:227], v[32:35]\n"
        "v_exp_f32 v104, v104\n"
        "v_mfma_f32_16x16x32_bf16 v[32:35], v[54:57], v[228:231], v[32:35]\n"
        "v_exp_f32 v105, v105\n"
        "v_mfma_f32_16x16x32_bf16 v[108:111], v[54:57], v[204:207], 0\n"
        "v_exp_f32 v85, v85\n"
        "v_mfma_f32_16x16x32_bf16 v[108:111], v[48:51], v[200:203], v[108:111]\n"
        "v_exp_f32 v106, v106\n"
        "v_mfma_f32_16x16x32_bf16 v[108:111], v[42:45], v[196:199], v[108:111]\n"
        "v_add_f32 v107, v104, v105\n"
        "v_add_f32 v101, v85, v106\n"
        "v_add_f32 v107, v107, v101\n"
        "v_mfma_f32_16x16x32_bf16 v[80:83], v[72:75], v[204:207], 0\n"
        "v_rcp_f32 v107, v107\n"
        "v_mfma_f32_16x16x32_bf16 v[80:83], v[66:69], v[200:203], v[80:83]\n"
        "v_mul_f32_dpp v104, v84, v104 quad_perm:[0,0,0,0] row_mask:0xf bank_mask:0xf\n"
        "v_mul_f32_dpp v105, v84, v105 quad_perm:[1,1,1,1] row_mask:0xf bank_mask:0xf\n"
        "v_mfma_f32_16x16x32_bf16 v[80:83], v[60:63], v[196:199], v[80:83]\n"
        "v_mul_f32_dpp v85, v84, v85 quad_perm:[2,2,2,2] row_mask:0xf bank_mask:0xf\n"
        "v_mul_f32_dpp v106, v84, v106 quad_perm:[3,3,3,3] row_mask:0xf bank_mask:0xf\n"
        "v_mul_f32 v104, v104, v107\n"
        "v_mfma_f32_16x16x32_bf16 v[76:79], v[60:63], v[232:235], 0\n"
        "v_mul_f32 v105, v105, v107\n"
        "v_mul_f32 v85, v85, v107\n"
        "v_mfma_f32_16x16x32_bf16 v[76:79], v[66:69], v[236:239], v[76:79]\n"
        "v_mul_f32 v106, v106, v107\n"
        "v_cvt_pk_bf16_f32 v99, v104, v105\n"
        "v_mfma_f32_16x16x32_bf16 v[76:79], v[72:75], v[240:243], v[76:79]\n"
        "v_cvt_pk_bf16_f32 v101, v85, v106\n"
        "v_and_b32 v58, v1, v99\n"
        "v_and_b32 v59, v1, v101\n"
        "v_mfma_f32_16x16x32_bf16 v[104:107], v[42:45], v[232:235], 0\n"
        "v_cvt_pk_bf16_f32 v92, v24, v25\n"
        "v_cvt_pk_bf16_f32 v93, v26, v27\n"
        "v_mfma_f32_16x16x32_bf16 v[104:107], v[48:51], v[236:239], v[104:107]\n"
        "v_and_b32_dpp v40, v99, v1 row_ror:12 row_mask:0xf bank_mask:0xf\n"
        "v_and_b32_dpp v41, v101, v1 row_ror:12 row_mask:0xf bank_mask:0xf\n"
        "v_mfma_f32_16x16x32_bf16 v[104:107], v[54:57], v[240:243], v[104:107]\n"
        "v_cvt_pk_bf16_f32 v84, v36, v37\n"
        "v_cvt_pk_bf16_f32 v85, v38, v39\n"
        "v_mov_b64 v[70:71], v[58:59]\n"
        "v_mfma_f32_16x16x32_bf16 v[36:39], v[92:95], v[58:61], 0\n"
        "v_cvt_pk_bf16_f32 v96, v20, v21\n"
        "v_cvt_pk_bf16_f32 v97, v22, v23\n"
        "v_mfma_f32_16x16x32_bf16 v[22:25], v[84:87], v[40:43], 0\n"
        "v_mov_b64 v[52:53], v[40:41]\n"
        "v_cvt_pk_bf16_f32 v88, v32, v33\n"
        "v_cvt_pk_bf16_f32 v89, v34, v35\n"
        "v_mfma_f32_16x16x32_bf16 v[32:35], v[94:97], v[68:71], 0\n"
        "v_mov_b64 v[64:65], v[58:59]\n"
        "v_cvt_pk_bf16_f32 v30, v80, v81\n"
        "v_mfma_f32_16x16x32_bf16 v[96:99], v[86:89], v[50:53], 0\n"
        "v_cvt_pk_bf16_f32 v31, v82, v83\n"
        "v_mov_b64 v[46:47], v[40:41]\n"
        "v_mfma_f32_16x16x32_bf16 v[80:83], v[54:57], v[216:219], 0\n"
        "v_cvt_pk_bf16_f32 v92, v108, v109\n"
        "v_cvt_pk_bf16_f32 v93, v110, v111\n"
        "v_mfma_f32_16x16x32_bf16 v[80:83], v[48:51], v[212:215], v[80:83]\n"
        "v_cvt_pk_bf16_f32 v26, v76, v77\n"
        "v_cvt_pk_bf16_f32 v27, v78, v79\n"
        "v_mfma_f32_16x16x32_bf16 v[80:83], v[42:45], v[208:211], v[80:83]\n"
        "v_cvt_pk_bf16_f32 v88, v104, v105\n"
        "v_cvt_pk_bf16_f32 v89, v106, v107\n"
        "v_mfma_f32_16x16x32_bf16 v[108:111], v[72:75], v[216:219], 0\n"
        "v_mov_b64 v[76:77], v[58:59]\n"
        "v_mov_b64 v[58:59], v[40:41]\n"
        "v_mfma_f32_16x16x32_bf16 v[108:111], v[66:69], v[212:215], v[108:111]\n"
        "v_cvt_pk_bf16_f32 v104, v36, v37\n"
        "v_cvt_pk_bf16_f32 v105, v38, v39\n"
        "v_mfma_f32_16x16x32_bf16 v[108:111], v[60:63], v[208:211], v[108:111]\n"
        "v_cvt_pk_bf16_f32 v84, v80, v81\n"
        "v_cvt_pk_bf16_f32 v85, v82, v83\n"
        "v_mfma_f32_16x16x32_bf16 v[80:83], v[28:31], v[62:65], 0\n"
        "v_cvt_pk_bf16_f32 v106, v32, v33\n"
        "v_cvt_pk_bf16_f32 v107, v34, v35\n"
        "v_mfma_f32_16x16x32_bf16 v[32:35], v[90:93], v[44:47], 0\n"
        "v_cvt_pk_bf16_f32 v20, v22, v23\n"
        "v_cvt_pk_bf16_f32 v21, v24, v25\n"
        "v_mfma_f32_16x16x32_bf16 v[38:41], v[60:63], v[244:247], 0\n"
        "v_cvt_pk_bf16_f32 v92, v108, v109\n"
        "v_cvt_pk_bf16_f32 v93, v110, v111\n"
        "v_mfma_f32_16x16x32_bf16 v[38:41], v[66:69], v[248:251], v[38:41]\n"
        "v_cvt_pk_bf16_f32 v108, v80, v81\n"
        "v_cvt_pk_bf16_f32 v109, v82, v83\n"
        "v_mfma_f32_16x16x32_bf16 v[38:41], v[72:75], v[252:255], v[38:41]\n"
        "v_cvt_pk_bf16_f32 v22, v96, v97\n"
        "v_cvt_pk_bf16_f32 v23, v98, v99\n"
        "v_mfma_f32_16x16x32_bf16 v[80:83], v[42:45], v[244:247], 0\n"
        "v_cvt_pk_bf16_f32 v24, v32, v33\n"
        "v_cvt_pk_bf16_f32 v25, v34, v35\n"
        "v_mfma_f32_16x16x32_bf16 v[80:83], v[48:51], v[248:251], v[80:83]\n"
        "v_mfma_f32_16x16x32_bf16 v[80:83], v[54:57], v[252:255], v[80:83]\n"
        "v_mfma_f32_16x16x32_bf16 v[34:37], v[26:29], v[70:73], 0\n"
        "v_cvt_pk_bf16_f32 v96, v38, v39\n"
        "v_cvt_pk_bf16_f32 v97, v40, v41\n"
        "v_mfma_f32_16x16x32_bf16 v[38:41], v[88:91], v[52:55], 0\n"
        "v_mfma_f32_16x16x32_bf16 v[30:33], v[92:95], v[64:67], 0\n"
        "s_waitcnt vmcnt(0)\n"
        "ds_read_b128 v[60:63], v0\n"
        "ds_read_b128 v[42:45], v0 offset:768\n"
        "ds_read_b128 v[66:69], v0 offset:64\n"
        "v_cvt_pk_bf16_f32 v88, v80, v81\n"
        "v_mfma_f32_16x16x32_bf16 v[78:81], v[84:87], v[46:49], 0\n"
        "s_add_u32 s73, s73, 1\n"
        "ds_read_b128 v[48:51], v0 offset:832\n"
        "v_cvt_pk_bf16_f32 v89, v82, v83\n"
        "v_cvt_pk_bf16_f32 v110, v34, v35\n"
        "v_cvt_pk_bf16_f32 v111, v36, v37\n"
        "v_mfma_f32_16x16x32_bf16 v[82:85], v[94:97], v[74:77], 0\n"
        "s_cmp_lt_u32 s73, 4\n"
        "ds_read_b128 v[72:75], v0 offset:128\n"
        "v_cvt_pk_bf16_f32 v96, v30, v31\n"
        "v_cvt_pk_bf16_f32 v97, v32, v33\n"
        "v_cvt_pk_bf16_f32 v26, v38, v39\n"
        "v_mfma_f32_16x16x32_bf16 v[34:37], v[86:89], v[56:59], 0\n"
        "s_cbranch_scc1 F19_%=\n"
        "F20_%=:\n"
        "ds_read_b128 v[54:57], v0 offset:896\n"
        "v_cvt_pk_bf16_f32 v98, v82, v83\n"
        "v_cvt_pk_bf16_f32 v99, v84, v85\n"
        "v_cvt_pk_bf16_f32 v27, v40, v41\n"
        "v_mfma_f32_16x16x32_bf16 v[82:85], v[104:107], v[104:107], 0\n"
        "v_cvt_pk_bf16_f32 v30, v78, v79\n"
        "v_cvt_pk_bf16_f32 v31, v80, v81\n"
        "v_mfma_f32_16x16x32_bf16 v[82:85], v[108:111], v[108:111], v[82:85]\n"
        "v_cvt_pk_bf16_f32 v32, v34, v35\n"
        "v_cvt_pk_bf16_f32 v33, v36, v37\n"
        "v_mfma_f32_16x16x32_bf16 v[82:85], v[96:99], v[96:99], v[82:85]\n"
        "v_mfma_f32_16x16x32_bf16 v[76:79], v[20:23], v[20:23], 0\n"
        "v_mfma_f32_16x16x32_bf16 v[76:79], v[24:27], v[24:27], v[76:79]\n"
        "v_mfma_f32_16x16x32_bf16 v[76:79], v[30:33], v[30:33], v[76:79]\n"
        "s_cmp_lt_u32 s30, s14\n"
        "s_cmp_lg_u32 s72, 0\n"
        "s_cbranch_scc1 PTOP_%=\n"
        "s_setprio 3\n"
        "s_nop 7\n"
        "F21_%=:\n"
        "ds_read_b32 v101, v102 offset:8\n"
        "s_waitcnt lgkmcnt(0)\n"
        "v_readfirstlane_b32 s40, v101\n"
        "s_nop 3\n"
        "s_cmp_ge_i32 s40, s68\n"
        "s_cbranch_scc1 F22_%=\n"
        "s_sleep 1\n"
        "s_sub_u32 s41, s41, 1\n"
        "s_cbranch_scc0 F21_%=\n"
        "s_endpgm\n"
        "F22_%=:\n"
        "v_cndmask_b32_e64 v101, v82, v83, s[48:49]\n"
        "v_cndmask_b32_e64 v93, v76, v77, s[48:49]\n"
        "v_cndmask_b32_e64 v101, v101, v84, s[50:51]\n"
        "v_cndmask_b32_e64 v93, v93, v78, s[50:51]\n"
        "v_cndmask_b32_e64 v101, v101, v85, s[52:53]\n"
        "v_cndmask_b32_e64 v93, v93, v79, s[52:53]\n"
        "ds_write_b128 v11, v[104:107]\n"
        "ds_write_b128 v11, v[20:23] offset:832\n"
        "v_cndmask_b32_dpp v101, v93, v101, vcc row_ror:4 row_mask:0xf bank_mask:0xf\n"
        "ds_write_b128 v11, v[108:111] offset:64\n"
        "ds_write_b128 v11, v[24:27] offset:896\n"
        "ds_write_b128 v11, v[96:99] offset:128\n"
        "ds_write_b128 v11, v[30:33] offset:960\n"
        "s_mov_b64 exec, s[46:47]\n"
        "ds_write_b32 v12, v101\n"
        "s_mov_b64 exec, -1\n"
        "s_mov_b64 exec, 2\n"
        "ds_inc_u32 v102, v0 offset:0\n"
        "s_mov_b64 exec, -1\n"
        "s_setprio 3\n"
        "s_waitcnt lgkmcnt(0)\n"
        "s_branch PFST_%=\n"
        ".p2align 6\n"
        "s_nop 0\n"
        "s_nop 0\n"
        "s_nop 0\n"
        "s_nop 0\n"
        "PTOP_%=:\n"
        "s_waitcnt lgkmcnt(5)\n"
        "v_mfma_f32_16x16x32_bf16 v[34:37], v[112:115], v[60:63], 0\n"
        "s_mov_b32 m0, s17\n"
        "v_cndmask_b32_e64 v88, v82, v83, s[48:49]\n"
        "s_nop 1\n"
        "v_cndmask_b32_e64 v89, v76, v77, s[48:49]\n"
        "s_waitcnt lgkmcnt(3)\n"
        "v_mfma_f32_16x16x32_bf16 v[34:37], v[116:119], v[66:69], v[34:37]\n"
        "ds_read_b32 v101, v102 offset:8\n"
        "v_cndmask_b32_e64 v88, v88, v84, s[50:51]\n"
        "s_waitcnt lgkmcnt(2)\n"
        "v_mfma_f32_16x16x32_bf16 v[34:37], v[120:123], v[72:75], v[34:37]\n"
        "s_waitcnt lgkmcnt(1)\n"
        "buffer_load_dwordx4 v15, s[8:11], s38 offen lds\n"
        "v_mfma_f32_16x16x32_bf16 v[38:41], v[132:135], v[72:75], 0\n"
        "v_cndmask_b32_e64 v89, v89, v78, s[50:51]\n"
        "v_cndmask_b32_e64 v88, v88, v85, s[52:53]\n"
        "v_mfma_f32_16x16x32_bf16 v[38:41], v[128:131], v[66:69], v[38:41]\n"
        "s_setprio 3 \n"
        "v_cndmask_b32_e64 v89, v89, v79, s[52:53]\n"
        "v_mfma_f32_16x16x32_bf16 v[38:41], v[124:127], v[60:63], v[38:41]\n"
        "buffer_load_dwordx4 v100, s[8:11], s38 offen offset:1024 lds\n"
        "v_mfma_f32_16x16x32_bf16 v[78:81], v[136:139], v[60:63], 0\n"
        "s_waitcnt lgkmcnt(0)\n"
        "v_readfirstlane_b32 s40, v101\n"
        "v_cndmask_b32_dpp v88, v89, v88, vcc row_ror:4 row_mask:0xf bank_mask:0xf\n"
        "v_mfma_f32_16x16x32_bf16 v[78:81], v[140:143], v[66:69], v[78:81]\n"
        "s_cmp_lt_i32 s40, s68\n"
        "v_mfma_f32_16x16x32_bf16 v[78:81], v[144:147], v[72:75], v[78:81]\n"
        "s_cbranch_scc1 L3_%=\n"
        "L4_%=:\n"
        "ds_write_b128 v11, v[104:107]\n"
        "s_mov_b64 exec, s[46:47]\n"
        "ds_write_b32 v12, v88\n"
        "s_mov_b64 exec, -1\n"
        "v_mfma_f32_16x16x32_bf16 v[82:85], v[156:159], v[72:75], 0\n"
        "v_mfma_f32_16x16x32_bf16 v[82:85], v[152:155], v[66:69], v[82:85]\n"
        "v_mfma_f32_16x16x32_bf16 v[82:85], v[148:151], v[60:63], v[82:85]\n"
        "buffer_load_dwordx4 v103, s[8:11], s38 offen offset:2048 lds\n"
        "s_mov_b32 m0, s71\n"
        "ds_write_b128 v11, v[20:23] offset:832\n"
        "s_cmp_eq_u32 s72, s30\n"
        "v_mfma_f32_16x16x32_bf16 v[104:107], v[160:163], v[60:63], 0\n"
        "s_setprio 3\n"
        "s_cbranch_scc1 L5_%=\n"
        "s_setprio 1\n"
        "L5_%=:\n"
        "v_mfma_f32_16x16x32_bf16 v[104:107], v[164:167], v[66:69], v[104:107]\n"
        "v_mfma_f32_16x16x32_bf16 v[104:107], v[168:171], v[72:75], v[104:107]\n"
        "buffer_load_dwordx4 v15, s[8:11], s39 offen lds\n"
        "ds_write_b128 v11, v[108:111] offset:64\n"
        "v_mfma_f32_16x16x32_bf16 v[108:111], v[180:183], v[72:75], 0\n"
        "v_mfma_f32_16x16x32_bf16 v[108:111], v[176:179], v[66:69], v[108:111]\n"
        "v_mfma_f32_16x16x32_bf16 v[108:111], v[172:175], v[60:63], v[108:111]\n"
        "buffer_load_dwordx4 v100, s[8:11], s39 offen offset:1024 lds\n"
        "ds_write_b128 v11, v[24:27] offset:896\n"
        "v_mfma_f32_16x16x32_bf16 v[20:23], v[60:63], v[60:63], 0\n"
        "v_mfma_f32_16x16x32_bf16 v[20:23], v[66:69], v[66:69], v[20:23]\n"
        "v_mfma_f32_16x16x32_bf16 v[20:23], v[72:75], v[72:75], v[20:23]\n"
        "buffer_load_dwordx4 v103, s[8:11], s39 offen offset:2048 lds\n"
        "ds_write_b128 v11, v[96:99] offset:128\n"
        "v_mov_b64 v[96:97], 0\n"
        "v_mov_b64 v[98:99], 0\n"
        "v_mfma_f32_16x16x32_bf16 v[24:27], v[42:45], v[42:45], 0\n"
        "v_mfma_f32_16x16x32_bf16 v[24:27], v[48:51], v[48:51], v[24:27]\n"
        "v_mfma_f32_16x16x32_bf16 v[24:27], v[54:57], v[54:57], v[24:27]\n"
        "ds_write_b128 v11, v[30:33] offset:960\n"
        "v_smfmac_f32_16x16x64_bf16 v[96:99], v[60:63], v[34:41], v10\n"
        "v_cndmask_b32_e64 v93, v20, v21, s[48:49]\n"
        "v_smfmac_f32_16x16x64_bf16 v[96:99], v[66:69], v[78:85], v10\n"
        "v_cndmask_b32_e64 v93, v93, v22, s[50:51]\n"
        "v_smfmac_f32_16x16x64_bf16 v[96:99], v[72:75], v[104:111], v10\n"
        "s_mov_b64 exec, 2\n"
        "ds_inc_u32 v102, v0 offset:0\n"
        "s_mov_b64 exec, -1\n"
        "v_mfma_f32_16x16x32_bf16 v[34:37], v[112:115], v[42:45], 0\n"
        "v_cndmask_b32_e64 v111, v24, v25, s[48:49]\n"
        "v_cndmask_b32_e64 v111, v111, v26, s[50:51]\n"
        "v_mfma_f32_16x16x32_bf16 v[34:37], v[116:119], v[48:51], v[34:37]\n"
        "s_and_b32 s69, s30, 1\n"
        "v_cndmask_b32_e64 v93, v93, v23, s[52:53]\n"
        "v_cndmask_b32_e64 v111, v111, v27, s[52:53]\n"
        "v_mfma_f32_16x16x32_bf16 v[34:37], v[120:123], v[54:57], v[34:37]\n"
        "s_lshl2_add_u32 s70, s69, 21600\n"
        "v_mov_b32 v102, s70\n"
        "v_cndmask_b32_dpp v93, v111, v93, vcc row_ror:4 row_mask:0xf bank_mask:0xf\n"
        "v_mfma_f32_16x16x32_bf16 v[38:41], v[132:135], v[54:57], 0\n"
        "s_mul_i32 s69, s69, 40064\n"
        "s_mul_i32 s70, s31, 1664\n"
        "v_mfma_f32_16x16x32_bf16 v[38:41], v[128:131], v[48:51], v[38:41]\n"
        "s_add_u32 s69, s69, s70\n"
        "v_add_u32 v11, s69, v3\n"
        "v_mfma_f32_16x16x32_bf16 v[38:41], v[124:127], v[42:45], v[38:41]\n"
        "v_add_u32 v12, s69, v2\n"
        "s_lshr_b32 s68, s30, 1\n"
        "v_mfma_f32_16x16x32_bf16 v[104:107], v[136:139], v[42:45], 0\n"
        "s_lshl2_add_u32 s68, s68, 4\n"
        "s_cmp_eq_u32 s31, 6\n"
        "s_cbranch_scc1 L8_%=\n"
        "L9_%=:\n"
        "v_mfma_f32_16x16x32_bf16 v[104:107], v[140:143], v[48:51], v[104:107]\n"
        "s_mov_b32 s72, s30\n"
        "s_mov_b32 s30, s34\n"
        "v_mfma_f32_16x16x32_bf16 v[104:107], v[144:147], v[54:57], v[104:107]\n"
        "s_mov_b32 s31, s35\n"
        "s_add_u32 s37, s37, 4\n"
        "v_mfma_f32_16x16x32_bf16 v[108:111], v[156:159], v[54:57], 0\n"
        "s_cmp_ge_u32 s37, 49\n"
        "s_cselect_b32 s69, 49, 0\n"
        "v_mfma_f32_16x16x32_bf16 v[108:111], v[152:155], v[48:51], v[108:111]\n"
        "s_sub_u32 s37, s37, s69\n"
        "s_sub_u32 s69, s37, 4\n"
        "s_cmp_lt_u32 s69, 4\n"
        "v_mfma_f32_16x16x32_bf16 v[108:111], v[148:151], v[42:45], v[108:111]\n"
        "s_subb_u32 s36, s36, -4\n"
        "s_cmp_ge_u32 s36, 6\n"
        "v_mfma_f32_16x16x32_bf16 v[76:79], v[160:163], v[42:45], 0\n"
        "s_cselect_b32 s69, 6, 0\n"
        "s_addc_u32 s34, s34, 0\n"
        "v_mfma_f32_16x16x32_bf16 v[76:79], v[164:167], v[48:51], v[76:79]\n"
        "s_sub_u32 s36, s36, s69\n"
        "s_cmp_ge_u32 s34, s74\n"
        "v_mfma_f32_16x16x32_bf16 v[76:79], v[168:171], v[54:57], v[76:79]\n"
        "s_cbranch_scc1 L10_%=\n"
        "L11_%=:\n"
        "s_mul_i32 s38, s34, 256\n"
        "v_mfma_f32_16x16x32_bf16 v[80:83], v[180:183], v[54:57], 0\n"
        "s_add_u32 s38, s38, s16\n"
        "s_cmp_ge_u32 s34, 20\n"
        "s_cselect_b32 s38, s22, s38\n"
        "v_mfma_f32_16x16x32_bf16 v[80:83], v[176:179], v[48:51], v[80:83]\n"
        "s_cmp_ge_u32 s34, 21\n"
        "s_cselect_b32 s38, s23, s38\n"
        "v_mfma_f32_16x16x32_bf16 v[80:83], v[172:175], v[42:45], v[80:83]\n"
        "s_mul_hi_u32 s70, s38, 58835169\n"
        "s_mul_i32 s38, s38, 1344\n"
        "v_mfma_f32_16x16x32_bf16 v[20:23], v[16:19], v[60:63], 0\n"
        "s_mul_i32 s70, s70, 588672\n"
        "s_add_u32 s38, s38, s70\n"
        "v_mfma_f32_16x16x32_bf16 v[20:23], v[16:19], v[66:69], v[20:23]\n"
        "s_mul_i32 s69, s36, 98304\n"
        "s_add_u32 s38, s38, s69\n"
        "s_mov_b32 s35, s36\n"
        "v_mfma_f32_16x16x32_bf16 v[20:23], v[16:19], v[72:75], v[20:23]\n"
        "s_mul_i32 s69, s36, -12\n"
        "v_mov_b64 v[30:31], 0\n"
        "v_mov_b64 v[32:33], 0\n"
        "v_mfma_f32_16x16x32_bf16 v[24:27], v[16:19], v[42:45], 0\n"
        "s_add_u32 s69, s69, 60\n"
        "s_lshl_b64 s[54:55], -1, s69\n"
        "s_lshl_b64 s[56:57], s[54:55], 24\n"
        "v_mfma_f32_16x16x32_bf16 v[24:27], v[16:19], v[48:51], v[24:27]\n"
        "s_ashr_i64 s[60:61], s[54:55], 8\n"
        "s_mov_b32 s58, s61\n"
        "v_mfma_f32_16x16x32_bf16 v[24:27], v[16:19], v[54:57], v[24:27]\n"
        "s_mov_b32 s59, s56\n"
        "v_cndmask_b32_e64 v15, v4, v7, s[56:57]\n"
        "v_smfmac_f32_16x16x64_bf16 v[30:33], v[42:45], v[34:41], v10\n"
        "v_cndmask_b32_e64 v100, v5, v8, s[58:59]\n"
        "v_smfmac_f32_16x16x64_bf16 v[30:33], v[48:51], v[104:111], v10\n"
        "v_cndmask_b32_e64 v103, v6, v9, s[60:61]\n"
        "v_smfmac_f32_16x16x64_bf16 v[30:33], v[54:57], v[76:83], v10\n"
        "s_cmp_eq_u32 s37, 3\n"
        "s_cbranch_scc1 L12_%=\n"
        "L13_%=:\n"
        "v_cndmask_b32_dpp v110, v24, v20, vcc row_ror:4 row_mask:0xf bank_mask:0xf\n"
        "v_mfma_f32_16x16x32_bf16 v[36:39], v[54:57], v[192:195], 0\n"
        "s_cmp_lt_u32 s34, s14\n"
        "v_mul_f32 v111, s28, v110\n"
        "v_fma_f32 v111, -v111, v110, v93\n"
        "v_mfma_f32_16x16x32_bf16 v[36:39], v[48:51], v[188:191], v[36:39]\n"
        "s_cselect_b32 s38, s38, 0x80000000\n"
        "s_add_u32 s39, s38, 100270464\n"
        "v_add_f32_e64 v111, s29, |v111|\n"
        "v_mfma_f32_16x16x32_bf16 v[36:39], v[42:45], v[184:187], v[36:39]\n"
        "v_rsq_f32 v84, v111\n"
        "v_cndmask_b32_dpp v104, v30, v96, vcc row_ror:4 row_mask:0xf bank_mask:0xf\n"
        "v_mfma_f32_16x16x32_bf16 v[24:27], v[72:75], v[192:195], 0\n"
        "v_cndmask_b32_e64 v84, 0, v84, s[46:47]\n"
        "v_cndmask_b32_dpp v105, v31, v97, vcc row_ror:4 row_mask:0xf bank_mask:0xf\n"
        "v_mfma_f32_16x16x32_bf16 v[24:27], v[66:69], v[188:191], v[24:27]\n"
        "v_cndmask_b32_dpp v85, v32, v98, vcc row_ror:4 row_mask:0xf bank_mask:0xf\n"
        "v_mul_f32_dpp v104, v84, v104 quad_perm:[0,0,0,0] row_mask:0xf bank_mask:0xf\n"
        "v_mfma_f32_16x16x32_bf16 v[24:27], v[60:63], v[184:187], v[24:27]\n"
        "v_mul_f32_dpp v105, v84, v105 quad_perm:[1,1,1,1] row_mask:0xf bank_mask:0xf\n"
        "v_mul_f32_dpp v85, v84, v85 quad_perm:[2,2,2,2] row_mask:0xf bank_mask:0xf\n"
        "v_mfma_f32_16x16x32_bf16 v[20:23], v[60:63], v[220:223], 0\n"
        "v_cndmask_b32_dpp v106, v33, v99, vcc row_ror:4 row_mask:0xf bank_mask:0xf\n"
        "v_max3_f32 v107, v104, v105, v85\n"
        "v_mfma_f32_16x16x32_bf16 v[20:23], v[66:69], v[224:227], v[20:23]\n"
        "v_mul_f32_dpp v106, v84, v106 quad_perm:[3,3,3,3] row_mask:0xf bank_mask:0xf\n"
        "v_max_f32 v107, v107, v106\n"
        "v_mfma_f32_16x16x32_bf16 v[20:23], v[72:75], v[228:231], v[20:23]\n"
        "v_mul_f32 v107, v107, v84\n"
        "v_fma_f32 v104, v104, v84, -v107\n"
        "v_fma_f32 v105, v105, v84, -v107\n"
        "v_mfma_f32_16x16x32_bf16 v[32:35], v[42:45], v[220:223], 0\n"
        "v_fma_f32 v85, v85, v84, -v107\n"
        "v_fma_f32 v106, v106, v84, -v107\n"
        "v_mfma_f32_16x16x32_bf16 v[32:35], v[48:51], v[224:227], v[32:35]\n"
        "v_exp_f32 v104, v104\n"
        "v_mfma_f32_16x16x32_bf16 v[32:35], v[54:57], v[228:231], v[32:35]\n"
        "v_exp_f32 v105, v105\n"
        "v_mfma_f32_16x16x32_bf16 v[108:111], v[54:57], v[204:207], 0\n"
        "v_exp_f32 v85, v85\n"
        "v_mfma_f32_16x16x32_bf16 v[108:111], v[48:51], v[200:203], v[108:111]\n"
        "v_exp_f32 v106, v106\n"
        "v_mfma_f32_16x16x32_bf16 v[108:111], v[42:45], v[196:199], v[108:111]\n"
        "v_add_f32 v107, v104, v105\n"
        "v_add_f32 v101, v85, v106\n"
        "v_add_f32 v107, v107, v101\n"
        "v_mfma_f32_16x16x32_bf16 v[80:83], v[72:75], v[204:207], 0\n"
        "v_rcp_f32 v107, v107\n"
        "v_mfma_f32_16x16x32_bf16 v[80:83], v[66:69], v[200:203], v[80:83]\n"
        "v_mul_f32_dpp v104, v84, v104 quad_perm:[0,0,0,0] row_mask:0xf bank_mask:0xf\n"
        "v_mul_f32_dpp v105, v84, v105 quad_perm:[1,1,1,1] row_mask:0xf bank_mask:0xf\n"
        "v_mfma_f32_16x16x32_bf16 v[80:83], v[60:63], v[196:199], v[80:83]\n"
        "v_mul_f32_dpp v85, v84, v85 quad_perm:[2,2,2,2] row_mask:0xf bank_mask:0xf\n"
        "v_mul_f32_dpp v106, v84, v106 quad_perm:[3,3,3,3] row_mask:0xf bank_mask:0xf\n"
        "v_mul_f32 v104, v104, v107\n"
        "v_mfma_f32_16x16x32_bf16 v[76:79], v[60:63], v[232:235], 0\n"
        "v_mul_f32 v105, v105, v107\n"
        "v_mul_f32 v85, v85, v107\n"
        "v_mfma_f32_16x16x32_bf16 v[76:79], v[66:69], v[236:239], v[76:79]\n"
        "v_mul_f32 v106, v106, v107\n"
        "v_cvt_pk_bf16_f32 v99, v104, v105\n"
        "v_mfma_f32_16x16x32_bf16 v[76:79], v[72:75], v[240:243], v[76:79]\n"
        "v_cvt_pk_bf16_f32 v101, v85, v106\n"
        "v_and_b32 v58, v1, v99\n"
        "v_and_b32 v59, v1, v101\n"
        "v_mfma_f32_16x16x32_bf16 v[104:107], v[42:45], v[232:235], 0\n"
        "v_cvt_pk_bf16_f32 v92, v24, v25\n"
        "v_cvt_pk_bf16_f32 v93, v26, v27\n"
        "v_mfma_f32_16x16x32_bf16 v[104:107], v[48:51], v[236:239], v[104:107]\n"
        "v_and_b32_dpp v40, v99, v1 row_ror:12 row_mask:0xf bank_mask:0xf\n"
        "v_and_b32_dpp v41, v101, v1 row_ror:12 row_mask:0xf bank_mask:0xf\n"
        "v_mfma_f32_16x16x32_bf16 v[104:107], v[54:57], v[240:243], v[104:107]\n"
        "v_cvt_pk_bf16_f32 v84, v36, v37\n"
        "v_cvt_pk_bf16_f32 v85, v38, v39\n"
        "v_mov_b64 v[70:71], v[58:59]\n"
        "v_mfma_f32_16x16x32_bf16 v[36:39], v[92:95], v[58:61], 0\n"
        "v_cvt_pk_bf16_f32 v96, v20, v21\n"
        "v_cvt_pk_bf16_f32 v97, v22, v23\n"
        "v_mfma_f32_16x16x32_bf16 v[22:25], v[84:87], v[40:43], 0\n"
        "v_mov_b64 v[52:53], v[40:41]\n"
        "v_cvt_pk_bf16_f32 v88, v32, v33\n"
        "v_cvt_pk_bf16_f32 v89, v34, v35\n"
        "v_mfma_f32_16x16x32_bf16 v[32:35], v[94:97], v[68:71], 0\n"
        "v_mov_b64 v[64:65], v[58:59]\n"
        "v_cvt_pk_bf16_f32 v30, v80, v81\n"
        "v_mfma_f32_16x16x32_bf16 v[96:99], v[86:89], v[50:53], 0\n"
        "v_cvt_pk_bf16_f32 v31, v82, v83\n"
        "v_mov_b64 v[46:47], v[40:41]\n"
        "v_mfma_f32_16x16x32_bf16 v[80:83], v[54:57], v[216:219], 0\n"
        "v_cvt_pk_bf16_f32 v92, v108, v109\n"
        "v_cvt_pk_bf16_f32 v93, v110, v111\n"
        "v_mfma_f32_16x16x32_bf16 v[80:83], v[48:51], v[212:215], v[80:83]\n"
        "v_cvt_pk_bf16_f32 v26, v76, v77\n"
        "v_cvt_pk_bf16_f32 v27, v78, v79\n"
        "v_mfma_f32_16x16x32_bf16 v[80:83], v[42:45], v[208:211], v[80:83]\n"
        "v_cvt_pk_bf16_f32 v88, v104, v105\n"
        "v_cvt_pk_bf16_f32 v89, v106, v107\n"
        "v_mfma_f32_16x16x32_bf16 v[108:111], v[72:75], v[216:219], 0\n"
        "v_mov_b64 v[76:77], v[58:59]\n"
        "v_mov_b64 v[58:59], v[40:41]\n"
        "v_mfma_f32_16x16x32_bf16 v[108:111], v[66:69], v[212:215], v[108:111]\n"
        "v_cvt_pk_bf16_f32 v104, v36, v37\n"
        "v_cvt_pk_bf16_f32 v105, v38, v39\n"
        "v_mfma_f32_16x16x32_bf16 v[108:111], v[60:63], v[208:211], v[108:111]\n"
        "v_cvt_pk_bf16_f32 v84, v80, v81\n"
        "v_cvt_pk_bf16_f32 v85, v82, v83\n"
        "v_mfma_f32_16x16x32_bf16 v[80:83], v[28:31], v[62:65], 0\n"
        "v_cvt_pk_bf16_f32 v106, v32, v33\n"
        "v_cvt_pk_bf16_f32 v107, v34, v35\n"
        "v_mfma_f32_16x16x32_bf16 v[32:35], v[90:93], v[44:47], 0\n"
        "v_cvt_pk_bf16_f32 v20, v22, v23\n"
        "v_cvt_pk_bf16_f32 v21, v24, v25\n"
        "v_mfma_f32_16x16x32_bf16 v[38:41], v[60:63], v[244:247], 0\n"
        "v_cvt_pk_bf16_f32 v92, v108, v109\n"
        "v_cvt_pk_bf16_f32 v93, v110, v111\n"
        "v_mfma_f32_16x16x32_bf16 v[38:41], v[66:69], v[248:251], v[38:41]\n"
        "v_cvt_pk_bf16_f32 v108, v80, v81\n"
        "v_cvt_pk_bf16_f32 v109, v82, v83\n"
        "v_mfma_f32_16x16x32_bf16 v[38:41], v[72:75], v[252:255], v[38:41]\n"
        "v_cvt_pk_bf16_f32 v22, v96, v97\n"
        "v_cvt_pk_bf16_f32 v23, v98, v99\n"
        "v_mfma_f32_16x16x32_bf16 v[80:83], v[42:45], v[244:247], 0\n"
        "v_cvt_pk_bf16_f32 v24, v32, v33\n"
        "v_cvt_pk_bf16_f32 v25, v34, v35\n"
        "v_mfma_f32_16x16x32_bf16 v[80:83], v[48:51], v[248:251], v[80:83]\n"
        "v_mfma_f32_16x16x32_bf16 v[80:83], v[54:57], v[252:255], v[80:83]\n"
        "v_mfma_f32_16x16x32_bf16 v[34:37], v[26:29], v[70:73], 0\n"
        "v_cvt_pk_bf16_f32 v96, v38, v39\n"
        "v_cvt_pk_bf16_f32 v97, v40, v41\n"
        "v_mfma_f32_16x16x32_bf16 v[38:41], v[88:91], v[52:55], 0\n"
        "v_mfma_f32_16x16x32_bf16 v[30:33], v[92:95], v[64:67], 0\n"
        "s_waitcnt vmcnt(0)\n"
        "ds_read_b128 v[60:63], v0\n"
        "ds_read_b128 v[42:45], v0 offset:768\n"
        "ds_read_b128 v[66:69], v0 offset:64\n"
        "v_cvt_pk_bf16_f32 v88, v80, v81\n"
        "v_mfma_f32_16x16x32_bf16 v[78:81], v[84:87], v[46:49], 0\n"
        "s_add_u32 s73, s73, 1\n"
        "ds_read_b128 v[48:51], v0 offset:832\n"
        "v_cvt_pk_bf16_f32 v89, v82, v83\n"
        "v_cvt_pk_bf16_f32 v110, v34, v35\n"
        "v_cvt_pk_bf16_f32 v111, v36, v37\n"
        "v_mfma_f32_16x16x32_bf16 v[82:85], v[94:97], v[74:77], 0\n"
        "s_cmp_lt_u32 s73, 4\n"
        "ds_read_b128 v[72:75], v0 offset:128\n"
        "v_cvt_pk_bf16_f32 v96, v30, v31\n"
        "v_cvt_pk_bf16_f32 v97, v32, v33\n"
        "v_cvt_pk_bf16_f32 v26, v38, v39\n"
        "v_mfma_f32_16x16x32_bf16 v[34:37], v[86:89], v[56:59], 0\n"
        "s_cbranch_scc1 L19_%=\n"
        "L20_%=:\n"
        "ds_read_b128 v[54:57], v0 offset:896\n"
        "v_cvt_pk_bf16_f32 v98, v82, v83\n"
        "v_cvt_pk_bf16_f32 v99, v84, v85\n"
        "v_cvt_pk_bf16_f32 v27, v40, v41\n"
        "v_mfma_f32_16x16x32_bf16 v[82:85], v[104:107], v[104:107], 0\n"
        "v_cvt_pk_bf16_f32 v30, v78, v79\n"
        "v_cvt_pk_bf16_f32 v31, v80, v81\n"
        "v_mfma_f32_16x16x32_bf16 v[82:85], v[108:111], v[108:111], v[82:85]\n"
        "v_cvt_pk_bf16_f32 v32, v34, v35\n"
        "v_cvt_pk_bf16_f32 v33, v36, v37\n"
        "v_mfma_f32_16x16x32_bf16 v[82:85], v[96:99], v[96:99], v[82:85]\n"
        "v_mfma_f32_16x16x32_bf16 v[76:79], v[20:23], v[20:23], 0\n"
        "v_mfma_f32_16x16x32_bf16 v[76:79], v[24:27], v[24:27], v[76:79]\n"
        "v_mfma_f32_16x16x32_bf16 v[76:79], v[30:33], v[30:33], v[76:79]\n"
        "s_cmp_lt_u32 s30, s14\n"
        "s_cbranch_scc1 PTOP_%=\n"
        "s_waitcnt lgkmcnt(0)\n"
        "s_nop 15\n"
        "L21_%=:\n"
        "ds_read_b32 v101, v102 offset:8\n"
        "s_waitcnt lgkmcnt(0)\n"
        "v_readfirstlane_b32 s40, v101\n"
        "s_nop 3\n"
        "s_cmp_ge_i32 s40, s68\n"
        "s_cbranch_scc1 L22_%=\n"
        "s_sleep 1\n"
        "s_sub_u32 s41, s41, 1\n"
        "s_cbranch_scc0 L21_%=\n"
        "s_endpgm\n"
        "L22_%=:\n"
        "v_cndmask_b32_e64 v101, v82, v83, s[48:49]\n"
        "v_cndmask_b32_e64 v93, v76, v77, s[48:49]\n"
        "v_cndmask_b32_e64 v101, v101, v84, s[50:51]\n"
        "v_cndmask_b32_e64 v93, v93, v78, s[50:51]\n"
        "v_cndmask_b32_e64 v101, v101, v85, s[52:53]\n"
        "v_cndmask_b32_e64 v93, v93, v79, s[52:53]\n"
        "ds_write_b128 v11, v[104:107]\n"
        "ds_write_b128 v11, v[20:23] offset:832\n"
        "v_cndmask_b32_dpp v101, v93, v101, vcc row_ror:4 row_mask:0xf bank_mask:0xf\n"
        "ds_write_b128 v11, v[108:111] offset:64\n"
        "ds_write_b128 v11, v[24:27] offset:896\n"
        "ds_write_b128 v11, v[96:99] offset:128\n"
        "ds_write_b128 v11, v[30:33] offset:960\n"
        "s_mov_b64 exec, s[46:47]\n"
        "ds_write_b32 v12, v101\n"
        "s_mov_b64 exec, -1\n"
        "s_mov_b64 exec, 2\n"
        "ds_inc_u32 v102, v0 offset:0\n"
        "s_mov_b64 exec, -1\n"
        "s_or_b32 s40, s18, s2\n"
        "s_cbranch_scc1 L31_%=\n"
        "L30_%=:\n"
        "s_mov_b64 exec, 1\n"
        "v_mov_b32 v20, 0\n"
        "v_mov_b32 v21, 1\n"
        "global_atomic_add v22, v20, v21, s[24:25] offset:256 sc0\n"
        "s_mov_b64 exec, -1\n"
        "s_mov_b64 exec, 1\n"
        "v_mov_b32 v20, 0\n"
        "v_mov_b32 v21, 0\n"
        "global_atomic_add v22, v20, v21, s[24:25] offset:0 sc0\n"
        "s_mov_b64 exec, -1\n"
        "s_waitcnt vmcnt(0)\n"
        "v_readfirstlane_b32 s40, v22\n"
        "s_nop 3\n"
        "s_cmp_ge_u32 s40, 512\n"
        "s_cbranch_scc1 L31_%=\n"
        "s_sleep 8\n"
        "s_sub_u32 s41, s41, 64\n"
        "s_cbranch_scc0 L30_%=\n"
        "L31_%=:\n"
        "s_endpgm\n"
        "L3_%=:\n"
        "s_nop 7\n"
        "L1_%=:\n"
        "ds_read_b32 v101, v102 offset:8\n"
        "s_waitcnt lgkmcnt(0)\n"
        "v_readfirstlane_b32 s40, v101\n"
        "s_nop 3\n"
        "s_cmp_ge_i32 s40, s68\n"
        "s_cbranch_scc1 L2_%=\n"
        "s_sleep 1\n"
        "s_sub_u32 s41, s41, 1\n"
        "s_cbranch_scc0 L1_%=\n"
        "s_endpgm\n"
        "L2_%=:\n"
        "s_nop 1\n"
        "s_branch L4_%=\n"
        "L8_%=:\n"
        "s_nop 7\n"
        "ds_read_u16 v11, v13 offset:19564\n"
        "ds_read_u16 v12, v13 offset:19566\n"
        "s_cmp_ge_u32 s30, 10\n"
        "s_cselect_b32 s69, 10, 0\n"
        "s_cmp_ge_u32 s30, 20\n"
        "s_cselect_b32 s69, 20, s69\n"
        "s_sub_u32 s69, s30, s69\n"
        "s_mul_i32 s69, s69, 832\n"
        "s_waitcnt lgkmcnt(0)\n"
        "v_add_u32 v11, s69, v11\n"
        "v_subrev_u32 v101, 8320, v11\n"
        "v_min_u32 v11, v11, v101\n"
        "v_add_u32 v11, 101856, v11\n"
        "v_add_u32 v12, s69, v12\n"
        "v_subrev_u32 v101, 8320, v12\n"
        "v_min_u32 v12, v12, v101\n"
        "v_add_u32 v12, 101856, v12\n"
        "s_mov_b32 s69, 21660\n"
        "v_writelane_b32 v102, s69, 1\n"
        "s_nop 1\n"
        "s_branch L9_%=\n"
        "L10_%=:\n"
        "s_nop 7\n"
        "v_mov_b32 v101, 21600\n"
        "L6_%=:\n"
        "ds_read_b32 v92, v101 offset:64\n"
        "s_waitcnt lgkmcnt(0)\n"
        "v_readfirstlane_b32 s40, v92\n"
        "s_nop 3\n"
        "s_cmp_ge_i32 s40, 1\n"
        "s_cbranch_scc1 L7_%=\n"
        "s_sleep 1\n"
        "s_sub_u32 s41, s41, 1\n"
        "s_cbranch_scc0 L6_%=\n"
        "s_endpgm\n"
        "L7_%=:\n"
        "v_mbcnt_lo_u32_b32 v92, -1, 0\n"
        "v_lshl_add_u32 v101, v92, 2, v101\n"
        "ds_read_b32 v92, v101 offset:68\n"
        "s_mov_b32 s14, s40\n"
        "s_mov_b32 s74, -1\n"
        "s_waitcnt lgkmcnt(0)\n"
        "v_readlane_b32 s22, v92, 0\n"
        "v_readlane_b32 s23, v92, 1\n"
        "s_nop 3\n"
        "s_nop 1\n"
        "s_branch L11_%=\n"
        "L12_%=:\n"
        "s_nop 7\n"
        "ds_read_b32 v15, v13 offset:19552\n"
        "ds_read_b32 v100, v13 offset:19556\n"
        "ds_read_b32 v103, v13 offset:19560\n"
        "s_mov_b32 s35, 6\n"
        "s_mov_b32 s38, 0\n"
        "s_mov_b32 s69, 1344\n"
        "s_waitcnt lgkmcnt(0)\n"
        "v_lshrrev_b32 v101, 28, v15\n"
        "v_add_u32 v101, s34, v101\n"
        "v_and_b32 v15, 0x0fffffff, v15\n"
        "v_mul_u32_u24 v92, 256, v101\n"
        "v_add_u32 v92, s16, v92\n"
        "v_cmpx_le_u32 vcc, 20, v101\n"
        "v_mov_b32 v92, s22\n"
        "s_mov_b64 exec, -1\n"
        "v_cmpx_le_u32 vcc, 21, v101\n"
        "v_mov_b32 v92, s23\n"
        "s_mov_b64 exec, -1\n"
        "v_mad_u32_u24 v15, v92, s69, v15\n"
        "v_mul_u32_u24 v92, 57457, v92\n"
        "v_lshrrev_b32 v92, 22, v92\n"
        "v_mul_u32_u24 v92, 588672, v92\n"
        "v_add_u32 v15, v15, v92\n"
        "v_lshrrev_b32 v101, 28, v100\n"
        "v_add_u32 v101, s34, v101\n"
        "v_and_b32 v100, 0x0fffffff, v100\n"
        "v_mul_u32_u24 v92, 256, v101\n"
        "v_add_u32 v92, s16, v92\n"
        "v_cmpx_le_u32 vcc, 20, v101\n"
        "v_mov_b32 v92, s22\n"
        "s_mov_b64 exec, -1\n"
        "v_cmpx_le_u32 vcc, 21, v101\n"
        "v_mov_b32 v92, s23\n"
        "s_mov_b64 exec, -1\n"
        "v_mad_u32_u24 v100, v92, s69, v100\n"
        "v_mul_u32_u24 v92, 57457, v92\n"
        "v_lshrrev_b32 v92, 22, v92\n"
        "v_mul_u32_u24 v92, 588672, v92\n"
        "v_add_u32 v100, v100, v92\n"
        "v_lshrrev_b32 v101, 28, v103\n"
        "v_add_u32 v101, s34, v101\n"
        "v_and_b32 v103, 0x0fffffff, v103\n"
        "v_mul_u32_u24 v92, 256, v101\n"
        "v_add_u32 v92, s16, v92\n"
        "v_cmpx_le_u32 vcc, 20, v101\n"
        "v_mov_b32 v92, s22\n"
        "s_mov_b64 exec, -1\n"
        "v_cmpx_le_u32 vcc, 21, v101\n"
        "v_mov_b32 v92, s23\n"
        "s_mov_b64 exec, -1\n"
        "v_mad_u32_u24 v103, v92, s69, v103\n"
        "v_mul_u32_u24 v92, 57457, v92\n"
        "v_lshrrev_b32 v92, 22, v92\n"
        "v_mul_u32_u24 v92, 588672, v92\n"
        "v_add_u32 v103, v103, v92\n"
        "s_nop 4\n"
        "s_mov_b64 vcc, s[44:45]\n"
        "s_nop 1\n"
        "s_branch L13_%=\n"
        "L19_%=:\n"
        "s_nop 7\n"
        "v_readfirstlane_b32 s62, v14\n"
        "s_cmp_eq_u32 s73, 1\n"
        "s_cbranch_scc1 L14_%=\n"
        "s_cmp_ge_u32 s73, 2\n"
        "s_cbranch_scc1 L15_%=\n"
        "s_mov_b32 s27, 20\n"
        "s_cmp_lg_u32 s2, 0\n"
        "s_cbranch_scc1 L18_%=\n"
        "s_not_b32 s26, s26\n"
        "s_mov_b64 exec, 1\n"
        "v_mov_b32 v101, 0\n"
        "v_mov_b32 v92, 1\n"
        "global_atomic_add v14, v101, v92, s[24:25] offset:256 sc0\n"
        "s_mov_b64 exec, -1\n"
        "s_branch L16_%=\n"
        "L18_%=:\n"
        "s_mov_b64 exec, 1\n"
        "v_mov_b32 v101, 0\n"
        "global_load_dword v14, v101, s[24:25] offset:256 sc1\n"
        "s_mov_b64 exec, -1\n"
        "s_branch L16_%=\n"
        "L14_%=:\n"
        "s_cmp_lg_u32 s62, s26\n"
        "s_cbranch_scc1 L17_%=\n"
        "s_sleep 8\n"
        "s_mov_b64 exec, 1\n"
        "v_mov_b32 v101, 0\n"
        "global_load_dword v14, v101, s[24:25] offset:256 sc1\n"
        "s_mov_b64 exec, -1\n"
        "s_waitcnt vmcnt(0)\n"
        "v_readfirstlane_b32 s62, v14\n"
        "s_sub_u32 s41, s41, 64\n"
        "s_cbranch_scc0 L14_%=\n"
        "s_endpgm\n"
        "L15_%=:\n"
        "s_cmp_lt_u32 s62, 209\n"
        "s_addc_u32 s27, s27, 0\n"
        "s_min_u32 s62, s62, 208\n"
        "s_add_u32 s62, s62, 5120\n"
        "s_lshl2_add_u32 s64, s73, 21600\n"
        "v_mov_b32 v101, s64\n"
        "v_mov_b32 v92, s62\n"
        "ds_write_b32 v101, v92 offset:60\n"
        "s_waitcnt lgkmcnt(0)\n"
        "s_cmp_lt_u32 s73, 3\n"
        "s_cbranch_scc1 L17_%=\n"
        "v_mov_b32 v92, s27\n"
        "ds_write_b32 v101, v92 offset:52\n"
        "s_waitcnt lgkmcnt(0)\n"
        "s_branch L16_%=\n"
        "L17_%=:\n"
        "s_mov_b64 exec, 1\n"
        "v_mov_b32 v101, 0\n"
        "v_mov_b32 v92, 1\n"
        "global_atomic_add v14, v101, v92, s[24:25] offset:0 sc0\n"
        "s_mov_b64 exec, -1\n"
        "L16_%=:\n"
        "s_nop 1\n"
        "s_branch L20_%=\n"
        "F8_%=:\n"
        "s_nop 7\n"
        "ds_read_u16 v11, v13 offset:19564\n"
        "ds_read_u16 v12, v13 offset:19566\n"
        "s_cmp_ge_u32 s30, 10\n"
        "s_cselect_b32 s69, 10, 0\n"
        "s_cmp_ge_u32 s30, 20\n"
        "s_cselect_b32 s69, 20, s69\n"
        "s_sub_u32 s69, s30, s69\n"
        "s_mul_i32 s69, s69, 832\n"
        "s_waitcnt lgkmcnt(0)\n"
        "v_add_u32 v11, s69, v11\n"
        "v_subrev_u32 v101, 8320, v11\n"
        "v_min_u32 v11, v11, v101\n"
        "v_add_u32 v11, 101856, v11\n"
        "v_add_u32 v12, s69, v12\n"
        "v_subrev_u32 v101, 8320, v12\n"
        "v_min_u32 v12, v12, v101\n"
        "v_add_u32 v12, 101856, v12\n"
        "s_mov_b32 s69, 21660\n"
        "v_writelane_b32 v102, s69, 1\n"
        "s_nop 1\n"
        "s_branch F9_%=\n"
        "F10_%=:\n"
        "s_nop 7\n"
        "v_mov_b32 v101, 21600\n"
        "F6_%=:\n"
        "ds_read_b32 v92, v101 offset:64\n"
        "s_waitcnt lgkmcnt(0)\n"
        "v_readfirstlane_b32 s40, v92\n"
        "s_nop 3\n"
        "s_cmp_ge_i32 s40, 1\n"
        "s_cbranch_scc1 F7_%=\n"
        "s_sleep 1\n"
        "s_sub_u32 s41, s41, 1\n"
        "s_cbranch_scc0 F6_%=\n"
        "s_endpgm\n"
        "F7_%=:\n"
        "v_mbcnt_lo_u32_b32 v92, -1, 0\n"
        "v_lshl_add_u32 v101, v92, 2, v101\n"
        "ds_read_b32 v92, v101 offset:68\n"
        "s_mov_b32 s14, s40\n"
        "s_mov_b32 s74, -1\n"
        "s_waitcnt lgkmcnt(0)\n"
        "v_readlane_b32 s22, v92, 0\n"
        "v_readlane_b32 s23, v92, 1\n"
        "s_nop 3\n"
        "s_nop 1\n"
        "s_branch F11_%=\n"
        "F12_%=:\n"
        "s_nop 7\n"
        "ds_read_b32 v15, v13 offset:19552\n"
        "ds_read_b32 v100, v13 offset:19556\n"
        "ds_read_b32 v103, v13 offset:19560\n"
        "s_mov_b32 s35, 6\n"
        "s_mov_b32 s38, 0\n"
        "s_mov_b32 s69, 1344\n"
        "s_waitcnt lgkmcnt(0)\n"
        "v_lshrrev_b32 v101, 28, v15\n"
        "v_add_u32 v101, s34, v101\n"
        "v_and_b32 v15, 0x0fffffff, v15\n"
        "v_mul_u32_u24 v92, 256, v101\n"
        "v_add_u32 v92, s16, v92\n"
        "v_cmpx_le_u32 vcc, 20, v101\n"
        "v_mov_b32 v92, s22\n"
        "s_mov_b64 exec, -1\n"
        "v_cmpx_le_u32 vcc, 21, v101\n"
        "v_mov_b32 v92, s23\n"
        "s_mov_b64 exec, -1\n"
        "v_mad_u32_u24 v15, v92, s69, v15\n"
        "v_mul_u32_u24 v92, 57457, v92\n"
        "v_lshrrev_b32 v92, 22, v92\n"
        "v_mul_u32_u24 v92, 588672, v92\n"
        "v_add_u32 v15, v15, v92\n"
        "v_lshrrev_b32 v101, 28, v100\n"
        "v_add_u32 v101, s34, v101\n"
        "v_and_b32 v100, 0x0fffffff, v100\n"
        "v_mul_u32_u24 v92, 256, v101\n"
        "v_add_u32 v92, s16, v92\n"
        "v_cmpx_le_u32 vcc, 20, v101\n"
        "v_mov_b32 v92, s22\n"
        "s_mov_b64 exec, -1\n"
        "v_cmpx_le_u32 vcc, 21, v101\n"
        "v_mov_b32 v92, s23\n"
        "s_mov_b64 exec, -1\n"
        "v_mad_u32_u24 v100, v92, s69, v100\n"
        "v_mul_u32_u24 v92, 57457, v92\n"
        "v_lshrrev_b32 v92, 22, v92\n"
        "v_mul_u32_u24 v92, 588672, v92\n"
        "v_add_u32 v100, v100, v92\n"
        "v_lshrrev_b32 v101, 28, v103\n"
        "v_add_u32 v101, s34, v101\n"
        "v_and_b32 v103, 0x0fffffff, v103\n"
        "v_mul_u32_u24 v92, 256, v101\n"
        "v_add_u32 v92, s16, v92\n"
        "v_cmpx_le_u32 vcc, 20, v101\n"
        "v_mov_b32 v92, s22\n"
        "s_mov_b64 exec, -1\n"
        "v_cmpx_le_u32 vcc, 21, v101\n"
        "v_mov_b32 v92, s23\n"
        "s_mov_b64 exec, -1\n"
        "v_mad_u32_u24 v103, v92, s69, v103\n"
        "v_mul_u32_u24 v92, 57457, v92\n"
        "v_lshrrev_b32 v92, 22, v92\n"
        "v_mul_u32_u24 v92, 588672, v92\n"
        "v_add_u32 v103, v103, v92\n"
        "s_nop 4\n"
        "s_mov_b64 vcc, s[44:45]\n"
        "s_nop 1\n"
        "s_branch F13_%=\n"
        "F19_%=:\n"
        "s_nop 7\n"
        "v_readfirstlane_b32 s62, v14\n"
        "s_cmp_eq_u32 s73, 1\n"
        "s_cbranch_scc1 F14_%=\n"
        "s_cmp_ge_u32 s73, 2\n"
        "s_cbranch_scc1 F15_%=\n"
        "s_mov_b32 s27, 20\n"
        "s_cmp_lg_u32 s2, 0\n"
        "s_cbranch_scc1 F18_%=\n"
        "s_not_b32 s26, s26\n"
        "s_mov_b64 exec, 1\n"
        "v_mov_b32 v101, 0\n"
        "v_mov_b32 v92, 1\n"
        "global_atomic_add v14, v101, v92, s[24:25] offset:256 sc0\n"
        "s_mov_b64 exec, -1\n"
        "s_branch F16_%=\n"
        "F18_%=:\n"
        "s_mov_b64 exec, 1\n"
        "v_mov_b32 v101, 0\n"
        "global_load_dword v14, v101, s[24:25] offset:256 sc1\n"
        "s_mov_b64 exec, -1\n"
        "s_branch F16_%=\n"
        "F14_%=:\n"
        "s_cmp_lg_u32 s62, s26\n"
        "s_cbranch_scc1 F17_%=\n"
        "s_sleep 8\n"
        "s_mov_b64 exec, 1\n"
        "v_mov_b32 v101, 0\n"
        "global_load_dword v14, v101, s[24:25] offset:256 sc1\n"
        "s_mov_b64 exec, -1\n"
        "s_waitcnt vmcnt(0)\n"
        "v_readfirstlane_b32 s62, v14\n"
        "s_sub_u32 s41, s41, 64\n"
        "s_cbranch_scc0 F14_%=\n"
        "s_endpgm\n"
        "F15_%=:\n"
        "s_cmp_lt_u32 s62, 209\n"
        "s_addc_u32 s27, s27, 0\n"
        "s_min_u32 s62, s62, 208\n"
        "s_add_u32 s62, s62, 5120\n"
        "s_lshl2_add_u32 s64, s73, 21600\n"
        "v_mov_b32 v101, s64\n"
        "v_mov_b32 v92, s62\n"
        "ds_write_b32 v101, v92 offset:60\n"
        "s_waitcnt lgkmcnt(0)\n"
        "s_cmp_lt_u32 s73, 3\n"
        "s_cbranch_scc1 F17_%=\n"
        "v_mov_b32 v92, s27\n"
        "ds_write_b32 v101, v92 offset:52\n"
        "s_waitcnt lgkmcnt(0)\n"
        "s_branch F16_%=\n"
        "F17_%=:\n"
        "s_mov_b64 exec, 1\n"
        "v_mov_b32 v101, 0\n"
        "v_mov_b32 v92, 1\n"
        "global_atomic_add v14, v101, v92, s[24:25] offset:0 sc0\n"
        "s_mov_b64 exec, -1\n"
        "F16_%=:\n"
        "s_nop 1\n"
        "s_branch F20_%=\n"
        "CONS_%=:\n"
        "s_mov_b32 s41, 0x400000\n"
        "s_mov_b32 s48, 0xffffffff\n"
        "s_mov_b32 s49, 0xffff0000\n"
        "s_mov_b32 s50, 0x33333333\n"
        "s_mov_b32 s51, 0x33333333\n"
        "s_mov_b32 s52, 0x3c2aaaab\n"
        "s_mov_b32 s53, 0x3a7ba882\n"
        "s_mov_b32 s56, 0x0\n"
        "s_mov_b32 s57, 0xfeff0000\n"
        "s_mov_b32 s62, 0x11111111\n"
        "s_mov_b32 s63, 0x11111111\n"
        "s_mov_b64 s[44:45], s[12:13]\n"
        "s_and_b32 s45, s45, 0xffff\n"
        "s_mov_b32 s47, 0x20000\n"
        "s_mov_b32 s70, 832\n"
        "s_mov_b32 s71, 17\n"
        "s_sub_u32 s69, s18, 7488\n"
        "s_mov_b32 s28, 0xffff0000\n"
        "s_mov_b32 s31, 0\n"
        "s_add_u32 s35, s15, s17\n"
        "s_mov_b32 s36, 0x20000000\n"
        "L58_%=:\n"
        "ds_read_b32 v251, v250 offset:52\n"
        "s_waitcnt lgkmcnt(0)\n"
        "v_readfirstlane_b32 s40, v251\n"
        "s_nop 3\n"
        "s_cmp_ge_i32 s40, 6\n"
        "s_cbranch_scc1 L59_%=\n"
        "s_nop 0\n"
        "s_sub_u32 s41, s41, 1\n"
        "s_cbranch_scc0 L58_%=\n"
        "s_endpgm\n"
        "L59_%=:\n"
        "s_waitcnt vmcnt(0)\n"
        "s_setprio 2\n"
        "v_add_u32 v24, 2, v1\n"
        "v_mov_b32 v25, 0x3f80\n"
        "s_mov_b32 exec_lo, 0\n"
        "s_mov_b32 exec_hi, 0x1000000\n"
        "ds_write_b16 v24, v25\n"
        "s_mov_b64 exec, -1\n"
        "v_add_u32 v24, 61792, v0\n"
        "ds_read_b128 v[168:171], v24 offset:0\n"
        "ds_read_b128 v[172:175], v24 offset:1024\n"
        "ds_read_b128 v[176:179], v24 offset:2048\n"
        "ds_read_b128 v[180:183], v24 offset:3072\n"
        "ds_read_b128 v[184:187], v24 offset:4096\n"
        "ds_read_b128 v[188:191], v24 offset:5120\n"
        "ds_read_b128 v[192:195], v24 offset:6144\n"
        "ds_read_b128 v[196:199], v24 offset:7168\n"
        "ds_read_b128 v[200:203], v24 offset:8192\n"
        "ds_read_b128 v[204:207], v24 offset:9216\n"
        "ds_read_b128 v[208:211], v24 offset:10240\n"
        "ds_read_b128 v[212:215], v24 offset:11264\n"
        "ds_read_b128 v[216:219], v24 offset:12288\n"
        "ds_read_b128 v[220:223], v24 offset:13312\n"
        "ds_read_b128 v[224:227], v24 offset:14336\n"
        "ds_read_b128 v[228:231], v24 offset:15360\n"
        "ds_read_b128 v[232:235], v24 offset:16384\n"
        "ds_read_b128 v[236:239], v24 offset:17408\n"
        "ds_read_b128 v[240:243], v0 offset:0\n"
        "ds_read_b128 v[244:247], v0 offset:1024\n"
        "ds_read_b128 v[248:251], v0 offset:2048\n"
        "ds_read_b128 v[252:255], v0 offset:3088\n"
        "v_mov_b32 v25, 21600\n"
        "s_waitcnt lgkmcnt(0)\n"
        "s_mov_b64 exec, 1\n"
        "ds_inc_u32 v25, v1 offset:12\n"
        "s_mov_b64 exec, -1\n"
        "s_call_b64 s[64:65], CGRQ_%=\n"
        "s_setprio 0\n"
        "L60_%=:\n"
        "ds_read_b32 v24, v0 offset:21600\n"
        "s_waitcnt lgkmcnt(0)\n"
        "v_readfirstlane_b32 s40, v24\n"
        "s_nop 3\n"
        "s_cmp_ge_i32 s40, 6\n"
        "s_cbranch_scc1 L61_%=\n"
        "s_nop 0\n"
        "s_sub_u32 s41, s41, 1\n"
        "s_cbranch_scc0 L60_%=\n"
        "s_endpgm\n"
        "L61_%=:\n"
        "ds_read_b128 v[124:127], v7 offset:0\n"
        "ds_read_b128 v[132:135], v7 offset:3328\n"
        "ds_read_b128 v[128:131], v7 offset:64\n"
        "ds_read_b128 v[136:139], v7 offset:3392\n"
        "ds_read_b128 v[160:163], v7 offset:128\n"
        "ds_read_b128 v[52:55], v7 offset:3456\n"
        "s_waitcnt lgkmcnt(0)\n"
        "s_branch CTOP_%=\n"
        ".p2align 6\n"
        "s_nop 0\n"
        "s_nop 0\n"
        "s_nop 0\n"
        "s_nop 0\n"
        "s_nop 0\n"
        "s_nop 0\n"
        "s_nop 0\n"
        "s_nop 0\n"
        "s_nop 0\n"
        "s_nop 0\n"
        "s_nop 0\n"
        "s_nop 0\n"
        "s_nop 0\n"
        "s_nop 0\n"
        "s_nop 0\n"
        "CFULL_%=:\n"
        "s_call_b64 s[64:65], CGRP_%=\n"
        "CTOP_%=:\n"
        "s_waitcnt lgkmcnt(5)\n"
        "v_mfma_f32_16x16x32_bf16 v[24:27], v[168:171], v[124:127], 0\n"
        "v_cvt_pk_bf16_f32 v156, v152, v56\n"
        "v_cvt_pk_bf16_f32 v157, v72, v20\n"
        "s_waitcnt lgkmcnt(3)\n"
        "v_mfma_f32_16x16x32_bf16 v[24:27], v[172:175], v[128:131], v[24:27]\n"
        "ds_read_b128 v[44:47], v16 offset:128\n"
        "v_cvt_pk_bf16_f32 v158, v84, v60\n"
        "s_waitcnt lgkmcnt(2)\n"
        "v_mfma_f32_16x16x32_bf16 v[24:27], v[176:179], v[160:163], v[24:27]\n"
        "ds_read_b128 v[48:51], v5\n"
        "v_cvt_pk_bf16_f32 v164, v153, v57\n"
        "v_mfma_f32_16x16x32_bf16 v[32:35], v[188:191], v[160:163], 0\n"
        "v_cvt_pk_bf16_f32 v165, v73, v21\n"
        "v_cvt_pk_bf16_f32 v166, v85, v61\n"
        "v_mfma_f32_16x16x32_bf16 v[32:35], v[184:187], v[128:131], v[32:35]\n"
        "v_cvt_pk_bf16_f32 v146, v154, v58\n"
        "v_cvt_pk_bf16_f32 v147, v74, v22\n"
        "v_mfma_f32_16x16x32_bf16 v[32:35], v[180:183], v[124:127], v[32:35]\n"
        "s_add_u32 s37, s36, 196224\n"
        "ds_read_b128 v[92:95], v7 offset:6656\n"
        "ds_read_b128 v[96:99], v7 offset:6720\n"
        "ds_read_b128 v[116:119], v7 offset:6784\n"
        "v_mfma_f32_16x16x32_bf16 v[76:79], v[192:195], v[124:127], 0\n"
        "v_cvt_pk_bf16_f32 v80, v24, v25\n"
        "v_cvt_pk_bf16_f32 v81, v26, v27\n"
        "v_mfma_f32_16x16x32_bf16 v[76:79], v[196:199], v[128:131], v[76:79]\n"
        "v_cvt_pk_bf16_f32 v82, v32, v33\n"
        "v_cvt_pk_bf16_f32 v83, v34, v35\n"
        "v_mfma_f32_16x16x32_bf16 v[76:79], v[200:203], v[160:163], v[76:79]\n"
        "buffer_store_dwordx3 v[156:158], v12, s[8:11], s37 offen offset:384 sc0 sc1 nt\n"
        "v_cvt_pk_bf16_f32 v148, v86, v62\n"
        "v_cvt_pk_bf16_f32 v150, v155, v59\n"
        "v_cvt_pk_bf16_f32 v151, v75, v23\n"
        "v_cvt_pk_bf16_f32 v152, v87, v63\n"
        "s_waitcnt lgkmcnt(3)\n"
        "v_mfma_f32_16x16x32_bf16 v[32:35], v[44:47], v[160:163], 0\n"
        "v_cvt_pk_bf16_f32 v72, v68, v36\n"
        "v_cvt_pk_bf16_f32 v84, v76, v77\n"
        "v_smfmac_f32_16x16x64_bf16 v[32:35], v[48:51], v[124:131], v4\n"
        "s_add_u32 s37, s36, 196224\n"
        "v_cvt_pk_bf16_f32 v85, v78, v79\n"
        "v_mfma_f32_16x16x32_bf16 v[24:27], v[44:47], v[52:55], 0\n"
        "v_cvt_pk_bf16_f32 v73, v40, v64\n"
        "v_cvt_pk_bf16_f32 v74, v104, v88\n"
        "v_smfmac_f32_16x16x64_bf16 v[24:27], v[48:51], v[132:139], v4\n"
        "s_waitcnt lgkmcnt(0)\n"
        "s_setprio 2\n"
        "v_cvt_pk_bf16_f32 v156, v69, v37\n"
        "v_cvt_pk_bf16_f32 v157, v41, v65\n"
        "v_mfma_f32_16x16x32_bf16 v[100:103], v[44:47], v[116:119], 0\n"
        "v_cvt_pk_bf16_f32 v158, v105, v89\n"
        "v_cvt_pk_bf16_f32 v76, v70, v38\n"
        "v_smfmac_f32_16x16x64_bf16 v[100:103], v[48:51], v[92:99], v4\n"
        "buffer_store_dwordx3 v[164:166], v13, s[8:11], s37 offen offset:1152 sc0 sc1 nt\n"
        "v_cvt_pk_bf16_f32 v77, v42, v66\n"
        "v_cvt_pk_bf16_f32 v78, v106, v90\n"
        "v_cvt_pk_bf16_f32 v88, v71, v39\n"
        "v_cvt_pk_bf16_f32 v89, v43, v67\n"
        "v_cvt_pk_bf16_f32 v90, v107, v91\n"
        "v_mfma_f32_16x16x32_bf16 v[20:23], v[212:215], v[160:163], 0\n"
        "s_add_u32 s37, s36, 294336\n"
        "ds_read_b64 v[154:155], v11 offset:192\n"
        "v_mfma_f32_16x16x32_bf16 v[20:23], v[208:211], v[128:131], v[20:23]\n"
        "ds_read_b32 v153, v10\n"
        "v_mfma_f32_16x16x32_bf16 v[20:23], v[204:207], v[124:127], v[20:23]\n"
        "v_mfma_f32_16x16x32_bf16 v[28:31], v[216:219], v[124:127], 0\n"
        "v_mfma_f32_16x16x32_bf16 v[28:31], v[220:223], v[128:131], v[28:31]\n"
        "v_mfma_f32_16x16x32_bf16 v[28:31], v[224:227], v[160:163], v[28:31]\n"
        "buffer_store_dwordx3 v[146:148], v12, s[8:11], s37 offen offset:576 sc0 sc1 nt\n"
        "s_add_u32 s37, s36, 392448\n"
        "s_waitcnt lgkmcnt(1)\n"
        "v_add_f32_dpp v155, v34, v35 quad_perm:[0,1,2,3] row_mask:0x1 bank_mask:0xf\n"
        "v_add_f32_dpp v154, v32, v33 quad_perm:[0,1,2,3] row_mask:0x1 bank_mask:0xf\n"
        "v_cvt_pk_bf16_f32 v86, v20, v21\n"
        "v_mfma_f32_16x16x32_bf16 v[44:47], v[236:239], v[160:163], 0\n"
        "v_cvt_pk_bf16_f32 v87, v22, v23\n"
        "v_cvt_pk_bf16_f32 v20, v28, v29\n"
        "v_mfma_f32_16x16x32_bf16 v[44:47], v[232:235], v[128:131], v[44:47]\n"
        "v_cvt_pk_bf16_f32 v21, v30, v31\n"
        "v_add_f32_dpp v155, v26, v27 quad_perm:[0,1,2,3] row_mask:0x2 bank_mask:0xf\n"
        "v_mfma_f32_16x16x32_bf16 v[44:47], v[228:231], v[124:127], v[44:47]\n"
        "buffer_store_dwordx3 v[150:152], v12, s[8:11], s37 offen sc0 sc1 nt\n"
        "v_add_f32_dpp v154, v24, v25 quad_perm:[0,1,2,3] row_mask:0x2 bank_mask:0xf\n"
        "v_add_f32_dpp v155, v102, v103 quad_perm:[0,1,2,3] row_mask:0x4 bank_mask:0xf\n"
        "v_mul_f32 v165, s52, v155\n"
        "v_add_f32_dpp v154, v100, v101 quad_perm:[0,1,2,3] row_mask:0x4 bank_mask:0xf\n"
        "s_waitcnt lgkmcnt(0)\n"
        "v_fma_f32 v165, -v165, v155, v153\n"
        "v_add_f32_e64 v165, s53, |v165|\n"
        "v_cvt_pk_bf16_f32 v23, v46, v47\n"
        "v_cvt_pk_bf16_f32 v22, v44, v45\n"
        "v_mfma_f32_16x16x32_bf16 v[100:103], v[124:127], v[80:83], 0\n"
        "v_rsq_f32 v153, v165\n"
        "v_mfma_f32_16x16x32_bf16 v[100:103], v[128:131], v[84:87], v[100:103]\n"
        "s_add_u32 s37, s36, 392448\n"
        "ds_read_b128 v[104:107], v8 offset:128\n"
        "v_and_b32 v56, s28, v153\n"
        "v_mfma_f32_16x16x32_bf16 v[100:103], v[160:163], v[20:23], v[100:103]\n"
        "ds_read2_b32 v[108:109], v6 offset0:0 offset1:2\n"
        "ds_read2_b32 v[110:111], v6 offset0:8 offset1:10\n"
        "v_mfma_f32_16x16x32_bf16 v[144:147], v[52:55], v[20:23], 0\n"
        "v_and_b32 v164, s28, v153\n"
        "v_sub_f32 v167, v153, v56\n"
        "v_mfma_f32_16x16x32_bf16 v[144:147], v[136:139], v[84:87], v[144:147]\n"
        "v_cvt_pk_bf16_f32 v57, v167, v56\n"
        "v_cvt_pk_bf16_f32 v165, v164, v167\n"
        "v_mfma_f32_16x16x32_bf16 v[144:147], v[132:135], v[80:83], v[144:147]\n"
        "buffer_store_dwordx3 v[72:74], v15, s[8:11], s37 offen offset:768 sc0 sc1 nt\n"
        "v_mul_f32 v154, v154, v153\n"
        "v_readlane_b32 s58, v153, 56\n"
        "s_mov_b64 exec, s[56:57]\n"
        "v_mov_b32 v154, 0xff800000\n"
        "s_mov_b64 exec, -1\n"
        "v_mfma_f32_16x16x4_4b_bf16 v[32:47], v[56:57], v[164:165], 0 blgp:4\n"
        "v_mul_f32 v154, s58, v154\n"
        "v_exp_f32 v154, v154\n"
        "v_mfma_f32_16x16x32_bf16 v[24:27], v[92:95], v[80:83], 0\n"
        "s_add_u32 s37, s36, 490560\n"
        "v_add_f32_dpp v167, v154, v154 row_ror:8 row_mask:0xf bank_mask:0xf\n"
        "v_mul_f32 v154, v154, v153\n"
        "v_mfma_f32_16x16x32_bf16 v[24:27], v[96:99], v[84:87], v[24:27]\n"
        "v_add_f32_dpp v167, v167, v167 row_ror:4 row_mask:0xf bank_mask:0xf\n"
        "v_mfma_f32_16x16x32_bf16 v[24:27], v[116:119], v[20:23], v[24:27]\n"
        "s_waitcnt lgkmcnt(0)\n"
        "v_add_f32_dpp v167, v167, v167 row_ror:2 row_mask:0xf bank_mask:0xf\n"
        "v_mfma_f32_16x16x32_bf16 v[148:151], v[104:107], v[20:23], 0\n"
        "v_mul_f32 v100, v100, v32\n"
        "v_mul_f32 v101, v101, v33\n"
        "v_smfmac_f32_16x16x64_bf16 v[148:151], v[108:111], v[80:87], v4\n"
        "buffer_store_dwordx3 v[156:158], v12, s[8:11], s37 offen offset:192 sc0 sc1 nt\n"
        "v_mul_f32 v102, v102, v34\n"
        "v_mul_f32 v103, v103, v35\n"
        "v_exp_f32 v100, v100\n"
        "v_mul_f32 v144, v144, v36\n"
        "v_mfma_f32_16x16x32_bf16 v[20:23], v[168:171], v[132:135], 0\n"
        "s_add_u32 s37, s36, 490560\n"
        "v_exp_f32 v101, v101\n"
        "v_mfma_f32_16x16x32_bf16 v[20:23], v[172:175], v[136:139], v[20:23]\n"
        "v_exp_f32 v102, v102\n"
        "v_mfma_f32_16x16x32_bf16 v[20:23], v[176:179], v[52:55], v[20:23]\n"
        "v_exp_f32 v103, v103\n"
        "v_add_f32 v151, v100, v101\n"
        "v_mfma_f32_16x16x32_bf16 v[28:31], v[188:191], v[52:55], 0\n"
        "v_add_f32 v151, v151, v102\n"
        "v_add_f32 v151, v151, v103\n"
        "v_mfma_f32_16x16x32_bf16 v[28:31], v[184:187], v[136:139], v[28:31]\n"
        "v_mul_f32 v145, v145, v37\n"
        "v_mul_f32 v146, v146, v38\n"
        "v_mfma_f32_16x16x32_bf16 v[28:31], v[180:183], v[132:135], v[28:31]\n"
        "buffer_store_dwordx3 v[76:78], v14, s[8:11], s37 offen offset:960 sc0 sc1 nt\n"
        "v_cvt_pk_bf16_f32 v68, v20, v21\n"
        "v_cvt_pk_bf16_f32 v69, v22, v23\n"
        "v_mul_f32 v147, v147, v39\n"
        "v_exp_f32 v144, v144\n"
        "v_mfma_f32_16x16x32_bf16 v[20:23], v[192:195], v[132:135], 0\n"
        "s_add_u32 s37, s36, 588672\n"
        "v_exp_f32 v145, v145\n"
        "v_mfma_f32_16x16x32_bf16 v[20:23], v[196:199], v[136:139], v[20:23]\n"
        "v_cvt_pk_bf16_f32 v70, v28, v29\n"
        "v_cvt_pk_bf16_f32 v71, v30, v31\n"
        "v_mfma_f32_16x16x32_bf16 v[20:23], v[200:203], v[52:55], v[20:23]\n"
        "buffer_store_dwordx3 v[88:90], v12, s[8:11], s37 offen offset:384 sc0 sc1 nt\n"
        "v_exp_f32 v146, v146\n"
        "v_exp_f32 v147, v147\n"
        "v_add_f32 v151, v151, v144\n"
        "v_add_f32 v151, v151, v145\n"
        "v_mfma_f32_16x16x32_bf16 v[28:31], v[212:215], v[52:55], 0\n"
        "v_add_f32 v151, v151, v146\n"
        "v_add_f32 v151, v151, v147\n"
        "v_mfma_f32_16x16x32_bf16 v[28:31], v[208:211], v[136:139], v[28:31]\n"
        "v_cvt_pk_bf16_f32 v72, v20, v21\n"
        "v_cvt_pk_bf16_f32 v73, v22, v23\n"
        "v_mfma_f32_16x16x32_bf16 v[28:31], v[204:207], v[132:135], v[28:31]\n"
        "v_mul_f32 v24, v24, v40\n"
        "v_mul_f32 v25, v25, v41\n"
        "v_mul_f32 v26, v26, v42\n"
        "v_mfma_f32_16x16x32_bf16 v[20:23], v[216:219], v[132:135], 0\n"
        "v_mul_f32 v27, v27, v43\n"
        "v_add_f32 v148, v148, v149\n"
        "v_mfma_f32_16x16x32_bf16 v[20:23], v[220:223], v[136:139], v[20:23]\n"
        "v_exp_f32 v24, v24\n"
        "v_mfma_f32_16x16x32_bf16 v[20:23], v[224:227], v[52:55], v[20:23]\n"
        "v_cvt_pk_bf16_f32 v74, v28, v29\n"
        "v_cvt_pk_bf16_f32 v75, v30, v31\n"
        "v_add_f32 v151, v151, v24\n"
        "v_mfma_f32_16x16x32_bf16 v[64:67], v[236:239], v[52:55], 0\n"
        "v_exp_f32 v25, v25\n"
        "v_mfma_f32_16x16x32_bf16 v[64:67], v[232:235], v[136:139], v[64:67]\n"
        "v_exp_f32 v26, v26\n"
        "v_mfma_f32_16x16x32_bf16 v[64:67], v[228:231], v[132:135], v[64:67]\n"
        "v_cvt_pk_bf16_f32 v48, v20, v21\n"
        "v_cvt_pk_bf16_f32 v49, v22, v23\n"
        "v_mfma_f32_16x16x4_4b_bf16 v[28:43], v[56:57], v[164:165], 0 blgp:5\n"
        "v_exp_f32 v27, v27\n"
        "v_add_f32 v151, v151, v25\n"
        "v_add_f32 v151, v151, v26\n"
        "v_mul_f32 v148, v148, v44\n"
        "v_exp_f32 v148, v148\n"
        "v_cvt_pk_bf16_f32 v50, v64, v65\n"
        "v_cvt_pk_bf16_f32 v51, v66, v67\n"
        "v_mfma_f32_16x16x32_bf16 v[58:61], v[124:127], v[68:71], 0\n"
        "v_add_f32 v151, v151, v27\n"
        "v_add_f32_dpp v167, v167, v167 row_ror:1 row_mask:0xf bank_mask:0xf\n"
        "v_mfma_f32_16x16x32_bf16 v[58:61], v[128:131], v[72:75], v[58:61]\n"
        "v_add_f32_dpp v151, v148, v151 quad_perm:[0,1,2,3] row_mask:0x4 bank_mask:0xf\n"
        "v_mfma_f32_16x16x32_bf16 v[58:61], v[160:163], v[48:51], v[58:61]\n"
        "v_mfma_f32_16x16x32_bf16 v[112:115], v[52:55], v[48:51], 0\n"
        "v_permlane32_swap_b32 v151, v167\n"
        "v_mfma_f32_16x16x32_bf16 v[112:115], v[136:139], v[72:75], v[112:115]\n"
        "v_add_f32 v151, v151, v167\n"
        "v_mov_b32 v167, v151\n"
        "v_mfma_f32_16x16x32_bf16 v[112:115], v[132:135], v[68:71], v[112:115]\n"
        "v_mfma_f32_16x16x32_bf16 v[20:23], v[92:95], v[68:71], 0\n"
        "v_permlane16_swap_b32 v151, v167\n"
        "v_mfma_f32_16x16x32_bf16 v[20:23], v[96:99], v[72:75], v[20:23]\n"
        "v_add_f32 v151, v151, v167\n"
        "v_mul_f32 v58, v58, v28\n"
        "v_mfma_f32_16x16x32_bf16 v[20:23], v[116:119], v[48:51], v[20:23]\n"
        "v_rcp_f32 v123, v151\n"
        "v_mul_f32 v59, v59, v29\n"
        "v_and_b32 v158, s28, v123\n"
        "v_mfma_f32_16x16x32_bf16 v[62:65], v[104:107], v[48:51], 0\n"
        "v_sub_f32 v152, v123, v158\n"
        "v_cvt_pk_bf16_f32 v159, v158, v152\n"
        "v_smfmac_f32_16x16x64_bf16 v[62:65], v[108:111], v[68:75], v4\n"
        "v_min3_f32 v167, v151, v123, v153\n"
        "v_mfma_f32_16x16x32_bf16 v[48:51], v[168:171], v[92:95], 0\n"
        "v_mul_f32 v60, v60, v30\n"
        "v_mul_f32 v61, v61, v31\n"
        "v_mfma_f32_16x16x32_bf16 v[48:51], v[172:175], v[96:99], v[48:51]\n"
        "v_exp_f32 v58, v58\n"
        "v_mfma_f32_16x16x32_bf16 v[48:51], v[176:179], v[116:119], v[48:51]\n"
        "v_exp_f32 v59, v59\n"
        "v_exp_f32 v60, v60\n"
        "v_mfma_f32_16x16x32_bf16 v[68:71], v[188:191], v[116:119], 0\n"
        "v_exp_f32 v61, v61\n"
        "v_mfma_f32_16x16x32_bf16 v[68:71], v[184:187], v[96:99], v[68:71]\n"
        "v_add_f32 v155, v58, v59\n"
        "v_add_f32 v155, v155, v60\n"
        "v_mfma_f32_16x16x32_bf16 v[68:71], v[180:183], v[92:95], v[68:71]\n"
        "v_cvt_pk_bf16_f32 v44, v48, v49\n"
        "v_cvt_pk_bf16_f32 v45, v50, v51\n"
        "v_add_f32 v155, v155, v61\n"
        "v_mfma_f32_16x16x32_bf16 v[28:31], v[192:195], v[92:95], 0\n"
        "v_mul_f32 v112, v112, v32\n"
        "v_mul_f32 v113, v113, v33\n"
        "v_mfma_f32_16x16x32_bf16 v[28:31], v[196:199], v[96:99], v[28:31]\n"
        "v_mul_f32 v114, v114, v34\n"
        "v_cvt_pk_bf16_f32 v46, v68, v69\n"
        "v_mfma_f32_16x16x32_bf16 v[28:31], v[200:203], v[116:119], v[28:31]\n"
        "v_cvt_pk_bf16_f32 v47, v70, v71\n"
        "v_mul_f32 v115, v115, v35\n"
        "v_exp_f32 v112, v112\n"
        "v_mfma_f32_16x16x32_bf16 v[68:71], v[212:215], v[116:119], 0\n"
        "v_exp_f32 v113, v113\n"
        "v_mfma_f32_16x16x32_bf16 v[68:71], v[208:211], v[96:99], v[68:71]\n"
        "v_exp_f32 v114, v114\n"
        "v_mfma_f32_16x16x32_bf16 v[68:71], v[204:207], v[92:95], v[68:71]\n"
        "v_cvt_pk_bf16_f32 v48, v28, v29\n"
        "v_cvt_pk_bf16_f32 v49, v30, v31\n"
        "v_add_f32 v155, v155, v112\n"
        "v_mfma_f32_16x16x32_bf16 v[64:67], v[216:219], v[92:95], 0\n"
        "v_exp_f32 v115, v115\n"
        "v_mfma_f32_16x16x32_bf16 v[64:67], v[220:223], v[96:99], v[64:67]\n"
        "v_add_f32 v155, v155, v113\n"
        "v_add_f32 v155, v155, v114\n"
        "v_mfma_f32_16x16x32_bf16 v[64:67], v[224:227], v[116:119], v[64:67]\n"
        "v_cvt_pk_bf16_f32 v50, v68, v69\n"
        "v_cvt_pk_bf16_f32 v51, v70, v71\n"
        "v_add_f32 v155, v155, v115\n"
        "v_mul_f32 v20, v20, v36\n"
        "v_mfma_f32_16x16x32_bf16 v[28:31], v[236:239], v[116:119], 0\n"
        "v_mul_f32 v21, v21, v37\n"
        "v_mul_f32 v22, v22, v38\n"
        "v_mfma_f32_16x16x32_bf16 v[28:31], v[232:235], v[96:99], v[28:31]\n"
        "v_cvt_pk_bf16_f32 v150, v64, v65\n"
        "v_cvt_pk_bf16_f32 v151, v66, v67\n"
        "v_mfma_f32_16x16x32_bf16 v[28:31], v[228:231], v[92:95], v[28:31]\n"
        "v_exp_f32 v20, v20\n"
        "v_mfma_f32_16x16x4_4b_bf16 v[76:91], v[56:57], v[158:159], 0 blgp:4\n"
        "v_exp_f32 v21, v21\n"
        "v_mul_f32 v23, v23, v39\n"
        "v_exp_f32 v22, v22\n"
        "v_exp_f32 v23, v23\n"
        "v_add_f32 v155, v155, v20\n"
        "v_add_f32 v155, v155, v21\n"
        "v_cvt_pk_bf16_f32 v152, v28, v29\n"
        "v_cvt_pk_bf16_f32 v153, v30, v31\n"
        "v_mfma_f32_16x16x32_bf16 v[36:39], v[124:127], v[44:47], 0\n"
        "v_add_f32 v155, v155, v22\n"
        "v_add_f32 v155, v155, v23\n"
        "v_mfma_f32_16x16x32_bf16 v[36:39], v[128:131], v[48:51], v[36:39]\n"
        "v_mul_f32 v100, v100, v76\n"
        "v_mul_f32 v101, v101, v77\n"
        "v_mfma_f32_16x16x32_bf16 v[36:39], v[160:163], v[150:153], v[36:39]\n"
        "v_mul_f32 v102, v102, v78\n"
        "v_mul_f32 v103, v103, v79\n"
        "v_mul_f32 v144, v144, v80\n"
        "v_mul_f32 v145, v145, v81\n"
        "v_mfma_f32_16x16x32_bf16 v[140:143], v[52:55], v[150:153], 0\n"
        "v_mul_f32 v146, v146, v82\n"
        "v_mul_f32 v147, v147, v83\n"
        "v_mfma_f32_16x16x32_bf16 v[140:143], v[136:139], v[48:51], v[140:143]\n"
        "v_mul_f32 v24, v24, v84\n"
        "v_mul_f32 v25, v25, v85\n"
        "v_mfma_f32_16x16x32_bf16 v[140:143], v[132:135], v[44:47], v[140:143]\n"
        "v_add_f32 v62, v62, v63\n"
        "v_mul_f32 v62, v62, v40\n"
        "v_mul_f32 v26, v26, v86\n"
        "v_mfma_f32_16x16x4_4b_bf16 v[64:79], v[56:57], v[164:165], 0 blgp:6\n"
        "v_exp_f32 v62, v62\n"
        "v_mul_f32 v27, v27, v87\n"
        "v_fma_f32 v148, v148, v88, v19\n"
        "v_add_f32_dpp v155, v62, v155 quad_perm:[0,1,2,3] row_mask:0x4 bank_mask:0xf\n"
        "v_cvt_pk_bf16_f32 v40, v100, v101\n"
        "v_cvt_pk_bf16_f32 v41, v102, v103\n"
        "v_cvt_pk_bf16_f32 v42, v144, v145\n"
        "v_cvt_pk_bf16_f32 v43, v146, v147\n"
        "v_cvt_pk_bf16_f32 v164, v24, v25\n"
        "v_mfma_f32_16x16x32_bf16 v[156:159], v[92:95], v[44:47], 0\n"
        "v_cvt_pk_bf16_f32 v166, v26, v27\n"
        "v_cvt_pk_bf16_f32 v165, v148, 0\n"
        "v_mfma_f32_16x16x32_bf16 v[156:159], v[96:99], v[48:51], v[156:159]\n"
        "v_mul_f32 v36, v36, v64\n"
        "v_mul_f32 v37, v37, v65\n"
        "v_mfma_f32_16x16x32_bf16 v[156:159], v[116:119], v[150:153], v[156:159]\n"
        "v_mul_f32 v38, v38, v66\n"
        "v_mul_f32 v39, v39, v67\n"
        "v_mul_f32 v140, v140, v68\n"
        "v_mfma_f32_16x16x32_bf16 v[92:95], v[104:107], v[150:153], 0\n"
        "ds_read_b128 v[88:91], v17 offset:0\n"
        "v_mul_f32 v141, v141, v69\n"
        "v_smfmac_f32_16x16x64_bf16 v[92:95], v[108:111], v[44:51], v4\n"
        "ds_read_b128 v[84:87], v17 offset:64\n"
        "ds_read_b128 v[150:153], v17 offset:128\n"
        "v_mfma_f32_16x16x32_bf16 v[48:51], v[124:127], v[240:243], 0\n"
        "v_exp_f32 v36, v36\n"
        "v_mfma_f32_16x16x32_bf16 v[48:51], v[128:131], v[244:247], v[48:51]\n"
        "ds_read_b128 v[80:83], v0 offset:5136\n"
        "v_mul_f32 v142, v142, v70\n"
        "v_mfma_f32_16x16x32_bf16 v[48:51], v[160:163], v[248:251], v[48:51]\n"
        "ds_read_b128 v[32:35], v0 offset:4112\n"
        "v_mul_f32 v143, v143, v71\n"
        "v_mfma_f32_16x16x32_bf16 v[24:27], v[52:55], v[248:251], 0\n"
        "v_exp_f32 v37, v37\n"
        "v_mfma_f32_16x16x32_bf16 v[24:27], v[136:139], v[244:247], v[24:27]\n"
        "v_exp_f32 v38, v38\n"
        "v_mfma_f32_16x16x32_bf16 v[24:27], v[132:135], v[240:243], v[24:27]\n"
        "s_waitcnt lgkmcnt(2)\n"
        "v_cvt_pk_bf16_f32 v44, v48, v49\n"
        "v_cvt_pk_bf16_f32 v45, v50, v51\n"
        "v_add_f32 v145, v36, v37\n"
        "v_mfma_f32_16x16x32_bf16 v[104:107], v[88:91], v[240:243], 0\n"
        "v_exp_f32 v39, v39\n"
        "v_mfma_f32_16x16x32_bf16 v[104:107], v[84:87], v[244:247], v[104:107]\n"
        "v_add_f32 v145, v145, v38\n"
        "v_cvt_pk_bf16_f32 v46, v24, v25\n"
        "v_mfma_f32_16x16x32_bf16 v[104:107], v[150:153], v[248:251], v[104:107]\n"
        "s_waitcnt lgkmcnt(0)\n"
        "v_cvt_pk_bf16_f32 v47, v26, v27\n"
        "v_add_f32 v145, v145, v39\n"
        "v_exp_f32 v140, v140\n"
        "v_mfma_f32_16x16x32_bf16 v[64:67], v[150:153], v[80:83], 0\n"
        "v_exp_f32 v141, v141\n"
        "v_mfma_f32_16x16x32_bf16 v[64:67], v[84:87], v[32:35], v[64:67]\n"
        "ds_read_b128 v[146:149], v0 offset:6176\n"
        "v_cvt_pk_bf16_f32 v116, v104, v105\n"
        "v_mfma_f32_16x16x32_bf16 v[64:67], v[88:91], v[252:255], v[64:67]\n"
        "ds_read_b128 v[24:27], v0 offset:7200\n"
        "ds_read_b128 v[108:111], v0 offset:8224\n"
        "v_mfma_f32_16x16x32_bf16 v[100:103], v[132:135], v[252:255], 0\n"
        "v_cvt_pk_bf16_f32 v120, v106, v107\n"
        "v_add_f32 v145, v145, v140\n"
        "v_mfma_f32_16x16x32_bf16 v[100:103], v[136:139], v[32:35], v[100:103]\n"
        "v_exp_f32 v142, v142\n"
        "v_mfma_f32_16x16x32_bf16 v[100:103], v[52:55], v[80:83], v[100:103]\n"
        "v_cvt_pk_bf16_f32 v94, v64, v65\n"
        "v_cvt_pk_bf16_f32 v98, v66, v67\n"
        "v_add_f32 v145, v145, v141\n"
        "v_mfma_f32_16x16x32_bf16 v[68:71], v[160:163], v[80:83], 0\n"
        "v_exp_f32 v143, v143\n"
        "v_mfma_f32_16x16x32_bf16 v[68:71], v[128:131], v[32:35], v[68:71]\n"
        "v_add_f32 v145, v145, v142\n"
        "v_add_f32 v145, v145, v143\n"
        "v_mfma_f32_16x16x32_bf16 v[68:71], v[124:127], v[252:255], v[68:71]\n"
        "s_waitcnt lgkmcnt(0)\n"
        "v_cvt_pk_bf16_f32 v106, v100, v101\n"
        "v_cvt_pk_bf16_f32 v107, v102, v103\n"
        "v_mul_f32 v156, v156, v72\n"
        "v_mul_f32 v157, v157, v73\n"
        "v_mfma_f32_16x16x32_bf16 v[32:35], v[124:127], v[146:149], 0\n"
        "v_mul_f32 v158, v158, v74\n"
        "v_mul_f32 v159, v159, v75\n"
        "v_mfma_f32_16x16x32_bf16 v[32:35], v[128:131], v[24:27], v[32:35]\n"
        "ds_read_b128 v[48:51], v0 offset:11312\n"
        "v_cvt_pk_bf16_f32 v104, v68, v69\n"
        "v_mfma_f32_16x16x32_bf16 v[32:35], v[160:163], v[108:111], v[32:35]\n"
        "ds_read_b128 v[80:83], v0 offset:10288\n"
        "ds_read_b128 v[64:67], v0 offset:9264\n"
        "v_mfma_f32_16x16x32_bf16 v[100:103], v[52:55], v[108:111], 0\n"
        "v_cvt_pk_bf16_f32 v105, v70, v71\n"
        "v_add_f32 v92, v92, v93\n"
        "v_mfma_f32_16x16x32_bf16 v[100:103], v[136:139], v[24:27], v[100:103]\n"
        "v_exp_f32 v156, v156\n"
        "v_mfma_f32_16x16x32_bf16 v[100:103], v[132:135], v[146:149], v[100:103]\n"
        "v_cvt_pk_bf16_f32 v28, v32, v33\n"
        "v_cvt_pk_bf16_f32 v29, v34, v35\n"
        "v_add_f32 v145, v145, v156\n"
        "v_mfma_f32_16x16x32_bf16 v[70:73], v[88:91], v[146:149], 0\n"
        "v_exp_f32 v157, v157\n"
        "v_mfma_f32_16x16x32_bf16 v[70:73], v[84:87], v[24:27], v[70:73]\n"
        "v_exp_f32 v158, v158\n"
        "v_mfma_f32_16x16x32_bf16 v[70:73], v[150:153], v[108:111], v[70:73]\n"
        "s_waitcnt lgkmcnt(0)\n"
        "v_cvt_pk_bf16_f32 v30, v100, v101\n"
        "v_cvt_pk_bf16_f32 v31, v102, v103\n"
        "v_exp_f32 v159, v159\n"
        "v_mfma_f32_16x16x32_bf16 v[32:35], v[150:153], v[48:51], 0\n"
        "v_add_f32 v145, v145, v157\n"
        "v_add_f32 v145, v145, v158\n"
        "v_mfma_f32_16x16x32_bf16 v[32:35], v[84:87], v[80:83], v[32:35]\n"
        "ds_read_b128 v[100:103], v0 offset:12352\n"
        "v_cvt_pk_bf16_f32 v144, v70, v71\n"
        "v_mfma_f32_16x16x32_bf16 v[32:35], v[88:91], v[64:67], v[32:35]\n"
        "ds_read_b128 v[108:111], v0 offset:13376\n"
        "ds_read_b128 v[24:27], v0 offset:14400\n"
        "v_mfma_f32_16x16x32_bf16 v[68:71], v[132:135], v[64:67], 0\n"
        "v_cvt_pk_bf16_f32 v148, v72, v73\n"
        "v_mul_f32 v92, v92, v76\n"
        "v_mfma_f32_16x16x32_bf16 v[68:71], v[136:139], v[80:83], v[68:71]\n"
        "v_add_f32 v145, v145, v159\n"
        "v_readlane_b32 s59, v123, 32\n"
        "v_mfma_f32_16x16x32_bf16 v[68:71], v[52:55], v[48:51], v[68:71]\n"
        "v_cvt_pk_bf16_f32 v117, v32, v33\n"
        "v_cvt_pk_bf16_f32 v121, v34, v35\n"
        "v_exp_f32 v92, v92\n"
        "v_mfma_f32_16x16x32_bf16 v[72:75], v[160:163], v[48:51], 0\n"
        "v_mul_f32 v154, s59, v154\n"
        "v_add_f32_dpp v145, v92, v145 quad_perm:[0,1,2,3] row_mask:0x4 bank_mask:0xf\n"
        "v_mfma_f32_16x16x32_bf16 v[72:75], v[128:131], v[80:83], v[72:75]\n"
        "v_cvt_pk_bf16_f32 v154, v154, 0\n"
        "v_cvt_pk_bf16_f32 v50, v68, v69\n"
        "v_mfma_f32_16x16x32_bf16 v[72:75], v[124:127], v[64:67], v[72:75]\n"
        "s_waitcnt lgkmcnt(0)\n"
        "v_cvt_pk_bf16_f32 v51, v70, v71\n"
        "v_permlane32_swap_b32 v155, v145\n"
        "v_mfma_f32_16x16x32_bf16 v[80:83], v[124:127], v[100:103], 0\n"
        "v_add_f32 v155, v155, v145\n"
        "v_mov_b32 v149, v155\n"
        "v_mfma_f32_16x16x32_bf16 v[80:83], v[128:131], v[108:111], v[80:83]\n"
        "ds_read_b128 v[32:35], v0 offset:17488\n"
        "v_cvt_pk_bf16_f32 v48, v72, v73\n"
        "v_mfma_f32_16x16x32_bf16 v[80:83], v[160:163], v[24:27], v[80:83]\n"
        "ds_read_b128 v[68:71], v0 offset:16464\n"
        "ds_read_b128 v[64:67], v0 offset:15440\n"
        "v_mfma_f32_16x16x32_bf16 v[76:79], v[52:55], v[24:27], 0\n"
        "v_cvt_pk_bf16_f32 v49, v74, v75\n"
        "v_mfma_f32_16x16x32_bf16 v[76:79], v[136:139], v[108:111], v[76:79]\n"
        "v_permlane16_swap_b32 v155, v149\n"
        "v_mfma_f32_16x16x32_bf16 v[76:79], v[132:135], v[100:103], v[76:79]\n"
        "v_add_f32 v155, v155, v149\n"
        "v_rcp_f32 v147, v155\n"
        "v_mfma_f32_16x16x32_bf16 v[72:75], v[88:91], v[100:103], 0\n"
        "ds_read2_b32 v[122:123], v11 offset0:0 offset1:3\n"
        "v_min3_f32 v167, v167, v155, v147\n"
        "v_mfma_f32_16x16x32_bf16 v[72:75], v[84:87], v[108:111], v[72:75]\n"
        "ds_read_b64_tr_b16 v[118:119], v9 offset:0\n"
        "v_and_b32 v102, s28, v147\n"
        "v_mfma_f32_16x16x32_bf16 v[72:75], v[150:153], v[24:27], v[72:75]\n"
        "s_waitcnt lgkmcnt(2)\n"
        "ds_read2_b32 v[100:101], v11 offset0:1 offset1:4\n"
        "v_sub_f32 v149, v147, v102\n"
        "v_cvt_pk_bf16_f32 v103, v102, v149\n"
        "v_cvt_pk_bf16_f32 v108, v80, v81\n"
        "v_mfma_f32_16x16x32_bf16 v[24:27], v[150:153], v[32:35], 0\n"
        "ds_read_b64_tr_b16 v[96:97], v9 offset:16\n"
        "v_cvt_pk_bf16_f32 v109, v82, v83\n"
        "v_mfma_f32_16x16x32_bf16 v[24:27], v[84:87], v[68:71], v[24:27]\n"
        "ds_read2_b32 v[150:151], v11 offset0:2 offset1:5\n"
        "v_cvt_pk_bf16_f32 v110, v76, v77\n"
        "v_mfma_f32_16x16x32_bf16 v[24:27], v[88:91], v[64:67], v[24:27]\n"
        "ds_read_b64_tr_b16 v[146:147], v9 offset:32\n"
        "v_cvt_pk_bf16_f32 v111, v78, v79\n"
        "v_cvt_pk_bf16_f32 v95, v72, v73\n"
        "v_mfma_f32_16x16x4_4b_bf16 v[76:91], v[56:57], v[102:103], 0 blgp:4\n"
        "v_cvt_pk_bf16_f32 v99, v74, v75\n"
        "v_cmp_nle_f32 vcc, 0x2b800000, v167\n"
        "v_mov_b32_dpp v167, v19 row_shr:4 row_mask:0xf bank_mask:0xf bound_ctrl:0\n"
        "ds_write_b16 v1, v154\n"
        "v_cvt_pk_bf16_f32 v145, v24, v25\n"
        "v_cvt_pk_bf16_f32 v149, v26, v27\n"
        "s_andn2_b64 vcc, vcc, s[56:57]\n"
        "s_cbranch_scc1 CREDOA_%=\n"
        "v_mfma_f32_16x16x32_bf16 v[24:27], v[132:135], v[64:67], 0\n"
        "s_mov_b32 exec_hi, 0xffff\n"
        "v_add_u32 v10, 40064, v10\n"
        "s_mov_b32 exec_hi, -1\n"
        "v_mfma_f32_16x16x32_bf16 v[24:27], v[136:139], v[68:71], v[24:27]\n"
        "v_mul_f32 v58, v58, v76\n"
        "v_mul_f32 v59, v59, v77\n"
        "v_mfma_f32_16x16x32_bf16 v[24:27], v[52:55], v[32:35], v[24:27]\n"
        "s_add_u32 s31, s31, 1\n"
        "v_mul_f32 v60, v60, v78\n"
        "v_mul_f32 v61, v61, v79\n"
        "v_mul_f32 v112, v112, v80\n"
        "v_mul_f32 v113, v113, v81\n"
        "v_mfma_f32_16x16x32_bf16 v[52:55], v[160:163], v[32:35], 0\n"
        "s_cmp_ge_u32 s31, s71\n"
        "v_mul_f32 v114, v114, v82\n"
        "v_mul_f32 v115, v115, v83\n"
        "v_mfma_f32_16x16x32_bf16 v[52:55], v[128:131], v[68:71], v[52:55]\n"
        "s_cbranch_scc1 L34A_%=\n"
        "L35A_%=:\n"
        "v_mul_f32 v20, v20, v84\n"
        "v_cvt_pk_bf16_f32 v34, v24, v25\n"
        "v_mfma_f32_16x16x32_bf16 v[52:55], v[124:127], v[64:67], v[52:55]\n"
        "s_add_u32 s16, s16, 256\n"
        "v_cvt_pk_bf16_f32 v35, v26, v27\n"
        "v_mul_f32 v21, v21, v85\n"
        "v_mfma_f32_16x16x32_bf16 v[124:127], v[40:43], v[44:47], 0\n"
        "s_waitcnt lgkmcnt(0)\n"
        "v_mul_f32 v22, v22, v86\n"
        "v_mul_f32 v23, v23, v87\n"
        "v_smfmac_f32_16x16x64_bf16 v[124:127], v[164:167], v[116:123], v18\n"
        "s_cmp_ge_u32 s31, 20\n"
        "s_cselect_b32 s39, s22, s16\n"
        "ds_read_b64_tr_b16 v[118:119], v9 offset:48\n"
        "v_mfma_f32_16x16x4_4b_bf16 v[72:87], v[56:57], v[102:103], 0 blgp:6\n"
        "v_cvt_pk_bf16_f32 v32, v52, v53\n"
        "v_cvt_pk_bf16_f32 v33, v54, v55\n"
        "v_fma_f32 v62, v62, v88, v19\n"
        "v_cvt_pk_bf16_f32 v68, v58, v59\n"
        "v_cvt_pk_bf16_f32 v69, v60, v61\n"
        "v_cvt_pk_bf16_f32 v70, v112, v113\n"
        "v_cvt_pk_bf16_f32 v71, v114, v115\n"
        "v_cvt_pk_bf16_f32 v24, v20, v21\n"
        "v_cvt_pk_bf16_f32 v26, v22, v23\n"
        "v_mfma_f32_16x16x32_bf16 v[88:91], v[40:43], v[104:107], 0\n"
        "s_cmp_ge_u32 s31, 21\n"
        "v_cvt_pk_bf16_f32 v25, v62, 0\n"
        "v_mul_f32 v36, v36, v72\n"
        "v_smfmac_f32_16x16x64_bf16 v[88:91], v[164:167], v[94:101], v18\n"
        "s_cselect_b32 s39, s23, s39\n"
        "ds_read_b64_tr_b16 v[96:97], v9 offset:64\n"
        "v_mfma_f32_16x16x32_bf16 v[56:59], v[40:43], v[28:31], 0\n"
        "s_mul_hi_u32 s38, s39, 58835169\n"
        "ds_read_b128 v[128:131], v2\n"
        "v_mul_f32 v37, v37, v73\n"
        "v_smfmac_f32_16x16x64_bf16 v[56:59], v[164:167], v[144:151], v18\n"
        "s_mul_i32 s39, s39, 1344\n"
        "ds_read_b64_tr_b16 v[146:147], v9 offset:80\n"
        "ds_read_b128 v[52:55], v3\n"
        "v_mfma_f32_16x16x32_bf16 v[112:115], v[40:43], v[48:51], 0\n"
        "s_waitcnt lgkmcnt(4)\n"
        "v_mul_f32 v38, v38, v74\n"
        "v_mul_f32 v39, v39, v75\n"
        "v_smfmac_f32_16x16x64_bf16 v[112:115], v[164:167], v[116:123], v18 abid:1\n"
        "s_mul_i32 s38, s38, 588672\n"
        "s_add_u32 s39, s39, s38\n"
        "v_mul_f32 v140, v140, v76\n"
        "v_cvt_pk_bf16_f32 v152, v124, v88\n"
        "v_mul_f32 v141, v141, v77\n"
        "v_mfma_f32_16x16x32_bf16 v[72:75], v[40:43], v[108:111], 0\n"
        "s_add_u32 s39, s39, s17\n"
        "v_mov_b64 v[60:61], 0\n"
        "s_waitcnt lgkmcnt(3)\n"
        "v_smfmac_f32_16x16x64_bf16 v[72:75], v[164:167], v[94:101], v18 abid:1\n"
        "s_mul_i32 s68, s31, 3\n"
        "v_mov_b64 v[62:63], 0\n"
        "v_cvt_pk_bf16_f32 v153, v56, v112\n"
        "v_mfma_f32_16x16x32_bf16 v[64:67], v[40:43], v[32:35], 0\n"
        "s_waitcnt lgkmcnt(1)\n"
        "v_mov_b64 v[20:21], 0\n"
        "v_mov_b64 v[22:23], 0\n"
        "v_smfmac_f32_16x16x64_bf16 v[64:67], v[164:167], v[144:151], v18 abid:1\n"
        "s_waitcnt lgkmcnt(0)\n"
        "s_bitcmp1_b32 s31, 0\n"
        "v_mov_b32_dpp v27, v19 row_shr:4 row_mask:0xf bank_mask:0xf bound_ctrl:0\n"
        "s_cselect_b32 s38, 3, 6\n"
        "v_smfmac_f32_16x16x64_bf16 v[60:63], v[128:131], v[44:51], v4\n"
        "s_add_u32 s68, s68, s38\n"
        "v_mov_b64 v[40:41], 0\n"
        "v_smfmac_f32_16x16x64_bf16 v[60:63], v[52:55], v[116:123], v4 abid:1\n"
        "s_add_u32 s37, s35, 0\n"
        "s_cmp_lt_u32 s31, s14\n"
        "ds_read_b64_tr_b16 v[118:119], v9 offset:3328\n"
        "v_mov_b64 v[42:43], 0\n"
        "v_smfmac_f32_16x16x64_bf16 v[20:23], v[128:131], v[104:111], v4\n"
        "s_cselect_b32 s68, s68, 0\n"
        "v_cvt_pk_bf16_f32 v154, v72, v64\n"
        "v_smfmac_f32_16x16x64_bf16 v[20:23], v[52:55], v[94:101], v4 abid:1\n"
        "s_and_b32 s38, s31, 7\n"
        "ds_read_b64_tr_b16 v[96:97], v9 offset:3344\n"
        "v_cvt_pk_bf16_f32 v160, v125, v89\n"
        "v_smfmac_f32_16x16x64_bf16 v[40:43], v[128:131], v[28:35], v4\n"
        "s_cselect_b32 s30, 1, -7\n"
        "v_cvt_pk_bf16_f32 v88, v126, v90\n"
        "v_smfmac_f32_16x16x64_bf16 v[40:43], v[52:55], v[144:151], v4 abid:1\n"
        "buffer_store_dwordx3 v[152:154], v12, s[8:11], s37 offen sc0 sc1 nt\n"
        "ds_read_b64_tr_b16 v[146:147], v9 offset:3360\n"
        "v_cvt_pk_bf16_f32 v164, v127, v91\n"
        "v_cvt_pk_bf16_f32 v161, v57, v113\n"
        "v_cvt_pk_bf16_f32 v89, v58, v114\n"
        "v_cvt_pk_bf16_f32 v165, v59, v115\n"
        "v_cvt_pk_bf16_f32 v162, v73, v65\n"
        "v_mfma_f32_16x16x32_bf16 v[152:155], v[68:71], v[44:47], 0\n"
        "s_waitcnt lgkmcnt(2)\n"
        "v_cvt_pk_bf16_f32 v90, v74, v66\n"
        "v_cvt_pk_bf16_f32 v166, v75, v67\n"
        "v_smfmac_f32_16x16x64_bf16 v[152:155], v[24:27], v[116:123], v18\n"
        "s_mul_i32 s29, s30, 192\n"
        "s_mul_i32 s30, s30, 392\n"
        "ds_read_b64_tr_b16 v[118:119], v9 offset:3376\n"
        "v_add_f32 v60, v60, v61\n"
        "v_add_f32 v62, v62, v63\n"
        "v_add_f32 v20, v20, v21\n"
        "v_mfma_f32_16x16x32_bf16 v[56:59], v[68:71], v[104:107], 0\n"
        "s_waitcnt lgkmcnt(2)\n"
        "v_add_f32 v22, v22, v23\n"
        "v_cvt_pk_bf16_f32 v112, v60, v20\n"
        "v_smfmac_f32_16x16x64_bf16 v[56:59], v[24:27], v[94:101], v18\n"
        "s_add_u32 s37, s35, 0\n"
        "s_cmp_eq_u32 s69, s18\n"
        "ds_read_b64_tr_b16 v[96:97], v9 offset:3392\n"
        "v_add_f32 v40, v40, v41\n"
        "v_add_f32 v42, v42, v43\n"
        "v_cvt_pk_bf16_f32 v113, v40, v62\n"
        "v_mfma_f32_16x16x32_bf16 v[72:75], v[68:71], v[28:31], 0\n"
        "s_waitcnt lgkmcnt(2)\n"
        "v_cvt_pk_bf16_f32 v114, v22, v42\n"
        "v_mul_f32 v142, v142, v78\n"
        "v_smfmac_f32_16x16x64_bf16 v[72:75], v[24:27], v[144:151], v18\n"
        "buffer_store_dwordx3 v[160:162], v15, s[8:11], s37 offen offset:768 sc0 sc1 nt\n"
        "ds_read_b64_tr_b16 v[146:147], v9 offset:3408\n"
        "s_cselect_b32 s34, -7488, s70\n"
        "v_mul_f32 v143, v143, v79\n"
        "v_mul_f32 v156, v156, v80\n"
        "v_mul_f32 v157, v157, v81\n"
        "v_mul_f32 v158, v158, v82\n"
        "v_mfma_f32_16x16x32_bf16 v[20:23], v[68:71], v[48:51], 0\n"
        "s_waitcnt lgkmcnt(2)\n"
        "s_add_u32 s69, s69, s34\n"
        "v_mul_f32 v159, v159, v83\n"
        "v_smfmac_f32_16x16x64_bf16 v[20:23], v[24:27], v[116:123], v18 abid:1\n"
        "s_add_u32 s37, s35, 98112\n"
        "ds_read_b64_tr_b16 v[118:119], v9 offset:6656\n"
        "v_fma_f32 v92, v92, v84, v19\n"
        "v_mfma_f32_16x16x32_bf16 v[84:87], v[68:71], v[108:111], 0\n"
        "s_waitcnt lgkmcnt(2)\n"
        "v_cvt_pk_bf16_f32 v80, v36, v37\n"
        "v_cvt_pk_bf16_f32 v81, v38, v39\n"
        "v_smfmac_f32_16x16x64_bf16 v[84:87], v[24:27], v[94:101], v18 abid:1\n"
        "ds_read_b64_tr_b16 v[96:97], v9 offset:6672\n"
        "v_cvt_pk_bf16_f32 v82, v140, v141\n"
        "v_mfma_f32_16x16x32_bf16 v[60:63], v[68:71], v[32:35], 0\n"
        "s_waitcnt lgkmcnt(2)\n"
        "v_cvt_pk_bf16_f32 v83, v142, v143\n"
        "v_cvt_pk_bf16_f32 v76, v156, v157\n"
        "v_smfmac_f32_16x16x64_bf16 v[60:63], v[24:27], v[144:151], v18 abid:1\n"
        "buffer_store_dwordx3 v[88:90], v12, s[8:11], s37 offen offset:192 sc0 sc1 nt\n"
        "ds_read_b64_tr_b16 v[146:147], v9 offset:6688\n"
        "ds_read_b32 v167, v0 offset:21604\n"
        "v_cvt_pk_bf16_f32 v78, v158, v159\n"
        "v_cvt_pk_bf16_f32 v77, v92, 0\n"
        "v_mov_b32_dpp v79, v19 row_shr:4 row_mask:0xf bank_mask:0xf bound_ctrl:0\n"
        "v_mfma_f32_16x16x32_bf16 v[68:71], v[80:83], v[44:47], 0\n"
        "s_waitcnt lgkmcnt(3)\n"
        "v_writelane_b32 v10, s69, 56\n"
        "v_add_u32 v11, s30, v11\n"
        "v_smfmac_f32_16x16x64_bf16 v[68:71], v[76:79], v[116:123], v18\n"
        "s_add_u32 s37, s35, 98112\n"
        "ds_read_b64_tr_b16 v[118:119], v9 offset:6704\n"
        "v_mfma_f32_16x16x32_bf16 v[36:39], v[80:83], v[104:107], 0\n"
        "s_waitcnt lgkmcnt(3)\n"
        "v_smfmac_f32_16x16x64_bf16 v[36:39], v[76:79], v[94:101], v18\n"
        "ds_read_b64_tr_b16 v[96:97], v9 offset:6720\n"
        "v_mfma_f32_16x16x32_bf16 v[40:43], v[80:83], v[28:31], 0\n"
        "s_waitcnt lgkmcnt(3)\n"
        "s_waitcnt lgkmcnt(2)\n"
        "v_readfirstlane_b32 s40, v167\n"
        "v_smfmac_f32_16x16x64_bf16 v[40:43], v[76:79], v[144:151], v18\n"
        "buffer_store_dwordx3 v[164:166], v14, s[8:11], s37 offen offset:960 sc0 sc1 nt\n"
        "ds_read_b64_tr_b16 v[146:147], v9 offset:6736\n"
        "s_add_u32 s37, s35, 588672\n"
        "s_add_u32 s46, s37, 1344\n"
        "s_cmp_lt_i32 s40, s68\n"
        "s_mov_b64 exec, 1\n"
        "ds_inc_u32 v0, v1 offset:21608\n"
        "s_mov_b64 exec, -1\n"
        "s_cbranch_scc1 L38A_%=\n"
        "L39A_%=:\n"
        "ds_read_b128 v[124:127], v7 offset:40064\n"
        "v_mfma_f32_16x16x32_bf16 v[64:67], v[80:83], v[48:51], 0\n"
        "s_waitcnt lgkmcnt(4)\n"
        "ds_read_b128 v[132:135], v7 offset:43392\n"
        "v_smfmac_f32_16x16x64_bf16 v[64:67], v[76:79], v[116:123], v18 abid:1\n"
        "buffer_store_dwordx3 v[112:114], v12, s[44:47], s37 offen offset:1152 sc0 sc1 nt\n"
        "ds_read_b128 v[128:131], v7 offset:40128\n"
        "ds_read_b128 v[136:139], v7 offset:43456\n"
        "ds_read_b128 v[160:163], v7 offset:40192\n"
        "ds_read_b128 v[52:55], v7 offset:43520\n"
        "v_mfma_f32_16x16x32_bf16 v[104:107], v[80:83], v[108:111], 0\n"
        "s_waitcnt lgkmcnt(7)\n"
        "s_mov_b32 s36, s35\n"
        "v_smfmac_f32_16x16x64_bf16 v[104:107], v[76:79], v[94:101], v18 abid:1\n"
        "s_mov_b64 exec, s[62:63]\n"
        "v_add_u32 v16, s29, v16\n"
        "v_add_u32 v8, s34, v8\n"
        "s_mov_b64 exec, -1\n"
        "v_mfma_f32_16x16x32_bf16 v[88:91], v[80:83], v[32:35], 0\n"
        "s_mov_b32 s35, s39\n"
        "v_smfmac_f32_16x16x64_bf16 v[88:91], v[76:79], v[144:151], v18 abid:1\n"
        "s_mov_b64 exec, s[50:51]\n"
        "v_add_u32 v5, s29, v5\n"
        "v_add_u32 v6, s34, v6\n"
        "s_mov_b64 exec, -1\n"
        "s_setprio 0\n"
        "CNEXTA_%=:\n"
        "s_cmp_ge_u32 s31, s14\n"
        "s_cbranch_scc1 CEND_%=\n"
        "s_waitcnt lgkmcnt(5)\n"
        "v_mfma_f32_16x16x32_bf16 v[24:27], v[168:171], v[124:127], 0\n"
        "v_cvt_pk_bf16_f32 v156, v152, v56\n"
        "v_cvt_pk_bf16_f32 v157, v72, v20\n"
        "s_waitcnt lgkmcnt(3)\n"
        "v_mfma_f32_16x16x32_bf16 v[24:27], v[172:175], v[128:131], v[24:27]\n"
        "ds_read_b128 v[44:47], v16 offset:128\n"
        "v_cvt_pk_bf16_f32 v158, v84, v60\n"
        "s_waitcnt lgkmcnt(2)\n"
        "v_mfma_f32_16x16x32_bf16 v[24:27], v[176:179], v[160:163], v[24:27]\n"
        "ds_read_b128 v[48:51], v5\n"
        "v_cvt_pk_bf16_f32 v164, v153, v57\n"
        "v_mfma_f32_16x16x32_bf16 v[32:35], v[188:191], v[160:163], 0\n"
        "v_cvt_pk_bf16_f32 v165, v73, v21\n"
        "v_cvt_pk_bf16_f32 v166, v85, v61\n"
        "v_mfma_f32_16x16x32_bf16 v[32:35], v[184:187], v[128:131], v[32:35]\n"
        "v_cvt_pk_bf16_f32 v146, v154, v58\n"
        "v_cvt_pk_bf16_f32 v147, v74, v22\n"
        "v_mfma_f32_16x16x32_bf16 v[32:35], v[180:183], v[124:127], v[32:35]\n"
        "s_add_u32 s37, s36, 196224\n"
        "ds_read_b128 v[92:95], v7 offset:46720\n"
        "ds_read_b128 v[96:99], v7 offset:46784\n"
        "ds_read_b128 v[116:119], v7 offset:46848\n"
        "v_mfma_f32_16x16x32_bf16 v[76:79], v[192:195], v[124:127], 0\n"
        "v_cvt_pk_bf16_f32 v80, v24, v25\n"
        "v_cvt_pk_bf16_f32 v81, v26, v27\n"
        "v_mfma_f32_16x16x32_bf16 v[76:79], v[196:199], v[128:131], v[76:79]\n"
        "v_cvt_pk_bf16_f32 v82, v32, v33\n"
        "v_cvt_pk_bf16_f32 v83, v34, v35\n"
        "v_mfma_f32_16x16x32_bf16 v[76:79], v[200:203], v[160:163], v[76:79]\n"
        "buffer_store_dwordx3 v[156:158], v12, s[8:11], s37 offen offset:384 sc0 sc1 nt\n"
        "v_cvt_pk_bf16_f32 v148, v86, v62\n"
        "v_cvt_pk_bf16_f32 v150, v155, v59\n"
        "v_cvt_pk_bf16_f32 v151, v75, v23\n"
        "v_cvt_pk_bf16_f32 v152, v87, v63\n"
        "s_waitcnt lgkmcnt(3)\n"
        "v_mfma_f32_16x16x32_bf16 v[32:35], v[44:47], v[160:163], 0\n"
        "v_cvt_pk_bf16_f32 v72, v68, v36\n"
        "v_cvt_pk_bf16_f32 v84, v76, v77\n"
        "v_smfmac_f32_16x16x64_bf16 v[32:35], v[48:51], v[124:131], v4\n"
        "s_add_u32 s37, s36, 196224\n"
        "v_cvt_pk_bf16_f32 v85, v78, v79\n"
        "v_mfma_f32_16x16x32_bf16 v[24:27], v[44:47], v[52:55], 0\n"
        "v_cvt_pk_bf16_f32 v73, v40, v64\n"
        "v_cvt_pk_bf16_f32 v74, v104, v88\n"
        "v_smfmac_f32_16x16x64_bf16 v[24:27], v[48:51], v[132:139], v4\n"
        "s_waitcnt lgkmcnt(0)\n"
        "s_setprio 2\n"
        "v_cvt_pk_bf16_f32 v156, v69, v37\n"
        "v_cvt_pk_bf16_f32 v157, v41, v65\n"
        "v_mfma_f32_16x16x32_bf16 v[100:103], v[44:47], v[116:119], 0\n"
        "v_cvt_pk_bf16_f32 v158, v105, v89\n"
        "v_cvt_pk_bf16_f32 v76, v70, v38\n"
        "v_smfmac_f32_16x16x64_bf16 v[100:103], v[48:51], v[92:99], v4\n"
        "buffer_store_dwordx3 v[164:166], v13, s[8:11], s37 offen offset:1152 sc0 sc1 nt\n"
        "v_cvt_pk_bf16_f32 v77, v42, v66\n"
        "v_cvt_pk_bf16_f32 v78, v106, v90\n"
        "v_cvt_pk_bf16_f32 v88, v71, v39\n"
        "v_cvt_pk_bf16_f32 v89, v43, v67\n"
        "v_cvt_pk_bf16_f32 v90, v107, v91\n"
        "v_mfma_f32_16x16x32_bf16 v[20:23], v[212:215], v[160:163], 0\n"
        "s_add_u32 s37, s36, 294336\n"
        "ds_read_b64 v[154:155], v11 offset:192\n"
        "v_mfma_f32_16x16x32_bf16 v[20:23], v[208:211], v[128:131], v[20:23]\n"
        "ds_read_b32 v153, v10\n"
        "v_mfma_f32_16x16x32_bf16 v[20:23], v[204:207], v[124:127], v[20:23]\n"
        "v_mfma_f32_16x16x32_bf16 v[28:31], v[216:219], v[124:127], 0\n"
        "v_mfma_f32_16x16x32_bf16 v[28:31], v[220:223], v[128:131], v[28:31]\n"
        "v_mfma_f32_16x16x32_bf16 v[28:31], v[224:227], v[160:163], v[28:31]\n"
        "buffer_store_dwordx3 v[146:148], v12, s[8:11], s37 offen offset:576 sc0 sc1 nt\n"
        "s_add_u32 s37, s36, 392448\n"
        "s_waitcnt lgkmcnt(1)\n"
        "v_add_f32_dpp v155, v34, v35 quad_perm:[0,1,2,3] row_mask:0x1 bank_mask:0xf\n"
        "v_add_f32_dpp v154, v32, v33 quad_perm:[0,1,2,3] row_mask:0x1 bank_mask:0xf\n"
        "v_cvt_pk_bf16_f32 v86, v20, v21\n"
        "v_mfma_f32_16x16x32_bf16 v[44:47], v[236:239], v[160:163], 0\n"
        "v_cvt_pk_bf16_f32 v87, v22, v23\n"
        "v_cvt_pk_bf16_f32 v20, v28, v29\n"
        "v_mfma_f32_16x16x32_bf16 v[44:47], v[232:235], v[128:131], v[44:47]\n"
        "v_cvt_pk_bf16_f32 v21, v30, v31\n"
        "v_add_f32_dpp v155, v26, v27 quad_perm:[0,1,2,3] row_mask:0x2 bank_mask:0xf\n"
        "v_mfma_f32_16x16x32_bf16 v[44:47], v[228:231], v[124:127], v[44:47]\n"
        "buffer_store_dwordx3 v[150:152], v12, s[8:11], s37 offen sc0 sc1 nt\n"
        "v_add_f32_dpp v154, v24, v25 quad_perm:[0,1,2,3] row_mask:0x2 bank_mask:0xf\n"
        "v_add_f32_dpp v155, v102, v103 quad_perm:[0,1,2,3] row_mask:0x4 bank_mask:0xf\n"
        "v_mul_f32 v165, s52, v155\n"
        "v_add_f32_dpp v154, v100, v101 quad_perm:[0,1,2,3] row_mask:0x4 bank_mask:0xf\n"
        "s_waitcnt lgkmcnt(0)\n"
        "v_fma_f32 v165, -v165, v155, v153\n"
        "v_add_f32_e64 v165, s53, |v165|\n"
        "v_cvt_pk_bf16_f32 v23, v46, v47\n"
        "v_cvt_pk_bf16_f32 v22, v44, v45\n"
        "v_mfma_f32_16x16x32_bf16 v[100:103], v[124:127], v[80:83], 0\n"
        "v_rsq_f32 v153, v165\n"
        "v_mfma_f32_16x16x32_bf16 v[100:103], v[128:131], v[84:87], v[100:103]\n"
        "s_add_u32 s37, s36, 392448\n"
        "ds_read_b128 v[104:107], v8 offset:128\n"
        "v_and_b32 v56, s28, v153\n"
        "v_mfma_f32_16x16x32_bf16 v[100:103], v[160:163], v[20:23], v[100:103]\n"
        "ds_read2_b32 v[108:109], v6 offset0:0 offset1:2\n"
        "ds_read2_b32 v[110:111], v6 offset0:8 offset1:10\n"
        "v_mfma_f32_16x16x32_bf16 v[144:147], v[52:55], v[20:23], 0\n"
        "v_and_b32 v164, s28, v153\n"
        "v_sub_f32 v167, v153, v56\n"
        "v_mfma_f32_16x16x32_bf16 v[144:147], v[136:139], v[84:87], v[144:147]\n"
        "v_cvt_pk_bf16_f32 v57, v167, v56\n"
        "v_cvt_pk_bf16_f32 v165, v164, v167\n"
        "v_mfma_f32_16x16x32_bf16 v[144:147], v[132:135], v[80:83], v[144:147]\n"
        "buffer_store_dwordx3 v[72:74], v15, s[8:11], s37 offen offset:768 sc0 sc1 nt\n"
        "v_mul_f32 v154, v154, v153\n"
        "v_readlane_b32 s58, v153, 56\n"
        "s_mov_b64 exec, s[56:57]\n"
        "v_mov_b32 v154, 0xff800000\n"
        "s_mov_b64 exec, -1\n"
        "v_mfma_f32_16x16x4_4b_bf16 v[32:47], v[56:57], v[164:165], 0 blgp:4\n"
        "v_mul_f32 v154, s58, v154\n"
        "v_exp_f32 v154, v154\n"
        "v_mfma_f32_16x16x32_bf16 v[24:27], v[92:95], v[80:83], 0\n"
        "s_add_u32 s37, s36, 490560\n"
        "v_add_f32_dpp v167, v154, v154 row_ror:8 row_mask:0xf bank_mask:0xf\n"
        "v_mul_f32 v154, v154, v153\n"
        "v_mfma_f32_16x16x32_bf16 v[24:27], v[96:99], v[84:87], v[24:27]\n"
        "v_add_f32_dpp v167, v167, v167 row_ror:4 row_mask:0xf bank_mask:0xf\n"
        "v_mfma_f32_16x16x32_bf16 v[24:27], v[116:119], v[20:23], v[24:27]\n"
        "s_waitcnt lgkmcnt(0)\n"
        "v_add_f32_dpp v167, v167, v167 row_ror:2 row_mask:0xf bank_mask:0xf\n"
        "v_mfma_f32_16x16x32_bf16 v[148:151], v[104:107], v[20:23], 0\n"
        "v_mul_f32 v100, v100, v32\n"
        "v_mul_f32 v101, v101, v33\n"
        "v_smfmac_f32_16x16x64_bf16 v[148:151], v[108:111], v[80:87], v4\n"
        "buffer_store_dwordx3 v[156:158], v12, s[8:11], s37 offen offset:192 sc0 sc1 nt\n"
        "v_mul_f32 v102, v102, v34\n"
        "v_mul_f32 v103, v103, v35\n"
        "v_exp_f32 v100, v100\n"
        "v_mul_f32 v144, v144, v36\n"
        "v_mfma_f32_16x16x32_bf16 v[20:23], v[168:171], v[132:135], 0\n"
        "s_add_u32 s37, s36, 490560\n"
        "v_exp_f32 v101, v101\n"
        "v_mfma_f32_16x16x32_bf16 v[20:23], v[172:175], v[136:139], v[20:23]\n"
        "v_exp_f32 v102, v102\n"
        "v_mfma_f32_16x16x32_bf16 v[20:23], v[176:179], v[52:55], v[20:23]\n"
        "v_exp_f32 v103, v103\n"
        "v_add_f32 v151, v100, v101\n"
        "v_mfma_f32_16x16x32_bf16 v[28:31], v[188:191], v[52:55], 0\n"
        "v_add_f32 v151, v151, v102\n"
        "v_add_f32 v151, v151, v103\n"
        "v_mfma_f32_16x16x32_bf16 v[28:31], v[184:187], v[136:139], v[28:31]\n"
        "v_mul_f32 v145, v145, v37\n"
        "v_mul_f32 v146, v146, v38\n"
        "v_mfma_f32_16x16x32_bf16 v[28:31], v[180:183], v[132:135], v[28:31]\n"
        "buffer_store_dwordx3 v[76:78], v14, s[8:11], s37 offen offset:960 sc0 sc1 nt\n"
        "v_cvt_pk_bf16_f32 v68, v20, v21\n"
        "v_cvt_pk_bf16_f32 v69, v22, v23\n"
        "v_mul_f32 v147, v147, v39\n"
        "v_exp_f32 v144, v144\n"
        "v_mfma_f32_16x16x32_bf16 v[20:23], v[192:195], v[132:135], 0\n"
        "s_add_u32 s37, s36, 588672\n"
        "v_exp_f32 v145, v145\n"
        "v_mfma_f32_16x16x32_bf16 v[20:23], v[196:199], v[136:139], v[20:23]\n"
        "v_cvt_pk_bf16_f32 v70, v28, v29\n"
        "v_cvt_pk_bf16_f32 v71, v30, v31\n"
        "v_mfma_f32_16x16x32_bf16 v[20:23], v[200:203], v[52:55], v[20:23]\n"
        "buffer_store_dwordx3 v[88:90], v12, s[8:11], s37 offen offset:384 sc0 sc1 nt\n"
        "v_exp_f32 v146, v146\n"
        "v_exp_f32 v147, v147\n"
        "v_add_f32 v151, v151, v144\n"
        "v_add_f32 v151, v151, v145\n"
        "v_mfma_f32_16x16x32_bf16 v[28:31], v[212:215], v[52:55], 0\n"
        "v_add_f32 v151, v151, v146\n"
        "v_add_f32 v151, v151, v147\n"
        "v_mfma_f32_16x16x32_bf16 v[28:31], v[208:211], v[136:139], v[28:31]\n"
        "v_cvt_pk_bf16_f32 v72, v20, v21\n"
        "v_cvt_pk_bf16_f32 v73, v22, v23\n"
        "v_mfma_f32_16x16x32_bf16 v[28:31], v[204:207], v[132:135], v[28:31]\n"
        "v_mul_f32 v24, v24, v40\n"
        "v_mul_f32 v25, v25, v41\n"
        "v_mul_f32 v26, v26, v42\n"
        "v_mfma_f32_16x16x32_bf16 v[20:23], v[216:219], v[132:135], 0\n"
        "v_mul_f32 v27, v27, v43\n"
        "v_add_f32 v148, v148, v149\n"
        "v_mfma_f32_16x16x32_bf16 v[20:23], v[220:223], v[136:139], v[20:23]\n"
        "v_exp_f32 v24, v24\n"
        "v_mfma_f32_16x16x32_bf16 v[20:23], v[224:227], v[52:55], v[20:23]\n"
        "v_cvt_pk_bf16_f32 v74, v28, v29\n"
        "v_cvt_pk_bf16_f32 v75, v30, v31\n"
        "v_add_f32 v151, v151, v24\n"
        "v_mfma_f32_16x16x32_bf16 v[64:67], v[236:239], v[52:55], 0\n"
        "v_exp_f32 v25, v25\n"
        "v_mfma_f32_16x16x32_bf16 v[64:67], v[232:235], v[136:139], v[64:67]\n"
        "v_exp_f32 v26, v26\n"
        "v_mfma_f32_16x16x32_bf16 v[64:67], v[228:231], v[132:135], v[64:67]\n"
        "v_cvt_pk_bf16_f32 v48, v20, v21\n"
        "v_cvt_pk_bf16_f32 v49, v22, v23\n"
        "v_mfma_f32_16x16x4_4b_bf16 v[28:43], v[56:57], v[164:165], 0 blgp:5\n"
        "v_exp_f32 v27, v27\n"
        "v_add_f32 v151, v151, v25\n"
        "v_add_f32 v151, v151, v26\n"
        "v_mul_f32 v148, v148, v44\n"
        "v_exp_f32 v148, v148\n"
        "v_cvt_pk_bf16_f32 v50, v64, v65\n"
        "v_cvt_pk_bf16_f32 v51, v66, v67\n"
        "v_mfma_f32_16x16x32_bf16 v[58:61], v[124:127], v[68:71], 0\n"
        "v_add_f32 v151, v151, v27\n"
        "v_add_f32_dpp v167, v167, v167 row_ror:1 row_mask:0xf bank_mask:0xf\n"
        "v_mfma_f32_16x16x32_bf16 v[58:61], v[128:131], v[72:75], v[58:61]\n"
        "v_add_f32_dpp v151, v148, v151 quad_perm:[0,1,2,3] row_mask:0x4 bank_mask:0xf\n"
        "v_mfma_f32_16x16x32_bf16 v[58:61], v[160:163], v[48:51], v[58:61]\n"
        "v_mfma_f32_16x16x32_bf16 v[112:115], v[52:55], v[48:51], 0\n"
        "v_permlane32_swap_b32 v151, v167\n"
        "v_mfma_f32_16x16x32_bf16 v[112:115], v[136:139], v[72:75], v[112:115]\n"
        "v_add_f32 v151, v151, v167\n"
        "v_mov_b32 v167, v151\n"
        "v_mfma_f32_16x16x32_bf16 v[112:115], v[132:135], v[68:71], v[112:115]\n"
        "v_mfma_f32_16x16x32_bf16 v[20:23], v[92:95], v[68:71], 0\n"
        "v_permlane16_swap_b32 v151, v167\n"
        "v_mfma_f32_16x16x32_bf16 v[20:23], v[96:99], v[72:75], v[20:23]\n"
        "v_add_f32 v151, v151, v167\n"
        "v_mul_f32 v58, v58, v28\n"
        "v_mfma_f32_16x16x32_bf16 v[20:23], v[116:119], v[48:51], v[20:23]\n"
        "v_rcp_f32 v123, v151\n"
        "v_mul_f32 v59, v59, v29\n"
        "v_and_b32 v158, s28, v123\n"
        "v_mfma_f32_16x16x32_bf16 v[62:65], v[104:107], v[48:51], 0\n"
        "v_sub_f32 v152, v123, v158\n"
        "v_cvt_pk_bf16_f32 v159, v158, v152\n"
        "v_smfmac_f32_16x16x64_bf16 v[62:65], v[108:111], v[68:75], v4\n"
        "v_min3_f32 v167, v151, v123, v153\n"
        "v_mfma_f32_16x16x32_bf16 v[48:51], v[168:171], v[92:95], 0\n"
        "v_mul_f32 v60, v60, v30\n"
        "v_mul_f32 v61, v61, v31\n"
        "v_mfma_f32_16x16x32_bf16 v[48:51], v[172:175], v[96:99], v[48:51]\n"
        "v_exp_f32 v58, v58\n"
        "v_mfma_f32_16x16x32_bf16 v[48:51], v[176:179], v[116:119], v[48:51]\n"
        "v_exp_f32 v59, v59\n"
        "v_exp_f32 v60, v60\n"
        "v_mfma_f32_16x16x32_bf16 v[68:71], v[188:191], v[116:119], 0\n"
        "v_exp_f32 v61, v61\n"
        "v_mfma_f32_16x16x32_bf16 v[68:71], v[184:187], v[96:99], v[68:71]\n"
        "v_add_f32 v155, v58, v59\n"
        "v_add_f32 v155, v155, v60\n"
        "v_mfma_f32_16x16x32_bf16 v[68:71], v[180:183], v[92:95], v[68:71]\n"
        "v_cvt_pk_bf16_f32 v44, v48, v49\n"
        "v_cvt_pk_bf16_f32 v45, v50, v51\n"
        "v_add_f32 v155, v155, v61\n"
        "v_mfma_f32_16x16x32_bf16 v[28:31], v[192:195], v[92:95], 0\n"
        "v_mul_f32 v112, v112, v32\n"
        "v_mul_f32 v113, v113, v33\n"
        "v_mfma_f32_16x16x32_bf16 v[28:31], v[196:199], v[96:99], v[28:31]\n"
        "v_mul_f32 v114, v114, v34\n"
        "v_cvt_pk_bf16_f32 v46, v68, v69\n"
        "v_mfma_f32_16x16x32_bf16 v[28:31], v[200:203], v[116:119], v[28:31]\n"
        "v_cvt_pk_bf16_f32 v47, v70, v71\n"
        "v_mul_f32 v115, v115, v35\n"
        "v_exp_f32 v112, v112\n"
        "v_mfma_f32_16x16x32_bf16 v[68:71], v[212:215], v[116:119], 0\n"
        "v_exp_f32 v113, v113\n"
        "v_mfma_f32_16x16x32_bf16 v[68:71], v[208:211], v[96:99], v[68:71]\n"
        "v_exp_f32 v114, v114\n"
        "v_mfma_f32_16x16x32_bf16 v[68:71], v[204:207], v[92:95], v[68:71]\n"
        "v_cvt_pk_bf16_f32 v48, v28, v29\n"
        "v_cvt_pk_bf16_f32 v49, v30, v31\n"
        "v_add_f32 v155, v155, v112\n"
        "v_mfma_f32_16x16x32_bf16 v[64:67], v[216:219], v[92:95], 0\n"
        "v_exp_f32 v115, v115\n"
        "v_mfma_f32_16x16x32_bf16 v[64:67], v[220:223], v[96:99], v[64:67]\n"
        "v_add_f32 v155, v155, v113\n"
        "v_add_f32 v155, v155, v114\n"
        "v_mfma_f32_16x16x32_bf16 v[64:67], v[224:227], v[116:119], v[64:67]\n"
        "v_cvt_pk_bf16_f32 v50, v68, v69\n"
        "v_cvt_pk_bf16_f32 v51, v70, v71\n"
        "v_add_f32 v155, v155, v115\n"
        "v_mul_f32 v20, v20, v36\n"
        "v_mfma_f32_16x16x32_bf16 v[28:31], v[236:239], v[116:119], 0\n"
        "v_mul_f32 v21, v21, v37\n"
        "v_mul_f32 v22, v22, v38\n"
        "v_mfma_f32_16x16x32_bf16 v[28:31], v[232:235], v[96:99], v[28:31]\n"
        "v_cvt_pk_bf16_f32 v150, v64, v65\n"
        "v_cvt_pk_bf16_f32 v151, v66, v67\n"
        "v_mfma_f32_16x16x32_bf16 v[28:31], v[228:231], v[92:95], v[28:31]\n"
        "v_exp_f32 v20, v20\n"
        "v_mfma_f32_16x16x4_4b_bf16 v[76:91], v[56:57], v[158:159], 0 blgp:4\n"
        "v_exp_f32 v21, v21\n"
        "v_mul_f32 v23, v23, v39\n"
        "v_exp_f32 v22, v22\n"
        "v_exp_f32 v23, v23\n"
        "v_add_f32 v155, v155, v20\n"
        "v_add_f32 v155, v155, v21\n"
        "v_cvt_pk_bf16_f32 v152, v28, v29\n"
        "v_cvt_pk_bf16_f32 v153, v30, v31\n"
        "v_mfma_f32_16x16x32_bf16 v[36:39], v[124:127], v[44:47], 0\n"
        "v_add_f32 v155, v155, v22\n"
        "v_add_f32 v155, v155, v23\n"
        "v_mfma_f32_16x16x32_bf16 v[36:39], v[128:131], v[48:51], v[36:39]\n"
        "v_mul_f32 v100, v100, v76\n"
        "v_mul_f32 v101, v101, v77\n"
        "v_mfma_f32_16x16x32_bf16 v[36:39], v[160:163], v[150:153], v[36:39]\n"
        "v_mul_f32 v102, v102, v78\n"
        "v_mul_f32 v103, v103, v79\n"
        "v_mul_f32 v144, v144, v80\n"
        "v_mul_f32 v145, v145, v81\n"
        "v_mfma_f32_16x16x32_bf16 v[140:143], v[52:55], v[150:153], 0\n"
        "v_mul_f32 v146, v146, v82\n"
        "v_mul_f32 v147, v147, v83\n"
        "v_mfma_f32_16x16x32_bf16 v[140:143], v[136:139], v[48:51], v[140:143]\n"
        "v_mul_f32 v24, v24, v84\n"
        "v_mul_f32 v25, v25, v85\n"
        "v_mfma_f32_16x16x32_bf16 v[140:143], v[132:135], v[44:47], v[140:143]\n"
        "v_add_f32 v62, v62, v63\n"
        "v_mul_f32 v62, v62, v40\n"
        "v_mul_f32 v26, v26, v86\n"
        "v_mfma_f32_16x16x4_4b_bf16 v[64:79], v[56:57], v[164:165], 0 blgp:6\n"
        "v_exp_f32 v62, v62\n"
        "v_mul_f32 v27, v27, v87\n"
        "v_fma_f32 v148, v148, v88, v19\n"
        "v_add_f32_dpp v155, v62, v155 quad_perm:[0,1,2,3] row_mask:0x4 bank_mask:0xf\n"
        "v_cvt_pk_bf16_f32 v40, v100, v101\n"
        "v_cvt_pk_bf16_f32 v41, v102, v103\n"
        "v_cvt_pk_bf16_f32 v42, v144, v145\n"
        "v_cvt_pk_bf16_f32 v43, v146, v147\n"
        "v_cvt_pk_bf16_f32 v164, v24, v25\n"
        "v_mfma_f32_16x16x32_bf16 v[156:159], v[92:95], v[44:47], 0\n"
        "v_cvt_pk_bf16_f32 v166, v26, v27\n"
        "v_cvt_pk_bf16_f32 v165, v148, 0\n"
        "v_mfma_f32_16x16x32_bf16 v[156:159], v[96:99], v[48:51], v[156:159]\n"
        "v_mul_f32 v36, v36, v64\n"
        "v_mul_f32 v37, v37, v65\n"
        "v_mfma_f32_16x16x32_bf16 v[156:159], v[116:119], v[150:153], v[156:159]\n"
        "v_mul_f32 v38, v38, v66\n"
        "v_mul_f32 v39, v39, v67\n"
        "v_mul_f32 v140, v140, v68\n"
        "v_mfma_f32_16x16x32_bf16 v[92:95], v[104:107], v[150:153], 0\n"
        "ds_read_b128 v[88:91], v17 offset:40064\n"
        "v_mul_f32 v141, v141, v69\n"
        "v_smfmac_f32_16x16x64_bf16 v[92:95], v[108:111], v[44:51], v4\n"
        "ds_read_b128 v[84:87], v17 offset:40128\n"
        "ds_read_b128 v[150:153], v17 offset:40192\n"
        "v_mfma_f32_16x16x32_bf16 v[48:51], v[124:127], v[240:243], 0\n"
        "v_exp_f32 v36, v36\n"
        "v_mfma_f32_16x16x32_bf16 v[48:51], v[128:131], v[244:247], v[48:51]\n"
        "ds_read_b128 v[80:83], v0 offset:5136\n"
        "v_mul_f32 v142, v142, v70\n"
        "v_mfma_f32_16x16x32_bf16 v[48:51], v[160:163], v[248:251], v[48:51]\n"
        "ds_read_b128 v[32:35], v0 offset:4112\n"
        "v_mul_f32 v143, v143, v71\n"
        "v_mfma_f32_16x16x32_bf16 v[24:27], v[52:55], v[248:251], 0\n"
        "v_exp_f32 v37, v37\n"
        "v_mfma_f32_16x16x32_bf16 v[24:27], v[136:139], v[244:247], v[24:27]\n"
        "v_exp_f32 v38, v38\n"
        "v_mfma_f32_16x16x32_bf16 v[24:27], v[132:135], v[240:243], v[24:27]\n"
        "s_waitcnt lgkmcnt(2)\n"
        "v_cvt_pk_bf16_f32 v44, v48, v49\n"
        "v_cvt_pk_bf16_f32 v45, v50, v51\n"
        "v_add_f32 v145, v36, v37\n"
        "v_mfma_f32_16x16x32_bf16 v[104:107], v[88:91], v[240:243], 0\n"
        "v_exp_f32 v39, v39\n"
        "v_mfma_f32_16x16x32_bf16 v[104:107], v[84:87], v[244:247], v[104:107]\n"
        "v_add_f32 v145, v145, v38\n"
        "v_cvt_pk_bf16_f32 v46, v24, v25\n"
        "v_mfma_f32_16x16x32_bf16 v[104:107], v[150:153], v[248:251], v[104:107]\n"
        "s_waitcnt lgkmcnt(0)\n"
        "v_cvt_pk_bf16_f32 v47, v26, v27\n"
        "v_add_f32 v145, v145, v39\n"
        "v_exp_f32 v140, v140\n"
        "v_mfma_f32_16x16x32_bf16 v[64:67], v[150:153], v[80:83], 0\n"
        "v_exp_f32 v141, v141\n"
        "v_mfma_f32_16x16x32_bf16 v[64:67], v[84:87], v[32:35], v[64:67]\n"
        "ds_read_b128 v[146:149], v0 offset:6176\n"
        "v_cvt_pk_bf16_f32 v116, v104, v105\n"
        "v_mfma_f32_16x16x32_bf16 v[64:67], v[88:91], v[252:255], v[64:67]\n"
        "ds_read_b128 v[24:27], v0 offset:7200\n"
        "ds_read_b128 v[108:111], v0 offset:8224\n"
        "v_mfma_f32_16x16x32_bf16 v[100:103], v[132:135], v[252:255], 0\n"
        "v_cvt_pk_bf16_f32 v120, v106, v107\n"
        "v_add_f32 v145, v145, v140\n"
        "v_mfma_f32_16x16x32_bf16 v[100:103], v[136:139], v[32:35], v[100:103]\n"
        "v_exp_f32 v142, v142\n"
        "v_mfma_f32_16x16x32_bf16 v[100:103], v[52:55], v[80:83], v[100:103]\n"
        "v_cvt_pk_bf16_f32 v94, v64, v65\n"
        "v_cvt_pk_bf16_f32 v98, v66, v67\n"
        "v_add_f32 v145, v145, v141\n"
        "v_mfma_f32_16x16x32_bf16 v[68:71], v[160:163], v[80:83], 0\n"
        "v_exp_f32 v143, v143\n"
        "v_mfma_f32_16x16x32_bf16 v[68:71], v[128:131], v[32:35], v[68:71]\n"
        "v_add_f32 v145, v145, v142\n"
        "v_add_f32 v145, v145, v143\n"
        "v_mfma_f32_16x16x32_bf16 v[68:71], v[124:127], v[252:255], v[68:71]\n"
        "s_waitcnt lgkmcnt(0)\n"
        "v_cvt_pk_bf16_f32 v106, v100, v101\n"
        "v_cvt_pk_bf16_f32 v107, v102, v103\n"
        "v_mul_f32 v156, v156, v72\n"
        "v_mul_f32 v157, v157, v73\n"
        "v_mfma_f32_16x16x32_bf16 v[32:35], v[124:127], v[146:149], 0\n"
        "v_mul_f32 v158, v158, v74\n"
        "v_mul_f32 v159, v159, v75\n"
        "v_mfma_f32_16x16x32_bf16 v[32:35], v[128:131], v[24:27], v[32:35]\n"
        "ds_read_b128 v[48:51], v0 offset:11312\n"
        "v_cvt_pk_bf16_f32 v104, v68, v69\n"
        "v_mfma_f32_16x16x32_bf16 v[32:35], v[160:163], v[108:111], v[32:35]\n"
        "ds_read_b128 v[80:83], v0 offset:10288\n"
        "ds_read_b128 v[64:67], v0 offset:9264\n"
        "v_mfma_f32_16x16x32_bf16 v[100:103], v[52:55], v[108:111], 0\n"
        "v_cvt_pk_bf16_f32 v105, v70, v71\n"
        "v_add_f32 v92, v92, v93\n"
        "v_mfma_f32_16x16x32_bf16 v[100:103], v[136:139], v[24:27], v[100:103]\n"
        "v_exp_f32 v156, v156\n"
        "v_mfma_f32_16x16x32_bf16 v[100:103], v[132:135], v[146:149], v[100:103]\n"
        "v_cvt_pk_bf16_f32 v28, v32, v33\n"
        "v_cvt_pk_bf16_f32 v29, v34, v35\n"
        "v_add_f32 v145, v145, v156\n"
        "v_mfma_f32_16x16x32_bf16 v[70:73], v[88:91], v[146:149], 0\n"
        "v_exp_f32 v157, v157\n"
        "v_mfma_f32_16x16x32_bf16 v[70:73], v[84:87], v[24:27], v[70:73]\n"
        "v_exp_f32 v158, v158\n"
        "v_mfma_f32_16x16x32_bf16 v[70:73], v[150:153], v[108:111], v[70:73]\n"
        "s_waitcnt lgkmcnt(0)\n"
        "v_cvt_pk_bf16_f32 v30, v100, v101\n"
        "v_cvt_pk_bf16_f32 v31, v102, v103\n"
        "v_exp_f32 v159, v159\n"
        "v_mfma_f32_16x16x32_bf16 v[32:35], v[150:153], v[48:51], 0\n"
        "v_add_f32 v145, v145, v157\n"
        "v_add_f32 v145, v145, v158\n"
        "v_mfma_f32_16x16x32_bf16 v[32:35], v[84:87], v[80:83], v[32:35]\n"
        "ds_read_b128 v[100:103], v0 offset:12352\n"
        "v_cvt_pk_bf16_f32 v144, v70, v71\n"
        "v_mfma_f32_16x16x32_bf16 v[32:35], v[88:91], v[64:67], v[32:35]\n"
        "ds_read_b128 v[108:111], v0 offset:13376\n"
        "ds_read_b128 v[24:27], v0 offset:14400\n"
        "v_mfma_f32_16x16x32_bf16 v[68:71], v[132:135], v[64:67], 0\n"
        "v_cvt_pk_bf16_f32 v148, v72, v73\n"
        "v_mul_f32 v92, v92, v76\n"
        "v_mfma_f32_16x16x32_bf16 v[68:71], v[136:139], v[80:83], v[68:71]\n"
        "v_add_f32 v145, v145, v159\n"
        "v_readlane_b32 s59, v123, 32\n"
        "v_mfma_f32_16x16x32_bf16 v[68:71], v[52:55], v[48:51], v[68:71]\n"
        "v_cvt_pk_bf16_f32 v117, v32, v33\n"
        "v_cvt_pk_bf16_f32 v121, v34, v35\n"
        "v_exp_f32 v92, v92\n"
        "v_mfma_f32_16x16x32_bf16 v[72:75], v[160:163], v[48:51], 0\n"
        "v_mul_f32 v154, s59, v154\n"
        "v_add_f32_dpp v145, v92, v145 quad_perm:[0,1,2,3] row_mask:0x4 bank_mask:0xf\n"
        "v_mfma_f32_16x16x32_bf16 v[72:75], v[128:131], v[80:83], v[72:75]\n"
        "v_cvt_pk_bf16_f32 v154, v154, 0\n"
        "v_cvt_pk_bf16_f32 v50, v68, v69\n"
        "v_mfma_f32_16x16x32_bf16 v[72:75], v[124:127], v[64:67], v[72:75]\n"
        "s_waitcnt lgkmcnt(0)\n"
        "v_cvt_pk_bf16_f32 v51, v70, v71\n"
        "v_permlane32_swap_b32 v155, v145\n"
        "v_mfma_f32_16x16x32_bf16 v[80:83], v[124:127], v[100:103], 0\n"
        "v_add_f32 v155, v155, v145\n"
        "v_mov_b32 v149, v155\n"
        "v_mfma_f32_16x16x32_bf16 v[80:83], v[128:131], v[108:111], v[80:83]\n"
        "ds_read_b128 v[32:35], v0 offset:17488\n"
        "v_cvt_pk_bf16_f32 v48, v72, v73\n"
        "v_mfma_f32_16x16x32_bf16 v[80:83], v[160:163], v[24:27], v[80:83]\n"
        "ds_read_b128 v[68:71], v0 offset:16464\n"
        "ds_read_b128 v[64:67], v0 offset:15440\n"
        "v_mfma_f32_16x16x32_bf16 v[76:79], v[52:55], v[24:27], 0\n"
        "v_cvt_pk_bf16_f32 v49, v74, v75\n"
        "v_mfma_f32_16x16x32_bf16 v[76:79], v[136:139], v[108:111], v[76:79]\n"
        "v_permlane16_swap_b32 v155, v149\n"
        "v_mfma_f32_16x16x32_bf16 v[76:79], v[132:135], v[100:103], v[76:79]\n"
        "v_add_f32 v155, v155, v149\n"
        "v_rcp_f32 v147, v155\n"
        "v_mfma_f32_16x16x32_bf16 v[72:75], v[88:91], v[100:103], 0\n"
        "ds_read2_b32 v[122:123], v11 offset0:0 offset1:3\n"
        "v_min3_f32 v167, v167, v155, v147\n"
        "v_mfma_f32_16x16x32_bf16 v[72:75], v[84:87], v[108:111], v[72:75]\n"
        "ds_read_b64_tr_b16 v[118:119], v9 offset:40064\n"
        "v_and_b32 v102, s28, v147\n"
        "v_mfma_f32_16x16x32_bf16 v[72:75], v[150:153], v[24:27], v[72:75]\n"
        "s_waitcnt lgkmcnt(2)\n"
        "ds_read2_b32 v[100:101], v11 offset0:1 offset1:4\n"
        "v_sub_f32 v149, v147, v102\n"
        "v_cvt_pk_bf16_f32 v103, v102, v149\n"
        "v_cvt_pk_bf16_f32 v108, v80, v81\n"
        "v_mfma_f32_16x16x32_bf16 v[24:27], v[150:153], v[32:35], 0\n"
        "ds_read_b64_tr_b16 v[96:97], v9 offset:40080\n"
        "v_cvt_pk_bf16_f32 v109, v82, v83\n"
        "v_mfma_f32_16x16x32_bf16 v[24:27], v[84:87], v[68:71], v[24:27]\n"
        "ds_read2_b32 v[150:151], v11 offset0:2 offset1:5\n"
        "v_cvt_pk_bf16_f32 v110, v76, v77\n"
        "v_mfma_f32_16x16x32_bf16 v[24:27], v[88:91], v[64:67], v[24:27]\n"
        "ds_read_b64_tr_b16 v[146:147], v9 offset:40096\n"
        "v_cvt_pk_bf16_f32 v111, v78, v79\n"
        "v_cvt_pk_bf16_f32 v95, v72, v73\n"
        "v_mfma_f32_16x16x4_4b_bf16 v[76:91], v[56:57], v[102:103], 0 blgp:4\n"
        "v_cvt_pk_bf16_f32 v99, v74, v75\n"
        "v_cmp_nle_f32 vcc, 0x2b800000, v167\n"
        "v_mov_b32_dpp v167, v19 row_shr:4 row_mask:0xf bank_mask:0xf bound_ctrl:0\n"
        "ds_write_b16 v1, v154\n"
        "v_cvt_pk_bf16_f32 v145, v24, v25\n"
        "v_cvt_pk_bf16_f32 v149, v26, v27\n"
        "s_andn2_b64 vcc, vcc, s[56:57]\n"
        "s_cbranch_scc1 CREDOB_%=\n"
        "v_mfma_f32_16x16x32_bf16 v[24:27], v[132:135], v[64:67], 0\n"
        "s_mov_b32 exec_hi, 0xffff\n"
        "v_add_u32 v10, -40064, v10\n"
        "s_mov_b32 exec_hi, -1\n"
        "v_mfma_f32_16x16x32_bf16 v[24:27], v[136:139], v[68:71], v[24:27]\n"
        "v_mul_f32 v58, v58, v76\n"
        "v_mul_f32 v59, v59, v77\n"
        "v_mfma_f32_16x16x32_bf16 v[24:27], v[52:55], v[32:35], v[24:27]\n"
        "s_add_u32 s31, s31, 1\n"
        "v_mul_f32 v60, v60, v78\n"
        "v_mul_f32 v61, v61, v79\n"
        "v_mul_f32 v112, v112, v80\n"
        "v_mul_f32 v113, v113, v81\n"
        "v_mfma_f32_16x16x32_bf16 v[52:55], v[160:163], v[32:35], 0\n"
        "s_cmp_ge_u32 s31, s71\n"
        "v_mul_f32 v114, v114, v82\n"
        "v_mul_f32 v115, v115, v83\n"
        "v_mfma_f32_16x16x32_bf16 v[52:55], v[128:131], v[68:71], v[52:55]\n"
        "s_cbranch_scc1 L34B_%=\n"
        "L35B_%=:\n"
        "v_mul_f32 v20, v20, v84\n"
        "v_cvt_pk_bf16_f32 v34, v24, v25\n"
        "v_mfma_f32_16x16x32_bf16 v[52:55], v[124:127], v[64:67], v[52:55]\n"
        "s_add_u32 s16, s16, 256\n"
        "v_cvt_pk_bf16_f32 v35, v26, v27\n"
        "v_mul_f32 v21, v21, v85\n"
        "v_mfma_f32_16x16x32_bf16 v[124:127], v[40:43], v[44:47], 0\n"
        "s_waitcnt lgkmcnt(0)\n"
        "v_mul_f32 v22, v22, v86\n"
        "v_mul_f32 v23, v23, v87\n"
        "v_smfmac_f32_16x16x64_bf16 v[124:127], v[164:167], v[116:123], v18\n"
        "s_cmp_ge_u32 s31, 20\n"
        "s_cselect_b32 s39, s22, s16\n"
        "ds_read_b64_tr_b16 v[118:119], v9 offset:40112\n"
        "v_mfma_f32_16x16x4_4b_bf16 v[72:87], v[56:57], v[102:103], 0 blgp:6\n"
        "v_cvt_pk_bf16_f32 v32, v52, v53\n"
        "v_cvt_pk_bf16_f32 v33, v54, v55\n"
        "v_fma_f32 v62, v62, v88, v19\n"
        "v_cvt_pk_bf16_f32 v68, v58, v59\n"
        "v_cvt_pk_bf16_f32 v69, v60, v61\n"
        "v_cvt_pk_bf16_f32 v70, v112, v113\n"
        "v_cvt_pk_bf16_f32 v71, v114, v115\n"
        "v_cvt_pk_bf16_f32 v24, v20, v21\n"
        "v_cvt_pk_bf16_f32 v26, v22, v23\n"
        "v_mfma_f32_16x16x32_bf16 v[88:91], v[40:43], v[104:107], 0\n"
        "s_cmp_ge_u32 s31, 21\n"
        "v_cvt_pk_bf16_f32 v25, v62, 0\n"
        "v_mul_f32 v36, v36, v72\n"
        "v_smfmac_f32_16x16x64_bf16 v[88:91], v[164:167], v[94:101], v18\n"
        "s_cselect_b32 s39, s23, s39\n"
        "ds_read_b64_tr_b16 v[96:97], v9 offset:40128\n"
        "v_mfma_f32_16x16x32_bf16 v[56:59], v[40:43], v[28:31], 0\n"
        "s_mul_hi_u32 s38, s39, 58835169\n"
        "ds_read_b128 v[128:131], v2\n"
        "v_mul_f32 v37, v37, v73\n"
        "v_smfmac_f32_16x16x64_bf16 v[56:59], v[164:167], v[144:151], v18\n"
        "s_mul_i32 s39, s39, 1344\n"
        "ds_read_b64_tr_b16 v[146:147], v9 offset:40144\n"
        "ds_read_b128 v[52:55], v3\n"
        "v_mfma_f32_16x16x32_bf16 v[112:115], v[40:43], v[48:51], 0\n"
        "s_waitcnt lgkmcnt(4)\n"
        "v_mul_f32 v38, v38, v74\n"
        "v_mul_f32 v39, v39, v75\n"
        "v_smfmac_f32_16x16x64_bf16 v[112:115], v[164:167], v[116:123], v18 abid:1\n"
        "s_mul_i32 s38, s38, 588672\n"
        "s_add_u32 s39, s39, s38\n"
        "v_mul_f32 v140, v140, v76\n"
        "v_cvt_pk_bf16_f32 v152, v124, v88\n"
        "v_mul_f32 v141, v141, v77\n"
        "v_mfma_f32_16x16x32_bf16 v[72:75], v[40:43], v[108:111], 0\n"
        "s_add_u32 s39, s39, s17\n"
        "v_mov_b64 v[60:61], 0\n"
        "s_waitcnt lgkmcnt(3)\n"
        "v_smfmac_f32_16x16x64_bf16 v[72:75], v[164:167], v[94:101], v18 abid:1\n"
        "s_mul_i32 s68, s31, 3\n"
        "v_mov_b64 v[62:63], 0\n"
        "v_cvt_pk_bf16_f32 v153, v56, v112\n"
        "v_mfma_f32_16x16x32_bf16 v[64:67], v[40:43], v[32:35], 0\n"
        "s_waitcnt lgkmcnt(1)\n"
        "v_mov_b64 v[20:21], 0\n"
        "v_mov_b64 v[22:23], 0\n"
        "v_smfmac_f32_16x16x64_bf16 v[64:67], v[164:167], v[144:151], v18 abid:1\n"
        "s_waitcnt lgkmcnt(0)\n"
        "s_bitcmp1_b32 s31, 0\n"
        "v_mov_b32_dpp v27, v19 row_shr:4 row_mask:0xf bank_mask:0xf bound_ctrl:0\n"
        "s_cselect_b32 s38, 3, 6\n"
        "v_smfmac_f32_16x16x64_bf16 v[60:63], v[128:131], v[44:51], v4\n"
        "s_add_u32 s68, s68, s38\n"
        "v_mov_b64 v[40:41], 0\n"
        "v_smfmac_f32_16x16x64_bf16 v[60:63], v[52:55], v[116:123], v4 abid:1\n"
        "s_add_u32 s37, s35, 0\n"
        "s_cmp_lt_u32 s31, s14\n"
        "ds_read_b64_tr_b16 v[118:119], v9 offset:43392\n"
        "v_mov_b64 v[42:43], 0\n"
        "v_smfmac_f32_16x16x64_bf16 v[20:23], v[128:131], v[104:111], v4\n"
        "s_cselect_b32 s68, s68, 0\n"
        "v_cvt_pk_bf16_f32 v154, v72, v64\n"
        "v_smfmac_f32_16x16x64_bf16 v[20:23], v[52:55], v[94:101], v4 abid:1\n"
        "s_and_b32 s38, s31, 7\n"
        "ds_read_b64_tr_b16 v[96:97], v9 offset:43408\n"
        "v_cvt_pk_bf16_f32 v160, v125, v89\n"
        "v_smfmac_f32_16x16x64_bf16 v[40:43], v[128:131], v[28:35], v4\n"
        "s_cselect_b32 s30, 1, -7\n"
        "v_cvt_pk_bf16_f32 v88, v126, v90\n"
        "v_smfmac_f32_16x16x64_bf16 v[40:43], v[52:55], v[144:151], v4 abid:1\n"
        "buffer_store_dwordx3 v[152:154], v12, s[8:11], s37 offen sc0 sc1 nt\n"
        "ds_read_b64_tr_b16 v[146:147], v9 offset:43424\n"
        "v_cvt_pk_bf16_f32 v164, v127, v91\n"
        "v_cvt_pk_bf16_f32 v161, v57, v113\n"
        "v_cvt_pk_bf16_f32 v89, v58, v114\n"
        "v_cvt_pk_bf16_f32 v165, v59, v115\n"
        "v_cvt_pk_bf16_f32 v162, v73, v65\n"
        "v_mfma_f32_16x16x32_bf16 v[152:155], v[68:71], v[44:47], 0\n"
        "s_waitcnt lgkmcnt(2)\n"
        "v_cvt_pk_bf16_f32 v90, v74, v66\n"
        "v_cvt_pk_bf16_f32 v166, v75, v67\n"
        "v_smfmac_f32_16x16x64_bf16 v[152:155], v[24:27], v[116:123], v18\n"
        "s_mul_i32 s29, s30, 192\n"
        "s_mul_i32 s30, s30, 392\n"
        "ds_read_b64_tr_b16 v[118:119], v9 offset:43440\n"
        "v_add_f32 v60, v60, v61\n"
        "v_add_f32 v62, v62, v63\n"
        "v_add_f32 v20, v20, v21\n"
        "v_mfma_f32_16x16x32_bf16 v[56:59], v[68:71], v[104:107], 0\n"
        "s_waitcnt lgkmcnt(2)\n"
        "v_add_f32 v22, v22, v23\n"
        "v_cvt_pk_bf16_f32 v112, v60, v20\n"
        "v_smfmac_f32_16x16x64_bf16 v[56:59], v[24:27], v[94:101], v18\n"
        "s_add_u32 s37, s35, 0\n"
        "s_cmp_eq_u32 s69, s18\n"
        "ds_read_b64_tr_b16 v[96:97], v9 offset:43456\n"
        "v_add_f32 v40, v40, v41\n"
        "v_add_f32 v42, v42, v43\n"
        "v_cvt_pk_bf16_f32 v113, v40, v62\n"
        "v_mfma_f32_16x16x32_bf16 v[72:75], v[68:71], v[28:31], 0\n"
        "s_waitcnt lgkmcnt(2)\n"
        "v_cvt_pk_bf16_f32 v114, v22, v42\n"
        "v_mul_f32 v142, v142, v78\n"
        "v_smfmac_f32_16x16x64_bf16 v[72:75], v[24:27], v[144:151], v18\n"
        "buffer_store_dwordx3 v[160:162], v15, s[8:11], s37 offen offset:768 sc0 sc1 nt\n"
        "ds_read_b64_tr_b16 v[146:147], v9 offset:43472\n"
        "s_cselect_b32 s34, -7488, s70\n"
        "v_mul_f32 v143, v143, v79\n"
        "v_mul_f32 v156, v156, v80\n"
        "v_mul_f32 v157, v157, v81\n"
        "v_mul_f32 v158, v158, v82\n"
        "v_mfma_f32_16x16x32_bf16 v[20:23], v[68:71], v[48:51], 0\n"
        "s_waitcnt lgkmcnt(2)\n"
        "s_add_u32 s69, s69, s34\n"
        "v_mul_f32 v159, v159, v83\n"
        "v_smfmac_f32_16x16x64_bf16 v[20:23], v[24:27], v[116:123], v18 abid:1\n"
        "s_add_u32 s37, s35, 98112\n"
        "ds_read_b64_tr_b16 v[118:119], v9 offset:46720\n"
        "v_fma_f32 v92, v92, v84, v19\n"
        "v_mfma_f32_16x16x32_bf16 v[84:87], v[68:71], v[108:111], 0\n"
        "s_waitcnt lgkmcnt(2)\n"
        "v_cvt_pk_bf16_f32 v80, v36, v37\n"
        "v_cvt_pk_bf16_f32 v81, v38, v39\n"
        "v_smfmac_f32_16x16x64_bf16 v[84:87], v[24:27], v[94:101], v18 abid:1\n"
        "ds_read_b64_tr_b16 v[96:97], v9 offset:46736\n"
        "v_cvt_pk_bf16_f32 v82, v140, v141\n"
        "v_mfma_f32_16x16x32_bf16 v[60:63], v[68:71], v[32:35], 0\n"
        "s_waitcnt lgkmcnt(2)\n"
        "v_cvt_pk_bf16_f32 v83, v142, v143\n"
        "v_cvt_pk_bf16_f32 v76, v156, v157\n"
        "v_smfmac_f32_16x16x64_bf16 v[60:63], v[24:27], v[144:151], v18 abid:1\n"
        "buffer_store_dwordx3 v[88:90], v12, s[8:11], s37 offen offset:192 sc0 sc1 nt\n"
        "ds_read_b64_tr_b16 v[146:147], v9 offset:46752\n"
        "ds_read_b32 v167, v0 offset:21600\n"
        "v_cvt_pk_bf16_f32 v78, v158, v159\n"
        "v_cvt_pk_bf16_f32 v77, v92, 0\n"
        "v_mov_b32_dpp v79, v19 row_shr:4 row_mask:0xf bank_mask:0xf bound_ctrl:0\n"
        "v_mfma_f32_16x16x32_bf16 v[68:71], v[80:83], v[44:47], 0\n"
        "s_waitcnt lgkmcnt(3)\n"
        "v_writelane_b32 v10, s69, 56\n"
        "v_add_u32 v11, s30, v11\n"
        "v_smfmac_f32_16x16x64_bf16 v[68:71], v[76:79], v[116:123], v18\n"
        "s_add_u32 s37, s35, 98112\n"
        "ds_read_b64_tr_b16 v[118:119], v9 offset:46768\n"
        "v_mfma_f32_16x16x32_bf16 v[36:39], v[80:83], v[104:107], 0\n"
        "s_waitcnt lgkmcnt(3)\n"
        "v_smfmac_f32_16x16x64_bf16 v[36:39], v[76:79], v[94:101], v18\n"
        "ds_read_b64_tr_b16 v[96:97], v9 offset:46784\n"
        "v_mfma_f32_16x16x32_bf16 v[40:43], v[80:83], v[28:31], 0\n"
        "s_waitcnt lgkmcnt(3)\n"
        "s_waitcnt lgkmcnt(2)\n"
        "v_readfirstlane_b32 s40, v167\n"
        "v_smfmac_f32_16x16x64_bf16 v[40:43], v[76:79], v[144:151], v18\n"
        "buffer_store_dwordx3 v[164:166], v14, s[8:11], s37 offen offset:960 sc0 sc1 nt\n"
        "ds_read_b64_tr_b16 v[146:147], v9 offset:46800\n"
        "s_add_u32 s37, s35, 588672\n"
        "s_add_u32 s46, s37, 1344\n"
        "s_cmp_lt_i32 s40, s68\n"
        "s_mov_b64 exec, 1\n"
        "ds_inc_u32 v0, v1 offset:21612\n"
        "s_mov_b64 exec, -1\n"
        "s_cbranch_scc1 L38B_%=\n"
        "L39B_%=:\n"
        "ds_read_b128 v[124:127], v7 offset:0\n"
        "v_mfma_f32_16x16x32_bf16 v[64:67], v[80:83], v[48:51], 0\n"
        "s_waitcnt lgkmcnt(4)\n"
        "ds_read_b128 v[132:135], v7 offset:3328\n"
        "v_smfmac_f32_16x16x64_bf16 v[64:67], v[76:79], v[116:123], v18 abid:1\n"
        "buffer_store_dwordx3 v[112:114], v12, s[44:47], s37 offen offset:1152 sc0 sc1 nt\n"
        "ds_read_b128 v[128:131], v7 offset:64\n"
        "ds_read_b128 v[136:139], v7 offset:3392\n"
        "ds_read_b128 v[160:163], v7 offset:128\n"
        "ds_read_b128 v[52:55], v7 offset:3456\n"
        "v_mfma_f32_16x16x32_bf16 v[104:107], v[80:83], v[108:111], 0\n"
        "s_waitcnt lgkmcnt(7)\n"
        "s_mov_b32 s36, s35\n"
        "v_smfmac_f32_16x16x64_bf16 v[104:107], v[76:79], v[94:101], v18 abid:1\n"
        "s_mov_b64 exec, s[62:63]\n"
        "v_add_u32 v16, s29, v16\n"
        "v_add_u32 v8, s34, v8\n"
        "s_mov_b64 exec, -1\n"
        "v_mfma_f32_16x16x32_bf16 v[88:91], v[80:83], v[32:35], 0\n"
        "s_mov_b32 s35, s39\n"
        "v_smfmac_f32_16x16x64_bf16 v[88:91], v[76:79], v[144:151], v18 abid:1\n"
        "s_mov_b64 exec, s[50:51]\n"
        "v_add_u32 v5, s29, v5\n"
        "v_add_u32 v6, s34, v6\n"
        "s_mov_b64 exec, -1\n"
        "s_setprio 0\n"
        "CNEXTB_%=:\n"
        "s_cmp_ge_u32 s31, s14\n"
        "s_cbranch_scc1 CEND_%=\n"
        "s_cmp_gt_i32 s29, 0\n"
        "s_cbranch_scc1 CTOP_%=\n"
        "s_branch CFULL_%=\n"
        "CEND_%=:\n"
        "s_waitcnt lgkmcnt(0)\n"
        "s_nop 15\n"
        "v_cvt_pk_bf16_f32 v248, v152, v56\n"
        "v_cvt_pk_bf16_f32 v249, v72, v20\n"
        "v_cvt_pk_bf16_f32 v250, v84, v60\n"
        "s_add_u32 s37, s36, 196224\n"
        "buffer_store_dwordx3 v[248:250], v12, s[8:11], s37 offen offset:384 sc0 sc1 nt\n"
        "v_cvt_pk_bf16_f32 v252, v153, v57\n"
        "v_cvt_pk_bf16_f32 v253, v73, v21\n"
        "v_cvt_pk_bf16_f32 v254, v85, v61\n"
        "s_add_u32 s37, s36, 196224\n"
        "buffer_store_dwordx3 v[252:254], v13, s[8:11], s37 offen offset:1152 sc0 sc1 nt\n"
        "v_cvt_pk_bf16_f32 v248, v154, v58\n"
        "v_cvt_pk_bf16_f32 v249, v74, v22\n"
        "v_cvt_pk_bf16_f32 v250, v86, v62\n"
        "s_add_u32 s37, s36, 294336\n"
        "buffer_store_dwordx3 v[248:250], v12, s[8:11], s37 offen offset:576 sc0 sc1 nt\n"
        "v_cvt_pk_bf16_f32 v252, v155, v59\n"
        "v_cvt_pk_bf16_f32 v253, v75, v23\n"
        "v_cvt_pk_bf16_f32 v254, v87, v63\n"
        "s_add_u32 s37, s36, 392448\n"
        "buffer_store_dwordx3 v[252:254], v12, s[8:11], s37 offen sc0 sc1 nt\n"
        "v_cvt_pk_bf16_f32 v248, v68, v36\n"
        "v_cvt_pk_bf16_f32 v249, v40, v64\n"
        "v_cvt_pk_bf16_f32 v250, v104, v88\n"
        "s_add_u32 s37, s36, 392448\n"
        "v_cvt_pk_bf16_f32 v252, v69, v37\n"
        "v_cvt_pk_bf16_f32 v253, v41, v65\n"
        "v_cvt_pk_bf16_f32 v254, v105, v89\n"
        "buffer_store_dwordx3 v[248:250], v15, s[8:11], s37 offen offset:768 sc0 sc1 nt\n"
        "s_add_u32 s37, s36, 490560\n"
        "v_cvt_pk_bf16_f32 v246, v70, v38\n"
        "v_cvt_pk_bf16_f32 v247, v42, v66\n"
        "v_cvt_pk_bf16_f32 v248, v106, v90\n"
        "buffer_store_dwordx3 v[252:254], v12, s[8:11], s37 offen offset:192 sc0 sc1 nt\n"
        "s_add_u32 s37, s36, 490560\n"
        "buffer_store_dwordx3 v[246:248], v14, s[8:11], s37 offen offset:960 sc0 sc1 nt\n"
        "s_add_u32 s37, s36, 588672\n"
        "v_cvt_pk_bf16_f32 v252, v71, v39\n"
        "v_cvt_pk_bf16_f32 v253, v43, v67\n"
        "v_cvt_pk_bf16_f32 v254, v107, v91\n"
        "buffer_store_dwordx3 v[252:254], v12, s[8:11], s37 offen offset:384 sc0 sc1 nt\n"
        "s_nop 1\n"
        "s_endpgm\n"
        "L34A_%=:\n"
        "s_nop 7\n"
        "v_mov_b32 v153, 21600\n"
        "L32A_%=:\n"
        "ds_read_b32 v163, v153 offset:64\n"
        "s_waitcnt lgkmcnt(0)\n"
        "v_readfirstlane_b32 s40, v163\n"
        "s_nop 3\n"
        "s_cmp_ge_i32 s40, 1\n"
        "s_cbranch_scc1 L33A_%=\n"
        "s_sleep 1\n"
        "s_sub_u32 s41, s41, 1\n"
        "s_cbranch_scc0 L32A_%=\n"
        "s_endpgm\n"
        "L33A_%=:\n"
        "v_mbcnt_lo_u32_b32 v163, -1, 0\n"
        "v_lshl_add_u32 v153, v163, 2, v153\n"
        "ds_read_b32 v163, v153 offset:68\n"
        "s_mov_b32 s14, s40\n"
        "s_mov_b32 s71, -1\n"
        "s_waitcnt lgkmcnt(0)\n"
        "v_readlane_b32 s22, v163, 0\n"
        "v_readlane_b32 s23, v163, 1\n"
        "s_nop 3\n"
        "s_nop 1\n"
        "s_branch L35A_%=\n"
        "L38A_%=:\n"
        "s_nop 7\n"
        "L36A_%=:\n"
        "ds_read_b32 v167, v0 offset:21604\n"
        "s_waitcnt lgkmcnt(0)\n"
        "v_readfirstlane_b32 s40, v167\n"
        "s_nop 3\n"
        "s_cmp_ge_i32 s40, s68\n"
        "s_cbranch_scc1 L37A_%=\n"
        "s_sleep 1\n"
        "s_sub_u32 s41, s41, 1\n"
        "s_cbranch_scc0 L36A_%=\n"
        "s_endpgm\n"
        "L37A_%=:\n"
        "s_nop 1\n"
        "s_branch L39A_%=\n"
        "L34B_%=:\n"
        "s_nop 7\n"
        "v_mov_b32 v153, 21600\n"
        "L32B_%=:\n"
        "ds_read_b32 v163, v153 offset:64\n"
        "s_waitcnt lgkmcnt(0)\n"
        "v_readfirstlane_b32 s40, v163\n"
        "s_nop 3\n"
        "s_cmp_ge_i32 s40, 1\n"
        "s_cbranch_scc1 L33B_%=\n"
        "s_sleep 1\n"
        "s_sub_u32 s41, s41, 1\n"
        "s_cbranch_scc0 L32B_%=\n"
        "s_endpgm\n"
        "L33B_%=:\n"
        "v_mbcnt_lo_u32_b32 v163, -1, 0\n"
        "v_lshl_add_u32 v153, v163, 2, v153\n"
        "ds_read_b32 v163, v153 offset:68\n"
        "s_mov_b32 s14, s40\n"
        "s_mov_b32 s71, -1\n"
        "s_waitcnt lgkmcnt(0)\n"
        "v_readlane_b32 s22, v163, 0\n"
        "v_readlane_b32 s23, v163, 1\n"
        "s_nop 3\n"
        "s_nop 1\n"
        "s_branch L35B_%=\n"
        "L38B_%=:\n"
        "s_nop 7\n"
        "L36B_%=:\n"
        "ds_read_b32 v167, v0 offset:21600\n"
        "s_waitcnt lgkmcnt(0)\n"
        "v_readfirstlane_b32 s40, v167\n"
        "s_nop 3\n"
        "s_cmp_ge_i32 s40, s68\n"
        "s_cbranch_scc1 L37B_%=\n"
        "s_sleep 1\n"
        "s_sub_u32 s41, s41, 1\n"
        "s_cbranch_scc0 L36B_%=\n"
        "s_endpgm\n"
        "L37B_%=:\n"
        "s_nop 1\n"
        "s_branch L39B_%=\n"
        "CGRP_%=:\n"
        "s_setprio 3\n"
        "CGRQ_%=:\n"
        "s_waitcnt lgkmcnt(0)\n"
        "s_nop 6\n"
        "ds_read_u16 v167, v0 offset:20576\n"
        "s_lshr_b32 s61, s31, 3\n"
        "s_add_u32 s61, s61, 1\n"
        "L40_%=:\n"
        "ds_read_b32 v166, v0 offset:21660\n"
        "s_waitcnt lgkmcnt(0)\n"
        "v_readfirstlane_b32 s40, v166\n"
        "s_nop 3\n"
        "s_cmp_ge_i32 s40, s61\n"
        "s_cbranch_scc1 L41_%=\n"
        "s_sleep 1\n"
        "s_sub_u32 s41, s41, 1\n"
        "s_cbranch_scc0 L40_%=\n"
        "s_endpgm\n"
        "L41_%=:\n"
        "s_sub_u32 s60, s69, 102048\n"
        "v_add_u32 v167, s60, v167\n"
        "v_subrev_u32 v165, 8320, v167\n"
        "v_min_u32 v167, v167, v165\n"
        "v_add_u32 v167, 101856, v167\n"
        "ds_read_b128 v[28:31], v167 offset:64\n"
        "ds_read_b128 v[24:27], v167 offset:128\n"
        "ds_read_b128 v[32:35], v167\n"
        "s_waitcnt lgkmcnt(1)\n"
        "s_waitcnt lgkmcnt(0)\n"
        "v_mfma_f32_16x16x32_bf16 v[44:47], v[168:171], v[32:35], 0\n"
        "ds_read_u16 v162, v0 offset:20578\n"
        "v_lshrrev_b32 v159, 8, v0\n"
        "v_mfma_f32_16x16x32_bf16 v[44:47], v[172:175], v[28:31], v[44:47]\n"
        "ds_read_u16 v163, v0 offset:20580\n"
        "v_mfma_f32_16x16x32_bf16 v[44:47], v[176:179], v[24:27], v[44:47]\n"
        "ds_read_u16 v167, v0 offset:20582\n"
        "ds_read_u16 v158, v0 offset:20588\n"
        "ds_read_u16 v166, v0 offset:20590\n"
        "v_mfma_f32_16x16x32_bf16 v[52:55], v[180:183], v[32:35], 0\n"
        "v_mfma_f32_16x16x32_bf16 v[52:55], v[184:187], v[28:31], v[52:55]\n"
        "v_mfma_f32_16x16x32_bf16 v[52:55], v[188:191], v[24:27], v[52:55]\n"
        "v_mfma_f32_16x16x32_bf16 v[48:51], v[192:195], v[32:35], 0\n"
        "s_waitcnt lgkmcnt(4)\n"
        "v_add_u32 v162, s60, v162\n"
        "v_subrev_u32 v165, 8320, v162\n"
        "v_mfma_f32_16x16x32_bf16 v[48:51], v[196:199], v[28:31], v[48:51]\n"
        "v_min_u32 v162, v162, v165\n"
        "v_add_u32 v162, 101856, v162\n"
        "v_mfma_f32_16x16x32_bf16 v[48:51], v[200:203], v[24:27], v[48:51]\n"
        "ds_read_b64 v[160:161], v162\n"
        "v_cvt_pk_bf16_f32 v76, v44, v45\n"
        "v_cvt_pk_bf16_f32 v77, v46, v47\n"
        "v_cvt_pk_bf16_f32 v78, v52, v53\n"
        "v_cvt_pk_bf16_f32 v79, v54, v55\n"
        "s_waitcnt lgkmcnt(1)\n"
        "v_add_u32 v163, s19, v163\n"
        "v_mfma_f32_16x16x32_bf16 v[44:47], v[204:207], v[32:35], 0\n"
        "v_cvt_pk_bf16_f32 v80, v48, v49\n"
        "v_cvt_pk_bf16_f32 v81, v50, v51\n"
        "v_mfma_f32_16x16x32_bf16 v[44:47], v[208:211], v[28:31], v[44:47]\n"
        "ds_read_b64 v[164:165], v158\n"
        "v_mad_i32_i24 v159, v159, -8, v163\n"
        "v_mfma_f32_16x16x32_bf16 v[44:47], v[212:215], v[24:27], v[44:47]\n"
        "ds_read_b64 v[156:157], v162 offset:16\n"
        "v_add_u32 v166, s19, v166\n"
        "v_add_u32 v167, s19, v167\n"
        "v_mfma_f32_16x16x32_bf16 v[52:55], v[216:219], v[32:35], 0\n"
        "ds_read_b64 v[150:151], v162 offset:32\n"
        "v_mfma_f32_16x16x32_bf16 v[52:55], v[220:223], v[28:31], v[52:55]\n"
        "v_mfma_f32_16x16x32_bf16 v[52:55], v[224:227], v[24:27], v[52:55]\n"
        "ds_write2_b32 v159, v76, v78 offset0:0 offset1:1\n"
        "v_cvt_pk_bf16_f32 v82, v44, v45\n"
        "v_cvt_pk_bf16_f32 v83, v46, v47\n"
        "v_mfma_f32_16x16x32_bf16 v[44:47], v[228:231], v[32:35], 0\n"
        "v_mfma_f32_16x16x32_bf16 v[44:47], v[232:235], v[28:31], v[44:47]\n"
        "v_mfma_f32_16x16x32_bf16 v[44:47], v[236:239], v[24:27], v[44:47]\n"
        "s_waitcnt lgkmcnt(2)\n"
        "ds_write2_b32 v159, v77, v79 offset0:16 offset1:17\n"
        "v_cvt_pk_bf16_f32 v48, v52, v53\n"
        "v_cvt_pk_bf16_f32 v49, v54, v55\n"
        "v_mfma_f32_4x4x4_16b_bf16 v[96:99], v[160:161], v[164:165], 0\n"
        "v_mfma_f32_4x4x4_16b_bf16 v[92:95], v[156:157], v[164:165], 0\n"
        "s_waitcnt lgkmcnt(2)\n"
        "v_mfma_f32_4x4x4_16b_bf16 v[52:55], v[150:151], v[164:165], 0\n"
        "v_mov_b32 v100, 0x3f803f80\n"
        "v_mov_b32 v101, 0x3f803f80\n"
        "v_mov_b32 v102, 0x3f803f80\n"
        "v_mov_b32 v103, 0x3f803f80\n"
        "v_cvt_pk_bf16_f32 v50, v44, v45\n"
        "v_mfma_f32_16x16x32_bf16 v[124:127], v[32:35], v[240:243], 0\n"
        "v_cvt_pk_bf16_f32 v51, v46, v47\n"
        "v_mfma_f32_16x16x32_bf16 v[124:127], v[28:31], v[244:247], v[124:127]\n"
        "ds_read_b128 v[108:111], v0 offset:4112\n"
        "v_mfma_f32_16x16x32_bf16 v[124:127], v[24:27], v[248:251], v[124:127]\n"
        "ds_read_b128 v[112:115], v0 offset:5136\n"
        "ds_write2_b32 v159, v80, v82 offset0:8 offset1:9\n"
        "v_mfma_f32_16x16x32_bf16 v[44:47], v[100:103], v[32:35], 0\n"
        "v_mfma_f32_16x16x32_bf16 v[44:47], v[100:103], v[28:31], v[44:47]\n"
        "ds_read_b128 v[120:123], v0 offset:6176\n"
        "v_mfma_f32_16x16x32_bf16 v[44:47], v[100:103], v[24:27], v[44:47]\n"
        "ds_read_b128 v[116:119], v0 offset:7200\n"
        "ds_read_b128 v[100:103], v0 offset:8224\n"
        "ds_read_b64 v[160:161], v162 offset:48\n"
        "v_cvt_pk_bf16_f32 v158, v124, v96\n"
        "v_cvt_pk_bf16_f32 v156, v125, v97\n"
        "v_cvt_pk_bf16_f32 v150, v126, v98\n"
        "v_cvt_pk_bf16_f32 v148, v127, v99\n"
        "v_mfma_f32_16x16x32_bf16 v[128:131], v[32:35], v[76:79], 0\n"
        "v_mfma_f32_16x16x32_bf16 v[128:131], v[28:31], v[80:83], v[128:131]\n"
        "ds_read_b64 v[146:147], v162 offset:64\n"
        "v_mfma_f32_16x16x32_bf16 v[128:131], v[24:27], v[48:51], v[128:131]\n"
        "s_waitcnt lgkmcnt(5)\n"
        "ds_write2_b32 v159, v81, v83 offset0:24 offset1:25\n"
        "v_mfma_f32_16x16x32_bf16 v[76:79], v[32:35], v[252:255], 0\n"
        "ds_read_b64 v[144:145], v162 offset:80\n"
        "v_mfma_f32_16x16x32_bf16 v[76:79], v[28:31], v[108:111], v[76:79]\n"
        "ds_read_b128 v[96:99], v0 offset:9264\n"
        "v_mfma_f32_16x16x32_bf16 v[76:79], v[24:27], v[112:115], v[76:79]\n"
        "s_waitcnt lgkmcnt(4)\n"
        "ds_read_b128 v[108:111], v0 offset:10288\n"
        "ds_read_b128 v[112:115], v0 offset:11312\n"
        "s_mov_b32 vcc_lo, 0x22222222\n"
        "v_cndmask_b32 v141, v128, v129, vcc\n"
        "s_mov_b32 vcc_lo, 0x44444444\n"
        "v_cndmask_b32 v141, v141, v130, vcc\n"
        "v_mfma_f32_16x16x32_bf16 v[124:127], v[32:35], v[120:123], 0\n"
        "s_mov_b32 vcc_lo, 0x88888888\n"
        "v_cndmask_b32 v141, v141, v131, vcc\n"
        "v_cvt_pk_bf16_f32 v159, v76, v92\n"
        "v_mfma_f32_16x16x32_bf16 v[124:127], v[28:31], v[116:119], v[124:127]\n"
        "ds_read_b128 v[132:135], v0 offset:12352\n"
        "v_cvt_pk_bf16_f32 v157, v77, v93\n"
        "v_mfma_f32_16x16x32_bf16 v[124:127], v[24:27], v[100:103], v[124:127]\n"
        "ds_read_b128 v[136:139], v0 offset:13376\n"
        "ds_read_b128 v[128:131], v0 offset:14400\n"
        "v_cvt_pk_bf16_f32 v151, v78, v94\n"
        "v_cvt_pk_bf16_f32 v149, v79, v95\n"
        "v_mfma_f32_4x4x4_16b_bf16 v[116:119], v[160:161], v[164:165], 0\n"
        "s_waitcnt lgkmcnt(5)\n"
        "v_mfma_f32_4x4x4_16b_bf16 v[80:83], v[146:147], v[164:165], 0\n"
        "ds_read_b128 v[120:123], v0 offset:15440\n"
        "ds_read_b128 v[100:103], v0 offset:16464\n"
        "ds_read_b128 v[92:95], v0 offset:17488\n"
        "v_mfma_f32_4x4x4_16b_bf16 v[76:79], v[144:145], v[164:165], 0\n"
        "v_cvt_pk_bf16_f32 v146, v124, v52\n"
        "v_cvt_pk_bf16_f32 v164, v125, v53\n"
        "v_cvt_pk_bf16_f32 v144, v126, v54\n"
        "v_cvt_pk_bf16_f32 v142, v127, v55\n"
        "s_waitcnt lgkmcnt(6)\n"
        "v_mfma_f32_16x16x32_bf16 v[52:55], v[32:35], v[96:99], 0\n"
        "v_mfma_f32_16x16x32_bf16 v[52:55], v[28:31], v[108:111], v[52:55]\n"
        "v_mfma_f32_16x16x32_bf16 v[52:55], v[24:27], v[112:115], v[52:55]\n"
        "s_waitcnt lgkmcnt(3)\n"
        "ds_write_b128 v163, v[48:51] offset:128\n"
        "v_mfma_f32_16x16x32_bf16 v[48:51], v[32:35], v[132:135], 0\n"
        "ds_read_b128 v[124:127], v7\n"
        "v_mfma_f32_16x16x32_bf16 v[48:51], v[28:31], v[136:139], v[48:51]\n"
        "ds_read_b128 v[132:135], v7 offset:3328\n"
        "v_mfma_f32_16x16x32_bf16 v[48:51], v[24:27], v[128:131], v[48:51]\n"
        "s_waitcnt lgkmcnt(3)\n"
        "ds_read_b128 v[128:131], v7 offset:64\n"
        "v_cvt_pk_bf16_f32 v147, v52, v116\n"
        "v_cvt_pk_bf16_f32 v165, v53, v117\n"
        "v_cvt_pk_bf16_f32 v145, v54, v118\n"
        "v_cvt_pk_bf16_f32 v143, v55, v119\n"
        "v_mfma_f32_16x16x32_bf16 v[96:99], v[32:35], v[120:123], 0\n"
        "ds_read_b128 v[136:139], v7 offset:3392\n"
        "v_cvt_pk_bf16_f32 v140, v48, v80\n"
        "v_mfma_f32_16x16x32_bf16 v[96:99], v[28:31], v[100:103], v[96:99]\n"
        "ds_read_b128 v[160:163], v7 offset:128\n"
        "v_cvt_pk_bf16_f32 v122, v49, v81\n"
        "v_mfma_f32_16x16x32_bf16 v[96:99], v[24:27], v[92:95], v[96:99]\n"
        "s_mov_b64 exec, 0x00f0000f\n"
        "ds_write2_b32 v166, v141, v44 offset0:0 offset1:1\n"
        "s_mov_b64 exec, -1\n"
        "ds_read_b128 v[52:55], v7 offset:3456\n"
        "v_cvt_pk_bf16_f32 v120, v50, v82\n"
        "v_cvt_pk_bf16_f32 v118, v51, v83\n"
        "s_mov_b32 exec_hi, 0\n"
        "ds_write2_b64 v167, v[158:159], v[156:157] offset0:0 offset1:49\n"
        "ds_write2_b64 v167, v[150:151], v[148:149] offset0:98 offset1:147\n"
        "s_mov_b32 exec_hi, -1\n"
        "v_cvt_pk_bf16_f32 v141, v96, v76\n"
        "v_cvt_pk_bf16_f32 v123, v97, v77\n"
        "v_cvt_pk_bf16_f32 v121, v98, v78\n"
        "v_cvt_pk_bf16_f32 v119, v99, v79\n"
        "s_mov_b32 exec_hi, 0\n"
        "ds_write2_b64 v167, v[146:147], v[164:165] offset0:1 offset1:50\n"
        "ds_write2_b64 v167, v[144:145], v[142:143] offset0:99 offset1:148\n"
        "s_mov_b32 exec_hi, -1\n"
        "s_mov_b32 exec_hi, 0\n"
        "ds_write2_b64 v167, v[140:141], v[122:123] offset0:2 offset1:51\n"
        "ds_write2_b64 v167, v[120:121], v[118:119] offset0:100 offset1:149\n"
        "s_mov_b32 exec_hi, -1\n"
        "s_waitcnt lgkmcnt(0)\n"
        "s_nop 6\n"
        "s_setprio 0\n"
        "s_setpc_b64 s[64:65]\n"
        "L52A_%=:\n"
        "s_nop 7\n"
        "v_mov_b32 v167, 21600\n"
        "L50A_%=:\n"
        "ds_read_b32 v166, v167 offset:64\n"
        "s_waitcnt lgkmcnt(0)\n"
        "v_readfirstlane_b32 s40, v166\n"
        "s_nop 3\n"
        "s_cmp_ge_i32 s40, 1\n"
        "s_cbranch_scc1 L51A_%=\n"
        "s_sleep 1\n"
        "s_sub_u32 s41, s41, 1\n"
        "s_cbranch_scc0 L50A_%=\n"
        "s_endpgm\n"
        "L51A_%=:\n"
        "v_mbcnt_lo_u32_b32 v166, -1, 0\n"
        "v_lshl_add_u32 v167, v166, 2, v167\n"
        "ds_read_b32 v166, v167 offset:68\n"
        "s_mov_b32 s14, s40\n"
        "s_mov_b32 s71, -1\n"
        "s_waitcnt lgkmcnt(0)\n"
        "v_readlane_b32 s22, v166, 0\n"
        "v_readlane_b32 s23, v166, 1\n"
        "s_nop 3\n"
        "s_nop 1\n"
        "s_branch L53A_%=\n"
        "L56A_%=:\n"
        "s_nop 7\n"
        "L54A_%=:\n"
        "ds_read_b32 v167, v0 offset:21604\n"
        "s_waitcnt lgkmcnt(0)\n"
        "v_readfirstlane_b32 s40, v167\n"
        "s_nop 3\n"
        "s_cmp_ge_i32 s40, s68\n"
        "s_cbranch_scc1 L55A_%=\n"
        "s_sleep 1\n"
        "s_sub_u32 s41, s41, 1\n"
        "s_cbranch_scc0 L54A_%=\n"
        "s_endpgm\n"
        "L55A_%=:\n"
        "s_nop 1\n"
        "s_branch L57A_%=\n"
        "L52B_%=:\n"
        "s_nop 7\n"
        "v_mov_b32 v167, 21600\n"
        "L50B_%=:\n"
        "ds_read_b32 v166, v167 offset:64\n"
        "s_waitcnt lgkmcnt(0)\n"
        "v_readfirstlane_b32 s40, v166\n"
        "s_nop 3\n"
        "s_cmp_ge_i32 s40, 1\n"
        "s_cbranch_scc1 L51B_%=\n"
        "s_sleep 1\n"
        "s_sub_u32 s41, s41, 1\n"
        "s_cbranch_scc0 L50B_%=\n"
        "s_endpgm\n"
        "L51B_%=:\n"
        "v_mbcnt_lo_u32_b32 v166, -1, 0\n"
        "v_lshl_add_u32 v167, v166, 2, v167\n"
        "ds_read_b32 v166, v167 offset:68\n"
        "s_mov_b32 s14, s40\n"
        "s_mov_b32 s71, -1\n"
        "s_waitcnt lgkmcnt(0)\n"
        "v_readlane_b32 s22, v166, 0\n"
        "v_readlane_b32 s23, v166, 1\n"
        "s_nop 3\n"
        "s_nop 1\n"
        "s_branch L53B_%=\n"
        "L56B_%=:\n"
        "s_nop 7\n"
        "L54B_%=:\n"
        "ds_read_b32 v167, v0 offset:21600\n"
        "s_waitcnt lgkmcnt(0)\n"
        "v_readfirstlane_b32 s40, v167\n"
        "s_nop 3\n"
        "s_cmp_ge_i32 s40, s68\n"
        "s_cbranch_scc1 L55B_%=\n"
        "s_sleep 1\n"
        "s_sub_u32 s41, s41, 1\n"
        "s_cbranch_scc0 L54B_%=\n"
        "s_endpgm\n"
        "L55B_%=:\n"
        "s_nop 1\n"
        "s_branch L57B_%=\n"
        "CREDOA_%=:\n"
        "s_waitcnt lgkmcnt(0)\n"
        "s_nop 15\n"
        "s_mov_b32 s36, 0x20000000\n"
        "ds_read_b128 v[124:127], v7 offset:0\n"
        "ds_read_b128 v[132:135], v7 offset:3328\n"
        "ds_read_b128 v[128:131], v7 offset:64\n"
        "ds_read_b128 v[136:139], v7 offset:3392\n"
        "ds_read_b128 v[160:163], v7 offset:128\n"
        "ds_read_b128 v[52:55], v7 offset:3456\n"
        "s_waitcnt lgkmcnt(0)\n"
        "s_branch MTOPA_%=\n"
        "CREDOB_%=:\n"
        "s_waitcnt lgkmcnt(0)\n"
        "s_nop 15\n"
        "s_mov_b32 s36, 0x20000000\n"
        "ds_read_b128 v[124:127], v7 offset:40064\n"
        "ds_read_b128 v[132:135], v7 offset:43392\n"
        "ds_read_b128 v[128:131], v7 offset:40128\n"
        "ds_read_b128 v[136:139], v7 offset:43456\n"
        "ds_read_b128 v[160:163], v7 offset:40192\n"
        "ds_read_b128 v[52:55], v7 offset:43520\n"
        "s_waitcnt lgkmcnt(0)\n"
        "s_branch MTOPB_%=\n"
        "MTOPA_%=:\n"
        "s_waitcnt lgkmcnt(5)\n"
        "v_mfma_f32_16x16x32_bf16 v[24:27], v[168:171], v[124:127], 0\n"
        "v_cvt_pk_bf16_f32 v112, v152, v56\n"
        "v_cvt_pk_bf16_f32 v113, v72, v20\n"
        "v_cvt_pk_bf16_f32 v114, v84, v60\n"
        "v_cvt_pk_bf16_f32 v116, v153, v57\n"
        "v_cvt_pk_bf16_f32 v117, v73, v21\n"
        "v_cvt_pk_bf16_f32 v118, v85, v61\n"
        "v_cvt_pk_bf16_f32 v120, v154, v58\n"
        "v_cvt_pk_bf16_f32 v121, v74, v22\n"
        "s_waitcnt lgkmcnt(3)\n"
        "v_mfma_f32_16x16x32_bf16 v[24:27], v[172:175], v[128:131], v[24:27]\n"
        "v_cvt_pk_bf16_f32 v122, v86, v62\n"
        "v_cvt_pk_bf16_f32 v140, v155, v59\n"
        "v_cvt_pk_bf16_f32 v141, v75, v23\n"
        "v_cvt_pk_bf16_f32 v142, v87, v63\n"
        "v_cvt_pk_bf16_f32 v144, v68, v36\n"
        "v_cvt_pk_bf16_f32 v145, v40, v64\n"
        "v_cvt_pk_bf16_f32 v146, v104, v88\n"
        "v_cvt_pk_bf16_f32 v148, v69, v37\n"
        "s_waitcnt lgkmcnt(1)\n"
        "v_mfma_f32_16x16x32_bf16 v[24:27], v[176:179], v[160:163], v[24:27]\n"
        "v_cvt_pk_bf16_f32 v149, v41, v65\n"
        "v_cvt_pk_bf16_f32 v150, v105, v89\n"
        "v_cvt_pk_bf16_f32 v152, v70, v38\n"
        "v_cvt_pk_bf16_f32 v153, v42, v66\n"
        "v_cvt_pk_bf16_f32 v154, v106, v90\n"
        "v_cvt_pk_bf16_f32 v156, v71, v39\n"
        "v_cvt_pk_bf16_f32 v157, v43, v67\n"
        "v_cvt_pk_bf16_f32 v158, v107, v91\n"
        "v_mfma_f32_16x16x32_bf16 v[32:35], v[188:191], v[160:163], 0\n"
        "v_cvt_pk_bf16_f32 v40, v24, v25\n"
        "v_cvt_pk_bf16_f32 v41, v26, v27\n"
        "v_mfma_f32_16x16x32_bf16 v[32:35], v[184:187], v[128:131], v[32:35]\n"
        "v_mfma_f32_16x16x32_bf16 v[32:35], v[180:183], v[124:127], v[32:35]\n"
        "s_add_u32 s37, s36, 196224\n"
        "ds_read_b128 v[28:31], v16 offset:128\n"
        "ds_read_b32 v159, v10\n"
        "v_mfma_f32_16x16x32_bf16 v[24:27], v[192:195], v[124:127], 0\n"
        "ds_read_b128 v[20:23], v5\n"
        "v_mfma_f32_16x16x32_bf16 v[24:27], v[196:199], v[128:131], v[24:27]\n"
        "ds_read_b64 v[164:165], v11 offset:192\n"
        "v_mfma_f32_16x16x32_bf16 v[24:27], v[200:203], v[160:163], v[24:27]\n"
        "buffer_store_dwordx3 v[112:114], v12, s[8:11], s37 offen offset:384 sc0 sc1 nt\n"
        "v_cvt_pk_bf16_f32 v42, v32, v33\n"
        "v_cvt_pk_bf16_f32 v43, v34, v35\n"
        "s_waitcnt lgkmcnt(3)\n"
        "v_mfma_f32_16x16x32_bf16 v[48:51], v[28:31], v[160:163], 0\n"
        "ds_read_b128 v[60:63], v7 offset:6784\n"
        "ds_read_i16 v167, v0 offset:20584\n"
        "ds_read_i16 v155, v0 offset:20586\n"
        "v_cvt_pk_bf16_f32 v44, v24, v25\n"
        "v_cvt_pk_bf16_f32 v45, v26, v27\n"
        "s_waitcnt lgkmcnt(3)\n"
        "v_smfmac_f32_16x16x64_bf16 v[48:51], v[20:23], v[124:131], v4\n"
        "s_add_u32 s37, s36, 196224\n"
        "ds_read_b128 v[32:35], v7 offset:6656\n"
        "ds_read_b128 v[36:39], v7 offset:6720\n"
        "v_mfma_f32_16x16x32_bf16 v[56:59], v[28:31], v[52:55], 0\n"
        "v_smfmac_f32_16x16x64_bf16 v[56:59], v[20:23], v[132:139], v4\n"
        "s_setprio 2\n"
        "s_waitcnt lgkmcnt(4)\n"
        "v_mfma_f32_16x16x32_bf16 v[24:27], v[28:31], v[60:63], 0\n"
        "buffer_store_dwordx3 v[116:118], v13, s[8:11], s37 offen offset:1152 sc0 sc1 nt\n"
        "v_add_f32_dpp v165, v50, v51 quad_perm:[0,1,2,3] row_mask:0x1 bank_mask:0xf\n"
        "s_waitcnt lgkmcnt(2)\n"
        "v_add_u32 v167, v9, v167\n"
        "v_add_f32_dpp v164, v48, v49 quad_perm:[0,1,2,3] row_mask:0x1 bank_mask:0xf\n"
        "v_add_f32_dpp v165, v58, v59 quad_perm:[0,1,2,3] row_mask:0x2 bank_mask:0xf\n"
        "v_add_u32 v155, v9, v155\n"
        "v_add_f32_dpp v164, v56, v57 quad_perm:[0,1,2,3] row_mask:0x2 bank_mask:0xf\n"
        "s_waitcnt lgkmcnt(0)\n"
        "v_smfmac_f32_16x16x64_bf16 v[24:27], v[20:23], v[32:39], v4\n"
        "v_mfma_f32_16x16x32_bf16 v[28:31], v[212:215], v[160:163], 0\n"
        "s_add_u32 s37, s36, 294336\n"
        "v_mfma_f32_16x16x32_bf16 v[28:31], v[208:211], v[128:131], v[28:31]\n"
        "v_mfma_f32_16x16x32_bf16 v[28:31], v[204:207], v[124:127], v[28:31]\n"
        "v_mfma_f32_16x16x32_bf16 v[20:23], v[216:219], v[124:127], 0\n"
        "buffer_store_dwordx3 v[120:122], v12, s[8:11], s37 offen offset:576 sc0 sc1 nt\n"
        "v_mfma_f32_16x16x32_bf16 v[20:23], v[220:223], v[128:131], v[20:23]\n"
        "v_mfma_f32_16x16x32_bf16 v[20:23], v[224:227], v[160:163], v[20:23]\n"
        "s_add_u32 s37, s36, 392448\n"
        "v_add_f32_dpp v165, v26, v27 quad_perm:[0,1,2,3] row_mask:0x4 bank_mask:0xf\n"
        "v_mul_f32 v166, s52, v165\n"
        "v_fma_f32 v166, -v166, v165, v159\n"
        "v_cvt_pk_bf16_f32 v46, v28, v29\n"
        "v_cvt_pk_bf16_f32 v47, v30, v31\n"
        "v_add_f32_e64 v166, s53, |v166|\n"
        "v_rsq_f32 v159, v166\n"
        "v_mfma_f32_16x16x32_bf16 v[56:59], v[236:239], v[160:163], 0\n"
        "s_mov_b32 exec_hi, 0xffff\n"
        "ds_write_b32 v10, v159\n"
        "s_mov_b32 exec_hi, -1\n"
        "v_cvt_pk_bf16_f32 v48, v20, v21\n"
        "v_cvt_pk_bf16_f32 v49, v22, v23\n"
        "ds_read_b32 v20, v167 offset:0\n"
        "v_add_f32_dpp v164, v24, v25 quad_perm:[0,1,2,3] row_mask:0x4 bank_mask:0xf\n"
        "v_mfma_f32_16x16x32_bf16 v[56:59], v[232:235], v[128:131], v[56:59]\n"
        "s_mov_b32 exec_hi, 0xffff\n"
        "v_add_u32 v10, 40064, v10\n"
        "s_mov_b32 exec_hi, -1\n"
        "ds_read_b32 v21, v167 offset:832\n"
        "ds_read_b32 v22, v167 offset:1664\n"
        "ds_read_b32 v23, v167 offset:2496\n"
        "ds_read_b32 v24, v167 offset:3328\n"
        "v_mul_f32 v164, v164, v159\n"
        "v_mfma_f32_16x16x32_bf16 v[56:59], v[228:231], v[124:127], v[56:59]\n"
        "buffer_store_dwordx3 v[140:142], v12, s[8:11], s37 offen sc0 sc1 nt\n"
        "ds_read_b32 v25, v167 offset:4160\n"
        "ds_read_b32 v26, v167 offset:4992\n"
        "ds_read_b32 v27, v167 offset:5824\n"
        "ds_read_b32 v28, v167 offset:6656\n"
        "ds_read_b32 v29, v167 offset:7488\n"
        "ds_read_b32 v30, v167 offset:8320\n"
        "ds_read_b32 v31, v167 offset:9152\n"
        "v_cvt_pk_bf16_f32 v50, v56, v57\n"
        "v_cvt_pk_bf16_f32 v51, v58, v59\n"
        "v_readlane_b32 s58, v159, 56\n"
        "v_mfma_f32_16x16x32_bf16 v[56:59], v[124:127], v[40:43], 0\n"
        "s_mov_b64 exec, s[56:57]\n"
        "v_mov_b32 v164, 0xff800000\n"
        "s_mov_b64 exec, -1\n"
        "ds_read_b32 v165, v155 offset:0\n"
        "ds_read_b32 v166, v155 offset:3328\n"
        "v_max_f32_dpp v151, v164, v164 row_ror:8 row_mask:0xf bank_mask:0xf\n"
        "v_mfma_f32_16x16x32_bf16 v[56:59], v[128:131], v[44:47], v[56:59]\n"
        "s_add_u32 s37, s36, 392448\n"
        "v_max_f32_dpp v151, v151, v151 row_ror:4 row_mask:0xf bank_mask:0xf\n"
        "ds_read_b32 v167, v155 offset:6656\n"
        "v_mfma_f32_16x16x32_bf16 v[56:59], v[160:163], v[48:51], v[56:59]\n"
        "v_max_f32_dpp v151, v151, v151 row_ror:2 row_mask:0xf bank_mask:0xf\n"
        "v_mfma_f32_16x16x32_bf16 v[72:75], v[52:55], v[48:51], 0\n"
        "v_mfma_f32_16x16x32_bf16 v[72:75], v[136:139], v[44:47], v[72:75]\n"
        "buffer_store_dwordx3 v[144:146], v15, s[8:11], s37 offen offset:768 sc0 sc1 nt\n"
        "v_max_f32_dpp v151, v151, v151 row_ror:1 row_mask:0xf bank_mask:0xf\n"
        "v_mfma_f32_16x16x32_bf16 v[72:75], v[132:135], v[40:43], v[72:75]\n"
        "ds_read_b128 v[88:91], v8 offset:128\n"
        "v_mfma_f32_16x16x32_bf16 v[68:71], v[32:35], v[40:43], 0\n"
        "s_add_u32 s37, s36, 490560\n"
        "ds_read2_b32 v[92:93], v6 offset0:0 offset1:2\n"
        "ds_read2_b32 v[94:95], v6 offset0:8 offset1:10\n"
        "s_waitcnt lgkmcnt(3)\n"
        "v_mul_f32 v56, v56, v20\n"
        "v_mul_f32 v57, v57, v21\n"
        "v_mul_f32 v58, v58, v22\n"
        "v_mul_f32 v59, v59, v23\n"
        "v_mul_f32 v72, v72, v24\n"
        "v_mfma_f32_16x16x32_bf16 v[68:71], v[36:39], v[44:47], v[68:71]\n"
        "v_max3_f32 v155, v56, v57, v58\n"
        "v_mul_f32 v73, v73, v25\n"
        "v_mul_f32 v74, v74, v26\n"
        "v_max3_f32 v155, v155, v59, v72\n"
        "v_mul_f32 v75, v75, v27\n"
        "v_max3_f32 v155, v155, v73, v74\n"
        "v_mfma_f32_16x16x32_bf16 v[68:71], v[60:63], v[48:51], v[68:71]\n"
        "s_waitcnt lgkmcnt(2)\n"
        "v_mfma_f32_16x16x32_bf16 v[76:79], v[88:91], v[48:51], 0\n"
        "buffer_store_dwordx3 v[148:150], v12, s[8:11], s37 offen offset:192 sc0 sc1 nt\n"
        "s_waitcnt lgkmcnt(0)\n"
        "v_smfmac_f32_16x16x64_bf16 v[76:79], v[92:95], v[40:47], v4\n"
        "v_mfma_f32_16x16x32_bf16 v[48:51], v[168:171], v[132:135], 0\n"
        "s_add_u32 s37, s36, 490560\n"
        "v_mfma_f32_16x16x32_bf16 v[48:51], v[172:175], v[136:139], v[48:51]\n"
        "v_mul_f32 v68, v68, v28\n"
        "v_mul_f32 v69, v69, v29\n"
        "v_mul_f32 v70, v70, v30\n"
        "v_max3_f32 v155, v155, v75, v68\n"
        "v_mul_f32 v71, v71, v31\n"
        "v_add_f32 v76, v76, v77\n"
        "v_mul_f32 v76, s58, v76\n"
        "v_max3_f32 v155, v155, v69, v70\n"
        "v_mfma_f32_16x16x32_bf16 v[48:51], v[176:179], v[52:55], v[48:51]\n"
        "s_mov_b64 exec, s[48:49]\n"
        "v_mov_b32 v76, 0xff800000\n"
        "s_mov_b64 exec, -1\n"
        "v_max3_f32 v155, v155, v71, v76\n"
        "v_mfma_f32_16x16x32_bf16 v[44:47], v[188:191], v[52:55], 0\n"
        "buffer_store_dwordx3 v[152:154], v14, s[8:11], s37 offen offset:960 sc0 sc1 nt\n"
        "v_permlane32_swap_b32 v155, v151\n"
        "v_max_f32 v155, v155, v151\n"
        "v_cvt_pk_bf16_f32 v40, v48, v49\n"
        "v_cvt_pk_bf16_f32 v41, v50, v51\n"
        "v_mov_b32 v154, v155\n"
        "v_mfma_f32_16x16x32_bf16 v[44:47], v[184:187], v[136:139], v[44:47]\n"
        "v_mfma_f32_16x16x32_bf16 v[44:47], v[180:183], v[132:135], v[44:47]\n"
        "s_add_u32 s37, s36, 588672\n"
        "v_permlane16_swap_b32 v155, v154\n"
        "v_max_f32 v155, v155, v154\n"
        "v_mov_b32 v151, v155\n"
        "v_mfma_f32_16x16x32_bf16 v[64:67], v[192:195], v[132:135], 0\n"
        "v_mfma_f32_16x16x32_bf16 v[64:67], v[196:199], v[136:139], v[64:67]\n"
        "v_permlane32_swap_b32 v155, v151\n"
        "v_mul_f32 v155, v155, v165\n"
        "v_cvt_pk_bf16_f32 v42, v44, v45\n"
        "v_cvt_pk_bf16_f32 v43, v46, v47\n"
        "v_fma_f32 v56, v56, v165, -v155\n"
        "v_fma_f32 v57, v57, v165, -v155\n"
        "v_fma_f32 v58, v58, v165, -v155\n"
        "v_mfma_f32_16x16x32_bf16 v[64:67], v[200:203], v[52:55], v[64:67]\n"
        "buffer_store_dwordx3 v[156:158], v12, s[8:11], s37 offen offset:384 sc0 sc1 nt\n"
        "v_exp_f32 v56, v56\n"
        "v_exp_f32 v57, v57\n"
        "v_exp_f32 v58, v58\n"
        "v_fma_f32 v59, v59, v165, -v155\n"
        "v_add_f32 v158, v56, v57\n"
        "v_exp_f32 v59, v59\n"
        "v_mfma_f32_16x16x32_bf16 v[48:51], v[212:215], v[52:55], 0\n"
        "v_cvt_pk_bf16_f32 v44, v64, v65\n"
        "v_cvt_pk_bf16_f32 v45, v66, v67\n"
        "v_fma_f32 v72, v72, v165, -v155\n"
        "v_add_f32 v158, v158, v58\n"
        "v_exp_f32 v72, v72\n"
        "v_fma_f32 v73, v73, v165, -v155\n"
        "v_add_f32 v158, v158, v59\n"
        "v_mfma_f32_16x16x32_bf16 v[48:51], v[208:211], v[136:139], v[48:51]\n"
        "v_exp_f32 v73, v73\n"
        "v_fma_f32 v74, v74, v165, -v155\n"
        "v_add_f32 v158, v158, v72\n"
        "v_exp_f32 v74, v74\n"
        "v_fma_f32 v75, v75, v165, -v155\n"
        "v_sub_f32 v164, v164, v151\n"
        "v_mfma_f32_16x16x32_bf16 v[48:51], v[204:207], v[132:135], v[48:51]\n"
        "v_add_f32 v158, v158, v73\n"
        "v_exp_f32 v75, v75\n"
        "v_fma_f32 v68, v68, v165, -v155\n"
        "v_mul_f32 v164, s58, v164\n"
        "v_add_f32 v158, v158, v74\n"
        "v_exp_f32 v68, v68\n"
        "v_mfma_f32_16x16x32_bf16 v[64:67], v[216:219], v[132:135], 0\n"
        "v_fma_f32 v69, v69, v165, -v155\n"
        "v_cvt_pk_bf16_f32 v46, v48, v49\n"
        "v_cvt_pk_bf16_f32 v47, v50, v51\n"
        "v_exp_f32 v164, v164\n"
        "v_add_f32 v158, v158, v75\n"
        "v_exp_f32 v69, v69\n"
        "v_mfma_f32_16x16x32_bf16 v[64:67], v[220:223], v[136:139], v[64:67]\n"
        "v_fma_f32 v70, v70, v165, -v155\n"
        "v_add_f32 v158, v158, v68\n"
        "v_exp_f32 v70, v70\n"
        "v_fma_f32 v71, v71, v165, -v155\n"
        "v_add_f32_dpp v157, v164, v164 row_ror:8 row_mask:0xf bank_mask:0xf\n"
        "v_add_f32 v158, v158, v69\n"
        "v_fma_f32 v76, v76, v165, -v155\n"
        "v_mfma_f32_16x16x32_bf16 v[64:67], v[224:227], v[52:55], v[64:67]\n"
        "v_exp_f32 v71, v71\n"
        "v_add_f32_dpp v157, v157, v157 row_ror:4 row_mask:0xf bank_mask:0xf\n"
        "v_add_f32 v158, v158, v70\n"
        "v_exp_f32 v76, v76\n"
        "v_add_f32_dpp v157, v157, v157 row_ror:2 row_mask:0xf bank_mask:0xf\n"
        "v_add_f32 v158, v158, v71\n"
        "v_mfma_f32_16x16x32_bf16 v[48:51], v[236:239], v[52:55], 0\n"
        "v_add_f32_dpp v157, v157, v157 row_ror:1 row_mask:0xf bank_mask:0xf\n"
        "v_cvt_pk_bf16_f32 v80, v64, v65\n"
        "v_cvt_pk_bf16_f32 v81, v66, v67\n"
        "v_add_f32 v158, v158, v76\n"
        "v_mul_f32 v56, v56, v20\n"
        "v_mul_f32 v57, v57, v21\n"
        "v_permlane32_swap_b32 v158, v157\n"
        "v_mfma_f32_16x16x32_bf16 v[48:51], v[232:235], v[136:139], v[48:51]\n"
        "v_add_f32 v158, v158, v157\n"
        "v_mov_b32 v165, v158\n"
        "v_mul_f32 v58, v58, v22\n"
        "v_mul_f32 v59, v59, v23\n"
        "v_permlane16_swap_b32 v158, v165\n"
        "v_add_f32 v158, v158, v165\n"
        "v_mul_f32 v72, v72, v24\n"
        "v_mfma_f32_16x16x32_bf16 v[48:51], v[228:231], v[132:135], v[48:51]\n"
        "v_rcp_f32 v158, v158\n"
        "v_mul_f32 v73, v73, v25\n"
        "v_mov_b32 v157, v158\n"
        "v_mul_f32 v74, v74, v26\n"
        "v_mul_f32 v75, v75, v27\n"
        "v_permlane32_swap_b32 v158, v157\n"
        "v_mfma_f32_16x16x32_bf16 v[64:67], v[124:127], v[40:43], 0\n"
        "v_mul_f32 v56, v56, v158\n"
        "v_cvt_pk_bf16_f32 v82, v48, v49\n"
        "v_cvt_pk_bf16_f32 v83, v50, v51\n"
        "v_mul_f32 v57, v57, v158\n"
        "v_mul_f32 v58, v58, v158\n"
        "v_mul_f32 v59, v59, v158\n"
        "v_mul_f32 v72, v72, v158\n"
        "v_mul_f32 v73, v73, v158\n"
        "v_mfma_f32_16x16x32_bf16 v[64:67], v[128:131], v[44:47], v[64:67]\n"
        "v_mul_f32 v74, v74, v158\n"
        "v_mul_f32 v75, v75, v158\n"
        "v_cvt_pk_bf16_f32 v48, v56, v57\n"
        "v_cvt_pk_bf16_f32 v49, v58, v59\n"
        "v_cvt_pk_bf16_f32 v50, v72, v73\n"
        "v_cvt_pk_bf16_f32 v51, v74, v75\n"
        "v_mul_f32 v68, v68, v28\n"
        "v_mul_f32 v69, v69, v29\n"
        "v_mfma_f32_16x16x32_bf16 v[64:67], v[160:163], v[80:83], v[64:67]\n"
        "v_mul_f32 v70, v70, v30\n"
        "v_mul_f32 v71, v71, v31\n"
        "v_mul_f32 v76, s58, v76\n"
        "v_mul_f32 v68, v68, v158\n"
        "v_mul_f32 v69, v69, v158\n"
        "v_mul_f32 v70, v70, v158\n"
        "v_mul_f32 v71, v71, v158\n"
        "v_fma_f32 v76, v76, v158, v19\n"
        "v_mfma_f32_16x16x32_bf16 v[84:87], v[52:55], v[80:83], 0\n"
        "v_mul_f32 v64, v64, v20\n"
        "v_mul_f32 v65, v65, v21\n"
        "v_mul_f32 v66, v66, v22\n"
        "v_mul_f32 v67, v67, v23\n"
        "v_max3_f32 v165, v64, v65, v66\n"
        "v_cvt_pk_bf16_f32 v56, v68, v69\n"
        "v_cvt_pk_bf16_f32 v58, v70, v71\n"
        "v_cvt_pk_bf16_f32 v57, v76, 0\n"
        "v_mfma_f32_16x16x32_bf16 v[84:87], v[136:139], v[44:47], v[84:87]\n"
        "v_mul_f32 v164, v164, v159\n"
        "v_mul_f32 v164, v164, v157\n"
        "v_cvt_pk_bf16_f32 v164, v164, 0\n"
        "ds_write_b16 v1, v164\n"
        "v_mfma_f32_16x16x32_bf16 v[84:87], v[132:135], v[40:43], v[84:87]\n"
        "v_mfma_f32_16x16x32_bf16 v[68:71], v[32:35], v[40:43], 0\n"
        "v_mfma_f32_16x16x32_bf16 v[68:71], v[36:39], v[44:47], v[68:71]\n"
        "v_mfma_f32_16x16x32_bf16 v[68:71], v[60:63], v[80:83], v[68:71]\n"
        "v_mfma_f32_16x16x32_bf16 v[72:75], v[88:91], v[80:83], 0\n"
        "v_smfmac_f32_16x16x64_bf16 v[72:75], v[92:95], v[40:47], v4\n"
        "v_mfma_f32_16x16x32_bf16 v[80:83], v[168:171], v[32:35], 0\n"
        "v_mfma_f32_16x16x32_bf16 v[80:83], v[172:175], v[36:39], v[80:83]\n"
        "v_mfma_f32_16x16x32_bf16 v[80:83], v[176:179], v[60:63], v[80:83]\n"
        "v_mul_f32 v84, v84, v24\n"
        "v_mul_f32 v85, v85, v25\n"
        "v_mul_f32 v86, v86, v26\n"
        "v_max3_f32 v165, v165, v67, v84\n"
        "v_mul_f32 v87, v87, v27\n"
        "v_add_f32 v72, v72, v73\n"
        "v_mul_f32 v68, v68, v28\n"
        "v_mul_f32 v72, s58, v72\n"
        "v_mfma_f32_16x16x32_bf16 v[40:43], v[188:191], v[60:63], 0\n"
        "s_mov_b64 exec, s[48:49]\n"
        "v_mov_b32 v72, 0xff800000\n"
        "s_mov_b64 exec, -1\n"
        "v_cvt_pk_bf16_f32 v76, v80, v81\n"
        "v_cvt_pk_bf16_f32 v77, v82, v83\n"
        "v_max3_f32 v165, v165, v85, v86\n"
        "v_mul_f32 v69, v69, v29\n"
        "v_mul_f32 v70, v70, v30\n"
        "v_max3_f32 v165, v165, v87, v68\n"
        "v_mfma_f32_16x16x32_bf16 v[40:43], v[184:187], v[36:39], v[40:43]\n"
        "v_mul_f32 v71, v71, v31\n"
        "v_max3_f32 v165, v165, v69, v70\n"
        "v_max3_f32 v165, v165, v71, v72\n"
        "v_mfma_f32_16x16x32_bf16 v[40:43], v[180:183], v[32:35], v[40:43]\n"
        "v_mfma_f32_16x16x32_bf16 v[100:103], v[192:195], v[32:35], 0\n"
        "v_mfma_f32_16x16x32_bf16 v[100:103], v[196:199], v[36:39], v[100:103]\n"
        "v_mfma_f32_16x16x32_bf16 v[100:103], v[200:203], v[60:63], v[100:103]\n"
        "v_mfma_f32_16x16x32_bf16 v[96:99], v[212:215], v[60:63], 0\n"
        "v_mfma_f32_16x16x32_bf16 v[96:99], v[208:211], v[36:39], v[96:99]\n"
        "v_mfma_f32_16x16x32_bf16 v[96:99], v[204:207], v[32:35], v[96:99]\n"
        "v_mfma_f32_16x16x32_bf16 v[44:47], v[216:219], v[32:35], 0\n"
        "v_mfma_f32_16x16x32_bf16 v[44:47], v[220:223], v[36:39], v[44:47]\n"
        "v_cvt_pk_bf16_f32 v78, v40, v41\n"
        "v_cvt_pk_bf16_f32 v79, v42, v43\n"
        "v_mfma_f32_16x16x32_bf16 v[44:47], v[224:227], v[60:63], v[44:47]\n"
        "v_cvt_pk_bf16_f32 v80, v100, v101\n"
        "v_cvt_pk_bf16_f32 v81, v102, v103\n"
        "v_mfma_f32_16x16x32_bf16 v[40:43], v[236:239], v[60:63], 0\n"
        "v_cvt_pk_bf16_f32 v82, v96, v97\n"
        "v_cvt_pk_bf16_f32 v83, v98, v99\n"
        "v_mfma_f32_16x16x32_bf16 v[40:43], v[232:235], v[36:39], v[40:43]\n"
        "v_mfma_f32_16x16x32_bf16 v[40:43], v[228:231], v[32:35], v[40:43]\n"
        "v_mfma_f32_16x16x32_bf16 v[108:111], v[124:127], v[76:79], 0\n"
        "v_cvt_pk_bf16_f32 v96, v44, v45\n"
        "v_cvt_pk_bf16_f32 v97, v46, v47\n"
        "v_mfma_f32_16x16x32_bf16 v[108:111], v[128:131], v[80:83], v[108:111]\n"
        "s_nop 3\n"
        "v_cvt_pk_bf16_f32 v98, v40, v41\n"
        "v_cvt_pk_bf16_f32 v99, v42, v43\n"
        "s_nop 1\n"
        "v_mfma_f32_16x16x32_bf16 v[108:111], v[160:163], v[96:99], v[108:111]\n"
        "v_mfma_f32_16x16x32_bf16 v[140:143], v[52:55], v[96:99], 0\n"
        "v_mfma_f32_16x16x32_bf16 v[140:143], v[136:139], v[80:83], v[140:143]\n"
        "v_mfma_f32_16x16x32_bf16 v[140:143], v[132:135], v[76:79], v[140:143]\n"
        "v_mfma_f32_16x16x32_bf16 v[100:103], v[32:35], v[76:79], 0\n"
        "v_mfma_f32_16x16x32_bf16 v[100:103], v[36:39], v[80:83], v[100:103]\n"
        "v_mfma_f32_16x16x32_bf16 v[100:103], v[60:63], v[96:99], v[100:103]\n"
        "v_mfma_f32_16x16x32_bf16 v[44:47], v[88:91], v[96:99], 0\n"
        "v_smfmac_f32_16x16x64_bf16 v[44:47], v[92:95], v[76:83], v4\n"
        "v_mul_f32 v108, v108, v20\n"
        "v_mul_f32 v109, v109, v21\n"
        "v_mul_f32 v110, v110, v22\n"
        "v_mul_f32 v111, v111, v23\n"
        "v_mul_f32 v140, v140, v24\n"
        "v_max3_f32 v159, v108, v109, v110\n"
        "v_mul_f32 v141, v141, v25\n"
        "v_mfma_f32_16x16x32_bf16 v[60:63], v[124:127], v[240:243], 0\n"
        "v_add_f32 v44, v44, v45\n"
        "v_mul_f32 v142, v142, v26\n"
        "v_max3_f32 v159, v159, v111, v140\n"
        "v_mul_f32 v143, v143, v27\n"
        "v_mul_f32 v100, v100, v28\n"
        "v_mul_f32 v44, s58, v44\n"
        "v_max3_f32 v159, v159, v141, v142\n"
        "v_mul_f32 v101, v101, v29\n"
        "v_mfma_f32_16x16x32_bf16 v[60:63], v[128:131], v[244:247], v[60:63]\n"
        "s_mov_b64 exec, s[48:49]\n"
        "v_mov_b32 v44, 0xff800000\n"
        "s_mov_b64 exec, -1\n"
        "v_mul_f32 v102, v102, v30\n"
        "v_max3_f32 v159, v159, v143, v100\n"
        "v_mul_f32 v103, v103, v31\n"
        "v_max3_f32 v159, v159, v101, v102\n"
        "v_max3_f32 v159, v159, v103, v44\n"
        "v_mfma_f32_16x16x32_bf16 v[60:63], v[160:163], v[248:251], v[60:63]\n"
        "ds_read_b128 v[32:35], v17 offset:0\n"
        "v_permlane32_swap_b32 v165, v159\n"
        "v_max_f32 v165, v165, v159\n"
        "v_mov_b32 v164, v165\n"
        "v_mfma_f32_16x16x32_bf16 v[76:79], v[52:55], v[248:251], 0\n"
        "ds_read_b128 v[36:39], v17 offset:64\n"
        "v_permlane16_swap_b32 v165, v164\n"
        "v_max_f32 v165, v165, v164\n"
        "v_cvt_pk_bf16_f32 v40, v60, v61\n"
        "v_cvt_pk_bf16_f32 v41, v62, v63\n"
        "v_mov_b32 v159, v165\n"
        "v_mfma_f32_16x16x32_bf16 v[76:79], v[136:139], v[244:247], v[76:79]\n"
        "ds_read_b128 v[60:63], v17 offset:128\n"
        "v_permlane32_swap_b32 v165, v159\n"
        "v_mul_f32 v165, v165, v166\n"
        "v_mul_f32 v159, v159, v167\n"
        "v_fma_f32 v64, v64, v166, -v165\n"
        "v_fma_f32 v65, v65, v166, -v165\n"
        "v_fma_f32 v108, v108, v167, -v159\n"
        "v_mfma_f32_16x16x32_bf16 v[76:79], v[132:135], v[240:243], v[76:79]\n"
        "s_waitcnt lgkmcnt(2)\n"
        "ds_read_b128 v[96:99], v0 offset:5136\n"
        "v_fma_f32 v109, v109, v167, -v159\n"
        "v_exp_f32 v64, v64\n"
        "v_exp_f32 v65, v65\n"
        "v_fma_f32 v66, v66, v166, -v165\n"
        "v_fma_f32 v110, v110, v167, -v159\n"
        "v_mfma_f32_16x16x32_bf16 v[80:83], v[32:35], v[240:243], 0\n"
        "s_waitcnt lgkmcnt(2)\n"
        "ds_read_b128 v[104:107], v0 offset:4112\n"
        "v_cvt_pk_bf16_f32 v42, v76, v77\n"
        "v_cvt_pk_bf16_f32 v43, v78, v79\n"
        "v_exp_f32 v108, v108\n"
        "v_exp_f32 v109, v109\n"
        "v_fma_f32 v67, v67, v166, -v165\n"
        "v_mfma_f32_16x16x32_bf16 v[80:83], v[36:39], v[244:247], v[80:83]\n"
        "s_waitcnt lgkmcnt(2)\n"
        "v_exp_f32 v66, v66\n"
        "v_exp_f32 v110, v110\n"
        "v_fma_f32 v111, v111, v167, -v159\n"
        "v_add_f32 v95, v64, v65\n"
        "v_exp_f32 v67, v67\n"
        "v_mfma_f32_16x16x32_bf16 v[80:83], v[60:63], v[248:251], v[80:83]\n"
        "s_waitcnt lgkmcnt(1)\n"
        "v_fma_f32 v84, v84, v166, -v165\n"
        "v_add_f32 v164, v108, v109\n"
        "v_exp_f32 v111, v111\n"
        "v_fma_f32 v140, v140, v167, -v159\n"
        "v_add_f32 v95, v95, v66\n"
        "v_exp_f32 v84, v84\n"
        "v_mfma_f32_16x16x32_bf16 v[112:115], v[60:63], v[96:99], 0\n"
        "s_waitcnt lgkmcnt(0)\n"
        "v_cvt_pk_bf16_f32 v76, v80, v81\n"
        "v_cvt_pk_bf16_f32 v80, v82, v83\n"
        "v_fma_f32 v85, v85, v166, -v165\n"
        "v_add_f32 v164, v164, v110\n"
        "v_exp_f32 v140, v140\n"
        "v_fma_f32 v141, v141, v167, -v159\n"
        "v_add_f32 v95, v95, v67\n"
        "v_mfma_f32_16x16x32_bf16 v[112:115], v[36:39], v[104:107], v[112:115]\n"
        "v_exp_f32 v85, v85\n"
        "v_fma_f32 v86, v86, v166, -v165\n"
        "v_add_f32 v164, v164, v111\n"
        "v_exp_f32 v141, v141\n"
        "v_fma_f32 v142, v142, v167, -v159\n"
        "v_add_f32 v95, v95, v84\n"
        "v_mfma_f32_16x16x32_bf16 v[112:115], v[32:35], v[252:255], v[112:115]\n"
        "v_exp_f32 v86, v86\n"
        "v_fma_f32 v87, v87, v166, -v165\n"
        "v_add_f32 v164, v164, v140\n"
        "v_exp_f32 v142, v142\n"
        "v_fma_f32 v143, v143, v167, -v159\n"
        "v_add_f32 v95, v95, v85\n"
        "v_mfma_f32_16x16x32_bf16 v[120:123], v[132:135], v[252:255], 0\n"
        "v_exp_f32 v87, v87\n"
        "v_cvt_pk_bf16_f32 v88, v112, v113\n"
        "v_cvt_pk_bf16_f32 v92, v114, v115\n"
        "v_fma_f32 v68, v68, v166, -v165\n"
        "v_add_f32 v164, v164, v141\n"
        "v_exp_f32 v143, v143\n"
        "v_mfma_f32_16x16x32_bf16 v[120:123], v[136:139], v[104:107], v[120:123]\n"
        "v_fma_f32 v100, v100, v167, -v159\n"
        "v_add_f32 v95, v95, v86\n"
        "v_exp_f32 v68, v68\n"
        "v_fma_f32 v69, v69, v166, -v165\n"
        "v_add_f32 v164, v164, v142\n"
        "v_exp_f32 v100, v100\n"
        "v_mfma_f32_16x16x32_bf16 v[120:123], v[52:55], v[96:99], v[120:123]\n"
        "ds_read_b128 v[144:147], v0 offset:6176\n"
        "v_fma_f32 v101, v101, v167, -v159\n"
        "v_add_f32 v95, v95, v87\n"
        "v_exp_f32 v69, v69\n"
        "v_fma_f32 v70, v70, v166, -v165\n"
        "v_add_f32 v164, v164, v143\n"
        "v_fma_f32 v102, v102, v167, -v159\n"
        "v_mfma_f32_16x16x32_bf16 v[148:151], v[160:163], v[96:99], 0\n"
        "ds_read_b128 v[116:119], v0 offset:7200\n"
        "v_cvt_pk_bf16_f32 v98, v120, v121\n"
        "v_cvt_pk_bf16_f32 v99, v122, v123\n"
        "v_exp_f32 v101, v101\n"
        "v_add_f32 v95, v95, v68\n"
        "v_exp_f32 v70, v70\n"
        "v_mfma_f32_16x16x32_bf16 v[148:151], v[128:131], v[104:107], v[148:151]\n"
        "ds_read_b128 v[112:115], v0 offset:8224\n"
        "v_fma_f32 v71, v71, v166, -v165\n"
        "v_add_f32 v164, v164, v100\n"
        "v_exp_f32 v102, v102\n"
        "v_fma_f32 v103, v103, v167, -v159\n"
        "v_add_f32 v95, v95, v69\n"
        "v_fma_f32 v72, v72, v166, -v165\n"
        "v_mfma_f32_16x16x32_bf16 v[148:151], v[124:127], v[252:255], v[148:151]\n"
        "s_waitcnt lgkmcnt(2)\n"
        "v_exp_f32 v71, v71\n"
        "v_add_f32 v164, v164, v101\n"
        "v_exp_f32 v103, v103\n"
        "v_fma_f32 v44, v44, v167, -v159\n"
        "v_add_f32 v95, v95, v70\n"
        "v_add_f32 v164, v164, v102\n"
        "v_mfma_f32_16x16x32_bf16 v[120:123], v[124:127], v[144:147], 0\n"
        "s_waitcnt lgkmcnt(1)\n"
        "v_cvt_pk_bf16_f32 v96, v148, v149\n"
        "v_cvt_pk_bf16_f32 v97, v150, v151\n"
        "v_exp_f32 v72, v72\n"
        "v_exp_f32 v44, v44\n"
        "v_add_f32 v95, v95, v71\n"
        "v_add_f32 v164, v164, v103\n"
        "v_mfma_f32_16x16x32_bf16 v[120:123], v[128:131], v[116:119], v[120:123]\n"
        "s_waitcnt lgkmcnt(0)\n"
        "v_add_f32 v95, v95, v72\n"
        "v_add_f32 v164, v164, v44\n"
        "v_mul_f32 v64, v64, v20\n"
        "v_mul_f32 v65, v65, v21\n"
        "v_permlane32_swap_b32 v95, v164\n"
        "v_add_f32 v95, v95, v164\n"
        "v_mov_b32 v167, v95\n"
        "v_mfma_f32_16x16x32_bf16 v[120:123], v[160:163], v[112:115], v[120:123]\n"
        "v_mul_f32 v66, v66, v22\n"
        "v_permlane16_swap_b32 v95, v167\n"
        "v_add_f32 v95, v95, v167\n"
        "v_rcp_f32 v95, v95\n"
        "v_mul_f32 v67, v67, v23\n"
        "v_mov_b32 v164, v95\n"
        "v_mfma_f32_16x16x32_bf16 v[156:159], v[52:55], v[112:115], 0\n"
        "v_mul_f32 v84, v84, v24\n"
        "v_cvt_pk_bf16_f32 v104, v120, v121\n"
        "v_cvt_pk_bf16_f32 v105, v122, v123\n"
        "v_permlane32_swap_b32 v95, v164\n"
        "v_mul_f32 v85, v85, v25\n"
        "v_mul_f32 v86, v86, v26\n"
        "v_mul_f32 v87, v87, v27\n"
        "v_mfma_f32_16x16x32_bf16 v[156:159], v[136:139], v[116:119], v[156:159]\n"
        "v_mul_f32 v64, v64, v95\n"
        "v_mul_f32 v65, v65, v95\n"
        "v_mul_f32 v66, v66, v95\n"
        "v_mul_f32 v67, v67, v95\n"
        "v_mul_f32 v84, v84, v95\n"
        "v_mul_f32 v85, v85, v95\n"
        "v_mul_f32 v86, v86, v95\n"
        "v_mul_f32 v87, v87, v95\n"
        "v_mfma_f32_16x16x32_bf16 v[156:159], v[132:135], v[144:147], v[156:159]\n"
        "ds_read_b128 v[148:151], v0 offset:11312\n"
        "v_cvt_pk_bf16_f32 v120, v64, v65\n"
        "v_cvt_pk_bf16_f32 v121, v66, v67\n"
        "v_cvt_pk_bf16_f32 v122, v84, v85\n"
        "v_cvt_pk_bf16_f32 v123, v86, v87\n"
        "v_mul_f32 v68, v68, v28\n"
        "v_mul_f32 v69, v69, v29\n"
        "v_mul_f32 v70, v70, v30\n"
        "v_mfma_f32_16x16x32_bf16 v[152:155], v[32:35], v[144:147], 0\n"
        "ds_read_b128 v[84:87], v0 offset:10288\n"
        "v_cvt_pk_bf16_f32 v106, v156, v157\n"
        "v_cvt_pk_bf16_f32 v107, v158, v159\n"
        "v_mul_f32 v71, v71, v31\n"
        "v_mul_f32 v72, s58, v72\n"
        "v_mul_f32 v68, v68, v95\n"
        "v_mul_f32 v69, v69, v95\n"
        "v_mul_f32 v70, v70, v95\n"
        "v_mfma_f32_16x16x32_bf16 v[152:155], v[36:39], v[116:119], v[152:155]\n"
        "ds_read_b128 v[144:147], v0 offset:9264\n"
        "v_mul_f32 v71, v71, v95\n"
        "v_fma_f32 v72, v72, v95, v19\n"
        "v_cvt_pk_bf16_f32 v64, v68, v69\n"
        "v_cvt_pk_bf16_f32 v66, v70, v71\n"
        "v_cvt_pk_bf16_f32 v65, v72, 0\n"
        "v_mul_f32 v108, v108, v20\n"
        "v_mul_f32 v109, v109, v21\n"
        "v_mfma_f32_16x16x32_bf16 v[152:155], v[60:63], v[112:115], v[152:155]\n"
        "s_waitcnt lgkmcnt(2)\n"
        "v_mul_f32 v110, v110, v22\n"
        "v_mul_f32 v111, v111, v23\n"
        "v_mul_f32 v140, v140, v24\n"
        "v_mul_f32 v141, v141, v25\n"
        "v_mul_f32 v142, v142, v26\n"
        "v_mul_f32 v143, v143, v27\n"
        "v_mul_f32 v108, v108, v164\n"
        "v_cvt_pk_bf16_f32 v112, v152, v153\n"
        "v_mfma_f32_16x16x32_bf16 v[20:23], v[60:63], v[148:151], 0\n"
        "s_waitcnt lgkmcnt(1)\n"
        "v_cvt_pk_bf16_f32 v116, v154, v155\n"
        "v_mul_f32 v109, v109, v164\n"
        "v_mul_f32 v110, v110, v164\n"
        "v_mul_f32 v111, v111, v164\n"
        "v_mul_f32 v140, v140, v164\n"
        "v_mul_f32 v141, v141, v164\n"
        "v_mul_f32 v142, v142, v164\n"
        "v_mul_f32 v143, v143, v164\n"
        "v_mfma_f32_16x16x32_bf16 v[20:23], v[36:39], v[84:87], v[20:23]\n"
        "s_waitcnt lgkmcnt(0)\n"
        "v_cvt_pk_bf16_f32 v24, v108, v109\n"
        "v_cvt_pk_bf16_f32 v25, v110, v111\n"
        "v_cvt_pk_bf16_f32 v26, v140, v141\n"
        "v_cvt_pk_bf16_f32 v27, v142, v143\n"
        "v_mul_f32 v100, v100, v28\n"
        "v_mul_f32 v101, v101, v29\n"
        "v_mul_f32 v102, v102, v30\n"
        "v_mul_f32 v103, v103, v31\n"
        "v_mfma_f32_16x16x32_bf16 v[20:23], v[32:35], v[144:147], v[20:23]\n"
        "v_mul_f32 v44, s58, v44\n"
        "v_mul_f32 v100, v100, v164\n"
        "v_mul_f32 v101, v101, v164\n"
        "v_mul_f32 v102, v102, v164\n"
        "v_mul_f32 v103, v103, v164\n"
        "v_fma_f32 v44, v44, v164, v19\n"
        "v_cvt_pk_bf16_f32 v28, v100, v101\n"
        "v_cvt_pk_bf16_f32 v30, v102, v103\n"
        "v_mfma_f32_16x16x32_bf16 v[68:71], v[132:135], v[144:147], 0\n"
        "v_cvt_pk_bf16_f32 v77, v20, v21\n"
        "v_cvt_pk_bf16_f32 v81, v22, v23\n"
        "v_cvt_pk_bf16_f32 v29, v44, 0\n"
        "v_mfma_f32_16x16x32_bf16 v[68:71], v[136:139], v[84:87], v[68:71]\n"
        "v_mfma_f32_16x16x32_bf16 v[68:71], v[52:55], v[148:151], v[68:71]\n"
        "ds_read_b128 v[20:23], v0 offset:12352\n"
        "v_mfma_f32_16x16x32_bf16 v[72:75], v[160:163], v[148:151], 0\n"
        "ds_read_b128 v[108:111], v0 offset:13376\n"
        "v_mfma_f32_16x16x32_bf16 v[72:75], v[128:131], v[84:87], v[72:75]\n"
        "ds_read_b128 v[140:143], v0 offset:14400\n"
        "v_mfma_f32_16x16x32_bf16 v[72:75], v[124:127], v[144:147], v[72:75]\n"
        "s_waitcnt lgkmcnt(2)\n"
        "v_mfma_f32_16x16x32_bf16 v[144:147], v[124:127], v[20:23], 0\n"
        "v_cvt_pk_bf16_f32 v46, v68, v69\n"
        "v_cvt_pk_bf16_f32 v47, v70, v71\n"
        "s_waitcnt lgkmcnt(1)\n"
        "v_mfma_f32_16x16x32_bf16 v[144:147], v[128:131], v[108:111], v[144:147]\n"
        "s_waitcnt lgkmcnt(0)\n"
        "v_mfma_f32_16x16x32_bf16 v[144:147], v[160:163], v[140:143], v[144:147]\n"
        "v_cvt_pk_bf16_f32 v44, v72, v73\n"
        "v_cvt_pk_bf16_f32 v45, v74, v75\n"
        "v_mfma_f32_16x16x32_bf16 v[148:151], v[52:55], v[140:143], 0\n"
        "v_mfma_f32_16x16x32_bf16 v[148:151], v[136:139], v[108:111], v[148:151]\n"
        "v_mfma_f32_16x16x32_bf16 v[148:151], v[132:135], v[20:23], v[148:151]\n"
        "ds_read_b128 v[72:75], v0 offset:17488\n"
        "v_mfma_f32_16x16x32_bf16 v[84:87], v[32:35], v[20:23], 0\n"
        "ds_read_b128 v[68:71], v0 offset:16464\n"
        "v_cvt_pk_bf16_f32 v100, v144, v145\n"
        "v_cvt_pk_bf16_f32 v101, v146, v147\n"
        "v_mfma_f32_16x16x32_bf16 v[84:87], v[36:39], v[108:111], v[84:87]\n"
        "ds_read_b128 v[20:23], v0 offset:15440\n"
        "v_mfma_f32_16x16x32_bf16 v[84:87], v[60:63], v[140:143], v[84:87]\n"
        "v_cvt_pk_bf16_f32 v102, v148, v149\n"
        "v_cvt_pk_bf16_f32 v103, v150, v151\n"
        "s_waitcnt lgkmcnt(2)\n"
        "v_mfma_f32_16x16x32_bf16 v[108:111], v[60:63], v[72:75], 0\n"
        "s_waitcnt lgkmcnt(1)\n"
        "v_mfma_f32_16x16x32_bf16 v[108:111], v[36:39], v[68:71], v[108:111]\n"
        "s_waitcnt lgkmcnt(0)\n"
        "v_mfma_f32_16x16x32_bf16 v[108:111], v[32:35], v[20:23], v[108:111]\n"
        "ds_read2_b32 v[82:83], v11 offset0:0 offset1:3\n"
        "v_cvt_pk_bf16_f32 v89, v84, v85\n"
        "v_cvt_pk_bf16_f32 v93, v86, v87\n"
        "v_mov_b32_dpp v59, v19 row_shr:4 row_mask:0xf bank_mask:0xf bound_ctrl:0\n"
        "v_mfma_f32_16x16x32_bf16 v[36:39], v[132:135], v[20:23], 0\n"
        "ds_read_b64_tr_b16 v[78:79], v9 offset:0\n"
        "v_mfma_f32_16x16x32_bf16 v[36:39], v[136:139], v[68:71], v[36:39]\n"
        "ds_read2_b32 v[94:95], v11 offset0:1 offset1:4\n"
        "v_cvt_pk_bf16_f32 v113, v108, v109\n"
        "v_cvt_pk_bf16_f32 v117, v110, v111\n"
        "v_mfma_f32_16x16x32_bf16 v[36:39], v[52:55], v[72:75], v[36:39]\n"
        "s_add_u32 s31, s31, 1\n"
        "ds_read_b64_tr_b16 v[90:91], v9 offset:16\n"
        "v_mfma_f32_16x16x32_bf16 v[32:35], v[160:163], v[72:75], 0\n"
        "s_cmp_ge_u32 s31, s71\n"
        "ds_read2_b32 v[118:119], v11 offset0:2 offset1:5\n"
        "v_mfma_f32_16x16x32_bf16 v[32:35], v[128:131], v[68:71], v[32:35]\n"
        "s_cbranch_scc1 L52A_%=\n"
        "L53A_%=:\n"
        "ds_read_b64_tr_b16 v[114:115], v9 offset:32\n"
        "v_cvt_pk_bf16_f32 v110, v36, v37\n"
        "v_cvt_pk_bf16_f32 v111, v38, v39\n"
        "v_mfma_f32_16x16x32_bf16 v[32:35], v[124:127], v[20:23], v[32:35]\n"
        "s_add_u32 s16, s16, 256\n"
        "v_mfma_f32_16x16x32_bf16 v[20:23], v[48:51], v[40:43], 0\n"
        "s_waitcnt lgkmcnt(4)\n"
        "s_cmp_ge_u32 s31, 20\n"
        "v_smfmac_f32_16x16x64_bf16 v[20:23], v[56:59], v[76:83], v18\n"
        "s_cselect_b32 s39, s22, s16\n"
        "v_mfma_f32_16x16x32_bf16 v[36:39], v[48:51], v[96:99], 0\n"
        "s_waitcnt lgkmcnt(2)\n"
        "v_cvt_pk_bf16_f32 v108, v32, v33\n"
        "v_cvt_pk_bf16_f32 v109, v34, v35\n"
        "s_cmp_ge_u32 s31, 21\n"
        "v_smfmac_f32_16x16x64_bf16 v[36:39], v[56:59], v[88:95], v18\n"
        "s_cselect_b32 s39, s23, s39\n"
        "ds_read_b64_tr_b16 v[78:79], v9 offset:48\n"
        "v_mfma_f32_16x16x32_bf16 v[32:35], v[48:51], v[104:107], 0\n"
        "s_waitcnt lgkmcnt(1)\n"
        "s_mul_hi_u32 s38, s39, 58835169\n"
        "v_smfmac_f32_16x16x64_bf16 v[32:35], v[56:59], v[112:119], v18\n"
        "s_mul_i32 s39, s39, 1344\n"
        "ds_read_b64_tr_b16 v[90:91], v9 offset:64\n"
        "v_cvt_pk_bf16_f32 v152, v20, v36\n"
        "v_cvt_pk_bf16_f32 v156, v21, v37\n"
        "v_cvt_pk_bf16_f32 v160, v22, v38\n"
        "v_cvt_pk_bf16_f32 v164, v23, v39\n"
        "v_mfma_f32_16x16x32_bf16 v[68:71], v[48:51], v[44:47], 0\n"
        "s_mul_i32 s38, s38, 588672\n"
        "s_waitcnt lgkmcnt(1)\n"
        "v_smfmac_f32_16x16x64_bf16 v[68:71], v[56:59], v[76:83], v18 abid:1\n"
        "s_add_u32 s39, s39, s38\n"
        "ds_read_b64_tr_b16 v[114:115], v9 offset:80\n"
        "v_mfma_f32_16x16x32_bf16 v[60:63], v[48:51], v[100:103], 0\n"
        "s_add_u32 s39, s39, s17\n"
        "ds_read_b128 v[36:39], v2\n"
        "v_mov_b64 v[20:21], 0\n"
        "v_mov_b64 v[22:23], 0\n"
        "s_waitcnt lgkmcnt(2)\n"
        "v_smfmac_f32_16x16x64_bf16 v[60:63], v[56:59], v[88:95], v18 abid:1\n"
        "s_mul_i32 s68, s31, 3\n"
        "ds_read_b128 v[52:55], v3\n"
        "v_cvt_pk_bf16_f32 v153, v32, v68\n"
        "v_cvt_pk_bf16_f32 v157, v33, v69\n"
        "v_cvt_pk_bf16_f32 v161, v34, v70\n"
        "v_cvt_pk_bf16_f32 v165, v35, v71\n"
        "v_mfma_f32_16x16x32_bf16 v[68:71], v[48:51], v[108:111], 0\n"
        "s_bitcmp1_b32 s31, 0\n"
        "v_mov_b64 v[32:33], 0\n"
        "v_mov_b64 v[34:35], 0\n"
        "s_waitcnt lgkmcnt(2)\n"
        "v_smfmac_f32_16x16x64_bf16 v[68:71], v[56:59], v[112:119], v18 abid:1\n"
        "s_cselect_b32 s38, 3, 6\n"
        "s_waitcnt lgkmcnt(1)\n"
        "v_mov_b32_dpp v67, v19 row_shr:4 row_mask:0xf bank_mask:0xf bound_ctrl:0\n"
        "v_smfmac_f32_16x16x64_bf16 v[20:23], v[36:39], v[40:47], v4\n"
        "s_add_u32 s68, s68, s38\n"
        "v_mov_b64 v[48:49], 0\n"
        "v_mov_b64 v[50:51], 0\n"
        "s_waitcnt lgkmcnt(0)\n"
        "v_smfmac_f32_16x16x64_bf16 v[20:23], v[52:55], v[76:83], v4 abid:1\n"
        "s_add_u32 s37, s35, 0\n"
        "s_cmp_lt_u32 s31, s14\n"
        "v_cvt_pk_bf16_f32 v154, v60, v68\n"
        "v_cvt_pk_bf16_f32 v158, v61, v69\n"
        "v_cvt_pk_bf16_f32 v162, v62, v70\n"
        "v_cvt_pk_bf16_f32 v166, v63, v71\n"
        "v_smfmac_f32_16x16x64_bf16 v[32:35], v[36:39], v[96:103], v4\n"
        "s_cselect_b32 s68, s68, 0\n"
        "v_add_f32 v20, v20, v21\n"
        "v_add_f32 v22, v22, v23\n"
        "v_smfmac_f32_16x16x64_bf16 v[32:35], v[52:55], v[88:95], v4 abid:1\n"
        "s_and_b32 s38, s31, 7\n"
        "ds_read_b64_tr_b16 v[78:79], v9 offset:3328\n"
        "v_smfmac_f32_16x16x64_bf16 v[48:51], v[36:39], v[104:111], v4\n"
        "buffer_store_dwordx3 v[152:154], v12, s[8:11], s37 offen sc0 sc1 nt\n"
        "s_cselect_b32 s30, 1, -7\n"
        "v_smfmac_f32_16x16x64_bf16 v[48:51], v[52:55], v[112:119], v4 abid:1\n"
        "s_mul_i32 s29, s30, 192\n"
        "ds_read_b64_tr_b16 v[90:91], v9 offset:3344\n"
        "v_add_f32 v32, v32, v33\n"
        "v_add_f32 v34, v34, v35\n"
        "v_cvt_pk_bf16_f32 v148, v20, v32\n"
        "v_mfma_f32_16x16x32_bf16 v[152:155], v[120:123], v[40:43], 0\n"
        "s_mul_i32 s30, s30, 392\n"
        "s_waitcnt lgkmcnt(1)\n"
        "v_smfmac_f32_16x16x64_bf16 v[152:155], v[64:67], v[76:83], v18\n"
        "s_add_u32 s37, s35, 0\n"
        "s_cmp_eq_u32 s69, s18\n"
        "ds_read_b64_tr_b16 v[114:115], v9 offset:3360\n"
        "v_add_f32 v48, v48, v49\n"
        "v_add_f32 v50, v50, v51\n"
        "v_cvt_pk_bf16_f32 v149, v48, v22\n"
        "v_cvt_pk_bf16_f32 v150, v34, v50\n"
        "v_mfma_f32_16x16x32_bf16 v[56:59], v[120:123], v[96:99], 0\n"
        "s_cselect_b32 s34, -7488, s70\n"
        "s_waitcnt lgkmcnt(1)\n"
        "v_smfmac_f32_16x16x64_bf16 v[56:59], v[64:67], v[88:95], v18\n"
        "s_add_u32 s69, s69, s34\n"
        "ds_read_b64_tr_b16 v[78:79], v9 offset:3376\n"
        "v_writelane_b32 v10, s69, 56\n"
        "v_add_u32 v11, s30, v11\n"
        "s_mov_b64 exec, s[62:63]\n"
        "v_add_u32 v16, s29, v16\n"
        "v_add_u32 v8, s34, v8\n"
        "s_mov_b64 exec, -1\n"
        "v_mfma_f32_16x16x32_bf16 v[72:75], v[120:123], v[104:107], 0\n"
        "buffer_store_dwordx3 v[156:158], v15, s[8:11], s37 offen offset:768 sc0 sc1 nt\n"
        "s_mov_b64 exec, s[50:51]\n"
        "v_add_u32 v5, s29, v5\n"
        "v_add_u32 v6, s34, v6\n"
        "s_mov_b64 exec, -1\n"
        "s_waitcnt lgkmcnt(1)\n"
        "v_smfmac_f32_16x16x64_bf16 v[72:75], v[64:67], v[112:119], v18\n"
        "ds_read_b64_tr_b16 v[90:91], v9 offset:3392\n"
        "v_mov_b32_dpp v31, v19 row_shr:4 row_mask:0xf bank_mask:0xf bound_ctrl:0\n"
        "v_mfma_f32_16x16x32_bf16 v[20:23], v[120:123], v[44:47], 0\n"
        "s_waitcnt lgkmcnt(1)\n"
        "v_smfmac_f32_16x16x64_bf16 v[20:23], v[64:67], v[76:83], v18 abid:1\n"
        "s_add_u32 s37, s35, 98112\n"
        "ds_read_b64_tr_b16 v[114:115], v9 offset:3408\n"
        "v_mfma_f32_16x16x32_bf16 v[84:87], v[120:123], v[100:103], 0\n"
        "s_waitcnt lgkmcnt(1)\n"
        "v_smfmac_f32_16x16x64_bf16 v[84:87], v[64:67], v[88:95], v18 abid:1\n"
        "ds_read_b64_tr_b16 v[78:79], v9 offset:6656\n"
        "v_mfma_f32_16x16x32_bf16 v[60:63], v[120:123], v[108:111], 0\n"
        "buffer_store_dwordx3 v[160:162], v12, s[8:11], s37 offen offset:192 sc0 sc1 nt\n"
        "ds_read_b32 v167, v0 offset:21604\n"
        "s_waitcnt lgkmcnt(2)\n"
        "v_smfmac_f32_16x16x64_bf16 v[60:63], v[64:67], v[112:119], v18 abid:1\n"
        "ds_read_b64_tr_b16 v[90:91], v9 offset:6672\n"
        "v_mfma_f32_16x16x32_bf16 v[68:71], v[24:27], v[40:43], 0\n"
        "s_waitcnt lgkmcnt(2)\n"
        "v_smfmac_f32_16x16x64_bf16 v[68:71], v[28:31], v[76:83], v18\n"
        "s_add_u32 s37, s35, 98112\n"
        "ds_read_b64_tr_b16 v[114:115], v9 offset:6688\n"
        "v_mfma_f32_16x16x32_bf16 v[36:39], v[24:27], v[96:99], 0\n"
        "s_waitcnt lgkmcnt(1)\n"
        "v_smfmac_f32_16x16x64_bf16 v[36:39], v[28:31], v[88:95], v18\n"
        "ds_read_b64_tr_b16 v[78:79], v9 offset:6704\n"
        "v_readfirstlane_b32 s40, v167\n"
        "v_mfma_f32_16x16x32_bf16 v[40:43], v[24:27], v[104:107], 0\n"
        "buffer_store_dwordx3 v[164:166], v14, s[8:11], s37 offen offset:960 sc0 sc1 nt\n"
        "s_add_u32 s37, s35, 588672\n"
        "s_add_u32 s46, s37, 1344\n"
        "s_cmp_lt_i32 s40, s68\n"
        "s_waitcnt lgkmcnt(1)\n"
        "v_smfmac_f32_16x16x64_bf16 v[40:43], v[28:31], v[112:119], v18\n"
        "buffer_store_dwordx3 v[148:150], v12, s[44:47], s37 offen offset:1152 sc0 sc1 nt\n"
        "ds_read_b64_tr_b16 v[90:91], v9 offset:6720\n"
        "v_mfma_f32_16x16x32_bf16 v[64:67], v[24:27], v[44:47], 0\n"
        "s_waitcnt lgkmcnt(1)\n"
        "v_smfmac_f32_16x16x64_bf16 v[64:67], v[28:31], v[76:83], v18 abid:1\n"
        "ds_read_b64_tr_b16 v[114:115], v9 offset:6736\n"
        "s_mov_b64 exec, 1\n"
        "ds_inc_u32 v0, v1 offset:21608\n"
        "s_mov_b64 exec, -1\n"
        "s_cbranch_scc1 L56A_%=\n"
        "L57A_%=:\n"
        "ds_read_b128 v[124:127], v7 offset:40064\n"
        "ds_read_b128 v[132:135], v7 offset:43392\n"
        "ds_read_b128 v[128:131], v7 offset:40128\n"
        "ds_read_b128 v[136:139], v7 offset:43456\n"
        "v_mfma_f32_16x16x32_bf16 v[104:107], v[24:27], v[100:103], 0\n"
        "s_waitcnt lgkmcnt(6)\n"
        "ds_read_b128 v[160:163], v7 offset:40192\n"
        "ds_read_b128 v[52:55], v7 offset:43520\n"
        "s_mov_b32 s36, s35\n"
        "v_smfmac_f32_16x16x64_bf16 v[104:107], v[28:31], v[88:95], v18 abid:1\n"
        "s_mov_b32 s35, s39\n"
        "v_mfma_f32_16x16x32_bf16 v[88:91], v[24:27], v[108:111], 0\n"
        "s_waitcnt lgkmcnt(7)\n"
        "v_smfmac_f32_16x16x64_bf16 v[88:91], v[28:31], v[112:119], v18 abid:1\n"
        "s_setprio 0\n"
        "s_waitcnt lgkmcnt(0)\n"
        "s_nop 15\n"
        "s_branch CNEXTA_%=\n"
        "MTOPB_%=:\n"
        "s_waitcnt lgkmcnt(5)\n"
        "v_mfma_f32_16x16x32_bf16 v[24:27], v[168:171], v[124:127], 0\n"
        "v_cvt_pk_bf16_f32 v112, v152, v56\n"
        "v_cvt_pk_bf16_f32 v113, v72, v20\n"
        "v_cvt_pk_bf16_f32 v114, v84, v60\n"
        "v_cvt_pk_bf16_f32 v116, v153, v57\n"
        "v_cvt_pk_bf16_f32 v117, v73, v21\n"
        "v_cvt_pk_bf16_f32 v118, v85, v61\n"
        "v_cvt_pk_bf16_f32 v120, v154, v58\n"
        "v_cvt_pk_bf16_f32 v121, v74, v22\n"
        "s_waitcnt lgkmcnt(3)\n"
        "v_mfma_f32_16x16x32_bf16 v[24:27], v[172:175], v[128:131], v[24:27]\n"
        "v_cvt_pk_bf16_f32 v122, v86, v62\n"
        "v_cvt_pk_bf16_f32 v140, v155, v59\n"
        "v_cvt_pk_bf16_f32 v141, v75, v23\n"
        "v_cvt_pk_bf16_f32 v142, v87, v63\n"
        "v_cvt_pk_bf16_f32 v144, v68, v36\n"
        "v_cvt_pk_bf16_f32 v145, v40, v64\n"
        "v_cvt_pk_bf16_f32 v146, v104, v88\n"
        "v_cvt_pk_bf16_f32 v148, v69, v37\n"
        "s_waitcnt lgkmcnt(1)\n"
        "v_mfma_f32_16x16x32_bf16 v[24:27], v[176:179], v[160:163], v[24:27]\n"
        "v_cvt_pk_bf16_f32 v149, v41, v65\n"
        "v_cvt_pk_bf16_f32 v150, v105, v89\n"
        "v_cvt_pk_bf16_f32 v152, v70, v38\n"
        "v_cvt_pk_bf16_f32 v153, v42, v66\n"
        "v_cvt_pk_bf16_f32 v154, v106, v90\n"
        "v_cvt_pk_bf16_f32 v156, v71, v39\n"
        "v_cvt_pk_bf16_f32 v157, v43, v67\n"
        "v_cvt_pk_bf16_f32 v158, v107, v91\n"
        "v_mfma_f32_16x16x32_bf16 v[32:35], v[188:191], v[160:163], 0\n"
        "v_cvt_pk_bf16_f32 v40, v24, v25\n"
        "v_cvt_pk_bf16_f32 v41, v26, v27\n"
        "v_mfma_f32_16x16x32_bf16 v[32:35], v[184:187], v[128:131], v[32:35]\n"
        "v_mfma_f32_16x16x32_bf16 v[32:35], v[180:183], v[124:127], v[32:35]\n"
        "s_add_u32 s37, s36, 196224\n"
        "ds_read_b128 v[28:31], v16 offset:128\n"
        "ds_read_b32 v159, v10\n"
        "v_mfma_f32_16x16x32_bf16 v[24:27], v[192:195], v[124:127], 0\n"
        "ds_read_b128 v[20:23], v5\n"
        "v_mfma_f32_16x16x32_bf16 v[24:27], v[196:199], v[128:131], v[24:27]\n"
        "ds_read_b64 v[164:165], v11 offset:192\n"
        "v_mfma_f32_16x16x32_bf16 v[24:27], v[200:203], v[160:163], v[24:27]\n"
        "buffer_store_dwordx3 v[112:114], v12, s[8:11], s37 offen offset:384 sc0 sc1 nt\n"
        "v_cvt_pk_bf16_f32 v42, v32, v33\n"
        "v_cvt_pk_bf16_f32 v43, v34, v35\n"
        "s_waitcnt lgkmcnt(3)\n"
        "v_mfma_f32_16x16x32_bf16 v[48:51], v[28:31], v[160:163], 0\n"
        "ds_read_b128 v[60:63], v7 offset:46848\n"
        "ds_read_i16 v167, v0 offset:20584\n"
        "ds_read_i16 v155, v0 offset:20586\n"
        "v_cvt_pk_bf16_f32 v44, v24, v25\n"
        "v_cvt_pk_bf16_f32 v45, v26, v27\n"
        "s_waitcnt lgkmcnt(3)\n"
        "v_smfmac_f32_16x16x64_bf16 v[48:51], v[20:23], v[124:131], v4\n"
        "s_add_u32 s37, s36, 196224\n"
        "ds_read_b128 v[32:35], v7 offset:46720\n"
        "ds_read_b128 v[36:39], v7 offset:46784\n"
        "v_mfma_f32_16x16x32_bf16 v[56:59], v[28:31], v[52:55], 0\n"
        "v_smfmac_f32_16x16x64_bf16 v[56:59], v[20:23], v[132:139], v4\n"
        "s_setprio 2\n"
        "s_waitcnt lgkmcnt(4)\n"
        "v_mfma_f32_16x16x32_bf16 v[24:27], v[28:31], v[60:63], 0\n"
        "buffer_store_dwordx3 v[116:118], v13, s[8:11], s37 offen offset:1152 sc0 sc1 nt\n"
        "v_add_f32_dpp v165, v50, v51 quad_perm:[0,1,2,3] row_mask:0x1 bank_mask:0xf\n"
        "s_waitcnt lgkmcnt(2)\n"
        "v_add_u32 v167, v9, v167\n"
        "v_add_f32_dpp v164, v48, v49 quad_perm:[0,1,2,3] row_mask:0x1 bank_mask:0xf\n"
        "v_add_f32_dpp v165, v58, v59 quad_perm:[0,1,2,3] row_mask:0x2 bank_mask:0xf\n"
        "v_add_u32 v155, v9, v155\n"
        "v_add_f32_dpp v164, v56, v57 quad_perm:[0,1,2,3] row_mask:0x2 bank_mask:0xf\n"
        "s_waitcnt lgkmcnt(0)\n"
        "v_smfmac_f32_16x16x64_bf16 v[24:27], v[20:23], v[32:39], v4\n"
        "v_mfma_f32_16x16x32_bf16 v[28:31], v[212:215], v[160:163], 0\n"
        "s_add_u32 s37, s36, 294336\n"
        "v_mfma_f32_16x16x32_bf16 v[28:31], v[208:211], v[128:131], v[28:31]\n"
        "v_mfma_f32_16x16x32_bf16 v[28:31], v[204:207], v[124:127], v[28:31]\n"
        "v_mfma_f32_16x16x32_bf16 v[20:23], v[216:219], v[124:127], 0\n"
        "buffer_store_dwordx3 v[120:122], v12, s[8:11], s37 offen offset:576 sc0 sc1 nt\n"
        "v_mfma_f32_16x16x32_bf16 v[20:23], v[220:223], v[128:131], v[20:23]\n"
        "v_mfma_f32_16x16x32_bf16 v[20:23], v[224:227], v[160:163], v[20:23]\n"
        "s_add_u32 s37, s36, 392448\n"
        "v_add_f32_dpp v165, v26, v27 quad_perm:[0,1,2,3] row_mask:0x4 bank_mask:0xf\n"
        "v_mul_f32 v166, s52, v165\n"
        "v_fma_f32 v166, -v166, v165, v159\n"
        "v_cvt_pk_bf16_f32 v46, v28, v29\n"
        "v_cvt_pk_bf16_f32 v47, v30, v31\n"
        "v_add_f32_e64 v166, s53, |v166|\n"
        "v_rsq_f32 v159, v166\n"
        "v_mfma_f32_16x16x32_bf16 v[56:59], v[236:239], v[160:163], 0\n"
        "s_mov_b32 exec_hi, 0xffff\n"
        "ds_write_b32 v10, v159\n"
        "s_mov_b32 exec_hi, -1\n"
        "v_cvt_pk_bf16_f32 v48, v20, v21\n"
        "v_cvt_pk_bf16_f32 v49, v22, v23\n"
        "ds_read_b32 v20, v167 offset:40064\n"
        "v_add_f32_dpp v164, v24, v25 quad_perm:[0,1,2,3] row_mask:0x4 bank_mask:0xf\n"
        "v_mfma_f32_16x16x32_bf16 v[56:59], v[232:235], v[128:131], v[56:59]\n"
        "s_mov_b32 exec_hi, 0xffff\n"
        "v_add_u32 v10, -40064, v10\n"
        "s_mov_b32 exec_hi, -1\n"
        "ds_read_b32 v21, v167 offset:40896\n"
        "ds_read_b32 v22, v167 offset:41728\n"
        "ds_read_b32 v23, v167 offset:42560\n"
        "ds_read_b32 v24, v167 offset:43392\n"
        "v_mul_f32 v164, v164, v159\n"
        "v_mfma_f32_16x16x32_bf16 v[56:59], v[228:231], v[124:127], v[56:59]\n"
        "buffer_store_dwordx3 v[140:142], v12, s[8:11], s37 offen sc0 sc1 nt\n"
        "ds_read_b32 v25, v167 offset:44224\n"
        "ds_read_b32 v26, v167 offset:45056\n"
        "ds_read_b32 v27, v167 offset:45888\n"
        "ds_read_b32 v28, v167 offset:46720\n"
        "ds_read_b32 v29, v167 offset:47552\n"
        "ds_read_b32 v30, v167 offset:48384\n"
        "ds_read_b32 v31, v167 offset:49216\n"
        "v_cvt_pk_bf16_f32 v50, v56, v57\n"
        "v_cvt_pk_bf16_f32 v51, v58, v59\n"
        "v_readlane_b32 s58, v159, 56\n"
        "v_mfma_f32_16x16x32_bf16 v[56:59], v[124:127], v[40:43], 0\n"
        "s_mov_b64 exec, s[56:57]\n"
        "v_mov_b32 v164, 0xff800000\n"
        "s_mov_b64 exec, -1\n"
        "ds_read_b32 v165, v155 offset:40064\n"
        "ds_read_b32 v166, v155 offset:43392\n"
        "v_max_f32_dpp v151, v164, v164 row_ror:8 row_mask:0xf bank_mask:0xf\n"
        "v_mfma_f32_16x16x32_bf16 v[56:59], v[128:131], v[44:47], v[56:59]\n"
        "s_add_u32 s37, s36, 392448\n"
        "v_max_f32_dpp v151, v151, v151 row_ror:4 row_mask:0xf bank_mask:0xf\n"
        "ds_read_b32 v167, v155 offset:46720\n"
        "v_mfma_f32_16x16x32_bf16 v[56:59], v[160:163], v[48:51], v[56:59]\n"
        "v_max_f32_dpp v151, v151, v151 row_ror:2 row_mask:0xf bank_mask:0xf\n"
        "v_mfma_f32_16x16x32_bf16 v[72:75], v[52:55], v[48:51], 0\n"
        "v_mfma_f32_16x16x32_bf16 v[72:75], v[136:139], v[44:47], v[72:75]\n"
        "buffer_store_dwordx3 v[144:146], v15, s[8:11], s37 offen offset:768 sc0 sc1 nt\n"
        "v_max_f32_dpp v151, v151, v151 row_ror:1 row_mask:0xf bank_mask:0xf\n"
        "v_mfma_f32_16x16x32_bf16 v[72:75], v[132:135], v[40:43], v[72:75]\n"
        "ds_read_b128 v[88:91], v8 offset:128\n"
        "v_mfma_f32_16x16x32_bf16 v[68:71], v[32:35], v[40:43], 0\n"
        "s_add_u32 s37, s36, 490560\n"
        "ds_read2_b32 v[92:93], v6 offset0:0 offset1:2\n"
        "ds_read2_b32 v[94:95], v6 offset0:8 offset1:10\n"
        "s_waitcnt lgkmcnt(3)\n"
        "v_mul_f32 v56, v56, v20\n"
        "v_mul_f32 v57, v57, v21\n"
        "v_mul_f32 v58, v58, v22\n"
        "v_mul_f32 v59, v59, v23\n"
        "v_mul_f32 v72, v72, v24\n"
        "v_mfma_f32_16x16x32_bf16 v[68:71], v[36:39], v[44:47], v[68:71]\n"
        "v_max3_f32 v155, v56, v57, v58\n"
        "v_mul_f32 v73, v73, v25\n"
        "v_mul_f32 v74, v74, v26\n"
        "v_max3_f32 v155, v155, v59, v72\n"
        "v_mul_f32 v75, v75, v27\n"
        "v_max3_f32 v155, v155, v73, v74\n"
        "v_mfma_f32_16x16x32_bf16 v[68:71], v[60:63], v[48:51], v[68:71]\n"
        "s_waitcnt lgkmcnt(2)\n"
        "v_mfma_f32_16x16x32_bf16 v[76:79], v[88:91], v[48:51], 0\n"
        "buffer_store_dwordx3 v[148:150], v12, s[8:11], s37 offen offset:192 sc0 sc1 nt\n"
        "s_waitcnt lgkmcnt(0)\n"
        "v_smfmac_f32_16x16x64_bf16 v[76:79], v[92:95], v[40:47], v4\n"
        "v_mfma_f32_16x16x32_bf16 v[48:51], v[168:171], v[132:135], 0\n"
        "s_add_u32 s37, s36, 490560\n"
        "v_mfma_f32_16x16x32_bf16 v[48:51], v[172:175], v[136:139], v[48:51]\n"
        "v_mul_f32 v68, v68, v28\n"
        "v_mul_f32 v69, v69, v29\n"
        "v_mul_f32 v70, v70, v30\n"
        "v_max3_f32 v155, v155, v75, v68\n"
        "v_mul_f32 v71, v71, v31\n"
        "v_add_f32 v76, v76, v77\n"
        "v_mul_f32 v76, s58, v76\n"
        "v_max3_f32 v155, v155, v69, v70\n"
        "v_mfma_f32_16x16x32_bf16 v[48:51], v[176:179], v[52:55], v[48:51]\n"
        "s_mov_b64 exec, s[48:49]\n"
        "v_mov_b32 v76, 0xff800000\n"
        "s_mov_b64 exec, -1\n"
        "v_max3_f32 v155, v155, v71, v76\n"
        "v_mfma_f32_16x16x32_bf16 v[44:47], v[188:191], v[52:55], 0\n"
        "buffer_store_dwordx3 v[152:154], v14, s[8:11], s37 offen offset:960 sc0 sc1 nt\n"
        "v_permlane32_swap_b32 v155, v151\n"
        "v_max_f32 v155, v155, v151\n"
        "v_cvt_pk_bf16_f32 v40, v48, v49\n"
        "v_cvt_pk_bf16_f32 v41, v50, v51\n"
        "v_mov_b32 v154, v155\n"
        "v_mfma_f32_16x16x32_bf16 v[44:47], v[184:187], v[136:139], v[44:47]\n"
        "v_mfma_f32_16x16x32_bf16 v[44:47], v[180:183], v[132:135], v[44:47]\n"
        "s_add_u32 s37, s36, 588672\n"
        "v_permlane16_swap_b32 v155, v154\n"
        "v_max_f32 v155, v155, v154\n"
        "v_mov_b32 v151, v155\n"
        "v_mfma_f32_16x16x32_bf16 v[64:67], v[192:195], v[132:135], 0\n"
        "v_mfma_f32_16x16x32_bf16 v[64:67], v[196:199], v[136:139], v[64:67]\n"
        "v_permlane32_swap_b32 v155, v151\n"
        "v_mul_f32 v155, v155, v165\n"
        "v_cvt_pk_bf16_f32 v42, v44, v45\n"
        "v_cvt_pk_bf16_f32 v43, v46, v47\n"
        "v_fma_f32 v56, v56, v165, -v155\n"
        "v_fma_f32 v57, v57, v165, -v155\n"
        "v_fma_f32 v58, v58, v165, -v155\n"
        "v_mfma_f32_16x16x32_bf16 v[64:67], v[200:203], v[52:55], v[64:67]\n"
        "buffer_store_dwordx3 v[156:158], v12, s[8:11], s37 offen offset:384 sc0 sc1 nt\n"
        "v_exp_f32 v56, v56\n"
        "v_exp_f32 v57, v57\n"
        "v_exp_f32 v58, v58\n"
        "v_fma_f32 v59, v59, v165, -v155\n"
        "v_add_f32 v158, v56, v57\n"
        "v_exp_f32 v59, v59\n"
        "v_mfma_f32_16x16x32_bf16 v[48:51], v[212:215], v[52:55], 0\n"
        "v_cvt_pk_bf16_f32 v44, v64, v65\n"
        "v_cvt_pk_bf16_f32 v45, v66, v67\n"
        "v_fma_f32 v72, v72, v165, -v155\n"
        "v_add_f32 v158, v158, v58\n"
        "v_exp_f32 v72, v72\n"
        "v_fma_f32 v73, v73, v165, -v155\n"
        "v_add_f32 v158, v158, v59\n"
        "v_mfma_f32_16x16x32_bf16 v[48:51], v[208:211], v[136:139], v[48:51]\n"
        "v_exp_f32 v73, v73\n"
        "v_fma_f32 v74, v74, v165, -v155\n"
        "v_add_f32 v158, v158, v72\n"
        "v_exp_f32 v74, v74\n"
        "v_fma_f32 v75, v75, v165, -v155\n"
        "v_sub_f32 v164, v164, v151\n"
        "v_mfma_f32_16x16x32_bf16 v[48:51], v[204:207], v[132:135], v[48:51]\n"
        "v_add_f32 v158, v158, v73\n"
        "v_exp_f32 v75, v75\n"
        "v_fma_f32 v68, v68, v165, -v155\n"
        "v_mul_f32 v164, s58, v164\n"
        "v_add_f32 v158, v158, v74\n"
        "v_exp_f32 v68, v68\n"
        "v_mfma_f32_16x16x32_bf16 v[64:67], v[216:219], v[132:135], 0\n"
        "v_fma_f32 v69, v69, v165, -v155\n"
        "v_cvt_pk_bf16_f32 v46, v48, v49\n"
        "v_cvt_pk_bf16_f32 v47, v50, v51\n"
        "v_exp_f32 v164, v164\n"
        "v_add_f32 v158, v158, v75\n"
        "v_exp_f32 v69, v69\n"
        "v_mfma_f32_16x16x32_bf16 v[64:67], v[220:223], v[136:139], v[64:67]\n"
        "v_fma_f32 v70, v70, v165, -v155\n"
        "v_add_f32 v158, v158, v68\n"
        "v_exp_f32 v70, v70\n"
        "v_fma_f32 v71, v71, v165, -v155\n"
        "v_add_f32_dpp v157, v164, v164 row_ror:8 row_mask:0xf bank_mask:0xf\n"
        "v_add_f32 v158, v158, v69\n"
        "v_fma_f32 v76, v76, v165, -v155\n"
        "v_mfma_f32_16x16x32_bf16 v[64:67], v[224:227], v[52:55], v[64:67]\n"
        "v_exp_f32 v71, v71\n"
        "v_add_f32_dpp v157, v157, v157 row_ror:4 row_mask:0xf bank_mask:0xf\n"
        "v_add_f32 v158, v158, v70\n"
        "v_exp_f32 v76, v76\n"
        "v_add_f32_dpp v157, v157, v157 row_ror:2 row_mask:0xf bank_mask:0xf\n"
        "v_add_f32 v158, v158, v71\n"
        "v_mfma_f32_16x16x32_bf16 v[48:51], v[236:239], v[52:55], 0\n"
        "v_add_f32_dpp v157, v157, v157 row_ror:1 row_mask:0xf bank_mask:0xf\n"
        "v_cvt_pk_bf16_f32 v80, v64, v65\n"
        "v_cvt_pk_bf16_f32 v81, v66, v67\n"
        "v_add_f32 v158, v158, v76\n"
        "v_mul_f32 v56, v56, v20\n"
        "v_mul_f32 v57, v57, v21\n"
        "v_permlane32_swap_b32 v158, v157\n"
        "v_mfma_f32_16x16x32_bf16 v[48:51], v[232:235], v[136:139], v[48:51]\n"
        "v_add_f32 v158, v158, v157\n"
        "v_mov_b32 v165, v158\n"
        "v_mul_f32 v58, v58, v22\n"
        "v_mul_f32 v59, v59, v23\n"
        "v_permlane16_swap_b32 v158, v165\n"
        "v_add_f32 v158, v158, v165\n"
        "v_mul_f32 v72, v72, v24\n"
        "v_mfma_f32_16x16x32_bf16 v[48:51], v[228:231], v[132:135], v[48:51]\n"
        "v_rcp_f32 v158, v158\n"
        "v_mul_f32 v73, v73, v25\n"
        "v_mov_b32 v157, v158\n"
        "v_mul_f32 v74, v74, v26\n"
        "v_mul_f32 v75, v75, v27\n"
        "v_permlane32_swap_b32 v158, v157\n"
        "v_mfma_f32_16x16x32_bf16 v[64:67], v[124:127], v[40:43], 0\n"
        "v_mul_f32 v56, v56, v158\n"
        "v_cvt_pk_bf16_f32 v82, v48, v49\n"
        "v_cvt_pk_bf16_f32 v83, v50, v51\n"
        "v_mul_f32 v57, v57, v158\n"
        "v_mul_f32 v58, v58, v158\n"
        "v_mul_f32 v59, v59, v158\n"
        "v_mul_f32 v72, v72, v158\n"
        "v_mul_f32 v73, v73, v158\n"
        "v_mfma_f32_16x16x32_bf16 v[64:67], v[128:131], v[44:47], v[64:67]\n"
        "v_mul_f32 v74, v74, v158\n"
        "v_mul_f32 v75, v75, v158\n"
        "v_cvt_pk_bf16_f32 v48, v56, v57\n"
        "v_cvt_pk_bf16_f32 v49, v58, v59\n"
        "v_cvt_pk_bf16_f32 v50, v72, v73\n"
        "v_cvt_pk_bf16_f32 v51, v74, v75\n"
        "v_mul_f32 v68, v68, v28\n"
        "v_mul_f32 v69, v69, v29\n"
        "v_mfma_f32_16x16x32_bf16 v[64:67], v[160:163], v[80:83], v[64:67]\n"
        "v_mul_f32 v70, v70, v30\n"
        "v_mul_f32 v71, v71, v31\n"
        "v_mul_f32 v76, s58, v76\n"
        "v_mul_f32 v68, v68, v158\n"
        "v_mul_f32 v69, v69, v158\n"
        "v_mul_f32 v70, v70, v158\n"
        "v_mul_f32 v71, v71, v158\n"
        "v_fma_f32 v76, v76, v158, v19\n"
        "v_mfma_f32_16x16x32_bf16 v[84:87], v[52:55], v[80:83], 0\n"
        "v_mul_f32 v64, v64, v20\n"
        "v_mul_f32 v65, v65, v21\n"
        "v_mul_f32 v66, v66, v22\n"
        "v_mul_f32 v67, v67, v23\n"
        "v_max3_f32 v165, v64, v65, v66\n"
        "v_cvt_pk_bf16_f32 v56, v68, v69\n"
        "v_cvt_pk_bf16_f32 v58, v70, v71\n"
        "v_cvt_pk_bf16_f32 v57, v76, 0\n"
        "v_mfma_f32_16x16x32_bf16 v[84:87], v[136:139], v[44:47], v[84:87]\n"
        "v_mul_f32 v164, v164, v159\n"
        "v_mul_f32 v164, v164, v157\n"
        "v_cvt_pk_bf16_f32 v164, v164, 0\n"
        "ds_write_b16 v1, v164\n"
        "v_mfma_f32_16x16x32_bf16 v[84:87], v[132:135], v[40:43], v[84:87]\n"
        "v_mfma_f32_16x16x32_bf16 v[68:71], v[32:35], v[40:43], 0\n"
        "v_mfma_f32_16x16x32_bf16 v[68:71], v[36:39], v[44:47], v[68:71]\n"
        "v_mfma_f32_16x16x32_bf16 v[68:71], v[60:63], v[80:83], v[68:71]\n"
        "v_mfma_f32_16x16x32_bf16 v[72:75], v[88:91], v[80:83], 0\n"
        "v_smfmac_f32_16x16x64_bf16 v[72:75], v[92:95], v[40:47], v4\n"
        "v_mfma_f32_16x16x32_bf16 v[80:83], v[168:171], v[32:35], 0\n"
        "v_mfma_f32_16x16x32_bf16 v[80:83], v[172:175], v[36:39], v[80:83]\n"
        "v_mfma_f32_16x16x32_bf16 v[80:83], v[176:179], v[60:63], v[80:83]\n"
        "v_mul_f32 v84, v84, v24\n"
        "v_mul_f32 v85, v85, v25\n"
        "v_mul_f32 v86, v86, v26\n"
        "v_max3_f32 v165, v165, v67, v84\n"
        "v_mul_f32 v87, v87, v27\n"
        "v_add_f32 v72, v72, v73\n"
        "v_mul_f32 v68, v68, v28\n"
        "v_mul_f32 v72, s58, v72\n"
        "v_mfma_f32_16x16x32_bf16 v[40:43], v[188:191], v[60:63], 0\n"
        "s_mov_b64 exec, s[48:49]\n"
        "v_mov_b32 v72, 0xff800000\n"
        "s_mov_b64 exec, -1\n"
        "v_cvt_pk_bf16_f32 v76, v80, v81\n"
        "v_cvt_pk_bf16_f32 v77, v82, v83\n"
        "v_max3_f32 v165, v165, v85, v86\n"
        "v_mul_f32 v69, v69, v29\n"
        "v_mul_f32 v70, v70, v30\n"
        "v_max3_f32 v165, v165, v87, v68\n"
        "v_mfma_f32_16x16x32_bf16 v[40:43], v[184:187], v[36:39], v[40:43]\n"
        "v_mul_f32 v71, v71, v31\n"
        "v_max3_f32 v165, v165, v69, v70\n"
        "v_max3_f32 v165, v165, v71, v72\n"
        "v_mfma_f32_16x16x32_bf16 v[40:43], v[180:183], v[32:35], v[40:43]\n"
        "v_mfma_f32_16x16x32_bf16 v[100:103], v[192:195], v[32:35], 0\n"
        "v_mfma_f32_16x16x32_bf16 v[100:103], v[196:199], v[36:39], v[100:103]\n"
        "v_mfma_f32_16x16x32_bf16 v[100:103], v[200:203], v[60:63], v[100:103]\n"
        "v_mfma_f32_16x16x32_bf16 v[96:99], v[212:215], v[60:63], 0\n"
        "v_mfma_f32_16x16x32_bf16 v[96:99], v[208:211], v[36:39], v[96:99]\n"
        "v_mfma_f32_16x16x32_bf16 v[96:99], v[204:207], v[32:35], v[96:99]\n"
        "v_mfma_f32_16x16x32_bf16 v[44:47], v[216:219], v[32:35], 0\n"
        "v_mfma_f32_16x16x32_bf16 v[44:47], v[220:223], v[36:39], v[44:47]\n"
        "v_cvt_pk_bf16_f32 v78, v40, v41\n"
        "v_cvt_pk_bf16_f32 v79, v42, v43\n"
        "v_mfma_f32_16x16x32_bf16 v[44:47], v[224:227], v[60:63], v[44:47]\n"
        "v_cvt_pk_bf16_f32 v80, v100, v101\n"
        "v_cvt_pk_bf16_f32 v81, v102, v103\n"
        "v_mfma_f32_16x16x32_bf16 v[40:43], v[236:239], v[60:63], 0\n"
        "v_cvt_pk_bf16_f32 v82, v96, v97\n"
        "v_cvt_pk_bf16_f32 v83, v98, v99\n"
        "v_mfma_f32_16x16x32_bf16 v[40:43], v[232:235], v[36:39], v[40:43]\n"
        "v_mfma_f32_16x16x32_bf16 v[40:43], v[228:231], v[32:35], v[40:43]\n"
        "v_mfma_f32_16x16x32_bf16 v[108:111], v[124:127], v[76:79], 0\n"
        "v_cvt_pk_bf16_f32 v96, v44, v45\n"
        "v_cvt_pk_bf16_f32 v97, v46, v47\n"
        "v_mfma_f32_16x16x32_bf16 v[108:111], v[128:131], v[80:83], v[108:111]\n"
        "s_nop 3\n"
        "v_cvt_pk_bf16_f32 v98, v40, v41\n"
        "v_cvt_pk_bf16_f32 v99, v42, v43\n"
        "s_nop 1\n"
        "v_mfma_f32_16x16x32_bf16 v[108:111], v[160:163], v[96:99], v[108:111]\n"
        "v_mfma_f32_16x16x32_bf16 v[140:143], v[52:55], v[96:99], 0\n"
        "v_mfma_f32_16x16x32_bf16 v[140:143], v[136:139], v[80:83], v[140:143]\n"
        "v_mfma_f32_16x16x32_bf16 v[140:143], v[132:135], v[76:79], v[140:143]\n"
        "v_mfma_f32_16x16x32_bf16 v[100:103], v[32:35], v[76:79], 0\n"
        "v_mfma_f32_16x16x32_bf16 v[100:103], v[36:39], v[80:83], v[100:103]\n"
        "v_mfma_f32_16x16x32_bf16 v[100:103], v[60:63], v[96:99], v[100:103]\n"
        "v_mfma_f32_16x16x32_bf16 v[44:47], v[88:91], v[96:99], 0\n"
        "v_smfmac_f32_16x16x64_bf16 v[44:47], v[92:95], v[76:83], v4\n"
        "v_mul_f32 v108, v108, v20\n"
        "v_mul_f32 v109, v109, v21\n"
        "v_mul_f32 v110, v110, v22\n"
        "v_mul_f32 v111, v111, v23\n"
        "v_mul_f32 v140, v140, v24\n"
        "v_max3_f32 v159, v108, v109, v110\n"
        "v_mul_f32 v141, v141, v25\n"
        "v_mfma_f32_16x16x32_bf16 v[60:63], v[124:127], v[240:243], 0\n"
        "v_add_f32 v44, v44, v45\n"
        "v_mul_f32 v142, v142, v26\n"
        "v_max3_f32 v159, v159, v111, v140\n"
        "v_mul_f32 v143, v143, v27\n"
        "v_mul_f32 v100, v100, v28\n"
        "v_mul_f32 v44, s58, v44\n"
        "v_max3_f32 v159, v159, v141, v142\n"
        "v_mul_f32 v101, v101, v29\n"
        "v_mfma_f32_16x16x32_bf16 v[60:63], v[128:131], v[244:247], v[60:63]\n"
        "s_mov_b64 exec, s[48:49]\n"
        "v_mov_b32 v44, 0xff800000\n"
        "s_mov_b64 exec, -1\n"
        "v_mul_f32 v102, v102, v30\n"
        "v_max3_f32 v159, v159, v143, v100\n"
        "v_mul_f32 v103, v103, v31\n"
        "v_max3_f32 v159, v159, v101, v102\n"
        "v_max3_f32 v159, v159, v103, v44\n"
        "v_mfma_f32_16x16x32_bf16 v[60:63], v[160:163], v[248:251], v[60:63]\n"
        "ds_read_b128 v[32:35], v17 offset:40064\n"
        "v_permlane32_swap_b32 v165, v159\n"
        "v_max_f32 v165, v165, v159\n"
        "v_mov_b32 v164, v165\n"
        "v_mfma_f32_16x16x32_bf16 v[76:79], v[52:55], v[248:251], 0\n"
        "ds_read_b128 v[36:39], v17 offset:40128\n"
        "v_permlane16_swap_b32 v165, v164\n"
        "v_max_f32 v165, v165, v164\n"
        "v_cvt_pk_bf16_f32 v40, v60, v61\n"
        "v_cvt_pk_bf16_f32 v41, v62, v63\n"
        "v_mov_b32 v159, v165\n"
        "v_mfma_f32_16x16x32_bf16 v[76:79], v[136:139], v[244:247], v[76:79]\n"
        "ds_read_b128 v[60:63], v17 offset:40192\n"
        "v_permlane32_swap_b32 v165, v159\n"
        "v_mul_f32 v165, v165, v166\n"
        "v_mul_f32 v159, v159, v167\n"
        "v_fma_f32 v64, v64, v166, -v165\n"
        "v_fma_f32 v65, v65, v166, -v165\n"
        "v_fma_f32 v108, v108, v167, -v159\n"
        "v_mfma_f32_16x16x32_bf16 v[76:79], v[132:135], v[240:243], v[76:79]\n"
        "s_waitcnt lgkmcnt(2)\n"
        "ds_read_b128 v[96:99], v0 offset:5136\n"
        "v_fma_f32 v109, v109, v167, -v159\n"
        "v_exp_f32 v64, v64\n"
        "v_exp_f32 v65, v65\n"
        "v_fma_f32 v66, v66, v166, -v165\n"
        "v_fma_f32 v110, v110, v167, -v159\n"
        "v_mfma_f32_16x16x32_bf16 v[80:83], v[32:35], v[240:243], 0\n"
        "s_waitcnt lgkmcnt(2)\n"
        "ds_read_b128 v[104:107], v0 offset:4112\n"
        "v_cvt_pk_bf16_f32 v42, v76, v77\n"
        "v_cvt_pk_bf16_f32 v43, v78, v79\n"
        "v_exp_f32 v108, v108\n"
        "v_exp_f32 v109, v109\n"
        "v_fma_f32 v67, v67, v166, -v165\n"
        "v_mfma_f32_16x16x32_bf16 v[80:83], v[36:39], v[244:247], v[80:83]\n"
        "s_waitcnt lgkmcnt(2)\n"
        "v_exp_f32 v66, v66\n"
        "v_exp_f32 v110, v110\n"
        "v_fma_f32 v111, v111, v167, -v159\n"
        "v_add_f32 v95, v64, v65\n"
        "v_exp_f32 v67, v67\n"
        "v_mfma_f32_16x16x32_bf16 v[80:83], v[60:63], v[248:251], v[80:83]\n"
        "s_waitcnt lgkmcnt(1)\n"
        "v_fma_f32 v84, v84, v166, -v165\n"
        "v_add_f32 v164, v108, v109\n"
        "v_exp_f32 v111, v111\n"
        "v_fma_f32 v140, v140, v167, -v159\n"
        "v_add_f32 v95, v95, v66\n"
        "v_exp_f32 v84, v84\n"
        "v_mfma_f32_16x16x32_bf16 v[112:115], v[60:63], v[96:99], 0\n"
        "s_waitcnt lgkmcnt(0)\n"
        "v_cvt_pk_bf16_f32 v76, v80, v81\n"
        "v_cvt_pk_bf16_f32 v80, v82, v83\n"
        "v_fma_f32 v85, v85, v166, -v165\n"
        "v_add_f32 v164, v164, v110\n"
        "v_exp_f32 v140, v140\n"
        "v_fma_f32 v141, v141, v167, -v159\n"
        "v_add_f32 v95, v95, v67\n"
        "v_mfma_f32_16x16x32_bf16 v[112:115], v[36:39], v[104:107], v[112:115]\n"
        "v_exp_f32 v85, v85\n"
        "v_fma_f32 v86, v86, v166, -v165\n"
        "v_add_f32 v164, v164, v111\n"
        "v_exp_f32 v141, v141\n"
        "v_fma_f32 v142, v142, v167, -v159\n"
        "v_add_f32 v95, v95, v84\n"
        "v_mfma_f32_16x16x32_bf16 v[112:115], v[32:35], v[252:255], v[112:115]\n"
        "v_exp_f32 v86, v86\n"
        "v_fma_f32 v87, v87, v166, -v165\n"
        "v_add_f32 v164, v164, v140\n"
        "v_exp_f32 v142, v142\n"
        "v_fma_f32 v143, v143, v167, -v159\n"
        "v_add_f32 v95, v95, v85\n"
        "v_mfma_f32_16x16x32_bf16 v[120:123], v[132:135], v[252:255], 0\n"
        "v_exp_f32 v87, v87\n"
        "v_cvt_pk_bf16_f32 v88, v112, v113\n"
        "v_cvt_pk_bf16_f32 v92, v114, v115\n"
        "v_fma_f32 v68, v68, v166, -v165\n"
        "v_add_f32 v164, v164, v141\n"
        "v_exp_f32 v143, v143\n"
        "v_mfma_f32_16x16x32_bf16 v[120:123], v[136:139], v[104:107], v[120:123]\n"
        "v_fma_f32 v100, v100, v167, -v159\n"
        "v_add_f32 v95, v95, v86\n"
        "v_exp_f32 v68, v68\n"
        "v_fma_f32 v69, v69, v166, -v165\n"
        "v_add_f32 v164, v164, v142\n"
        "v_exp_f32 v100, v100\n"
        "v_mfma_f32_16x16x32_bf16 v[120:123], v[52:55], v[96:99], v[120:123]\n"
        "ds_read_b128 v[144:147], v0 offset:6176\n"
        "v_fma_f32 v101, v101, v167, -v159\n"
        "v_add_f32 v95, v95, v87\n"
        "v_exp_f32 v69, v69\n"
        "v_fma_f32 v70, v70, v166, -v165\n"
        "v_add_f32 v164, v164, v143\n"
        "v_fma_f32 v102, v102, v167, -v159\n"
        "v_mfma_f32_16x16x32_bf16 v[148:151], v[160:163], v[96:99], 0\n"
        "ds_read_b128 v[116:119], v0 offset:7200\n"
        "v_cvt_pk_bf16_f32 v98, v120, v121\n"
        "v_cvt_pk_bf16_f32 v99, v122, v123\n"
        "v_exp_f32 v101, v101\n"
        "v_add_f32 v95, v95, v68\n"
        "v_exp_f32 v70, v70\n"
        "v_mfma_f32_16x16x32_bf16 v[148:151], v[128:131], v[104:107], v[148:151]\n"
        "ds_read_b128 v[112:115], v0 offset:8224\n"
        "v_fma_f32 v71, v71, v166, -v165\n"
        "v_add_f32 v164, v164, v100\n"
        "v_exp_f32 v102, v102\n"
        "v_fma_f32 v103, v103, v167, -v159\n"
        "v_add_f32 v95, v95, v69\n"
        "v_fma_f32 v72, v72, v166, -v165\n"
        "v_mfma_f32_16x16x32_bf16 v[148:151], v[124:127], v[252:255], v[148:151]\n"
        "s_waitcnt lgkmcnt(2)\n"
        "v_exp_f32 v71, v71\n"
        "v_add_f32 v164, v164, v101\n"
        "v_exp_f32 v103, v103\n"
        "v_fma_f32 v44, v44, v167, -v159\n"
        "v_add_f32 v95, v95, v70\n"
        "v_add_f32 v164, v164, v102\n"
        "v_mfma_f32_16x16x32_bf16 v[120:123], v[124:127], v[144:147], 0\n"
        "s_waitcnt lgkmcnt(1)\n"
        "v_cvt_pk_bf16_f32 v96, v148, v149\n"
        "v_cvt_pk_bf16_f32 v97, v150, v151\n"
        "v_exp_f32 v72, v72\n"
        "v_exp_f32 v44, v44\n"
        "v_add_f32 v95, v95, v71\n"
        "v_add_f32 v164, v164, v103\n"
        "v_mfma_f32_16x16x32_bf16 v[120:123], v[128:131], v[116:119], v[120:123]\n"
        "s_waitcnt lgkmcnt(0)\n"
        "v_add_f32 v95, v95, v72\n"
        "v_add_f32 v164, v164, v44\n"
        "v_mul_f32 v64, v64, v20\n"
        "v_mul_f32 v65, v65, v21\n"
        "v_permlane32_swap_b32 v95, v164\n"
        "v_add_f32 v95, v95, v164\n"
        "v_mov_b32 v167, v95\n"
        "v_mfma_f32_16x16x32_bf16 v[120:123], v[160:163], v[112:115], v[120:123]\n"
        "v_mul_f32 v66, v66, v22\n"
        "v_permlane16_swap_b32 v95, v167\n"
        "v_add_f32 v95, v95, v167\n"
        "v_rcp_f32 v95, v95\n"
        "v_mul_f32 v67, v67, v23\n"
        "v_mov_b32 v164, v95\n"
        "v_mfma_f32_16x16x32_bf16 v[156:159], v[52:55], v[112:115], 0\n"
        "v_mul_f32 v84, v84, v24\n"
        "v_cvt_pk_bf16_f32 v104, v120, v121\n"
        "v_cvt_pk_bf16_f32 v105, v122, v123\n"
        "v_permlane32_swap_b32 v95, v164\n"
        "v_mul_f32 v85, v85, v25\n"
        "v_mul_f32 v86, v86, v26\n"
        "v_mul_f32 v87, v87, v27\n"
        "v_mfma_f32_16x16x32_bf16 v[156:159], v[136:139], v[116:119], v[156:159]\n"
        "v_mul_f32 v64, v64, v95\n"
        "v_mul_f32 v65, v65, v95\n"
        "v_mul_f32 v66, v66, v95\n"
        "v_mul_f32 v67, v67, v95\n"
        "v_mul_f32 v84, v84, v95\n"
        "v_mul_f32 v85, v85, v95\n"
        "v_mul_f32 v86, v86, v95\n"
        "v_mul_f32 v87, v87, v95\n"
        "v_mfma_f32_16x16x32_bf16 v[156:159], v[132:135], v[144:147], v[156:159]\n"
        "ds_read_b128 v[148:151], v0 offset:11312\n"
        "v_cvt_pk_bf16_f32 v120, v64, v65\n"
        "v_cvt_pk_bf16_f32 v121, v66, v67\n"
        "v_cvt_pk_bf16_f32 v122, v84, v85\n"
        "v_cvt_pk_bf16_f32 v123, v86, v87\n"
        "v_mul_f32 v68, v68, v28\n"
        "v_mul_f32 v69, v69, v29\n"
        "v_mul_f32 v70, v70, v30\n"
        "v_mfma_f32_16x16x32_bf16 v[152:155], v[32:35], v[144:147], 0\n"
        "ds_read_b128 v[84:87], v0 offset:10288\n"
        "v_cvt_pk_bf16_f32 v106, v156, v157\n"
        "v_cvt_pk_bf16_f32 v107, v158, v159\n"
        "v_mul_f32 v71, v71, v31\n"
        "v_mul_f32 v72, s58, v72\n"
        "v_mul_f32 v68, v68, v95\n"
        "v_mul_f32 v69, v69, v95\n"
        "v_mul_f32 v70, v70, v95\n"
        "v_mfma_f32_16x16x32_bf16 v[152:155], v[36:39], v[116:119], v[152:155]\n"
        "ds_read_b128 v[144:147], v0 offset:9264\n"
        "v_mul_f32 v71, v71, v95\n"
        "v_fma_f32 v72, v72, v95, v19\n"
        "v_cvt_pk_bf16_f32 v64, v68, v69\n"
        "v_cvt_pk_bf16_f32 v66, v70, v71\n"
        "v_cvt_pk_bf16_f32 v65, v72, 0\n"
        "v_mul_f32 v108, v108, v20\n"
        "v_mul_f32 v109, v109, v21\n"
        "v_mfma_f32_16x16x32_bf16 v[152:155], v[60:63], v[112:115], v[152:155]\n"
        "s_waitcnt lgkmcnt(2)\n"
        "v_mul_f32 v110, v110, v22\n"
        "v_mul_f32 v111, v111, v23\n"
        "v_mul_f32 v140, v140, v24\n"
        "v_mul_f32 v141, v141, v25\n"
        "v_mul_f32 v142, v142, v26\n"
        "v_mul_f32 v143, v143, v27\n"
        "v_mul_f32 v108, v108, v164\n"
        "v_cvt_pk_bf16_f32 v112, v152, v153\n"
        "v_mfma_f32_16x16x32_bf16 v[20:23], v[60:63], v[148:151], 0\n"
        "s_waitcnt lgkmcnt(1)\n"
        "v_cvt_pk_bf16_f32 v116, v154, v155\n"
        "v_mul_f32 v109, v109, v164\n"
        "v_mul_f32 v110, v110, v164\n"
        "v_mul_f32 v111, v111, v164\n"
        "v_mul_f32 v140, v140, v164\n"
        "v_mul_f32 v141, v141, v164\n"
        "v_mul_f32 v142, v142, v164\n"
        "v_mul_f32 v143, v143, v164\n"
        "v_mfma_f32_16x16x32_bf16 v[20:23], v[36:39], v[84:87], v[20:23]\n"
        "s_waitcnt lgkmcnt(0)\n"
        "v_cvt_pk_bf16_f32 v24, v108, v109\n"
        "v_cvt_pk_bf16_f32 v25, v110, v111\n"
        "v_cvt_pk_bf16_f32 v26, v140, v141\n"
        "v_cvt_pk_bf16_f32 v27, v142, v143\n"
        "v_mul_f32 v100, v100, v28\n"
        "v_mul_f32 v101, v101, v29\n"
        "v_mul_f32 v102, v102, v30\n"
        "v_mul_f32 v103, v103, v31\n"
        "v_mfma_f32_16x16x32_bf16 v[20:23], v[32:35], v[144:147], v[20:23]\n"
        "v_mul_f32 v44, s58, v44\n"
        "v_mul_f32 v100, v100, v164\n"
        "v_mul_f32 v101, v101, v164\n"
        "v_mul_f32 v102, v102, v164\n"
        "v_mul_f32 v103, v103, v164\n"
        "v_fma_f32 v44, v44, v164, v19\n"
        "v_cvt_pk_bf16_f32 v28, v100, v101\n"
        "v_cvt_pk_bf16_f32 v30, v102, v103\n"
        "v_mfma_f32_16x16x32_bf16 v[68:71], v[132:135], v[144:147], 0\n"
        "v_cvt_pk_bf16_f32 v77, v20, v21\n"
        "v_cvt_pk_bf16_f32 v81, v22, v23\n"
        "v_cvt_pk_bf16_f32 v29, v44, 0\n"
        "v_mfma_f32_16x16x32_bf16 v[68:71], v[136:139], v[84:87], v[68:71]\n"
        "v_mfma_f32_16x16x32_bf16 v[68:71], v[52:55], v[148:151], v[68:71]\n"
        "ds_read_b128 v[20:23], v0 offset:12352\n"
        "v_mfma_f32_16x16x32_bf16 v[72:75], v[160:163], v[148:151], 0\n"
        "ds_read_b128 v[108:111], v0 offset:13376\n"
        "v_mfma_f32_16x16x32_bf16 v[72:75], v[128:131], v[84:87], v[72:75]\n"
        "ds_read_b128 v[140:143], v0 offset:14400\n"
        "v_mfma_f32_16x16x32_bf16 v[72:75], v[124:127], v[144:147], v[72:75]\n"
        "s_waitcnt lgkmcnt(2)\n"
        "v_mfma_f32_16x16x32_bf16 v[144:147], v[124:127], v[20:23], 0\n"
        "v_cvt_pk_bf16_f32 v46, v68, v69\n"
        "v_cvt_pk_bf16_f32 v47, v70, v71\n"
        "s_waitcnt lgkmcnt(1)\n"
        "v_mfma_f32_16x16x32_bf16 v[144:147], v[128:131], v[108:111], v[144:147]\n"
        "s_waitcnt lgkmcnt(0)\n"
        "v_mfma_f32_16x16x32_bf16 v[144:147], v[160:163], v[140:143], v[144:147]\n"
        "v_cvt_pk_bf16_f32 v44, v72, v73\n"
        "v_cvt_pk_bf16_f32 v45, v74, v75\n"
        "v_mfma_f32_16x16x32_bf16 v[148:151], v[52:55], v[140:143], 0\n"
        "v_mfma_f32_16x16x32_bf16 v[148:151], v[136:139], v[108:111], v[148:151]\n"
        "v_mfma_f32_16x16x32_bf16 v[148:151], v[132:135], v[20:23], v[148:151]\n"
        "ds_read_b128 v[72:75], v0 offset:17488\n"
        "v_mfma_f32_16x16x32_bf16 v[84:87], v[32:35], v[20:23], 0\n"
        "ds_read_b128 v[68:71], v0 offset:16464\n"
        "v_cvt_pk_bf16_f32 v100, v144, v145\n"
        "v_cvt_pk_bf16_f32 v101, v146, v147\n"
        "v_mfma_f32_16x16x32_bf16 v[84:87], v[36:39], v[108:111], v[84:87]\n"
        "ds_read_b128 v[20:23], v0 offset:15440\n"
        "v_mfma_f32_16x16x32_bf16 v[84:87], v[60:63], v[140:143], v[84:87]\n"
        "v_cvt_pk_bf16_f32 v102, v148, v149\n"
        "v_cvt_pk_bf16_f32 v103, v150, v151\n"
        "s_waitcnt lgkmcnt(2)\n"
        "v_mfma_f32_16x16x32_bf16 v[108:111], v[60:63], v[72:75], 0\n"
        "s_waitcnt lgkmcnt(1)\n"
        "v_mfma_f32_16x16x32_bf16 v[108:111], v[36:39], v[68:71], v[108:111]\n"
        "s_waitcnt lgkmcnt(0)\n"
        "v_mfma_f32_16x16x32_bf16 v[108:111], v[32:35], v[20:23], v[108:111]\n"
        "ds_read2_b32 v[82:83], v11 offset0:0 offset1:3\n"
        "v_cvt_pk_bf16_f32 v89, v84, v85\n"
        "v_cvt_pk_bf16_f32 v93, v86, v87\n"
        "v_mov_b32_dpp v59, v19 row_shr:4 row_mask:0xf bank_mask:0xf bound_ctrl:0\n"
        "v_mfma_f32_16x16x32_bf16 v[36:39], v[132:135], v[20:23], 0\n"
        "ds_read_b64_tr_b16 v[78:79], v9 offset:40064\n"
        "v_mfma_f32_16x16x32_bf16 v[36:39], v[136:139], v[68:71], v[36:39]\n"
        "ds_read2_b32 v[94:95], v11 offset0:1 offset1:4\n"
        "v_cvt_pk_bf16_f32 v113, v108, v109\n"
        "v_cvt_pk_bf16_f32 v117, v110, v111\n"
        "v_mfma_f32_16x16x32_bf16 v[36:39], v[52:55], v[72:75], v[36:39]\n"
        "s_add_u32 s31, s31, 1\n"
        "ds_read_b64_tr_b16 v[90:91], v9 offset:40080\n"
        "v_mfma_f32_16x16x32_bf16 v[32:35], v[160:163], v[72:75], 0\n"
        "s_cmp_ge_u32 s31, s71\n"
        "ds_read2_b32 v[118:119], v11 offset0:2 offset1:5\n"
        "v_mfma_f32_16x16x32_bf16 v[32:35], v[128:131], v[68:71], v[32:35]\n"
        "s_cbranch_scc1 L52B_%=\n"
        "L53B_%=:\n"
        "ds_read_b64_tr_b16 v[114:115], v9 offset:40096\n"
        "v_cvt_pk_bf16_f32 v110, v36, v37\n"
        "v_cvt_pk_bf16_f32 v111, v38, v39\n"
        "v_mfma_f32_16x16x32_bf16 v[32:35], v[124:127], v[20:23], v[32:35]\n"
        "s_add_u32 s16, s16, 256\n"
        "v_mfma_f32_16x16x32_bf16 v[20:23], v[48:51], v[40:43], 0\n"
        "s_waitcnt lgkmcnt(4)\n"
        "s_cmp_ge_u32 s31, 20\n"
        "v_smfmac_f32_16x16x64_bf16 v[20:23], v[56:59], v[76:83], v18\n"
        "s_cselect_b32 s39, s22, s16\n"
        "v_mfma_f32_16x16x32_bf16 v[36:39], v[48:51], v[96:99], 0\n"
        "s_waitcnt lgkmcnt(2)\n"
        "v_cvt_pk_bf16_f32 v108, v32, v33\n"
        "v_cvt_pk_bf16_f32 v109, v34, v35\n"
        "s_cmp_ge_u32 s31, 21\n"
        "v_smfmac_f32_16x16x64_bf16 v[36:39], v[56:59], v[88:95], v18\n"
        "s_cselect_b32 s39, s23, s39\n"
        "ds_read_b64_tr_b16 v[78:79], v9 offset:40112\n"
        "v_mfma_f32_16x16x32_bf16 v[32:35], v[48:51], v[104:107], 0\n"
        "s_waitcnt lgkmcnt(1)\n"
        "s_mul_hi_u32 s38, s39, 58835169\n"
        "v_smfmac_f32_16x16x64_bf16 v[32:35], v[56:59], v[112:119], v18\n"
        "s_mul_i32 s39, s39, 1344\n"
        "ds_read_b64_tr_b16 v[90:91], v9 offset:40128\n"
        "v_cvt_pk_bf16_f32 v152, v20, v36\n"
        "v_cvt_pk_bf16_f32 v156, v21, v37\n"
        "v_cvt_pk_bf16_f32 v160, v22, v38\n"
        "v_cvt_pk_bf16_f32 v164, v23, v39\n"
        "v_mfma_f32_16x16x32_bf16 v[68:71], v[48:51], v[44:47], 0\n"
        "s_mul_i32 s38, s38, 588672\n"
        "s_waitcnt lgkmcnt(1)\n"
        "v_smfmac_f32_16x16x64_bf16 v[68:71], v[56:59], v[76:83], v18 abid:1\n"
        "s_add_u32 s39, s39, s38\n"
        "ds_read_b64_tr_b16 v[114:115], v9 offset:40144\n"
        "v_mfma_f32_16x16x32_bf16 v[60:63], v[48:51], v[100:103], 0\n"
        "s_add_u32 s39, s39, s17\n"
        "ds_read_b128 v[36:39], v2\n"
        "v_mov_b64 v[20:21], 0\n"
        "v_mov_b64 v[22:23], 0\n"
        "s_waitcnt lgkmcnt(2)\n"
        "v_smfmac_f32_16x16x64_bf16 v[60:63], v[56:59], v[88:95], v18 abid:1\n"
        "s_mul_i32 s68, s31, 3\n"
        "ds_read_b128 v[52:55], v3\n"
        "v_cvt_pk_bf16_f32 v153, v32, v68\n"
        "v_cvt_pk_bf16_f32 v157, v33, v69\n"
        "v_cvt_pk_bf16_f32 v161, v34, v70\n"
        "v_cvt_pk_bf16_f32 v165, v35, v71\n"
        "v_mfma_f32_16x16x32_bf16 v[68:71], v[48:51], v[108:111], 0\n"
        "s_bitcmp1_b32 s31, 0\n"
        "v_mov_b64 v[32:33], 0\n"
        "v_mov_b64 v[34:35], 0\n"
        "s_waitcnt lgkmcnt(2)\n"
        "v_smfmac_f32_16x16x64_bf16 v[68:71], v[56:59], v[112:119], v18 abid:1\n"
        "s_cselect_b32 s38, 3, 6\n"
        "s_waitcnt lgkmcnt(1)\n"
        "v_mov_b32_dpp v67, v19 row_shr:4 row_mask:0xf bank_mask:0xf bound_ctrl:0\n"
        "v_smfmac_f32_16x16x64_bf16 v[20:23], v[36:39], v[40:47], v4\n"
        "s_add_u32 s68, s68, s38\n"
        "v_mov_b64 v[48:49], 0\n"
        "v_mov_b64 v[50:51], 0\n"
        "s_waitcnt lgkmcnt(0)\n"
        "v_smfmac_f32_16x16x64_bf16 v[20:23], v[52:55], v[76:83], v4 abid:1\n"
        "s_add_u32 s37, s35, 0\n"
        "s_cmp_lt_u32 s31, s14\n"
        "v_cvt_pk_bf16_f32 v154, v60, v68\n"
        "v_cvt_pk_bf16_f32 v158, v61, v69\n"
        "v_cvt_pk_bf16_f32 v162, v62, v70\n"
        "v_cvt_pk_bf16_f32 v166, v63, v71\n"
        "v_smfmac_f32_16x16x64_bf16 v[32:35], v[36:39], v[96:103], v4\n"
        "s_cselect_b32 s68, s68, 0\n"
        "v_add_f32 v20, v20, v21\n"
        "v_add_f32 v22, v22, v23\n"
        "v_smfmac_f32_16x16x64_bf16 v[32:35], v[52:55], v[88:95], v4 abid:1\n"
        "s_and_b32 s38, s31, 7\n"
        "ds_read_b64_tr_b16 v[78:79], v9 offset:43392\n"
        "v_smfmac_f32_16x16x64_bf16 v[48:51], v[36:39], v[104:111], v4\n"
        "buffer_store_dwordx3 v[152:154], v12, s[8:11], s37 offen sc0 sc1 nt\n"
        "s_cselect_b32 s30, 1, -7\n"
        "v_smfmac_f32_16x16x64_bf16 v[48:51], v[52:55], v[112:119], v4 abid:1\n"
        "s_mul_i32 s29, s30, 192\n"
        "ds_read_b64_tr_b16 v[90:91], v9 offset:43408\n"
        "v_add_f32 v32, v32, v33\n"
        "v_add_f32 v34, v34, v35\n"
        "v_cvt_pk_bf16_f32 v148, v20, v32\n"
        "v_mfma_f32_16x16x32_bf16 v[152:155], v[120:123], v[40:43], 0\n"
        "s_mul_i32 s30, s30, 392\n"
        "s_waitcnt lgkmcnt(1)\n"
        "v_smfmac_f32_16x16x64_bf16 v[152:155], v[64:67], v[76:83], v18\n"
        "s_add_u32 s37, s35, 0\n"
        "s_cmp_eq_u32 s69, s18\n"
        "ds_read_b64_tr_b16 v[114:115], v9 offset:43424\n"
        "v_add_f32 v48, v48, v49\n"
        "v_add_f32 v50, v50, v51\n"
        "v_cvt_pk_bf16_f32 v149, v48, v22\n"
        "v_cvt_pk_bf16_f32 v150, v34, v50\n"
        "v_mfma_f32_16x16x32_bf16 v[56:59], v[120:123], v[96:99], 0\n"
        "s_cselect_b32 s34, -7488, s70\n"
        "s_waitcnt lgkmcnt(1)\n"
        "v_smfmac_f32_16x16x64_bf16 v[56:59], v[64:67], v[88:95], v18\n"
        "s_add_u32 s69, s69, s34\n"
        "ds_read_b64_tr_b16 v[78:79], v9 offset:43440\n"
        "v_writelane_b32 v10, s69, 56\n"
        "v_add_u32 v11, s30, v11\n"
        "s_mov_b64 exec, s[62:63]\n"
        "v_add_u32 v16, s29, v16\n"
        "v_add_u32 v8, s34, v8\n"
        "s_mov_b64 exec, -1\n"
        "v_mfma_f32_16x16x32_bf16 v[72:75], v[120:123], v[104:107], 0\n"
        "buffer_store_dwordx3 v[156:158], v15, s[8:11], s37 offen offset:768 sc0 sc1 nt\n"
        "s_mov_b64 exec, s[50:51]\n"
        "v_add_u32 v5, s29, v5\n"
        "v_add_u32 v6, s34, v6\n"
        "s_mov_b64 exec, -1\n"
        "s_waitcnt lgkmcnt(1)\n"
        "v_smfmac_f32_16x16x64_bf16 v[72:75], v[64:67], v[112:119], v18\n"
        "ds_read_b64_tr_b16 v[90:91], v9 offset:43456\n"
        "v_mov_b32_dpp v31, v19 row_shr:4 row_mask:0xf bank_mask:0xf bound_ctrl:0\n"
        "v_mfma_f32_16x16x32_bf16 v[20:23], v[120:123], v[44:47], 0\n"
        "s_waitcnt lgkmcnt(1)\n"
        "v_smfmac_f32_16x16x64_bf16 v[20:23], v[64:67], v[76:83], v18 abid:1\n"
        "s_add_u32 s37, s35, 98112\n"
        "ds_read_b64_tr_b16 v[114:115], v9 offset:43472\n"
        "v_mfma_f32_16x16x32_bf16 v[84:87], v[120:123], v[100:103], 0\n"
        "s_waitcnt lgkmcnt(1)\n"
        "v_smfmac_f32_16x16x64_bf16 v[84:87], v[64:67], v[88:95], v18 abid:1\n"
        "ds_read_b64_tr_b16 v[78:79], v9 offset:46720\n"
        "v_mfma_f32_16x16x32_bf16 v[60:63], v[120:123], v[108:111], 0\n"
        "buffer_store_dwordx3 v[160:162], v12, s[8:11], s37 offen offset:192 sc0 sc1 nt\n"
        "ds_read_b32 v167, v0 offset:21600\n"
        "s_waitcnt lgkmcnt(2)\n"
        "v_smfmac_f32_16x16x64_bf16 v[60:63], v[64:67], v[112:119], v18 abid:1\n"
        "ds_read_b64_tr_b16 v[90:91], v9 offset:46736\n"
        "v_mfma_f32_16x16x32_bf16 v[68:71], v[24:27], v[40:43], 0\n"
        "s_waitcnt lgkmcnt(2)\n"
        "v_smfmac_f32_16x16x64_bf16 v[68:71], v[28:31], v[76:83], v18\n"
        "s_add_u32 s37, s35, 98112\n"
        "ds_read_b64_tr_b16 v[114:115], v9 offset:46752\n"
        "v_mfma_f32_16x16x32_bf16 v[36:39], v[24:27], v[96:99], 0\n"
        "s_waitcnt lgkmcnt(1)\n"
        "v_smfmac_f32_16x16x64_bf16 v[36:39], v[28:31], v[88:95], v18\n"
        "ds_read_b64_tr_b16 v[78:79], v9 offset:46768\n"
        "v_readfirstlane_b32 s40, v167\n"
        "v_mfma_f32_16x16x32_bf16 v[40:43], v[24:27], v[104:107], 0\n"
        "buffer_store_dwordx3 v[164:166], v14, s[8:11], s37 offen offset:960 sc0 sc1 nt\n"
        "s_add_u32 s37, s35, 588672\n"
        "s_add_u32 s46, s37, 1344\n"
        "s_cmp_lt_i32 s40, s68\n"
        "s_waitcnt lgkmcnt(1)\n"
        "v_smfmac_f32_16x16x64_bf16 v[40:43], v[28:31], v[112:119], v18\n"
        "buffer_store_dwordx3 v[148:150], v12, s[44:47], s37 offen offset:1152 sc0 sc1 nt\n"
        "ds_read_b64_tr_b16 v[90:91], v9 offset:46784\n"
        "v_mfma_f32_16x16x32_bf16 v[64:67], v[24:27], v[44:47], 0\n"
        "s_waitcnt lgkmcnt(1)\n"
        "v_smfmac_f32_16x16x64_bf16 v[64:67], v[28:31], v[76:83], v18 abid:1\n"
        "ds_read_b64_tr_b16 v[114:115], v9 offset:46800\n"
        "s_mov_b64 exec, 1\n"
        "ds_inc_u32 v0, v1 offset:21612\n"
        "s_mov_b64 exec, -1\n"
        "s_cbranch_scc1 L56B_%=\n"
        "L57B_%=:\n"
        "ds_read_b128 v[124:127], v7 offset:0\n"
        "ds_read_b128 v[132:135], v7 offset:3328\n"
        "ds_read_b128 v[128:131], v7 offset:64\n"
        "ds_read_b128 v[136:139], v7 offset:3392\n"
        "v_mfma_f32_16x16x32_bf16 v[104:107], v[24:27], v[100:103], 0\n"
        "s_waitcnt lgkmcnt(6)\n"
        "ds_read_b128 v[160:163], v7 offset:128\n"
        "ds_read_b128 v[52:55], v7 offset:3456\n"
        "s_mov_b32 s36, s35\n"
        "v_smfmac_f32_16x16x64_bf16 v[104:107], v[28:31], v[88:95], v18 abid:1\n"
        "s_mov_b32 s35, s39\n"
        "v_mfma_f32_16x16x32_bf16 v[88:91], v[24:27], v[108:111], 0\n"
        "s_waitcnt lgkmcnt(7)\n"
        "v_smfmac_f32_16x16x64_bf16 v[88:91], v[28:31], v[112:119], v18 abid:1\n"
        "s_setprio 0\n"
        "s_waitcnt lgkmcnt(0)\n"
        "s_nop 15\n"
        "s_branch CNEXTB_%=\n"
        :: "{v0}"(tid), "{s[0:1]}"(kap), "{s2}"(bid), "{s[4:5]}"(tab) : CLOBBERS);
}

extern "C" void kernel_launch(void* const* d_in, const int* in_sizes, int n_in, void* d_out, int out_size, void* d_ws, size_t ws_size, hipStream_t stream) {
    (void)in_sizes; (void)n_in; (void)out_size; (void)ws_size;
    (void)hipFuncSetAttribute((const void*)swin_kernel, hipFuncAttributeMaxDynamicSharedMemorySize, LDS_BYTES);
    swin_kernel<<<256, 512, LDS_BYTES, stream>>>((const char*)d_in[0], (const char*)d_in[1], (const char*)d_in[2], (const char*)d_in[3], (const char*)d_in[4], (char*)d_out, (const char*)d_ws);
}
```

```cpp
#define LTOK_ 208
#define FPAD 32
#define NPR 36
#define NWC 22
#define ASP 0
#define LK0 0
#define LK1 0
#define LK2 1
#define LK3 1
#include <hip/hip_runtime.h>
#include <hip/hip_bf16.h>

constexpr int TOKB = 192, ROWB = 511 * TOKB;
constexpr unsigned FRAMEB = 511u * ROWB;
constexpr int LTOK = LTOK_;
constexpr int LFRAME = 48 * LTOK + FPAD;
constexpr int LBUF = 4 * LFRAME;
constexpr int LSTASH = 10 * 4 * LTOK;
constexpr int LSTG = 6208;
constexpr int VST = 392, LEXT = 1536 + 8 * VST;
constexpr int WT = 3088;
constexpr int LDS_AUX = (54 - NPR) / 3 * WT, LDS_TAB = LDS_AUX + 1024, LDS_CNT = LDS_TAB + 2048, LDS_BUF = LDS_CNT + 128, LDS_STASH = LDS_BUF + 2 * LBUF,
              LDS_STG = LDS_STASH + LSTASH, LDS_SCRAP = LDS_STG + 4 * LSTG, LDS_EXT = LDS_SCRAP + 512, LDS_BYTES = 163840;
static_assert(NWC >= 18 && NPR % 3 == 0 && LDS_EXT + 4 * LEXT + 256 <= LDS_BYTES && NPR * 1024 <= LBUF, "LDS");
constexpr unsigned RAWM = 18432, RAW_T = LDS_BUF, RAW_TV = RAW_T + 2 * RAWM + 128, RAW_SV = (36 - NPR) / 3 * WT, RAW_SO = LDS_EXT, RAW_SQ = LDS_BUF + LBUF, RAW_SK = RAW_SQ + RAWM;
static_assert(RAW_SV + RAWM <= LDS_AUX && RAW_SO + RAWM <= LDS_BYTES && RAW_SK + RAWM <= LDS_STASH && RAW_TV + 2 * RAWM <= LDS_STASH && RAWM + 6 * WT <= 2 * RAWM + 128, "fold LDS");

constexpr int phi(int v) { return v ^ (v >> 1); }
struct Tab {
    unsigned img[3][64][4];
    unsigned p[4][7][64][4];
    unsigned c[4][6][64][4];
    unsigned d[4][7][64][4];
    static constexpr void job(unsigned (*q)[64][4], int lane, unsigned L, unsigned R, int mode, int tau, unsigned st0, unsigned st1) {
        const int i = lane & 15, g = lane >> 4;
        for (int a = 0; a < 2; ++a) {
            const unsigned ra = (8 * (i >> 2) + 4 * a + (i & 3)) * 192 + ((g ^ ((2 * (i >> 2) + a) & 3)) * 16);
            q[0][lane][a] = L + ra;
            q[0][lane][2 + a] = mode == 0 ? R + tau * 6144 + ra : mode == 2 ? R + tau * 6144 + (16 * a + i) * 192 + ((g ^ (i >> 2)) * 16)
                                          : R + tau * 64 + (8 * g + 4 * a + (i >> 2)) * 192 + (((i & 3) ^ ((2 * g + a) & 3)) * 16);
        }
        q[1][lane][0] = st0; q[1][lane][1] = st1; q[1][lane][2] = LDS_SCRAP + g * 32;
    }
    static constexpr unsigned scat(bool win, int lane, int tau, int tb) {
        const int i = lane & 15, c = 32 * tau + 8 * (i >> 2) + 4 * tb + (i & 3), r = c / 24, o = c % 24, q = win ? o / 6 : o & 3, t = win ? o % 6 : o >> 2;
        return (win ? RAW_SV : 3 * t < NPR - 18 ? LDS_BUF + 18 * 1024 : -(NPR - 18) / 3 * WT) + t * WT + ((lane & 48) + 4 * q + r) * 16;
    }
    constexpr Tab() : img{}, p{}, c{}, d{} {
        for (int e = 0; e < 48; ++e) img[0][e >> 2][e & 3] = 0x3f803f80u;
        img[0][12][0] = 0x7f800000u;
        for (int e = 0; e < 8; ++e) img[0][36 + 10 + e][e >> 1] = e & 1 ? 0x3f800000u : 0x3f80u;
        for (int k = 0; k < 4; ++k) for (int lane = 0; lane < 64; ++lane) {
            const int i = lane & 15, g = lane >> 4, rowv = 8 * (i >> 2) + (i & 3);
            unsigned vi[20] = {};
            {
                const int pp = i >> 2, t = i & 3;
                const int t1 = (k + 3 - LK1) & 3;
                if (t1 < 3) job(p[k], lane, RAW_TV, RAW_TV + RAWM, 1, t1, scat(false, lane, t1, 0), scat(false, lane, t1, 1));
                const unsigned wl = lane * 16, lm = pp == g ? ~0u : 0u, idp = LDS_AUX + 576 + (t == g ? 10 + pp : 0) * 16, sidx = i & 1 ? 0xeeeeddddu : 0x4444ddddu;
                unsigned vA[3] = {};
                for (int m = 0; m < 3; ++m) {
                    const int n = 64 * m + lane, tf = n / 96, q = n % 96 / 12, s = (n % 12) ^ tf;
                    const unsigned cc = tf * FRAMEB + (3 * (s & 3) + (s >> 2)) * 16 + 2048 - 1024 * m;
                    vA[m] = cc + q * TOKB;
                    img[1][lane][m] = (cc + 6 * ROWB + 6 * TOKB) | ((unsigned)(2 * (q & 3) + (q >> 2)) << 28);
                }
                const unsigned vA0 = vA[0], vA1 = vA[1], vA2 = vA[2], vB0 = vA0 + (ROWB - 7 * TOKB), vB1 = vA1 + (ROWB - 7 * TOKB), vB2 = vA2 + (ROWB - 7 * TOKB);
                const unsigned dA = LDS_BUF + t * LFRAME + pp * LTOK + g * 16, rA = LDS_BUF + t * LFRAME + (pp == g ? pp : 4 + g) * LTOK + 192;
                img[1][lane][3] = (2 * pp * (4 * LTOK) + t * LTOK + g * 16) | (((pp == g ? 2 * pp : 2 * g + 1) * (4 * LTOK) + t * LTOK + 192) << 16);
                const unsigned cd0 = k < 3 ? dA + k * 8 * LTOK : LDS_STASH + (img[1][lane][3] & 0xffffu), cr0 = k < 3 ? rA + k * 8 * LTOK : LDS_STASH + (img[1][lane][3] >> 16);
                const unsigned xr = LDS_STG + k * LSTG + (t >> 1) * 3104 + ((t & 1) * 8 + pp) * TOKB + (g ^ (t & 1)) * 16;
                const unsigned axp = LDS_AUX + (t > ASP ? 384 : 0);
                const unsigned pf = g * FRAMEB + (i >> 1) * TOKB + (i & 1) * 188 + 2048;
                vi[0] = xr; vi[1] = lm; vi[2] = rA; vi[3] = dA; vi[4] = vA0; vi[5] = vA1; vi[6] = vA2; vi[7] = vB0; vi[8] = vB1; vi[9] = vB2; vi[10] = sidx; vi[11] = cd0; vi[12] = cr0; vi[13] = wl; vi[14] = idp; vi[15] = pf; vi[16] = axp;
                for (int e = 0; e < 20; ++e) p[k][2 + (e >> 2)][lane][e & 3] = vi[e];
            }
            {
                const int t0 = (k + 3 - LK0) & 3, t2 = (k + 3 - LK2) & 3, t3 = (k + 3 - LK3) & 3;
                const unsigned s0 = LDS_BUF + t0 * 6144 + lane * 16, s3 = LDS_BUF + LBUF + t3 * 6144 + lane * 16;
                if (t0 < 3) job(c[k], lane, RAW_T, RAW_T + RAWM, 2, t0, s0, s0 + 3072); else job(c[k], lane, RAW_T, RAW_T + RAWM, 1, 0, LDS_SCRAP + rowv * 4, 0);
                if (t2 < 3) job(c[k] + 4, lane, RAW_SV, RAW_SO, 1, t2, scat(true, lane, t2, 0), scat(true, lane, t2, 1));
                if (t3 < 3) job(d[k], lane, RAW_SQ, RAW_SK, 0, t3, s3, s3 + 3072); else job(d[k], lane, RAW_SQ, RAW_SK, 1, 0, LDS_SCRAP + rowv * 4, 0);
                for (int m = 0; m < 3; ++m) {
                    const int n = 64 * m + lane, row = n / 12, pc = ((n % 12) ^ ((row >> 2) & 3)) * 16;
                    c[k][3][lane][m] = (24 * (row & 3) + 12 * ((row >> 2) & 1) + 2 * (row >> 3)) * 576 + pc;
                    c[k][2][lane][m] = row * 192 + pc;
                }
                const int dg = 16 * (2 * (g & 1) + (g >> 1)), pix = 4 * (i & 3) + phi(i >> 2);
                const unsigned wl = lane * 16, fr = LDS_BUF + k * LFRAME, ext = LDS_EXT + k * LEXT;
                const unsigned ta = fr + pix * LTOK + dg, zr = LDS_AUX + 384 + g * 16, t30 = i & 3 ? zr : LDS_STASH + k * LTOK + dg;
                const unsigned xa = fr + (4 * (i >> 2) + phi(g)) * LTOK + 96 * (i & 1) + 8 * ((i >> 1) & 1);
                const int sg = 8 * ((i >> 1) & 1) + 4 * (i >> 3) + 2 * ((i >> 2) & 1) + (i & 1);
                const unsigned tv = fr + (32 + 4 * (sg & 3) + phi(sg >> 2)) * LTOK + dg, c1f = (i >> 2) == 2 * g ? 0x3f800000u : 0u;
                const unsigned sidx = g < 2 ? 0x0e0e0404u | ((i >> 2) == 2 * g ? (i & 3) * 0x00100010u : (i >> 2) == 2 * g + 1 ? (i & 3) * 0x40004000u : 0u) : 0xeeee4444u;
                const unsigned ga0 = phi(g) * TOKB + 48 * (i & 3) + 12 * (i >> 2), wr = ROWB - 7 * TOKB;
                const unsigned ga1 = ga0 + (phi(g) >= 1 ? wr : 0), ga2 = ga0 + (phi(g) >= 2 ? wr : 0), ga3 = ga0 + (phi(g) >= 3 ? wr : 0);
                const unsigned pb = LDS_SCRAP + k * 128, pr0 = i < 4 && (i >> 1) == (g >> 1) ? pb + (i & 1) * 32 + (g & 1) * 16 : zr, pr1 = i == 0 || i == 2 ? pb + 64 + g * 16 : zr;
                const unsigned pw = g < 2 ? pb + ((i >> 1) & 1) * 32 + (i >> 3) * 16 + ((i >> 2) & 1) * 8 + g * 4 + (i & 1) * 2
                                          : pb + 64 + (i >> 2) * 16 + (g & 1) * 4 + ((i >> 1) & 1) * 8 + (i & 1) * 2 + (lane == 57 ? 6 : 0);
                const unsigned six2 = (i & 1 ? 0xeeeeu : 0x4444u) | ((i & 2 ? 0xeeee0000u : 0x44440000u) & (g < 2 ? 0x0f0f0000u : 0xffff0000u));
                const unsigned exs0 = i & 2 ? LDS_AUX + g * 16 : ext + (i & 1) * 64 + g * 16, t3s0 = i & 2 ? zr : LDS_STASH + k * LTOK + 64 * (g >> 1) + 16 * (g & 1) + 4 * (i & 1);
                const unsigned ex0 = i & 1 ? zr : i & 2 ? LDS_AUX + g * 16 : ext + g * 16, exv0 = ext + 1536 + i * 24;
                const unsigned rla0 = lane < 48 ? fr + (16 * g + pix) * LTOK + 192 : lane == 56 ? LDS_STASH + k * LTOK + 192 : LDS_AUX + 192;
                img[2][lane][0] = ((i & 7) * (4 * LTOK) + dg) | ((((4 * g + (i & 3)) & 7) * (4 * LTOK) + 96 * ((i >> 2) & 1) + 8 * (i >> 3)) << 16);
                img[2][lane][1] = ((i & 7) * 192u + g * 16) | ((1536u + i * 24 + (g & 1) * 4 * VST) << 16);
                img[2][lane][2] = ((fr + phi(g) * LTOK + 192 - xa) & 0xffffu) | ((fr + pix * LTOK + 192 - xa) << 16);
                img[2][lane][3] = (LDS_AUX + 576 + (10 + (i & 3)) * 16) | ((1536u + (i & 7) * VST + 384) << 16);
                vi[0] = wl; vi[1] = pw; vi[2] = pr0; vi[3] = pr1; vi[4] = six2; vi[5] = exs0; vi[6] = t3s0; vi[7] = ta; vi[8] = t30; vi[9] = xa; vi[10] = rla0; vi[11] = exv0; vi[12] = ga0; vi[13] = ga1; vi[14] = ga2; vi[15] = ga3; vi[16] = ex0; vi[17] = tv; vi[18] = sidx; vi[19] = c1f;
                for (int e = 0; e < 20; ++e) d[k][2 + (e >> 2)][lane][e & 3] = vi[e];
            }
        }
    }
};
static __device__ const Tab TAB __attribute__((aligned(128))) = Tab();

#define CLOBBERS "v1", "v2", "v3", "v4", "v5", "v6", "v7", "v8", "v9", "v10", "v11", "v12", "v13", "v14", "v15", "v16", "v17", "v18", "v19", "v20", "v21", "v22", "v23", "v24", "v25", "v26", "v27", "v28", "v29", "v30", "v31", "v32", "v33", "v34", "v35", "v36", "v37", "v38", "v39", "v40", "v41", "v42", "v43", "v44", "v45", "v46", "v47", "v48", "v49", "v50", "v51", "v52", "v53", "v54", "v55", "v56", "v57", "v58", "v59", "v60", "v61", "v62", "v63", "v64", "v65", "v66", "v67", "v68", "v69", "v70", "v71", "v72", "v73", "v74", "v75", "v76", "v77", "v78", "v79", "v80", "v81", "v82", "v83", "v84", "v85", "v86", "v87", "v88", "v89", "v90", "v91", "v92", "v93", "v94", "v95", "v96", "v97", "v98", "v99", "v100", "v101", "v102", "v103", "v104", "v105", "v106", "v107", "v108", "v109", "v110", "v111", "v112", "v113", "v114", "v115", "v116", "v117", "v118", "v119", "v120", "v121", "v122", "v123", "v124", "v125", "v126", "v127", "v128", "v129", "v130", "v131", "v132", "v133", "v134", "v135", "v136", "v137", "v138", "v139", "v140", "v141", "v142", "v143", "v144", "v145", "v146", "v147", "v148", "v149", "v150", "v151", "v152", "v153", "v154", "v155", "v156", "v157", "v158", "v159", "v160", "v161", "v162", "v163", "v164", "v165", "v166", "v167", "v168", "v169", "v170", "v171", "v172", "v173", "v174", "v175", "v176", "v177", "v178", "v179", "v180", "v181", "v182", "v183", "v184", "v185", "v186", "v187", "v188", "v189", "v190", "v191", "v192", "v193", "v194", "v195", "v196", "v197", "v198", "v199", "v200", "v201", "v202", "v203", "v204", "v205", "v206", "v207", "v208", "v209", "v210", "v211", "v212", "v213", "v214", "v215", "v216", "v217", "v218", "v219", "v220", "v221", "v222", "v223", "v224", "v225", "v226", "v227", "v228", "v229", "v230", "v231", "v232", "v233", "v234", "v235", "v236", "v237", "v238", "v239", "v240", "v241", "v242", "v243", "v244", "v245", "v246", "v247", "v248", "v249", "v250", "v251", "v252", "v253", "v254", "v255", "s3", "s6", "s7", "s8", "s9", "s10", "s11", "s12", "s13", "s14", "s15", "s16", "s17", "s18", "s19", "s20", "s21", "s22", "s23", "s24", "s25", "s26", "s27", "s28", "s29", "s30", "s31", "s34", "s35", "s36", "s37", "s38", "s39", "s40", "s41", "s42", "s43", "s44", "s45", "s46", "s47", "s48", "s49", "s50", "s51", "s52", "s53", "s54", "s55", "s56", "s57", "s58", "s59", "s60", "s61", "s62", "s63", "s64", "s65", "s66", "s67", "s68", "s69", "s70", "s71", "s72", "s73", "s74", "s75", "s76", "s77", "vcc", "scc", "memory"
__global__ __launch_bounds__(512, 2) void swin_kernel(const char* x, const char* wqkv_t, const char* wo_t, const char* wqkv_s, const char* wo_s, char* out, const char* ws) {
    const unsigned long long kap = (unsigned long long)__builtin_amdgcn_kernarg_segment_ptr(), tab = (unsigned long long)&TAB;
    const unsigned tid = threadIdx.x, bid = blockIdx.x;
    asm volatile(
        "s_load_dwordx8 s[44:51], s[0:1], 0x0\n"
        "s_load_dwordx4 s[52:55], s[0:1], 0x20\n"
        "s_load_dwordx2 s[56:57], s[0:1], 0x30\n"
        "v_readfirstlane_b32 s3, v0\n"
        "v_and_b32 v56, 63, v0\n"
        "v_lshlrev_b32 v57, 4, v56\n"
        "s_bfe_u32 s3, s3, 0x30006\n"
        "s_and_b32 s6, s3, 3\n"
        "s_mul_i32 s64, s3, 9216\n"
        "s_add_u32 s64, s64, 21728\n"
        "s_mov_b32 s27, 0x20000\n"
        "s_cmp_lt_u32 s3, 4\n"
        "s_cbranch_scc0 PPRO_%=\n"
        "s_mul_i32 s7, s6, 6144\n"
        "s_add_u32 s7, s7, 33792\n"
        "s_add_u32 s22, s4, s7\n"
        "s_addc_u32 s23, s5, 0\n"
        "global_load_dwordx4 v[240:243], v57, s[22:23] offset:-2048\n"
        "global_load_dwordx4 v[244:247], v57, s[22:23] offset:-1024\n"
        "global_load_dwordx4 v[36:39], v57, s[22:23] offset:1024\n"
        "global_load_dwordx4 v[48:51], v57, s[22:23] offset:2048\n"
        "global_load_dwordx4 v[52:55], v57, s[22:23] offset:3072\n"
        "s_mov_b32 s65, 13824\n"
        "v_add_u32 v24, 0, v56\n"
        "v_mul_u32_u24 v21, 171, v24\n"
        "v_lshrrev_b32 v21, 11, v21\n"
        "v_mad_i32_i24 v24, v21, -12, v24\n"
        "v_bfe_u32 v58, v21, 2, 2\n"
        "v_xor_b32 v58, v24, v58\n"
        "v_and_b32 v27, 7, v21\n"
        "v_mul_u32_u24 v27, 576, v27\n"
        "v_lshl_add_u32 v58, v58, 4, v27\n"
        "v_add_u32 v25, 64, v56\n"
        "v_mul_u32_u24 v22, 171, v25\n"
        "v_lshrrev_b32 v22, 11, v22\n"
        "v_mad_i32_i24 v25, v22, -12, v25\n"
        "v_bfe_u32 v59, v22, 2, 2\n"
        "v_xor_b32 v59, v25, v59\n"
        "v_and_b32 v27, 7, v22\n"
        "v_mul_u32_u24 v27, 576, v27\n"
        "v_lshl_add_u32 v59, v59, 4, v27\n"
        "v_lshrrev_b32 v27, 3, v22\n"
        "v_mad_u32_u24 v59, v27, s65, v59\n"
        "v_add_u32 v26, 128, v56\n"
        "v_mul_u32_u24 v23, 171, v26\n"
        "v_lshrrev_b32 v23, 11, v23\n"
        "v_mad_i32_i24 v26, v23, -12, v26\n"
        "v_bfe_u32 v60, v23, 2, 2\n"
        "v_xor_b32 v60, v26, v60\n"
        "v_and_b32 v27, 7, v23\n"
        "v_mul_u32_u24 v27, 576, v27\n"
        "v_lshl_add_u32 v60, v60, 4, v27\n"
        "v_add_u32 v60, s65, v60\n"
        "s_lshr_b32 s61, s6, 1\n"
        "s_mul_i32 s61, s61, 192\n"
        "s_mov_b32 s62, 27648\n"
        "s_mov_b32 s63, 4608\n"
        "s_add_u32 s7, s62, s63\n"
        "s_lshl_b32 s29, s63, 1\n"
        "s_add_u32 s60, s7, s63\n"
        "s_bitcmp1_b32 s6, 0\n"
        "s_cselect_b32 s28, s7, 0\n"
        "s_cselect_b32 s29, s29, s62\n"
        "s_cselect_b32 s60, s60, s63\n"
        "s_add_u32 s28, s28, s61\n"
        "s_add_u32 s29, s29, s61\n"
        "s_add_u32 s60, s60, s61\n"
        "s_mov_b32 s26, 55296\n"
        "s_waitcnt lgkmcnt(0)\n"
        "s_mov_b32 s24, s46\n"
        "s_and_b32 s25, s47, 0xffff\n"
        "s_add_u32 m0, s64, 0\n"
        "s_nop 0\n"
        "buffer_load_dwordx4 v58, s[24:27], s28 offen lds\n"
        "s_add_u32 m0, s64, 1024\n"
        "s_nop 0\n"
        "buffer_load_dwordx4 v59, s[24:27], s28 offen lds\n"
        "s_add_u32 m0, s64, 2048\n"
        "s_nop 0\n"
        "buffer_load_dwordx4 v60, s[24:27], s28 offen lds\n"
        "s_add_u32 m0, s64, 3072\n"
        "s_nop 0\n"
        "buffer_load_dwordx4 v58, s[24:27], s29 offen lds\n"
        "s_add_u32 m0, s64, 4096\n"
        "s_nop 0\n"
        "buffer_load_dwordx4 v59, s[24:27], s29 offen lds\n"
        "s_add_u32 m0, s64, 5120\n"
        "s_nop 0\n"
        "buffer_load_dwordx4 v60, s[24:27], s29 offen lds\n"
        "s_add_u32 m0, s64, 6144\n"
        "s_nop 0\n"
        "buffer_load_dwordx4 v58, s[24:27], s60 offen lds\n"
        "s_add_u32 m0, s64, 7168\n"
        "s_nop 0\n"
        "buffer_load_dwordx4 v59, s[24:27], s60 offen lds\n"
        "s_add_u32 m0, s64, 8192\n"
        "s_nop 0\n"
        "buffer_load_dwordx4 v60, s[24:27], s60 offen lds\n"
        "s_barrier\n"
        "s_setprio 1\n"
        "v_mov_b32 v250, 21600\n"
        "s_mov_b32 s41, 0x400000\n"
        "s_and_b32 s7, s2, 7\n"
        "s_lshl_b32 s7, s7, 5\n"
        "s_lshr_b32 s14, s2, 3\n"
        "s_add_u32 s7, s7, s14\n"
        "s_and_b32 s16, s7, 255\n"
        "s_sub_u32 s7, s7, s16\n"
        "s_mul_i32 s7, s7, 20\n"
        "s_add_u32 s7, s7, s16\n"
        "s_mov_b32 s14, 22\n"
        "s_mul_i32 s15, s7, 57457\n"
        "s_lshr_b32 s15, s15, 22\n"
        "s_mov_b32 s16, s7\n"
        "s_mul_i32 s15, s15, 588672\n"
        "s_mul_i32 s7, s16, 1344\n"
        "s_add_u32 s15, s15, s7\n"
        "s_mov_b32 s8, s54\n"
        "s_and_b32 s9, s55, 0xffff\n"
        "s_mov_b32 s10, 200540928\n"
        "s_mov_b32 s11, 0x20000\n"
        "s_mov_b64 s[12:13], s[54:55]\n"
        "s_mul_i32 s17, s6, 50135232\n"
        "s_mul_i32 s18, s6, 208\n"
        "s_add_u32 s18, s18, 109536\n"
        "s_mul_i32 s19, s6, 4672\n"
        "s_add_u32 s19, s19, 135520\n"
        "s_mul_i32 s7, s6, 7168\n"
        "s_add_u32 s7, s7, 59392\n"
        "s_add_u32 s22, s4, s7\n"
        "s_addc_u32 s23, s5, 0\n"
        "s_waitcnt vmcnt(0)\n"
        "s_mov_b64 exec, 1\n"
        "ds_inc_u32 v250, v250 offset:16\n"
        "s_mov_b64 exec, -1\n"
        "L67_%=:\n"
        "ds_read_b32 v251, v250 offset:16\n"
        "s_waitcnt lgkmcnt(0)\n"
        "v_readfirstlane_b32 s40, v251\n"
        "s_nop 3\n"
        "s_cmp_ge_i32 s40, 4\n"
        "s_cbranch_scc1 L68_%=\n"
        "s_nop 0\n"
        "s_sub_u32 s41, s41, 1\n"
        "s_cbranch_scc0 L67_%=\n"
        "s_endpgm\n"
        "L68_%=:\n"
        "s_cmp_eq_u32 s6, 0\n"
        "s_cbranch_scc1 L69_%=\n"
        "s_mov_b32 s34, 0x4162ccfd\n"
        "s_mov_b32 s35, 21624\n"
        "s_mov_b32 s36, 21624\n"
        "s_mov_b32 s37, 21628\n"
        "s_call_b64 s[30:31], FOLDM_%=\n"
        "s_branch L70_%=\n"
        "L69_%=:\n"
        "s_mov_b32 s34, 0xbe173353\n"
        "s_mov_b32 s35, 21656\n"
        "s_mov_b32 s36, 21656\n"
        "s_mov_b32 s37, 21624\n"
        "s_call_b64 s[30:31], FOLDR_%=\n"
        "s_cmp_lg_u32 s2, 0\n"
        "s_cbranch_scc1 L66_%=\n"
        "s_mov_b64 s[24:25], s[56:57]\n"
        "s_mov_b64 exec, 1\n"
        "v_mov_b32 v27, 0\n"
        "v_mov_b32 v28, 0\n"
        "global_atomic_swap v252, v27, v28, s[24:25] offset:0 sc0\n"
        "s_mov_b64 exec, -1\n"
        "s_waitcnt vmcnt(0)\n"
        "s_mov_b64 exec, 1\n"
        "v_mov_b32 v27, 0\n"
        "v_mov_b32 v28, 1\n"
        "global_atomic_add v252, v27, v28, s[24:25] offset:256 sc0\n"
        "s_mov_b64 exec, -1\n"
        "L66_%=:\n"
        "L70_%=:\n"
        "s_setprio 2\n"
        "s_waitcnt vmcnt(0)\n"
        "v_mov_b64 v[240:241], v[48:49]\n"
        "v_mov_b64 v[242:243], v[50:51]\n"
        "v_mov_b64 v[244:245], v[52:53]\n"
        "v_mov_b64 v[246:247], v[54:55]\n"
        "L71_%=:\n"
        "ds_read_b32 v251, v250 offset:32\n"
        "s_waitcnt lgkmcnt(0)\n"
        "v_readfirstlane_b32 s40, v251\n"
        "s_nop 3\n"
        "s_cmp_ge_i32 s40, 4\n"
        "s_cbranch_scc1 L72_%=\n"
        "s_nop 0\n"
        "s_sub_u32 s41, s41, 1\n"
        "s_cbranch_scc0 L71_%=\n"
        "s_endpgm\n"
        "L72_%=:\n"
        "s_cmp_eq_u32 s6, 1\n"
        "s_cbranch_scc1 L73_%=\n"
        "s_mov_b32 s34, 0x411da57f\n"
        "s_mov_b32 s35, 21640\n"
        "s_mov_b32 s36, 21640\n"
        "s_mov_b32 s37, 21652\n"
        "s_call_b64 s[30:31], FOLDW_%=\n"
        "s_branch L74_%=\n"
        "L73_%=:\n"
        "s_mov_b64 exec, 1\n"
        "ds_inc_u32 v250, v250 offset:40\n"
        "s_mov_b64 exec, -1\n"
        "s_mov_b32 s24, s50\n"
        "s_and_b32 s25, s51, 0xffff\n"
        "s_mov_b32 s26, 55296\n"
        "s_mov_b32 m0, 61792\n"
        "s_mov_b32 s28, 0\n"
        "buffer_load_dwordx4 v36, s[24:27], s28 offen lds\n"
        "s_mov_b32 m0, 62816\n"
        "s_nop 0\n"
        "buffer_load_dwordx4 v37, s[24:27], s28 offen lds\n"
        "s_mov_b32 m0, 63840\n"
        "s_nop 0\n"
        "buffer_load_dwordx4 v38, s[24:27], s28 offen lds\n"
        "s_mov_b32 m0, 64864\n"
        "s_mov_b32 s28, 576\n"
        "buffer_load_dwordx4 v36, s[24:27], s28 offen lds\n"
        "s_mov_b32 m0, 65888\n"
        "s_nop 0\n"
        "buffer_load_dwordx4 v37, s[24:27], s28 offen lds\n"
        "s_mov_b32 m0, 66912\n"
        "s_nop 0\n"
        "buffer_load_dwordx4 v38, s[24:27], s28 offen lds\n"
        "s_mov_b32 m0, 67936\n"
        "s_mov_b32 s28, 2304\n"
        "buffer_load_dwordx4 v36, s[24:27], s28 offen lds\n"
        "s_mov_b32 m0, 68960\n"
        "s_nop 0\n"
        "buffer_load_dwordx4 v37, s[24:27], s28 offen lds\n"
        "s_mov_b32 m0, 69984\n"
        "s_nop 0\n"
        "buffer_load_dwordx4 v38, s[24:27], s28 offen lds\n"
        "s_mov_b32 m0, 71008\n"
        "s_mov_b32 s28, 2880\n"
        "buffer_load_dwordx4 v36, s[24:27], s28 offen lds\n"
        "s_mov_b32 m0, 72032\n"
        "s_nop 0\n"
        "buffer_load_dwordx4 v37, s[24:27], s28 offen lds\n"
        "s_mov_b32 m0, 73056\n"
        "s_nop 0\n"
        "buffer_load_dwordx4 v38, s[24:27], s28 offen lds\n"
        "s_mov_b32 m0, 74080\n"
        "s_mov_b32 s28, 4608\n"
        "buffer_load_dwordx4 v36, s[24:27], s28 offen lds\n"
        "s_mov_b32 m0, 75104\n"
        "s_nop 0\n"
        "buffer_load_dwordx4 v37, s[24:27], s28 offen lds\n"
        "s_mov_b32 m0, 76128\n"
        "s_nop 0\n"
        "buffer_load_dwordx4 v38, s[24:27], s28 offen lds\n"
        "s_mov_b32 m0, 77152\n"
        "s_mov_b32 s28, 5184\n"
        "buffer_load_dwordx4 v36, s[24:27], s28 offen lds\n"
        "s_mov_b32 m0, 78176\n"
        "s_nop 0\n"
        "buffer_load_dwordx4 v37, s[24:27], s28 offen lds\n"
        "s_mov_b32 m0, 79200\n"
        "s_nop 0\n"
        "buffer_load_dwordx4 v38, s[24:27], s28 offen lds\n"
        "s_mov_b32 s24, s50\n"
        "s_and_b32 s25, s51, 0xffff\n"
        "s_mov_b32 s26, 55296\n"
        "s_mov_b32 m0, 80224\n"
        "s_mov_b32 s28, 192\n"
        "buffer_load_dwordx4 v36, s[24:27], s28 offen lds\n"
        "s_mov_b32 m0, 81248\n"
        "s_nop 0\n"
        "buffer_load_dwordx4 v37, s[24:27], s28 offen lds\n"
        "s_mov_b32 m0, 82272\n"
        "s_nop 0\n"
        "buffer_load_dwordx4 v38, s[24:27], s28 offen lds\n"
        "s_mov_b32 m0, 83296\n"
        "s_mov_b32 s28, 768\n"
        "buffer_load_dwordx4 v36, s[24:27], s28 offen lds\n"
        "s_mov_b32 m0, 84320\n"
        "s_nop 0\n"
        "buffer_load_dwordx4 v37, s[24:27], s28 offen lds\n"
        "s_mov_b32 m0, 85344\n"
        "s_nop 0\n"
        "buffer_load_dwordx4 v38, s[24:27], s28 offen lds\n"
        "s_mov_b32 m0, 86368\n"
        "s_mov_b32 s28, 2496\n"
        "buffer_load_dwordx4 v36, s[24:27], s28 offen lds\n"
        "s_mov_b32 m0, 87392\n"
        "s_nop 0\n"
        "buffer_load_dwordx4 v37, s[24:27], s28 offen lds\n"
        "s_mov_b32 m0, 88416\n"
        "s_nop 0\n"
        "buffer_load_dwordx4 v38, s[24:27], s28 offen lds\n"
        "s_mov_b32 m0, 89440\n"
        "s_mov_b32 s28, 3072\n"
        "buffer_load_dwordx4 v36, s[24:27], s28 offen lds\n"
        "s_mov_b32 m0, 90464\n"
        "s_nop 0\n"
        "buffer_load_dwordx4 v37, s[24:27], s28 offen lds\n"
        "s_mov_b32 m0, 91488\n"
        "s_nop 0\n"
        "buffer_load_dwordx4 v38, s[24:27], s28 offen lds\n"
        "s_mov_b32 m0, 92512\n"
        "s_mov_b32 s28, 4800\n"
        "buffer_load_dwordx4 v36, s[24:27], s28 offen lds\n"
        "s_mov_b32 m0, 93536\n"
        "s_nop 0\n"
        "buffer_load_dwordx4 v37, s[24:27], s28 offen lds\n"
        "s_mov_b32 m0, 94560\n"
        "s_nop 0\n"
        "buffer_load_dwordx4 v38, s[24:27], s28 offen lds\n"
        "s_mov_b32 m0, 95584\n"
        "s_mov_b32 s28, 5376\n"
        "buffer_load_dwordx4 v36, s[24:27], s28 offen lds\n"
        "s_mov_b32 m0, 96608\n"
        "s_nop 0\n"
        "buffer_load_dwordx4 v37, s[24:27], s28 offen lds\n"
        "s_mov_b32 m0, 97632\n"
        "s_nop 0\n"
        "buffer_load_dwordx4 v38, s[24:27], s28 offen lds\n"
        "global_load_dwordx4 v[40:43], v57, s[22:23] offset:-3072\n"
        "global_load_dwordx4 v[44:47], v57, s[22:23] offset:-2048\n"
        "global_load_dwordx4 v[0:3], v57, s[22:23] offset:-1024\n"
        "global_load_dwordx4 v[4:7], v57, s[22:23] offset:0\n"
        "global_load_dwordx4 v[8:11], v57, s[22:23] offset:1024\n"
        "global_load_dwordx4 v[12:15], v57, s[22:23] offset:2048\n"
        "global_load_dwordx4 v[16:19], v57, s[22:23] offset:3072\n"
        "L74_%=:\n"
        "s_setprio 2\n"
        "s_waitcnt vmcnt(0)\n"
        "v_mov_b64 v[240:241], v[40:41]\n"
        "v_mov_b64 v[242:243], v[42:43]\n"
        "v_mov_b64 v[244:245], v[44:45]\n"
        "v_mov_b64 v[246:247], v[46:47]\n"
        "s_cmp_eq_u32 s6, 1\n"
        "s_cbranch_scc1 L79_%=\n"
        "s_mov_b32 s34, 0x4162ccfd\n"
        "s_mov_b32 s35, 21648\n"
        "s_mov_b32 s36, 21648\n"
        "s_mov_b32 s37, 21652\n"
        "L75_%=:\n"
        "ds_read_b32 v251, v250 offset:44\n"
        "s_waitcnt lgkmcnt(0)\n"
        "v_readfirstlane_b32 s40, v251\n"
        "s_nop 3\n"
        "s_cmp_ge_i32 s40, 1\n"
        "s_cbranch_scc1 L76_%=\n"
        "s_nop 0\n"
        "s_sub_u32 s41, s41, 1\n"
        "s_cbranch_scc0 L75_%=\n"
        "s_endpgm\n"
        "L76_%=:\n"
        "s_call_b64 s[30:31], FOLDM_%=\n"
        "s_branch L80_%=\n"
        "L79_%=:\n"
        "s_mov_b64 exec, 1\n"
        "ds_inc_u32 v250, v250 offset:44\n"
        "s_mov_b64 exec, -1\n"
        "L77_%=:\n"
        "ds_read_b32 v251, v250 offset:40\n"
        "s_waitcnt lgkmcnt(0)\n"
        "v_readfirstlane_b32 s40, v251\n"
        "s_nop 3\n"
        "s_cmp_ge_i32 s40, 4\n"
        "s_cbranch_scc1 L78_%=\n"
        "s_nop 0\n"
        "s_sub_u32 s41, s41, 1\n"
        "s_cbranch_scc0 L77_%=\n"
        "s_endpgm\n"
        "L78_%=:\n"
        "s_mov_b32 s34, 0xbe173353\n"
        "s_mov_b32 s35, 21656\n"
        "s_mov_b32 s36, 21656\n"
        "s_mov_b32 s37, 21648\n"
        "s_call_b64 s[30:31], FOLDR_%=\n"
        "L80_%=:\n"
        "s_branch CONS_%=\n"
        "FOLDM_%=:\n"
        "ds_read_b128 v[88:91], v242\n"
        "ds_read_b128 v[92:95], v240\n"
        "ds_read_b128 v[180:183], v242 offset:64\n"
        "ds_read_b128 v[172:175], v240 offset:64\n"
        "ds_read_b128 v[184:187], v242 offset:128\n"
        "ds_read_b128 v[164:167], v240 offset:128\n"
        "ds_read_b128 v[168:171], v241\n"
        "ds_read_b128 v[100:103], v241 offset:64\n"
        "ds_read_b128 v[104:107], v241 offset:128\n"
        "ds_read_b128 v[140:143], v240 offset:6144\n"
        "ds_read_b128 v[132:135], v240 offset:6208\n"
        "ds_read_b128 v[96:99], v240 offset:6272\n"
        "ds_read_b128 v[136:139], v241 offset:6144\n"
        "ds_read_b128 v[128:131], v241 offset:6208\n"
        "ds_read_b128 v[108:111], v241 offset:6272\n"
        "s_waitcnt lgkmcnt(13)\n"
        "v_mfma_f32_16x16x32_bf16 v[64:67], v[92:95], v[88:91], 0\n"
        "s_waitcnt lgkmcnt(11)\n"
        "ds_read_b128 v[152:155], v240 offset:12288\n"
        "v_mfma_f32_16x16x32_bf16 v[64:67], v[172:175], v[180:183], v[64:67]\n"
        "s_waitcnt lgkmcnt(8)\n"
        "ds_read_b128 v[160:163], v240 offset:12352\n"
        "v_mfma_f32_16x16x32_bf16 v[64:67], v[164:167], v[184:187], v[64:67]\n"
        "ds_read_b128 v[112:115], v240 offset:12416\n"
        "v_mfma_f32_16x16x32_bf16 v[68:71], v[168:171], v[88:91], 0\n"
        "ds_read_b128 v[116:119], v241 offset:12288\n"
        "v_mfma_f32_16x16x32_bf16 v[68:71], v[100:103], v[180:183], v[68:71]\n"
        "s_waitcnt lgkmcnt(4)\n"
        "ds_read_b128 v[120:123], v241 offset:12352\n"
        "v_mfma_f32_16x16x32_bf16 v[68:71], v[104:107], v[184:187], v[68:71]\n"
        "ds_read_b128 v[124:127], v241 offset:12416\n"
        "v_mfma_f32_16x16x32_bf16 v[72:75], v[140:143], v[88:91], 0\n"
        "ds_read_b128 v[144:147], v243\n"
        "v_mfma_f32_16x16x32_bf16 v[72:75], v[132:135], v[180:183], v[72:75]\n"
        "ds_read_b128 v[148:151], v243 offset:64\n"
        "v_mfma_f32_16x16x32_bf16 v[72:75], v[96:99], v[184:187], v[72:75]\n"
        "ds_read_b128 v[156:159], v243 offset:128\n"
        "v_mfma_f32_16x16x32_bf16 v[76:79], v[136:139], v[88:91], 0\n"
        "v_add_f32 v176, v64, v68\n"
        "v_add_f32 v177, v65, v69\n"
        "v_add_f32 v178, v66, v70\n"
        "v_add_f32 v179, v67, v71\n"
        "v_mfma_f32_16x16x32_bf16 v[76:79], v[128:131], v[180:183], v[76:79]\n"
        "v_mfma_f32_16x16x32_bf16 v[76:79], v[108:111], v[184:187], v[76:79]\n"
        "s_waitcnt lgkmcnt(3)\n"
        "v_add_f32 v176, v176, v72\n"
        "v_add_f32 v177, v177, v73\n"
        "v_add_f32 v178, v178, v74\n"
        "v_add_f32 v179, v179, v75\n"
        "v_mfma_f32_16x16x32_bf16 v[80:83], v[152:155], v[88:91], 0\n"
        "v_mfma_f32_16x16x32_bf16 v[80:83], v[160:163], v[180:183], v[80:83]\n"
        "v_mfma_f32_16x16x32_bf16 v[80:83], v[112:115], v[184:187], v[80:83]\n"
        "v_add_f32 v176, v176, v76\n"
        "v_add_f32 v177, v177, v77\n"
        "v_add_f32 v178, v178, v78\n"
        "v_add_f32 v179, v179, v79\n"
        "v_mfma_f32_16x16x32_bf16 v[84:87], v[116:119], v[88:91], 0\n"
        "v_mfma_f32_16x16x32_bf16 v[84:87], v[120:123], v[180:183], v[84:87]\n"
        "v_mfma_f32_16x16x32_bf16 v[84:87], v[124:127], v[184:187], v[84:87]\n"
        "s_waitcnt lgkmcnt(0)\n"
        "v_add_f32 v176, v176, v80\n"
        "v_add_f32 v177, v177, v81\n"
        "v_add_f32 v178, v178, v82\n"
        "v_add_f32 v179, v179, v83\n"
        "v_mov_b32 v239, s35\n"
        "s_waitcnt vmcnt(6)\n"
        "s_mov_b64 exec, 1\n"
        "ds_inc_u32 v239, v239 offset:0\n"
        "s_mov_b64 exec, -1\n"
        "v_add_f32 v176, v176, v84\n"
        "v_add_f32 v177, v177, v85\n"
        "v_add_f32 v178, v178, v86\n"
        "v_add_f32 v179, v179, v87\n"
        "v_add_f32 v176, v176, v177\n"
        "v_add_f32 v178, v178, v179\n"
        "v_add_f32 v176, v176, v178\n"
        "v_mov_b32 v177, v176\n"
        "v_mfma_f32_16x16x32_bf16 v[88:91], v[92:95], v[144:147], 0\n"
        "v_mfma_f32_16x16x32_bf16 v[88:91], v[172:175], v[148:151], v[88:91]\n"
        "v_permlane32_swap_b32 v176, v177\n"
        "v_add_f32 v176, v176, v177\n"
        "v_mov_b32 v177, v176\n"
        "v_mfma_f32_16x16x32_bf16 v[88:91], v[164:167], v[156:159], v[88:91]\n"
        "v_mfma_f32_16x16x32_bf16 v[92:95], v[168:171], v[144:147], 0\n"
        "v_permlane16_swap_b32 v176, v177\n"
        "v_add_f32 v176, v176, v177\n"
        "v_mul_f32 v176, 0xbc2aaaab, v176\n"
        "v_mul_f32 v176, s34, v176\n"
        "v_fma_f32 v64, v64, s34, v176\n"
        "v_fma_f32 v65, v65, s34, v176\n"
        "v_fma_f32 v66, v66, s34, v176\n"
        "v_fma_f32 v67, v67, s34, v176\n"
        "v_fma_f32 v68, v68, s34, v176\n"
        "v_fma_f32 v69, v69, s34, v176\n"
        "v_fma_f32 v70, v70, s34, v176\n"
        "v_fma_f32 v71, v71, s34, v176\n"
        "v_fma_f32 v72, v72, s34, v176\n"
        "v_fma_f32 v73, v73, s34, v176\n"
        "v_fma_f32 v74, v74, s34, v176\n"
        "v_fma_f32 v75, v75, s34, v176\n"
        "v_fma_f32 v76, v76, s34, v176\n"
        "v_fma_f32 v77, v77, s34, v176\n"
        "v_fma_f32 v78, v78, s34, v176\n"
        "v_fma_f32 v79, v79, s34, v176\n"
        "v_fma_f32 v80, v80, s34, v176\n"
        "v_fma_f32 v81, v81, s34, v176\n"
        "v_fma_f32 v82, v82, s34, v176\n"
        "v_fma_f32 v83, v83, s34, v176\n"
        "v_fma_f32 v84, v84, s34, v176\n"
        "v_fma_f32 v85, v85, s34, v176\n"
        "v_fma_f32 v86, v86, s34, v176\n"
        "v_fma_f32 v87, v87, s34, v176\n"
        "v_mfma_f32_16x16x32_bf16 v[92:95], v[100:103], v[148:151], v[92:95]\n"
        "v_mfma_f32_16x16x32_bf16 v[92:95], v[104:107], v[156:159], v[92:95]\n"
        "v_mfma_f32_16x16x32_bf16 v[100:103], v[140:143], v[144:147], 0\n"
        "v_mfma_f32_16x16x32_bf16 v[100:103], v[132:135], v[148:151], v[100:103]\n"
        "v_mfma_f32_16x16x32_bf16 v[100:103], v[96:99], v[156:159], v[100:103]\n"
        "v_mfma_f32_16x16x32_bf16 v[104:107], v[136:139], v[144:147], 0\n"
        "v_mfma_f32_16x16x32_bf16 v[104:107], v[128:131], v[148:151], v[104:107]\n"
        "v_mfma_f32_16x16x32_bf16 v[104:107], v[108:111], v[156:159], v[104:107]\n"
        "v_mfma_f32_16x16x32_bf16 v[96:99], v[152:155], v[144:147], 0\n"
        "v_mfma_f32_16x16x32_bf16 v[96:99], v[160:163], v[148:151], v[96:99]\n"
        "v_add_f32 v128, v88, v92\n"
        "v_add_f32 v129, v89, v93\n"
        "v_add_f32 v130, v90, v94\n"
        "v_add_f32 v131, v91, v95\n"
        "v_add_f32 v128, v128, v100\n"
        "v_add_f32 v129, v129, v101\n"
        "v_add_f32 v130, v130, v102\n"
        "v_add_f32 v131, v131, v103\n"
        "v_add_f32 v128, v128, v104\n"
        "v_add_f32 v129, v129, v105\n"
        "v_add_f32 v130, v130, v106\n"
        "v_add_f32 v131, v131, v107\n"
        "v_mfma_f32_16x16x32_bf16 v[96:99], v[112:115], v[156:159], v[96:99]\n"
        "v_mfma_f32_16x16x32_bf16 v[108:111], v[116:119], v[144:147], 0\n"
        "v_mfma_f32_16x16x32_bf16 v[108:111], v[120:123], v[148:151], v[108:111]\n"
        "v_mfma_f32_16x16x32_bf16 v[108:111], v[124:127], v[156:159], v[108:111]\n"
        "v_mov_b32 v239, s37\n"
        "s_nop 3\n"
        "v_add_f32 v128, v128, v96\n"
        "v_add_f32 v129, v129, v97\n"
        "v_add_f32 v130, v130, v98\n"
        "v_add_f32 v131, v131, v99\n"
        "v_add_f32 v128, v128, v108\n"
        "v_add_f32 v129, v129, v109\n"
        "v_add_f32 v130, v130, v110\n"
        "v_add_f32 v131, v131, v111\n"
        "v_add_f32 v128, v128, v129\n"
        "v_add_f32 v130, v130, v131\n"
        "v_add_f32 v128, v128, v130\n"
        "v_mov_b32 v129, v128\n"
        "s_nop 1\n"
        "v_permlane32_swap_b32 v128, v129\n"
        "v_add_f32 v128, v128, v129\n"
        "v_mov_b32 v129, v128\n"
        "s_nop 1\n"
        "v_permlane16_swap_b32 v128, v129\n"
        "v_add_f32 v128, v128, v129\n"
        "v_mul_f32 v128, 0xbc2aaaab, v128\n"
        "v_mul_f32 v128, s34, v128\n"
        "v_fma_f32 v88, v88, s34, v128\n"
        "v_fma_f32 v89, v89, s34, v128\n"
        "v_fma_f32 v90, v90, s34, v128\n"
        "v_fma_f32 v91, v91, s34, v128\n"
        "v_fma_f32 v92, v92, s34, v128\n"
        "v_fma_f32 v93, v93, s34, v128\n"
        "v_fma_f32 v94, v94, s34, v128\n"
        "v_fma_f32 v95, v95, s34, v128\n"
        "v_fma_f32 v100, v100, s34, v128\n"
        "v_fma_f32 v101, v101, s34, v128\n"
        "v_fma_f32 v102, v102, s34, v128\n"
        "v_fma_f32 v103, v103, s34, v128\n"
        "v_fma_f32 v104, v104, s34, v128\n"
        "v_fma_f32 v105, v105, s34, v128\n"
        "v_fma_f32 v106, v106, s34, v128\n"
        "v_fma_f32 v107, v107, s34, v128\n"
        "v_fma_f32 v96, v96, s34, v128\n"
        "v_fma_f32 v97, v97, s34, v128\n"
        "v_fma_f32 v98, v98, s34, v128\n"
        "v_fma_f32 v99, v99, s34, v128\n"
        "v_fma_f32 v108, v108, s34, v128\n"
        "v_fma_f32 v109, v109, s34, v128\n"
        "v_fma_f32 v110, v110, s34, v128\n"
        "v_fma_f32 v111, v111, s34, v128\n"
        "v_mov_b32 v237, s36\n"
        "L62_%=:\n"
        "ds_read_b32 v238, v237 offset:0\n"
        "s_waitcnt lgkmcnt(0)\n"
        "v_readfirstlane_b32 s40, v238\n"
        "s_nop 3\n"
        "s_cmp_ge_i32 s40, 4\n"
        "s_cbranch_scc1 L63_%=\n"
        "s_nop 0\n"
        "s_sub_u32 s41, s41, 1\n"
        "s_cbranch_scc0 L62_%=\n"
        "s_endpgm\n"
        "L63_%=:\n"
        "ds_read_b128 v[132:135], v246\n"
        "ds_read_b128 v[128:131], v246 offset:16\n"
        "ds_read_b128 v[124:127], v246 offset:128\n"
        "ds_read_b128 v[120:123], v246 offset:144\n"
        "ds_read_b128 v[116:119], v246 offset:256\n"
        "ds_read_b128 v[112:115], v246 offset:272\n"
        "s_waitcnt lgkmcnt(5)\n"
        "v_add_f32 v64, v64, v132\n"
        "v_add_f32 v65, v65, v133\n"
        "v_add_f32 v66, v66, v134\n"
        "v_add_f32 v67, v67, v135\n"
        "s_waitcnt lgkmcnt(0)\n"
        "v_add_f32 v68, v68, v128\n"
        "v_add_f32 v69, v69, v129\n"
        "v_add_f32 v70, v70, v130\n"
        "v_add_f32 v71, v71, v131\n"
        "v_add_f32 v88, v88, v132\n"
        "v_add_f32 v89, v89, v133\n"
        "v_add_f32 v90, v90, v134\n"
        "v_add_f32 v91, v91, v135\n"
        "v_add_f32 v92, v92, v128\n"
        "v_add_f32 v93, v93, v129\n"
        "v_add_f32 v94, v94, v130\n"
        "v_add_f32 v95, v95, v131\n"
        "v_add_f32 v72, v72, v124\n"
        "v_add_f32 v73, v73, v125\n"
        "v_add_f32 v74, v74, v126\n"
        "v_add_f32 v75, v75, v127\n"
        "v_add_f32 v76, v76, v120\n"
        "v_add_f32 v77, v77, v121\n"
        "v_add_f32 v78, v78, v122\n"
        "v_add_f32 v79, v79, v123\n"
        "v_add_f32 v100, v100, v124\n"
        "v_add_f32 v101, v101, v125\n"
        "v_add_f32 v102, v102, v126\n"
        "v_add_f32 v103, v103, v127\n"
        "v_add_f32 v104, v104, v120\n"
        "v_add_f32 v105, v105, v121\n"
        "v_add_f32 v106, v106, v122\n"
        "v_add_f32 v107, v107, v123\n"
        "v_add_f32 v80, v80, v116\n"
        "v_add_f32 v81, v81, v117\n"
        "v_add_f32 v82, v82, v118\n"
        "v_add_f32 v83, v83, v119\n"
        "v_add_f32 v84, v84, v112\n"
        "v_add_f32 v85, v85, v113\n"
        "v_add_f32 v86, v86, v114\n"
        "v_add_f32 v87, v87, v115\n"
        "v_add_f32 v96, v96, v116\n"
        "v_add_f32 v97, v97, v117\n"
        "v_add_f32 v98, v98, v118\n"
        "v_add_f32 v99, v99, v119\n"
        "v_add_f32 v108, v108, v112\n"
        "v_add_f32 v109, v109, v113\n"
        "v_add_f32 v110, v110, v114\n"
        "v_add_f32 v111, v111, v115\n"
        "v_cvt_pk_bf16_f32 v112, v64, v65\n"
        "v_cvt_pk_bf16_f32 v113, v66, v67\n"
        "v_cvt_pk_bf16_f32 v114, v68, v69\n"
        "v_cvt_pk_bf16_f32 v115, v70, v71\n"
        "v_cvt_pk_bf16_f32 v64, v88, v89\n"
        "v_cvt_pk_bf16_f32 v65, v90, v91\n"
        "v_cvt_pk_bf16_f32 v66, v92, v93\n"
        "v_cvt_pk_bf16_f32 v67, v94, v95\n"
        "v_cvt_pk_bf16_f32 v68, v72, v73\n"
        "v_cvt_pk_bf16_f32 v69, v74, v75\n"
        "v_cvt_pk_bf16_f32 v70, v76, v77\n"
        "v_cvt_pk_bf16_f32 v71, v78, v79\n"
        "v_cvt_pk_bf16_f32 v72, v100, v101\n"
        "v_cvt_pk_bf16_f32 v73, v102, v103\n"
        "v_cvt_pk_bf16_f32 v74, v104, v105\n"
        "v_cvt_pk_bf16_f32 v75, v106, v107\n"
        "v_cvt_pk_bf16_f32 v76, v80, v81\n"
        "v_cvt_pk_bf16_f32 v77, v82, v83\n"
        "v_cvt_pk_bf16_f32 v78, v84, v85\n"
        "v_cvt_pk_bf16_f32 v79, v86, v87\n"
        "v_cvt_pk_bf16_f32 v80, v96, v97\n"
        "v_cvt_pk_bf16_f32 v81, v98, v99\n"
        "v_cvt_pk_bf16_f32 v82, v108, v109\n"
        "v_cvt_pk_bf16_f32 v83, v110, v111\n"
        "ds_write_b128 v244, v[112:115]\n"
        "ds_write_b128 v245, v[64:67]\n"
        "ds_write_b128 v244, v[68:71] offset:1024\n"
        "ds_write_b128 v245, v[72:75] offset:1024\n"
        "ds_write_b128 v244, v[76:79] offset:2048\n"
        "ds_write_b128 v245, v[80:83] offset:2048\n"
        "s_mov_b64 exec, 1\n"
        "ds_inc_u32 v239, v239 offset:0\n"
        "s_mov_b64 exec, -1\n"
        "s_setpc_b64 s[30:31]\n"
        "FOLDR_%=:\n"
        "ds_read_b64_tr_b16 v[84:85], v242 offset:0\n"
        "ds_read_b64_tr_b16 v[86:87], v243 offset:0\n"
        "ds_read_b64_tr_b16 v[92:93], v242 offset:6144\n"
        "ds_read_b64_tr_b16 v[94:95], v243 offset:6144\n"
        "ds_read_b64_tr_b16 v[80:81], v242 offset:12288\n"
        "ds_read_b64_tr_b16 v[82:83], v243 offset:12288\n"
        "ds_read_b64_tr_b16 v[76:77], v242 offset:8\n"
        "ds_read_b64_tr_b16 v[78:79], v243 offset:8\n"
        "ds_read_b64_tr_b16 v[68:69], v242 offset:6152\n"
        "ds_read_b64_tr_b16 v[70:71], v243 offset:6152\n"
        "ds_read_b64_tr_b16 v[72:73], v242 offset:12296\n"
        "ds_read_b64_tr_b16 v[74:75], v243 offset:12296\n"
        "v_mov_b32 v64, 0x3f803f80\n"
        "v_mov_b32 v65, 0x3f803f80\n"
        "v_mov_b32 v66, 0x3f803f80\n"
        "v_mov_b32 v67, 0x3f803f80\n"
        "ds_read_b64_tr_b16 v[88:89], v242 offset:64\n"
        "ds_read_b64_tr_b16 v[90:91], v243 offset:64\n"
        "s_waitcnt lgkmcnt(12)\n"
        "v_mfma_f32_16x16x32_bf16 v[104:107], v[84:87], v[64:67], 0\n"
        "s_waitcnt lgkmcnt(10)\n"
        "v_mfma_f32_16x16x32_bf16 v[104:107], v[92:95], v[64:67], v[104:107]\n"
        "s_waitcnt lgkmcnt(6)\n"
        "ds_read_b64_tr_b16 v[96:97], v242 offset:6208\n"
        "ds_read_b64_tr_b16 v[98:99], v243 offset:6208\n"
        "v_mfma_f32_16x16x32_bf16 v[104:107], v[80:83], v[64:67], v[104:107]\n"
        "ds_read_b64_tr_b16 v[92:93], v242 offset:12352\n"
        "ds_read_b64_tr_b16 v[94:95], v243 offset:12352\n"
        "v_mfma_f32_16x16x32_bf16 v[100:103], v[76:79], v[64:67], 0\n"
        "s_waitcnt lgkmcnt(6)\n"
        "ds_read_b64_tr_b16 v[80:81], v242 offset:72\n"
        "ds_read_b64_tr_b16 v[82:83], v243 offset:72\n"
        "v_mfma_f32_16x16x32_bf16 v[100:103], v[68:71], v[64:67], v[100:103]\n"
        "ds_read_b64_tr_b16 v[76:77], v242 offset:6216\n"
        "ds_read_b64_tr_b16 v[78:79], v243 offset:6216\n"
        "v_cvt_pk_bf16_f32 v68, v104, v105\n"
        "v_cvt_pk_bf16_f32 v69, v106, v107\n"
        "v_mfma_f32_16x16x32_bf16 v[100:103], v[72:75], v[64:67], v[100:103]\n"
        "s_waitcnt lgkmcnt(8)\n"
        "ds_read_b64_tr_b16 v[84:85], v242 offset:12360\n"
        "ds_read_b64_tr_b16 v[86:87], v243 offset:12360\n"
        "v_mfma_f32_16x16x32_bf16 v[104:107], v[88:91], v[64:67], 0\n"
        "ds_read_b64_tr_b16 v[72:73], v242 offset:128\n"
        "ds_read_b64_tr_b16 v[74:75], v243 offset:128\n"
        "s_waitcnt lgkmcnt(10)\n"
        "v_mfma_f32_16x16x32_bf16 v[104:107], v[96:99], v[64:67], v[104:107]\n"
        "ds_read_b64_tr_b16 v[88:89], v242 offset:6272\n"
        "ds_read_b64_tr_b16 v[90:91], v243 offset:6272\n"
        "v_cvt_pk_bf16_f32 v70, v100, v101\n"
        "v_cvt_pk_bf16_f32 v71, v102, v103\n"
        "s_waitcnt lgkmcnt(9)\n"
        "v_mfma_f32_16x16x32_bf16 v[104:107], v[92:95], v[64:67], v[104:107]\n"
        "s_waitcnt lgkmcnt(8)\n"
        "ds_read_b64_tr_b16 v[100:101], v242 offset:12416\n"
        "ds_read_b64_tr_b16 v[102:103], v243 offset:12416\n"
        "v_mfma_f32_16x16x32_bf16 v[108:111], v[80:83], v[64:67], 0\n"
        "s_waitcnt lgkmcnt(8)\n"
        "ds_read_b64_tr_b16 v[92:93], v242 offset:136\n"
        "ds_read_b64_tr_b16 v[94:95], v243 offset:136\n"
        "v_mfma_f32_16x16x32_bf16 v[108:111], v[76:79], v[64:67], v[108:111]\n"
        "ds_read_b64_tr_b16 v[96:97], v242 offset:6280\n"
        "ds_read_b64_tr_b16 v[98:99], v243 offset:6280\n"
        "v_cvt_pk_bf16_f32 v80, v104, v105\n"
        "v_cvt_pk_bf16_f32 v81, v106, v107\n"
        "s_waitcnt lgkmcnt(8)\n"
        "v_mfma_f32_16x16x32_bf16 v[108:111], v[84:87], v[64:67], v[108:111]\n"
        "ds_read_b64_tr_b16 v[76:77], v242 offset:12424\n"
        "ds_read_b64_tr_b16 v[78:79], v243 offset:12424\n"
        "v_mfma_f32_16x16x32_bf16 v[116:119], v[72:75], v[64:67], 0\n"
        "s_waitcnt lgkmcnt(8)\n"
        "ds_read_b128 v[72:75], v240\n"
        "v_mfma_f32_16x16x32_bf16 v[116:119], v[88:91], v[64:67], v[116:119]\n"
        "ds_read_b128 v[104:107], v241\n"
        "s_waitcnt lgkmcnt(8)\n"
        "v_mfma_f32_16x16x32_bf16 v[116:119], v[100:103], v[64:67], v[116:119]\n"
        "ds_read_b128 v[84:87], v240 offset:6144\n"
        "v_cvt_pk_bf16_f32 v82, v108, v109\n"
        "v_cvt_pk_bf16_f32 v83, v110, v111\n"
        "s_waitcnt lgkmcnt(7)\n"
        "v_mfma_f32_16x16x32_bf16 v[112:115], v[92:95], v[64:67], 0\n"
        "s_waitcnt lgkmcnt(5)\n"
        "ds_read_b128 v[92:95], v241 offset:6144\n"
        "v_mfma_f32_16x16x32_bf16 v[112:115], v[96:99], v[64:67], v[112:115]\n"
        "ds_read_b128 v[100:103], v240 offset:12288\n"
        "v_cvt_pk_bf16_f32 v88, v116, v117\n"
        "v_cvt_pk_bf16_f32 v89, v118, v119\n"
        "s_waitcnt lgkmcnt(5)\n"
        "v_mfma_f32_16x16x32_bf16 v[112:115], v[76:79], v[64:67], v[112:115]\n"
        "s_waitcnt lgkmcnt(4)\n"
        "ds_read_b128 v[116:119], v241 offset:12288\n"
        "v_mfma_f32_16x16x32_bf16 v[64:67], v[68:71], v[72:75], 0\n"
        "s_waitcnt lgkmcnt(4)\n"
        "ds_read_b128 v[120:123], v240 offset:64\n"
        "v_mfma_f32_16x16x32_bf16 v[72:75], v[68:71], v[104:107], 0\n"
        "ds_read_b128 v[108:111], v241 offset:64\n"
        "s_waitcnt lgkmcnt(5)\n"
        "v_mfma_f32_16x16x32_bf16 v[76:79], v[68:71], v[84:87], 0\n"
        "ds_read_b128 v[104:107], v240 offset:6208\n"
        "v_cvt_pk_bf16_f32 v90, v112, v113\n"
        "v_cvt_pk_bf16_f32 v91, v114, v115\n"
        "s_waitcnt lgkmcnt(5)\n"
        "v_mfma_f32_16x16x32_bf16 v[84:87], v[68:71], v[92:95], 0\n"
        "ds_read_b128 v[124:127], v241 offset:6208\n"
        "s_waitcnt lgkmcnt(5)\n"
        "v_mfma_f32_16x16x32_bf16 v[92:95], v[68:71], v[100:103], 0\n"
        "ds_read_b128 v[112:115], v240 offset:12352\n"
        "s_waitcnt lgkmcnt(5)\n"
        "v_mfma_f32_16x16x32_bf16 v[96:99], v[68:71], v[116:119], 0\n"
        "ds_read_b128 v[100:103], v241 offset:12352\n"
        "s_waitcnt lgkmcnt(5)\n"
        "v_mfma_f32_16x16x32_bf16 v[64:67], v[80:83], v[120:123], v[64:67]\n"
        "ds_read_b128 v[68:71], v240 offset:128\n"
        "s_waitcnt lgkmcnt(5)\n"
        "v_mfma_f32_16x16x32_bf16 v[72:75], v[80:83], v[108:111], v[72:75]\n"
        "ds_read_b128 v[116:119], v241 offset:128\n"
        "s_waitcnt lgkmcnt(5)\n"
        "v_mfma_f32_16x16x32_bf16 v[76:79], v[80:83], v[104:107], v[76:79]\n"
        "ds_read_b128 v[108:111], v240 offset:6272\n"
        "s_waitcnt lgkmcnt(5)\n"
        "v_mfma_f32_16x16x32_bf16 v[84:87], v[80:83], v[124:127], v[84:87]\n"
        "ds_read_b128 v[104:107], v241 offset:6272\n"
        "s_waitcnt lgkmcnt(5)\n"
        "v_mfma_f32_16x16x32_bf16 v[92:95], v[80:83], v[112:115], v[92:95]\n"
        "ds_read_b128 v[120:123], v240 offset:12416\n"
        "s_waitcnt lgkmcnt(5)\n"
        "v_mfma_f32_16x16x32_bf16 v[96:99], v[80:83], v[100:103], v[96:99]\n"
        "ds_read_b128 v[112:115], v241 offset:12416\n"
        "s_waitcnt lgkmcnt(5)\n"
        "v_mfma_f32_16x16x32_bf16 v[64:67], v[88:91], v[68:71], v[64:67]\n"
        "s_waitcnt lgkmcnt(4)\n"
        "v_mfma_f32_16x16x32_bf16 v[72:75], v[88:91], v[116:119], v[72:75]\n"
        "v_mov_b32 v239, s35\n"
        "s_waitcnt lgkmcnt(3)\n"
        "v_mfma_f32_16x16x32_bf16 v[76:79], v[88:91], v[108:111], v[76:79]\n"
        "s_waitcnt vmcnt(6)\n"
        "s_mov_b64 exec, 1\n"
        "ds_inc_u32 v239, v239 offset:0\n"
        "s_mov_b64 exec, -1\n"
        "s_waitcnt lgkmcnt(3)\n"
        "v_mfma_f32_16x16x32_bf16 v[84:87], v[88:91], v[104:107], v[84:87]\n"
        "v_add_f32 v238, v64, v72\n"
        "s_waitcnt lgkmcnt(2)\n"
        "v_mfma_f32_16x16x32_bf16 v[92:95], v[88:91], v[120:123], v[92:95]\n"
        "v_add_f32 v238, v238, v76\n"
        "s_waitcnt lgkmcnt(1)\n"
        "v_mfma_f32_16x16x32_bf16 v[96:99], v[88:91], v[112:115], v[96:99]\n"
        "v_mov_b32 v239, s37\n"
        "s_nop 0\n"
        "v_add_f32 v238, v238, v84\n"
        "s_nop 1\n"
        "v_add_f32 v238, v238, v92\n"
        "s_nop 1\n"
        "v_add_f32 v238, v238, v96\n"
        "s_nop 1\n"
        "v_add_f32_dpp v238, v238, v238 row_ror:8 row_mask:0xf bank_mask:0xf\n"
        "s_nop 1\n"
        "v_add_f32_dpp v238, v238, v238 row_ror:4 row_mask:0xf bank_mask:0xf\n"
        "s_nop 1\n"
        "v_add_f32_dpp v238, v238, v238 row_ror:2 row_mask:0xf bank_mask:0xf\n"
        "s_nop 1\n"
        "v_add_f32_dpp v238, v238, v238 row_ror:1 row_mask:0xf bank_mask:0xf\n"
        "v_mul_f32 v238, 0xbc2aaaab, v238\n"
        "v_mul_f32 v238, s34, v238\n"
        "v_fma_f32 v64, v64, s34, v238\n"
        "v_fma_f32 v72, v72, s34, v238\n"
        "v_fma_f32 v76, v76, s34, v238\n"
        "v_fma_f32 v84, v84, s34, v238\n"
        "v_fma_f32 v92, v92, s34, v238\n"
        "v_fma_f32 v96, v96, s34, v238\n"
        "ds_write_b32 v244, v64\n"
        "ds_write_b32 v244, v72 offset:16\n"
        "ds_write_b32 v244, v76 offset:128\n"
        "ds_write_b32 v244, v84 offset:144\n"
        "ds_write_b32 v244, v92 offset:256\n"
        "ds_write_b32 v244, v96 offset:272\n"
        "s_mov_b64 exec, 1\n"
        "ds_inc_u32 v239, v239 offset:0\n"
        "s_mov_b64 exec, -1\n"
        "s_setpc_b64 s[30:31]\n"
        "FOLDW_%=:\n"
        "ds_read_b64_tr_b16 v[64:65], v242 offset:0\n"
        "ds_read_b64_tr_b16 v[66:67], v243 offset:0\n"
        "ds_read_b128 v[172:175], v240\n"
        "ds_read_b64_tr_b16 v[180:181], v242 offset:6144\n"
        "ds_read_b64_tr_b16 v[182:183], v243 offset:6144\n"
        "ds_read_b128 v[120:123], v240 offset:64\n"
        "ds_read_b64_tr_b16 v[184:185], v242 offset:12288\n"
        "ds_read_b64_tr_b16 v[186:187], v243 offset:12288\n"
        "ds_read_b128 v[168:171], v240 offset:128\n"
        "ds_read_b128 v[160:163], v241\n"
        "ds_read_b128 v[148:151], v241 offset:64\n"
        "ds_read_b128 v[132:135], v241 offset:128\n"
        "ds_read_b128 v[124:127], v240 offset:6144\n"
        "ds_read_b128 v[104:107], v240 offset:6208\n"
        "ds_read_b128 v[92:95], v240 offset:6272\n"
        "ds_read_b128 v[96:99], v241 offset:6144\n"
        "ds_read_b128 v[88:91], v241 offset:6208\n"
        "ds_read_b128 v[84:87], v241 offset:6272\n"
        "s_waitcnt lgkmcnt(15)\n"
        "v_mfma_f32_16x16x32_bf16 v[136:139], v[172:175], v[64:67], 0\n"
        "ds_read_b128 v[100:103], v240 offset:12288\n"
        "s_waitcnt lgkmcnt(12)\n"
        "v_mfma_f32_16x16x32_bf16 v[136:139], v[120:123], v[180:183], v[136:139]\n"
        "s_waitcnt lgkmcnt(10)\n"
        "ds_read_b128 v[108:111], v240 offset:12352\n"
        "v_mfma_f32_16x16x32_bf16 v[136:139], v[168:171], v[184:187], v[136:139]\n"
        "s_waitcnt lgkmcnt(7)\n"
        "ds_read_b128 v[68:71], v240 offset:12416\n"
        "v_mfma_f32_16x16x32_bf16 v[140:143], v[160:163], v[64:67], 0\n"
        "ds_read_b128 v[72:75], v241 offset:12288\n"
        "v_mfma_f32_16x16x32_bf16 v[140:143], v[148:151], v[180:183], v[140:143]\n"
        "ds_read_b128 v[76:79], v241 offset:12352\n"
        "v_mfma_f32_16x16x32_bf16 v[140:143], v[132:135], v[184:187], v[140:143]\n"
        "ds_read_b128 v[80:83], v241 offset:12416\n"
        "v_mfma_f32_16x16x32_bf16 v[144:147], v[124:127], v[64:67], 0\n"
        "s_waitcnt lgkmcnt(6)\n"
        "ds_read_b64_tr_b16 v[112:113], v242 offset:8\n"
        "ds_read_b64_tr_b16 v[114:115], v243 offset:8\n"
        "v_mfma_f32_16x16x32_bf16 v[144:147], v[104:107], v[180:183], v[144:147]\n"
        "ds_read_b64_tr_b16 v[116:117], v242 offset:6152\n"
        "ds_read_b64_tr_b16 v[118:119], v243 offset:6152\n"
        "v_add_f32 v164, v136, v140\n"
        "v_add_f32 v165, v137, v141\n"
        "v_add_f32 v166, v138, v142\n"
        "v_add_f32 v167, v139, v143\n"
        "v_mfma_f32_16x16x32_bf16 v[144:147], v[92:95], v[184:187], v[144:147]\n"
        "ds_read_b64_tr_b16 v[128:129], v242 offset:12296\n"
        "ds_read_b64_tr_b16 v[130:131], v243 offset:12296\n"
        "v_mfma_f32_16x16x32_bf16 v[152:155], v[96:99], v[64:67], 0\n"
        "v_mfma_f32_16x16x32_bf16 v[152:155], v[88:91], v[180:183], v[152:155]\n"
        "v_mfma_f32_16x16x32_bf16 v[152:155], v[84:87], v[184:187], v[152:155]\n"
        "s_waitcnt lgkmcnt(6)\n"
        "v_mfma_f32_16x16x32_bf16 v[156:159], v[100:103], v[64:67], 0\n"
        "v_mfma_f32_16x16x32_bf16 v[156:159], v[108:111], v[180:183], v[156:159]\n"
        "v_add_f32 v164, v164, v144\n"
        "v_add_f32 v165, v165, v145\n"
        "v_add_f32 v166, v166, v146\n"
        "v_add_f32 v167, v167, v147\n"
        "v_mfma_f32_16x16x32_bf16 v[156:159], v[68:71], v[184:187], v[156:159]\n"
        "v_add_f32 v164, v164, v152\n"
        "v_add_f32 v165, v165, v153\n"
        "v_add_f32 v166, v166, v154\n"
        "v_add_f32 v167, v167, v155\n"
        "v_mfma_f32_16x16x32_bf16 v[176:179], v[72:75], v[64:67], 0\n"
        "v_mfma_f32_16x16x32_bf16 v[176:179], v[76:79], v[180:183], v[176:179]\n"
        "v_mfma_f32_16x16x32_bf16 v[176:179], v[80:83], v[184:187], v[176:179]\n"
        "s_waitcnt lgkmcnt(0)\n"
        "v_mov_b32 v239, s35\n"
        "v_add_f32 v164, v164, v156\n"
        "v_add_f32 v165, v165, v157\n"
        "v_add_f32 v166, v166, v158\n"
        "v_add_f32 v167, v167, v159\n"
        "s_waitcnt vmcnt(6)\n"
        "s_mov_b64 exec, 1\n"
        "ds_inc_u32 v239, v239 offset:0\n"
        "s_mov_b64 exec, -1\n"
        "v_add_f32 v164, v164, v176\n"
        "v_add_f32 v165, v165, v177\n"
        "v_mfma_f32_16x16x32_bf16 v[64:67], v[172:175], v[112:115], 0\n"
        "v_add_f32 v166, v166, v178\n"
        "v_add_f32 v167, v167, v179\n"
        "v_add_f32 v164, v164, v165\n"
        "v_add_f32 v166, v166, v167\n"
        "v_add_f32 v164, v164, v166\n"
        "v_mov_b32 v165, v164\n"
        "v_mfma_f32_16x16x32_bf16 v[64:67], v[120:123], v[116:119], v[64:67]\n"
        "v_mfma_f32_16x16x32_bf16 v[64:67], v[168:171], v[128:131], v[64:67]\n"
        "v_permlane32_swap_b32 v164, v165\n"
        "v_add_f32 v164, v164, v165\n"
        "v_mov_b32 v165, v164\n"
        "v_mfma_f32_16x16x32_bf16 v[120:123], v[160:163], v[112:115], 0\n"
        "global_load_dwordx4 v[40:43], v57, s[22:23] offset:-3072\n"
        "v_permlane16_swap_b32 v164, v165\n"
        "v_add_f32 v164, v164, v165\n"
        "v_mul_f32 v164, 0xbc2aaaab, v164\n"
        "v_mul_f32 v164, s34, v164\n"
        "v_fma_f32 v136, v136, s34, v164\n"
        "v_fma_f32 v137, v137, s34, v164\n"
        "v_fma_f32 v138, v138, s34, v164\n"
        "v_fma_f32 v139, v139, s34, v164\n"
        "v_fma_f32 v140, v140, s34, v164\n"
        "v_fma_f32 v141, v141, s34, v164\n"
        "v_mfma_f32_16x16x32_bf16 v[120:123], v[148:151], v[116:119], v[120:123]\n"
        "v_fma_f32 v142, v142, s34, v164\n"
        "v_fma_f32 v143, v143, s34, v164\n"
        "v_fma_f32 v144, v144, s34, v164\n"
        "v_fma_f32 v145, v145, s34, v164\n"
        "v_fma_f32 v146, v146, s34, v164\n"
        "v_fma_f32 v147, v147, s34, v164\n"
        "v_fma_f32 v152, v152, s34, v164\n"
        "v_fma_f32 v153, v153, s34, v164\n"
        "v_fma_f32 v154, v154, s34, v164\n"
        "v_fma_f32 v155, v155, s34, v164\n"
        "v_fma_f32 v156, v156, s34, v164\n"
        "v_fma_f32 v157, v157, s34, v164\n"
        "v_fma_f32 v158, v158, s34, v164\n"
        "v_fma_f32 v159, v159, s34, v164\n"
        "v_mfma_f32_16x16x32_bf16 v[120:123], v[132:135], v[128:131], v[120:123]\n"
        "global_load_dwordx4 v[44:47], v57, s[22:23] offset:-2048\n"
        "v_fma_f32 v176, v176, s34, v164\n"
        "v_fma_f32 v177, v177, s34, v164\n"
        "v_fma_f32 v178, v178, s34, v164\n"
        "v_fma_f32 v179, v179, s34, v164\n"
        "v_cvt_pk_bf16_f32 v132, v136, v137\n"
        "v_cvt_pk_bf16_f32 v133, v138, v139\n"
        "v_cvt_pk_bf16_f32 v134, v140, v141\n"
        "v_add_f32 v148, v64, v120\n"
        "v_add_f32 v149, v65, v121\n"
        "v_add_f32 v150, v66, v122\n"
        "v_add_f32 v151, v67, v123\n"
        "v_cvt_pk_bf16_f32 v135, v142, v143\n"
        "v_mfma_f32_16x16x32_bf16 v[140:143], v[124:127], v[112:115], 0\n"
        "v_cvt_pk_bf16_f32 v136, v144, v145\n"
        "v_cvt_pk_bf16_f32 v137, v146, v147\n"
        "v_cvt_pk_bf16_f32 v138, v152, v153\n"
        "v_cvt_pk_bf16_f32 v139, v154, v155\n"
        "v_cvt_pk_bf16_f32 v124, v156, v157\n"
        "v_cvt_pk_bf16_f32 v125, v158, v159\n"
        "v_cvt_pk_bf16_f32 v126, v176, v177\n"
        "v_cvt_pk_bf16_f32 v127, v178, v179\n"
        "v_mfma_f32_16x16x32_bf16 v[140:143], v[104:107], v[116:119], v[140:143]\n"
        "global_load_dwordx4 v[0:3], v57, s[22:23] offset:-1024\n"
        "v_mfma_f32_16x16x32_bf16 v[140:143], v[92:95], v[128:131], v[140:143]\n"
        "v_mfma_f32_16x16x32_bf16 v[104:107], v[96:99], v[112:115], 0\n"
        "global_load_dwordx4 v[4:7], v57, s[22:23] offset:0\n"
        "v_mfma_f32_16x16x32_bf16 v[104:107], v[88:91], v[116:119], v[104:107]\n"
        "v_mfma_f32_16x16x32_bf16 v[104:107], v[84:87], v[128:131], v[104:107]\n"
        "global_load_dwordx4 v[8:11], v57, s[22:23] offset:1024\n"
        "v_mfma_f32_16x16x32_bf16 v[84:87], v[100:103], v[112:115], 0\n"
        "v_mfma_f32_16x16x32_bf16 v[84:87], v[108:111], v[116:119], v[84:87]\n"
        "global_load_dwordx4 v[12:15], v57, s[22:23] offset:2048\n"
        "v_add_f32 v148, v148, v140\n"
        "v_add_f32 v149, v149, v141\n"
        "v_add_f32 v150, v150, v142\n"
        "v_add_f32 v151, v151, v143\n"
        "v_add_f32 v148, v148, v104\n"
        "v_add_f32 v149, v149, v105\n"
        "v_add_f32 v150, v150, v106\n"
        "v_add_f32 v151, v151, v107\n"
        "v_mfma_f32_16x16x32_bf16 v[84:87], v[68:71], v[128:131], v[84:87]\n"
        "v_mfma_f32_16x16x32_bf16 v[88:91], v[72:75], v[112:115], 0\n"
        "global_load_dwordx4 v[16:19], v57, s[22:23] offset:3072\n"
        "v_mfma_f32_16x16x32_bf16 v[88:91], v[76:79], v[116:119], v[88:91]\n"
        "v_mfma_f32_16x16x32_bf16 v[88:91], v[80:83], v[128:131], v[88:91]\n"
        "v_mov_b32 v239, s37\n"
        "s_nop 2\n"
        "v_add_f32 v148, v148, v84\n"
        "v_add_f32 v149, v149, v85\n"
        "v_add_f32 v150, v150, v86\n"
        "v_add_f32 v151, v151, v87\n"
        "v_add_f32 v148, v148, v88\n"
        "v_add_f32 v149, v149, v89\n"
        "v_add_f32 v150, v150, v90\n"
        "v_add_f32 v151, v151, v91\n"
        "v_add_f32 v148, v148, v149\n"
        "v_add_f32 v150, v150, v151\n"
        "v_add_f32 v148, v148, v150\n"
        "v_mov_b32 v149, v148\n"
        "s_nop 1\n"
        "v_permlane32_swap_b32 v148, v149\n"
        "v_add_f32 v148, v148, v149\n"
        "v_mov_b32 v149, v148\n"
        "s_nop 1\n"
        "v_permlane16_swap_b32 v148, v149\n"
        "v_add_f32 v148, v148, v149\n"
        "v_mul_f32 v148, 0xbc2aaaab, v148\n"
        "v_mul_f32 v148, s34, v148\n"
        "v_fma_f32 v64, v64, s34, v148\n"
        "v_fma_f32 v65, v65, s34, v148\n"
        "v_fma_f32 v66, v66, s34, v148\n"
        "v_fma_f32 v67, v67, s34, v148\n"
        "v_fma_f32 v120, v120, s34, v148\n"
        "v_fma_f32 v121, v121, s34, v148\n"
        "v_fma_f32 v122, v122, s34, v148\n"
        "v_fma_f32 v123, v123, s34, v148\n"
        "v_fma_f32 v140, v140, s34, v148\n"
        "v_fma_f32 v141, v141, s34, v148\n"
        "v_fma_f32 v142, v142, s34, v148\n"
        "v_fma_f32 v143, v143, s34, v148\n"
        "v_fma_f32 v104, v104, s34, v148\n"
        "v_fma_f32 v105, v105, s34, v148\n"
        "v_fma_f32 v106, v106, s34, v148\n"
        "v_fma_f32 v107, v107, s34, v148\n"
        "v_fma_f32 v84, v84, s34, v148\n"
        "v_fma_f32 v85, v85, s34, v148\n"
        "v_fma_f32 v86, v86, s34, v148\n"
        "v_fma_f32 v87, v87, s34, v148\n"
        "v_fma_f32 v88, v88, s34, v148\n"
        "v_fma_f32 v89, v89, s34, v148\n"
        "v_fma_f32 v90, v90, s34, v148\n"
        "v_fma_f32 v91, v91, s34, v148\n"
        "v_cvt_pk_bf16_f32 v68, v64, v65\n"
        "v_cvt_pk_bf16_f32 v69, v66, v67\n"
        "v_cvt_pk_bf16_f32 v70, v120, v121\n"
        "v_cvt_pk_bf16_f32 v71, v122, v123\n"
        "v_cvt_pk_bf16_f32 v64, v140, v141\n"
        "v_cvt_pk_bf16_f32 v65, v142, v143\n"
        "v_cvt_pk_bf16_f32 v66, v104, v105\n"
        "v_cvt_pk_bf16_f32 v67, v106, v107\n"
        "v_cvt_pk_bf16_f32 v72, v84, v85\n"
        "v_cvt_pk_bf16_f32 v73, v86, v87\n"
        "v_cvt_pk_bf16_f32 v74, v88, v89\n"
        "v_cvt_pk_bf16_f32 v75, v90, v91\n"
        "s_nop 1\n"
        "v_mov_b32 v238, s36\n"
        "L64_%=:\n"
        "ds_read_b32 v237, v238 offset:0\n"
        "s_waitcnt lgkmcnt(0)\n"
        "v_readfirstlane_b32 s40, v237\n"
        "s_nop 3\n"
        "s_cmp_ge_i32 s40, 4\n"
        "s_cbranch_scc1 L65_%=\n"
        "s_nop 0\n"
        "s_sub_u32 s41, s41, 1\n"
        "s_cbranch_scc0 L64_%=\n"
        "s_endpgm\n"
        "L65_%=:\n"
        "ds_write_b128 v244, v[132:135]\n"
        "ds_write_b128 v244, v[136:139] offset:1024\n"
        "ds_write_b128 v244, v[124:127] offset:2048\n"
        "ds_write_b128 v245, v[68:71]\n"
        "ds_write_b128 v245, v[64:67] offset:1024\n"
        "ds_write_b128 v245, v[72:75] offset:2048\n"
        "s_mov_b64 exec, 1\n"
        "ds_inc_u32 v239, v239 offset:0\n"
        "s_mov_b64 exec, -1\n"
        "s_setpc_b64 s[30:31]\n"
        "PPRO_%=:\n"
        "s_add_u32 s64, s64, 128\n"
        "s_cmp_lg_u32 s6, 0\n"
        "s_cbranch_scc1 L82_%=\n"
        "s_mov_b32 exec_hi, 0\n"
        "v_lshlrev_b32 v27, 2, v56\n"
        "v_add_u32 v27, 21600, v27\n"
        "v_mov_b32 v28, 0\n"
        "ds_write_b32 v27, v28\n"
        "s_mov_b64 exec, 0x1fffff\n"
        "s_bfe_u32 s7, s2, 0x50003\n"
        "s_mul_i32 s7, s7, 2656\n"
        "v_lshlrev_b32 v27, 7, v56\n"
        "v_add_u32 v27, s7, v27\n"
        "global_load_dword v252, v27, s[4:5]\n"
        "s_mov_b64 exec, -1\n"
        "L82_%=:\n"
        "s_cmp_lt_u32 s6, 2\n"
        "s_cselect_b32 s65, 3, 1\n"
        "s_cselect_b32 s61, 384, 0\n"
        "s_cselect_b32 s62, 9, 1\n"
        "s_cselect_b32 s63, 3, 4\n"
        "s_cselect_b32 s26, 3, 1\n"
        "s_cselect_b32 s34, 9, 1\n"
        "s_cselect_b32 s70, 72, 1\n"
        "s_cselect_b32 s71, 36, 4\n"
        "s_cselect_b32 s72, 6, 8\n"
        "s_mul_i32 s70, s70, 192\n"
        "s_mul_i32 s71, s71, 192\n"
        "s_mul_i32 s72, s72, 192\n"
        "s_mul_i32 s65, s65, 192\n"
        "s_mul_i32 s62, s62, 3072\n"
        "s_mul_i32 s63, s63, 1536\n"
        "s_mul_i32 s26, s26, 18432\n"
        "s_mul_i32 s34, s34, 1536\n"
        "s_add_u32 s7, s62, s63\n"
        "s_lshl_b32 s29, s63, 1\n"
        "s_add_u32 s60, s7, s63\n"
        "s_bitcmp1_b32 s6, 0\n"
        "s_cselect_b32 s28, s7, 0\n"
        "s_cselect_b32 s29, s29, s62\n"
        "s_cselect_b32 s60, s60, s63\n"
        "s_add_u32 s28, s28, s61\n"
        "s_add_u32 s29, s29, s61\n"
        "s_add_u32 s60, s60, s61\n"
        "v_add_u32 v24, 0, v56\n"
        "v_mul_u32_u24 v21, 171, v24\n"
        "v_lshrrev_b32 v21, 11, v21\n"
        "v_mad_i32_i24 v24, v21, -12, v24\n"
        "v_bfe_u32 v58, v21, 2, 2\n"
        "v_xor_b32 v58, v24, v58\n"
        "v_and_b32 v27, 3, v21\n"
        "v_mul_u32_u24 v27, s70, v27\n"
        "v_lshl_add_u32 v32, v58, 4, v27\n"
        "v_bfe_u32 v27, v21, 2, 1\n"
        "v_mad_u32_u24 v32, v27, s71, v32\n"
        "v_lshrrev_b32 v27, 3, v21\n"
        "v_mad_u32_u24 v32, v27, s72, v32\n"
        "v_and_b32 v27, 7, v21\n"
        "v_mul_u32_u24 v27, s65, v27\n"
        "v_lshl_add_u32 v58, v58, 4, v27\n"
        "v_add_u32 v25, 64, v56\n"
        "v_mul_u32_u24 v22, 171, v25\n"
        "v_lshrrev_b32 v22, 11, v22\n"
        "v_mad_i32_i24 v25, v22, -12, v25\n"
        "v_bfe_u32 v59, v22, 2, 2\n"
        "v_xor_b32 v59, v25, v59\n"
        "v_and_b32 v27, 3, v22\n"
        "v_mul_u32_u24 v27, s70, v27\n"
        "v_lshl_add_u32 v33, v59, 4, v27\n"
        "v_bfe_u32 v27, v22, 2, 1\n"
        "v_mad_u32_u24 v33, v27, s71, v33\n"
        "v_lshrrev_b32 v27, 3, v22\n"
        "v_mad_u32_u24 v33, v27, s72, v33\n"
        "v_and_b32 v27, 7, v22\n"
        "v_mul_u32_u24 v27, s65, v27\n"
        "v_lshl_add_u32 v59, v59, 4, v27\n"
        "v_lshrrev_b32 v27, 3, v22\n"
        "v_mad_u32_u24 v59, v27, s34, v59\n"
        "v_add_u32 v26, 128, v56\n"
        "v_mul_u32_u24 v23, 171, v26\n"
        "v_lshrrev_b32 v23, 11, v23\n"
        "v_mad_i32_i24 v26, v23, -12, v26\n"
        "v_bfe_u32 v60, v23, 2, 2\n"
        "v_xor_b32 v60, v26, v60\n"
        "v_and_b32 v27, 3, v23\n"
        "v_mul_u32_u24 v27, s70, v27\n"
        "v_lshl_add_u32 v34, v60, 4, v27\n"
        "v_bfe_u32 v27, v23, 2, 1\n"
        "v_mad_u32_u24 v34, v27, s71, v34\n"
        "v_lshrrev_b32 v27, 3, v23\n"
        "v_mad_u32_u24 v34, v27, s72, v34\n"
        "v_and_b32 v27, 7, v23\n"
        "v_mul_u32_u24 v27, s65, v27\n"
        "v_lshl_add_u32 v60, v60, 4, v27\n"
        "v_add_u32 v60, s34, v60\n"
        "s_and_b32 s7, s2, 7\n"
        "s_lshl_b32 s7, s7, 5\n"
        "s_lshr_b32 s14, s2, 3\n"
        "s_add_u32 s7, s7, s14\n"
        "s_and_b32 s16, s7, 255\n"
        "s_sub_u32 s7, s7, s16\n"
        "s_mul_i32 s7, s7, 20\n"
        "s_add_u32 s7, s7, s16\n"
        "s_mov_b32 s14, 22\n"
        "s_mul_i32 s15, s7, 57457\n"
        "s_lshr_b32 s15, s15, 22\n"
        "s_mov_b32 s16, s7\n"
        "s_mul_i32 s15, s15, 588672\n"
        "s_mul_i32 s7, s16, 1344\n"
        "s_add_u32 s15, s15, s7\n"
        "s_mul_i32 s74, s6, 98112\n"
        "s_add_u32 s74, s74, s15\n"
        "s_mov_b32 s36, 96768\n"
        "s_mov_b32 s40, 599187\n"
        "s_cmp_eq_u32 s6, 3\n"
        "s_cselect_b32 s7, s16, s6\n"
        "s_cselect_b32 s36, 588672, s36\n"
        "s_cselect_b32 s74, 589824, s74\n"
        "s_cselect_b32 s37, 512, 1\n"
        "s_cselect_b32 s38, 256, 4\n"
        "s_cselect_b32 s39, 7, 1\n"
        "s_cselect_b32 s40, 57457, s40\n"
        "s_mul_i32 s39, s39, 192\n"
        "s_mov_b32 s35, 50135232\n"
        "v_mov_b32 v29, 2048\n"
        "v_bfe_u32 v27, v21, 2, 1\n"
        "v_and_b32 v21, 3, v21\n"
        "v_mul_u32_u24 v21, s37, v21\n"
        "v_mad_u32_u24 v21, v27, s38, v21\n"
        "v_add_u32 v21, s7, v21\n"
        "v_and_b32 v27, 3, v24\n"
        "v_mad_u32_u24 v29, v27, 48, v29\n"
        "v_lshrrev_b32 v27, 2, v24\n"
        "v_lshl_add_u32 v29, v27, 4, v29\n"
        "v_mul_u32_u24 v27, s40, v21\n"
        "v_mul_u32_u24 v21, s39, v21\n"
        "v_lshrrev_b32 v27, 22, v27\n"
        "v_mul_u32_u24 v27, s36, v27\n"
        "v_add3_u32 v29, v29, v21, v27\n"
        "v_lshrrev_b32 v27, 3, v22\n"
        "v_xor_b32 v25, v25, v27\n"
        "v_mul_lo_u32 v30, v27, s35\n"
        "v_add_u32 v30, 1024, v30\n"
        "v_bfe_u32 v27, v22, 2, 1\n"
        "v_and_b32 v22, 3, v22\n"
        "v_mul_u32_u24 v22, s37, v22\n"
        "v_mad_u32_u24 v22, v27, s38, v22\n"
        "v_add_u32 v22, s7, v22\n"
        "v_and_b32 v27, 3, v25\n"
        "v_mad_u32_u24 v30, v27, 48, v30\n"
        "v_lshrrev_b32 v27, 2, v25\n"
        "v_lshl_add_u32 v30, v27, 4, v30\n"
        "v_mul_u32_u24 v27, s40, v22\n"
        "v_mul_u32_u24 v22, s39, v22\n"
        "v_lshrrev_b32 v27, 22, v27\n"
        "v_mul_u32_u24 v27, s36, v27\n"
        "v_add3_u32 v30, v30, v22, v27\n"
        "v_mov_b32 v31, 50135232\n"
        "v_xor_b32 v26, 1, v26\n"
        "v_bfe_u32 v27, v23, 2, 1\n"
        "v_and_b32 v23, 3, v23\n"
        "v_mul_u32_u24 v23, s37, v23\n"
        "v_mad_u32_u24 v23, v27, s38, v23\n"
        "v_add_u32 v23, s7, v23\n"
        "v_and_b32 v27, 3, v26\n"
        "v_mad_u32_u24 v31, v27, 48, v31\n"
        "v_lshrrev_b32 v27, 2, v26\n"
        "v_lshl_add_u32 v31, v27, 4, v31\n"
        "v_mul_u32_u24 v27, s40, v23\n"
        "v_mul_u32_u24 v23, s39, v23\n"
        "v_lshrrev_b32 v27, 22, v27\n"
        "v_mul_u32_u24 v27, s36, v27\n"
        "v_add3_u32 v31, v31, v23, v27\n"
        "s_mul_i32 s17, s6, 6208\n"
        "s_add_u32 s17, s17, 110176\n"
        "s_mov_b32 s18, s6\n"
        "s_add_u32 s75, s74, 100270464\n"
        "v_mov_b32 v250, 21600\n"
        "s_mov_b32 s41, 0x400000\n"
        "s_waitcnt lgkmcnt(0)\n"
        "s_cmp_lg_u32 s6, 0\n"
        "s_cbranch_scc1 L83_%=\n"
        "s_bitcmp1_b32 s2, 7\n"
        "s_cselect_b64 s[8:9], s[50:51], s[46:47]\n"
        "s_cselect_b64 s[10:11], s[52:53], s[48:49]\n"
        "s_bfe_u32 s7, s2, 0x40003\n"
        "s_cmp_ge_u32 s7, 12\n"
        "s_cselect_b64 s[8:9], s[10:11], s[8:9]\n"
        "s_cselect_b32 s10, 12, 0\n"
        "s_sub_u32 s7, s7, s10\n"
        "s_mul_i32 s7, s7, 4608\n"
        "v_lshlrev_b32 v27, 7, v56\n"
        "v_add_u32 v27, s7, v27\n"
        "s_mov_b32 exec_hi, 0xf\n"
        "global_load_dword v252, v27, s[8:9]\n"
        "s_mov_b32 exec_hi, -1\n"
        "s_mov_b64 s[24:25], s[56:57]\n"
        "s_mov_b64 exec, 1\n"
        "v_mov_b32 v27, 0\n"
        "global_load_dword v253, v27, s[24:25] offset:256 sc1\n"
        "s_mov_b64 exec, -1\n"
        "L83_%=:\n"
        "s_cmp_lt_u32 s6, 2\n"
        "s_cselect_b64 s[24:25], s[46:47], s[48:49]\n"
        "s_and_b32 s25, s25, 0xffff\n"
        "s_sub_u32 s8, s44, 2048\n"
        "s_subb_u32 s9, s45, 0\n"
        "s_and_b32 s9, s9, 0xffff\n"
        "s_mov_b32 s10, 200542976\n"
        "s_mov_b32 s11, 0x20000\n"
        "s_mul_i32 s7, s6, 7168\n"
        "s_add_u32 s7, s7, 6144\n"
        "s_add_u32 s22, s4, s7\n"
        "s_addc_u32 s23, s5, 0\n"
        "s_sub_u32 s7, s6, 0\n"
        "s_and_b32 s7, s7, 3\n"
        "s_min_u32 s7, s7, 2\n"
        "s_lshl_b32 s7, s7, 10\n"
        "s_add_u32 s73, s7, 18528\n"
        "v_add_u32 v27, s7, v57\n"
        "s_barrier\n"
        "s_mov_b32 m0, s73\n"
        "s_nop 0\n"
        "global_load_lds_dwordx4 v27, s[4:5]\n"
        "global_load_dwordx4 v[240:243], v57, s[22:23] offset:-3072\n"
        "global_load_dwordx4 v[244:247], v57, s[22:23] offset:-2048\n"
        "s_add_u32 m0, s64, 0\n"
        "s_nop 0\n"
        "buffer_load_dwordx4 v58, s[24:27], s28 offen lds\n"
        "s_add_u32 m0, s64, 1024\n"
        "s_nop 0\n"
        "buffer_load_dwordx4 v59, s[24:27], s28 offen lds\n"
        "s_add_u32 m0, s64, 2048\n"
        "s_nop 0\n"
        "buffer_load_dwordx4 v60, s[24:27], s28 offen lds\n"
        "s_add_u32 m0, s64, 3072\n"
        "s_nop 0\n"
        "buffer_load_dwordx4 v58, s[24:27], s29 offen lds\n"
        "s_add_u32 m0, s64, 4096\n"
        "s_nop 0\n"
        "buffer_load_dwordx4 v59, s[24:27], s29 offen lds\n"
        "s_add_u32 m0, s64, 5120\n"
        "s_nop 0\n"
        "buffer_load_dwordx4 v60, s[24:27], s29 offen lds\n"
        "s_add_u32 m0, s64, 6144\n"
        "s_nop 0\n"
        "buffer_load_dwordx4 v58, s[24:27], s60 offen lds\n"
        "s_add_u32 m0, s64, 7168\n"
        "s_nop 0\n"
        "buffer_load_dwordx4 v59, s[24:27], s60 offen lds\n"
        "s_add_u32 m0, s64, 8192\n"
        "s_nop 0\n"
        "buffer_load_dwordx4 v60, s[24:27], s60 offen lds\n"
        "s_cmp_lt_u32 s6, 2\n"
        "s_cselect_b32 s62, 3, 16\n"
        "s_cselect_b32 s63, 12, 32\n"
        "s_cselect_b64 s[24:25], s[50:51], s[52:53]\n"
        "s_cselect_b32 s64, 0, 117088\n"
        "s_mul_i32 s62, s62, 192\n"
        "s_mul_i32 s63, s63, 192\n"
        "s_and_b32 s25, s25, 0xffff\n"
        "s_mul_i32 s7, s6, 9216\n"
        "s_add_u32 s64, s64, s7\n"
        "s_add_u32 s64, s64, 0\n"
        "s_add_u32 s7, s62, s63\n"
        "s_lshl_b32 s29, s63, 1\n"
        "s_add_u32 s60, s7, s63\n"
        "s_bitcmp1_b32 s6, 0\n"
        "s_cselect_b32 s28, s7, 0\n"
        "s_cselect_b32 s29, s29, s62\n"
        "s_cselect_b32 s60, s60, s63\n"
        "s_add_u32 s28, s28, s61\n"
        "s_add_u32 s29, s29, s61\n"
        "s_add_u32 s60, s60, s61\n"
        "s_add_u32 m0, s64, 0\n"
        "s_nop 0\n"
        "buffer_load_dwordx4 v32, s[24:27], s28 offen lds\n"
        "s_add_u32 m0, s64, 1024\n"
        "s_nop 0\n"
        "buffer_load_dwordx4 v33, s[24:27], s28 offen lds\n"
        "s_add_u32 m0, s64, 2048\n"
        "s_nop 0\n"
        "buffer_load_dwordx4 v34, s[24:27], s28 offen lds\n"
        "s_add_u32 m0, s64, 3072\n"
        "s_nop 0\n"
        "buffer_load_dwordx4 v32, s[24:27], s29 offen lds\n"
        "s_add_u32 m0, s64, 4096\n"
        "s_nop 0\n"
        "buffer_load_dwordx4 v33, s[24:27], s29 offen lds\n"
        "s_add_u32 m0, s64, 5120\n"
        "s_nop 0\n"
        "buffer_load_dwordx4 v34, s[24:27], s29 offen lds\n"
        "s_add_u32 m0, s64, 6144\n"
        "s_nop 0\n"
        "buffer_load_dwordx4 v32, s[24:27], s60 offen lds\n"
        "s_add_u32 m0, s64, 7168\n"
        "s_nop 0\n"
        "buffer_load_dwordx4 v33, s[24:27], s60 offen lds\n"
        "s_add_u32 m0, s64, 8192\n"
        "s_nop 0\n"
        "buffer_load_dwordx4 v34, s[24:27], s60 offen lds\n"
        "s_setprio 3\n"
        "s_waitcnt vmcnt(9)\n"
        "s_mov_b64 exec, 1\n"
        "ds_inc_u32 v250, v250 offset:20\n"
        "s_mov_b64 exec, -1\n"
        "L84_%=:\n"
        "ds_read_b32 v251, v250 offset:20\n"
        "s_waitcnt lgkmcnt(0)\n"
        "v_readfirstlane_b32 s40, v251\n"
        "s_nop 3\n"
        "s_cmp_ge_i32 s40, 4\n"
        "s_cbranch_scc1 L85_%=\n"
        "s_nop 0\n"
        "s_sub_u32 s41, s41, 1\n"
        "s_cbranch_scc0 L84_%=\n"
        "s_endpgm\n"
        "L85_%=:\n"
        "s_add_u32 m0, s17, 0\n"
        "s_nop 0\n"
        "buffer_load_dwordx4 v29, s[8:11], s74 offen lds\n"
        "buffer_load_dwordx4 v30, s[8:11], s74 offen offset:1024 lds\n"
        "buffer_load_dwordx4 v31, s[8:11], s74 offen offset:2048 lds\n"
        "s_add_u32 m0, s17, 3104\n"
        "s_nop 0\n"
        "buffer_load_dwordx4 v29, s[8:11], s75 offen lds\n"
        "buffer_load_dwordx4 v30, s[8:11], s75 offen offset:1024 lds\n"
        "buffer_load_dwordx4 v31, s[8:11], s75 offen offset:2048 lds\n"
        "s_cmp_eq_u32 s6, 0\n"
        "s_cbranch_scc1 L86_%=\n"
        "s_mov_b32 s34, 0x411cc471\n"
        "s_mov_b32 s35, 21632\n"
        "s_mov_b32 s36, 21624\n"
        "s_mov_b32 s37, 21628\n"
        "s_call_b64 s[30:31], FOLDW_%=\n"
        "s_branch L87_%=\n"
        "L86_%=:\n"
        "s_waitcnt vmcnt(6)\n"
        "s_mov_b64 exec, 1\n"
        "ds_inc_u32 v250, v250 offset:32\n"
        "s_mov_b64 exec, -1\n"
        "global_load_dwordx4 v[40:43], v57, s[22:23] offset:-3072\n"
        "global_load_dwordx4 v[44:47], v57, s[22:23] offset:-2048\n"
        "global_load_dwordx4 v[0:3], v57, s[22:23] offset:-1024\n"
        "global_load_dwordx4 v[4:7], v57, s[22:23] offset:0\n"
        "global_load_dwordx4 v[8:11], v57, s[22:23] offset:1024\n"
        "global_load_dwordx4 v[12:15], v57, s[22:23] offset:2048\n"
        "global_load_dwordx4 v[16:19], v57, s[22:23] offset:3072\n"
        "L87_%=:\n"
        "s_mov_b64 s[24:25], s[56:57]\n"
        "s_setprio 3\n"
        "s_mov_b32 s44, 0xf0000f\n"
        "s_mov_b32 s45, 0xf0000f00\n"
        "s_mov_b32 s46, 0xff000ff\n"
        "s_mov_b32 s47, 0xf00fff00\n"
        "s_mov_b32 s48, 0x22222222\n"
        "s_mov_b32 s49, 0x22222222\n"
        "s_mov_b32 s50, 0x44444444\n"
        "s_mov_b32 s51, 0x44444444\n"
        "s_mov_b32 s52, 0x88888888\n"
        "s_mov_b32 s53, 0x88888888\n"
        "s_mov_b64 vcc, s[44:45]\n"
        "s_mov_b32 s28, 0x3c2aaaab\n"
        "s_mov_b32 s29, 0x3a7ba882\n"
        "s_mov_b32 s30, 0\n"
        "s_mov_b32 s34, 0\n"
        "s_cmp_eq_u32 s18, 3\n"
        "s_cselect_b32 s31, 6, s18\n"
        "s_mov_b32 s36, s18\n"
        "s_mov_b32 s37, s18\n"
        "s_add_u32 s71, s17, 3104\n"
        "s_mov_b32 s68, 4\n"
        "s_mov_b32 s63, 0x07060302\n"
        "s_mov_b32 s74, 16\n"
        "s_mov_b32 s72, -1\n"
        "s_mov_b32 s41, 0x400000\n"
        "L23_%=:\n"
        "ds_read_b32 v251, v250 offset:28\n"
        "s_waitcnt lgkmcnt(0)\n"
        "v_readfirstlane_b32 s40, v251\n"
        "s_nop 3\n"
        "s_cmp_ge_i32 s40, 6\n"
        "s_cbranch_scc1 L24_%=\n"
        "s_nop 0\n"
        "s_sub_u32 s41, s41, 1\n"
        "s_cbranch_scc0 L23_%=\n"
        "s_endpgm\n"
        "L24_%=:\n"
        "s_waitcnt vmcnt(0)\n"
        "v_readfirstlane_b32 s26, v253\n"
        "ds_read_b128 v[16:19], v16\n"
        "ds_read_b64 v[94:95], v14\n"
        "ds_read_b64 v[86:87], v14\n"
        "ds_read_b64 v[28:29], v14\n"
        "ds_read_b64 v[90:91], v14\n"
        "v_mov_b32 v102, 21600\n"
        "v_add_u32 v20, 21728, v13\n"
        "ds_read_b128 v[112:115], v20 offset:0\n"
        "ds_read_b128 v[116:119], v20 offset:1024\n"
        "ds_read_b128 v[120:123], v20 offset:2048\n"
        "ds_read_b128 v[124:127], v20 offset:3072\n"
        "ds_read_b128 v[128:131], v20 offset:4096\n"
        "ds_read_b128 v[132:135], v20 offset:5120\n"
        "ds_read_b128 v[136:139], v20 offset:6144\n"
        "ds_read_b128 v[140:143], v20 offset:7168\n"
        "ds_read_b128 v[144:147], v20 offset:8192\n"
        "ds_read_b128 v[148:151], v20 offset:9216\n"
        "ds_read_b128 v[152:155], v20 offset:10240\n"
        "ds_read_b128 v[156:159], v20 offset:11264\n"
        "ds_read_b128 v[160:163], v20 offset:12288\n"
        "ds_read_b128 v[164:167], v20 offset:13312\n"
        "ds_read_b128 v[168:171], v20 offset:14336\n"
        "ds_read_b128 v[172:175], v20 offset:15360\n"
        "ds_read_b128 v[176:179], v20 offset:16384\n"
        "ds_read_b128 v[180:183], v20 offset:17408\n"
        "ds_read_b128 v[184:187], v20 offset:18432\n"
        "ds_read_b128 v[188:191], v20 offset:19456\n"
        "ds_read_b128 v[192:195], v20 offset:20480\n"
        "ds_read_b128 v[196:199], v20 offset:21520\n"
        "ds_read_b128 v[200:203], v20 offset:22544\n"
        "ds_read_b128 v[204:207], v20 offset:23568\n"
        "ds_read_b128 v[208:211], v20 offset:24608\n"
        "ds_read_b128 v[212:215], v20 offset:25632\n"
        "ds_read_b128 v[216:219], v20 offset:26656\n"
        "ds_read_b128 v[220:223], v20 offset:27696\n"
        "ds_read_b128 v[224:227], v20 offset:28720\n"
        "ds_read_b128 v[228:231], v20 offset:29744\n"
        "ds_read_b128 v[232:235], v20 offset:30784\n"
        "ds_read_b128 v[236:239], v20 offset:31808\n"
        "ds_read_b128 v[240:243], v20 offset:32832\n"
        "ds_read_b128 v[244:247], v20 offset:33872\n"
        "ds_read_b128 v[248:251], v20 offset:34896\n"
        "ds_read_b128 v[252:255], v20 offset:35920\n"
        "ds_read_b128 v[60:63], v0 offset:0\n"
        "ds_read_b128 v[66:69], v0 offset:64\n"
        "ds_read_b128 v[72:75], v0 offset:128\n"
        "ds_read_b128 v[42:45], v0 offset:768\n"
        "ds_read_b128 v[48:51], v0 offset:832\n"
        "ds_read_b128 v[54:57], v0 offset:896\n"
        "s_add_u32 s37, s37, 4\n"
        "s_cmp_ge_u32 s37, 49\n"
        "s_cselect_b32 s69, 49, 0\n"
        "s_sub_u32 s37, s37, s69\n"
        "s_sub_u32 s69, s37, 4\n"
        "s_cmp_lt_u32 s69, 4\n"
        "s_subb_u32 s36, s36, -4\n"
        "s_cmp_ge_u32 s36, 6\n"
        "s_cselect_b32 s69, 6, 0\n"
        "s_addc_u32 s34, s34, 0\n"
        "s_sub_u32 s36, s36, s69\n"
        "s_cmp_ge_u32 s34, s74\n"
        "s_cbranch_scc0 L27_%=\n"
        "s_nop 1\n"
        "v_mov_b32 v22, 21600\n"
        "L25_%=:\n"
        "ds_read_b32 v23, v22 offset:64\n"
        "s_waitcnt lgkmcnt(0)\n"
        "v_readfirstlane_b32 s40, v23\n"
        "s_nop 3\n"
        "s_cmp_ge_i32 s40, 1\n"
        "s_cbranch_scc1 L26_%=\n"
        "s_sleep 1\n"
        "s_sub_u32 s41, s41, 1\n"
        "s_cbranch_scc0 L25_%=\n"
        "s_endpgm\n"
        "L26_%=:\n"
        "v_mbcnt_lo_u32_b32 v23, -1, 0\n"
        "v_lshl_add_u32 v22, v23, 2, v22\n"
        "ds_read_b32 v23, v22 offset:68\n"
        "s_mov_b32 s14, s40\n"
        "s_mov_b32 s74, -1\n"
        "s_waitcnt lgkmcnt(0)\n"
        "v_readlane_b32 s22, v23, 0\n"
        "v_readlane_b32 s23, v23, 1\n"
        "s_nop 3\n"
        "L27_%=:\n"
        "s_mul_i32 s38, s34, 256\n"
        "s_add_u32 s38, s38, s16\n"
        "s_cmp_ge_u32 s34, 20\n"
        "s_cselect_b32 s38, s22, s38\n"
        "s_cmp_ge_u32 s34, 21\n"
        "s_cselect_b32 s38, s23, s38\n"
        "s_mul_hi_u32 s70, s38, 58835169\n"
        "s_mul_i32 s38, s38, 1344\n"
        "s_mul_i32 s70, s70, 588672\n"
        "s_add_u32 s38, s38, s70\n"
        "s_mul_i32 s69, s36, 98304\n"
        "s_add_u32 s38, s38, s69\n"
        "s_mov_b32 s35, s36\n"
        "s_mul_i32 s69, s36, -12\n"
        "s_add_u32 s69, s69, 60\n"
        "s_lshl_b64 s[54:55], -1, s69\n"
        "s_lshl_b64 s[56:57], s[54:55], 24\n"
        "s_ashr_i64 s[60:61], s[54:55], 8\n"
        "s_mov_b32 s58, s61\n"
        "s_mov_b32 s59, s56\n"
        "v_cndmask_b32_e64 v15, v4, v7, s[56:57]\n"
        "v_cndmask_b32_e64 v100, v5, v8, s[58:59]\n"
        "v_cndmask_b32_e64 v103, v6, v9, s[60:61]\n"
        "s_cmp_eq_u32 s37, 3\n"
        "s_cbranch_scc0 L28_%=\n"
        "s_nop 1\n"
        "ds_read_b32 v15, v13 offset:19552\n"
        "ds_read_b32 v100, v13 offset:19556\n"
        "ds_read_b32 v103, v13 offset:19560\n"
        "s_mov_b32 s35, 6\n"
        "s_mov_b32 s38, 0\n"
        "s_mov_b32 s69, 1344\n"
        "s_waitcnt lgkmcnt(0)\n"
        "v_lshrrev_b32 v22, 28, v15\n"
        "v_add_u32 v22, s34, v22\n"
        "v_and_b32 v15, 0x0fffffff, v15\n"
        "v_mul_u32_u24 v23, 256, v22\n"
        "v_add_u32 v23, s16, v23\n"
        "v_cmpx_le_u32 vcc, 20, v22\n"
        "v_mov_b32 v23, s22\n"
        "s_mov_b64 exec, -1\n"
        "v_cmpx_le_u32 vcc, 21, v22\n"
        "v_mov_b32 v23, s23\n"
        "s_mov_b64 exec, -1\n"
        "v_mad_u32_u24 v15, v23, s69, v15\n"
        "v_mul_u32_u24 v23, 57457, v23\n"
        "v_lshrrev_b32 v23, 22, v23\n"
        "v_mul_u32_u24 v23, 588672, v23\n"
        "v_add_u32 v15, v15, v23\n"
        "v_lshrrev_b32 v22, 28, v100\n"
        "v_add_u32 v22, s34, v22\n"
        "v_and_b32 v100, 0x0fffffff, v100\n"
        "v_mul_u32_u24 v23, 256, v22\n"
        "v_add_u32 v23, s16, v23\n"
        "v_cmpx_le_u32 vcc, 20, v22\n"
        "v_mov_b32 v23, s22\n"
        "s_mov_b64 exec, -1\n"
        "v_cmpx_le_u32 vcc, 21, v22\n"
        "v_mov_b32 v23, s23\n"
        "s_mov_b64 exec, -1\n"
        "v_mad_u32_u24 v100, v23, s69, v100\n"
        "v_mul_u32_u24 v23, 57457, v23\n"
        "v_lshrrev_b32 v23, 22, v23\n"
        "v_mul_u32_u24 v23, 588672, v23\n"
        "v_add_u32 v100, v100, v23\n"
        "v_lshrrev_b32 v22, 28, v103\n"
        "v_add_u32 v22, s34, v22\n"
        "v_and_b32 v103, 0x0fffffff, v103\n"
        "v_mul_u32_u24 v23, 256, v22\n"
        "v_add_u32 v23, s16, v23\n"
        "v_cmpx_le_u32 vcc, 20, v22\n"
        "v_mov_b32 v23, s22\n"
        "s_mov_b64 exec, -1\n"
        "v_cmpx_le_u32 vcc, 21, v22\n"
        "v_mov_b32 v23, s23\n"
        "s_mov_b64 exec, -1\n"
        "v_mad_u32_u24 v103, v23, s69, v103\n"
        "v_mul_u32_u24 v23, 57457, v23\n"
        "v_lshrrev_b32 v23, 22, v23\n"
        "v_mul_u32_u24 v23, 588672, v23\n"
        "v_add_u32 v103, v103, v23\n"
        "s_nop 4\n"
        "s_mov_b64 vcc, s[44:45]\n"
        "L28_%=:\n"
        "s_cmp_lt_u32 s34, s14\n"
        "s_cselect_b32 s38, s38, 0x80000000\n"
        "s_add_u32 s39, s38, 100270464\n"
        "s_waitcnt lgkmcnt(0)\n"
        "s_mov_b64 exec, 1\n"
        "ds_inc_u32 v102, v0 offset:8\n"
        "s_mov_b64 exec, -1\n"
        "s_cmp_eq_u32 s18, 0\n"
        "s_cselect_b32 s73, -20, 8\n"
        "s_setprio 1\n"
        "PFST_%=:\n"
        "v_mfma_f32_16x16x32_bf16 v[34:37], v[112:115], v[60:63], 0\n"
        "s_mov_b32 m0, s17\n"
        "v_mfma_f32_16x16x32_bf16 v[34:37], v[116:119], v[66:69], v[34:37]\n"
        "v_mfma_f32_16x16x32_bf16 v[34:37], v[120:123], v[72:75], v[34:37]\n"
        "buffer_load_dwordx4 v15, s[8:11], s38 offen lds\n"
        "v_mfma_f32_16x16x32_bf16 v[38:41], v[132:135], v[72:75], 0\n"
        "v_mfma_f32_16x16x32_bf16 v[38:41], v[128:131], v[66:69], v[38:41]\n"
        "s_setprio 3\n"
        "v_mfma_f32_16x16x32_bf16 v[38:41], v[124:127], v[60:63], v[38:41]\n"
        "buffer_load_dwordx4 v100, s[8:11], s38 offen offset:1024 lds\n"
        "v_mfma_f32_16x16x32_bf16 v[78:81], v[136:139], v[60:63], 0\n"
        "v_mfma_f32_16x16x32_bf16 v[78:81], v[140:143], v[66:69], v[78:81]\n"
        "v_mfma_f32_16x16x32_bf16 v[78:81], v[144:147], v[72:75], v[78:81]\n"
        "v_mfma_f32_16x16x32_bf16 v[82:85], v[156:159], v[72:75], 0\n"
        "v_mfma_f32_16x16x32_bf16 v[82:85], v[152:155], v[66:69], v[82:85]\n"
        "v_mfma_f32_16x16x32_bf16 v[82:85], v[148:151], v[60:63], v[82:85]\n"
        "buffer_load_dwordx4 v103, s[8:11], s38 offen offset:2048 lds\n"
        "s_mov_b32 m0, s71\n"
        "s_cmp_eq_u32 s72, s30\n"
        "v_mfma_f32_16x16x32_bf16 v[104:107], v[160:163], v[60:63], 0\n"
        "s_setprio 3\n"
        "s_cbranch_scc1 F5_%=\n"
        "s_setprio 1\n"
        "F5_%=:\n"
        "v_mfma_f32_16x16x32_bf16 v[104:107], v[164:167], v[66:69], v[104:107]\n"
        "v_mfma_f32_16x16x32_bf16 v[104:107], v[168:171], v[72:75], v[104:107]\n"
        "buffer_load_dwordx4 v15, s[8:11], s39 offen lds\n"
        "v_mfma_f32_16x16x32_bf16 v[108:111], v[180:183], v[72:75], 0\n"
        "v_mfma_f32_16x16x32_bf16 v[108:111], v[176:179], v[66:69], v[108:111]\n"
        "v_mfma_f32_16x16x32_bf16 v[108:111], v[172:175], v[60:63], v[108:111]\n"
        "buffer_load_dwordx4 v100, s[8:11], s39 offen offset:1024 lds\n"
        "v_mfma_f32_16x16x32_bf16 v[20:23], v[60:63], v[60:63], 0\n"
        "v_mfma_f32_16x16x32_bf16 v[20:23], v[66:69], v[66:69], v[20:23]\n"
        "v_mfma_f32_16x16x32_bf16 v[20:23], v[72:75], v[72:75], v[20:23]\n"
        "buffer_load_dwordx4 v103, s[8:11], s39 offen offset:2048 lds\n"
        "v_mov_b64 v[96:97], 0\n"
        "v_mov_b64 v[98:99], 0\n"
        "v_mfma_f32_16x16x32_bf16 v[24:27], v[42:45], v[42:45], 0\n"
        "v_mfma_f32_16x16x32_bf16 v[24:27], v[48:51], v[48:51], v[24:27]\n"
        "v_mfma_f32_16x16x32_bf16 v[24:27], v[54:57], v[54:57], v[24:27]\n"
        "v_smfmac_f32_16x16x64_bf16 v[96:99], v[60:63], v[34:41], v10\n"
        "s_nop 0\n"
        "v_cndmask_b32_e64 v93, v20, v21, s[48:49]\n"
        "v_smfmac_f32_16x16x64_bf16 v[96:99], v[66:69], v[78:85], v10\n"
        "v_cndmask_b32_e64 v93, v93, v22, s[50:51]\n"
        "v_smfmac_f32_16x16x64_bf16 v[96:99], v[72:75], v[104:111], v10\n"
        "v_mfma_f32_16x16x32_bf16 v[34:37], v[112:115], v[42:45], 0\n"
        "s_nop 0\n"
        "v_cndmask_b32_e64 v111, v24, v25, s[48:49]\n"
        "v_cndmask_b32_e64 v111, v111, v26, s[50:51]\n"
        "v_mfma_f32_16x16x32_bf16 v[34:37], v[116:119], v[48:51], v[34:37]\n"
        "s_and_b32 s69, s30, 1\n"
        "v_cndmask_b32_e64 v93, v93, v23, s[52:53]\n"
        "v_cndmask_b32_e64 v111, v111, v27, s[52:53]\n"
        "v_mfma_f32_16x16x32_bf16 v[34:37], v[120:123], v[54:57], v[34:37]\n"
        "s_lshl2_add_u32 s70, s69, 21600\n"
        "v_mov_b32 v102, s70\n"
        "v_cndmask_b32_dpp v93, v111, v93, vcc row_ror:4 row_mask:0xf bank_mask:0xf\n"
        "v_mfma_f32_16x16x32_bf16 v[38:41], v[132:135], v[54:57], 0\n"
        "s_mul_i32 s69, s69, 40064\n"
        "s_mul_i32 s70, s31, 1664\n"
        "v_mfma_f32_16x16x32_bf16 v[38:41], v[128:131], v[48:51], v[38:41]\n"
        "s_add_u32 s69, s69, s70\n"
        "v_add_u32 v11, s69, v3\n"
        "v_mfma_f32_16x16x32_bf16 v[38:41], v[124:127], v[42:45], v[38:41]\n"
        "v_add_u32 v12, s69, v2\n"
        "s_lshr_b32 s68, s30, 1\n"
        "v_mfma_f32_16x16x32_bf16 v[104:107], v[136:139], v[42:45], 0\n"
        "s_lshl2_add_u32 s68, s68, 4\n"
        "s_cmp_eq_u32 s31, 6\n"
        "s_cbranch_scc1 F8_%=\n"
        "F9_%=:\n"
        "v_mfma_f32_16x16x32_bf16 v[104:107], v[140:143], v[48:51], v[104:107]\n"
        "s_mov_b32 s72, s30\n"
        "s_mov_b32 s30, s34\n"
        "v_mfma_f32_16x16x32_bf16 v[104:107], v[144:147], v[54:57], v[104:107]\n"
        "s_mov_b32 s31, s35\n"
        "s_add_u32 s37, s37, 4\n"
        "v_mfma_f32_16x16x32_bf16 v[108:111], v[156:159], v[54:57], 0\n"
        "s_cmp_ge_u32 s37, 49\n"
        "s_cselect_b32 s69, 49, 0\n"
        "v_mfma_f32_16x16x32_bf16 v[108:111], v[152:155], v[48:51], v[108:111]\n"
        "s_sub_u32 s37, s37, s69\n"
        "s_sub_u32 s69, s37, 4\n"
        "s_cmp_lt_u32 s69, 4\n"
        "v_mfma_f32_16x16x32_bf16 v[108:111], v[148:151], v[42:45], v[108:111]\n"
        "s_subb_u32 s36, s36, -4\n"
        "s_cmp_ge_u32 s36, 6\n"
        "v_mfma_f32_16x16x32_bf16 v[76:79], v[160:163], v[42:45], 0\n"
        "s_cselect_b32 s69, 6, 0\n"
        "s_addc_u32 s34, s34, 0\n"
        "v_mfma_f32_16x16x32_bf16 v[76:79], v[164:167], v[48:51], v[76:79]\n"
        "s_sub_u32 s36, s36, s69\n"
        "s_cmp_ge_u32 s34, s74\n"
        "v_mfma_f32_16x16x32_bf16 v[76:79], v[168:171], v[54:57], v[76:79]\n"
        "s_cbranch_scc1 F10_%=\n"
        "F11_%=:\n"
        "s_mul_i32 s38, s34, 256\n"
        "v_mfma_f32_16x16x32_bf16 v[80:83], v[180:183], v[54:57], 0\n"
        "s_add_u32 s38, s38, s16\n"
        "s_cmp_ge_u32 s34, 20\n"
        "s_cselect_b32 s38, s22, s38\n"
        "v_mfma_f32_16x16x32_bf16 v[80:83], v[176:179], v[48:51], v[80:83]\n"
        "s_cmp_ge_u32 s34, 21\n"
        "s_cselect_b32 s38, s23, s38\n"
        "v_mfma_f32_16x16x32_bf16 v[80:83], v[172:175], v[42:45], v[80:83]\n"
        "s_mul_hi_u32 s70, s38, 58835169\n"
        "s_mul_i32 s38, s38, 1344\n"
        "v_mfma_f32_16x16x32_bf16 v[20:23], v[16:19], v[60:63], 0\n"
        "s_mul_i32 s70, s70, 588672\n"
        "s_add_u32 s38, s38, s70\n"
        "v_mfma_f32_16x16x32_bf16 v[20:23], v[16:19], v[66:69], v[20:23]\n"
        "s_mul_i32 s69, s36, 98304\n"
        "s_add_u32 s38, s38, s69\n"
        "s_mov_b32 s35, s36\n"
        "v_mfma_f32_16x16x32_bf16 v[20:23], v[16:19], v[72:75], v[20:23]\n"
        "s_mul_i32 s69, s36, -12\n"
        "v_mov_b64 v[30:31], 0\n"
        "v_mov_b64 v[32:33], 0\n"
        "v_mfma_f32_16x16x32_bf16 v[24:27], v[16:19], v[42:45], 0\n"
        "s_add_u32 s69, s69, 60\n"
        "s_lshl_b64 s[54:55], -1, s69\n"
        "s_lshl_b64 s[56:57], s[54:55], 24\n"
        "v_mfma_f32_16x16x32_bf16 v[24:27], v[16:19], v[48:51], v[24:27]\n"
        "s_ashr_i64 s[60:61], s[54:55], 8\n"
        "s_mov_b32 s58, s61\n"
        "v_mfma_f32_16x16x32_bf16 v[24:27], v[16:19], v[54:57], v[24:27]\n"
        "s_mov_b32 s59, s56\n"
        "v_cndmask_b32_e64 v15, v4, v7, s[56:57]\n"
        "v_smfmac_f32_16x16x64_bf16 v[30:33], v[42:45], v[34:41], v10\n"
        "v_cndmask_b32_e64 v100, v5, v8, s[58:59]\n"
        "v_smfmac_f32_16x16x64_bf16 v[30:33], v[48:51], v[104:111], v10\n"
        "v_cndmask_b32_e64 v103, v6, v9, s[60:61]\n"
        "v_smfmac_f32_16x16x64_bf16 v[30:33], v[54:57], v[76:83], v10\n"
        "s_cmp_eq_u32 s37, 3\n"
        "s_cbranch_scc1 F12_%=\n"
        "F13_%=:\n"
        "v_cndmask_b32_dpp v110, v24, v20, vcc row_ror:4 row_mask:0xf bank_mask:0xf\n"
        "v_mfma_f32_16x16x32_bf16 v[36:39], v[54:57], v[192:195], 0\n"
        "s_cmp_lt_u32 s34, s14\n"
        "v_mul_f32 v111, s28, v110\n"
        "v_fma_f32 v111, -v111, v110, v93\n"
        "v_mfma_f32_16x16x32_bf16 v[36:39], v[48:51], v[188:191], v[36:39]\n"
        "s_cselect_b32 s38, s38, 0x80000000\n"
        "s_add_u32 s39, s38, 100270464\n"
        "v_add_f32_e64 v111, s29, |v111|\n"
        "v_mfma_f32_16x16x32_bf16 v[36:39], v[42:45], v[184:187], v[36:39]\n"
        "v_rsq_f32 v84, v111\n"
        "v_cndmask_b32_dpp v104, v30, v96, vcc row_ror:4 row_mask:0xf bank_mask:0xf\n"
        "v_mfma_f32_16x16x32_bf16 v[24:27], v[72:75], v[192:195], 0\n"
        "v_cndmask_b32_e64 v84, 0, v84, s[46:47]\n"
        "v_cndmask_b32_dpp v105, v31, v97, vcc row_ror:4 row_mask:0xf bank_mask:0xf\n"
        "v_mfma_f32_16x16x32_bf16 v[24:27], v[66:69], v[188:191], v[24:27]\n"
        "v_cndmask_b32_dpp v85, v32, v98, vcc row_ror:4 row_mask:0xf bank_mask:0xf\n"
        "v_mul_f32_dpp v104, v84, v104 quad_perm:[0,0,0,0] row_mask:0xf bank_mask:0xf\n"
        "v_mfma_f32_16x16x32_bf16 v[24:27], v[60:63], v[184:187], v[24:27]\n"
        "v_mul_f32_dpp v105, v84, v105 quad_perm:[1,1,1,1] row_mask:0xf bank_mask:0xf\n"
        "v_mul_f32_dpp v85, v84, v85 quad_perm:[2,2,2,2] row_mask:0xf bank_mask:0xf\n"
        "v_mfma_f32_16x16x32_bf16 v[20:23], v[60:63], v[220:223], 0\n"
        "v_cndmask_b32_dpp v106, v33, v99, vcc row_ror:4 row_mask:0xf bank_mask:0xf\n"
        "v_max3_f32 v107, v104, v105, v85\n"
        "v_mfma_f32_16x16x32_bf16 v[20:23], v[66:69], v[224:227], v[20:23]\n"
        "v_mul_f32_dpp v106, v84, v106 quad_perm:[3,3,3,3] row_mask:0xf bank_mask:0xf\n"
        "v_max_f32 v107, v107, v106\n"
        "v_mfma_f32_16x16x32_bf16 v[20:23], v[72:75], v[228:231], v[20:23]\n"
        "v_mul_f32 v107, v107, v84\n"
        "v_fma_f32 v104, v104, v84, -v107\n"
        "v_fma_f32 v105, v105, v84, -v107\n"
        "v_mfma_f32_16x16x32_bf16 v[32:35], v[42:45], v[220:223], 0\n"
        "v_fma_f32 v85, v85, v84, -v107\n"
        "v_fma_f32 v106, v106, v84, -v107\n"
        "v_mfma_f32_16x16x32_bf16 v[32:35], v[48:51], v[224:227], v[32:35]\n"
        "v_exp_f32 v104, v104\n"
        "v_mfma_f32_16x16x32_bf16 v[32:35], v[54:57], v[228:231], v[32:35]\n"
        "v_exp_f32 v105, v105\n"
        "v_mfma_f32_16x16x32_bf16 v[108:111], v[54:57], v[204:207], 0\n"
        "v_exp_f32 v85, v85\n"
        "v_mfma_f32_16x16x32_bf16 v[108:111], v[48:51], v[200:203], v[108:111]\n"
        "v_exp_f32 v106, v106\n"
        "v_mfma_f32_16x16x32_bf16 v[108:111], v[42:45], v[196:199], v[108:111]\n"
        "v_add_f32 v107, v104, v105\n"
        "v_add_f32 v101, v85, v106\n"
        "v_add_f32 v107, v107, v101\n"
        "v_mfma_f32_16x16x32_bf16 v[80:83], v[72:75], v[204:207], 0\n"
        "v_rcp_f32 v107, v107\n"
        "v_mfma_f32_16x16x32_bf16 v[80:83], v[66:69], v[200:203], v[80:83]\n"
        "v_mul_f32_dpp v104, v84, v104 quad_perm:[0,0,0,0] row_mask:0xf bank_mask:0xf\n"
        "v_mul_f32_dpp v105, v84, v105 quad_perm:[1,1,1,1] row_mask:0xf bank_mask:0xf\n"
        "v_mfma_f32_16x16x32_bf16 v[80:83], v[60:63], v[196:199], v[80:83]\n"
        "v_mul_f32_dpp v85, v84, v85 quad_perm:[2,2,2,2] row_mask:0xf bank_mask:0xf\n"
        "v_mul_f32_dpp v106, v84, v106 quad_perm:[3,3,3,3] row_mask:0xf bank_mask:0xf\n"
        "v_mul_f32 v104, v104, v107\n"
        "v_mfma_f32_16x16x32_bf16 v[76:79], v[60:63], v[232:235], 0\n"
        "v_mul_f32 v105, v105, v107\n"
        "v_mul_f32 v85, v85, v107\n"
        "v_mfma_f32_16x16x32_bf16 v[76:79], v[66:69], v[236:239], v[76:79]\n"
        "v_mul_f32 v106, v106, v107\n"
        "v_cvt_pk_bf16_f32 v99, v104, v105\n"
        "v_mfma_f32_16x16x32_bf16 v[76:79], v[72:75], v[240:243], v[76:79]\n"
        "v_cvt_pk_bf16_f32 v101, v85, v106\n"
        "v_and_b32 v58, v1, v99\n"
        "v_and_b32 v59, v1, v101\n"
        "v_mfma_f32_16x16x32_bf16 v[104:107], v[42:45], v[232:235], 0\n"
        "v_cvt_pk_bf16_f32 v92, v24, v25\n"
        "v_cvt_pk_bf16_f32 v93, v26, v27\n"
        "v_mfma_f32_16x16x32_bf16 v[104:107], v[48:51], v[236:239], v[104:107]\n"
        "v_and_b32_dpp v40, v99, v1 row_ror:12 row_mask:0xf bank_mask:0xf\n"
        "v_and_b32_dpp v41, v101, v1 row_ror:12 row_mask:0xf bank_mask:0xf\n"
        "v_mfma_f32_16x16x32_bf16 v[104:107], v[54:57], v[240:243], v[104:107]\n"
        "v_cvt_pk_bf16_f32 v84, v36, v37\n"
        "v_cvt_pk_bf16_f32 v85, v38, v39\n"
        "v_mov_b64 v[70:71], v[58:59]\n"
        "v_mfma_f32_16x16x32_bf16 v[36:39], v[92:95], v[58:61], 0\n"
        "v_cvt_pk_bf16_f32 v96, v20, v21\n"
        "v_cvt_pk_bf16_f32 v97, v22, v23\n"
        "v_mfma_f32_16x16x32_bf16 v[22:25], v[84:87], v[40:43], 0\n"
        "v_mov_b64 v[52:53], v[40:41]\n"
        "v_cvt_pk_bf16_f32 v88, v32, v33\n"
        "v_cvt_pk_bf16_f32 v89, v34, v35\n"
        "v_mfma_f32_16x16x32_bf16 v[32:35], v[94:97], v[68:71], 0\n"
        "v_mov_b64 v[64:65], v[58:59]\n"
        "v_cvt_pk_bf16_f32 v30, v80, v81\n"
        "v_mfma_f32_16x16x32_bf16 v[96:99], v[86:89], v[50:53], 0\n"
        "v_cvt_pk_bf16_f32 v31, v82, v83\n"
        "v_mov_b64 v[46:47], v[40:41]\n"
        "v_mfma_f32_16x16x32_bf16 v[80:83], v[54:57], v[216:219], 0\n"
        "v_cvt_pk_bf16_f32 v92, v108, v109\n"
        "v_cvt_pk_bf16_f32 v93, v110, v111\n"
        "v_mfma_f32_16x16x32_bf16 v[80:83], v[48:51], v[212:215], v[80:83]\n"
        "v_cvt_pk_bf16_f32 v26, v76, v77\n"
        "v_cvt_pk_bf16_f32 v27, v78, v79\n"
        "v_mfma_f32_16x16x32_bf16 v[80:83], v[42:45], v[208:211], v[80:83]\n"
        "v_cvt_pk_bf16_f32 v88, v104, v105\n"
        "v_cvt_pk_bf16_f32 v89, v106, v107\n"
        "v_mfma_f32_16x16x32_bf16 v[108:111], v[72:75], v[216:219], 0\n"
        "v_mov_b64 v[76:77], v[58:59]\n"
        "v_mov_b64 v[58:59], v[40:41]\n"
        "v_mfma_f32_16x16x32_bf16 v[108:111], v[66:69], v[212:215], v[108:111]\n"
        "v_cvt_pk_bf16_f32 v104, v36, v37\n"
        "v_cvt_pk_bf16_f32 v105, v38, v39\n"
        "v_mfma_f32_16x16x32_bf16 v[108:111], v[60:63], v[208:211], v[108:111]\n"
        "v_cvt_pk_bf16_f32 v84, v80, v81\n"
        "v_cvt_pk_bf16_f32 v85, v82, v83\n"
        "v_mfma_f32_16x16x32_bf16 v[80:83], v[28:31], v[62:65], 0\n"
        "v_cvt_pk_bf16_f32 v106, v32, v33\n"
        "v_cvt_pk_bf16_f32 v107, v34, v35\n"
        "v_mfma_f32_16x16x32_bf16 v[32:35], v[90:93], v[44:47], 0\n"
        "v_cvt_pk_bf16_f32 v20, v22, v23\n"
        "v_cvt_pk_bf16_f32 v21, v24, v25\n"
        "v_mfma_f32_16x16x32_bf16 v[38:41], v[60:63], v[244:247], 0\n"
        "v_cvt_pk_bf16_f32 v92, v108, v109\n"
        "v_cvt_pk_bf16_f32 v93, v110, v111\n"
        "v_mfma_f32_16x16x32_bf16 v[38:41], v[66:69], v[248:251], v[38:41]\n"
        "v_cvt_pk_bf16_f32 v108, v80, v81\n"
        "v_cvt_pk_bf16_f32 v109, v82, v83\n"
        "v_mfma_f32_16x16x32_bf16 v[38:41], v[72:75], v[252:255], v[38:41]\n"
        "v_cvt_pk_bf16_f32 v22, v96, v97\n"
        "v_cvt_pk_bf16_f32 v23, v98, v99\n"
        "v_mfma_f32_16x16x32_bf16 v[80:83], v[42:45], v[244:247], 0\n"
        "v_cvt_pk_bf16_f32 v24, v32, v33\n"
        "v_cvt_pk_bf16_f32 v25, v34, v35\n"
        "v_mfma_f32_16x16x32_bf16 v[80:83], v[48:51], v[248:251], v[80:83]\n"
        "v_mfma_f32_16x16x32_bf16 v[80:83], v[54:57], v[252:255], v[80:83]\n"
        "v_mfma_f32_16x16x32_bf16 v[34:37], v[26:29], v[70:73], 0\n"
        "v_cvt_pk_bf16_f32 v96, v38, v39\n"
        "v_cvt_pk_bf16_f32 v97, v40, v41\n"
        "v_mfma_f32_16x16x32_bf16 v[38:41], v[88:91], v[52:55], 0\n"
        "v_mfma_f32_16x16x32_bf16 v[30:33], v[92:95], v[64:67], 0\n"
        "s_waitcnt vmcnt(0)\n"
        "ds_read_b128 v[60:63], v0\n"
        "ds_read_b128 v[42:45], v0 offset:768\n"
        "ds_read_b128 v[66:69], v0 offset:64\n"
        "v_cvt_pk_bf16_f32 v88, v80, v81\n"
        "v_mfma_f32_16x16x32_bf16 v[78:81], v[84:87], v[46:49], 0\n"
        "s_add_u32 s73, s73, 1\n"
        "ds_read_b128 v[48:51], v0 offset:832\n"
        "v_cvt_pk_bf16_f32 v89, v82, v83\n"
        "v_cvt_pk_bf16_f32 v110, v34, v35\n"
        "v_cvt_pk_bf16_f32 v111, v36, v37\n"
        "v_mfma_f32_16x16x32_bf16 v[82:85], v[94:97], v[74:77], 0\n"
        "s_cmp_lt_u32 s73, 4\n"
        "ds_read_b128 v[72:75], v0 offset:128\n"
        "v_cvt_pk_bf16_f32 v96, v30, v31\n"
        "v_cvt_pk_bf16_f32 v97, v32, v33\n"
        "v_cvt_pk_bf16_f32 v26, v38, v39\n"
        "v_mfma_f32_16x16x32_bf16 v[34:37], v[86:89], v[56:59], 0\n"
        "s_cbranch_scc1 F19_%=\n"
        "F20_%=:\n"
        "ds_read_b128 v[54:57], v0 offset:896\n"
        "v_cvt_pk_bf16_f32 v98, v82, v83\n"
        "v_cvt_pk_bf16_f32 v99, v84, v85\n"
        "v_cvt_pk_bf16_f32 v27, v40, v41\n"
        "v_mfma_f32_16x16x32_bf16 v[82:85], v[104:107], v[104:107], 0\n"
        "v_cvt_pk_bf16_f32 v30, v78, v79\n"
        "v_cvt_pk_bf16_f32 v31, v80, v81\n"
        "v_mfma_f32_16x16x32_bf16 v[82:85], v[108:111], v[108:111], v[82:85]\n"
        "v_cvt_pk_bf16_f32 v32, v34, v35\n"
        "v_cvt_pk_bf16_f32 v33, v36, v37\n"
        "v_mfma_f32_16x16x32_bf16 v[82:85], v[96:99], v[96:99], v[82:85]\n"
        "v_mfma_f32_16x16x32_bf16 v[76:79], v[20:23], v[20:23], 0\n"
        "v_mfma_f32_16x16x32_bf16 v[76:79], v[24:27], v[24:27], v[76:79]\n"
        "v_mfma_f32_16x16x32_bf16 v[76:79], v[30:33], v[30:33], v[76:79]\n"
        "s_cmp_lt_u32 s30, s14\n"
        "s_cmp_lg_u32 s72, 0\n"
        "s_cbranch_scc1 PTOP_%=\n"
        "s_setprio 3\n"
        "s_nop 7\n"
        "F21_%=:\n"
        "ds_read_b32 v101, v102 offset:8\n"
        "s_waitcnt lgkmcnt(0)\n"
        "v_readfirstlane_b32 s40, v101\n"
        "s_nop 3\n"
        "s_cmp_ge_i32 s40, s68\n"
        "s_cbranch_scc1 F22_%=\n"
        "s_sleep 1\n"
        "s_sub_u32 s41, s41, 1\n"
        "s_cbranch_scc0 F21_%=\n"
        "s_endpgm\n"
        "F22_%=:\n"
        "v_cndmask_b32_e64 v101, v82, v83, s[48:49]\n"
        "v_cndmask_b32_e64 v93, v76, v77, s[48:49]\n"
        "v_cndmask_b32_e64 v101, v101, v84, s[50:51]\n"
        "v_cndmask_b32_e64 v93, v93, v78, s[50:51]\n"
        "v_cndmask_b32_e64 v101, v101, v85, s[52:53]\n"
        "v_cndmask_b32_e64 v93, v93, v79, s[52:53]\n"
        "ds_write_b128 v11, v[104:107]\n"
        "ds_write_b128 v11, v[20:23] offset:832\n"
        "v_cndmask_b32_dpp v101, v93, v101, vcc row_ror:4 row_mask:0xf bank_mask:0xf\n"
        "ds_write_b128 v11, v[108:111] offset:64\n"
        "ds_write_b128 v11, v[24:27] offset:896\n"
        "ds_write_b128 v11, v[96:99] offset:128\n"
        "ds_write_b128 v11, v[30:33] offset:960\n"
        "s_mov_b64 exec, s[46:47]\n"
        "ds_write_b32 v12, v101\n"
        "s_mov_b64 exec, -1\n"
        "s_mov_b64 exec, 2\n"
        "ds_inc_u32 v102, v0 offset:0\n"
        "s_mov_b64 exec, -1\n"
        "s_setprio 3\n"
        "s_waitcnt lgkmcnt(0)\n"
        "s_branch PFST_%=\n"
        ".p2align 6\n"
        "s_nop 0\n"
        "s_nop 0\n"
        "s_nop 0\n"
        "s_nop 0\n"
        "PTOP_%=:\n"
        "s_waitcnt lgkmcnt(5)\n"
        "v_mfma_f32_16x16x32_bf16 v[34:37], v[112:115], v[60:63], 0\n"
        "s_mov_b32 m0, s17\n"
        "v_cndmask_b32_e64 v88, v82, v83, s[48:49]\n"
        "s_nop 1\n"
        "v_cndmask_b32_e64 v89, v76, v77, s[48:49]\n"
        "s_waitcnt lgkmcnt(3)\n"
        "v_mfma_f32_16x16x32_bf16 v[34:37], v[116:119], v[66:69], v[34:37]\n"
        "ds_read_b32 v101, v102 offset:8\n"
        "v_cndmask_b32_e64 v88, v88, v84, s[50:51]\n"
        "s_waitcnt lgkmcnt(2)\n"
        "v_mfma_f32_16x16x32_bf16 v[34:37], v[120:123], v[72:75], v[34:37]\n"
        "s_waitcnt lgkmcnt(1)\n"
        "buffer_load_dwordx4 v15, s[8:11], s38 offen lds\n"
        "v_mfma_f32_16x16x32_bf16 v[38:41], v[132:135], v[72:75], 0\n"
        "v_cndmask_b32_e64 v89, v89, v78, s[50:51]\n"
        "v_cndmask_b32_e64 v88, v88, v85, s[52:53]\n"
        "v_mfma_f32_16x16x32_bf16 v[38:41], v[128:131], v[66:69], v[38:41]\n"
        "s_setprio 3 \n"
        "v_cndmask_b32_e64 v89, v89, v79, s[52:53]\n"
        "v_mfma_f32_16x16x32_bf16 v[38:41], v[124:127], v[60:63], v[38:41]\n"
        "buffer_load_dwordx4 v100, s[8:11], s38 offen offset:1024 lds\n"
        "v_mfma_f32_16x16x32_bf16 v[78:81], v[136:139], v[60:63], 0\n"
        "s_waitcnt lgkmcnt(0)\n"
        "v_readfirstlane_b32 s40, v101\n"
        "v_cndmask_b32_dpp v88, v89, v88, vcc row_ror:4 row_mask:0xf bank_mask:0xf\n"
        "v_mfma_f32_16x16x32_bf16 v[78:81], v[140:143], v[66:69], v[78:81]\n"
        "s_cmp_lt_i32 s40, s68\n"
        "v_mfma_f32_16x16x32_bf16 v[78:81], v[144:147], v[72:75], v[78:81]\n"
        "s_cbranch_scc1 L3_%=\n"
        "L4_%=:\n"
        "ds_write_b128 v11, v[104:107]\n"
        "s_mov_b64 exec, s[46:47]\n"
        "ds_write_b32 v12, v88\n"
        "s_mov_b64 exec, -1\n"
        "v_mfma_f32_16x16x32_bf16 v[82:85], v[156:159], v[72:75], 0\n"
        "v_mfma_f32_16x16x32_bf16 v[82:85], v[152:155], v[66:69], v[82:85]\n"
        "v_mfma_f32_16x16x32_bf16 v[82:85], v[148:151], v[60:63], v[82:85]\n"
        "buffer_load_dwordx4 v103, s[8:11], s38 offen offset:2048 lds\n"
        "s_mov_b32 m0, s71\n"
        "ds_write_b128 v11, v[20:23] offset:832\n"
        "s_cmp_eq_u32 s72, s30\n"
        "v_mfma_f32_16x16x32_bf16 v[104:107], v[160:163], v[60:63], 0\n"
        "s_setprio 3\n"
        "s_cbranch_scc1 L5_%=\n"
        "s_setprio 1\n"
        "L5_%=:\n"
        "v_mfma_f32_16x16x32_bf16 v[104:107], v[164:167], v[66:69], v[104:107]\n"
        "v_mfma_f32_16x16x32_bf16 v[104:107], v[168:171], v[72:75], v[104:107]\n"
        "buffer_load_dwordx4 v15, s[8:11], s39 offen lds\n"
        "ds_write_b128 v11, v[108:111] offset:64\n"
        "v_mfma_f32_16x16x32_bf16 v[108:111], v[180:183], v[72:75], 0\n"
        "v_mfma_f32_16x16x32_bf16 v[108:111], v[176:179], v[66:69], v[108:111]\n"
        "v_mfma_f32_16x16x32_bf16 v[108:111], v[172:175], v[60:63], v[108:111]\n"
        "buffer_load_dwordx4 v100, s[8:11], s39 offen offset:1024 lds\n"
        "ds_write_b128 v11, v[24:27] offset:896\n"
        "v_mfma_f32_16x16x32_bf16 v[20:23], v[60:63], v[60:63], 0\n"
        "v_mfma_f32_16x16x32_bf16 v[20:23], v[66:69], v[66:69], v[20:23]\n"
        "v_mfma_f32_16x16x32_bf16 v[20:23], v[72:75], v[72:75], v[20:23]\n"
        "buffer_load_dwordx4 v103, s[8:11], s39 offen offset:2048 lds\n"
        "ds_write_b128 v11, v[96:99] offset:128\n"
        "v_mov_b64 v[96:97], 0\n"
        "v_mov_b64 v[98:99], 0\n"
        "v_mfma_f32_16x16x32_bf16 v[24:27], v[42:45], v[42:45], 0\n"
        "v_mfma_f32_16x16x32_bf16 v[24:27], v[48:51], v[48:51], v[24:27]\n"
        "v_mfma_f32_16x16x32_bf16 v[24:27], v[54:57], v[54:57], v[24:27]\n"
        "ds_write_b128 v11, v[30:33] offset:960\n"
        "v_smfmac_f32_16x16x64_bf16 v[96:99], v[60:63], v[34:41], v10\n"
        "v_cndmask_b32_e64 v93, v20, v21, s[48:49]\n"
        "v_smfmac_f32_16x16x64_bf16 v[96:99], v[66:69], v[78:85], v10\n"
        "v_cndmask_b32_e64 v93, v93, v22, s[50:51]\n"
        "v_smfmac_f32_16x16x64_bf16 v[96:99], v[72:75], v[104:111], v10\n"
        "s_mov_b64 exec, 2\n"
        "ds_inc_u32 v102, v0 offset:0\n"
        "s_mov_b64 exec, -1\n"
        "v_mfma_f32_16x16x32_bf16 v[34:37], v[112:115], v[42:45], 0\n"
        "v_cndmask_b32_e64 v111, v24, v25, s[48:49]\n"
        "v_cndmask_b32_e64 v111, v111, v26, s[50:51]\n"
        "v_mfma_f32_16x16x32_bf16 v[34:37], v[116:119], v[48:51], v[34:37]\n"
        "s_and_b32 s69, s30, 1\n"
        "v_cndmask_b32_e64 v93, v93, v23, s[52:53]\n"
        "v_cndmask_b32_e64 v111, v111, v27, s[52:53]\n"
        "v_mfma_f32_16x16x32_bf16 v[34:37], v[120:123], v[54:57], v[34:37]\n"
        "s_lshl2_add_u32 s70, s69, 21600\n"
        "v_mov_b32 v102, s70\n"
        "v_cndmask_b32_dpp v93, v111, v93, vcc row_ror:4 row_mask:0xf bank_mask:0xf\n"
        "v_mfma_f32_16x16x32_bf16 v[38:41], v[132:135], v[54:57], 0\n"
        "s_mul_i32 s69, s69, 40064\n"
        "s_mul_i32 s70, s31, 1664\n"
        "v_mfma_f32_16x16x32_bf16 v[38:41], v[128:131], v[48:51], v[38:41]\n"
        "s_add_u32 s69, s69, s70\n"
        "v_add_u32 v11, s69, v3\n"
        "v_mfma_f32_16x16x32_bf16 v[38:41], v[124:127], v[42:45], v[38:41]\n"
        "v_add_u32 v12, s69, v2\n"
        "s_lshr_b32 s68, s30, 1\n"
        "v_mfma_f32_16x16x32_bf16 v[104:107], v[136:139], v[42:45], 0\n"
        "s_lshl2_add_u32 s68, s68, 4\n"
        "s_cmp_eq_u32 s31, 6\n"
        "s_cbranch_scc1 L8_%=\n"
        "L9_%=:\n"
        "v_mfma_f32_16x16x32_bf16 v[104:107], v[140:143], v[48:51], v[104:107]\n"
        "s_mov_b32 s72, s30\n"
        "s_mov_b32 s30, s34\n"
        "v_mfma_f32_16x16x32_bf16 v[104:107], v[144:147], v[54:57], v[104:107]\n"
        "s_mov_b32 s31, s35\n"
        "s_add_u32 s37, s37, 4\n"
        "v_mfma_f32_16x16x32_bf16 v[108:111], v[156:159], v[54:57], 0\n"
        "s_cmp_ge_u32 s37, 49\n"
        "s_cselect_b32 s69, 49, 0\n"
        "v_mfma_f32_16x16x32_bf16 v[108:111], v[152:155], v[48:51], v[108:111]\n"
        "s_sub_u32 s37, s37, s69\n"
        "s_sub_u32 s69, s37, 4\n"
        "s_cmp_lt_u32 s69, 4\n"
        "v_mfma_f32_16x16x32_bf16 v[108:111], v[148:151], v[42:45], v[108:111]\n"
        "s_subb_u32 s36, s36, -4\n"
        "s_cmp_ge_u32 s36, 6\n"
        "v_mfma_f32_16x16x32_bf16 v[76:79], v[160:163], v[42:45], 0\n"
        "s_cselect_b32 s69, 6, 0\n"
        "s_addc_u32 s34, s34, 0\n"
        "v_mfma_f32_16x16x32_bf16 v[76:79], v[164:167], v[48:51], v[76:79]\n"
        "s_sub_u32 s36, s36, s69\n"
        "s_cmp_ge_u32 s34, s74\n"
        "v_mfma_f32_16x16x32_bf16 v[76:79], v[168:171], v[54:57], v[76:79]\n"
        "s_cbranch_scc1 L10_%=\n"
        "L11_%=:\n"
        "s_mul_i32 s38, s34, 256\n"
        "v_mfma_f32_16x16x32_bf16 v[80:83], v[180:183], v[54:57], 0\n"
        "s_add_u32 s38, s38, s16\n"
        "s_cmp_ge_u32 s34, 20\n"
        "s_cselect_b32 s38, s22, s38\n"
        "v_mfma_f32_16x16x32_bf16 v[80:83], v[176:179], v[48:51], v[80:83]\n"
        "s_cmp_ge_u32 s34, 21\n"
        "s_cselect_b32 s38, s23, s38\n"
        "v_mfma_f32_16x16x32_bf16 v[80:83], v[172:175], v[42:45], v[80:83]\n"
        "s_mul_hi_u32 s70, s38, 58835169\n"
        "s_mul_i32 s38, s38, 1344\n"
        "v_mfma_f32_16x16x32_bf16 v[20:23], v[16:19], v[60:63], 0\n"
        "s_mul_i32 s70, s70, 588672\n"
        "s_add_u32 s38, s38, s70\n"
        "v_mfma_f32_16x16x32_bf16 v[20:23], v[16:19], v[66:69], v[20:23]\n"
        "s_mul_i32 s69, s36, 98304\n"
        "s_add_u32 s38, s38, s69\n"
        "s_mov_b32 s35, s36\n"
        "v_mfma_f32_16x16x32_bf16 v[20:23], v[16:19], v[72:75], v[20:23]\n"
        "s_mul_i32 s69, s36, -12\n"
        "v_mov_b64 v[30:31], 0\n"
        "v_mov_b64 v[32:33], 0\n"
        "v_mfma_f32_16x16x32_bf16 v[24:27], v[16:19], v[42:45], 0\n"
        "s_add_u32 s69, s69, 60\n"
        "s_lshl_b64 s[54:55], -1, s69\n"
        "s_lshl_b64 s[56:57], s[54:55], 24\n"
        "v_mfma_f32_16x16x32_bf16 v[24:27], v[16:19], v[48:51], v[24:27]\n"
        "s_ashr_i64 s[60:61], s[54:55], 8\n"
        "s_mov_b32 s58, s61\n"
        "v_mfma_f32_16x16x32_bf16 v[24:27], v[16:19], v[54:57], v[24:27]\n"
        "s_mov_b32 s59, s56\n"
        "v_cndmask_b32_e64 v15, v4, v7, s[56:57]\n"
        "v_smfmac_f32_16x16x64_bf16 v[30:33], v[42:45], v[34:41], v10\n"
        "v_cndmask_b32_e64 v100, v5, v8, s[58:59]\n"
        "v_smfmac_f32_16x16x64_bf16 v[30:33], v[48:51], v[104:111], v10\n"
        "v_cndmask_b32_e64 v103, v6, v9, s[60:61]\n"
        "v_smfmac_f32_16x16x64_bf16 v[30:33], v[54:57], v[76:83], v10\n"
        "s_cmp_eq_u32 s37, 3\n"
        "s_cbranch_scc1 L12_%=\n"
        "L13_%=:\n"
        "v_cndmask_b32_dpp v110, v24, v20, vcc row_ror:4 row_mask:0xf bank_mask:0xf\n"
        "v_mfma_f32_16x16x32_bf16 v[36:39], v[54:57], v[192:195], 0\n"
        "s_cmp_lt_u32 s34, s14\n"
        "v_mul_f32 v111, s28, v110\n"
        "v_fma_f32 v111, -v111, v110, v93\n"
        "v_mfma_f32_16x16x32_bf16 v[36:39], v[48:51], v[188:191], v[36:39]\n"
        "s_cselect_b32 s38, s38, 0x80000000\n"
        "s_add_u32 s39, s38, 100270464\n"
        "v_add_f32_e64 v111, s29, |v111|\n"
        "v_mfma_f32_16x16x32_bf16 v[36:39], v[42:45], v[184:187], v[36:39]\n"
        "v_rsq_f32 v84, v111\n"
        "v_cndmask_b32_dpp v104, v30, v96, vcc row_ror:4 row_mask:0xf bank_mask:0xf\n"
        "v_mfma_f32_16x16x32_bf16 v[24:27], v[72:75], v[192:195], 0\n"
        "v_cndmask_b32_e64 v84, 0, v84, s[46:47]\n"
        "v_cndmask_b32_dpp v105, v31, v97, vcc row_ror:4 row_mask:0xf bank_mask:0xf\n"
        "v_mfma_f32_16x16x32_bf16 v[24:27], v[66:69], v[188:191], v[24:27]\n"
        "v_cndmask_b32_dpp v85, v32, v98, vcc row_ror:4 row_mask:0xf bank_mask:0xf\n"
        "v_mul_f32_dpp v104, v84, v104 quad_perm:[0,0,0,0] row_mask:0xf bank_mask:0xf\n"
        "v_mfma_f32_16x16x32_bf16 v[24:27], v[60:63], v[184:187], v[24:27]\n"
        "v_mul_f32_dpp v105, v84, v105 quad_perm:[1,1,1,1] row_mask:0xf bank_mask:0xf\n"
        "v_mul_f32_dpp v85, v84, v85 quad_perm:[2,2,2,2] row_mask:0xf bank_mask:0xf\n"
        "v_mfma_f32_16x16x32_bf16 v[20:23], v[60:63], v[220:223], 0\n"
        "v_cndmask_b32_dpp v106, v33, v99, vcc row_ror:4 row_mask:0xf bank_mask:0xf\n"
        "v_max3_f32 v107, v104, v105, v85\n"
        "v_mfma_f32_16x16x32_bf16 v[20:23], v[66:69], v[224:227], v[20:23]\n"
        "v_mul_f32_dpp v106, v84, v106 quad_perm:[3,3,3,3] row_mask:0xf bank_mask:0xf\n"
        "v_max_f32 v107, v107, v106\n"
        "v_mfma_f32_16x16x32_bf16 v[20:23], v[72:75], v[228:231], v[20:23]\n"
        "v_mul_f32 v107, v107, v84\n"
        "v_fma_f32 v104, v104, v84, -v107\n"
        "v_fma_f32 v105, v105, v84, -v107\n"
        "v_mfma_f32_16x16x32_bf16 v[32:35], v[42:45], v[220:223], 0\n"
        "v_fma_f32 v85, v85, v84, -v107\n"
        "v_fma_f32 v106, v106, v84, -v107\n"
        "v_mfma_f32_16x16x32_bf16 v[32:35], v[48:51], v[224:227], v[32:35]\n"
        "v_exp_f32 v104, v104\n"
        "v_mfma_f32_16x16x32_bf16 v[32:35], v[54:57], v[228:231], v[32:35]\n"
        "v_exp_f32 v105, v105\n"
        "v_mfma_f32_16x16x32_bf16 v[108:111], v[54:57], v[204:207], 0\n"
        "v_exp_f32 v85, v85\n"
        "v_mfma_f32_16x16x32_bf16 v[108:111], v[48:51], v[200:203], v[108:111]\n"
        "v_exp_f32 v106, v106\n"
        "v_mfma_f32_16x16x32_bf16 v[108:111], v[42:45], v[196:199], v[108:111]\n"
        "v_add_f32 v107, v104, v105\n"
        "v_add_f32 v101, v85, v106\n"
        "v_add_f32 v107, v107, v101\n"
        "v_mfma_f32_16x16x32_bf16 v[80:83], v[72:75], v[204:207], 0\n"
        "v_rcp_f32 v107, v107\n"
        "v_mfma_f32_16x16x32_bf16 v[80:83], v[66:69], v[200:203], v[80:83]\n"
        "v_mul_f32_dpp v104, v84, v104 quad_perm:[0,0,0,0] row_mask:0xf bank_mask:0xf\n"
        "v_mul_f32_dpp v105, v84, v105 quad_perm:[1,1,1,1] row_mask:0xf bank_mask:0xf\n"
        "v_mfma_f32_16x16x32_bf16 v[80:83], v[60:63], v[196:199], v[80:83]\n"
        "v_mul_f32_dpp v85, v84, v85 quad_perm:[2,2,2,2] row_mask:0xf bank_mask:0xf\n"
        "v_mul_f32_dpp v106, v84, v106 quad_perm:[3,3,3,3] row_mask:0xf bank_mask:0xf\n"
        "v_mul_f32 v104, v104, v107\n"
        "v_mfma_f32_16x16x32_bf16 v[76:79], v[60:63], v[232:235], 0\n"
        "v_mul_f32 v105, v105, v107\n"
        "v_mul_f32 v85, v85, v107\n"
        "v_mfma_f32_16x16x32_bf16 v[76:79], v[66:69], v[236:239], v[76:79]\n"
        "v_mul_f32 v106, v106, v107\n"
        "v_cvt_pk_bf16_f32 v99, v104, v105\n"
        "v_mfma_f32_16x16x32_bf16 v[76:79], v[72:75], v[240:243], v[76:79]\n"
        "v_cvt_pk_bf16_f32 v101, v85, v106\n"
        "v_and_b32 v58, v1, v99\n"
        "v_and_b32 v59, v1, v101\n"
        "v_mfma_f32_16x16x32_bf16 v[104:107], v[42:45], v[232:235], 0\n"
        "v_cvt_pk_bf16_f32 v92, v24, v25\n"
        "v_cvt_pk_bf16_f32 v93, v26, v27\n"
        "v_mfma_f32_16x16x32_bf16 v[104:107], v[48:51], v[236:239], v[104:107]\n"
        "v_and_b32_dpp v40, v99, v1 row_ror:12 row_mask:0xf bank_mask:0xf\n"
        "v_and_b32_dpp v41, v101, v1 row_ror:12 row_mask:0xf bank_mask:0xf\n"
        "v_mfma_f32_16x16x32_bf16 v[104:107], v[54:57], v[240:243], v[104:107]\n"
        "v_cvt_pk_bf16_f32 v84, v36, v37\n"
        "v_cvt_pk_bf16_f32 v85, v38, v39\n"
        "v_mov_b64 v[70:71], v[58:59]\n"
        "v_mfma_f32_16x16x32_bf16 v[36:39], v[92:95], v[58:61], 0\n"
        "v_cvt_pk_bf16_f32 v96, v20, v21\n"
        "v_cvt_pk_bf16_f32 v97, v22, v23\n"
        "v_mfma_f32_16x16x32_bf16 v[22:25], v[84:87], v[40:43], 0\n"
        "v_mov_b64 v[52:53], v[40:41]\n"
        "v_cvt_pk_bf16_f32 v88, v32, v33\n"
        "v_cvt_pk_bf16_f32 v89, v34, v35\n"
        "v_mfma_f32_16x16x32_bf16 v[32:35], v[94:97], v[68:71], 0\n"
        "v_mov_b64 v[64:65], v[58:59]\n"
        "v_cvt_pk_bf16_f32 v30, v80, v81\n"
        "v_mfma_f32_16x16x32_bf16 v[96:99], v[86:89], v[50:53], 0\n"
        "v_cvt_pk_bf16_f32 v31, v82, v83\n"
        "v_mov_b64 v[46:47], v[40:41]\n"
        "v_mfma_f32_16x16x32_bf16 v[80:83], v[54:57], v[216:219], 0\n"
        "v_cvt_pk_bf16_f32 v92, v108, v109\n"
        "v_cvt_pk_bf16_f32 v93, v110, v111\n"
        "v_mfma_f32_16x16x32_bf16 v[80:83], v[48:51], v[212:215], v[80:83]\n"
        "v_cvt_pk_bf16_f32 v26, v76, v77\n"
        "v_cvt_pk_bf16_f32 v27, v78, v79\n"
        "v_mfma_f32_16x16x32_bf16 v[80:83], v[42:45], v[208:211], v[80:83]\n"
        "v_cvt_pk_bf16_f32 v88, v104, v105\n"
        "v_cvt_pk_bf16_f32 v89, v106, v107\n"
        "v_mfma_f32_16x16x32_bf16 v[108:111], v[72:75], v[216:219], 0\n"
        "v_mov_b64 v[76:77], v[58:59]\n"
        "v_mov_b64 v[58:59], v[40:41]\n"
        "v_mfma_f32_16x16x32_bf16 v[108:111], v[66:69], v[212:215], v[108:111]\n"
        "v_cvt_pk_bf16_f32 v104, v36, v37\n"
        "v_cvt_pk_bf16_f32 v105, v38, v39\n"
        "v_mfma_f32_16x16x32_bf16 v[108:111], v[60:63], v[208:211], v[108:111]\n"
        "v_cvt_pk_bf16_f32 v84, v80, v81\n"
        "v_cvt_pk_bf16_f32 v85, v82, v83\n"
        "v_mfma_f32_16x16x32_bf16 v[80:83], v[28:31], v[62:65], 0\n"
        "v_cvt_pk_bf16_f32 v106, v32, v33\n"
        "v_cvt_pk_bf16_f32 v107, v34, v35\n"
        "v_mfma_f32_16x16x32_bf16 v[32:35], v[90:93], v[44:47], 0\n"
        "v_cvt_pk_bf16_f32 v20, v22, v23\n"
        "v_cvt_pk_bf16_f32 v21, v24, v25\n"
        "v_mfma_f32_16x16x32_bf16 v[38:41], v[60:63], v[244:247], 0\n"
        "v_cvt_pk_bf16_f32 v92, v108, v109\n"
        "v_cvt_pk_bf16_f32 v93, v110, v111\n"
        "v_mfma_f32_16x16x32_bf16 v[38:41], v[66:69], v[248:251], v[38:41]\n"
        "v_cvt_pk_bf16_f32 v108, v80, v81\n"
        "v_cvt_pk_bf16_f32 v109, v82, v83\n"
        "v_mfma_f32_16x16x32_bf16 v[38:41], v[72:75], v[252:255], v[38:41]\n"
        "v_cvt_pk_bf16_f32 v22, v96, v97\n"
        "v_cvt_pk_bf16_f32 v23, v98, v99\n"
        "v_mfma_f32_16x16x32_bf16 v[80:83], v[42:45], v[244:247], 0\n"
        "v_cvt_pk_bf16_f32 v24, v32, v33\n"
        "v_cvt_pk_bf16_f32 v25, v34, v35\n"
        "v_mfma_f32_16x16x32_bf16 v[80:83], v[48:51], v[248:251], v[80:83]\n"
        "v_mfma_f32_16x16x32_bf16 v[80:83], v[54:57], v[252:255], v[80:83]\n"
        "v_mfma_f32_16x16x32_bf16 v[34:37], v[26:29], v[70:73], 0\n"
        "v_cvt_pk_bf16_f32 v96, v38, v39\n"
        "v_cvt_pk_bf16_f32 v97, v40, v41\n"
        "v_mfma_f32_16x16x32_bf16 v[38:41], v[88:91], v[52:55], 0\n"
        "v_mfma_f32_16x16x32_bf16 v[30:33], v[92:95], v[64:67], 0\n"
        "s_waitcnt vmcnt(0)\n"
        "ds_read_b128 v[60:63], v0\n"
        "ds_read_b128 v[42:45], v0 offset:768\n"
        "ds_read_b128 v[66:69], v0 offset:64\n"
        "v_cvt_pk_bf16_f32 v88, v80, v81\n"
        "v_mfma_f32_16x16x32_bf16 v[78:81], v[84:87], v[46:49], 0\n"
        "s_add_u32 s73, s73, 1\n"
        "ds_read_b128 v[48:51], v0 offset:832\n"
        "v_cvt_pk_bf16_f32 v89, v82, v83\n"
        "v_cvt_pk_bf16_f32 v110, v34, v35\n"
        "v_cvt_pk_bf16_f32 v111, v36, v37\n"
        "v_mfma_f32_16x16x32_bf16 v[82:85], v[94:97], v[74:77], 0\n"
        "s_cmp_lt_u32 s73, 4\n"
        "ds_read_b128 v[72:75], v0 offset:128\n"
        "v_cvt_pk_bf16_f32 v96, v30, v31\n"
        "v_cvt_pk_bf16_f32 v97, v32, v33\n"
        "v_cvt_pk_bf16_f32 v26, v38, v39\n"
        "v_mfma_f32_16x16x32_bf16 v[34:37], v[86:89], v[56:59], 0\n"
        "s_cbranch_scc1 L19_%=\n"
        "L20_%=:\n"
        "ds_read_b128 v[54:57], v0 offset:896\n"
        "v_cvt_pk_bf16_f32 v98, v82, v83\n"
        "v_cvt_pk_bf16_f32 v99, v84, v85\n"
        "v_cvt_pk_bf16_f32 v27, v40, v41\n"
        "v_mfma_f32_16x16x32_bf16 v[82:85], v[104:107], v[104:107], 0\n"
        "v_cvt_pk_bf16_f32 v30, v78, v79\n"
        "v_cvt_pk_bf16_f32 v31, v80, v81\n"
        "v_mfma_f32_16x16x32_bf16 v[82:85], v[108:111], v[108:111], v[82:85]\n"
        "v_cvt_pk_bf16_f32 v32, v34, v35\n"
        "v_cvt_pk_bf16_f32 v33, v36, v37\n"
        "v_mfma_f32_16x16x32_bf16 v[82:85], v[96:99], v[96:99], v[82:85]\n"
        "v_mfma_f32_16x16x32_bf16 v[76:79], v[20:23], v[20:23], 0\n"
        "v_mfma_f32_16x16x32_bf16 v[76:79], v[24:27], v[24:27], v[76:79]\n"
        "v_mfma_f32_16x16x32_bf16 v[76:79], v[30:33], v[30:33], v[76:79]\n"
        "s_cmp_lt_u32 s30, s14\n"
        "s_cbranch_scc1 PTOP_%=\n"
        "s_waitcnt lgkmcnt(0)\n"
        "s_nop 15\n"
        "L21_%=:\n"
        "ds_read_b32 v101, v102 offset:8\n"
        "s_waitcnt lgkmcnt(0)\n"
        "v_readfirstlane_b32 s40, v101\n"
        "s_nop 3\n"
        "s_cmp_ge_i32 s40, s68\n"
        "s_cbranch_scc1 L22_%=\n"
        "s_sleep 1\n"
        "s_sub_u32 s41, s41, 1\n"
        "s_cbranch_scc0 L21_%=\n"
        "s_endpgm\n"
        "L22_%=:\n"
        "v_cndmask_b32_e64 v101, v82, v83, s[48:49]\n"
        "v_cndmask_b32_e64 v93, v76, v77, s[48:49]\n"
        "v_cndmask_b32_e64 v101, v101, v84, s[50:51]\n"
        "v_cndmask_b32_e64 v93, v93, v78, s[50:51]\n"
        "v_cndmask_b32_e64 v101, v101, v85, s[52:53]\n"
        "v_cndmask_b32_e64 v93, v93, v79, s[52:53]\n"
        "ds_write_b128 v11, v[104:107]\n"
        "ds_write_b128 v11, v[20:23] offset:832\n"
        "v_cndmask_b32_dpp v101, v93, v101, vcc row_ror:4 row_mask:0xf bank_mask:0xf\n"
        "ds_write_b128 v11, v[108:111] offset:64\n"
        "ds_write_b128 v11, v[24:27] offset:896\n"
        "ds_write_b128 v11, v[96:99] offset:128\n"
        "ds_write_b128 v11, v[30:33] offset:960\n"
        "s_mov_b64 exec, s[46:47]\n"
        "ds_write_b32 v12, v101\n"
        "s_mov_b64 exec, -1\n"
        "s_mov_b64 exec, 2\n"
        "ds_inc_u32 v102, v0 offset:0\n"
        "s_mov_b64 exec, -1\n"
        "s_or_b32 s40, s18, s2\n"
        "s_cbranch_scc1 L31_%=\n"
        "L30_%=:\n"
        "s_mov_b64 exec, 1\n"
        "v_mov_b32 v20, 0\n"
        "v_mov_b32 v21, 1\n"
        "global_atomic_add v22, v20, v21, s[24:25] offset:256 sc0\n"
        "s_mov_b64 exec, -1\n"
        "s_mov_b64 exec, 1\n"
        "v_mov_b32 v20, 0\n"
        "v_mov_b32 v21, 0\n"
        "global_atomic_add v22, v20, v21, s[24:25] offset:0 sc0\n"
        "s_mov_b64 exec, -1\n"
        "s_waitcnt vmcnt(0)\n"
        "v_readfirstlane_b32 s40, v22\n"
        "s_nop 3\n"
        "s_cmp_ge_u32 s40, 512\n"
        "s_cbranch_scc1 L31_%=\n"
        "s_sleep 8\n"
        "s_sub_u32 s41, s41, 64\n"
        "s_cbranch_scc0 L30_%=\n"
        "L31_%=:\n"
        "s_endpgm\n"
        "L3_%=:\n"
        "s_nop 7\n"
        "L1_%=:\n"
        "ds_read_b32 v101, v102 offset:8\n"
        "s_waitcnt lgkmcnt(0)\n"
        "v_readfirstlane_b32 s40, v101\n"
        "s_nop 3\n"
        "s_cmp_ge_i32 s40, s68\n"
        "s_cbranch_scc1 L2_%=\n"
        "s_sleep 1\n"
        "s_sub_u32 s41, s41, 1\n"
        "s_cbranch_scc0 L1_%=\n"
        "s_endpgm\n"
        "L2_%=:\n"
        "s_nop 1\n"
        "s_branch L4_%=\n"
        "L8_%=:\n"
        "s_nop 7\n"
        "ds_read_u16 v11, v13 offset:19564\n"
        "ds_read_u16 v12, v13 offset:19566\n"
        "s_cmp_ge_u32 s30, 10\n"
        "s_cselect_b32 s69, 10, 0\n"
        "s_cmp_ge_u32 s30, 20\n"
        "s_cselect_b32 s69, 20, s69\n"
        "s_sub_u32 s69, s30, s69\n"
        "s_mul_i32 s69, s69, 832\n"
        "s_waitcnt lgkmcnt(0)\n"
        "v_add_u32 v11, s69, v11\n"
        "v_subrev_u32 v101, 8320, v11\n"
        "v_min_u32 v11, v11, v101\n"
        "v_add_u32 v11, 101856, v11\n"
        "v_add_u32 v12, s69, v12\n"
        "v_subrev_u32 v101, 8320, v12\n"
        "v_min_u32 v12, v12, v101\n"
        "v_add_u32 v12, 101856, v12\n"
        "s_mov_b32 s69, 21660\n"
        "v_writelane_b32 v102, s69, 1\n"
        "s_nop 1\n"
        "s_branch L9_%=\n"
        "L10_%=:\n"
        "s_nop 7\n"
        "v_mov_b32 v101, 21600\n"
        "L6_%=:\n"
        "ds_read_b32 v92, v101 offset:64\n"
        "s_waitcnt lgkmcnt(0)\n"
        "v_readfirstlane_b32 s40, v92\n"
        "s_nop 3\n"
        "s_cmp_ge_i32 s40, 1\n"
        "s_cbranch_scc1 L7_%=\n"
        "s_sleep 1\n"
        "s_sub_u32 s41, s41, 1\n"
        "s_cbranch_scc0 L6_%=\n"
        "s_endpgm\n"
        "L7_%=:\n"
        "v_mbcnt_lo_u32_b32 v92, -1, 0\n"
        "v_lshl_add_u32 v101, v92, 2, v101\n"
        "ds_read_b32 v92, v101 offset:68\n"
        "s_mov_b32 s14, s40\n"
        "s_mov_b32 s74, -1\n"
        "s_waitcnt lgkmcnt(0)\n"
        "v_readlane_b32 s22, v92, 0\n"
        "v_readlane_b32 s23, v92, 1\n"
        "s_nop 3\n"
        "s_nop 1\n"
        "s_branch L11_%=\n"
        "L12_%=:\n"
        "s_nop 7\n"
        "ds_read_b32 v15, v13 offset:19552\n"
        "ds_read_b32 v100, v13 offset:19556\n"
        "ds_read_b32 v103, v13 offset:19560\n"
        "s_mov_b32 s35, 6\n"
        "s_mov_b32 s38, 0\n"
        "s_mov_b32 s69, 1344\n"
        "s_waitcnt lgkmcnt(0)\n"
        "v_lshrrev_b32 v101, 28, v15\n"
        "v_add_u32 v101, s34, v101\n"
        "v_and_b32 v15, 0x0fffffff, v15\n"
        "v_mul_u32_u24 v92, 256, v101\n"
        "v_add_u32 v92, s16, v92\n"
        "v_cmpx_le_u32 vcc, 20, v101\n"
        "v_mov_b32 v92, s22\n"
        "s_mov_b64 exec, -1\n"
        "v_cmpx_le_u32 vcc, 21, v101\n"
        "v_mov_b32 v92, s23\n"
        "s_mov_b64 exec, -1\n"
        "v_mad_u32_u24 v15, v92, s69, v15\n"
        "v_mul_u32_u24 v92, 57457, v92\n"
        "v_lshrrev_b32 v92, 22, v92\n"
        "v_mul_u32_u24 v92, 588672, v92\n"
        "v_add_u32 v15, v15, v92\n"
        "v_lshrrev_b32 v101, 28, v100\n"
        "v_add_u32 v101, s34, v101\n"
        "v_and_b32 v100, 0x0fffffff, v100\n"
        "v_mul_u32_u24 v92, 256, v101\n"
        "v_add_u32 v92, s16, v92\n"
        "v_cmpx_le_u32 vcc, 20, v101\n"
        "v_mov_b32 v92, s22\n"
        "s_mov_b64 exec, -1\n"
        "v_cmpx_le_u32 vcc, 21, v101\n"
        "v_mov_b32 v92, s23\n"
        "s_mov_b64 exec, -1\n"
        "v_mad_u32_u24 v100, v92, s69, v100\n"
        "v_mul_u32_u24 v92, 57457, v92\n"
        "v_lshrrev_b32 v92, 22, v92\n"
        "v_mul_u32_u24 v92, 588672, v92\n"
        "v_add_u32 v100, v100, v92\n"
        "v_lshrrev_b32 v101, 28, v103\n"
        "v_add_u32 v101, s34, v101\n"
        "v_and_b32 v103, 0x0fffffff, v103\n"
        "v_mul_u32_u24 v92, 256, v101\n"
        "v_add_u32 v92, s16, v92\n"
        "v_cmpx_le_u32 vcc, 20, v101\n"
        "v_mov_b32 v92, s22\n"
        "s_mov_b64 exec, -1\n"
        "v_cmpx_le_u32 vcc, 21, v101\n"
        "v_mov_b32 v92, s23\n"
        "s_mov_b64 exec, -1\n"
        "v_mad_u32_u24 v103, v92, s69, v103\n"
        "v_mul_u32_u24 v92, 57457, v92\n"
        "v_lshrrev_b32 v92, 22, v92\n"
        "v_mul_u32_u24 v92, 588672, v92\n"
        "v_add_u32 v103, v103, v92\n"
        "s_nop 4\n"
        "s_mov_b64 vcc, s[44:45]\n"
        "s_nop 1\n"
        "s_branch L13_%=\n"
        "L19_%=:\n"
        "s_nop 7\n"
        "v_readfirstlane_b32 s62, v14\n"
        "s_cmp_eq_u32 s73, 1\n"
        "s_cbranch_scc1 L14_%=\n"
        "s_cmp_ge_u32 s73, 2\n"
        "s_cbranch_scc1 L15_%=\n"
        "s_mov_b32 s27, 20\n"
        "s_cmp_lg_u32 s2, 0\n"
        "s_cbranch_scc1 L18_%=\n"
        "s_not_b32 s26, s26\n"
        "s_mov_b64 exec, 1\n"
        "v_mov_b32 v101, 0\n"
        "v_mov_b32 v92, 1\n"
        "global_atomic_add v14, v101, v92, s[24:25] offset:256 sc0\n"
        "s_mov_b64 exec, -1\n"
        "s_branch L16_%=\n"
        "L18_%=:\n"
        "s_mov_b64 exec, 1\n"
        "v_mov_b32 v101, 0\n"
        "global_load_dword v14, v101, s[24:25] offset:256 sc1\n"
        "s_mov_b64 exec, -1\n"
        "s_branch L16_%=\n"
        "L14_%=:\n"
        "s_cmp_lg_u32 s62, s26\n"
        "s_cbranch_scc1 L17_%=\n"
        "s_sleep 8\n"
        "s_mov_b64 exec, 1\n"
        "v_mov_b32 v101, 0\n"
        "global_load_dword v14, v101, s[24:25] offset:256 sc1\n"
        "s_mov_b64 exec, -1\n"
        "s_waitcnt vmcnt(0)\n"
        "v_readfirstlane_b32 s62, v14\n"
        "s_sub_u32 s41, s41, 64\n"
        "s_cbranch_scc0 L14_%=\n"
        "s_endpgm\n"
        "L15_%=:\n"
        "s_cmp_lt_u32 s62, 209\n"
        "s_addc_u32 s27, s27, 0\n"
        "s_min_u32 s62, s62, 208\n"
        "s_add_u32 s62, s62, 5120\n"
        "s_lshl2_add_u32 s64, s73, 21600\n"
        "v_mov_b32 v101, s64\n"
        "v_mov_b32 v92, s62\n"
        "ds_write_b32 v101, v92 offset:60\n"
        "s_waitcnt lgkmcnt(0)\n"
        "s_cmp_lt_u32 s73, 3\n"
        "s_cbranch_scc1 L17_%=\n"
        "v_mov_b32 v92, s27\n"
        "ds_write_b32 v101, v92 offset:52\n"
        "s_waitcnt lgkmcnt(0)\n"
        "s_branch L16_%=\n"
        "L17_%=:\n"
        "s_mov_b64 exec, 1\n"
        "v_mov_b32 v101, 0\n"
        "v_mov_b32 v92, 1\n"
        "global_atomic_add v14, v101, v92, s[24:25] offset:0 sc0\n"
        "s_mov_b64 exec, -1\n"
        "L16_%=:\n"
        "s_nop 1\n"
        "s_branch L20_%=\n"
        "F8_%=:\n"
        "s_nop 7\n"
        "ds_read_u16 v11, v13 offset:19564\n"
        "ds_read_u16 v12, v13 offset:19566\n"
        "s_cmp_ge_u32 s30, 10\n"
        "s_cselect_b32 s69, 10, 0\n"
        "s_cmp_ge_u32 s30, 20\n"
        "s_cselect_b32 s69, 20, s69\n"
        "s_sub_u32 s69, s30, s69\n"
        "s_mul_i32 s69, s69, 832\n"
        "s_waitcnt lgkmcnt(0)\n"
        "v_add_u32 v11, s69, v11\n"
        "v_subrev_u32 v101, 8320, v11\n"
        "v_min_u32 v11, v11, v101\n"
        "v_add_u32 v11, 101856, v11\n"
        "v_add_u32 v12, s69, v12\n"
        "v_subrev_u32 v101, 8320, v12\n"
        "v_min_u32 v12, v12, v101\n"
        "v_add_u32 v12, 101856, v12\n"
        "s_mov_b32 s69, 21660\n"
        "v_writelane_b32 v102, s69, 1\n"
        "s_nop 1\n"
        "s_branch F9_%=\n"
        "F10_%=:\n"
        "s_nop 7\n"
        "v_mov_b32 v101, 21600\n"
        "F6_%=:\n"
        "ds_read_b32 v92, v101 offset:64\n"
        "s_waitcnt lgkmcnt(0)\n"
        "v_readfirstlane_b32 s40, v92\n"
        "s_nop 3\n"
        "s_cmp_ge_i32 s40, 1\n"
        "s_cbranch_scc1 F7_%=\n"
        "s_sleep 1\n"
        "s_sub_u32 s41, s41, 1\n"
        "s_cbranch_scc0 F6_%=\n"
        "s_endpgm\n"
        "F7_%=:\n"
        "v_mbcnt_lo_u32_b32 v92, -1, 0\n"
        "v_lshl_add_u32 v101, v92, 2, v101\n"
        "ds_read_b32 v92, v101 offset:68\n"
        "s_mov_b32 s14, s40\n"
        "s_mov_b32 s74, -1\n"
        "s_waitcnt lgkmcnt(0)\n"
        "v_readlane_b32 s22, v92, 0\n"
        "v_readlane_b32 s23, v92, 1\n"
        "s_nop 3\n"
        "s_nop 1\n"
        "s_branch F11_%=\n"
        "F12_%=:\n"
        "s_nop 7\n"
        "ds_read_b32 v15, v13 offset:19552\n"
        "ds_read_b32 v100, v13 offset:19556\n"
        "ds_read_b32 v103, v13 offset:19560\n"
        "s_mov_b32 s35, 6\n"
        "s_mov_b32 s38, 0\n"
        "s_mov_b32 s69, 1344\n"
        "s_waitcnt lgkmcnt(0)\n"
        "v_lshrrev_b32 v101, 28, v15\n"
        "v_add_u32 v101, s34, v101\n"
        "v_and_b32 v15, 0x0fffffff, v15\n"
        "v_mul_u32_u24 v92, 256, v101\n"
        "v_add_u32 v92, s16, v92\n"
        "v_cmpx_le_u32 vcc, 20, v101\n"
        "v_mov_b32 v92, s22\n"
        "s_mov_b64 exec, -1\n"
        "v_cmpx_le_u32 vcc, 21, v101\n"
        "v_mov_b32 v92, s23\n"
        "s_mov_b64 exec, -1\n"
        "v_mad_u32_u24 v15, v92, s69, v15\n"
        "v_mul_u32_u24 v92, 57457, v92\n"
        "v_lshrrev_b32 v92, 22, v92\n"
        "v_mul_u32_u24 v92, 588672, v92\n"
        "v_add_u32 v15, v15, v92\n"
        "v_lshrrev_b32 v101, 28, v100\n"
        "v_add_u32 v101, s34, v101\n"
        "v_and_b32 v100, 0x0fffffff, v100\n"
        "v_mul_u32_u24 v92, 256, v101\n"
        "v_add_u32 v92, s16, v92\n"
        "v_cmpx_le_u32 vcc, 20, v101\n"
        "v_mov_b32 v92, s22\n"
        "s_mov_b64 exec, -1\n"
        "v_cmpx_le_u32 vcc, 21, v101\n"
        "v_mov_b32 v92, s23\n"
        "s_mov_b64 exec, -1\n"
        "v_mad_u32_u24 v100, v92, s69, v100\n"
        "v_mul_u32_u24 v92, 57457, v92\n"
        "v_lshrrev_b32 v92, 22, v92\n"
        "v_mul_u32_u24 v92, 588672, v92\n"
        "v_add_u32 v100, v100, v92\n"
        "v_lshrrev_b32 v101, 28, v103\n"
        "v_add_u32 v101, s34, v101\n"
        "v_and_b32 v103, 0x0fffffff, v103\n"
        "v_mul_u32_u24 v92, 256, v101\n"
        "v_add_u32 v92, s16, v92\n"
        "v_cmpx_le_u32 vcc, 20, v101\n"
        "v_mov_b32 v92, s22\n"
        "s_mov_b64 exec, -1\n"
        "v_cmpx_le_u32 vcc, 21, v101\n"
        "v_mov_b32 v92, s23\n"
        "s_mov_b64 exec, -1\n"
        "v_mad_u32_u24 v103, v92, s69, v103\n"
        "v_mul_u32_u24 v92, 57457, v92\n"
        "v_lshrrev_b32 v92, 22, v92\n"
        "v_mul_u32_u24 v92, 588672, v92\n"
        "v_add_u32 v103, v103, v92\n"
        "s_nop 4\n"
        "s_mov_b64 vcc, s[44:45]\n"
        "s_nop 1\n"
        "s_branch F13_%=\n"
        "F19_%=:\n"
        "s_nop 7\n"
        "v_readfirstlane_b32 s62, v14\n"
        "s_cmp_eq_u32 s73, 1\n"
        "s_cbranch_scc1 F14_%=\n"
        "s_cmp_ge_u32 s73, 2\n"
        "s_cbranch_scc1 F15_%=\n"
        "s_mov_b32 s27, 20\n"
        "s_cmp_lg_u32 s2, 0\n"
        "s_cbranch_scc1 F18_%=\n"
        "s_not_b32 s26, s26\n"
        "s_mov_b64 exec, 1\n"
        "v_mov_b32 v101, 0\n"
        "v_mov_b32 v92, 1\n"
        "global_atomic_add v14, v101, v92, s[24:25] offset:256 sc0\n"
        "s_mov_b64 exec, -1\n"
        "s_branch F16_%=\n"
        "F18_%=:\n"
        "s_mov_b64 exec, 1\n"
        "v_mov_b32 v101, 0\n"
        "global_load_dword v14, v101, s[24:25] offset:256 sc1\n"
        "s_mov_b64 exec, -1\n"
        "s_branch F16_%=\n"
        "F14_%=:\n"
        "s_cmp_lg_u32 s62, s26\n"
        "s_cbranch_scc1 F17_%=\n"
        "s_sleep 8\n"
        "s_mov_b64 exec, 1\n"
        "v_mov_b32 v101, 0\n"
        "global_load_dword v14, v101, s[24:25] offset:256 sc1\n"
        "s_mov_b64 exec, -1\n"
        "s_waitcnt vmcnt(0)\n"
        "v_readfirstlane_b32 s62, v14\n"
        "s_sub_u32 s41, s41, 64\n"
        "s_cbranch_scc0 F14_%=\n"
        "s_endpgm\n"
        "F15_%=:\n"
        "s_cmp_lt_u32 s62, 209\n"
        "s_addc_u32 s27, s27, 0\n"
        "s_min_u32 s62, s62, 208\n"
        "s_add_u32 s62, s62, 5120\n"
        "s_lshl2_add_u32 s64, s73, 21600\n"
        "v_mov_b32 v101, s64\n"
        "v_mov_b32 v92, s62\n"
        "ds_write_b32 v101, v92 offset:60\n"
        "s_waitcnt lgkmcnt(0)\n"
        "s_cmp_lt_u32 s73, 3\n"
        "s_cbranch_scc1 F17_%=\n"
        "v_mov_b32 v92, s27\n"
        "ds_write_b32 v101, v92 offset:52\n"
        "s_waitcnt lgkmcnt(0)\n"
        "s_branch F16_%=\n"
        "F17_%=:\n"
        "s_mov_b64 exec, 1\n"
        "v_mov_b32 v101, 0\n"
        "v_mov_b32 v92, 1\n"
        "global_atomic_add v14, v101, v92, s[24:25] offset:0 sc0\n"
        "s_mov_b64 exec, -1\n"
        "F16_%=:\n"
        "s_nop 1\n"
        "s_branch F20_%=\n"
        "CONS_%=:\n"
        "s_mov_b32 s41, 0x400000\n"
        "s_mov_b32 s48, 0xffffffff\n"
        "s_mov_b32 s49, 0xffff0000\n"
        "s_mov_b32 s50, 0x33333333\n"
        "s_mov_b32 s51, 0x33333333\n"
        "s_mov_b32 s52, 0x3c2aaaab\n"
        "s_mov_b32 s53, 0x3a7ba882\n"
        "s_mov_b32 s56, 0x0\n"
        "s_mov_b32 s57, 0xfeff0000\n"
        "s_mov_b32 s54, 0x11111111\n"
        "s_mov_b32 s55, 0x11111111\n"
        "s_mov_b32 s63, 0x07060302\n"
        "s_mov_b64 s[44:45], s[12:13]\n"
        "s_and_b32 s45, s45, 0xffff\n"
        "s_mov_b32 s47, 0x20000\n"
        "s_mov_b32 s70, 832\n"
        "s_mov_b32 s71, 17\n"
        "s_sub_u32 s69, s18, 7488\n"
        "s_mov_b32 s28, 0xffff0000\n"
        "s_mov_b32 s31, 0\n"
        "s_add_u32 s35, s15, s17\n"
        "s_mov_b32 s36, 0x20000000\n"
        "L58_%=:\n"
        "ds_read_b32 v251, v250 offset:52\n"
        "s_waitcnt lgkmcnt(0)\n"
        "v_readfirstlane_b32 s40, v251\n"
        "s_nop 3\n"
        "s_cmp_ge_i32 s40, 6\n"
        "s_cbranch_scc1 L59_%=\n"
        "s_nop 0\n"
        "s_sub_u32 s41, s41, 1\n"
        "s_cbranch_scc0 L58_%=\n"
        "s_endpgm\n"
        "L59_%=:\n"
        "s_waitcnt vmcnt(0)\n"
        "s_setprio 2\n"
        "v_add_u32 v24, 2, v1\n"
        "v_mov_b32 v25, 0x3f80\n"
        "s_mov_b32 exec_lo, 0\n"
        "s_mov_b32 exec_hi, 0x1000000\n"
        "ds_write_b16 v24, v25\n"
        "s_mov_b64 exec, -1\n"
        "v_add_u32 v24, 61792, v0\n"
        "ds_read_b128 v[168:171], v24 offset:0\n"
        "ds_read_b128 v[172:175], v24 offset:1024\n"
        "ds_read_b128 v[176:179], v24 offset:2048\n"
        "ds_read_b128 v[180:183], v24 offset:3072\n"
        "ds_read_b128 v[184:187], v24 offset:4096\n"
        "ds_read_b128 v[188:191], v24 offset:5120\n"
        "ds_read_b128 v[192:195], v24 offset:6144\n"
        "ds_read_b128 v[196:199], v24 offset:7168\n"
        "ds_read_b128 v[200:203], v24 offset:8192\n"
        "ds_read_b128 v[204:207], v24 offset:9216\n"
        "ds_read_b128 v[208:211], v24 offset:10240\n"
        "ds_read_b128 v[212:215], v24 offset:11264\n"
        "ds_read_b128 v[216:219], v24 offset:12288\n"
        "ds_read_b128 v[220:223], v24 offset:13312\n"
        "ds_read_b128 v[224:227], v24 offset:14336\n"
        "ds_read_b128 v[228:231], v24 offset:15360\n"
        "ds_read_b128 v[232:235], v24 offset:16384\n"
        "ds_read_b128 v[236:239], v24 offset:17408\n"
        "ds_read_b128 v[240:243], v0 offset:0\n"
        "ds_read_b128 v[244:247], v0 offset:1024\n"
        "ds_read_b128 v[248:251], v0 offset:2048\n"
        "ds_read_b128 v[252:255], v0 offset:3088\n"
        "v_mov_b32 v25, 21600\n"
        "s_waitcnt lgkmcnt(0)\n"
        "s_mov_b64 exec, 1\n"
        "ds_inc_u32 v25, v1 offset:12\n"
        "s_mov_b64 exec, -1\n"
        "s_call_b64 s[64:65], CGRQ_%=\n"
        "s_setprio 0\n"
        "L60_%=:\n"
        "ds_read_b32 v24, v0 offset:21600\n"
        "s_waitcnt lgkmcnt(0)\n"
        "v_readfirstlane_b32 s40, v24\n"
        "s_nop 3\n"
        "s_cmp_ge_i32 s40, 6\n"
        "s_cbranch_scc1 L61_%=\n"
        "s_nop 0\n"
        "s_sub_u32 s41, s41, 1\n"
        "s_cbranch_scc0 L60_%=\n"
        "s_endpgm\n"
        "L61_%=:\n"
        "ds_read_b128 v[124:127], v7 offset:0\n"
        "ds_read_b128 v[132:135], v7 offset:3328\n"
        "ds_read_b128 v[128:131], v7 offset:64\n"
        "ds_read_b128 v[136:139], v7 offset:3392\n"
        "ds_read_b128 v[160:163], v7 offset:128\n"
        "ds_read_b128 v[52:55], v7 offset:3456\n"
        "s_waitcnt lgkmcnt(0)\n"
        "s_branch CTOP_%=\n"
        ".p2align 6\n"
        "s_nop 0\n"
        "s_nop 0\n"
        "s_nop 0\n"
        "s_nop 0\n"
        "s_nop 0\n"
        "s_nop 0\n"
        "s_nop 0\n"
        "s_nop 0\n"
        "s_nop 0\n"
        "s_nop 0\n"
        "s_nop 0\n"
        "s_nop 0\n"
        "s_nop 0\n"
        "s_nop 0\n"
        "s_nop 0\n"
        "CFULL_%=:\n"
        "s_call_b64 s[64:65], CGRP_%=\n"
        "CTOP_%=:\n"
        "s_waitcnt lgkmcnt(5)\n"
        "v_mfma_f32_16x16x32_bf16 v[24:27], v[168:171], v[124:127], 0\n"
        "v_cvt_pk_bf16_f32 v156, v152, v56\n"
        "v_cvt_pk_bf16_f32 v157, v72, v20\n"
        "s_waitcnt lgkmcnt(3)\n"
        "v_mfma_f32_16x16x32_bf16 v[24:27], v[172:175], v[128:131], v[24:27]\n"
        "ds_read_b128 v[44:47], v16 offset:128\n"
        "v_cvt_pk_bf16_f32 v158, v84, v60\n"
        "s_waitcnt lgkmcnt(2)\n"
        "v_mfma_f32_16x16x32_bf16 v[24:27], v[176:179], v[160:163], v[24:27]\n"
        "ds_read_b128 v[48:51], v5\n"
        "v_cvt_pk_bf16_f32 v164, v153, v57\n"
        "v_mfma_f32_16x16x32_bf16 v[32:35], v[188:191], v[160:163], 0\n"
        "v_cvt_pk_bf16_f32 v165, v73, v21\n"
        "v_cvt_pk_bf16_f32 v166, v85, v61\n"
        "v_mfma_f32_16x16x32_bf16 v[32:35], v[184:187], v[128:131], v[32:35]\n"
        "v_cvt_pk_bf16_f32 v146, v154, v58\n"
        "v_cvt_pk_bf16_f32 v147, v74, v22\n"
        "v_mfma_f32_16x16x32_bf16 v[32:35], v[180:183], v[124:127], v[32:35]\n"
        "s_add_u32 s37, s36, 196224\n"
        "ds_read_b128 v[92:95], v7 offset:6656\n"
        "ds_read_b128 v[96:99], v7 offset:6720\n"
        "ds_read_b128 v[116:119], v7 offset:6784\n"
        "v_mfma_f32_16x16x32_bf16 v[76:79], v[192:195], v[124:127], 0\n"
        "v_perm_b32 v80, v25, v24, s63\n"
        "v_perm_b32 v81, v27, v26, s63\n"
        "v_mfma_f32_16x16x32_bf16 v[76:79], v[196:199], v[128:131], v[76:79]\n"
        "v_perm_b32 v82, v33, v32, s63\n"
        "v_perm_b32 v83, v35, v34, s63\n"
        "v_mfma_f32_16x16x32_bf16 v[76:79], v[200:203], v[160:163], v[76:79]\n"
        "buffer_store_dwordx3 v[156:158], v12, s[8:11], s37 offen offset:384 sc0 sc1 nt\n"
        "v_cvt_pk_bf16_f32 v148, v86, v62\n"
        "v_cvt_pk_bf16_f32 v150, v155, v59\n"
        "v_cvt_pk_bf16_f32 v151, v75, v23\n"
        "v_cvt_pk_bf16_f32 v152, v87, v63\n"
        "s_waitcnt lgkmcnt(3)\n"
        "v_mfma_f32_16x16x32_bf16 v[32:35], v[44:47], v[160:163], 0\n"
        "v_cvt_pk_bf16_f32 v72, v68, v36\n"
        "v_perm_b32 v84, v77, v76, s63\n"
        "v_smfmac_f32_16x16x64_bf16 v[32:35], v[48:51], v[124:131], v4\n"
        "s_add_u32 s37, s36, 196224\n"
        "v_perm_b32 v85, v79, v78, s63\n"
        "v_mfma_f32_16x16x32_bf16 v[24:27], v[44:47], v[52:55], 0\n"
        "v_cvt_pk_bf16_f32 v73, v40, v64\n"
        "v_cvt_pk_bf16_f32 v74, v104, v88\n"
        "v_smfmac_f32_16x16x64_bf16 v[24:27], v[48:51], v[132:139], v4\n"
        "s_waitcnt lgkmcnt(0)\n"
        "s_setprio 2\n"
        "v_cvt_pk_bf16_f32 v156, v69, v37\n"
        "v_cvt_pk_bf16_f32 v157, v41, v65\n"
        "v_mfma_f32_16x16x32_bf16 v[100:103], v[44:47], v[116:119], 0\n"
        "v_cvt_pk_bf16_f32 v158, v105, v89\n"
        "v_cvt_pk_bf16_f32 v76, v70, v38\n"
        "v_smfmac_f32_16x16x64_bf16 v[100:103], v[48:51], v[92:99], v4\n"
        "buffer_store_dwordx3 v[164:166], v13, s[8:11], s37 offen offset:1152 sc0 sc1 nt\n"
        "v_cvt_pk_bf16_f32 v77, v42, v66\n"
        "v_cvt_pk_bf16_f32 v78, v106, v90\n"
        "v_cvt_pk_bf16_f32 v88, v71, v39\n"
        "v_cvt_pk_bf16_f32 v89, v43, v67\n"
        "v_cvt_pk_bf16_f32 v90, v107, v91\n"
        "v_mfma_f32_16x16x32_bf16 v[20:23], v[212:215], v[160:163], 0\n"
        "s_add_u32 s37, s36, 294336\n"
        "ds_read_b64 v[154:155], v11 offset:192\n"
        "v_mfma_f32_16x16x32_bf16 v[20:23], v[208:211], v[128:131], v[20:23]\n"
        "ds_read_b32 v153, v10\n"
        "v_mfma_f32_16x16x32_bf16 v[20:23], v[204:207], v[124:127], v[20:23]\n"
        "v_mfma_f32_16x16x32_bf16 v[28:31], v[216:219], v[124:127], 0\n"
        "v_mfma_f32_16x16x32_bf16 v[28:31], v[220:223], v[128:131], v[28:31]\n"
        "v_mfma_f32_16x16x32_bf16 v[28:31], v[224:227], v[160:163], v[28:31]\n"
        "buffer_store_dwordx3 v[146:148], v12, s[8:11], s37 offen offset:576 sc0 sc1 nt\n"
        "s_add_u32 s37, s36, 392448\n"
        "s_waitcnt lgkmcnt(1)\n"
        "v_add_f32_dpp v155, v34, v35 quad_perm:[0,1,2,3] row_mask:0x1 bank_mask:0xf\n"
        "v_add_f32_dpp v154, v32, v33 quad_perm:[0,1,2,3] row_mask:0x1 bank_mask:0xf\n"
        "v_perm_b32 v86, v21, v20, s63\n"
        "v_mfma_f32_16x16x32_bf16 v[44:47], v[236:239], v[160:163], 0\n"
        "v_perm_b32 v87, v23, v22, s63\n"
        "v_perm_b32 v20, v29, v28, s63\n"
        "v_mfma_f32_16x16x32_bf16 v[44:47], v[232:235], v[128:131], v[44:47]\n"
        "v_perm_b32 v21, v31, v30, s63\n"
        "v_add_f32_dpp v155, v26, v27 quad_perm:[0,1,2,3] row_mask:0x2 bank_mask:0xf\n"
        "v_mfma_f32_16x16x32_bf16 v[44:47], v[228:231], v[124:127], v[44:47]\n"
        "buffer_store_dwordx3 v[150:152], v12, s[8:11], s37 offen sc0 sc1 nt\n"
        "v_add_f32_dpp v154, v24, v25 quad_perm:[0,1,2,3] row_mask:0x2 bank_mask:0xf\n"
        "v_add_f32_dpp v155, v102, v103 quad_perm:[0,1,2,3] row_mask:0x4 bank_mask:0xf\n"
        "v_mul_f32 v165, s52, v155\n"
        "v_add_f32_dpp v154, v100, v101 quad_perm:[0,1,2,3] row_mask:0x4 bank_mask:0xf\n"
        "s_waitcnt lgkmcnt(0)\n"
        "v_fma_f32 v165, -v165, v155, v153\n"
        "v_add_f32_e64 v165, s53, |v165|\n"
        "v_perm_b32 v23, v47, v46, s63\n"
        "v_perm_b32 v22, v45, v44, s63\n"
        "v_mfma_f32_16x16x32_bf16 v[100:103], v[124:127], v[80:83], 0\n"
        "v_rsq_f32 v153, v165\n"
        "v_mfma_f32_16x16x32_bf16 v[100:103], v[128:131], v[84:87], v[100:103]\n"
        "s_add_u32 s37, s36, 392448\n"
        "ds_read_b128 v[104:107], v8 offset:128\n"
        "v_and_b32 v56, s28, v153\n"
        "v_mfma_f32_16x16x32_bf16 v[100:103], v[160:163], v[20:23], v[100:103]\n"
        "ds_read2_b32 v[108:109], v6 offset0:0 offset1:2\n"
        "ds_read2_b32 v[110:111], v6 offset0:8 offset1:10\n"
        "v_mfma_f32_16x16x32_bf16 v[144:147], v[52:55], v[20:23], 0\n"
        "v_and_b32 v164, s28, v153\n"
        "v_sub_f32 v167, v153, v56\n"
        "v_mfma_f32_16x16x32_bf16 v[144:147], v[136:139], v[84:87], v[144:147]\n"
        "v_cvt_pk_bf16_f32 v57, v167, v56\n"
        "v_cvt_pk_bf16_f32 v165, v164, v167\n"
        "v_mfma_f32_16x16x32_bf16 v[144:147], v[132:135], v[80:83], v[144:147]\n"
        "buffer_store_dwordx3 v[72:74], v15, s[8:11], s37 offen offset:768 sc0 sc1 nt\n"
        "v_mul_f32 v154, v154, v153\n"
        "v_readlane_b32 s58, v153, 56\n"
        "s_mov_b64 exec, s[56:57]\n"
        "v_mov_b32 v154, 0xff800000\n"
        "s_mov_b64 exec, -1\n"
        "v_mfma_f32_16x16x4_4b_bf16 v[32:47], v[56:57], v[164:165], 0 blgp:4\n"
        "v_mul_f32 v154, s58, v154\n"
        "v_exp_f32 v154, v154\n"
        "v_mfma_f32_16x16x32_bf16 v[24:27], v[92:95], v[80:83], 0\n"
        "s_add_u32 s37, s36, 490560\n"
        "v_add_f32_dpp v167, v154, v154 row_ror:8 row_mask:0xf bank_mask:0xf\n"
        "v_mul_f32 v154, v154, v153\n"
        "v_mfma_f32_16x16x32_bf16 v[24:27], v[96:99], v[84:87], v[24:27]\n"
        "v_add_f32_dpp v167, v167, v167 row_ror:4 row_mask:0xf bank_mask:0xf\n"
        "v_mfma_f32_16x16x32_bf16 v[24:27], v[116:119], v[20:23], v[24:27]\n"
        "s_waitcnt lgkmcnt(0)\n"
        "v_add_f32_dpp v167, v167, v167 row_ror:2 row_mask:0xf bank_mask:0xf\n"
        "v_mfma_f32_16x16x32_bf16 v[148:151], v[104:107], v[20:23], 0\n"
        "v_mul_f32 v100, v100, v32\n"
        "v_mul_f32 v101, v101, v33\n"
        "v_smfmac_f32_16x16x64_bf16 v[148:151], v[108:111], v[80:87], v4\n"
        "buffer_store_dwordx3 v[156:158], v12, s[8:11], s37 offen offset:192 sc0 sc1 nt\n"
        "v_mul_f32 v102, v102, v34\n"
        "v_mul_f32 v103, v103, v35\n"
        "v_exp_f32 v100, v100\n"
        "v_mul_f32 v144, v144, v36\n"
        "v_mfma_f32_16x16x32_bf16 v[20:23], v[168:171], v[132:135], 0\n"
        "s_add_u32 s37, s36, 490560\n"
        "v_exp_f32 v101, v101\n"
        "v_mfma_f32_16x16x32_bf16 v[20:23], v[172:175], v[136:139], v[20:23]\n"
        "v_exp_f32 v102, v102\n"
        "v_mfma_f32_16x16x32_bf16 v[20:23], v[176:179], v[52:55], v[20:23]\n"
        "v_exp_f32 v103, v103\n"
        "v_add_f32 v151, v100, v101\n"
        "v_mfma_f32_16x16x32_bf16 v[28:31], v[188:191], v[52:55], 0\n"
        "v_add_f32 v151, v151, v102\n"
        "v_add_f32 v151, v151, v103\n"
        "v_mfma_f32_16x16x32_bf16 v[28:31], v[184:187], v[136:139], v[28:31]\n"
        "v_mul_f32 v145, v145, v37\n"
        "v_mul_f32 v146, v146, v38\n"
        "v_mfma_f32_16x16x32_bf16 v[28:31], v[180:183], v[132:135], v[28:31]\n"
        "buffer_store_dwordx3 v[76:78], v14, s[8:11], s37 offen offset:960 sc0 sc1 nt\n"
        "v_perm_b32 v68, v21, v20, s63\n"
        "v_perm_b32 v69, v23, v22, s63\n"
        "v_mul_f32 v147, v147, v39\n"
        "v_exp_f32 v144, v144\n"
        "v_mfma_f32_16x16x32_bf16 v[20:23], v[192:195], v[132:135], 0\n"
        "s_add_u32 s37, s36, 588672\n"
        "v_exp_f32 v145, v145\n"
        "v_mfma_f32_16x16x32_bf16 v[20:23], v[196:199], v[136:139], v[20:23]\n"
        "v_perm_b32 v70, v29, v28, s63\n"
        "v_perm_b32 v71, v31, v30, s63\n"
        "v_mfma_f32_16x16x32_bf16 v[20:23], v[200:203], v[52:55], v[20:23]\n"
        "buffer_store_dwordx3 v[88:90], v12, s[8:11], s37 offen offset:384 sc0 sc1 nt\n"
        "v_exp_f32 v146, v146\n"
        "v_exp_f32 v147, v147\n"
        "v_add_f32 v151, v151, v144\n"
        "v_add_f32 v151, v151, v145\n"
        "v_mfma_f32_16x16x32_bf16 v[28:31], v[212:215], v[52:55], 0\n"
        "v_add_f32 v151, v151, v146\n"
        "v_add_f32 v151, v151, v147\n"
        "v_mfma_f32_16x16x32_bf16 v[28:31], v[208:211], v[136:139], v[28:31]\n"
        "v_perm_b32 v72, v21, v20, s63\n"
        "v_perm_b32 v73, v23, v22, s63\n"
        "v_mfma_f32_16x16x32_bf16 v[28:31], v[204:207], v[132:135], v[28:31]\n"
        "v_mul_f32 v24, v24, v40\n"
        "v_mul_f32 v25, v25, v41\n"
        "v_mul_f32 v26, v26, v42\n"
        "v_mfma_f32_16x16x32_bf16 v[20:23], v[216:219], v[132:135], 0\n"
        "v_mul_f32 v27, v27, v43\n"
        "v_add_f32 v148, v148, v149\n"
        "v_mfma_f32_16x16x32_bf16 v[20:23], v[220:223], v[136:139], v[20:23]\n"
        "v_exp_f32 v24, v24\n"
        "v_mfma_f32_16x16x32_bf16 v[20:23], v[224:227], v[52:55], v[20:23]\n"
        "v_perm_b32 v74, v29, v28, s63\n"
        "v_perm_b32 v75, v31, v30, s63\n"
        "v_add_f32 v151, v151, v24\n"
        "v_mfma_f32_16x16x32_bf16 v[64:67], v[236:239], v[52:55], 0\n"
        "v_exp_f32 v25, v25\n"
        "v_mfma_f32_16x16x32_bf16 v[64:67], v[232:235], v[136:139], v[64:67]\n"
        "v_exp_f32 v26, v26\n"
        "v_mfma_f32_16x16x32_bf16 v[64:67], v[228:231], v[132:135], v[64:67]\n"
        "v_perm_b32 v48, v21, v20, s63\n"
        "v_perm_b32 v49, v23, v22, s63\n"
        "v_mfma_f32_16x16x4_4b_bf16 v[28:43], v[56:57], v[164:165], 0 blgp:5\n"
        "v_exp_f32 v27, v27\n"
        "v_add_f32 v151, v151, v25\n"
        "v_add_f32 v151, v151, v26\n"
        "v_mul_f32 v148, v148, v44\n"
        "v_exp_f32 v148, v148\n"
        "v_perm_b32 v50, v65, v64, s63\n"
        "v_perm_b32 v51, v67, v66, s63\n"
        "v_mfma_f32_16x16x32_bf16 v[58:61], v[124:127], v[68:71], 0\n"
        "v_add_f32 v151, v151, v27\n"
        "v_add_f32_dpp v167, v167, v167 row_ror:1 row_mask:0xf bank_mask:0xf\n"
        "v_mfma_f32_16x16x32_bf16 v[58:61], v[128:131], v[72:75], v[58:61]\n"
        "v_add_f32_dpp v151, v148, v151 quad_perm:[0,1,2,3] row_mask:0x4 bank_mask:0xf\n"
        "v_mfma_f32_16x16x32_bf16 v[58:61], v[160:163], v[48:51], v[58:61]\n"
        "v_mfma_f32_16x16x32_bf16 v[112:115], v[52:55], v[48:51], 0\n"
        "v_permlane32_swap_b32 v151, v167\n"
        "v_mfma_f32_16x16x32_bf16 v[112:115], v[136:139], v[72:75], v[112:115]\n"
        "v_add_f32 v151, v151, v167\n"
        "v_mov_b32 v167, v151\n"
        "v_mfma_f32_16x16x32_bf16 v[112:115], v[132:135], v[68:71], v[112:115]\n"
        "v_mfma_f32_16x16x32_bf16 v[20:23], v[92:95], v[68:71], 0\n"
        "v_permlane16_swap_b32 v151, v167\n"
        "v_mfma_f32_16x16x32_bf16 v[20:23], v[96:99], v[72:75], v[20:23]\n"
        "v_add_f32 v151, v151, v167\n"
        "v_mul_f32 v58, v58, v28\n"
        "v_mfma_f32_16x16x32_bf16 v[20:23], v[116:119], v[48:51], v[20:23]\n"
        "v_rcp_f32 v123, v151\n"
        "v_mul_f32 v59, v59, v29\n"
        "v_and_b32 v158, s28, v123\n"
        "v_mfma_f32_16x16x32_bf16 v[62:65], v[104:107], v[48:51], 0\n"
        "v_sub_f32 v152, v123, v158\n"
        "v_cvt_pk_bf16_f32 v159, v158, v152\n"
        "v_smfmac_f32_16x16x64_bf16 v[62:65], v[108:111], v[68:75], v4\n"
        "v_min3_f32 v167, v151, v123, v153\n"
        "v_mfma_f32_16x16x32_bf16 v[48:51], v[168:171], v[92:95], 0\n"
        "v_mul_f32 v60, v60, v30\n"
        "v_mul_f32 v61, v61, v31\n"
        "v_mfma_f32_16x16x32_bf16 v[48:51], v[172:175], v[96:99], v[48:51]\n"
        "v_exp_f32 v58, v58\n"
        "v_mfma_f32_16x16x32_bf16 v[48:51], v[176:179], v[116:119], v[48:51]\n"
        "v_exp_f32 v59, v59\n"
        "v_exp_f32 v60, v60\n"
        "v_mfma_f32_16x16x32_bf16 v[68:71], v[188:191], v[116:119], 0\n"
        "v_exp_f32 v61, v61\n"
        "v_mfma_f32_16x16x32_bf16 v[68:71], v[184:187], v[96:99], v[68:71]\n"
        "v_add_f32 v155, v58, v59\n"
        "v_add_f32 v155, v155, v60\n"
        "v_mfma_f32_16x16x32_bf16 v[68:71], v[180:183], v[92:95], v[68:71]\n"
        "v_perm_b32 v44, v49, v48, s63\n"
        "v_perm_b32 v45, v51, v50, s63\n"
        "v_add_f32 v155, v155, v61\n"
        "v_mfma_f32_16x16x32_bf16 v[28:31], v[192:195], v[92:95], 0\n"
        "v_mul_f32 v112, v112, v32\n"
        "v_mul_f32 v113, v113, v33\n"
        "v_mfma_f32_16x16x32_bf16 v[28:31], v[196:199], v[96:99], v[28:31]\n"
        "v_mul_f32 v114, v114, v34\n"
        "v_perm_b32 v46, v69, v68, s63\n"
        "v_mfma_f32_16x16x32_bf16 v[28:31], v[200:203], v[116:119], v[28:31]\n"
        "v_perm_b32 v47, v71, v70, s63\n"
        "v_mul_f32 v115, v115, v35\n"
        "v_exp_f32 v112, v112\n"
        "v_mfma_f32_16x16x32_bf16 v[68:71], v[212:215], v[116:119], 0\n"
        "v_exp_f32 v113, v113\n"
        "v_mfma_f32_16x16x32_bf16 v[68:71], v[208:211], v[96:99], v[68:71]\n"
        "v_exp_f32 v114, v114\n"
        "v_mfma_f32_16x16x32_bf16 v[68:71], v[204:207], v[92:95], v[68:71]\n"
        "v_perm_b32 v48, v29, v28, s63\n"
        "v_perm_b32 v49, v31, v30, s63\n"
        "v_add_f32 v155, v155, v112\n"
        "v_mfma_f32_16x16x32_bf16 v[64:67], v[216:219], v[92:95], 0\n"
        "v_exp_f32 v115, v115\n"
        "v_mfma_f32_16x16x32_bf16 v[64:67], v[220:223], v[96:99], v[64:67]\n"
        "v_add_f32 v155, v155, v113\n"
        "v_add_f32 v155, v155, v114\n"
        "v_mfma_f32_16x16x32_bf16 v[64:67], v[224:227], v[116:119], v[64:67]\n"
        "v_perm_b32 v50, v69, v68, s63\n"
        "v_perm_b32 v51, v71, v70, s63\n"
        "v_add_f32 v155, v155, v115\n"
        "v_mul_f32 v20, v20, v36\n"
        "v_mfma_f32_16x16x32_bf16 v[28:31], v[236:239], v[116:119], 0\n"
        "v_mul_f32 v21, v21, v37\n"
        "v_mul_f32 v22, v22, v38\n"
        "v_mfma_f32_16x16x32_bf16 v[28:31], v[232:235], v[96:99], v[28:31]\n"
        "v_perm_b32 v150, v65, v64, s63\n"
        "v_perm_b32 v151, v67, v66, s63\n"
        "v_mfma_f32_16x16x32_bf16 v[28:31], v[228:231], v[92:95], v[28:31]\n"
        "v_exp_f32 v20, v20\n"
        "v_mfma_f32_16x16x4_4b_bf16 v[76:91], v[56:57], v[158:159], 0 blgp:4\n"
        "v_exp_f32 v21, v21\n"
        "v_mul_f32 v23, v23, v39\n"
        "v_exp_f32 v22, v22\n"
        "v_exp_f32 v23, v23\n"
        "v_add_f32 v155, v155, v20\n"
        "v_add_f32 v155, v155, v21\n"
        "v_perm_b32 v152, v29, v28, s63\n"
        "v_perm_b32 v153, v31, v30, s63\n"
        "v_mfma_f32_16x16x32_bf16 v[36:39], v[124:127], v[44:47], 0\n"
        "v_add_f32 v155, v155, v22\n"
        "v_add_f32 v155, v155, v23\n"
        "v_mfma_f32_16x16x32_bf16 v[36:39], v[128:131], v[48:51], v[36:39]\n"
        "v_mul_f32 v100, v100, v76\n"
        "v_mul_f32 v101, v101, v77\n"
        "v_mfma_f32_16x16x32_bf16 v[36:39], v[160:163], v[150:153], v[36:39]\n"
        "v_mul_f32 v102, v102, v78\n"
        "v_mul_f32 v103, v103, v79\n"
        "v_mul_f32 v144, v144, v80\n"
        "v_mul_f32 v145, v145, v81\n"
        "v_mfma_f32_16x16x32_bf16 v[140:143], v[52:55], v[150:153], 0\n"
        "v_mul_f32 v146, v146, v82\n"
        "v_mul_f32 v147, v147, v83\n"
        "v_mfma_f32_16x16x32_bf16 v[140:143], v[136:139], v[48:51], v[140:143]\n"
        "v_mul_f32 v24, v24, v84\n"
        "v_mul_f32 v25, v25, v85\n"
        "v_mfma_f32_16x16x32_bf16 v[140:143], v[132:135], v[44:47], v[140:143]\n"
        "v_add_f32 v62, v62, v63\n"
        "v_mul_f32 v62, v62, v40\n"
        "v_mul_f32 v26, v26, v86\n"
        "v_mfma_f32_16x16x4_4b_bf16 v[64:79], v[56:57], v[164:165], 0 blgp:6\n"
        "v_exp_f32 v62, v62\n"
        "v_mul_f32 v27, v27, v87\n"
        "v_fma_f32 v148, v148, v88, v19\n"
        "v_add_f32_dpp v155, v62, v155 quad_perm:[0,1,2,3] row_mask:0x4 bank_mask:0xf\n"
        "v_perm_b32 v40, v101, v100, s63\n"
        "v_perm_b32 v41, v103, v102, s63\n"
        "v_perm_b32 v42, v145, v144, s63\n"
        "v_perm_b32 v43, v147, v146, s63\n"
        "v_perm_b32 v164, v25, v24, s63\n"
        "v_mfma_f32_16x16x32_bf16 v[156:159], v[92:95], v[44:47], 0\n"
        "v_perm_b32 v166, v27, v26, s63\n"
        "v_perm_b32 v165, 0, v148, s63\n"
        "v_mfma_f32_16x16x32_bf16 v[156:159], v[96:99], v[48:51], v[156:159]\n"
        "v_mul_f32 v36, v36, v64\n"
        "v_mul_f32 v37, v37, v65\n"
        "v_mfma_f32_16x16x32_bf16 v[156:159], v[116:119], v[150:153], v[156:159]\n"
        "v_mul_f32 v38, v38, v66\n"
        "v_mul_f32 v39, v39, v67\n"
        "v_mul_f32 v140, v140, v68\n"
        "v_mfma_f32_16x16x32_bf16 v[92:95], v[104:107], v[150:153], 0\n"
        "ds_read_b128 v[88:91], v17 offset:0\n"
        "v_mul_f32 v141, v141, v69\n"
        "v_smfmac_f32_16x16x64_bf16 v[92:95], v[108:111], v[44:51], v4\n"
        "ds_read_b128 v[84:87], v17 offset:64\n"
        "ds_read_b128 v[150:153], v17 offset:128\n"
        "v_mfma_f32_16x16x32_bf16 v[48:51], v[124:127], v[240:243], 0\n"
        "v_exp_f32 v36, v36\n"
        "v_mfma_f32_16x16x32_bf16 v[48:51], v[128:131], v[244:247], v[48:51]\n"
        "ds_read_b128 v[80:83], v0 offset:5136\n"
        "v_mul_f32 v142, v142, v70\n"
        "v_mfma_f32_16x16x32_bf16 v[48:51], v[160:163], v[248:251], v[48:51]\n"
        "ds_read_b128 v[32:35], v0 offset:4112\n"
        "v_mul_f32 v143, v143, v71\n"
        "v_mfma_f32_16x16x32_bf16 v[24:27], v[52:55], v[248:251], 0\n"
        "v_exp_f32 v37, v37\n"
        "v_mfma_f32_16x16x32_bf16 v[24:27], v[136:139], v[244:247], v[24:27]\n"
        "v_exp_f32 v38, v38\n"
        "v_mfma_f32_16x16x32_bf16 v[24:27], v[132:135], v[240:243], v[24:27]\n"
        "s_waitcnt lgkmcnt(2)\n"
        "v_perm_b32 v44, v49, v48, s63\n"
        "v_perm_b32 v45, v51, v50, s63\n"
        "v_add_f32 v145, v36, v37\n"
        "v_mfma_f32_16x16x32_bf16 v[104:107], v[88:91], v[240:243], 0\n"
        "v_exp_f32 v39, v39\n"
        "v_mfma_f32_16x16x32_bf16 v[104:107], v[84:87], v[244:247], v[104:107]\n"
        "v_add_f32 v145, v145, v38\n"
        "v_perm_b32 v46, v25, v24, s63\n"
        "v_mfma_f32_16x16x32_bf16 v[104:107], v[150:153], v[248:251], v[104:107]\n"
        "s_waitcnt lgkmcnt(0)\n"
        "v_perm_b32 v47, v27, v26, s63\n"
        "v_add_f32 v145, v145, v39\n"
        "v_exp_f32 v140, v140\n"
        "v_mfma_f32_16x16x32_bf16 v[64:67], v[150:153], v[80:83], 0\n"
        "v_exp_f32 v141, v141\n"
        "v_mfma_f32_16x16x32_bf16 v[64:67], v[84:87], v[32:35], v[64:67]\n"
        "ds_read_b128 v[146:149], v0 offset:6176\n"
        "v_perm_b32 v116, v105, v104, s63\n"
        "v_mfma_f32_16x16x32_bf16 v[64:67], v[88:91], v[252:255], v[64:67]\n"
        "ds_read_b128 v[24:27], v0 offset:7200\n"
        "ds_read_b128 v[108:111], v0 offset:8224\n"
        "v_mfma_f32_16x16x32_bf16 v[100:103], v[132:135], v[252:255], 0\n"
        "v_perm_b32 v120, v107, v106, s63\n"
        "v_add_f32 v145, v145, v140\n"
        "v_mfma_f32_16x16x32_bf16 v[100:103], v[136:139], v[32:35], v[100:103]\n"
        "v_exp_f32 v142, v142\n"
        "v_mfma_f32_16x16x32_bf16 v[100:103], v[52:55], v[80:83], v[100:103]\n"
        "v_perm_b32 v94, v65, v64, s63\n"
        "v_perm_b32 v98, v67, v66, s63\n"
        "v_add_f32 v145, v145, v141\n"
        "v_mfma_f32_16x16x32_bf16 v[68:71], v[160:163], v[80:83], 0\n"
        "v_exp_f32 v143, v143\n"
        "v_mfma_f32_16x16x32_bf16 v[68:71], v[128:131], v[32:35], v[68:71]\n"
        "v_add_f32 v145, v145, v142\n"
        "v_add_f32 v145, v145, v143\n"
        "v_mfma_f32_16x16x32_bf16 v[68:71], v[124:127], v[252:255], v[68:71]\n"
        "s_waitcnt lgkmcnt(0)\n"
        "v_perm_b32 v106, v101, v100, s63\n"
        "v_perm_b32 v107, v103, v102, s63\n"
        "v_mul_f32 v156, v156, v72\n"
        "v_mul_f32 v157, v157, v73\n"
        "v_mfma_f32_16x16x32_bf16 v[32:35], v[124:127], v[146:149], 0\n"
        "v_mul_f32 v158, v158, v74\n"
        "v_mul_f32 v159, v159, v75\n"
        "v_mfma_f32_16x16x32_bf16 v[32:35], v[128:131], v[24:27], v[32:35]\n"
        "ds_read_b128 v[48:51], v0 offset:11312\n"
        "v_perm_b32 v104, v69, v68, s63\n"
        "v_mfma_f32_16x16x32_bf16 v[32:35], v[160:163], v[108:111], v[32:35]\n"
        "ds_read_b128 v[80:83], v0 offset:10288\n"
        "ds_read_b128 v[64:67], v0 offset:9264\n"
        "v_mfma_f32_16x16x32_bf16 v[100:103], v[52:55], v[108:111], 0\n"
        "v_perm_b32 v105, v71, v70, s63\n"
        "v_add_f32 v92, v92, v93\n"
        "v_mfma_f32_16x16x32_bf16 v[100:103], v[136:139], v[24:27], v[100:103]\n"
        "v_exp_f32 v156, v156\n"
        "v_mfma_f32_16x16x32_bf16 v[100:103], v[132:135], v[146:149], v[100:103]\n"
        "v_perm_b32 v28, v33, v32, s63\n"
        "v_perm_b32 v29, v35, v34, s63\n"
        "v_add_f32 v145, v145, v156\n"
        "v_mfma_f32_16x16x32_bf16 v[70:73], v[88:91], v[146:149], 0\n"
        "v_exp_f32 v157, v157\n"
        "v_mfma_f32_16x16x32_bf16 v[70:73], v[84:87], v[24:27], v[70:73]\n"
        "v_exp_f32 v158, v158\n"
        "v_mfma_f32_16x16x32_bf16 v[70:73], v[150:153], v[108:111], v[70:73]\n"
        "s_waitcnt lgkmcnt(0)\n"
        "v_perm_b32 v30, v101, v100, s63\n"
        "v_perm_b32 v31, v103, v102, s63\n"
        "v_exp_f32 v159, v159\n"
        "v_mfma_f32_16x16x32_bf16 v[32:35], v[150:153], v[48:51], 0\n"
        "v_add_f32 v145, v145, v157\n"
        "v_add_f32 v145, v145, v158\n"
        "v_mfma_f32_16x16x32_bf16 v[32:35], v[84:87], v[80:83], v[32:35]\n"
        "ds_read_b128 v[100:103], v0 offset:12352\n"
        "v_perm_b32 v144, v71, v70, s63\n"
        "v_mfma_f32_16x16x32_bf16 v[32:35], v[88:91], v[64:67], v[32:35]\n"
        "ds_read_b128 v[108:111], v0 offset:13376\n"
        "ds_read_b128 v[24:27], v0 offset:14400\n"
        "v_mfma_f32_16x16x32_bf16 v[68:71], v[132:135], v[64:67], 0\n"
        "v_perm_b32 v148, v73, v72, s63\n"
        "v_mul_f32 v92, v92, v76\n"
        "v_mfma_f32_16x16x32_bf16 v[68:71], v[136:139], v[80:83], v[68:71]\n"
        "v_add_f32 v145, v145, v159\n"
        "v_readlane_b32 s59, v123, 32\n"
        "v_mfma_f32_16x16x32_bf16 v[68:71], v[52:55], v[48:51], v[68:71]\n"
        "v_perm_b32 v117, v33, v32, s63\n"
        "v_perm_b32 v121, v35, v34, s63\n"
        "v_exp_f32 v92, v92\n"
        "v_mfma_f32_16x16x32_bf16 v[72:75], v[160:163], v[48:51], 0\n"
        "v_mul_f32 v154, s59, v154\n"
        "v_add_f32_dpp v145, v92, v145 quad_perm:[0,1,2,3] row_mask:0x4 bank_mask:0xf\n"
        "v_mfma_f32_16x16x32_bf16 v[72:75], v[128:131], v[80:83], v[72:75]\n"
        "v_perm_b32 v154, 0, v154, s63\n"
        "v_perm_b32 v50, v69, v68, s63\n"
        "v_mfma_f32_16x16x32_bf16 v[72:75], v[124:127], v[64:67], v[72:75]\n"
        "s_waitcnt lgkmcnt(0)\n"
        "v_perm_b32 v51, v71, v70, s63\n"
        "v_permlane32_swap_b32 v155, v145\n"
        "v_mfma_f32_16x16x32_bf16 v[80:83], v[124:127], v[100:103], 0\n"
        "v_add_f32 v155, v155, v145\n"
        "v_mov_b32 v149, v155\n"
        "v_mfma_f32_16x16x32_bf16 v[80:83], v[128:131], v[108:111], v[80:83]\n"
        "ds_read_b128 v[32:35], v0 offset:17488\n"
        "v_perm_b32 v48, v73, v72, s63\n"
        "v_mfma_f32_16x16x32_bf16 v[80:83], v[160:163], v[24:27], v[80:83]\n"
        "ds_read_b128 v[68:71], v0 offset:16464\n"
        "ds_read_b128 v[64:67], v0 offset:15440\n"
        "v_mfma_f32_16x16x32_bf16 v[76:79], v[52:55], v[24:27], 0\n"
        "v_perm_b32 v49, v75, v74, s63\n"
        "v_mfma_f32_16x16x32_bf16 v[76:79], v[136:139], v[108:111], v[76:79]\n"
        "v_permlane16_swap_b32 v155, v149\n"
        "v_mfma_f32_16x16x32_bf16 v[76:79], v[132:135], v[100:103], v[76:79]\n"
        "v_add_f32 v155, v155, v149\n"
        "v_rcp_f32 v147, v155\n"
        "v_mfma_f32_16x16x32_bf16 v[72:75], v[88:91], v[100:103], 0\n"
        "ds_read2_b32 v[122:123], v11 offset0:0 offset1:3\n"
        "v_min3_f32 v167, v167, v155, v147\n"
        "v_mfma_f32_16x16x32_bf16 v[72:75], v[84:87], v[108:111], v[72:75]\n"
        "ds_read_b64_tr_b16 v[118:119], v9 offset:0\n"
        "v_and_b32 v102, s28, v147\n"
        "v_mfma_f32_16x16x32_bf16 v[72:75], v[150:153], v[24:27], v[72:75]\n"
        "s_waitcnt lgkmcnt(2)\n"
        "ds_read2_b32 v[100:101], v11 offset0:1 offset1:4\n"
        "v_sub_f32 v149, v147, v102\n"
        "v_cvt_pk_bf16_f32 v103, v102, v149\n"
        "v_perm_b32 v108, v81, v80, s63\n"
        "v_mfma_f32_16x16x32_bf16 v[24:27], v[150:153], v[32:35], 0\n"
        "ds_read_b64_tr_b16 v[96:97], v9 offset:16\n"
        "v_perm_b32 v109, v83, v82, s63\n"
        "v_mfma_f32_16x16x32_bf16 v[24:27], v[84:87], v[68:71], v[24:27]\n"
        "ds_read2_b32 v[150:151], v11 offset0:2 offset1:5\n"
        "v_perm_b32 v110, v77, v76, s63\n"
        "v_mfma_f32_16x16x32_bf16 v[24:27], v[88:91], v[64:67], v[24:27]\n"
        "ds_read_b64_tr_b16 v[146:147], v9 offset:32\n"
        "v_perm_b32 v111, v79, v78, s63\n"
        "v_perm_b32 v95, v73, v72, s63\n"
        "v_mfma_f32_16x16x4_4b_bf16 v[76:91], v[56:57], v[102:103], 0 blgp:4\n"
        "v_perm_b32 v99, v75, v74, s63\n"
        "v_cmp_nle_f32 vcc, 0x2b800000, v167\n"
        "v_mov_b32_dpp v167, v19 row_shr:4 row_mask:0xf bank_mask:0xf bound_ctrl:0\n"
        "ds_write_b16 v1, v154\n"
        "v_perm_b32 v145, v25, v24, s63\n"
        "v_perm_b32 v149, v27, v26, s63\n"
        "s_andn2_b64 vcc, vcc, s[56:57]\n"
        "s_cbranch_scc1 CREDOA_%=\n"
        "v_mfma_f32_16x16x32_bf16 v[24:27], v[132:135], v[64:67], 0\n"
        "s_mov_b32 exec_hi, 0xffff\n"
        "v_add_u32 v10, 40064, v10\n"
        "s_mov_b32 exec_hi, -1\n"
        "v_mfma_f32_16x16x32_bf16 v[24:27], v[136:139], v[68:71], v[24:27]\n"
        "v_mul_f32 v58, v58, v76\n"
        "v_mul_f32 v59, v59, v77\n"
        "v_mfma_f32_16x16x32_bf16 v[24:27], v[52:55], v[32:35], v[24:27]\n"
        "s_add_u32 s31, s31, 1\n"
        "v_mul_f32 v60, v60, v78\n"
        "v_mul_f32 v61, v61, v79\n"
        "v_mul_f32 v112, v112, v80\n"
        "v_mul_f32 v113, v113, v81\n"
        "v_mfma_f32_16x16x32_bf16 v[52:55], v[160:163], v[32:35], 0\n"
        "s_cmp_ge_u32 s31, s71\n"
        "v_mul_f32 v114, v114, v82\n"
        "v_mul_f32 v115, v115, v83\n"
        "v_mfma_f32_16x16x32_bf16 v[52:55], v[128:131], v[68:71], v[52:55]\n"
        "s_cbranch_scc1 L34A_%=\n"
        "L35A_%=:\n"
        "v_mul_f32 v20, v20, v84\n"
        "v_perm_b32 v34, v25, v24, s63\n"
        "v_mfma_f32_16x16x32_bf16 v[52:55], v[124:127], v[64:67], v[52:55]\n"
        "s_add_u32 s16, s16, 256\n"
        "v_perm_b32 v35, v27, v26, s63\n"
        "v_mul_f32 v21, v21, v85\n"
        "v_mfma_f32_16x16x32_bf16 v[124:127], v[40:43], v[44:47], 0\n"
        "s_waitcnt lgkmcnt(0)\n"
        "v_mul_f32 v22, v22, v86\n"
        "v_mul_f32 v23, v23, v87\n"
        "v_smfmac_f32_16x16x64_bf16 v[124:127], v[164:167], v[116:123], v18\n"
        "s_cmp_ge_u32 s31, 20\n"
        "s_cselect_b32 s39, s22, s16\n"
        "ds_read_b64_tr_b16 v[118:119], v9 offset:48\n"
        "v_mfma_f32_16x16x4_4b_bf16 v[72:87], v[56:57], v[102:103], 0 blgp:6\n"
        "v_perm_b32 v32, v53, v52, s63\n"
        "v_perm_b32 v33, v55, v54, s63\n"
        "v_fma_f32 v62, v62, v88, v19\n"
        "v_perm_b32 v68, v59, v58, s63\n"
        "v_perm_b32 v69, v61, v60, s63\n"
        "v_perm_b32 v70, v113, v112, s63\n"
        "v_perm_b32 v71, v115, v114, s63\n"
        "v_perm_b32 v24, v21, v20, s63\n"
        "v_perm_b32 v26, v23, v22, s63\n"
        "v_mfma_f32_16x16x32_bf16 v[88:91], v[40:43], v[104:107], 0\n"
        "s_cmp_ge_u32 s31, 21\n"
        "v_perm_b32 v25, 0, v62, s63\n"
        "v_mul_f32 v36, v36, v72\n"
        "v_smfmac_f32_16x16x64_bf16 v[88:91], v[164:167], v[94:101], v18\n"
        "s_cselect_b32 s39, s23, s39\n"
        "ds_read_b64_tr_b16 v[96:97], v9 offset:64\n"
        "v_mfma_f32_16x16x32_bf16 v[56:59], v[40:43], v[28:31], 0\n"
        "s_mul_hi_u32 s38, s39, 58835169\n"
        "ds_read_b128 v[128:131], v2\n"
        "v_mul_f32 v37, v37, v73\n"
        "v_smfmac_f32_16x16x64_bf16 v[56:59], v[164:167], v[144:151], v18\n"
        "s_mul_i32 s39, s39, 1344\n"
        "ds_read_b64_tr_b16 v[146:147], v9 offset:80\n"
        "ds_read_b128 v[52:55], v3\n"
        "v_mfma_f32_16x16x32_bf16 v[112:115], v[40:43], v[48:51], 0\n"
        "s_waitcnt lgkmcnt(4)\n"
        "v_mul_f32 v38, v38, v74\n"
        "v_mul_f32 v39, v39, v75\n"
        "v_smfmac_f32_16x16x64_bf16 v[112:115], v[164:167], v[116:123], v18 abid:1\n"
        "s_mul_i32 s38, s38, 588672\n"
        "s_add_u32 s39, s39, s38\n"
        "v_mul_f32 v140, v140, v76\n"
        "v_cvt_pk_bf16_f32 v152, v124, v88\n"
        "v_mul_f32 v141, v141, v77\n"
        "v_mfma_f32_16x16x32_bf16 v[72:75], v[40:43], v[108:111], 0\n"
        "s_add_u32 s39, s39, s17\n"
        "v_mov_b64 v[60:61], 0\n"
        "s_waitcnt lgkmcnt(3)\n"
        "v_smfmac_f32_16x16x64_bf16 v[72:75], v[164:167], v[94:101], v18 abid:1\n"
        "s_mul_i32 s68, s31, 3\n"
        "v_mov_b64 v[62:63], 0\n"
        "v_cvt_pk_bf16_f32 v153, v56, v112\n"
        "v_mfma_f32_16x16x32_bf16 v[64:67], v[40:43], v[32:35], 0\n"
        "s_waitcnt lgkmcnt(1)\n"
        "v_mov_b64 v[20:21], 0\n"
        "v_mov_b64 v[22:23], 0\n"
        "v_smfmac_f32_16x16x64_bf16 v[64:67], v[164:167], v[144:151], v18 abid:1\n"
        "s_waitcnt lgkmcnt(0)\n"
        "s_bitcmp1_b32 s31, 0\n"
        "v_mov_b32_dpp v27, v19 row_shr:4 row_mask:0xf bank_mask:0xf bound_ctrl:0\n"
        "s_cselect_b32 s38, 3, 6\n"
        "v_smfmac_f32_16x16x64_bf16 v[60:63], v[128:131], v[44:51], v4\n"
        "s_add_u32 s68, s68, s38\n"
        "v_mov_b64 v[40:41], 0\n"
        "v_smfmac_f32_16x16x64_bf16 v[60:63], v[52:55], v[116:123], v4 abid:1\n"
        "s_add_u32 s37, s35, 0\n"
        "s_cmp_lt_u32 s31, s14\n"
        "ds_read_b64_tr_b16 v[118:119], v9 offset:3328\n"
        "v_mov_b64 v[42:43], 0\n"
        "v_smfmac_f32_16x16x64_bf16 v[20:23], v[128:131], v[104:111], v4\n"
        "s_cselect_b32 s68, s68, 0\n"
        "v_cvt_pk_bf16_f32 v154, v72, v64\n"
        "v_smfmac_f32_16x16x64_bf16 v[20:23], v[52:55], v[94:101], v4 abid:1\n"
        "s_and_b32 s38, s31, 7\n"
        "ds_read_b64_tr_b16 v[96:97], v9 offset:3344\n"
        "v_cvt_pk_bf16_f32 v160, v125, v89\n"
        "v_smfmac_f32_16x16x64_bf16 v[40:43], v[128:131], v[28:35], v4\n"
        "s_cselect_b32 s30, 1, -7\n"
        "v_cvt_pk_bf16_f32 v88, v126, v90\n"
        "v_smfmac_f32_16x16x64_bf16 v[40:43], v[52:55], v[144:151], v4 abid:1\n"
        "buffer_store_dwordx3 v[152:154], v12, s[8:11], s37 offen sc0 sc1 nt\n"
        "ds_read_b64_tr_b16 v[146:147], v9 offset:3360\n"
        "v_cvt_pk_bf16_f32 v164, v127, v91\n"
        "v_cvt_pk_bf16_f32 v161, v57, v113\n"
        "v_cvt_pk_bf16_f32 v89, v58, v114\n"
        "v_cvt_pk_bf16_f32 v165, v59, v115\n"
        "v_cvt_pk_bf16_f32 v162, v73, v65\n"
        "v_mfma_f32_16x16x32_bf16 v[152:155], v[68:71], v[44:47], 0\n"
        "s_waitcnt lgkmcnt(2)\n"
        "v_cvt_pk_bf16_f32 v90, v74, v66\n"
        "v_cvt_pk_bf16_f32 v166, v75, v67\n"
        "v_smfmac_f32_16x16x64_bf16 v[152:155], v[24:27], v[116:123], v18\n"
        "s_mul_i32 s29, s30, 192\n"
        "s_mul_i32 s30, s30, 392\n"
        "ds_read_b64_tr_b16 v[118:119], v9 offset:3376\n"
        "v_add_f32 v60, v60, v61\n"
        "v_add_f32 v62, v62, v63\n"
        "v_add_f32 v20, v20, v21\n"
        "v_mfma_f32_16x16x32_bf16 v[56:59], v[68:71], v[104:107], 0\n"
        "s_waitcnt lgkmcnt(2)\n"
        "v_add_f32 v22, v22, v23\n"
        "v_cvt_pk_bf16_f32 v112, v60, v20\n"
        "v_smfmac_f32_16x16x64_bf16 v[56:59], v[24:27], v[94:101], v18\n"
        "s_add_u32 s37, s35, 0\n"
        "s_cmp_eq_u32 s69, s18\n"
        "ds_read_b64_tr_b16 v[96:97], v9 offset:3392\n"
        "v_add_f32 v40, v40, v41\n"
        "v_add_f32 v42, v42, v43\n"
        "v_cvt_pk_bf16_f32 v113, v40, v62\n"
        "v_mfma_f32_16x16x32_bf16 v[72:75], v[68:71], v[28:31], 0\n"
        "s_waitcnt lgkmcnt(2)\n"
        "v_cvt_pk_bf16_f32 v114, v22, v42\n"
        "v_mul_f32 v142, v142, v78\n"
        "v_smfmac_f32_16x16x64_bf16 v[72:75], v[24:27], v[144:151], v18\n"
        "buffer_store_dwordx3 v[160:162], v15, s[8:11], s37 offen offset:768 sc0 sc1 nt\n"
        "ds_read_b64_tr_b16 v[146:147], v9 offset:3408\n"
        "s_cselect_b32 s34, -7488, s70\n"
        "v_mul_f32 v143, v143, v79\n"
        "v_mul_f32 v156, v156, v80\n"
        "v_mul_f32 v157, v157, v81\n"
        "v_mul_f32 v158, v158, v82\n"
        "v_mfma_f32_16x16x32_bf16 v[20:23], v[68:71], v[48:51], 0\n"
        "s_waitcnt lgkmcnt(2)\n"
        "s_add_u32 s69, s69, s34\n"
        "v_mul_f32 v159, v159, v83\n"
        "v_smfmac_f32_16x16x64_bf16 v[20:23], v[24:27], v[116:123], v18 abid:1\n"
        "s_add_u32 s37, s35, 98112\n"
        "ds_read_b64_tr_b16 v[118:119], v9 offset:6656\n"
        "v_fma_f32 v92, v92, v84, v19\n"
        "v_mfma_f32_16x16x32_bf16 v[84:87], v[68:71], v[108:111], 0\n"
        "s_waitcnt lgkmcnt(2)\n"
        "v_perm_b32 v80, v37, v36, s63\n"
        "v_perm_b32 v81, v39, v38, s63\n"
        "v_smfmac_f32_16x16x64_bf16 v[84:87], v[24:27], v[94:101], v18 abid:1\n"
        "ds_read_b64_tr_b16 v[96:97], v9 offset:6672\n"
        "v_perm_b32 v82, v141, v140, s63\n"
        "v_mfma_f32_16x16x32_bf16 v[60:63], v[68:71], v[32:35], 0\n"
        "s_waitcnt lgkmcnt(2)\n"
        "v_perm_b32 v83, v143, v142, s63\n"
        "v_perm_b32 v76, v157, v156, s63\n"
        "v_smfmac_f32_16x16x64_bf16 v[60:63], v[24:27], v[144:151], v18 abid:1\n"
        "buffer_store_dwordx3 v[88:90], v12, s[8:11], s37 offen offset:192 sc0 sc1 nt\n"
        "ds_read_b64_tr_b16 v[146:147], v9 offset:6688\n"
        "ds_read_b32 v167, v0 offset:21604\n"
        "v_perm_b32 v78, v159, v158, s63\n"
        "v_perm_b32 v77, 0, v92, s63\n"
        "v_mov_b32_dpp v79, v19 row_shr:4 row_mask:0xf bank_mask:0xf bound_ctrl:0\n"
        "v_mfma_f32_16x16x32_bf16 v[68:71], v[80:83], v[44:47], 0\n"
        "s_waitcnt lgkmcnt(3)\n"
        "v_writelane_b32 v10, s69, 56\n"
        "v_add_u32 v11, s30, v11\n"
        "v_smfmac_f32_16x16x64_bf16 v[68:71], v[76:79], v[116:123], v18\n"
        "s_add_u32 s37, s35, 98112\n"
        "ds_read_b64_tr_b16 v[118:119], v9 offset:6704\n"
        "v_mfma_f32_16x16x32_bf16 v[36:39], v[80:83], v[104:107], 0\n"
        "s_waitcnt lgkmcnt(3)\n"
        "v_smfmac_f32_16x16x64_bf16 v[36:39], v[76:79], v[94:101], v18\n"
        "ds_read_b64_tr_b16 v[96:97], v9 offset:6720\n"
        "v_mfma_f32_16x16x32_bf16 v[40:43], v[80:83], v[28:31], 0\n"
        "s_waitcnt lgkmcnt(3)\n"
        "s_waitcnt lgkmcnt(2)\n"
        "v_readfirstlane_b32 s40, v167\n"
        "v_smfmac_f32_16x16x64_bf16 v[40:43], v[76:79], v[144:151], v18\n"
        "buffer_store_dwordx3 v[164:166], v14, s[8:11], s37 offen offset:960 sc0 sc1 nt\n"
        "ds_read_b64_tr_b16 v[146:147], v9 offset:6736\n"
        "s_add_u32 s37, s35, 588672\n"
        "s_add_u32 s46, s37, 1344\n"
        "s_cmp_lt_i32 s40, s68\n"
        "s_mov_b64 exec, 1\n"
        "ds_inc_u32 v0, v1 offset:21608\n"
        "s_mov_b64 exec, -1\n"
        "s_cbranch_scc1 L38A_%=\n"
        "L39A_%=:\n"
        "ds_read_b128 v[124:127], v7 offset:40064\n"
        "v_mfma_f32_16x16x32_bf16 v[64:67], v[80:83], v[48:51], 0\n"
        "s_waitcnt lgkmcnt(4)\n"
        "ds_read_b128 v[132:135], v7 offset:43392\n"
        "v_smfmac_f32_16x16x64_bf16 v[64:67], v[76:79], v[116:123], v18 abid:1\n"
        "buffer_store_dwordx3 v[112:114], v12, s[44:47], s37 offen offset:1152 sc0 sc1 nt\n"
        "ds_read_b128 v[128:131], v7 offset:40128\n"
        "ds_read_b128 v[136:139], v7 offset:43456\n"
        "ds_read_b128 v[160:163], v7 offset:40192\n"
        "ds_read_b128 v[52:55], v7 offset:43520\n"
        "v_mfma_f32_16x16x32_bf16 v[104:107], v[80:83], v[108:111], 0\n"
        "s_waitcnt lgkmcnt(7)\n"
        "s_mov_b32 s36, s35\n"
        "v_smfmac_f32_16x16x64_bf16 v[104:107], v[76:79], v[94:101], v18 abid:1\n"
        "s_mov_b64 exec, s[54:55]\n"
        "v_add_u32 v16, s29, v16\n"
        "v_add_u32 v8, s34, v8\n"
        "s_mov_b64 exec, -1\n"
        "v_mfma_f32_16x16x32_bf16 v[88:91], v[80:83], v[32:35], 0\n"
        "s_mov_b32 s35, s39\n"
        "v_smfmac_f32_16x16x64_bf16 v[88:91], v[76:79], v[144:151], v18 abid:1\n"
        "s_mov_b64 exec, s[50:51]\n"
        "v_add_u32 v5, s29, v5\n"
        "v_add_u32 v6, s34, v6\n"
        "s_mov_b64 exec, -1\n"
        "s_setprio 0\n"
        "CNEXTA_%=:\n"
        "s_cmp_ge_u32 s31, s14\n"
        "s_cbranch_scc1 CEND_%=\n"
        "s_waitcnt lgkmcnt(5)\n"
        "v_mfma_f32_16x16x32_bf16 v[24:27], v[168:171], v[124:127], 0\n"
        "v_cvt_pk_bf16_f32 v156, v152, v56\n"
        "v_cvt_pk_bf16_f32 v157, v72, v20\n"
        "s_waitcnt lgkmcnt(3)\n"
        "v_mfma_f32_16x16x32_bf16 v[24:27], v[172:175], v[128:131], v[24:27]\n"
        "ds_read_b128 v[44:47], v16 offset:128\n"
        "v_cvt_pk_bf16_f32 v158, v84, v60\n"
        "s_waitcnt lgkmcnt(2)\n"
        "v_mfma_f32_16x16x32_bf16 v[24:27], v[176:179], v[160:163], v[24:27]\n"
        "ds_read_b128 v[48:51], v5\n"
        "v_cvt_pk_bf16_f32 v164, v153, v57\n"
        "v_mfma_f32_16x16x32_bf16 v[32:35], v[188:191], v[160:163], 0\n"
        "v_cvt_pk_bf16_f32 v165, v73, v21\n"
        "v_cvt_pk_bf16_f32 v166, v85, v61\n"
        "v_mfma_f32_16x16x32_bf16 v[32:35], v[184:187], v[128:131], v[32:35]\n"
        "v_cvt_pk_bf16_f32 v146, v154, v58\n"
        "v_cvt_pk_bf16_f32 v147, v74, v22\n"
        "v_mfma_f32_16x16x32_bf16 v[32:35], v[180:183], v[124:127], v[32:35]\n"
        "s_add_u32 s37, s36, 196224\n"
        "ds_read_b128 v[92:95], v7 offset:46720\n"
        "ds_read_b128 v[96:99], v7 offset:46784\n"
        "ds_read_b128 v[116:119], v7 offset:46848\n"
        "v_mfma_f32_16x16x32_bf16 v[76:79], v[192:195], v[124:127], 0\n"
        "v_perm_b32 v80, v25, v24, s63\n"
        "v_perm_b32 v81, v27, v26, s63\n"
        "v_mfma_f32_16x16x32_bf16 v[76:79], v[196:199], v[128:131], v[76:79]\n"
        "v_perm_b32 v82, v33, v32, s63\n"
        "v_perm_b32 v83, v35, v34, s63\n"
        "v_mfma_f32_16x16x32_bf16 v[76:79], v[200:203], v[160:163], v[76:79]\n"
        "buffer_store_dwordx3 v[156:158], v12, s[8:11], s37 offen offset:384 sc0 sc1 nt\n"
        "v_cvt_pk_bf16_f32 v148, v86, v62\n"
        "v_cvt_pk_bf16_f32 v150, v155, v59\n"
        "v_cvt_pk_bf16_f32 v151, v75, v23\n"
        "v_cvt_pk_bf16_f32 v152, v87, v63\n"
        "s_waitcnt lgkmcnt(3)\n"
        "v_mfma_f32_16x16x32_bf16 v[32:35], v[44:47], v[160:163], 0\n"
        "v_cvt_pk_bf16_f32 v72, v68, v36\n"
        "v_perm_b32 v84, v77, v76, s63\n"
        "v_smfmac_f32_16x16x64_bf16 v[32:35], v[48:51], v[124:131], v4\n"
        "s_add_u32 s37, s36, 196224\n"
        "v_perm_b32 v85, v79, v78, s63\n"
        "v_mfma_f32_16x16x32_bf16 v[24:27], v[44:47], v[52:55], 0\n"
        "v_cvt_pk_bf16_f32 v73, v40, v64\n"
        "v_cvt_pk_bf16_f32 v74, v104, v88\n"
        "v_smfmac_f32_16x16x64_bf16 v[24:27], v[48:51], v[132:139], v4\n"
        "s_waitcnt lgkmcnt(0)\n"
        "s_setprio 2\n"
        "v_cvt_pk_bf16_f32 v156, v69, v37\n"
        "v_cvt_pk_bf16_f32 v157, v41, v65\n"
        "v_mfma_f32_16x16x32_bf16 v[100:103], v[44:47], v[116:119], 0\n"
        "v_cvt_pk_bf16_f32 v158, v105, v89\n"
        "v_cvt_pk_bf16_f32 v76, v70, v38\n"
        "v_smfmac_f32_16x16x64_bf16 v[100:103], v[48:51], v[92:99], v4\n"
        "buffer_store_dwordx3 v[164:166], v13, s[8:11], s37 offen offset:1152 sc0 sc1 nt\n"
        "v_cvt_pk_bf16_f32 v77, v42, v66\n"
        "v_cvt_pk_bf16_f32 v78, v106, v90\n"
        "v_cvt_pk_bf16_f32 v88, v71, v39\n"
        "v_cvt_pk_bf16_f32 v89, v43, v67\n"
        "v_cvt_pk_bf16_f32 v90, v107, v91\n"
        "v_mfma_f32_16x16x32_bf16 v[20:23], v[212:215], v[160:163], 0\n"
        "s_add_u32 s37, s36, 294336\n"
        "ds_read_b64 v[154:155], v11 offset:192\n"
        "v_mfma_f32_16x16x32_bf16 v[20:23], v[208:211], v[128:131], v[20:23]\n"
        "ds_read_b32 v153, v10\n"
        "v_mfma_f32_16x16x32_bf16 v[20:23], v[204:207], v[124:127], v[20:23]\n"
        "v_mfma_f32_16x16x32_bf16 v[28:31], v[216:219], v[124:127], 0\n"
        "v_mfma_f32_16x16x32_bf16 v[28:31], v[220:223], v[128:131], v[28:31]\n"
        "v_mfma_f32_16x16x32_bf16 v[28:31], v[224:227], v[160:163], v[28:31]\n"
        "buffer_store_dwordx3 v[146:148], v12, s[8:11], s37 offen offset:576 sc0 sc1 nt\n"
        "s_add_u32 s37, s36, 392448\n"
        "s_waitcnt lgkmcnt(1)\n"
        "v_add_f32_dpp v155, v34, v35 quad_perm:[0,1,2,3] row_mask:0x1 bank_mask:0xf\n"
        "v_add_f32_dpp v154, v32, v33 quad_perm:[0,1,2,3] row_mask:0x1 bank_mask:0xf\n"
        "v_perm_b32 v86, v21, v20, s63\n"
        "v_mfma_f32_16x16x32_bf16 v[44:47], v[236:239], v[160:163], 0\n"
        "v_perm_b32 v87, v23, v22, s63\n"
        "v_perm_b32 v20, v29, v28, s63\n"
        "v_mfma_f32_16x16x32_bf16 v[44:47], v[232:235], v[128:131], v[44:47]\n"
        "v_perm_b32 v21, v31, v30, s63\n"
        "v_add_f32_dpp v155, v26, v27 quad_perm:[0,1,2,3] row_mask:0x2 bank_mask:0xf\n"
        "v_mfma_f32_16x16x32_bf16 v[44:47], v[228:231], v[124:127], v[44:47]\n"
        "buffer_store_dwordx3 v[150:152], v12, s[8:11], s37 offen sc0 sc1 nt\n"
        "v_add_f32_dpp v154, v24, v25 quad_perm:[0,1,2,3] row_mask:0x2 bank_mask:0xf\n"
        "v_add_f32_dpp v155, v102, v103 quad_perm:[0,1,2,3] row_mask:0x4 bank_mask:0xf\n"
        "v_mul_f32 v165, s52, v155\n"
        "v_add_f32_dpp v154, v100, v101 quad_perm:[0,1,2,3] row_mask:0x4 bank_mask:0xf\n"
        "s_waitcnt lgkmcnt(0)\n"
        "v_fma_f32 v165, -v165, v155, v153\n"
        "v_add_f32_e64 v165, s53, |v165|\n"
        "v_perm_b32 v23, v47, v46, s63\n"
        "v_perm_b32 v22, v45, v44, s63\n"
        "v_mfma_f32_16x16x32_bf16 v[100:103], v[124:127], v[80:83], 0\n"
        "v_rsq_f32 v153, v165\n"
        "v_mfma_f32_16x16x32_bf16 v[100:103], v[128:131], v[84:87], v[100:103]\n"
        "s_add_u32 s37, s36, 392448\n"
        "ds_read_b128 v[104:107], v8 offset:128\n"
        "v_and_b32 v56, s28, v153\n"
        "v_mfma_f32_16x16x32_bf16 v[100:103], v[160:163], v[20:23], v[100:103]\n"
        "ds_read2_b32 v[108:109], v6 offset0:0 offset1:2\n"
        "ds_read2_b32 v[110:111], v6 offset0:8 offset1:10\n"
        "v_mfma_f32_16x16x32_bf16 v[144:147], v[52:55], v[20:23], 0\n"
        "v_and_b32 v164, s28, v153\n"
        "v_sub_f32 v167, v153, v56\n"
        "v_mfma_f32_16x16x32_bf16 v[144:147], v[136:139], v[84:87], v[144:147]\n"
        "v_cvt_pk_bf16_f32 v57, v167, v56\n"
        "v_cvt_pk_bf16_f32 v165, v164, v167\n"
        "v_mfma_f32_16x16x32_bf16 v[144:147], v[132:135], v[80:83], v[144:147]\n"
        "buffer_store_dwordx3 v[72:74], v15, s[8:11], s37 offen offset:768 sc0 sc1 nt\n"
        "v_mul_f32 v154, v154, v153\n"
        "v_readlane_b32 s58, v153, 56\n"
        "s_mov_b64 exec, s[56:57]\n"
        "v_mov_b32 v154, 0xff800000\n"
        "s_mov_b64 exec, -1\n"
        "v_mfma_f32_16x16x4_4b_bf16 v[32:47], v[56:57], v[164:165], 0 blgp:4\n"
        "v_mul_f32 v154, s58, v154\n"
        "v_exp_f32 v154, v154\n"
        "v_mfma_f32_16x16x32_bf16 v[24:27], v[92:95], v[80:83], 0\n"
        "s_add_u32 s37, s36, 490560\n"
        "v_add_f32_dpp v167, v154, v154 row_ror:8 row_mask:0xf bank_mask:0xf\n"
        "v_mul_f32 v154, v154, v153\n"
        "v_mfma_f32_16x16x32_bf16 v[24:27], v[96:99], v[84:87], v[24:27]\n"
        "v_add_f32_dpp v167, v167, v167 row_ror:4 row_mask:0xf bank_mask:0xf\n"
        "v_mfma_f32_16x16x32_bf16 v[24:27], v[116:119], v[20:23], v[24:27]\n"
        "s_waitcnt lgkmcnt(0)\n"
        "v_add_f32_dpp v167, v167, v167 row_ror:2 row_mask:0xf bank_mask:0xf\n"
        "v_mfma_f32_16x16x32_bf16 v[148:151], v[104:107], v[20:23], 0\n"
        "v_mul_f32 v100, v100, v32\n"
        "v_mul_f32 v101, v101, v33\n"
        "v_smfmac_f32_16x16x64_bf16 v[148:151], v[108:111], v[80:87], v4\n"
        "buffer_store_dwordx3 v[156:158], v12, s[8:11], s37 offen offset:192 sc0 sc1 nt\n"
        "v_mul_f32 v102, v102, v34\n"
        "v_mul_f32 v103, v103, v35\n"
        "v_exp_f32 v100, v100\n"
        "v_mul_f32 v144, v144, v36\n"
        "v_mfma_f32_16x16x32_bf16 v[20:23], v[168:171], v[132:135], 0\n"
        "s_add_u32 s37, s36, 490560\n"
        "v_exp_f32 v101, v101\n"
        "v_mfma_f32_16x16x32_bf16 v[20:23], v[172:175], v[136:139], v[20:23]\n"
        "v_exp_f32 v102, v102\n"
        "v_mfma_f32_16x16x32_bf16 v[20:23], v[176:179], v[52:55], v[20:23]\n"
        "v_exp_f32 v103, v103\n"
        "v_add_f32 v151, v100, v101\n"
        "v_mfma_f32_16x16x32_bf16 v[28:31], v[188:191], v[52:55], 0\n"
        "v_add_f32 v151, v151, v102\n"
        "v_add_f32 v151, v151, v103\n"
        "v_mfma_f32_16x16x32_bf16 v[28:31], v[184:187], v[136:139], v[28:31]\n"
        "v_mul_f32 v145, v145, v37\n"
        "v_mul_f32 v146, v146, v38\n"
        "v_mfma_f32_16x16x32_bf16 v[28:31], v[180:183], v[132:135], v[28:31]\n"
        "buffer_store_dwordx3 v[76:78], v14, s[8:11], s37 offen offset:960 sc0 sc1 nt\n"
        "v_perm_b32 v68, v21, v20, s63\n"
        "v_perm_b32 v69, v23, v22, s63\n"
        "v_mul_f32 v147, v147, v39\n"
        "v_exp_f32 v144, v144\n"
        "v_mfma_f32_16x16x32_bf16 v[20:23], v[192:195], v[132:135], 0\n"
        "s_add_u32 s37, s36, 588672\n"
        "v_exp_f32 v145, v145\n"
        "v_mfma_f32_16x16x32_bf16 v[20:23], v[196:199], v[136:139], v[20:23]\n"
        "v_perm_b32 v70, v29, v28, s63\n"
        "v_perm_b32 v71, v31, v30, s63\n"
        "v_mfma_f32_16x16x32_bf16 v[20:23], v[200:203], v[52:55], v[20:23]\n"
        "buffer_store_dwordx3 v[88:90], v12, s[8:11], s37 offen offset:384 sc0 sc1 nt\n"
        "v_exp_f32 v146, v146\n"
        "v_exp_f32 v147, v147\n"
        "v_add_f32 v151, v151, v144\n"
        "v_add_f32 v151, v151, v145\n"
        "v_mfma_f32_16x16x32_bf16 v[28:31], v[212:215], v[52:55], 0\n"
        "v_add_f32 v151, v151, v146\n"
        "v_add_f32 v151, v151, v147\n"
        "v_mfma_f32_16x16x32_bf16 v[28:31], v[208:211], v[136:139], v[28:31]\n"
        "v_perm_b32 v72, v21, v20, s63\n"
        "v_perm_b32 v73, v23, v22, s63\n"
        "v_mfma_f32_16x16x32_bf16 v[28:31], v[204:207], v[132:135], v[28:31]\n"
        "v_mul_f32 v24, v24, v40\n"
        "v_mul_f32 v25, v25, v41\n"
        "v_mul_f32 v26, v26, v42\n"
        "v_mfma_f32_16x16x32_bf16 v[20:23], v[216:219], v[132:135], 0\n"
        "v_mul_f32 v27, v27, v43\n"
        "v_add_f32 v148, v148, v149\n"
        "v_mfma_f32_16x16x32_bf16 v[20:23], v[220:223], v[136:139], v[20:23]\n"
        "v_exp_f32 v24, v24\n"
        "v_mfma_f32_16x16x32_bf16 v[20:23], v[224:227], v[52:55], v[20:23]\n"
        "v_perm_b32 v74, v29, v28, s63\n"
        "v_perm_b32 v75, v31, v30, s63\n"
        "v_add_f32 v151, v151, v24\n"
        "v_mfma_f32_16x16x32_bf16 v[64:67], v[236:239], v[52:55], 0\n"
        "v_exp_f32 v25, v25\n"
        "v_mfma_f32_16x16x32_bf16 v[64:67], v[232:235], v[136:139], v[64:67]\n"
        "v_exp_f32 v26, v26\n"
        "v_mfma_f32_16x16x32_bf16 v[64:67], v[228:231], v[132:135], v[64:67]\n"
        "v_perm_b32 v48, v21, v20, s63\n"
        "v_perm_b32 v49, v23, v22, s63\n"
        "v_mfma_f32_16x16x4_4b_bf16 v[28:43], v[56:57], v[164:165], 0 blgp:5\n"
        "v_exp_f32 v27, v27\n"
        "v_add_f32 v151, v151, v25\n"
        "v_add_f32 v151, v151, v26\n"
        "v_mul_f32 v148, v148, v44\n"
        "v_exp_f32 v148, v148\n"
        "v_perm_b32 v50, v65, v64, s63\n"
        "v_perm_b32 v51, v67, v66, s63\n"
        "v_mfma_f32_16x16x32_bf16 v[58:61], v[124:127], v[68:71], 0\n"
        "v_add_f32 v151, v151, v27\n"
        "v_add_f32_dpp v167, v167, v167 row_ror:1 row_mask:0xf bank_mask:0xf\n"
        "v_mfma_f32_16x16x32_bf16 v[58:61], v[128:131], v[72:75], v[58:61]\n"
        "v_add_f32_dpp v151, v148, v151 quad_perm:[0,1,2,3] row_mask:0x4 bank_mask:0xf\n"
        "v_mfma_f32_16x16x32_bf16 v[58:61], v[160:163], v[48:51], v[58:61]\n"
        "v_mfma_f32_16x16x32_bf16 v[112:115], v[52:55], v[48:51], 0\n"
        "v_permlane32_swap_b32 v151, v167\n"
        "v_mfma_f32_16x16x32_bf16 v[112:115], v[136:139], v[72:75], v[112:115]\n"
        "v_add_f32 v151, v151, v167\n"
        "v_mov_b32 v167, v151\n"
        "v_mfma_f32_16x16x32_bf16 v[112:115], v[132:135], v[68:71], v[112:115]\n"
        "v_mfma_f32_16x16x32_bf16 v[20:23], v[92:95], v[68:71], 0\n"
        "v_permlane16_swap_b32 v151, v167\n"
        "v_mfma_f32_16x16x32_bf16 v[20:23], v[96:99], v[72:75], v[20:23]\n"
        "v_add_f32 v151, v151, v167\n"
        "v_mul_f32 v58, v58, v28\n"
        "v_mfma_f32_16x16x32_bf16 v[20:23], v[116:119], v[48:51], v[20:23]\n"
        "v_rcp_f32 v123, v151\n"
        "v_mul_f32 v59, v59, v29\n"
        "v_and_b32 v158, s28, v123\n"
        "v_mfma_f32_16x16x32_bf16 v[62:65], v[104:107], v[48:51], 0\n"
        "v_sub_f32 v152, v123, v158\n"
        "v_cvt_pk_bf16_f32 v159, v158, v152\n"
        "v_smfmac_f32_16x16x64_bf16 v[62:65], v[108:111], v[68:75], v4\n"
        "v_min3_f32 v167, v151, v123, v153\n"
        "v_mfma_f32_16x16x32_bf16 v[48:51], v[168:171], v[92:95], 0\n"
        "v_mul_f32 v60, v60, v30\n"
        "v_mul_f32 v61, v61, v31\n"
        "v_mfma_f32_16x16x32_bf16 v[48:51], v[172:175], v[96:99], v[48:51]\n"
        "v_exp_f32 v58, v58\n"
        "v_mfma_f32_16x16x32_bf16 v[48:51], v[176:179], v[116:119], v[48:51]\n"
        "v_exp_f32 v59, v59\n"
        "v_exp_f32 v60, v60\n"
        "v_mfma_f32_16x16x32_bf16 v[68:71], v[188:191], v[116:119], 0\n"
        "v_exp_f32 v61, v61\n"
        "v_mfma_f32_16x16x32_bf16 v[68:71], v[184:187], v[96:99], v[68:71]\n"
        "v_add_f32 v155, v58, v59\n"
        "v_add_f32 v155, v155, v60\n"
        "v_mfma_f32_16x16x32_bf16 v[68:71], v[180:183], v[92:95], v[68:71]\n"
        "v_perm_b32 v44, v49, v48, s63\n"
        "v_perm_b32 v45, v51, v50, s63\n"
        "v_add_f32 v155, v155, v61\n"
        "v_mfma_f32_16x16x32_bf16 v[28:31], v[192:195], v[92:95], 0\n"
        "v_mul_f32 v112, v112, v32\n"
        "v_mul_f32 v113, v113, v33\n"
        "v_mfma_f32_16x16x32_bf16 v[28:31], v[196:199], v[96:99], v[28:31]\n"
        "v_mul_f32 v114, v114, v34\n"
        "v_perm_b32 v46, v69, v68, s63\n"
        "v_mfma_f32_16x16x32_bf16 v[28:31], v[200:203], v[116:119], v[28:31]\n"
        "v_perm_b32 v47, v71, v70, s63\n"
        "v_mul_f32 v115, v115, v35\n"
        "v_exp_f32 v112, v112\n"
        "v_mfma_f32_16x16x32_bf16 v[68:71], v[212:215], v[116:119], 0\n"
        "v_exp_f32 v113, v113\n"
        "v_mfma_f32_16x16x32_bf16 v[68:71], v[208:211], v[96:99], v[68:71]\n"
        "v_exp_f32 v114, v114\n"
        "v_mfma_f32_16x16x32_bf16 v[68:71], v[204:207], v[92:95], v[68:71]\n"
        "v_perm_b32 v48, v29, v28, s63\n"
        "v_perm_b32 v49, v31, v30, s63\n"
        "v_add_f32 v155, v155, v112\n"
        "v_mfma_f32_16x16x32_bf16 v[64:67], v[216:219], v[92:95], 0\n"
        "v_exp_f32 v115, v115\n"
        "v_mfma_f32_16x16x32_bf16 v[64:67], v[220:223], v[96:99], v[64:67]\n"
        "v_add_f32 v155, v155, v113\n"
        "v_add_f32 v155, v155, v114\n"
        "v_mfma_f32_16x16x32_bf16 v[64:67], v[224:227], v[116:119], v[64:67]\n"
        "v_perm_b32 v50, v69, v68, s63\n"
        "v_perm_b32 v51, v71, v70, s63\n"
        "v_add_f32 v155, v155, v115\n"
        "v_mul_f32 v20, v20, v36\n"
        "v_mfma_f32_16x16x32_bf16 v[28:31], v[236:239], v[116:119], 0\n"
        "v_mul_f32 v21, v21, v37\n"
        "v_mul_f32 v22, v22, v38\n"
        "v_mfma_f32_16x16x32_bf16 v[28:31], v[232:235], v[96:99], v[28:31]\n"
        "v_perm_b32 v150, v65, v64, s63\n"
        "v_perm_b32 v151, v67, v66, s63\n"
        "v_mfma_f32_16x16x32_bf16 v[28:31], v[228:231], v[92:95], v[28:31]\n"
        "v_exp_f32 v20, v20\n"
        "v_mfma_f32_16x16x4_4b_bf16 v[76:91], v[56:57], v[158:159], 0 blgp:4\n"
        "v_exp_f32 v21, v21\n"
        "v_mul_f32 v23, v23, v39\n"
        "v_exp_f32 v22, v22\n"
        "v_exp_f32 v23, v23\n"
        "v_add_f32 v155, v155, v20\n"
        "v_add_f32 v155, v155, v21\n"
        "v_perm_b32 v152, v29, v28, s63\n"
        "v_perm_b32 v153, v31, v30, s63\n"
        "v_mfma_f32_16x16x32_bf16 v[36:39], v[124:127], v[44:47], 0\n"
        "v_add_f32 v155, v155, v22\n"
        "v_add_f32 v155, v155, v23\n"
        "v_mfma_f32_16x16x32_bf16 v[36:39], v[128:131], v[48:51], v[36:39]\n"
        "v_mul_f32 v100, v100, v76\n"
        "v_mul_f32 v101, v101, v77\n"
        "v_mfma_f32_16x16x32_bf16 v[36:39], v[160:163], v[150:153], v[36:39]\n"
        "v_mul_f32 v102, v102, v78\n"
        "v_mul_f32 v103, v103, v79\n"
        "v_mul_f32 v144, v144, v80\n"
        "v_mul_f32 v145, v145, v81\n"
        "v_mfma_f32_16x16x32_bf16 v[140:143], v[52:55], v[150:153], 0\n"
        "v_mul_f32 v146, v146, v82\n"
        "v_mul_f32 v147, v147, v83\n"
        "v_mfma_f32_16x16x32_bf16 v[140:143], v[136:139], v[48:51], v[140:143]\n"
        "v_mul_f32 v24, v24, v84\n"
        "v_mul_f32 v25, v25, v85\n"
        "v_mfma_f32_16x16x32_bf16 v[140:143], v[132:135], v[44:47], v[140:143]\n"
        "v_add_f32 v62, v62, v63\n"
        "v_mul_f32 v62, v62, v40\n"
        "v_mul_f32 v26, v26, v86\n"
        "v_mfma_f32_16x16x4_4b_bf16 v[64:79], v[56:57], v[164:165], 0 blgp:6\n"
        "v_exp_f32 v62, v62\n"
        "v_mul_f32 v27, v27, v87\n"
        "v_fma_f32 v148, v148, v88, v19\n"
        "v_add_f32_dpp v155, v62, v155 quad_perm:[0,1,2,3] row_mask:0x4 bank_mask:0xf\n"
        "v_perm_b32 v40, v101, v100, s63\n"
        "v_perm_b32 v41, v103, v102, s63\n"
        "v_perm_b32 v42, v145, v144, s63\n"
        "v_perm_b32 v43, v147, v146, s63\n"
        "v_perm_b32 v164, v25, v24, s63\n"
        "v_mfma_f32_16x16x32_bf16 v[156:159], v[92:95], v[44:47], 0\n"
        "v_perm_b32 v166, v27, v26, s63\n"
        "v_perm_b32 v165, 0, v148, s63\n"
        "v_mfma_f32_16x16x32_bf16 v[156:159], v[96:99], v[48:51], v[156:159]\n"
        "v_mul_f32 v36, v36, v64\n"
        "v_mul_f32 v37, v37, v65\n"
        "v_mfma_f32_16x16x32_bf16 v[156:159], v[116:119], v[150:153], v[156:159]\n"
        "v_mul_f32 v38, v38, v66\n"
        "v_mul_f32 v39, v39, v67\n"
        "v_mul_f32 v140, v140, v68\n"
        "v_mfma_f32_16x16x32_bf16 v[92:95], v[104:107], v[150:153], 0\n"
        "ds_read_b128 v[88:91], v17 offset:40064\n"
        "v_mul_f32 v141, v141, v69\n"
        "v_smfmac_f32_16x16x64_bf16 v[92:95], v[108:111], v[44:51], v4\n"
        "ds_read_b128 v[84:87], v17 offset:40128\n"
        "ds_read_b128 v[150:153], v17 offset:40192\n"
        "v_mfma_f32_16x16x32_bf16 v[48:51], v[124:127], v[240:243], 0\n"
        "v_exp_f32 v36, v36\n"
        "v_mfma_f32_16x16x32_bf16 v[48:51], v[128:131], v[244:247], v[48:51]\n"
        "ds_read_b128 v[80:83], v0 offset:5136\n"
        "v_mul_f32 v142, v142, v70\n"
        "v_mfma_f32_16x16x32_bf16 v[48:51], v[160:163], v[248:251], v[48:51]\n"
        "ds_read_b128 v[32:35], v0 offset:4112\n"
        "v_mul_f32 v143, v143, v71\n"
        "v_mfma_f32_16x16x32_bf16 v[24:27], v[52:55], v[248:251], 0\n"
        "v_exp_f32 v37, v37\n"
        "v_mfma_f32_16x16x32_bf16 v[24:27], v[136:139], v[244:247], v[24:27]\n"
        "v_exp_f32 v38, v38\n"
        "v_mfma_f32_16x16x32_bf16 v[24:27], v[132:135], v[240:243], v[24:27]\n"
        "s_waitcnt lgkmcnt(2)\n"
        "v_perm_b32 v44, v49, v48, s63\n"
        "v_perm_b32 v45, v51, v50, s63\n"
        "v_add_f32 v145, v36, v37\n"
        "v_mfma_f32_16x16x32_bf16 v[104:107], v[88:91], v[240:243], 0\n"
        "v_exp_f32 v39, v39\n"
        "v_mfma_f32_16x16x32_bf16 v[104:107], v[84:87], v[244:247], v[104:107]\n"
        "v_add_f32 v145, v145, v38\n"
        "v_perm_b32 v46, v25, v24, s63\n"
        "v_mfma_f32_16x16x32_bf16 v[104:107], v[150:153], v[248:251], v[104:107]\n"
        "s_waitcnt lgkmcnt(0)\n"
        "v_perm_b32 v47, v27, v26, s63\n"
        "v_add_f32 v145, v145, v39\n"
        "v_exp_f32 v140, v140\n"
        "v_mfma_f32_16x16x32_bf16 v[64:67], v[150:153], v[80:83], 0\n"
        "v_exp_f32 v141, v141\n"
        "v_mfma_f32_16x16x32_bf16 v[64:67], v[84:87], v[32:35], v[64:67]\n"
        "ds_read_b128 v[146:149], v0 offset:6176\n"
        "v_perm_b32 v116, v105, v104, s63\n"
        "v_mfma_f32_16x16x32_bf16 v[64:67], v[88:91], v[252:255], v[64:67]\n"
        "ds_read_b128 v[24:27], v0 offset:7200\n"
        "ds_read_b128 v[108:111], v0 offset:8224\n"
        "v_mfma_f32_16x16x32_bf16 v[100:103], v[132:135], v[252:255], 0\n"
        "v_perm_b32 v120, v107, v106, s63\n"
        "v_add_f32 v145, v145, v140\n"
        "v_mfma_f32_16x16x32_bf16 v[100:103], v[136:139], v[32:35], v[100:103]\n"
        "v_exp_f32 v142, v142\n"
        "v_mfma_f32_16x16x32_bf16 v[100:103], v[52:55], v[80:83], v[100:103]\n"
        "v_perm_b32 v94, v65, v64, s63\n"
        "v_perm_b32 v98, v67, v66, s63\n"
        "v_add_f32 v145, v145, v141\n"
        "v_mfma_f32_16x16x32_bf16 v[68:71], v[160:163], v[80:83], 0\n"
        "v_exp_f32 v143, v143\n"
        "v_mfma_f32_16x16x32_bf16 v[68:71], v[128:131], v[32:35], v[68:71]\n"
        "v_add_f32 v145, v145, v142\n"
        "v_add_f32 v145, v145, v143\n"
        "v_mfma_f32_16x16x32_bf16 v[68:71], v[124:127], v[252:255], v[68:71]\n"
        "s_waitcnt lgkmcnt(0)\n"
        "v_perm_b32 v106, v101, v100, s63\n"
        "v_perm_b32 v107, v103, v102, s63\n"
        "v_mul_f32 v156, v156, v72\n"
        "v_mul_f32 v157, v157, v73\n"
        "v_mfma_f32_16x16x32_bf16 v[32:35], v[124:127], v[146:149], 0\n"
        "v_mul_f32 v158, v158, v74\n"
        "v_mul_f32 v159, v159, v75\n"
        "v_mfma_f32_16x16x32_bf16 v[32:35], v[128:131], v[24:27], v[32:35]\n"
        "ds_read_b128 v[48:51], v0 offset:11312\n"
        "v_perm_b32 v104, v69, v68, s63\n"
        "v_mfma_f32_16x16x32_bf16 v[32:35], v[160:163], v[108:111], v[32:35]\n"
        "ds_read_b128 v[80:83], v0 offset:10288\n"
        "ds_read_b128 v[64:67], v0 offset:9264\n"
        "v_mfma_f32_16x16x32_bf16 v[100:103], v[52:55], v[108:111], 0\n"
        "v_perm_b32 v105, v71, v70, s63\n"
        "v_add_f32 v92, v92, v93\n"
        "v_mfma_f32_16x16x32_bf16 v[100:103], v[136:139], v[24:27], v[100:103]\n"
        "v_exp_f32 v156, v156\n"
        "v_mfma_f32_16x16x32_bf16 v[100:103], v[132:135], v[146:149], v[100:103]\n"
        "v_perm_b32 v28, v33, v32, s63\n"
        "v_perm_b32 v29, v35, v34, s63\n"
        "v_add_f32 v145, v145, v156\n"
        "v_mfma_f32_16x16x32_bf16 v[70:73], v[88:91], v[146:149], 0\n"
        "v_exp_f32 v157, v157\n"
        "v_mfma_f32_16x16x32_bf16 v[70:73], v[84:87], v[24:27], v[70:73]\n"
        "v_exp_f32 v158, v158\n"
        "v_mfma_f32_16x16x32_bf16 v[70:73], v[150:153], v[108:111], v[70:73]\n"
        "s_waitcnt lgkmcnt(0)\n"
        "v_perm_b32 v30, v101, v100, s63\n"
        "v_perm_b32 v31, v103, v102, s63\n"
        "v_exp_f32 v159, v159\n"
        "v_mfma_f32_16x16x32_bf16 v[32:35], v[150:153], v[48:51], 0\n"
        "v_add_f32 v145, v145, v157\n"
        "v_add_f32 v145, v145, v158\n"
        "v_mfma_f32_16x16x32_bf16 v[32:35], v[84:87], v[80:83], v[32:35]\n"
        "ds_read_b128 v[100:103], v0 offset:12352\n"
        "v_perm_b32 v144, v71, v70, s63\n"
        "v_mfma_f32_16x16x32_bf16 v[32:35], v[88:91], v[64:67], v[32:35]\n"
        "ds_read_b128 v[108:111], v0 offset:13376\n"
        "ds_read_b128 v[24:27], v0 offset:14400\n"
        "v_mfma_f32_16x16x32_bf16 v[68:71], v[132:135], v[64:67], 0\n"
        "v_perm_b32 v148, v73, v72, s63\n"
        "v_mul_f32 v92, v92, v76\n"
        "v_mfma_f32_16x16x32_bf16 v[68:71], v[136:139], v[80:83], v[68:71]\n"
        "v_add_f32 v145, v145, v159\n"
        "v_readlane_b32 s59, v123, 32\n"
        "v_mfma_f32_16x16x32_bf16 v[68:71], v[52:55], v[48:51], v[68:71]\n"
        "v_perm_b32 v117, v33, v32, s63\n"
        "v_perm_b32 v121, v35, v34, s63\n"
        "v_exp_f32 v92, v92\n"
        "v_mfma_f32_16x16x32_bf16 v[72:75], v[160:163], v[48:51], 0\n"
        "v_mul_f32 v154, s59, v154\n"
        "v_add_f32_dpp v145, v92, v145 quad_perm:[0,1,2,3] row_mask:0x4 bank_mask:0xf\n"
        "v_mfma_f32_16x16x32_bf16 v[72:75], v[128:131], v[80:83], v[72:75]\n"
        "v_perm_b32 v154, 0, v154, s63\n"
        "v_perm_b32 v50, v69, v68, s63\n"
        "v_mfma_f32_16x16x32_bf16 v[72:75], v[124:127], v[64:67], v[72:75]\n"
        "s_waitcnt lgkmcnt(0)\n"
        "v_perm_b32 v51, v71, v70, s63\n"
        "v_permlane32_swap_b32 v155, v145\n"
        "v_mfma_f32_16x16x32_bf16 v[80:83], v[124:127], v[100:103], 0\n"
        "v_add_f32 v155, v155, v145\n"
        "v_mov_b32 v149, v155\n"
        "v_mfma_f32_16x16x32_bf16 v[80:83], v[128:131], v[108:111], v[80:83]\n"
        "ds_read_b128 v[32:35], v0 offset:17488\n"
        "v_perm_b32 v48, v73, v72, s63\n"
        "v_mfma_f32_16x16x32_bf16 v[80:83], v[160:163], v[24:27], v[80:83]\n"
        "ds_read_b128 v[68:71], v0 offset:16464\n"
        "ds_read_b128 v[64:67], v0 offset:15440\n"
        "v_mfma_f32_16x16x32_bf16 v[76:79], v[52:55], v[24:27], 0\n"
        "v_perm_b32 v49, v75, v74, s63\n"
        "v_mfma_f32_16x16x32_bf16 v[76:79], v[136:139], v[108:111], v[76:79]\n"
        "v_permlane16_swap_b32 v155, v149\n"
        "v_mfma_f32_16x16x32_bf16 v[76:79], v[132:135], v[100:103], v[76:79]\n"
        "v_add_f32 v155, v155, v149\n"
        "v_rcp_f32 v147, v155\n"
        "v_mfma_f32_16x16x32_bf16 v[72:75], v[88:91], v[100:103], 0\n"
        "ds_read2_b32 v[122:123], v11 offset0:0 offset1:3\n"
        "v_min3_f32 v167, v167, v155, v147\n"
        "v_mfma_f32_16x16x32_bf16 v[72:75], v[84:87], v[108:111], v[72:75]\n"
        "ds_read_b64_tr_b16 v[118:119], v9 offset:40064\n"
        "v_and_b32 v102, s28, v147\n"
        "v_mfma_f32_16x16x32_bf16 v[72:75], v[150:153], v[24:27], v[72:75]\n"
        "s_waitcnt lgkmcnt(2)\n"
        "ds_read2_b32 v[100:101], v11 offset0:1 offset1:4\n"
        "v_sub_f32 v149, v147, v102\n"
        "v_cvt_pk_bf16_f32 v103, v102, v149\n"
        "v_perm_b32 v108, v81, v80, s63\n"
        "v_mfma_f32_16x16x32_bf16 v[24:27], v[150:153], v[32:35], 0\n"
        "ds_read_b64_tr_b16 v[96:97], v9 offset:40080\n"
        "v_perm_b32 v109, v83, v82, s63\n"
        "v_mfma_f32_16x16x32_bf16 v[24:27], v[84:87], v[68:71], v[24:27]\n"
        "ds_read2_b32 v[150:151], v11 offset0:2 offset1:5\n"
        "v_perm_b32 v110, v77, v76, s63\n"
        "v_mfma_f32_16x16x32_bf16 v[24:27], v[88:91], v[64:67], v[24:27]\n"
        "ds_read_b64_tr_b16 v[146:147], v9 offset:40096\n"
        "v_perm_b32 v111, v79, v78, s63\n"
        "v_perm_b32 v95, v73, v72, s63\n"
        "v_mfma_f32_16x16x4_4b_bf16 v[76:91], v[56:57], v[102:103], 0 blgp:4\n"
        "v_perm_b32 v99, v75, v74, s63\n"
        "v_cmp_nle_f32 vcc, 0x2b800000, v167\n"
        "v_mov_b32_dpp v167, v19 row_shr:4 row_mask:0xf bank_mask:0xf bound_ctrl:0\n"
        "ds_write_b16 v1, v154\n"
        "v_perm_b32 v145, v25, v24, s63\n"
        "v_perm_b32 v149, v27, v26, s63\n"
        "s_andn2_b64 vcc, vcc, s[56:57]\n"
        "s_cbranch_scc1 CREDOB_%=\n"
        "v_mfma_f32_16x16x32_bf16 v[24:27], v[132:135], v[64:67], 0\n"
        "s_mov_b32 exec_hi, 0xffff\n"
        "v_add_u32 v10, -40064, v10\n"
        "s_mov_b32 exec_hi, -1\n"
        "v_mfma_f32_16x16x32_bf16 v[24:27], v[136:139], v[68:71], v[24:27]\n"
        "v_mul_f32 v58, v58, v76\n"
        "v_mul_f32 v59, v59, v77\n"
        "v_mfma_f32_16x16x32_bf16 v[24:27], v[52:55], v[32:35], v[24:27]\n"
        "s_add_u32 s31, s31, 1\n"
        "v_mul_f32 v60, v60, v78\n"
        "v_mul_f32 v61, v61, v79\n"
        "v_mul_f32 v112, v112, v80\n"
        "v_mul_f32 v113, v113, v81\n"
        "v_mfma_f32_16x16x32_bf16 v[52:55], v[160:163], v[32:35], 0\n"
        "s_cmp_ge_u32 s31, s71\n"
        "v_mul_f32 v114, v114, v82\n"
        "v_mul_f32 v115, v115, v83\n"
        "v_mfma_f32_16x16x32_bf16 v[52:55], v[128:131], v[68:71], v[52:55]\n"
        "s_cbranch_scc1 L34B_%=\n"
        "L35B_%=:\n"
        "v_mul_f32 v20, v20, v84\n"
        "v_perm_b32 v34, v25, v24, s63\n"
        "v_mfma_f32_16x16x32_bf16 v[52:55], v[124:127], v[64:67], v[52:55]\n"
        "s_add_u32 s16, s16, 256\n"
        "v_perm_b32 v35, v27, v26, s63\n"
        "v_mul_f32 v21, v21, v85\n"
        "v_mfma_f32_16x16x32_bf16 v[124:127], v[40:43], v[44:47], 0\n"
        "s_waitcnt lgkmcnt(0)\n"
        "v_mul_f32 v22, v22, v86\n"
        "v_mul_f32 v23, v23, v87\n"
        "v_smfmac_f32_16x16x64_bf16 v[124:127], v[164:167], v[116:123], v18\n"
        "s_cmp_ge_u32 s31, 20\n"
        "s_cselect_b32 s39, s22, s16\n"
        "ds_read_b64_tr_b16 v[118:119], v9 offset:40112\n"
        "v_mfma_f32_16x16x4_4b_bf16 v[72:87], v[56:57], v[102:103], 0 blgp:6\n"
        "v_perm_b32 v32, v53, v52, s63\n"
        "v_perm_b32 v33, v55, v54, s63\n"
        "v_fma_f32 v62, v62, v88, v19\n"
        "v_perm_b32 v68, v59, v58, s63\n"
        "v_perm_b32 v69, v61, v60, s63\n"
        "v_perm_b32 v70, v113, v112, s63\n"
        "v_perm_b32 v71, v115, v114, s63\n"
        "v_perm_b32 v24, v21, v20, s63\n"
        "v_perm_b32 v26, v23, v22, s63\n"
        "v_mfma_f32_16x16x32_bf16 v[88:91], v[40:43], v[104:107], 0\n"
        "s_cmp_ge_u32 s31, 21\n"
        "v_perm_b32 v25, 0, v62, s63\n"
        "v_mul_f32 v36, v36, v72\n"
        "v_smfmac_f32_16x16x64_bf16 v[88:91], v[164:167], v[94:101], v18\n"
        "s_cselect_b32 s39, s23, s39\n"
        "ds_read_b64_tr_b16 v[96:97], v9 offset:40128\n"
        "v_mfma_f32_16x16x32_bf16 v[56:59], v[40:43], v[28:31], 0\n"
        "s_mul_hi_u32 s38, s39, 58835169\n"
        "ds_read_b128 v[128:131], v2\n"
        "v_mul_f32 v37, v37, v73\n"
        "v_smfmac_f32_16x16x64_bf16 v[56:59], v[164:167], v[144:151], v18\n"
        "s_mul_i32 s39, s39, 1344\n"
        "ds_read_b64_tr_b16 v[146:147], v9 offset:40144\n"
        "ds_read_b128 v[52:55], v3\n"
        "v_mfma_f32_16x16x32_bf16 v[112:115], v[40:43], v[48:51], 0\n"
        "s_waitcnt lgkmcnt(4)\n"
        "v_mul_f32 v38, v38, v74\n"
        "v_mul_f32 v39, v39, v75\n"
        "v_smfmac_f32_16x16x64_bf16 v[112:115], v[164:167], v[116:123], v18 abid:1\n"
        "s_mul_i32 s38, s38, 588672\n"
        "s_add_u32 s39, s39, s38\n"
        "v_mul_f32 v140, v140, v76\n"
        "v_cvt_pk_bf16_f32 v152, v124, v88\n"
        "v_mul_f32 v141, v141, v77\n"
        "v_mfma_f32_16x16x32_bf16 v[72:75], v[40:43], v[108:111], 0\n"
        "s_add_u32 s39, s39, s17\n"
        "v_mov_b64 v[60:61], 0\n"
        "s_waitcnt lgkmcnt(3)\n"
        "v_smfmac_f32_16x16x64_bf16 v[72:75], v[164:167], v[94:101], v18 abid:1\n"
        "s_mul_i32 s68, s31, 3\n"
        "v_mov_b64 v[62:63], 0\n"
        "v_cvt_pk_bf16_f32 v153, v56, v112\n"
        "v_mfma_f32_16x16x32_bf16 v[64:67], v[40:43], v[32:35], 0\n"
        "s_waitcnt lgkmcnt(1)\n"
        "v_mov_b64 v[20:21], 0\n"
        "v_mov_b64 v[22:23], 0\n"
        "v_smfmac_f32_16x16x64_bf16 v[64:67], v[164:167], v[144:151], v18 abid:1\n"
        "s_waitcnt lgkmcnt(0)\n"
        "s_bitcmp1_b32 s31, 0\n"
        "v_mov_b32_dpp v27, v19 row_shr:4 row_mask:0xf bank_mask:0xf bound_ctrl:0\n"
        "s_cselect_b32 s38, 3, 6\n"
        "v_smfmac_f32_16x16x64_bf16 v[60:63], v[128:131], v[44:51], v4\n"
        "s_add_u32 s68, s68, s38\n"
        "v_mov_b64 v[40:41], 0\n"
        "v_smfmac_f32_16x16x64_bf16 v[60:63], v[52:55], v[116:123], v4 abid:1\n"
        "s_add_u32 s37, s35, 0\n"
        "s_cmp_lt_u32 s31, s14\n"
        "ds_read_b64_tr_b16 v[118:119], v9 offset:43392\n"
        "v_mov_b64 v[42:43], 0\n"
        "v_smfmac_f32_16x16x64_bf16 v[20:23], v[128:131], v[104:111], v4\n"
        "s_cselect_b32 s68, s68, 0\n"
        "v_cvt_pk_bf16_f32 v154, v72, v64\n"
        "v_smfmac_f32_16x16x64_bf16 v[20:23], v[52:55], v[94:101], v4 abid:1\n"
        "s_and_b32 s38, s31, 7\n"
        "ds_read_b64_tr_b16 v[96:97], v9 offset:43408\n"
        "v_cvt_pk_bf16_f32 v160, v125, v89\n"
        "v_smfmac_f32_16x16x64_bf16 v[40:43], v[128:131], v[28:35], v4\n"
        "s_cselect_b32 s30, 1, -7\n"
        "v_cvt_pk_bf16_f32 v88, v126, v90\n"
        "v_smfmac_f32_16x16x64_bf16 v[40:43], v[52:55], v[144:151], v4 abid:1\n"
        "buffer_store_dwordx3 v[152:154], v12, s[8:11], s37 offen sc0 sc1 nt\n"
        "ds_read_b64_tr_b16 v[146:147], v9 offset:43424\n"
        "v_cvt_pk_bf16_f32 v164, v127, v91\n"
        "v_cvt_pk_bf16_f32 v161, v57, v113\n"
        "v_cvt_pk_bf16_f32 v89, v58, v114\n"
        "v_cvt_pk_bf16_f32 v165, v59, v115\n"
        "v_cvt_pk_bf16_f32 v162, v73, v65\n"
        "v_mfma_f32_16x16x32_bf16 v[152:155], v[68:71], v[44:47], 0\n"
        "s_waitcnt lgkmcnt(2)\n"
        "v_cvt_pk_bf16_f32 v90, v74, v66\n"
        "v_cvt_pk_bf16_f32 v166, v75, v67\n"
        "v_smfmac_f32_16x16x64_bf16 v[152:155], v[24:27], v[116:123], v18\n"
        "s_mul_i32 s29, s30, 192\n"
        "s_mul_i32 s30, s30, 392\n"
        "ds_read_b64_tr_b16 v[118:119], v9 offset:43440\n"
        "v_add_f32 v60, v60, v61\n"
        "v_add_f32 v62, v62, v63\n"
        "v_add_f32 v20, v20, v21\n"
        "v_mfma_f32_16x16x32_bf16 v[56:59], v[68:71], v[104:107], 0\n"
        "s_waitcnt lgkmcnt(2)\n"
        "v_add_f32 v22, v22, v23\n"
        "v_cvt_pk_bf16_f32 v112, v60, v20\n"
        "v_smfmac_f32_16x16x64_bf16 v[56:59], v[24:27], v[94:101], v18\n"
        "s_add_u32 s37, s35, 0\n"
        "s_cmp_eq_u32 s69, s18\n"
        "ds_read_b64_tr_b16 v[96:97], v9 offset:43456\n"
        "v_add_f32 v40, v40, v41\n"
        "v_add_f32 v42, v42, v43\n"
        "v_cvt_pk_bf16_f32 v113, v40, v62\n"
        "v_mfma_f32_16x16x32_bf16 v[72:75], v[68:71], v[28:31], 0\n"
        "s_waitcnt lgkmcnt(2)\n"
        "v_cvt_pk_bf16_f32 v114, v22, v42\n"
        "v_mul_f32 v142, v142, v78\n"
        "v_smfmac_f32_16x16x64_bf16 v[72:75], v[24:27], v[144:151], v18\n"
        "buffer_store_dwordx3 v[160:162], v15, s[8:11], s37 offen offset:768 sc0 sc1 nt\n"
        "ds_read_b64_tr_b16 v[146:147], v9 offset:43472\n"
        "s_cselect_b32 s34, -7488, s70\n"
        "v_mul_f32 v143, v143, v79\n"
        "v_mul_f32 v156, v156, v80\n"
        "v_mul_f32 v157, v157, v81\n"
        "v_mul_f32 v158, v158, v82\n"
        "v_mfma_f32_16x16x32_bf16 v[20:23], v[68:71], v[48:51], 0\n"
        "s_waitcnt lgkmcnt(2)\n"
        "s_add_u32 s69, s69, s34\n"
        "v_mul_f32 v159, v159, v83\n"
        "v_smfmac_f32_16x16x64_bf16 v[20:23], v[24:27], v[116:123], v18 abid:1\n"
        "s_add_u32 s37, s35, 98112\n"
        "ds_read_b64_tr_b16 v[118:119], v9 offset:46720\n"
        "v_fma_f32 v92, v92, v84, v19\n"
        "v_mfma_f32_16x16x32_bf16 v[84:87], v[68:71], v[108:111], 0\n"
        "s_waitcnt lgkmcnt(2)\n"
        "v_perm_b32 v80, v37, v36, s63\n"
        "v_perm_b32 v81, v39, v38, s63\n"
        "v_smfmac_f32_16x16x64_bf16 v[84:87], v[24:27], v[94:101], v18 abid:1\n"
        "ds_read_b64_tr_b16 v[96:97], v9 offset:46736\n"
        "v_perm_b32 v82, v141, v140, s63\n"
        "v_mfma_f32_16x16x32_bf16 v[60:63], v[68:71], v[32:35], 0\n"
        "s_waitcnt lgkmcnt(2)\n"
        "v_perm_b32 v83, v143, v142, s63\n"
        "v_perm_b32 v76, v157, v156, s63\n"
        "v_smfmac_f32_16x16x64_bf16 v[60:63], v[24:27], v[144:151], v18 abid:1\n"
        "buffer_store_dwordx3 v[88:90], v12, s[8:11], s37 offen offset:192 sc0 sc1 nt\n"
        "ds_read_b64_tr_b16 v[146:147], v9 offset:46752\n"
        "ds_read_b32 v167, v0 offset:21600\n"
        "v_perm_b32 v78, v159, v158, s63\n"
        "v_perm_b32 v77, 0, v92, s63\n"
        "v_mov_b32_dpp v79, v19 row_shr:4 row_mask:0xf bank_mask:0xf bound_ctrl:0\n"
        "v_mfma_f32_16x16x32_bf16 v[68:71], v[80:83], v[44:47], 0\n"
        "s_waitcnt lgkmcnt(3)\n"
        "v_writelane_b32 v10, s69, 56\n"
        "v_add_u32 v11, s30, v11\n"
        "v_smfmac_f32_16x16x64_bf16 v[68:71], v[76:79], v[116:123], v18\n"
        "s_add_u32 s37, s35, 98112\n"
        "ds_read_b64_tr_b16 v[118:119], v9 offset:46768\n"
        "v_mfma_f32_16x16x32_bf16 v[36:39], v[80:83], v[104:107], 0\n"
        "s_waitcnt lgkmcnt(3)\n"
        "v_smfmac_f32_16x16x64_bf16 v[36:39], v[76:79], v[94:101], v18\n"
        "ds_read_b64_tr_b16 v[96:97], v9 offset:46784\n"
        "v_mfma_f32_16x16x32_bf16 v[40:43], v[80:83], v[28:31], 0\n"
        "s_waitcnt lgkmcnt(3)\n"
        "s_waitcnt lgkmcnt(2)\n"
        "v_readfirstlane_b32 s40, v167\n"
        "v_smfmac_f32_16x16x64_bf16 v[40:43], v[76:79], v[144:151], v18\n"
        "buffer_store_dwordx3 v[164:166], v14, s[8:11], s37 offen offset:960 sc0 sc1 nt\n"
        "ds_read_b64_tr_b16 v[146:147], v9 offset:46800\n"
        "s_add_u32 s37, s35, 588672\n"
        "s_add_u32 s46, s37, 1344\n"
        "s_cmp_lt_i32 s40, s68\n"
        "s_mov_b64 exec, 1\n"
        "ds_inc_u32 v0, v1 offset:21612\n"
        "s_mov_b64 exec, -1\n"
        "s_cbranch_scc1 L38B_%=\n"
        "L39B_%=:\n"
        "ds_read_b128 v[124:127], v7 offset:0\n"
        "v_mfma_f32_16x16x32_bf16 v[64:67], v[80:83], v[48:51], 0\n"
        "s_waitcnt lgkmcnt(4)\n"
        "ds_read_b128 v[132:135], v7 offset:3328\n"
        "v_smfmac_f32_16x16x64_bf16 v[64:67], v[76:79], v[116:123], v18 abid:1\n"
        "buffer_store_dwordx3 v[112:114], v12, s[44:47], s37 offen offset:1152 sc0 sc1 nt\n"
        "ds_read_b128 v[128:131], v7 offset:64\n"
        "ds_read_b128 v[136:139], v7 offset:3392\n"
        "ds_read_b128 v[160:163], v7 offset:128\n"
        "ds_read_b128 v[52:55], v7 offset:3456\n"
        "v_mfma_f32_16x16x32_bf16 v[104:107], v[80:83], v[108:111], 0\n"
        "s_waitcnt lgkmcnt(7)\n"
        "s_mov_b32 s36, s35\n"
        "v_smfmac_f32_16x16x64_bf16 v[104:107], v[76:79], v[94:101], v18 abid:1\n"
        "s_mov_b64 exec, s[54:55]\n"
        "v_add_u32 v16, s29, v16\n"
        "v_add_u32 v8, s34, v8\n"
        "s_mov_b64 exec, -1\n"
        "v_mfma_f32_16x16x32_bf16 v[88:91], v[80:83], v[32:35], 0\n"
        "s_mov_b32 s35, s39\n"
        "v_smfmac_f32_16x16x64_bf16 v[88:91], v[76:79], v[144:151], v18 abid:1\n"
        "s_mov_b64 exec, s[50:51]\n"
        "v_add_u32 v5, s29, v5\n"
        "v_add_u32 v6, s34, v6\n"
        "s_mov_b64 exec, -1\n"
        "s_setprio 0\n"
        "CNEXTB_%=:\n"
        "s_cmp_ge_u32 s31, s14\n"
        "s_cbranch_scc1 CEND_%=\n"
        "s_cmp_gt_i32 s29, 0\n"
        "s_cbranch_scc1 CTOP_%=\n"
        "s_branch CFULL_%=\n"
        "CEND_%=:\n"
        "s_waitcnt lgkmcnt(0)\n"
        "s_nop 15\n"
        "v_cvt_pk_bf16_f32 v248, v152, v56\n"
        "v_cvt_pk_bf16_f32 v249, v72, v20\n"
        "v_cvt_pk_bf16_f32 v250, v84, v60\n"
        "s_add_u32 s37, s36, 196224\n"
        "buffer_store_dwordx3 v[248:250], v12, s[8:11], s37 offen offset:384 sc0 sc1 nt\n"
        "v_cvt_pk_bf16_f32 v252, v153, v57\n"
        "v_cvt_pk_bf16_f32 v253, v73, v21\n"
        "v_cvt_pk_bf16_f32 v254, v85, v61\n"
        "s_add_u32 s37, s36, 196224\n"
        "buffer_store_dwordx3 v[252:254], v13, s[8:11], s37 offen offset:1152 sc0 sc1 nt\n"
        "v_cvt_pk_bf16_f32 v248, v154, v58\n"
        "v_cvt_pk_bf16_f32 v249, v74, v22\n"
        "v_cvt_pk_bf16_f32 v250, v86, v62\n"
        "s_add_u32 s37, s36, 294336\n"
        "buffer_store_dwordx3 v[248:250], v12, s[8:11], s37 offen offset:576 sc0 sc1 nt\n"
        "v_cvt_pk_bf16_f32 v252, v155, v59\n"
        "v_cvt_pk_bf16_f32 v253, v75, v23\n"
        "v_cvt_pk_bf16_f32 v254, v87, v63\n"
        "s_add_u32 s37, s36, 392448\n"
        "buffer_store_dwordx3 v[252:254], v12, s[8:11], s37 offen sc0 sc1 nt\n"
        "v_cvt_pk_bf16_f32 v248, v68, v36\n"
        "v_cvt_pk_bf16_f32 v249, v40, v64\n"
        "v_cvt_pk_bf16_f32 v250, v104, v88\n"
        "s_add_u32 s37, s36, 392448\n"
        "v_cvt_pk_bf16_f32 v252, v69, v37\n"
        "v_cvt_pk_bf16_f32 v253, v41, v65\n"
        "v_cvt_pk_bf16_f32 v254, v105, v89\n"
        "buffer_store_dwordx3 v[248:250], v15, s[8:11], s37 offen offset:768 sc0 sc1 nt\n"
        "s_add_u32 s37, s36, 490560\n"
        "v_cvt_pk_bf16_f32 v246, v70, v38\n"
        "v_cvt_pk_bf16_f32 v247, v42, v66\n"
        "v_cvt_pk_bf16_f32 v248, v106, v90\n"
        "buffer_store_dwordx3 v[252:254], v12, s[8:11], s37 offen offset:192 sc0 sc1 nt\n"
        "s_add_u32 s37, s36, 490560\n"
        "buffer_store_dwordx3 v[246:248], v14, s[8:11], s37 offen offset:960 sc0 sc1 nt\n"
        "s_add_u32 s37, s36, 588672\n"
        "v_cvt_pk_bf16_f32 v252, v71, v39\n"
        "v_cvt_pk_bf16_f32 v253, v43, v67\n"
        "v_cvt_pk_bf16_f32 v254, v107, v91\n"
        "buffer_store_dwordx3 v[252:254], v12, s[8:11], s37 offen offset:384 sc0 sc1 nt\n"
        "s_nop 1\n"
        "s_endpgm\n"
        "L34A_%=:\n"
        "s_nop 7\n"
        "v_mov_b32 v153, 21600\n"
        "L32A_%=:\n"
        "ds_read_b32 v163, v153 offset:64\n"
        "s_waitcnt lgkmcnt(0)\n"
        "v_readfirstlane_b32 s40, v163\n"
        "s_nop 3\n"
        "s_cmp_ge_i32 s40, 1\n"
        "s_cbranch_scc1 L33A_%=\n"
        "s_sleep 1\n"
        "s_sub_u32 s41, s41, 1\n"
        "s_cbranch_scc0 L32A_%=\n"
        "s_endpgm\n"
        "L33A_%=:\n"
        "v_mbcnt_lo_u32_b32 v163, -1, 0\n"
        "v_lshl_add_u32 v153, v163, 2, v153\n"
        "ds_read_b32 v163, v153 offset:68\n"
        "s_mov_b32 s14, s40\n"
        "s_mov_b32 s71, -1\n"
        "s_waitcnt lgkmcnt(0)\n"
        "v_readlane_b32 s22, v163, 0\n"
        "v_readlane_b32 s23, v163, 1\n"
        "s_nop 3\n"
        "s_nop 1\n"
        "s_branch L35A_%=\n"
        "L38A_%=:\n"
        "s_nop 7\n"
        "L36A_%=:\n"
        "ds_read_b32 v167, v0 offset:21604\n"
        "s_waitcnt lgkmcnt(0)\n"
        "v_readfirstlane_b32 s40, v167\n"
        "s_nop 3\n"
        "s_cmp_ge_i32 s40, s68\n"
        "s_cbranch_scc1 L37A_%=\n"
        "s_sleep 1\n"
        "s_sub_u32 s41, s41, 1\n"
        "s_cbranch_scc0 L36A_%=\n"
        "s_endpgm\n"
        "L37A_%=:\n"
        "s_nop 1\n"
        "s_branch L39A_%=\n"
        "L34B_%=:\n"
        "s_nop 7\n"
        "v_mov_b32 v153, 21600\n"
        "L32B_%=:\n"
        "ds_read_b32 v163, v153 offset:64\n"
        "s_waitcnt lgkmcnt(0)\n"
        "v_readfirstlane_b32 s40, v163\n"
        "s_nop 3\n"
        "s_cmp_ge_i32 s40, 1\n"
        "s_cbranch_scc1 L33B_%=\n"
        "s_sleep 1\n"
        "s_sub_u32 s41, s41, 1\n"
        "s_cbranch_scc0 L32B_%=\n"
        "s_endpgm\n"
        "L33B_%=:\n"
        "v_mbcnt_lo_u32_b32 v163, -1, 0\n"
        "v_lshl_add_u32 v153, v163, 2, v153\n"
        "ds_read_b32 v163, v153 offset:68\n"
        "s_mov_b32 s14, s40\n"
        "s_mov_b32 s71, -1\n"
        "s_waitcnt lgkmcnt(0)\n"
        "v_readlane_b32 s22, v163, 0\n"
        "v_readlane_b32 s23, v163, 1\n"
        "s_nop 3\n"
        "s_nop 1\n"
        "s_branch L35B_%=\n"
        "L38B_%=:\n"
        "s_nop 7\n"
        "L36B_%=:\n"
        "ds_read_b32 v167, v0 offset:21600\n"
        "s_waitcnt lgkmcnt(0)\n"
        "v_readfirstlane_b32 s40, v167\n"
        "s_nop 3\n"
        "s_cmp_ge_i32 s40, s68\n"
        "s_cbranch_scc1 L37B_%=\n"
        "s_sleep 1\n"
        "s_sub_u32 s41, s41, 1\n"
        "s_cbranch_scc0 L36B_%=\n"
        "s_endpgm\n"
        "L37B_%=:\n"
        "s_nop 1\n"
        "s_branch L39B_%=\n"
        "CGRP_%=:\n"
        "s_setprio 3\n"
        "CGRQ_%=:\n"
        "s_waitcnt lgkmcnt(0)\n"
        "s_nop 6\n"
        "ds_read_u16 v167, v0 offset:20576\n"
        "s_lshr_b32 s61, s31, 3\n"
        "s_add_u32 s61, s61, 1\n"
        "L40_%=:\n"
        "ds_read_b32 v166, v0 offset:21660\n"
        "s_waitcnt lgkmcnt(0)\n"
        "v_readfirstlane_b32 s40, v166\n"
        "s_nop 3\n"
        "s_cmp_ge_i32 s40, s61\n"
        "s_cbranch_scc1 L41_%=\n"
        "s_sleep 1\n"
        "s_sub_u32 s41, s41, 1\n"
        "s_cbranch_scc0 L40_%=\n"
        "s_endpgm\n"
        "L41_%=:\n"
        "s_sub_u32 s60, s69, 102048\n"
        "v_add_u32 v167, s60, v167\n"
        "v_subrev_u32 v165, 8320, v167\n"
        "v_min_u32 v167, v167, v165\n"
        "v_add_u32 v167, 101856, v167\n"
        "ds_read_b128 v[28:31], v167 offset:64\n"
        "ds_read_b128 v[24:27], v167 offset:128\n"
        "ds_read_b128 v[32:35], v167\n"
        "s_waitcnt lgkmcnt(1)\n"
        "s_waitcnt lgkmcnt(0)\n"
        "v_mfma_f32_16x16x32_bf16 v[44:47], v[168:171], v[32:35], 0\n"
        "ds_read_u16 v162, v0 offset:20578\n"
        "v_lshrrev_b32 v159, 8, v0\n"
        "v_mfma_f32_16x16x32_bf16 v[44:47], v[172:175], v[28:31], v[44:47]\n"
        "ds_read_u16 v163, v0 offset:20580\n"
        "v_mfma_f32_16x16x32_bf16 v[44:47], v[176:179], v[24:27], v[44:47]\n"
        "ds_read_u16 v167, v0 offset:20582\n"
        "ds_read_u16 v158, v0 offset:20588\n"
        "ds_read_u16 v166, v0 offset:20590\n"
        "v_mfma_f32_16x16x32_bf16 v[52:55], v[180:183], v[32:35], 0\n"
        "v_mfma_f32_16x16x32_bf16 v[52:55], v[184:187], v[28:31], v[52:55]\n"
        "v_mfma_f32_16x16x32_bf16 v[52:55], v[188:191], v[24:27], v[52:55]\n"
        "v_mfma_f32_16x16x32_bf16 v[48:51], v[192:195], v[32:35], 0\n"
        "s_waitcnt lgkmcnt(4)\n"
        "v_add_u32 v162, s60, v162\n"
        "v_subrev_u32 v165, 8320, v162\n"
        "v_mfma_f32_16x16x32_bf16 v[48:51], v[196:199], v[28:31], v[48:51]\n"
        "v_min_u32 v162, v162, v165\n"
        "v_add_u32 v162, 101856, v162\n"
        "v_mfma_f32_16x16x32_bf16 v[48:51], v[200:203], v[24:27], v[48:51]\n"
        "ds_read_b64 v[160:161], v162\n"
        "v_perm_b32 v76, v45, v44, s63\n"
        "v_perm_b32 v77, v47, v46, s63\n"
        "v_perm_b32 v78, v53, v52, s63\n"
        "v_perm_b32 v79, v55, v54, s63\n"
        "s_waitcnt lgkmcnt(1)\n"
        "v_add_u32 v163, s19, v163\n"
        "v_mfma_f32_16x16x32_bf16 v[44:47], v[204:207], v[32:35], 0\n"
        "v_perm_b32 v80, v49, v48, s63\n"
        "v_perm_b32 v81, v51, v50, s63\n"
        "v_mfma_f32_16x16x32_bf16 v[44:47], v[208:211], v[28:31], v[44:47]\n"
        "ds_read_b64 v[164:165], v158\n"
        "v_mad_i32_i24 v159, v159, -8, v163\n"
        "v_mfma_f32_16x16x32_bf16 v[44:47], v[212:215], v[24:27], v[44:47]\n"
        "ds_read_b64 v[156:157], v162 offset:16\n"
        "v_add_u32 v166, s19, v166\n"
        "v_add_u32 v167, s19, v167\n"
        "v_mfma_f32_16x16x32_bf16 v[52:55], v[216:219], v[32:35], 0\n"
        "ds_read_b64 v[150:151], v162 offset:32\n"
        "v_mfma_f32_16x16x32_bf16 v[52:55], v[220:223], v[28:31], v[52:55]\n"
        "v_mfma_f32_16x16x32_bf16 v[52:55], v[224:227], v[24:27], v[52:55]\n"
        "ds_write2_b32 v159, v76, v78 offset0:0 offset1:1\n"
        "v_perm_b32 v82, v45, v44, s63\n"
        "v_perm_b32 v83, v47, v46, s63\n"
        "v_mfma_f32_16x16x32_bf16 v[44:47], v[228:231], v[32:35], 0\n"
        "v_mfma_f32_16x16x32_bf16 v[44:47], v[232:235], v[28:31], v[44:47]\n"
        "v_mfma_f32_16x16x32_bf16 v[44:47], v[236:239], v[24:27], v[44:47]\n"
        "s_waitcnt lgkmcnt(2)\n"
        "ds_write2_b32 v159, v77, v79 offset0:16 offset1:17\n"
        "v_perm_b32 v48, v53, v52, s63\n"
        "v_perm_b32 v49, v55, v54, s63\n"
        "v_mfma_f32_4x4x4_16b_bf16 v[96:99], v[160:161], v[164:165], 0\n"
        "v_mfma_f32_4x4x4_16b_bf16 v[92:95], v[156:157], v[164:165], 0\n"
        "s_waitcnt lgkmcnt(2)\n"
        "v_mfma_f32_4x4x4_16b_bf16 v[52:55], v[150:151], v[164:165], 0\n"
        "v_mov_b32 v100, 0x3f803f80\n"
        "v_mov_b32 v101, 0x3f803f80\n"
        "v_mov_b32 v102, 0x3f803f80\n"
        "v_mov_b32 v103, 0x3f803f80\n"
        "v_perm_b32 v50, v45, v44, s63\n"
        "v_mfma_f32_16x16x32_bf16 v[124:127], v[32:35], v[240:243], 0\n"
        "v_perm_b32 v51, v47, v46, s63\n"
        "v_mfma_f32_16x16x32_bf16 v[124:127], v[28:31], v[244:247], v[124:127]\n"
        "ds_read_b128 v[108:111], v0 offset:4112\n"
        "v_mfma_f32_16x16x32_bf16 v[124:127], v[24:27], v[248:251], v[124:127]\n"
        "ds_read_b128 v[112:115], v0 offset:5136\n"
        "ds_write2_b32 v159, v80, v82 offset0:8 offset1:9\n"
        "v_mfma_f32_16x16x32_bf16 v[44:47], v[100:103], v[32:35], 0\n"
        "v_mfma_f32_16x16x32_bf16 v[44:47], v[100:103], v[28:31], v[44:47]\n"
        "ds_read_b128 v[120:123], v0 offset:6176\n"
        "v_mfma_f32_16x16x32_bf16 v[44:47], v[100:103], v[24:27], v[44:47]\n"
        "ds_read_b128 v[116:119], v0 offset:7200\n"
        "ds_read_b128 v[100:103], v0 offset:8224\n"
        "ds_read_b64 v[160:161], v162 offset:48\n"
        "v_perm_b32 v158, v96, v124, s63\n"
        "v_perm_b32 v156, v97, v125, s63\n"
        "v_perm_b32 v150, v98, v126, s63\n"
        "v_perm_b32 v148, v99, v127, s63\n"
        "v_mfma_f32_16x16x32_bf16 v[128:131], v[32:35], v[76:79], 0\n"
        "v_mfma_f32_16x16x32_bf16 v[128:131], v[28:31], v[80:83], v[128:131]\n"
        "ds_read_b64 v[146:147], v162 offset:64\n"
        "v_mfma_f32_16x16x32_bf16 v[128:131], v[24:27], v[48:51], v[128:131]\n"
        "s_waitcnt lgkmcnt(5)\n"
        "ds_write2_b32 v159, v81, v83 offset0:24 offset1:25\n"
        "v_mfma_f32_16x16x32_bf16 v[76:79], v[32:35], v[252:255], 0\n"
        "ds_read_b64 v[144:145], v162 offset:80\n"
        "v_mfma_f32_16x16x32_bf16 v[76:79], v[28:31], v[108:111], v[76:79]\n"
        "ds_read_b128 v[96:99], v0 offset:9264\n"
        "v_mfma_f32_16x16x32_bf16 v[76:79], v[24:27], v[112:115], v[76:79]\n"
        "s_waitcnt lgkmcnt(4)\n"
        "ds_read_b128 v[108:111], v0 offset:10288\n"
        "ds_read_b128 v[112:115], v0 offset:11312\n"
        "s_mov_b32 vcc_lo, 0x22222222\n"
        "v_cndmask_b32 v141, v128, v129, vcc\n"
        "s_mov_b32 vcc_lo, 0x44444444\n"
        "v_cndmask_b32 v141, v141, v130, vcc\n"
        "v_mfma_f32_16x16x32_bf16 v[124:127], v[32:35], v[120:123], 0\n"
        "s_mov_b32 vcc_lo, 0x88888888\n"
        "v_cndmask_b32 v141, v141, v131, vcc\n"
        "v_perm_b32 v159, v92, v76, s63\n"
        "v_mfma_f32_16x16x32_bf16 v[124:127], v[28:31], v[116:119], v[124:127]\n"
        "ds_read_b128 v[132:135], v0 offset:12352\n"
        "v_perm_b32 v157, v93, v77, s63\n"
        "v_mfma_f32_16x16x32_bf16 v[124:127], v[24:27], v[100:103], v[124:127]\n"
        "ds_read_b128 v[136:139], v0 offset:13376\n"
        "ds_read_b128 v[128:131], v0 offset:14400\n"
        "v_perm_b32 v151, v94, v78, s63\n"
        "v_perm_b32 v149, v95, v79, s63\n"
        "v_mfma_f32_4x4x4_16b_bf16 v[116:119], v[160:161], v[164:165], 0\n"
        "s_waitcnt lgkmcnt(5)\n"
        "v_mfma_f32_4x4x4_16b_bf16 v[80:83], v[146:147], v[164:165], 0\n"
        "ds_read_b128 v[120:123], v0 offset:15440\n"
        "ds_read_b128 v[100:103], v0 offset:16464\n"
        "ds_read_b128 v[92:95], v0 offset:17488\n"
        "v_mfma_f32_4x4x4_16b_bf16 v[76:79], v[144:145], v[164:165], 0\n"
        "v_perm_b32 v146, v52, v124, s63\n"
        "v_perm_b32 v164, v53, v125, s63\n"
        "v_perm_b32 v144, v54, v126, s63\n"
        "v_perm_b32 v142, v55, v127, s63\n"
        "s_waitcnt lgkmcnt(6)\n"
        "v_mfma_f32_16x16x32_bf16 v[52:55], v[32:35], v[96:99], 0\n"
        "v_mfma_f32_16x16x32_bf16 v[52:55], v[28:31], v[108:111], v[52:55]\n"
        "v_mfma_f32_16x16x32_bf16 v[52:55], v[24:27], v[112:115], v[52:55]\n"
        "s_waitcnt lgkmcnt(3)\n"
        "ds_write_b128 v163, v[48:51] offset:128\n"
        "v_mfma_f32_16x16x32_bf16 v[48:51], v[32:35], v[132:135], 0\n"
        "ds_read_b128 v[124:127], v7\n"
        "v_mfma_f32_16x16x32_bf16 v[48:51], v[28:31], v[136:139], v[48:51]\n"
        "ds_read_b128 v[132:135], v7 offset:3328\n"
        "v_mfma_f32_16x16x32_bf16 v[48:51], v[24:27], v[128:131], v[48:51]\n"
        "s_waitcnt lgkmcnt(3)\n"
        "ds_read_b128 v[128:131], v7 offset:64\n"
        "v_perm_b32 v147, v116, v52, s63\n"
        "v_perm_b32 v165, v117, v53, s63\n"
        "v_perm_b32 v145, v118, v54, s63\n"
        "v_perm_b32 v143, v119, v55, s63\n"
        "v_mfma_f32_16x16x32_bf16 v[96:99], v[32:35], v[120:123], 0\n"
        "ds_read_b128 v[136:139], v7 offset:3392\n"
        "v_perm_b32 v140, v80, v48, s63\n"
        "v_mfma_f32_16x16x32_bf16 v[96:99], v[28:31], v[100:103], v[96:99]\n"
        "ds_read_b128 v[160:163], v7 offset:128\n"
        "v_perm_b32 v122, v81, v49, s63\n"
        "v_mfma_f32_16x16x32_bf16 v[96:99], v[24:27], v[92:95], v[96:99]\n"
        "s_mov_b64 exec, 0x00f0000f\n"
        "ds_write2_b32 v166, v141, v44 offset0:0 offset1:1\n"
        "s_mov_b64 exec, -1\n"
        "ds_read_b128 v[52:55], v7 offset:3456\n"
        "v_perm_b32 v120, v82, v50, s63\n"
        "v_perm_b32 v118, v83, v51, s63\n"
        "s_mov_b32 exec_hi, 0\n"
        "ds_write2_b64 v167, v[158:159], v[156:157] offset0:0 offset1:49\n"
        "ds_write2_b64 v167, v[150:151], v[148:149] offset0:98 offset1:147\n"
        "s_mov_b32 exec_hi, -1\n"
        "v_perm_b32 v141, v76, v96, s63\n"
        "v_perm_b32 v123, v77, v97, s63\n"
        "v_perm_b32 v121, v78, v98, s63\n"
        "v_perm_b32 v119, v79, v99, s63\n"
        "s_mov_b32 exec_hi, 0\n"
        "ds_write2_b64 v167, v[146:147], v[164:165] offset0:1 offset1:50\n"
        "ds_write2_b64 v167, v[144:145], v[142:143] offset0:99 offset1:148\n"
        "s_mov_b32 exec_hi, -1\n"
        "s_mov_b32 exec_hi, 0\n"
        "ds_write2_b64 v167, v[140:141], v[122:123] offset0:2 offset1:51\n"
        "ds_write2_b64 v167, v[120:121], v[118:119] offset0:100 offset1:149\n"
        "s_mov_b32 exec_hi, -1\n"
        "s_waitcnt lgkmcnt(0)\n"
        "s_nop 6\n"
        "s_setprio 0\n"
        "s_setpc_b64 s[64:65]\n"
        "L52A_%=:\n"
        "s_nop 7\n"
        "v_mov_b32 v167, 21600\n"
        "L50A_%=:\n"
        "ds_read_b32 v166, v167 offset:64\n"
        "s_waitcnt lgkmcnt(0)\n"
        "v_readfirstlane_b32 s40, v166\n"
        "s_nop 3\n"
        "s_cmp_ge_i32 s40, 1\n"
        "s_cbranch_scc1 L51A_%=\n"
        "s_sleep 1\n"
        "s_sub_u32 s41, s41, 1\n"
        "s_cbranch_scc0 L50A_%=\n"
        "s_endpgm\n"
        "L51A_%=:\n"
        "v_mbcnt_lo_u32_b32 v166, -1, 0\n"
        "v_lshl_add_u32 v167, v166, 2, v167\n"
        "ds_read_b32 v166, v167 offset:68\n"
        "s_mov_b32 s14, s40\n"
        "s_mov_b32 s71, -1\n"
        "s_waitcnt lgkmcnt(0)\n"
        "v_readlane_b32 s22, v166, 0\n"
        "v_readlane_b32 s23, v166, 1\n"
        "s_nop 3\n"
        "s_nop 1\n"
        "s_branch L53A_%=\n"
        "L56A_%=:\n"
        "s_nop 7\n"
        "L54A_%=:\n"
        "ds_read_b32 v167, v0 offset:21604\n"
        "s_waitcnt lgkmcnt(0)\n"
        "v_readfirstlane_b32 s40, v167\n"
        "s_nop 3\n"
        "s_cmp_ge_i32 s40, s68\n"
        "s_cbranch_scc1 L55A_%=\n"
        "s_sleep 1\n"
        "s_sub_u32 s41, s41, 1\n"
        "s_cbranch_scc0 L54A_%=\n"
        "s_endpgm\n"
        "L55A_%=:\n"
        "s_nop 1\n"
        "s_branch L57A_%=\n"
        "L52B_%=:\n"
        "s_nop 7\n"
        "v_mov_b32 v167, 21600\n"
        "L50B_%=:\n"
        "ds_read_b32 v166, v167 offset:64\n"
        "s_waitcnt lgkmcnt(0)\n"
        "v_readfirstlane_b32 s40, v166\n"
        "s_nop 3\n"
        "s_cmp_ge_i32 s40, 1\n"
        "s_cbranch_scc1 L51B_%=\n"
        "s_sleep 1\n"
        "s_sub_u32 s41, s41, 1\n"
        "s_cbranch_scc0 L50B_%=\n"
        "s_endpgm\n"
        "L51B_%=:\n"
        "v_mbcnt_lo_u32_b32 v166, -1, 0\n"
        "v_lshl_add_u32 v167, v166, 2, v167\n"
        "ds_read_b32 v166, v167 offset:68\n"
        "s_mov_b32 s14, s40\n"
        "s_mov_b32 s71, -1\n"
        "s_waitcnt lgkmcnt(0)\n"
        "v_readlane_b32 s22, v166, 0\n"
        "v_readlane_b32 s23, v166, 1\n"
        "s_nop 3\n"
        "s_nop 1\n"
        "s_branch L53B_%=\n"
        "L56B_%=:\n"
        "s_nop 7\n"
        "L54B_%=:\n"
        "ds_read_b32 v167, v0 offset:21600\n"
        "s_waitcnt lgkmcnt(0)\n"
        "v_readfirstlane_b32 s40, v167\n"
        "s_nop 3\n"
        "s_cmp_ge_i32 s40, s68\n"
        "s_cbranch_scc1 L55B_%=\n"
        "s_sleep 1\n"
        "s_sub_u32 s41, s41, 1\n"
        "s_cbranch_scc0 L54B_%=\n"
        "s_endpgm\n"
        "L55B_%=:\n"
        "s_nop 1\n"
        "s_branch L57B_%=\n"
        "CREDOA_%=:\n"
        "s_waitcnt lgkmcnt(0)\n"
        "s_nop 15\n"
        "s_mov_b32 s36, 0x20000000\n"
        "ds_read_b128 v[124:127], v7 offset:0\n"
        "ds_read_b128 v[132:135], v7 offset:3328\n"
        "ds_read_b128 v[128:131], v7 offset:64\n"
        "ds_read_b128 v[136:139], v7 offset:3392\n"
        "ds_read_b128 v[160:163], v7 offset:128\n"
        "ds_read_b128 v[52:55], v7 offset:3456\n"
        "s_waitcnt lgkmcnt(0)\n"
        "s_branch MTOPA_%=\n"
        "CREDOB_%=:\n"
        "s_waitcnt lgkmcnt(0)\n"
        "s_nop 15\n"
        "s_mov_b32 s36, 0x20000000\n"
        "ds_read_b128 v[124:127], v7 offset:40064\n"
        "ds_read_b128 v[132:135], v7 offset:43392\n"
        "ds_read_b128 v[128:131], v7 offset:40128\n"
        "ds_read_b128 v[136:139], v7 offset:43456\n"
        "ds_read_b128 v[160:163], v7 offset:40192\n"
        "ds_read_b128 v[52:55], v7 offset:43520\n"
        "s_waitcnt lgkmcnt(0)\n"
        "s_branch MTOPB_%=\n"
        "MTOPA_%=:\n"
        "s_waitcnt lgkmcnt(5)\n"
        "v_mfma_f32_16x16x32_bf16 v[24:27], v[168:171], v[124:127], 0\n"
        "v_cvt_pk_bf16_f32 v112, v152, v56\n"
        "v_cvt_pk_bf16_f32 v113, v72, v20\n"
        "v_cvt_pk_bf16_f32 v114, v84, v60\n"
        "v_cvt_pk_bf16_f32 v116, v153, v57\n"
        "v_cvt_pk_bf16_f32 v117, v73, v21\n"
        "v_cvt_pk_bf16_f32 v118, v85, v61\n"
        "v_cvt_pk_bf16_f32 v120, v154, v58\n"
        "v_cvt_pk_bf16_f32 v121, v74, v22\n"
        "s_waitcnt lgkmcnt(3)\n"
        "v_mfma_f32_16x16x32_bf16 v[24:27], v[172:175], v[128:131], v[24:27]\n"
        "v_cvt_pk_bf16_f32 v122, v86, v62\n"
        "v_cvt_pk_bf16_f32 v140, v155, v59\n"
        "v_cvt_pk_bf16_f32 v141, v75, v23\n"
        "v_cvt_pk_bf16_f32 v142, v87, v63\n"
        "v_cvt_pk_bf16_f32 v144, v68, v36\n"
        "v_cvt_pk_bf16_f32 v145, v40, v64\n"
        "v_cvt_pk_bf16_f32 v146, v104, v88\n"
        "v_cvt_pk_bf16_f32 v148, v69, v37\n"
        "s_waitcnt lgkmcnt(1)\n"
        "v_mfma_f32_16x16x32_bf16 v[24:27], v[176:179], v[160:163], v[24:27]\n"
        "v_cvt_pk_bf16_f32 v149, v41, v65\n"
        "v_cvt_pk_bf16_f32 v150, v105, v89\n"
        "v_cvt_pk_bf16_f32 v152, v70, v38\n"
        "v_cvt_pk_bf16_f32 v153, v42, v66\n"
        "v_cvt_pk_bf16_f32 v154, v106, v90\n"
        "v_cvt_pk_bf16_f32 v156, v71, v39\n"
        "v_cvt_pk_bf16_f32 v157, v43, v67\n"
        "v_cvt_pk_bf16_f32 v158, v107, v91\n"
        "v_mfma_f32_16x16x32_bf16 v[32:35], v[188:191], v[160:163], 0\n"
        "v_perm_b32 v40, v25, v24, s63\n"
        "v_perm_b32 v41, v27, v26, s63\n"
        "v_mfma_f32_16x16x32_bf16 v[32:35], v[184:187], v[128:131], v[32:35]\n"
        "v_mfma_f32_16x16x32_bf16 v[32:35], v[180:183], v[124:127], v[32:35]\n"
        "s_add_u32 s37, s36, 196224\n"
        "ds_read_b128 v[28:31], v16 offset:128\n"
        "ds_read_b32 v159, v10\n"
        "v_mfma_f32_16x16x32_bf16 v[24:27], v[192:195], v[124:127], 0\n"
        "ds_read_b128 v[20:23], v5\n"
        "v_mfma_f32_16x16x32_bf16 v[24:27], v[196:199], v[128:131], v[24:27]\n"
        "ds_read_b64 v[164:165], v11 offset:192\n"
        "v_mfma_f32_16x16x32_bf16 v[24:27], v[200:203], v[160:163], v[24:27]\n"
        "buffer_store_dwordx3 v[112:114], v12, s[8:11], s37 offen offset:384 sc0 sc1 nt\n"
        "v_perm_b32 v42, v33, v32, s63\n"
        "v_perm_b32 v43, v35, v34, s63\n"
        "s_waitcnt lgkmcnt(3)\n"
        "v_mfma_f32_16x16x32_bf16 v[48:51], v[28:31], v[160:163], 0\n"
        "ds_read_b128 v[60:63], v7 offset:6784\n"
        "ds_read_i16 v167, v0 offset:20584\n"
        "ds_read_i16 v155, v0 offset:20586\n"
        "v_perm_b32 v44, v25, v24, s63\n"
        "v_perm_b32 v45, v27, v26, s63\n"
        "s_waitcnt lgkmcnt(3)\n"
        "v_smfmac_f32_16x16x64_bf16 v[48:51], v[20:23], v[124:131], v4\n"
        "s_add_u32 s37, s36, 196224\n"
        "ds_read_b128 v[32:35], v7 offset:6656\n"
        "ds_read_b128 v[36:39], v7 offset:6720\n"
        "v_mfma_f32_16x16x32_bf16 v[56:59], v[28:31], v[52:55], 0\n"
        "v_smfmac_f32_16x16x64_bf16 v[56:59], v[20:23], v[132:139], v4\n"
        "s_setprio 2\n"
        "s_waitcnt lgkmcnt(4)\n"
        "v_mfma_f32_16x16x32_bf16 v[24:27], v[28:31], v[60:63], 0\n"
        "buffer_store_dwordx3 v[116:118], v13, s[8:11], s37 offen offset:1152 sc0 sc1 nt\n"
        "v_add_f32_dpp v165, v50, v51 quad_perm:[0,1,2,3] row_mask:0x1 bank_mask:0xf\n"
        "s_waitcnt lgkmcnt(2)\n"
        "v_add_u32 v167, v9, v167\n"
        "v_add_f32_dpp v164, v48, v49 quad_perm:[0,1,2,3] row_mask:0x1 bank_mask:0xf\n"
        "v_add_f32_dpp v165, v58, v59 quad_perm:[0,1,2,3] row_mask:0x2 bank_mask:0xf\n"
        "v_add_u32 v155, v9, v155\n"
        "v_add_f32_dpp v164, v56, v57 quad_perm:[0,1,2,3] row_mask:0x2 bank_mask:0xf\n"
        "s_waitcnt lgkmcnt(0)\n"
        "v_smfmac_f32_16x16x64_bf16 v[24:27], v[20:23], v[32:39], v4\n"
        "v_mfma_f32_16x16x32_bf16 v[28:31], v[212:215], v[160:163], 0\n"
        "s_add_u32 s37, s36, 294336\n"
        "v_mfma_f32_16x16x32_bf16 v[28:31], v[208:211], v[128:131], v[28:31]\n"
        "v_mfma_f32_16x16x32_bf16 v[28:31], v[204:207], v[124:127], v[28:31]\n"
        "v_mfma_f32_16x16x32_bf16 v[20:23], v[216:219], v[124:127], 0\n"
        "buffer_store_dwordx3 v[120:122], v12, s[8:11], s37 offen offset:576 sc0 sc1 nt\n"
        "v_mfma_f32_16x16x32_bf16 v[20:23], v[220:223], v[128:131], v[20:23]\n"
        "v_mfma_f32_16x16x32_bf16 v[20:23], v[224:227], v[160:163], v[20:23]\n"
        "s_add_u32 s37, s36, 392448\n"
        "v_add_f32_dpp v165, v26, v27 quad_perm:[0,1,2,3] row_mask:0x4 bank_mask:0xf\n"
        "v_mul_f32 v166, s52, v165\n"
        "v_fma_f32 v166, -v166, v165, v159\n"
        "v_perm_b32 v46, v29, v28, s63\n"
        "v_perm_b32 v47, v31, v30, s63\n"
        "v_add_f32_e64 v166, s53, |v166|\n"
        "v_rsq_f32 v159, v166\n"
        "v_mfma_f32_16x16x32_bf16 v[56:59], v[236:239], v[160:163], 0\n"
        "s_mov_b32 exec_hi, 0xffff\n"
        "ds_write_b32 v10, v159\n"
        "s_mov_b32 exec_hi, -1\n"
        "v_perm_b32 v48, v21, v20, s63\n"
        "v_perm_b32 v49, v23, v22, s63\n"
        "ds_read_b32 v20, v167 offset:0\n"
        "v_add_f32_dpp v164, v24, v25 quad_perm:[0,1,2,3] row_mask:0x4 bank_mask:0xf\n"
        "v_mfma_f32_16x16x32_bf16 v[56:59], v[232:235], v[128:131], v[56:59]\n"
        "s_mov_b32 exec_hi, 0xffff\n"
        "v_add_u32 v10, 40064, v10\n"
        "s_mov_b32 exec_hi, -1\n"
        "ds_read_b32 v21, v167 offset:832\n"
        "ds_read_b32 v22, v167 offset:1664\n"
        "ds_read_b32 v23, v167 offset:2496\n"
        "ds_read_b32 v24, v167 offset:3328\n"
        "v_mul_f32 v164, v164, v159\n"
        "v_mfma_f32_16x16x32_bf16 v[56:59], v[228:231], v[124:127], v[56:59]\n"
        "buffer_store_dwordx3 v[140:142], v12, s[8:11], s37 offen sc0 sc1 nt\n"
        "ds_read_b32 v25, v167 offset:4160\n"
        "ds_read_b32 v26, v167 offset:4992\n"
        "ds_read_b32 v27, v167 offset:5824\n"
        "ds_read_b32 v28, v167 offset:6656\n"
        "ds_read_b32 v29, v167 offset:7488\n"
        "ds_read_b32 v30, v167 offset:8320\n"
        "ds_read_b32 v31, v167 offset:9152\n"
        "v_perm_b32 v50, v57, v56, s63\n"
        "v_perm_b32 v51, v59, v58, s63\n"
        "v_readlane_b32 s58, v159, 56\n"
        "v_mfma_f32_16x16x32_bf16 v[56:59], v[124:127], v[40:43], 0\n"
        "s_mov_b64 exec, s[56:57]\n"
        "v_mov_b32 v164, 0xff800000\n"
        "s_mov_b64 exec, -1\n"
        "ds_read_b32 v165, v155 offset:0\n"
        "ds_read_b32 v166, v155 offset:3328\n"
        "v_max_f32_dpp v151, v164, v164 row_ror:8 row_mask:0xf bank_mask:0xf\n"
        "v_mfma_f32_16x16x32_bf16 v[56:59], v[128:131], v[44:47], v[56:59]\n"
        "s_add_u32 s37, s36, 392448\n"
        "v_max_f32_dpp v151, v151, v151 row_ror:4 row_mask:0xf bank_mask:0xf\n"
        "ds_read_b32 v167, v155 offset:6656\n"
        "v_mfma_f32_16x16x32_bf16 v[56:59], v[160:163], v[48:51], v[56:59]\n"
        "v_max_f32_dpp v151, v151, v151 row_ror:2 row_mask:0xf bank_mask:0xf\n"
        "v_mfma_f32_16x16x32_bf16 v[72:75], v[52:55], v[48:51], 0\n"
        "v_mfma_f32_16x16x32_bf16 v[72:75], v[136:139], v[44:47], v[72:75]\n"
        "buffer_store_dwordx3 v[144:146], v15, s[8:11], s37 offen offset:768 sc0 sc1 nt\n"
        "v_max_f32_dpp v151, v151, v151 row_ror:1 row_mask:0xf bank_mask:0xf\n"
        "v_mfma_f32_16x16x32_bf16 v[72:75], v[132:135], v[40:43], v[72:75]\n"
        "ds_read_b128 v[88:91], v8 offset:128\n"
        "v_mfma_f32_16x16x32_bf16 v[68:71], v[32:35], v[40:43], 0\n"
        "s_add_u32 s37, s36, 490560\n"
        "ds_read2_b32 v[92:93], v6 offset0:0 offset1:2\n"
        "ds_read2_b32 v[94:95], v6 offset0:8 offset1:10\n"
        "s_waitcnt lgkmcnt(3)\n"
        "v_mul_f32 v56, v56, v20\n"
        "v_mul_f32 v57, v57, v21\n"
        "v_mul_f32 v58, v58, v22\n"
        "v_mul_f32 v59, v59, v23\n"
        "v_mul_f32 v72, v72, v24\n"
        "v_mfma_f32_16x16x32_bf16 v[68:71], v[36:39], v[44:47], v[68:71]\n"
        "v_max3_f32 v155, v56, v57, v58\n"
        "v_mul_f32 v73, v73, v25\n"
        "v_mul_f32 v74, v74, v26\n"
        "v_max3_f32 v155, v155, v59, v72\n"
        "v_mul_f32 v75, v75, v27\n"
        "v_max3_f32 v155, v155, v73, v74\n"
        "v_mfma_f32_16x16x32_bf16 v[68:71], v[60:63], v[48:51], v[68:71]\n"
        "s_waitcnt lgkmcnt(2)\n"
        "v_mfma_f32_16x16x32_bf16 v[76:79], v[88:91], v[48:51], 0\n"
        "buffer_store_dwordx3 v[148:150], v12, s[8:11], s37 offen offset:192 sc0 sc1 nt\n"
        "s_waitcnt lgkmcnt(0)\n"
        "v_smfmac_f32_16x16x64_bf16 v[76:79], v[92:95], v[40:47], v4\n"
        "v_mfma_f32_16x16x32_bf16 v[48:51], v[168:171], v[132:135], 0\n"
        "s_add_u32 s37, s36, 490560\n"
        "v_mfma_f32_16x16x32_bf16 v[48:51], v[172:175], v[136:139], v[48:51]\n"
        "v_mul_f32 v68, v68, v28\n"
        "v_mul_f32 v69, v69, v29\n"
        "v_mul_f32 v70, v70, v30\n"
        "v_max3_f32 v155, v155, v75, v68\n"
        "v_mul_f32 v71, v71, v31\n"
        "v_add_f32 v76, v76, v77\n"
        "v_mul_f32 v76, s58, v76\n"
        "v_max3_f32 v155, v155, v69, v70\n"
        "v_mfma_f32_16x16x32_bf16 v[48:51], v[176:179], v[52:55], v[48:51]\n"
        "s_mov_b64 exec, s[48:49]\n"
        "v_mov_b32 v76, 0xff800000\n"
        "s_mov_b64 exec, -1\n"
        "v_max3_f32 v155, v155, v71, v76\n"
        "v_mfma_f32_16x16x32_bf16 v[44:47], v[188:191], v[52:55], 0\n"
        "buffer_store_dwordx3 v[152:154], v14, s[8:11], s37 offen offset:960 sc0 sc1 nt\n"
        "v_permlane32_swap_b32 v155, v151\n"
        "v_max_f32 v155, v155, v151\n"
        "v_perm_b32 v40, v49, v48, s63\n"
        "v_perm_b32 v41, v51, v50, s63\n"
        "v_mov_b32 v154, v155\n"
        "v_mfma_f32_16x16x32_bf16 v[44:47], v[184:187], v[136:139], v[44:47]\n"
        "v_mfma_f32_16x16x32_bf16 v[44:47], v[180:183], v[132:135], v[44:47]\n"
        "s_add_u32 s37, s36, 588672\n"
        "v_permlane16_swap_b32 v155, v154\n"
        "v_max_f32 v155, v155, v154\n"
        "v_mov_b32 v151, v155\n"
        "v_mfma_f32_16x16x32_bf16 v[64:67], v[192:195], v[132:135], 0\n"
        "v_mfma_f32_16x16x32_bf16 v[64:67], v[196:199], v[136:139], v[64:67]\n"
        "v_permlane32_swap_b32 v155, v151\n"
        "v_mul_f32 v155, v155, v165\n"
        "v_perm_b32 v42, v45, v44, s63\n"
        "v_perm_b32 v43, v47, v46, s63\n"
        "v_fma_f32 v56, v56, v165, -v155\n"
        "v_fma_f32 v57, v57, v165, -v155\n"
        "v_fma_f32 v58, v58, v165, -v155\n"
        "v_mfma_f32_16x16x32_bf16 v[64:67], v[200:203], v[52:55], v[64:67]\n"
        "buffer_store_dwordx3 v[156:158], v12, s[8:11], s37 offen offset:384 sc0 sc1 nt\n"
        "v_exp_f32 v56, v56\n"
        "v_exp_f32 v57, v57\n"
        "v_exp_f32 v58, v58\n"
        "v_fma_f32 v59, v59, v165, -v155\n"
        "v_add_f32 v158, v56, v57\n"
        "v_exp_f32 v59, v59\n"
        "v_mfma_f32_16x16x32_bf16 v[48:51], v[212:215], v[52:55], 0\n"
        "v_perm_b32 v44, v65, v64, s63\n"
        "v_perm_b32 v45, v67, v66, s63\n"
        "v_fma_f32 v72, v72, v165, -v155\n"
        "v_add_f32 v158, v158, v58\n"
        "v_exp_f32 v72, v72\n"
        "v_fma_f32 v73, v73, v165, -v155\n"
        "v_add_f32 v158, v158, v59\n"
        "v_mfma_f32_16x16x32_bf16 v[48:51], v[208:211], v[136:139], v[48:51]\n"
        "v_exp_f32 v73, v73\n"
        "v_fma_f32 v74, v74, v165, -v155\n"
        "v_add_f32 v158, v158, v72\n"
        "v_exp_f32 v74, v74\n"
        "v_fma_f32 v75, v75, v165, -v155\n"
        "v_sub_f32 v164, v164, v151\n"
        "v_mfma_f32_16x16x32_bf16 v[48:51], v[204:207], v[132:135], v[48:51]\n"
        "v_add_f32 v158, v158, v73\n"
        "v_exp_f32 v75, v75\n"
        "v_fma_f32 v68, v68, v165, -v155\n"
        "v_mul_f32 v164, s58, v164\n"
        "v_add_f32 v158, v158, v74\n"
        "v_exp_f32 v68, v68\n"
        "v_mfma_f32_16x16x32_bf16 v[64:67], v[216:219], v[132:135], 0\n"
        "v_fma_f32 v69, v69, v165, -v155\n"
        "v_perm_b32 v46, v49, v48, s63\n"
        "v_perm_b32 v47, v51, v50, s63\n"
        "v_exp_f32 v164, v164\n"
        "v_add_f32 v158, v158, v75\n"
        "v_exp_f32 v69, v69\n"
        "v_mfma_f32_16x16x32_bf16 v[64:67], v[220:223], v[136:139], v[64:67]\n"
        "v_fma_f32 v70, v70, v165, -v155\n"
        "v_add_f32 v158, v158, v68\n"
        "v_exp_f32 v70, v70\n"
        "v_fma_f32 v71, v71, v165, -v155\n"
        "v_add_f32_dpp v157, v164, v164 row_ror:8 row_mask:0xf bank_mask:0xf\n"
        "v_add_f32 v158, v158, v69\n"
        "v_fma_f32 v76, v76, v165, -v155\n"
        "v_mfma_f32_16x16x32_bf16 v[64:67], v[224:227], v[52:55], v[64:67]\n"
        "v_exp_f32 v71, v71\n"
        "v_add_f32_dpp v157, v157, v157 row_ror:4 row_mask:0xf bank_mask:0xf\n"
        "v_add_f32 v158, v158, v70\n"
        "v_exp_f32 v76, v76\n"
        "v_add_f32_dpp v157, v157, v157 row_ror:2 row_mask:0xf bank_mask:0xf\n"
        "v_add_f32 v158, v158, v71\n"
        "v_mfma_f32_16x16x32_bf16 v[48:51], v[236:239], v[52:55], 0\n"
        "v_add_f32_dpp v157, v157, v157 row_ror:1 row_mask:0xf bank_mask:0xf\n"
        "v_perm_b32 v80, v65, v64, s63\n"
        "v_perm_b32 v81, v67, v66, s63\n"
        "v_add_f32 v158, v158, v76\n"
        "v_mul_f32 v56, v56, v20\n"
        "v_mul_f32 v57, v57, v21\n"
        "v_permlane32_swap_b32 v158, v157\n"
        "v_mfma_f32_16x16x32_bf16 v[48:51], v[232:235], v[136:139], v[48:51]\n"
        "v_add_f32 v158, v158, v157\n"
        "v_mov_b32 v165, v158\n"
        "v_mul_f32 v58, v58, v22\n"
        "v_mul_f32 v59, v59, v23\n"
        "v_permlane16_swap_b32 v158, v165\n"
        "v_add_f32 v158, v158, v165\n"
        "v_mul_f32 v72, v72, v24\n"
        "v_mfma_f32_16x16x32_bf16 v[48:51], v[228:231], v[132:135], v[48:51]\n"
        "v_rcp_f32 v158, v158\n"
        "v_mul_f32 v73, v73, v25\n"
        "v_mov_b32 v157, v158\n"
        "v_mul_f32 v74, v74, v26\n"
        "v_mul_f32 v75, v75, v27\n"
        "v_permlane32_swap_b32 v158, v157\n"
        "v_mfma_f32_16x16x32_bf16 v[64:67], v[124:127], v[40:43], 0\n"
        "v_mul_f32 v56, v56, v158\n"
        "v_perm_b32 v82, v49, v48, s63\n"
        "v_perm_b32 v83, v51, v50, s63\n"
        "v_mul_f32 v57, v57, v158\n"
        "v_mul_f32 v58, v58, v158\n"
        "v_mul_f32 v59, v59, v158\n"
        "v_mul_f32 v72, v72, v158\n"
        "v_mul_f32 v73, v73, v158\n"
        "v_mfma_f32_16x16x32_bf16 v[64:67], v[128:131], v[44:47], v[64:67]\n"
        "v_mul_f32 v74, v74, v158\n"
        "v_mul_f32 v75, v75, v158\n"
        "v_perm_b32 v48, v57, v56, s63\n"
        "v_perm_b32 v49, v59, v58, s63\n"
        "v_perm_b32 v50, v73, v72, s63\n"
        "v_perm_b32 v51, v75, v74, s63\n"
        "v_mul_f32 v68, v68, v28\n"
        "v_mul_f32 v69, v69, v29\n"
        "v_mfma_f32_16x16x32_bf16 v[64:67], v[160:163], v[80:83], v[64:67]\n"
        "v_mul_f32 v70, v70, v30\n"
        "v_mul_f32 v71, v71, v31\n"
        "v_mul_f32 v76, s58, v76\n"
        "v_mul_f32 v68, v68, v158\n"
        "v_mul_f32 v69, v69, v158\n"
        "v_mul_f32 v70, v70, v158\n"
        "v_mul_f32 v71, v71, v158\n"
        "v_fma_f32 v76, v76, v158, v19\n"
        "v_mfma_f32_16x16x32_bf16 v[84:87], v[52:55], v[80:83], 0\n"
        "v_mul_f32 v64, v64, v20\n"
        "v_mul_f32 v65, v65, v21\n"
        "v_mul_f32 v66, v66, v22\n"
        "v_mul_f32 v67, v67, v23\n"
        "v_max3_f32 v165, v64, v65, v66\n"
        "v_perm_b32 v56, v69, v68, s63\n"
        "v_perm_b32 v58, v71, v70, s63\n"
        "v_perm_b32 v57, 0, v76, s63\n"
        "v_mfma_f32_16x16x32_bf16 v[84:87], v[136:139], v[44:47], v[84:87]\n"
        "v_mul_f32 v164, v164, v159\n"
        "v_mul_f32 v164, v164, v157\n"
        "v_perm_b32 v164, 0, v164, s63\n"
        "ds_write_b16 v1, v164\n"
        "v_mfma_f32_16x16x32_bf16 v[84:87], v[132:135], v[40:43], v[84:87]\n"
        "v_mfma_f32_16x16x32_bf16 v[68:71], v[32:35], v[40:43], 0\n"
        "v_mfma_f32_16x16x32_bf16 v[68:71], v[36:39], v[44:47], v[68:71]\n"
        "v_mfma_f32_16x16x32_bf16 v[68:71], v[60:63], v[80:83], v[68:71]\n"
        "v_mfma_f32_16x16x32_bf16 v[72:75], v[88:91], v[80:83], 0\n"
        "v_smfmac_f32_16x16x64_bf16 v[72:75], v[92:95], v[40:47], v4\n"
        "v_mfma_f32_16x16x32_bf16 v[80:83], v[168:171], v[32:35], 0\n"
        "v_mfma_f32_16x16x32_bf16 v[80:83], v[172:175], v[36:39], v[80:83]\n"
        "v_mfma_f32_16x16x32_bf16 v[80:83], v[176:179], v[60:63], v[80:83]\n"
        "v_mul_f32 v84, v84, v24\n"
        "v_mul_f32 v85, v85, v25\n"
        "v_mul_f32 v86, v86, v26\n"
        "v_max3_f32 v165, v165, v67, v84\n"
        "v_mul_f32 v87, v87, v27\n"
        "v_add_f32 v72, v72, v73\n"
        "v_mul_f32 v68, v68, v28\n"
        "v_mul_f32 v72, s58, v72\n"
        "v_mfma_f32_16x16x32_bf16 v[40:43], v[188:191], v[60:63], 0\n"
        "s_mov_b64 exec, s[48:49]\n"
        "v_mov_b32 v72, 0xff800000\n"
        "s_mov_b64 exec, -1\n"
        "v_perm_b32 v76, v81, v80, s63\n"
        "v_perm_b32 v77, v83, v82, s63\n"
        "v_max3_f32 v165, v165, v85, v86\n"
        "v_mul_f32 v69, v69, v29\n"
        "v_mul_f32 v70, v70, v30\n"
        "v_max3_f32 v165, v165, v87, v68\n"
        "v_mfma_f32_16x16x32_bf16 v[40:43], v[184:187], v[36:39], v[40:43]\n"
        "v_mul_f32 v71, v71, v31\n"
        "v_max3_f32 v165, v165, v69, v70\n"
        "v_max3_f32 v165, v165, v71, v72\n"
        "v_mfma_f32_16x16x32_bf16 v[40:43], v[180:183], v[32:35], v[40:43]\n"
        "v_mfma_f32_16x16x32_bf16 v[100:103], v[192:195], v[32:35], 0\n"
        "v_mfma_f32_16x16x32_bf16 v[100:103], v[196:199], v[36:39], v[100:103]\n"
        "v_mfma_f32_16x16x32_bf16 v[100:103], v[200:203], v[60:63], v[100:103]\n"
        "v_mfma_f32_16x16x32_bf16 v[96:99], v[212:215], v[60:63], 0\n"
        "v_mfma_f32_16x16x32_bf16 v[96:99], v[208:211], v[36:39], v[96:99]\n"
        "v_mfma_f32_16x16x32_bf16 v[96:99], v[204:207], v[32:35], v[96:99]\n"
        "v_mfma_f32_16x16x32_bf16 v[44:47], v[216:219], v[32:35], 0\n"
        "v_mfma_f32_16x16x32_bf16 v[44:47], v[220:223], v[36:39], v[44:47]\n"
        "v_perm_b32 v78, v41, v40, s63\n"
        "v_perm_b32 v79, v43, v42, s63\n"
        "v_mfma_f32_16x16x32_bf16 v[44:47], v[224:227], v[60:63], v[44:47]\n"
        "v_perm_b32 v80, v101, v100, s63\n"
        "v_perm_b32 v81, v103, v102, s63\n"
        "v_mfma_f32_16x16x32_bf16 v[40:43], v[236:239], v[60:63], 0\n"
        "v_perm_b32 v82, v97, v96, s63\n"
        "v_perm_b32 v83, v99, v98, s63\n"
        "v_mfma_f32_16x16x32_bf16 v[40:43], v[232:235], v[36:39], v[40:43]\n"
        "v_mfma_f32_16x16x32_bf16 v[40:43], v[228:231], v[32:35], v[40:43]\n"
        "v_mfma_f32_16x16x32_bf16 v[108:111], v[124:127], v[76:79], 0\n"
        "v_perm_b32 v96, v45, v44, s63\n"
        "v_perm_b32 v97, v47, v46, s63\n"
        "v_mfma_f32_16x16x32_bf16 v[108:111], v[128:131], v[80:83], v[108:111]\n"
        "s_nop 3\n"
        "v_perm_b32 v98, v41, v40, s63\n"
        "v_perm_b32 v99, v43, v42, s63\n"
        "s_nop 1\n"
        "v_mfma_f32_16x16x32_bf16 v[108:111], v[160:163], v[96:99], v[108:111]\n"
        "v_mfma_f32_16x16x32_bf16 v[140:143], v[52:55], v[96:99], 0\n"
        "v_mfma_f32_16x16x32_bf16 v[140:143], v[136:139], v[80:83], v[140:143]\n"
        "v_mfma_f32_16x16x32_bf16 v[140:143], v[132:135], v[76:79], v[140:143]\n"
        "v_mfma_f32_16x16x32_bf16 v[100:103], v[32:35], v[76:79], 0\n"
        "v_mfma_f32_16x16x32_bf16 v[100:103], v[36:39], v[80:83], v[100:103]\n"
        "v_mfma_f32_16x16x32_bf16 v[100:103], v[60:63], v[96:99], v[100:103]\n"
        "v_mfma_f32_16x16x32_bf16 v[44:47], v[88:91], v[96:99], 0\n"
        "v_smfmac_f32_16x16x64_bf16 v[44:47], v[92:95], v[76:83], v4\n"
        "v_mul_f32 v108, v108, v20\n"
        "v_mul_f32 v109, v109, v21\n"
        "v_mul_f32 v110, v110, v22\n"
        "v_mul_f32 v111, v111, v23\n"
        "v_mul_f32 v140, v140, v24\n"
        "v_max3_f32 v159, v108, v109, v110\n"
        "v_mul_f32 v141, v141, v25\n"
        "v_mfma_f32_16x16x32_bf16 v[60:63], v[124:127], v[240:243], 0\n"
        "v_add_f32 v44, v44, v45\n"
        "v_mul_f32 v142, v142, v26\n"
        "v_max3_f32 v159, v159, v111, v140\n"
        "v_mul_f32 v143, v143, v27\n"
        "v_mul_f32 v100, v100, v28\n"
        "v_mul_f32 v44, s58, v44\n"
        "v_max3_f32 v159, v159, v141, v142\n"
        "v_mul_f32 v101, v101, v29\n"
        "v_mfma_f32_16x16x32_bf16 v[60:63], v[128:131], v[244:247], v[60:63]\n"
        "s_mov_b64 exec, s[48:49]\n"
        "v_mov_b32 v44, 0xff800000\n"
        "s_mov_b64 exec, -1\n"
        "v_mul_f32 v102, v102, v30\n"
        "v_max3_f32 v159, v159, v143, v100\n"
        "v_mul_f32 v103, v103, v31\n"
        "v_max3_f32 v159, v159, v101, v102\n"
        "v_max3_f32 v159, v159, v103, v44\n"
        "v_mfma_f32_16x16x32_bf16 v[60:63], v[160:163], v[248:251], v[60:63]\n"
        "ds_read_b128 v[32:35], v17 offset:0\n"
        "v_permlane32_swap_b32 v165, v159\n"
        "v_max_f32 v165, v165, v159\n"
        "v_mov_b32 v164, v165\n"
        "v_mfma_f32_16x16x32_bf16 v[76:79], v[52:55], v[248:251], 0\n"
        "ds_read_b128 v[36:39], v17 offset:64\n"
        "v_permlane16_swap_b32 v165, v164\n"
        "v_max_f32 v165, v165, v164\n"
        "v_perm_b32 v40, v61, v60, s63\n"
        "v_perm_b32 v41, v63, v62, s63\n"
        "v_mov_b32 v159, v165\n"
        "v_mfma_f32_16x16x32_bf16 v[76:79], v[136:139], v[244:247], v[76:79]\n"
        "ds_read_b128 v[60:63], v17 offset:128\n"
        "v_permlane32_swap_b32 v165, v159\n"
        "v_mul_f32 v165, v165, v166\n"
        "v_mul_f32 v159, v159, v167\n"
        "v_fma_f32 v64, v64, v166, -v165\n"
        "v_fma_f32 v65, v65, v166, -v165\n"
        "v_fma_f32 v108, v108, v167, -v159\n"
        "v_mfma_f32_16x16x32_bf16 v[76:79], v[132:135], v[240:243], v[76:79]\n"
        "s_waitcnt lgkmcnt(2)\n"
        "ds_read_b128 v[96:99], v0 offset:5136\n"
        "v_fma_f32 v109, v109, v167, -v159\n"
        "v_exp_f32 v64, v64\n"
        "v_exp_f32 v65, v65\n"
        "v_fma_f32 v66, v66, v166, -v165\n"
        "v_fma_f32 v110, v110, v167, -v159\n"
        "v_mfma_f32_16x16x32_bf16 v[80:83], v[32:35], v[240:243], 0\n"
        "s_waitcnt lgkmcnt(2)\n"
        "ds_read_b128 v[104:107], v0 offset:4112\n"
        "v_perm_b32 v42, v77, v76, s63\n"
        "v_perm_b32 v43, v79, v78, s63\n"
        "v_exp_f32 v108, v108\n"
        "v_exp_f32 v109, v109\n"
        "v_fma_f32 v67, v67, v166, -v165\n"
        "v_mfma_f32_16x16x32_bf16 v[80:83], v[36:39], v[244:247], v[80:83]\n"
        "s_waitcnt lgkmcnt(2)\n"
        "v_exp_f32 v66, v66\n"
        "v_exp_f32 v110, v110\n"
        "v_fma_f32 v111, v111, v167, -v159\n"
        "v_add_f32 v95, v64, v65\n"
        "v_exp_f32 v67, v67\n"
        "v_mfma_f32_16x16x32_bf16 v[80:83], v[60:63], v[248:251], v[80:83]\n"
        "s_waitcnt lgkmcnt(1)\n"
        "v_fma_f32 v84, v84, v166, -v165\n"
        "v_add_f32 v164, v108, v109\n"
        "v_exp_f32 v111, v111\n"
        "v_fma_f32 v140, v140, v167, -v159\n"
        "v_add_f32 v95, v95, v66\n"
        "v_exp_f32 v84, v84\n"
        "v_mfma_f32_16x16x32_bf16 v[112:115], v[60:63], v[96:99], 0\n"
        "s_waitcnt lgkmcnt(0)\n"
        "v_perm_b32 v76, v81, v80, s63\n"
        "v_perm_b32 v80, v83, v82, s63\n"
        "v_fma_f32 v85, v85, v166, -v165\n"
        "v_add_f32 v164, v164, v110\n"
        "v_exp_f32 v140, v140\n"
        "v_fma_f32 v141, v141, v167, -v159\n"
        "v_add_f32 v95, v95, v67\n"
        "v_mfma_f32_16x16x32_bf16 v[112:115], v[36:39], v[104:107], v[112:115]\n"
        "v_exp_f32 v85, v85\n"
        "v_fma_f32 v86, v86, v166, -v165\n"
        "v_add_f32 v164, v164, v111\n"
        "v_exp_f32 v141, v141\n"
        "v_fma_f32 v142, v142, v167, -v159\n"
        "v_add_f32 v95, v95, v84\n"
        "v_mfma_f32_16x16x32_bf16 v[112:115], v[32:35], v[252:255], v[112:115]\n"
        "v_exp_f32 v86, v86\n"
        "v_fma_f32 v87, v87, v166, -v165\n"
        "v_add_f32 v164, v164, v140\n"
        "v_exp_f32 v142, v142\n"
        "v_fma_f32 v143, v143, v167, -v159\n"
        "v_add_f32 v95, v95, v85\n"
        "v_mfma_f32_16x16x32_bf16 v[120:123], v[132:135], v[252:255], 0\n"
        "v_exp_f32 v87, v87\n"
        "v_perm_b32 v88, v113, v112, s63\n"
        "v_perm_b32 v92, v115, v114, s63\n"
        "v_fma_f32 v68, v68, v166, -v165\n"
        "v_add_f32 v164, v164, v141\n"
        "v_exp_f32 v143, v143\n"
        "v_mfma_f32_16x16x32_bf16 v[120:123], v[136:139], v[104:107], v[120:123]\n"
        "v_fma_f32 v100, v100, v167, -v159\n"
        "v_add_f32 v95, v95, v86\n"
        "v_exp_f32 v68, v68\n"
        "v_fma_f32 v69, v69, v166, -v165\n"
        "v_add_f32 v164, v164, v142\n"
        "v_exp_f32 v100, v100\n"
        "v_mfma_f32_16x16x32_bf16 v[120:123], v[52:55], v[96:99], v[120:123]\n"
        "ds_read_b128 v[144:147], v0 offset:6176\n"
        "v_fma_f32 v101, v101, v167, -v159\n"
        "v_add_f32 v95, v95, v87\n"
        "v_exp_f32 v69, v69\n"
        "v_fma_f32 v70, v70, v166, -v165\n"
        "v_add_f32 v164, v164, v143\n"
        "v_fma_f32 v102, v102, v167, -v159\n"
        "v_mfma_f32_16x16x32_bf16 v[148:151], v[160:163], v[96:99], 0\n"
        "ds_read_b128 v[116:119], v0 offset:7200\n"
        "v_perm_b32 v98, v121, v120, s63\n"
        "v_perm_b32 v99, v123, v122, s63\n"
        "v_exp_f32 v101, v101\n"
        "v_add_f32 v95, v95, v68\n"
        "v_exp_f32 v70, v70\n"
        "v_mfma_f32_16x16x32_bf16 v[148:151], v[128:131], v[104:107], v[148:151]\n"
        "ds_read_b128 v[112:115], v0 offset:8224\n"
        "v_fma_f32 v71, v71, v166, -v165\n"
        "v_add_f32 v164, v164, v100\n"
        "v_exp_f32 v102, v102\n"
        "v_fma_f32 v103, v103, v167, -v159\n"
        "v_add_f32 v95, v95, v69\n"
        "v_fma_f32 v72, v72, v166, -v165\n"
        "v_mfma_f32_16x16x32_bf16 v[148:151], v[124:127], v[252:255], v[148:151]\n"
        "s_waitcnt lgkmcnt(2)\n"
        "v_exp_f32 v71, v71\n"
        "v_add_f32 v164, v164, v101\n"
        "v_exp_f32 v103, v103\n"
        "v_fma_f32 v44, v44, v167, -v159\n"
        "v_add_f32 v95, v95, v70\n"
        "v_add_f32 v164, v164, v102\n"
        "v_mfma_f32_16x16x32_bf16 v[120:123], v[124:127], v[144:147], 0\n"
        "s_waitcnt lgkmcnt(1)\n"
        "v_perm_b32 v96, v149, v148, s63\n"
        "v_perm_b32 v97, v151, v150, s63\n"
        "v_exp_f32 v72, v72\n"
        "v_exp_f32 v44, v44\n"
        "v_add_f32 v95, v95, v71\n"
        "v_add_f32 v164, v164, v103\n"
        "v_mfma_f32_16x16x32_bf16 v[120:123], v[128:131], v[116:119], v[120:123]\n"
        "s_waitcnt lgkmcnt(0)\n"
        "v_add_f32 v95, v95, v72\n"
        "v_add_f32 v164, v164, v44\n"
        "v_mul_f32 v64, v64, v20\n"
        "v_mul_f32 v65, v65, v21\n"
        "v_permlane32_swap_b32 v95, v164\n"
        "v_add_f32 v95, v95, v164\n"
        "v_mov_b32 v167, v95\n"
        "v_mfma_f32_16x16x32_bf16 v[120:123], v[160:163], v[112:115], v[120:123]\n"
        "v_mul_f32 v66, v66, v22\n"
        "v_permlane16_swap_b32 v95, v167\n"
        "v_add_f32 v95, v95, v167\n"
        "v_rcp_f32 v95, v95\n"
        "v_mul_f32 v67, v67, v23\n"
        "v_mov_b32 v164, v95\n"
        "v_mfma_f32_16x16x32_bf16 v[156:159], v[52:55], v[112:115], 0\n"
        "v_mul_f32 v84, v84, v24\n"
        "v_perm_b32 v104, v121, v120, s63\n"
        "v_perm_b32 v105, v123, v122, s63\n"
        "v_permlane32_swap_b32 v95, v164\n"
        "v_mul_f32 v85, v85, v25\n"
        "v_mul_f32 v86, v86, v26\n"
        "v_mul_f32 v87, v87, v27\n"
        "v_mfma_f32_16x16x32_bf16 v[156:159], v[136:139], v[116:119], v[156:159]\n"
        "v_mul_f32 v64, v64, v95\n"
        "v_mul_f32 v65, v65, v95\n"
        "v_mul_f32 v66, v66, v95\n"
        "v_mul_f32 v67, v67, v95\n"
        "v_mul_f32 v84, v84, v95\n"
        "v_mul_f32 v85, v85, v95\n"
        "v_mul_f32 v86, v86, v95\n"
        "v_mul_f32 v87, v87, v95\n"
        "v_mfma_f32_16x16x32_bf16 v[156:159], v[132:135], v[144:147], v[156:159]\n"
        "ds_read_b128 v[148:151], v0 offset:11312\n"
        "v_perm_b32 v120, v65, v64, s63\n"
        "v_perm_b32 v121, v67, v66, s63\n"
        "v_perm_b32 v122, v85, v84, s63\n"
        "v_perm_b32 v123, v87, v86, s63\n"
        "v_mul_f32 v68, v68, v28\n"
        "v_mul_f32 v69, v69, v29\n"
        "v_mul_f32 v70, v70, v30\n"
        "v_mfma_f32_16x16x32_bf16 v[152:155], v[32:35], v[144:147], 0\n"
        "ds_read_b128 v[84:87], v0 offset:10288\n"
        "v_perm_b32 v106, v157, v156, s63\n"
        "v_perm_b32 v107, v159, v158, s63\n"
        "v_mul_f32 v71, v71, v31\n"
        "v_mul_f32 v72, s58, v72\n"
        "v_mul_f32 v68, v68, v95\n"
        "v_mul_f32 v69, v69, v95\n"
        "v_mul_f32 v70, v70, v95\n"
        "v_mfma_f32_16x16x32_bf16 v[152:155], v[36:39], v[116:119], v[152:155]\n"
        "ds_read_b128 v[144:147], v0 offset:9264\n"
        "v_mul_f32 v71, v71, v95\n"
        "v_fma_f32 v72, v72, v95, v19\n"
        "v_perm_b32 v64, v69, v68, s63\n"
        "v_perm_b32 v66, v71, v70, s63\n"
        "v_perm_b32 v65, 0, v72, s63\n"
        "v_mul_f32 v108, v108, v20\n"
        "v_mul_f32 v109, v109, v21\n"
        "v_mfma_f32_16x16x32_bf16 v[152:155], v[60:63], v[112:115], v[152:155]\n"
        "s_waitcnt lgkmcnt(2)\n"
        "v_mul_f32 v110, v110, v22\n"
        "v_mul_f32 v111, v111, v23\n"
        "v_mul_f32 v140, v140, v24\n"
        "v_mul_f32 v141, v141, v25\n"
        "v_mul_f32 v142, v142, v26\n"
        "v_mul_f32 v143, v143, v27\n"
        "v_mul_f32 v108, v108, v164\n"
        "v_perm_b32 v112, v153, v152, s63\n"
        "v_mfma_f32_16x16x32_bf16 v[20:23], v[60:63], v[148:151], 0\n"
        "s_waitcnt lgkmcnt(1)\n"
        "v_perm_b32 v116, v155, v154, s63\n"
        "v_mul_f32 v109, v109, v164\n"
        "v_mul_f32 v110, v110, v164\n"
        "v_mul_f32 v111, v111, v164\n"
        "v_mul_f32 v140, v140, v164\n"
        "v_mul_f32 v141, v141, v164\n"
        "v_mul_f32 v142, v142, v164\n"
        "v_mul_f32 v143, v143, v164\n"
        "v_mfma_f32_16x16x32_bf16 v[20:23], v[36:39], v[84:87], v[20:23]\n"
        "s_waitcnt lgkmcnt(0)\n"
        "v_perm_b32 v24, v109, v108, s63\n"
        "v_perm_b32 v25, v111, v110, s63\n"
        "v_perm_b32 v26, v141, v140, s63\n"
        "v_perm_b32 v27, v143, v142, s63\n"
        "v_mul_f32 v100, v100, v28\n"
        "v_mul_f32 v101, v101, v29\n"
        "v_mul_f32 v102, v102, v30\n"
        "v_mul_f32 v103, v103, v31\n"
        "v_mfma_f32_16x16x32_bf16 v[20:23], v[32:35], v[144:147], v[20:23]\n"
        "v_mul_f32 v44, s58, v44\n"
        "v_mul_f32 v100, v100, v164\n"
        "v_mul_f32 v101, v101, v164\n"
        "v_mul_f32 v102, v102, v164\n"
        "v_mul_f32 v103, v103, v164\n"
        "v_fma_f32 v44, v44, v164, v19\n"
        "v_perm_b32 v28, v101, v100, s63\n"
        "v_perm_b32 v30, v103, v102, s63\n"
        "v_mfma_f32_16x16x32_bf16 v[68:71], v[132:135], v[144:147], 0\n"
        "v_perm_b32 v77, v21, v20, s63\n"
        "v_perm_b32 v81, v23, v22, s63\n"
        "v_perm_b32 v29, 0, v44, s63\n"
        "v_mfma_f32_16x16x32_bf16 v[68:71], v[136:139], v[84:87], v[68:71]\n"
        "v_mfma_f32_16x16x32_bf16 v[68:71], v[52:55], v[148:151], v[68:71]\n"
        "ds_read_b128 v[20:23], v0 offset:12352\n"
        "v_mfma_f32_16x16x32_bf16 v[72:75], v[160:163], v[148:151], 0\n"
        "ds_read_b128 v[108:111], v0 offset:13376\n"
        "v_mfma_f32_16x16x32_bf16 v[72:75], v[128:131], v[84:87], v[72:75]\n"
        "ds_read_b128 v[140:143], v0 offset:14400\n"
        "v_mfma_f32_16x16x32_bf16 v[72:75], v[124:127], v[144:147], v[72:75]\n"
        "s_waitcnt lgkmcnt(2)\n"
        "v_mfma_f32_16x16x32_bf16 v[144:147], v[124:127], v[20:23], 0\n"
        "v_perm_b32 v46, v69, v68, s63\n"
        "v_perm_b32 v47, v71, v70, s63\n"
        "s_waitcnt lgkmcnt(1)\n"
        "v_mfma_f32_16x16x32_bf16 v[144:147], v[128:131], v[108:111], v[144:147]\n"
        "s_waitcnt lgkmcnt(0)\n"
        "v_mfma_f32_16x16x32_bf16 v[144:147], v[160:163], v[140:143], v[144:147]\n"
        "v_perm_b32 v44, v73, v72, s63\n"
        "v_perm_b32 v45, v75, v74, s63\n"
        "v_mfma_f32_16x16x32_bf16 v[148:151], v[52:55], v[140:143], 0\n"
        "v_mfma_f32_16x16x32_bf16 v[148:151], v[136:139], v[108:111], v[148:151]\n"
        "v_mfma_f32_16x16x32_bf16 v[148:151], v[132:135], v[20:23], v[148:151]\n"
        "ds_read_b128 v[72:75], v0 offset:17488\n"
        "v_mfma_f32_16x16x32_bf16 v[84:87], v[32:35], v[20:23], 0\n"
        "ds_read_b128 v[68:71], v0 offset:16464\n"
        "v_perm_b32 v100, v145, v144, s63\n"
        "v_perm_b32 v101, v147, v146, s63\n"
        "v_mfma_f32_16x16x32_bf16 v[84:87], v[36:39], v[108:111], v[84:87]\n"
        "ds_read_b128 v[20:23], v0 offset:15440\n"
        "v_mfma_f32_16x16x32_bf16 v[84:87], v[60:63], v[140:143], v[84:87]\n"
        "v_perm_b32 v102, v149, v148, s63\n"
        "v_perm_b32 v103, v151, v150, s63\n"
        "s_waitcnt lgkmcnt(2)\n"
        "v_mfma_f32_16x16x32_bf16 v[108:111], v[60:63], v[72:75], 0\n"
        "s_waitcnt lgkmcnt(1)\n"
        "v_mfma_f32_16x16x32_bf16 v[108:111], v[36:39], v[68:71], v[108:111]\n"
        "s_waitcnt lgkmcnt(0)\n"
        "v_mfma_f32_16x16x32_bf16 v[108:111], v[32:35], v[20:23], v[108:111]\n"
        "ds_read2_b32 v[82:83], v11 offset0:0 offset1:3\n"
        "v_perm_b32 v89, v85, v84, s63\n"
        "v_perm_b32 v93, v87, v86, s63\n"
        "v_mov_b32_dpp v59, v19 row_shr:4 row_mask:0xf bank_mask:0xf bound_ctrl:0\n"
        "v_mfma_f32_16x16x32_bf16 v[36:39], v[132:135], v[20:23], 0\n"
        "ds_read_b64_tr_b16 v[78:79], v9 offset:0\n"
        "v_mfma_f32_16x16x32_bf16 v[36:39], v[136:139], v[68:71], v[36:39]\n"
        "ds_read2_b32 v[94:95], v11 offset0:1 offset1:4\n"
        "v_perm_b32 v113, v109, v108, s63\n"
        "v_perm_b32 v117, v111, v110, s63\n"
        "v_mfma_f32_16x16x32_bf16 v[36:39], v[52:55], v[72:75], v[36:39]\n"
        "s_add_u32 s31, s31, 1\n"
        "ds_read_b64_tr_b16 v[90:91], v9 offset:16\n"
        "v_mfma_f32_16x16x32_bf16 v[32:35], v[160:163], v[72:75], 0\n"
        "s_cmp_ge_u32 s31, s71\n"
        "ds_read2_b32 v[118:119], v11 offset0:2 offset1:5\n"
        "v_mfma_f32_16x16x32_bf16 v[32:35], v[128:131], v[68:71], v[32:35]\n"
        "s_cbranch_scc1 L52A_%=\n"
        "L53A_%=:\n"
        "ds_read_b64_tr_b16 v[114:115], v9 offset:32\n"
        "v_perm_b32 v110, v37, v36, s63\n"
        "v_perm_b32 v111, v39, v38, s63\n"
        "v_mfma_f32_16x16x32_bf16 v[32:35], v[124:127], v[20:23], v[32:35]\n"
        "s_add_u32 s16, s16, 256\n"
        "v_mfma_f32_16x16x32_bf16 v[20:23], v[48:51], v[40:43], 0\n"
        "s_waitcnt lgkmcnt(4)\n"
        "s_cmp_ge_u32 s31, 20\n"
        "v_smfmac_f32_16x16x64_bf16 v[20:23], v[56:59], v[76:83], v18\n"
        "s_cselect_b32 s39, s22, s16\n"
        "v_mfma_f32_16x16x32_bf16 v[36:39], v[48:51], v[96:99], 0\n"
        "s_waitcnt lgkmcnt(2)\n"
        "v_perm_b32 v108, v33, v32, s63\n"
        "v_perm_b32 v109, v35, v34, s63\n"
        "s_cmp_ge_u32 s31, 21\n"
        "v_smfmac_f32_16x16x64_bf16 v[36:39], v[56:59], v[88:95], v18\n"
        "s_cselect_b32 s39, s23, s39\n"
        "ds_read_b64_tr_b16 v[78:79], v9 offset:48\n"
        "v_mfma_f32_16x16x32_bf16 v[32:35], v[48:51], v[104:107], 0\n"
        "s_waitcnt lgkmcnt(1)\n"
        "s_mul_hi_u32 s38, s39, 58835169\n"
        "v_smfmac_f32_16x16x64_bf16 v[32:35], v[56:59], v[112:119], v18\n"
        "s_mul_i32 s39, s39, 1344\n"
        "ds_read_b64_tr_b16 v[90:91], v9 offset:64\n"
        "v_cvt_pk_bf16_f32 v152, v20, v36\n"
        "v_cvt_pk_bf16_f32 v156, v21, v37\n"
        "v_cvt_pk_bf16_f32 v160, v22, v38\n"
        "v_cvt_pk_bf16_f32 v164, v23, v39\n"
        "v_mfma_f32_16x16x32_bf16 v[68:71], v[48:51], v[44:47], 0\n"
        "s_mul_i32 s38, s38, 588672\n"
        "s_waitcnt lgkmcnt(1)\n"
        "v_smfmac_f32_16x16x64_bf16 v[68:71], v[56:59], v[76:83], v18 abid:1\n"
        "s_add_u32 s39, s39, s38\n"
        "ds_read_b64_tr_b16 v[114:115], v9 offset:80\n"
        "v_mfma_f32_16x16x32_bf16 v[60:63], v[48:51], v[100:103], 0\n"
        "s_add_u32 s39, s39, s17\n"
        "ds_read_b128 v[36:39], v2\n"
        "v_mov_b64 v[20:21], 0\n"
        "v_mov_b64 v[22:23], 0\n"
        "s_waitcnt lgkmcnt(2)\n"
        "v_smfmac_f32_16x16x64_bf16 v[60:63], v[56:59], v[88:95], v18 abid:1\n"
        "s_mul_i32 s68, s31, 3\n"
        "ds_read_b128 v[52:55], v3\n"
        "v_cvt_pk_bf16_f32 v153, v32, v68\n"
        "v_cvt_pk_bf16_f32 v157, v33, v69\n"
        "v_cvt_pk_bf16_f32 v161, v34, v70\n"
        "v_cvt_pk_bf16_f32 v165, v35, v71\n"
        "v_mfma_f32_16x16x32_bf16 v[68:71], v[48:51], v[108:111], 0\n"
        "s_bitcmp1_b32 s31, 0\n"
        "v_mov_b64 v[32:33], 0\n"
        "v_mov_b64 v[34:35], 0\n"
        "s_waitcnt lgkmcnt(2)\n"
        "v_smfmac_f32_16x16x64_bf16 v[68:71], v[56:59], v[112:119], v18 abid:1\n"
        "s_cselect_b32 s38, 3, 6\n"
        "s_waitcnt lgkmcnt(1)\n"
        "v_mov_b32_dpp v67, v19 row_shr:4 row_mask:0xf bank_mask:0xf bound_ctrl:0\n"
        "v_smfmac_f32_16x16x64_bf16 v[20:23], v[36:39], v[40:47], v4\n"
        "s_add_u32 s68, s68, s38\n"
        "v_mov_b64 v[48:49], 0\n"
        "v_mov_b64 v[50:51], 0\n"
        "s_waitcnt lgkmcnt(0)\n"
        "v_smfmac_f32_16x16x64_bf16 v[20:23], v[52:55], v[76:83], v4 abid:1\n"
        "s_add_u32 s37, s35, 0\n"
        "s_cmp_lt_u32 s31, s14\n"
        "v_cvt_pk_bf16_f32 v154, v60, v68\n"
        "v_cvt_pk_bf16_f32 v158, v61, v69\n"
        "v_cvt_pk_bf16_f32 v162, v62, v70\n"
        "v_cvt_pk_bf16_f32 v166, v63, v71\n"
        "v_smfmac_f32_16x16x64_bf16 v[32:35], v[36:39], v[96:103], v4\n"
        "s_cselect_b32 s68, s68, 0\n"
        "v_add_f32 v20, v20, v21\n"
        "v_add_f32 v22, v22, v23\n"
        "v_smfmac_f32_16x16x64_bf16 v[32:35], v[52:55], v[88:95], v4 abid:1\n"
        "s_and_b32 s38, s31, 7\n"
        "ds_read_b64_tr_b16 v[78:79], v9 offset:3328\n"
        "v_smfmac_f32_16x16x64_bf16 v[48:51], v[36:39], v[104:111], v4\n"
        "buffer_store_dwordx3 v[152:154], v12, s[8:11], s37 offen sc0 sc1 nt\n"
        "s_cselect_b32 s30, 1, -7\n"
        "v_smfmac_f32_16x16x64_bf16 v[48:51], v[52:55], v[112:119], v4 abid:1\n"
        "s_mul_i32 s29, s30, 192\n"
        "ds_read_b64_tr_b16 v[90:91], v9 offset:3344\n"
        "v_add_f32 v32, v32, v33\n"
        "v_add_f32 v34, v34, v35\n"
        "v_cvt_pk_bf16_f32 v148, v20, v32\n"
        "v_mfma_f32_16x16x32_bf16 v[152:155], v[120:123], v[40:43], 0\n"
        "s_mul_i32 s30, s30, 392\n"
        "s_waitcnt lgkmcnt(1)\n"
        "v_smfmac_f32_16x16x64_bf16 v[152:155], v[64:67], v[76:83], v18\n"
        "s_add_u32 s37, s35, 0\n"
        "s_cmp_eq_u32 s69, s18\n"
        "ds_read_b64_tr_b16 v[114:115], v9 offset:3360\n"
        "v_add_f32 v48, v48, v49\n"
        "v_add_f32 v50, v50, v51\n"
        "v_cvt_pk_bf16_f32 v149, v48, v22\n"
        "v_cvt_pk_bf16_f32 v150, v34, v50\n"
        "v_mfma_f32_16x16x32_bf16 v[56:59], v[120:123], v[96:99], 0\n"
        "s_cselect_b32 s34, -7488, s70\n"
        "s_waitcnt lgkmcnt(1)\n"
        "v_smfmac_f32_16x16x64_bf16 v[56:59], v[64:67], v[88:95], v18\n"
        "s_add_u32 s69, s69, s34\n"
        "ds_read_b64_tr_b16 v[78:79], v9 offset:3376\n"
        "v_writelane_b32 v10, s69, 56\n"
        "v_add_u32 v11, s30, v11\n"
        "s_mov_b64 exec, s[54:55]\n"
        "v_add_u32 v16, s29, v16\n"
        "v_add_u32 v8, s34, v8\n"
        "s_mov_b64 exec, -1\n"
        "v_mfma_f32_16x16x32_bf16 v[72:75], v[120:123], v[104:107], 0\n"
        "buffer_store_dwordx3 v[156:158], v15, s[8:11], s37 offen offset:768 sc0 sc1 nt\n"
        "s_mov_b64 exec, s[50:51]\n"
        "v_add_u32 v5, s29, v5\n"
        "v_add_u32 v6, s34, v6\n"
        "s_mov_b64 exec, -1\n"
        "s_waitcnt lgkmcnt(1)\n"
        "v_smfmac_f32_16x16x64_bf16 v[72:75], v[64:67], v[112:119], v18\n"
        "ds_read_b64_tr_b16 v[90:91], v9 offset:3392\n"
        "v_mov_b32_dpp v31, v19 row_shr:4 row_mask:0xf bank_mask:0xf bound_ctrl:0\n"
        "v_mfma_f32_16x16x32_bf16 v[20:23], v[120:123], v[44:47], 0\n"
        "s_waitcnt lgkmcnt(1)\n"
        "v_smfmac_f32_16x16x64_bf16 v[20:23], v[64:67], v[76:83], v18 abid:1\n"
        "s_add_u32 s37, s35, 98112\n"
        "ds_read_b64_tr_b16 v[114:115], v9 offset:3408\n"
        "v_mfma_f32_16x16x32_bf16 v[84:87], v[120:123], v[100:103], 0\n"
        "s_waitcnt lgkmcnt(1)\n"
        "v_smfmac_f32_16x16x64_bf16 v[84:87], v[64:67], v[88:95], v18 abid:1\n"
        "ds_read_b64_tr_b16 v[78:79], v9 offset:6656\n"
        "v_mfma_f32_16x16x32_bf16 v[60:63], v[120:123], v[108:111], 0\n"
        "buffer_store_dwordx3 v[160:162], v12, s[8:11], s37 offen offset:192 sc0 sc1 nt\n"
        "ds_read_b32 v167, v0 offset:21604\n"
        "s_waitcnt lgkmcnt(2)\n"
        "v_smfmac_f32_16x16x64_bf16 v[60:63], v[64:67], v[112:119], v18 abid:1\n"
        "ds_read_b64_tr_b16 v[90:91], v9 offset:6672\n"
        "v_mfma_f32_16x16x32_bf16 v[68:71], v[24:27], v[40:43], 0\n"
        "s_waitcnt lgkmcnt(2)\n"
        "v_smfmac_f32_16x16x64_bf16 v[68:71], v[28:31], v[76:83], v18\n"
        "s_add_u32 s37, s35, 98112\n"
        "ds_read_b64_tr_b16 v[114:115], v9 offset:6688\n"
        "v_mfma_f32_16x16x32_bf16 v[36:39], v[24:27], v[96:99], 0\n"
        "s_waitcnt lgkmcnt(1)\n"
        "v_smfmac_f32_16x16x64_bf16 v[36:39], v[28:31], v[88:95], v18\n"
        "ds_read_b64_tr_b16 v[78:79], v9 offset:6704\n"
        "v_readfirstlane_b32 s40, v167\n"
        "v_mfma_f32_16x16x32_bf16 v[40:43], v[24:27], v[104:107], 0\n"
        "buffer_store_dwordx3 v[164:166], v14, s[8:11], s37 offen offset:960 sc0 sc1 nt\n"
        "s_add_u32 s37, s35, 588672\n"
        "s_add_u32 s46, s37, 1344\n"
        "s_cmp_lt_i32 s40, s68\n"
        "s_waitcnt lgkmcnt(1)\n"
        "v_smfmac_f32_16x16x64_bf16 v[40:43], v[28:31], v[112:119], v18\n"
        "buffer_store_dwordx3 v[148:150], v12, s[44:47], s37 offen offset:1152 sc0 sc1 nt\n"
        "ds_read_b64_tr_b16 v[90:91], v9 offset:6720\n"
        "v_mfma_f32_16x16x32_bf16 v[64:67], v[24:27], v[44:47], 0\n"
        "s_waitcnt lgkmcnt(1)\n"
        "v_smfmac_f32_16x16x64_bf16 v[64:67], v[28:31], v[76:83], v18 abid:1\n"
        "ds_read_b64_tr_b16 v[114:115], v9 offset:6736\n"
        "s_mov_b64 exec, 1\n"
        "ds_inc_u32 v0, v1 offset:21608\n"
        "s_mov_b64 exec, -1\n"
        "s_cbranch_scc1 L56A_%=\n"
        "L57A_%=:\n"
        "ds_read_b128 v[124:127], v7 offset:40064\n"
        "ds_read_b128 v[132:135], v7 offset:43392\n"
        "ds_read_b128 v[128:131], v7 offset:40128\n"
        "ds_read_b128 v[136:139], v7 offset:43456\n"
        "v_mfma_f32_16x16x32_bf16 v[104:107], v[24:27], v[100:103], 0\n"
        "s_waitcnt lgkmcnt(6)\n"
        "ds_read_b128 v[160:163], v7 offset:40192\n"
        "ds_read_b128 v[52:55], v7 offset:43520\n"
        "s_mov_b32 s36, s35\n"
        "v_smfmac_f32_16x16x64_bf16 v[104:107], v[28:31], v[88:95], v18 abid:1\n"
        "s_mov_b32 s35, s39\n"
        "v_mfma_f32_16x16x32_bf16 v[88:91], v[24:27], v[108:111], 0\n"
        "s_waitcnt lgkmcnt(7)\n"
        "v_smfmac_f32_16x16x64_bf16 v[88:91], v[28:31], v[112:119], v18 abid:1\n"
        "s_setprio 0\n"
        "s_waitcnt lgkmcnt(0)\n"
        "s_nop 15\n"
        "s_branch CNEXTA_%=\n"
        "MTOPB_%=:\n"
        "s_waitcnt lgkmcnt(5)\n"
        "v_mfma_f32_16x16x32_bf16 v[24:27], v[168:171], v[124:127], 0\n"
        "v_cvt_pk_bf16_f32 v112, v152, v56\n"
        "v_cvt_pk_bf16_f32 v113, v72, v20\n"
        "v_cvt_pk_bf16_f32 v114, v84, v60\n"
        "v_cvt_pk_bf16_f32 v116, v153, v57\n"
        "v_cvt_pk_bf16_f32 v117, v73, v21\n"
        "v_cvt_pk_bf16_f32 v118, v85, v61\n"
        "v_cvt_pk_bf16_f32 v120, v154, v58\n"
        "v_cvt_pk_bf16_f32 v121, v74, v22\n"
        "s_waitcnt lgkmcnt(3)\n"
        "v_mfma_f32_16x16x32_bf16 v[24:27], v[172:175], v[128:131], v[24:27]\n"
        "v_cvt_pk_bf16_f32 v122, v86, v62\n"
        "v_cvt_pk_bf16_f32 v140, v155, v59\n"
        "v_cvt_pk_bf16_f32 v141, v75, v23\n"
        "v_cvt_pk_bf16_f32 v142, v87, v63\n"
        "v_cvt_pk_bf16_f32 v144, v68, v36\n"
        "v_cvt_pk_bf16_f32 v145, v40, v64\n"
        "v_cvt_pk_bf16_f32 v146, v104, v88\n"
        "v_cvt_pk_bf16_f32 v148, v69, v37\n"
        "s_waitcnt lgkmcnt(1)\n"
        "v_mfma_f32_16x16x32_bf16 v[24:27], v[176:179], v[160:163], v[24:27]\n"
        "v_cvt_pk_bf16_f32 v149, v41, v65\n"
        "v_cvt_pk_bf16_f32 v150, v105, v89\n"
        "v_cvt_pk_bf16_f32 v152, v70, v38\n"
        "v_cvt_pk_bf16_f32 v153, v42, v66\n"
        "v_cvt_pk_bf16_f32 v154, v106, v90\n"
        "v_cvt_pk_bf16_f32 v156, v71, v39\n"
        "v_cvt_pk_bf16_f32 v157, v43, v67\n"
        "v_cvt_pk_bf16_f32 v158, v107, v91\n"
        "v_mfma_f32_16x16x32_bf16 v[32:35], v[188:191], v[160:163], 0\n"
        "v_perm_b32 v40, v25, v24, s63\n"
        "v_perm_b32 v41, v27, v26, s63\n"
        "v_mfma_f32_16x16x32_bf16 v[32:35], v[184:187], v[128:131], v[32:35]\n"
        "v_mfma_f32_16x16x32_bf16 v[32:35], v[180:183], v[124:127], v[32:35]\n"
        "s_add_u32 s37, s36, 196224\n"
        "ds_read_b128 v[28:31], v16 offset:128\n"
        "ds_read_b32 v159, v10\n"
        "v_mfma_f32_16x16x32_bf16 v[24:27], v[192:195], v[124:127], 0\n"
        "ds_read_b128 v[20:23], v5\n"
        "v_mfma_f32_16x16x32_bf16 v[24:27], v[196:199], v[128:131], v[24:27]\n"
        "ds_read_b64 v[164:165], v11 offset:192\n"
        "v_mfma_f32_16x16x32_bf16 v[24:27], v[200:203], v[160:163], v[24:27]\n"
        "buffer_store_dwordx3 v[112:114], v12, s[8:11], s37 offen offset:384 sc0 sc1 nt\n"
        "v_perm_b32 v42, v33, v32, s63\n"
        "v_perm_b32 v43, v35, v34, s63\n"
        "s_waitcnt lgkmcnt(3)\n"
        "v_mfma_f32_16x16x32_bf16 v[48:51], v[28:31], v[160:163], 0\n"
        "ds_read_b128 v[60:63], v7 offset:46848\n"
        "ds_read_i16 v167, v0 offset:20584\n"
        "ds_read_i16 v155, v0 offset:20586\n"
        "v_perm_b32 v44, v25, v24, s63\n"
        "v_perm_b32 v45, v27, v26, s63\n"
        "s_waitcnt lgkmcnt(3)\n"
        "v_smfmac_f32_16x16x64_bf16 v[48:51], v[20:23], v[124:131], v4\n"
        "s_add_u32 s37, s36, 196224\n"
        "ds_read_b128 v[32:35], v7 offset:46720\n"
        "ds_read_b128 v[36:39], v7 offset:46784\n"
        "v_mfma_f32_16x16x32_bf16 v[56:59], v[28:31], v[52:55], 0\n"
        "v_smfmac_f32_16x16x64_bf16 v[56:59], v[20:23], v[132:139], v4\n"
        "s_setprio 2\n"
        "s_waitcnt lgkmcnt(4)\n"
        "v_mfma_f32_16x16x32_bf16 v[24:27], v[28:31], v[60:63], 0\n"
        "buffer_store_dwordx3 v[116:118], v13, s[8:11], s37 offen offset:1152 sc0 sc1 nt\n"
        "v_add_f32_dpp v165, v50, v51 quad_perm:[0,1,2,3] row_mask:0x1 bank_mask:0xf\n"
        "s_waitcnt lgkmcnt(2)\n"
        "v_add_u32 v167, v9, v167\n"
        "v_add_f32_dpp v164, v48, v49 quad_perm:[0,1,2,3] row_mask:0x1 bank_mask:0xf\n"
        "v_add_f32_dpp v165, v58, v59 quad_perm:[0,1,2,3] row_mask:0x2 bank_mask:0xf\n"
        "v_add_u32 v155, v9, v155\n"
        "v_add_f32_dpp v164, v56, v57 quad_perm:[0,1,2,3] row_mask:0x2 bank_mask:0xf\n"
        "s_waitcnt lgkmcnt(0)\n"
        "v_smfmac_f32_16x16x64_bf16 v[24:27], v[20:23], v[32:39], v4\n"
        "v_mfma_f32_16x16x32_bf16 v[28:31], v[212:215], v[160:163], 0\n"
        "s_add_u32 s37, s36, 294336\n"
        "v_mfma_f32_16x16x32_bf16 v[28:31], v[208:211], v[128:131], v[28:31]\n"
        "v_mfma_f32_16x16x32_bf16 v[28:31], v[204:207], v[124:127], v[28:31]\n"
        "v_mfma_f32_16x16x32_bf16 v[20:23], v[216:219], v[124:127], 0\n"
        "buffer_store_dwordx3 v[120:122], v12, s[8:11], s37 offen offset:576 sc0 sc1 nt\n"
        "v_mfma_f32_16x16x32_bf16 v[20:23], v[220:223], v[128:131], v[20:23]\n"
        "v_mfma_f32_16x16x32_bf16 v[20:23], v[224:227], v[160:163], v[20:23]\n"
        "s_add_u32 s37, s36, 392448\n"
        "v_add_f32_dpp v165, v26, v27 quad_perm:[0,1,2,3] row_mask:0x4 bank_mask:0xf\n"
        "v_mul_f32 v166, s52, v165\n"
        "v_fma_f32 v166, -v166, v165, v159\n"
        "v_perm_b32 v46, v29, v28, s63\n"
        "v_perm_b32 v47, v31, v30, s63\n"
        "v_add_f32_e64 v166, s53, |v166|\n"
        "v_rsq_f32 v159, v166\n"
        "v_mfma_f32_16x16x32_bf16 v[56:59], v[236:239], v[160:163], 0\n"
        "s_mov_b32 exec_hi, 0xffff\n"
        "ds_write_b32 v10, v159\n"
        "s_mov_b32 exec_hi, -1\n"
        "v_perm_b32 v48, v21, v20, s63\n"
        "v_perm_b32 v49, v23, v22, s63\n"
        "ds_read_b32 v20, v167 offset:40064\n"
        "v_add_f32_dpp v164, v24, v25 quad_perm:[0,1,2,3] row_mask:0x4 bank_mask:0xf\n"
        "v_mfma_f32_16x16x32_bf16 v[56:59], v[232:235], v[128:131], v[56:59]\n"
        "s_mov_b32 exec_hi, 0xffff\n"
        "v_add_u32 v10, -40064, v10\n"
        "s_mov_b32 exec_hi, -1\n"
        "ds_read_b32 v21, v167 offset:40896\n"
        "ds_read_b32 v22, v167 offset:41728\n"
        "ds_read_b32 v23, v167 offset:42560\n"
        "ds_read_b32 v24, v167 offset:43392\n"
        "v_mul_f32 v164, v164, v159\n"
        "v_mfma_f32_16x16x32_bf16 v[56:59], v[228:231], v[124:127], v[56:59]\n"
        "buffer_store_dwordx3 v[140:142], v12, s[8:11], s37 offen sc0 sc1 nt\n"
        "ds_read_b32 v25, v167 offset:44224\n"
        "ds_read_b32 v26, v167 offset:45056\n"
        "ds_read_b32 v27, v167 offset:45888\n"
        "ds_read_b32 v28, v167 offset:46720\n"
        "ds_read_b32 v29, v167 offset:47552\n"
        "ds_read_b32 v30, v167 offset:48384\n"
        "ds_read_b32 v31, v167 offset:49216\n"
        "v_perm_b32 v50, v57, v56, s63\n"
        "v_perm_b32 v51, v59, v58, s63\n"
        "v_readlane_b32 s58, v159, 56\n"
        "v_mfma_f32_16x16x32_bf16 v[56:59], v[124:127], v[40:43], 0\n"
        "s_mov_b64 exec, s[56:57]\n"
        "v_mov_b32 v164, 0xff800000\n"
        "s_mov_b64 exec, -1\n"
        "ds_read_b32 v165, v155 offset:40064\n"
        "ds_read_b32 v166, v155 offset:43392\n"
        "v_max_f32_dpp v151, v164, v164 row_ror:8 row_mask:0xf bank_mask:0xf\n"
        "v_mfma_f32_16x16x32_bf16 v[56:59], v[128:131], v[44:47], v[56:59]\n"
        "s_add_u32 s37, s36, 392448\n"
        "v_max_f32_dpp v151, v151, v151 row_ror:4 row_mask:0xf bank_mask:0xf\n"
        "ds_read_b32 v167, v155 offset:46720\n"
        "v_mfma_f32_16x16x32_bf16 v[56:59], v[160:163], v[48:51], v[56:59]\n"
        "v_max_f32_dpp v151, v151, v151 row_ror:2 row_mask:0xf bank_mask:0xf\n"
        "v_mfma_f32_16x16x32_bf16 v[72:75], v[52:55], v[48:51], 0\n"
        "v_mfma_f32_16x16x32_bf16 v[72:75], v[136:139], v[44:47], v[72:75]\n"
        "buffer_store_dwordx3 v[144:146], v15, s[8:11], s37 offen offset:768 sc0 sc1 nt\n"
        "v_max_f32_dpp v151, v151, v151 row_ror:1 row_mask:0xf bank_mask:0xf\n"
        "v_mfma_f32_16x16x32_bf16 v[72:75], v[132:135], v[40:43], v[72:75]\n"
        "ds_read_b128 v[88:91], v8 offset:128\n"
        "v_mfma_f32_16x16x32_bf16 v[68:71], v[32:35], v[40:43], 0\n"
        "s_add_u32 s37, s36, 490560\n"
        "ds_read2_b32 v[92:93], v6 offset0:0 offset1:2\n"
        "ds_read2_b32 v[94:95], v6 offset0:8 offset1:10\n"
        "s_waitcnt lgkmcnt(3)\n"
        "v_mul_f32 v56, v56, v20\n"
        "v_mul_f32 v57, v57, v21\n"
        "v_mul_f32 v58, v58, v22\n"
        "v_mul_f32 v59, v59, v23\n"
        "v_mul_f32 v72, v72, v24\n"
        "v_mfma_f32_16x16x32_bf16 v[68:71], v[36:39], v[44:47], v[68:71]\n"
        "v_max3_f32 v155, v56, v57, v58\n"
        "v_mul_f32 v73, v73, v25\n"
        "v_mul_f32 v74, v74, v26\n"
        "v_max3_f32 v155, v155, v59, v72\n"
        "v_mul_f32 v75, v75, v27\n"
        "v_max3_f32 v155, v155, v73, v74\n"
        "v_mfma_f32_16x16x32_bf16 v[68:71], v[60:63], v[48:51], v[68:71]\n"
        "s_waitcnt lgkmcnt(2)\n"
        "v_mfma_f32_16x16x32_bf16 v[76:79], v[88:91], v[48:51], 0\n"
        "buffer_store_dwordx3 v[148:150], v12, s[8:11], s37 offen offset:192 sc0 sc1 nt\n"
        "s_waitcnt lgkmcnt(0)\n"
        "v_smfmac_f32_16x16x64_bf16 v[76:79], v[92:95], v[40:47], v4\n"
        "v_mfma_f32_16x16x32_bf16 v[48:51], v[168:171], v[132:135], 0\n"
        "s_add_u32 s37, s36, 490560\n"
        "v_mfma_f32_16x16x32_bf16 v[48:51], v[172:175], v[136:139], v[48:51]\n"
        "v_mul_f32 v68, v68, v28\n"
        "v_mul_f32 v69, v69, v29\n"
        "v_mul_f32 v70, v70, v30\n"
        "v_max3_f32 v155, v155, v75, v68\n"
        "v_mul_f32 v71, v71, v31\n"
        "v_add_f32 v76, v76, v77\n"
        "v_mul_f32 v76, s58, v76\n"
        "v_max3_f32 v155, v155, v69, v70\n"
        "v_mfma_f32_16x16x32_bf16 v[48:51], v[176:179], v[52:55], v[48:51]\n"
        "s_mov_b64 exec, s[48:49]\n"
        "v_mov_b32 v76, 0xff800000\n"
        "s_mov_b64 exec, -1\n"
        "v_max3_f32 v155, v155, v71, v76\n"
        "v_mfma_f32_16x16x32_bf16 v[44:47], v[188:191], v[52:55], 0\n"
        "buffer_store_dwordx3 v[152:154], v14, s[8:11], s37 offen offset:960 sc0 sc1 nt\n"
        "v_permlane32_swap_b32 v155, v151\n"
        "v_max_f32 v155, v155, v151\n"
        "v_perm_b32 v40, v49, v48, s63\n"
        "v_perm_b32 v41, v51, v50, s63\n"
        "v_mov_b32 v154, v155\n"
        "v_mfma_f32_16x16x32_bf16 v[44:47], v[184:187], v[136:139], v[44:47]\n"
        "v_mfma_f32_16x16x32_bf16 v[44:47], v[180:183], v[132:135], v[44:47]\n"
        "s_add_u32 s37, s36, 588672\n"
        "v_permlane16_swap_b32 v155, v154\n"
        "v_max_f32 v155, v155, v154\n"
        "v_mov_b32 v151, v155\n"
        "v_mfma_f32_16x16x32_bf16 v[64:67], v[192:195], v[132:135], 0\n"
        "v_mfma_f32_16x16x32_bf16 v[64:67], v[196:199], v[136:139], v[64:67]\n"
        "v_permlane32_swap_b32 v155, v151\n"
        "v_mul_f32 v155, v155, v165\n"
        "v_perm_b32 v42, v45, v44, s63\n"
        "v_perm_b32 v43, v47, v46, s63\n"
        "v_fma_f32 v56, v56, v165, -v155\n"
        "v_fma_f32 v57, v57, v165, -v155\n"
        "v_fma_f32 v58, v58, v165, -v155\n"
        "v_mfma_f32_16x16x32_bf16 v[64:67], v[200:203], v[52:55], v[64:67]\n"
        "buffer_store_dwordx3 v[156:158], v12, s[8:11], s37 offen offset:384 sc0 sc1 nt\n"
        "v_exp_f32 v56, v56\n"
        "v_exp_f32 v57, v57\n"
        "v_exp_f32 v58, v58\n"
        "v_fma_f32 v59, v59, v165, -v155\n"
        "v_add_f32 v158, v56, v57\n"
        "v_exp_f32 v59, v59\n"
        "v_mfma_f32_16x16x32_bf16 v[48:51], v[212:215], v[52:55], 0\n"
        "v_perm_b32 v44, v65, v64, s63\n"
        "v_perm_b32 v45, v67, v66, s63\n"
        "v_fma_f32 v72, v72, v165, -v155\n"
        "v_add_f32 v158, v158, v58\n"
        "v_exp_f32 v72, v72\n"
        "v_fma_f32 v73, v73, v165, -v155\n"
        "v_add_f32 v158, v158, v59\n"
        "v_mfma_f32_16x16x32_bf16 v[48:51], v[208:211], v[136:139], v[48:51]\n"
        "v_exp_f32 v73, v73\n"
        "v_fma_f32 v74, v74, v165, -v155\n"
        "v_add_f32 v158, v158, v72\n"
        "v_exp_f32 v74, v74\n"
        "v_fma_f32 v75, v75, v165, -v155\n"
        "v_sub_f32 v164, v164, v151\n"
        "v_mfma_f32_16x16x32_bf16 v[48:51], v[204:207], v[132:135], v[48:51]\n"
        "v_add_f32 v158, v158, v73\n"
        "v_exp_f32 v75, v75\n"
        "v_fma_f32 v68, v68, v165, -v155\n"
        "v_mul_f32 v164, s58, v164\n"
        "v_add_f32 v158, v158, v74\n"
        "v_exp_f32 v68, v68\n"
        "v_mfma_f32_16x16x32_bf16 v[64:67], v[216:219], v[132:135], 0\n"
        "v_fma_f32 v69, v69, v165, -v155\n"
        "v_perm_b32 v46, v49, v48, s63\n"
        "v_perm_b32 v47, v51, v50, s63\n"
        "v_exp_f32 v164, v164\n"
        "v_add_f32 v158, v158, v75\n"
        "v_exp_f32 v69, v69\n"
        "v_mfma_f32_16x16x32_bf16 v[64:67], v[220:223], v[136:139], v[64:67]\n"
        "v_fma_f32 v70, v70, v165, -v155\n"
        "v_add_f32 v158, v158, v68\n"
        "v_exp_f32 v70, v70\n"
        "v_fma_f32 v71, v71, v165, -v155\n"
        "v_add_f32_dpp v157, v164, v164 row_ror:8 row_mask:0xf bank_mask:0xf\n"
        "v_add_f32 v158, v158, v69\n"
        "v_fma_f32 v76, v76, v165, -v155\n"
        "v_mfma_f32_16x16x32_bf16 v[64:67], v[224:227], v[52:55], v[64:67]\n"
        "v_exp_f32 v71, v71\n"
        "v_add_f32_dpp v157, v157, v157 row_ror:4 row_mask:0xf bank_mask:0xf\n"
        "v_add_f32 v158, v158, v70\n"
        "v_exp_f32 v76, v76\n"
        "v_add_f32_dpp v157, v157, v157 row_ror:2 row_mask:0xf bank_mask:0xf\n"
        "v_add_f32 v158, v158, v71\n"
        "v_mfma_f32_16x16x32_bf16 v[48:51], v[236:239], v[52:55], 0\n"
        "v_add_f32_dpp v157, v157, v157 row_ror:1 row_mask:0xf bank_mask:0xf\n"
        "v_perm_b32 v80, v65, v64, s63\n"
        "v_perm_b32 v81, v67, v66, s63\n"
        "v_add_f32 v158, v158, v76\n"
        "v_mul_f32 v56, v56, v20\n"
        "v_mul_f32 v57, v57, v21\n"
        "v_permlane32_swap_b32 v158, v157\n"
        "v_mfma_f32_16x16x32_bf16 v[48:51], v[232:235], v[136:139], v[48:51]\n"
        "v_add_f32 v158, v158, v157\n"
        "v_mov_b32 v165, v158\n"
        "v_mul_f32 v58, v58, v22\n"
        "v_mul_f32 v59, v59, v23\n"
        "v_permlane16_swap_b32 v158, v165\n"
        "v_add_f32 v158, v158, v165\n"
        "v_mul_f32 v72, v72, v24\n"
        "v_mfma_f32_16x16x32_bf16 v[48:51], v[228:231], v[132:135], v[48:51]\n"
        "v_rcp_f32 v158, v158\n"
        "v_mul_f32 v73, v73, v25\n"
        "v_mov_b32 v157, v158\n"
        "v_mul_f32 v74, v74, v26\n"
        "v_mul_f32 v75, v75, v27\n"
        "v_permlane32_swap_b32 v158, v157\n"
        "v_mfma_f32_16x16x32_bf16 v[64:67], v[124:127], v[40:43], 0\n"
        "v_mul_f32 v56, v56, v158\n"
        "v_perm_b32 v82, v49, v48, s63\n"
        "v_perm_b32 v83, v51, v50, s63\n"
        "v_mul_f32 v57, v57, v158\n"
        "v_mul_f32 v58, v58, v158\n"
        "v_mul_f32 v59, v59, v158\n"
        "v_mul_f32 v72, v72, v158\n"
        "v_mul_f32 v73, v73, v158\n"
        "v_mfma_f32_16x16x32_bf16 v[64:67], v[128:131], v[44:47], v[64:67]\n"
        "v_mul_f32 v74, v74, v158\n"
        "v_mul_f32 v75, v75, v158\n"
        "v_perm_b32 v48, v57, v56, s63\n"
        "v_perm_b32 v49, v59, v58, s63\n"
        "v_perm_b32 v50, v73, v72, s63\n"
        "v_perm_b32 v51, v75, v74, s63\n"
        "v_mul_f32 v68, v68, v28\n"
        "v_mul_f32 v69, v69, v29\n"
        "v_mfma_f32_16x16x32_bf16 v[64:67], v[160:163], v[80:83], v[64:67]\n"
        "v_mul_f32 v70, v70, v30\n"
        "v_mul_f32 v71, v71, v31\n"
        "v_mul_f32 v76, s58, v76\n"
        "v_mul_f32 v68, v68, v158\n"
        "v_mul_f32 v69, v69, v158\n"
        "v_mul_f32 v70, v70, v158\n"
        "v_mul_f32 v71, v71, v158\n"
        "v_fma_f32 v76, v76, v158, v19\n"
        "v_mfma_f32_16x16x32_bf16 v[84:87], v[52:55], v[80:83], 0\n"
        "v_mul_f32 v64, v64, v20\n"
        "v_mul_f32 v65, v65, v21\n"
        "v_mul_f32 v66, v66, v22\n"
        "v_mul_f32 v67, v67, v23\n"
        "v_max3_f32 v165, v64, v65, v66\n"
        "v_perm_b32 v56, v69, v68, s63\n"
        "v_perm_b32 v58, v71, v70, s63\n"
        "v_perm_b32 v57, 0, v76, s63\n"
        "v_mfma_f32_16x16x32_bf16 v[84:87], v[136:139], v[44:47], v[84:87]\n"
        "v_mul_f32 v164, v164, v159\n"
        "v_mul_f32 v164, v164, v157\n"
        "v_perm_b32 v164, 0, v164, s63\n"
        "ds_write_b16 v1, v164\n"
        "v_mfma_f32_16x16x32_bf16 v[84:87], v[132:135], v[40:43], v[84:87]\n"
        "v_mfma_f32_16x16x32_bf16 v[68:71], v[32:35], v[40:43], 0\n"
        "v_mfma_f32_16x16x32_bf16 v[68:71], v[36:39], v[44:47], v[68:71]\n"
        "v_mfma_f32_16x16x32_bf16 v[68:71], v[60:63], v[80:83], v[68:71]\n"
        "v_mfma_f32_16x16x32_bf16 v[72:75], v[88:91], v[80:83], 0\n"
        "v_smfmac_f32_16x16x64_bf16 v[72:75], v[92:95], v[40:47], v4\n"
        "v_mfma_f32_16x16x32_bf16 v[80:83], v[168:171], v[32:35], 0\n"
        "v_mfma_f32_16x16x32_bf16 v[80:83], v[172:175], v[36:39], v[80:83]\n"
        "v_mfma_f32_16x16x32_bf16 v[80:83], v[176:179], v[60:63], v[80:83]\n"
        "v_mul_f32 v84, v84, v24\n"
        "v_mul_f32 v85, v85, v25\n"
        "v_mul_f32 v86, v86, v26\n"
        "v_max3_f32 v165, v165, v67, v84\n"
        "v_mul_f32 v87, v87, v27\n"
        "v_add_f32 v72, v72, v73\n"
        "v_mul_f32 v68, v68, v28\n"
        "v_mul_f32 v72, s58, v72\n"
        "v_mfma_f32_16x16x32_bf16 v[40:43], v[188:191], v[60:63], 0\n"
        "s_mov_b64 exec, s[48:49]\n"
        "v_mov_b32 v72, 0xff800000\n"
        "s_mov_b64 exec, -1\n"
        "v_perm_b32 v76, v81, v80, s63\n"
        "v_perm_b32 v77, v83, v82, s63\n"
        "v_max3_f32 v165, v165, v85, v86\n"
        "v_mul_f32 v69, v69, v29\n"
        "v_mul_f32 v70, v70, v30\n"
        "v_max3_f32 v165, v165, v87, v68\n"
        "v_mfma_f32_16x16x32_bf16 v[40:43], v[184:187], v[36:39], v[40:43]\n"
        "v_mul_f32 v71, v71, v31\n"
        "v_max3_f32 v165, v165, v69, v70\n"
        "v_max3_f32 v165, v165, v71, v72\n"
        "v_mfma_f32_16x16x32_bf16 v[40:43], v[180:183], v[32:35], v[40:43]\n"
        "v_mfma_f32_16x16x32_bf16 v[100:103], v[192:195], v[32:35], 0\n"
        "v_mfma_f32_16x16x32_bf16 v[100:103], v[196:199], v[36:39], v[100:103]\n"
        "v_mfma_f32_16x16x32_bf16 v[100:103], v[200:203], v[60:63], v[100:103]\n"
        "v_mfma_f32_16x16x32_bf16 v[96:99], v[212:215], v[60:63], 0\n"
        "v_mfma_f32_16x16x32_bf16 v[96:99], v[208:211], v[36:39], v[96:99]\n"
        "v_mfma_f32_16x16x32_bf16 v[96:99], v[204:207], v[32:35], v[96:99]\n"
        "v_mfma_f32_16x16x32_bf16 v[44:47], v[216:219], v[32:35], 0\n"
        "v_mfma_f32_16x16x32_bf16 v[44:47], v[220:223], v[36:39], v[44:47]\n"
        "v_perm_b32 v78, v41, v40, s63\n"
        "v_perm_b32 v79, v43, v42, s63\n"
        "v_mfma_f32_16x16x32_bf16 v[44:47], v[224:227], v[60:63], v[44:47]\n"
        "v_perm_b32 v80, v101, v100, s63\n"
        "v_perm_b32 v81, v103, v102, s63\n"
        "v_mfma_f32_16x16x32_bf16 v[40:43], v[236:239], v[60:63], 0\n"
        "v_perm_b32 v82, v97, v96, s63\n"
        "v_perm_b32 v83, v99, v98, s63\n"
        "v_mfma_f32_16x16x32_bf16 v[40:43], v[232:235], v[36:39], v[40:43]\n"
        "v_mfma_f32_16x16x32_bf16 v[40:43], v[228:231], v[32:35], v[40:43]\n"
        "v_mfma_f32_16x16x32_bf16 v[108:111], v[124:127], v[76:79], 0\n"
        "v_perm_b32 v96, v45, v44, s63\n"
        "v_perm_b32 v97, v47, v46, s63\n"
        "v_mfma_f32_16x16x32_bf16 v[108:111], v[128:131], v[80:83], v[108:111]\n"
        "s_nop 3\n"
        "v_perm_b32 v98, v41, v40, s63\n"
        "v_perm_b32 v99, v43, v42, s63\n"
        "s_nop 1\n"
        "v_mfma_f32_16x16x32_bf16 v[108:111], v[160:163], v[96:99], v[108:111]\n"
        "v_mfma_f32_16x16x32_bf16 v[140:143], v[52:55], v[96:99], 0\n"
        "v_mfma_f32_16x16x32_bf16 v[140:143], v[136:139], v[80:83], v[140:143]\n"
        "v_mfma_f32_16x16x32_bf16 v[140:143], v[132:135], v[76:79], v[140:143]\n"
        "v_mfma_f32_16x16x32_bf16 v[100:103], v[32:35], v[76:79], 0\n"
        "v_mfma_f32_16x16x32_bf16 v[100:103], v[36:39], v[80:83], v[100:103]\n"
        "v_mfma_f32_16x16x32_bf16 v[100:103], v[60:63], v[96:99], v[100:103]\n"
        "v_mfma_f32_16x16x32_bf16 v[44:47], v[88:91], v[96:99], 0\n"
        "v_smfmac_f32_16x16x64_bf16 v[44:47], v[92:95], v[76:83], v4\n"
        "v_mul_f32 v108, v108, v20\n"
        "v_mul_f32 v109, v109, v21\n"
        "v_mul_f32 v110, v110, v22\n"
        "v_mul_f32 v111, v111, v23\n"
        "v_mul_f32 v140, v140, v24\n"
        "v_max3_f32 v159, v108, v109, v110\n"
        "v_mul_f32 v141, v141, v25\n"
        "v_mfma_f32_16x16x32_bf16 v[60:63], v[124:127], v[240:243], 0\n"
        "v_add_f32 v44, v44, v45\n"
        "v_mul_f32 v142, v142, v26\n"
        "v_max3_f32 v159, v159, v111, v140\n"
        "v_mul_f32 v143, v143, v27\n"
        "v_mul_f32 v100, v100, v28\n"
        "v_mul_f32 v44, s58, v44\n"
        "v_max3_f32 v159, v159, v141, v142\n"
        "v_mul_f32 v101, v101, v29\n"
        "v_mfma_f32_16x16x32_bf16 v[60:63], v[128:131], v[244:247], v[60:63]\n"
        "s_mov_b64 exec, s[48:49]\n"
        "v_mov_b32 v44, 0xff800000\n"
        "s_mov_b64 exec, -1\n"
        "v_mul_f32 v102, v102, v30\n"
        "v_max3_f32 v159, v159, v143, v100\n"
        "v_mul_f32 v103, v103, v31\n"
        "v_max3_f32 v159, v159, v101, v102\n"
        "v_max3_f32 v159, v159, v103, v44\n"
        "v_mfma_f32_16x16x32_bf16 v[60:63], v[160:163], v[248:251], v[60:63]\n"
        "ds_read_b128 v[32:35], v17 offset:40064\n"
        "v_permlane32_swap_b32 v165, v159\n"
        "v_max_f32 v165, v165, v159\n"
        "v_mov_b32 v164, v165\n"
        "v_mfma_f32_16x16x32_bf16 v[76:79], v[52:55], v[248:251], 0\n"
        "ds_read_b128 v[36:39], v17 offset:40128\n"
        "v_permlane16_swap_b32 v165, v164\n"
        "v_max_f32 v165, v165, v164\n"
        "v_perm_b32 v40, v61, v60, s63\n"
        "v_perm_b32 v41, v63, v62, s63\n"
        "v_mov_b32 v159, v165\n"
        "v_mfma_f32_16x16x32_bf16 v[76:79], v[136:139], v[244:247], v[76:79]\n"
        "ds_read_b128 v[60:63], v17 offset:40192\n"
        "v_permlane32_swap_b32 v165, v159\n"
        "v_mul_f32 v165, v165, v166\n"
        "v_mul_f32 v159, v159, v167\n"
        "v_fma_f32 v64, v64, v166, -v165\n"
        "v_fma_f32 v65, v65, v166, -v165\n"
        "v_fma_f32 v108, v108, v167, -v159\n"
        "v_mfma_f32_16x16x32_bf16 v[76:79], v[132:135], v[240:243], v[76:79]\n"
        "s_waitcnt lgkmcnt(2)\n"
        "ds_read_b128 v[96:99], v0 offset:5136\n"
        "v_fma_f32 v109, v109, v167, -v159\n"
        "v_exp_f32 v64, v64\n"
        "v_exp_f32 v65, v65\n"
        "v_fma_f32 v66, v66, v166, -v165\n"
        "v_fma_f32 v110, v110, v167, -v159\n"
        "v_mfma_f32_16x16x32_bf16 v[80:83], v[32:35], v[240:243], 0\n"
        "s_waitcnt lgkmcnt(2)\n"
        "ds_read_b128 v[104:107], v0 offset:4112\n"
        "v_perm_b32 v42, v77, v76, s63\n"
        "v_perm_b32 v43, v79, v78, s63\n"
        "v_exp_f32 v108, v108\n"
        "v_exp_f32 v109, v109\n"
        "v_fma_f32 v67, v67, v166, -v165\n"
        "v_mfma_f32_16x16x32_bf16 v[80:83], v[36:39], v[244:247], v[80:83]\n"
        "s_waitcnt lgkmcnt(2)\n"
        "v_exp_f32 v66, v66\n"
        "v_exp_f32 v110, v110\n"
        "v_fma_f32 v111, v111, v167, -v159\n"
        "v_add_f32 v95, v64, v65\n"
        "v_exp_f32 v67, v67\n"
        "v_mfma_f32_16x16x32_bf16 v[80:83], v[60:63], v[248:251], v[80:83]\n"
        "s_waitcnt lgkmcnt(1)\n"
        "v_fma_f32 v84, v84, v166, -v165\n"
        "v_add_f32 v164, v108, v109\n"
        "v_exp_f32 v111, v111\n"
        "v_fma_f32 v140, v140, v167, -v159\n"
        "v_add_f32 v95, v95, v66\n"
        "v_exp_f32 v84, v84\n"
        "v_mfma_f32_16x16x32_bf16 v[112:115], v[60:63], v[96:99], 0\n"
        "s_waitcnt lgkmcnt(0)\n"
        "v_perm_b32 v76, v81, v80, s63\n"
        "v_perm_b32 v80, v83, v82, s63\n"
        "v_fma_f32 v85, v85, v166, -v165\n"
        "v_add_f32 v164, v164, v110\n"
        "v_exp_f32 v140, v140\n"
        "v_fma_f32 v141, v141, v167, -v159\n"
        "v_add_f32 v95, v95, v67\n"
        "v_mfma_f32_16x16x32_bf16 v[112:115], v[36:39], v[104:107], v[112:115]\n"
        "v_exp_f32 v85, v85\n"
        "v_fma_f32 v86, v86, v166, -v165\n"
        "v_add_f32 v164, v164, v111\n"
        "v_exp_f32 v141, v141\n"
        "v_fma_f32 v142, v142, v167, -v159\n"
        "v_add_f32 v95, v95, v84\n"
        "v_mfma_f32_16x16x32_bf16 v[112:115], v[32:35], v[252:255], v[112:115]\n"
        "v_exp_f32 v86, v86\n"
        "v_fma_f32 v87, v87, v166, -v165\n"
        "v_add_f32 v164, v164, v140\n"
        "v_exp_f32 v142, v142\n"
        "v_fma_f32 v143, v143, v167, -v159\n"
        "v_add_f32 v95, v95, v85\n"
        "v_mfma_f32_16x16x32_bf16 v[120:123], v[132:135], v[252:255], 0\n"
        "v_exp_f32 v87, v87\n"
        "v_perm_b32 v88, v113, v112, s63\n"
        "v_perm_b32 v92, v115, v114, s63\n"
        "v_fma_f32 v68, v68, v166, -v165\n"
        "v_add_f32 v164, v164, v141\n"
        "v_exp_f32 v143, v143\n"
        "v_mfma_f32_16x16x32_bf16 v[120:123], v[136:139], v[104:107], v[120:123]\n"
        "v_fma_f32 v100, v100, v167, -v159\n"
        "v_add_f32 v95, v95, v86\n"
        "v_exp_f32 v68, v68\n"
        "v_fma_f32 v69, v69, v166, -v165\n"
        "v_add_f32 v164, v164, v142\n"
        "v_exp_f32 v100, v100\n"
        "v_mfma_f32_16x16x32_bf16 v[120:123], v[52:55], v[96:99], v[120:123]\n"
        "ds_read_b128 v[144:147], v0 offset:6176\n"
        "v_fma_f32 v101, v101, v167, -v159\n"
        "v_add_f32 v95, v95, v87\n"
        "v_exp_f32 v69, v69\n"
        "v_fma_f32 v70, v70, v166, -v165\n"
        "v_add_f32 v164, v164, v143\n"
        "v_fma_f32 v102, v102, v167, -v159\n"
        "v_mfma_f32_16x16x32_bf16 v[148:151], v[160:163], v[96:99], 0\n"
        "ds_read_b128 v[116:119], v0 offset:7200\n"
        "v_perm_b32 v98, v121, v120, s63\n"
        "v_perm_b32 v99, v123, v122, s63\n"
        "v_exp_f32 v101, v101\n"
        "v_add_f32 v95, v95, v68\n"
        "v_exp_f32 v70, v70\n"
        "v_mfma_f32_16x16x32_bf16 v[148:151], v[128:131], v[104:107], v[148:151]\n"
        "ds_read_b128 v[112:115], v0 offset:8224\n"
        "v_fma_f32 v71, v71, v166, -v165\n"
        "v_add_f32 v164, v164, v100\n"
        "v_exp_f32 v102, v102\n"
        "v_fma_f32 v103, v103, v167, -v159\n"
        "v_add_f32 v95, v95, v69\n"
        "v_fma_f32 v72, v72, v166, -v165\n"
        "v_mfma_f32_16x16x32_bf16 v[148:151], v[124:127], v[252:255], v[148:151]\n"
        "s_waitcnt lgkmcnt(2)\n"
        "v_exp_f32 v71, v71\n"
        "v_add_f32 v164, v164, v101\n"
        "v_exp_f32 v103, v103\n"
        "v_fma_f32 v44, v44, v167, -v159\n"
        "v_add_f32 v95, v95, v70\n"
        "v_add_f32 v164, v164, v102\n"
        "v_mfma_f32_16x16x32_bf16 v[120:123], v[124:127], v[144:147], 0\n"
        "s_waitcnt lgkmcnt(1)\n"
        "v_perm_b32 v96, v149, v148, s63\n"
        "v_perm_b32 v97, v151, v150, s63\n"
        "v_exp_f32 v72, v72\n"
        "v_exp_f32 v44, v44\n"
        "v_add_f32 v95, v95, v71\n"
        "v_add_f32 v164, v164, v103\n"
        "v_mfma_f32_16x16x32_bf16 v[120:123], v[128:131], v[116:119], v[120:123]\n"
        "s_waitcnt lgkmcnt(0)\n"
        "v_add_f32 v95, v95, v72\n"
        "v_add_f32 v164, v164, v44\n"
        "v_mul_f32 v64, v64, v20\n"
        "v_mul_f32 v65, v65, v21\n"
        "v_permlane32_swap_b32 v95, v164\n"
        "v_add_f32 v95, v95, v164\n"
        "v_mov_b32 v167, v95\n"
        "v_mfma_f32_16x16x32_bf16 v[120:123], v[160:163], v[112:115], v[120:123]\n"
        "v_mul_f32 v66, v66, v22\n"
        "v_permlane16_swap_b32 v95, v167\n"
        "v_add_f32 v95, v95, v167\n"
        "v_rcp_f32 v95, v95\n"
        "v_mul_f32 v67, v67, v23\n"
        "v_mov_b32 v164, v95\n"
        "v_mfma_f32_16x16x32_bf16 v[156:159], v[52:55], v[112:115], 0\n"
        "v_mul_f32 v84, v84, v24\n"
        "v_perm_b32 v104, v121, v120, s63\n"
        "v_perm_b32 v105, v123, v122, s63\n"
        "v_permlane32_swap_b32 v95, v164\n"
        "v_mul_f32 v85, v85, v25\n"
        "v_mul_f32 v86, v86, v26\n"
        "v_mul_f32 v87, v87, v27\n"
        "v_mfma_f32_16x16x32_bf16 v[156:159], v[136:139], v[116:119], v[156:159]\n"
        "v_mul_f32 v64, v64, v95\n"
        "v_mul_f32 v65, v65, v95\n"
        "v_mul_f32 v66, v66, v95\n"
        "v_mul_f32 v67, v67, v95\n"
        "v_mul_f32 v84, v84, v95\n"
        "v_mul_f32 v85, v85, v95\n"
        "v_mul_f32 v86, v86, v95\n"
        "v_mul_f32 v87, v87, v95\n"
        "v_mfma_f32_16x16x32_bf16 v[156:159], v[132:135], v[144:147], v[156:159]\n"
        "ds_read_b128 v[148:151], v0 offset:11312\n"
        "v_perm_b32 v120, v65, v64, s63\n"
        "v_perm_b32 v121, v67, v66, s63\n"
        "v_perm_b32 v122, v85, v84, s63\n"
        "v_perm_b32 v123, v87, v86, s63\n"
        "v_mul_f32 v68, v68, v28\n"
        "v_mul_f32 v69, v69, v29\n"
        "v_mul_f32 v70, v70, v30\n"
        "v_mfma_f32_16x16x32_bf16 v[152:155], v[32:35], v[144:147], 0\n"
        "ds_read_b128 v[84:87], v0 offset:10288\n"
        "v_perm_b32 v106, v157, v156, s63\n"
        "v_perm_b32 v107, v159, v158, s63\n"
        "v_mul_f32 v71, v71, v31\n"
        "v_mul_f32 v72, s58, v72\n"
        "v_mul_f32 v68, v68, v95\n"
        "v_mul_f32 v69, v69, v95\n"
        "v_mul_f32 v70, v70, v95\n"
        "v_mfma_f32_16x16x32_bf16 v[152:155], v[36:39], v[116:119], v[152:155]\n"
        "ds_read_b128 v[144:147], v0 offset:9264\n"
        "v_mul_f32 v71, v71, v95\n"
        "v_fma_f32 v72, v72, v95, v19\n"
        "v_perm_b32 v64, v69, v68, s63\n"
        "v_perm_b32 v66, v71, v70, s63\n"
        "v_perm_b32 v65, 0, v72, s63\n"
        "v_mul_f32 v108, v108, v20\n"
        "v_mul_f32 v109, v109, v21\n"
        "v_mfma_f32_16x16x32_bf16 v[152:155], v[60:63], v[112:115], v[152:155]\n"
        "s_waitcnt lgkmcnt(2)\n"
        "v_mul_f32 v110, v110, v22\n"
        "v_mul_f32 v111, v111, v23\n"
        "v_mul_f32 v140, v140, v24\n"
        "v_mul_f32 v141, v141, v25\n"
        "v_mul_f32 v142, v142, v26\n"
        "v_mul_f32 v143, v143, v27\n"
        "v_mul_f32 v108, v108, v164\n"
        "v_perm_b32 v112, v153, v152, s63\n"
        "v_mfma_f32_16x16x32_bf16 v[20:23], v[60:63], v[148:151], 0\n"
        "s_waitcnt lgkmcnt(1)\n"
        "v_perm_b32 v116, v155, v154, s63\n"
        "v_mul_f32 v109, v109, v164\n"
        "v_mul_f32 v110, v110, v164\n"
        "v_mul_f32 v111, v111, v164\n"
        "v_mul_f32 v140, v140, v164\n"
        "v_mul_f32 v141, v141, v164\n"
        "v_mul_f32 v142, v142, v164\n"
        "v_mul_f32 v143, v143, v164\n"
        "v_mfma_f32_16x16x32_bf16 v[20:23], v[36:39], v[84:87], v[20:23]\n"
        "s_waitcnt lgkmcnt(0)\n"
        "v_perm_b32 v24, v109, v108, s63\n"
        "v_perm_b32 v25, v111, v110, s63\n"
        "v_perm_b32 v26, v141, v140, s63\n"
        "v_perm_b32 v27, v143, v142, s63\n"
        "v_mul_f32 v100, v100, v28\n"
        "v_mul_f32 v101, v101, v29\n"
        "v_mul_f32 v102, v102, v30\n"
        "v_mul_f32 v103, v103, v31\n"
        "v_mfma_f32_16x16x32_bf16 v[20:23], v[32:35], v[144:147], v[20:23]\n"
        "v_mul_f32 v44, s58, v44\n"
        "v_mul_f32 v100, v100, v164\n"
        "v_mul_f32 v101, v101, v164\n"
        "v_mul_f32 v102, v102, v164\n"
        "v_mul_f32 v103, v103, v164\n"
        "v_fma_f32 v44, v44, v164, v19\n"
        "v_perm_b32 v28, v101, v100, s63\n"
        "v_perm_b32 v30, v103, v102, s63\n"
        "v_mfma_f32_16x16x32_bf16 v[68:71], v[132:135], v[144:147], 0\n"
        "v_perm_b32 v77, v21, v20, s63\n"
        "v_perm_b32 v81, v23, v22, s63\n"
        "v_perm_b32 v29, 0, v44, s63\n"
        "v_mfma_f32_16x16x32_bf16 v[68:71], v[136:139], v[84:87], v[68:71]\n"
        "v_mfma_f32_16x16x32_bf16 v[68:71], v[52:55], v[148:151], v[68:71]\n"
        "ds_read_b128 v[20:23], v0 offset:12352\n"
        "v_mfma_f32_16x16x32_bf16 v[72:75], v[160:163], v[148:151], 0\n"
        "ds_read_b128 v[108:111], v0 offset:13376\n"
        "v_mfma_f32_16x16x32_bf16 v[72:75], v[128:131], v[84:87], v[72:75]\n"
        "ds_read_b128 v[140:143], v0 offset:14400\n"
        "v_mfma_f32_16x16x32_bf16 v[72:75], v[124:127], v[144:147], v[72:75]\n"
        "s_waitcnt lgkmcnt(2)\n"
        "v_mfma_f32_16x16x32_bf16 v[144:147], v[124:127], v[20:23], 0\n"
        "v_perm_b32 v46, v69, v68, s63\n"
        "v_perm_b32 v47, v71, v70, s63\n"
        "s_waitcnt lgkmcnt(1)\n"
        "v_mfma_f32_16x16x32_bf16 v[144:147], v[128:131], v[108:111], v[144:147]\n"
        "s_waitcnt lgkmcnt(0)\n"
        "v_mfma_f32_16x16x32_bf16 v[144:147], v[160:163], v[140:143], v[144:147]\n"
        "v_perm_b32 v44, v73, v72, s63\n"
        "v_perm_b32 v45, v75, v74, s63\n"
        "v_mfma_f32_16x16x32_bf16 v[148:151], v[52:55], v[140:143], 0\n"
        "v_mfma_f32_16x16x32_bf16 v[148:151], v[136:139], v[108:111], v[148:151]\n"
        "v_mfma_f32_16x16x32_bf16 v[148:151], v[132:135], v[20:23], v[148:151]\n"
        "ds_read_b128 v[72:75], v0 offset:17488\n"
        "v_mfma_f32_16x16x32_bf16 v[84:87], v[32:35], v[20:23], 0\n"
        "ds_read_b128 v[68:71], v0 offset:16464\n"
        "v_perm_b32 v100, v145, v144, s63\n"
        "v_perm_b32 v101, v147, v146, s63\n"
        "v_mfma_f32_16x16x32_bf16 v[84:87], v[36:39], v[108:111], v[84:87]\n"
        "ds_read_b128 v[20:23], v0 offset:15440\n"
        "v_mfma_f32_16x16x32_bf16 v[84:87], v[60:63], v[140:143], v[84:87]\n"
        "v_perm_b32 v102, v149, v148, s63\n"
        "v_perm_b32 v103, v151, v150, s63\n"
        "s_waitcnt lgkmcnt(2)\n"
        "v_mfma_f32_16x16x32_bf16 v[108:111], v[60:63], v[72:75], 0\n"
        "s_waitcnt lgkmcnt(1)\n"
        "v_mfma_f32_16x16x32_bf16 v[108:111], v[36:39], v[68:71], v[108:111]\n"
        "s_waitcnt lgkmcnt(0)\n"
        "v_mfma_f32_16x16x32_bf16 v[108:111], v[32:35], v[20:23], v[108:111]\n"
        "ds_read2_b32 v[82:83], v11 offset0:0 offset1:3\n"
        "v_perm_b32 v89, v85, v84, s63\n"
        "v_perm_b32 v93, v87, v86, s63\n"
        "v_mov_b32_dpp v59, v19 row_shr:4 row_mask:0xf bank_mask:0xf bound_ctrl:0\n"
        "v_mfma_f32_16x16x32_bf16 v[36:39], v[132:135], v[20:23], 0\n"
        "ds_read_b64_tr_b16 v[78:79], v9 offset:40064\n"
        "v_mfma_f32_16x16x32_bf16 v[36:39], v[136:139], v[68:71], v[36:39]\n"
        "ds_read2_b32 v[94:95], v11 offset0:1 offset1:4\n"
        "v_perm_b32 v113, v109, v108, s63\n"
        "v_perm_b32 v117, v111, v110, s63\n"
        "v_mfma_f32_16x16x32_bf16 v[36:39], v[52:55], v[72:75], v[36:39]\n"
        "s_add_u32 s31, s31, 1\n"
        "ds_read_b64_tr_b16 v[90:91], v9 offset:40080\n"
        "v_mfma_f32_16x16x32_bf16 v[32:35], v[160:163], v[72:75], 0\n"
        "s_cmp_ge_u32 s31, s71\n"
        "ds_read2_b32 v[118:119], v11 offset0:2 offset1:5\n"
        "v_mfma_f32_16x16x32_bf16 v[32:35], v[128:131], v[68:71], v[32:35]\n"
        "s_cbranch_scc1 L52B_%=\n"
        "L53B_%=:\n"
        "ds_read_b64_tr_b16 v[114:115], v9 offset:40096\n"
        "v_perm_b32 v110, v37, v36, s63\n"
        "v_perm_b32 v111, v39, v38, s63\n"
        "v_mfma_f32_16x16x32_bf16 v[32:35], v[124:127], v[20:23], v[32:35]\n"
        "s_add_u32 s16, s16, 256\n"
        "v_mfma_f32_16x16x32_bf16 v[20:23], v[48:51], v[40:43], 0\n"
        "s_waitcnt lgkmcnt(4)\n"
        "s_cmp_ge_u32 s31, 20\n"
        "v_smfmac_f32_16x16x64_bf16 v[20:23], v[56:59], v[76:83], v18\n"
        "s_cselect_b32 s39, s22, s16\n"
        "v_mfma_f32_16x16x32_bf16 v[36:39], v[48:51], v[96:99], 0\n"
        "s_waitcnt lgkmcnt(2)\n"
        "v_perm_b32 v108, v33, v32, s63\n"
        "v_perm_b32 v109, v35, v34, s63\n"
        "s_cmp_ge_u32 s31, 21\n"
        "v_smfmac_f32_16x16x64_bf16 v[36:39], v[56:59], v[88:95], v18\n"
        "s_cselect_b32 s39, s23, s39\n"
        "ds_read_b64_tr_b16 v[78:79], v9 offset:40112\n"
        "v_mfma_f32_16x16x32_bf16 v[32:35], v[48:51], v[104:107], 0\n"
        "s_waitcnt lgkmcnt(1)\n"
        "s_mul_hi_u32 s38, s39, 58835169\n"
        "v_smfmac_f32_16x16x64_bf16 v[32:35], v[56:59], v[112:119], v18\n"
        "s_mul_i32 s39, s39, 1344\n"
        "ds_read_b64_tr_b16 v[90:91], v9 offset:40128\n"
        "v_cvt_pk_bf16_f32 v152, v20, v36\n"
        "v_cvt_pk_bf16_f32 v156, v21, v37\n"
        "v_cvt_pk_bf16_f32 v160, v22, v38\n"
        "v_cvt_pk_bf16_f32 v164, v23, v39\n"
        "v_mfma_f32_16x16x32_bf16 v[68:71], v[48:51], v[44:47], 0\n"
        "s_mul_i32 s38, s38, 588672\n"
        "s_waitcnt lgkmcnt(1)\n"
        "v_smfmac_f32_16x16x64_bf16 v[68:71], v[56:59], v[76:83], v18 abid:1\n"
        "s_add_u32 s39, s39, s38\n"
        "ds_read_b64_tr_b16 v[114:115], v9 offset:40144\n"
        "v_mfma_f32_16x16x32_bf16 v[60:63], v[48:51], v[100:103], 0\n"
        "s_add_u32 s39, s39, s17\n"
        "ds_read_b128 v[36:39], v2\n"
        "v_mov_b64 v[20:21], 0\n"
        "v_mov_b64 v[22:23], 0\n"
        "s_waitcnt lgkmcnt(2)\n"
        "v_smfmac_f32_16x16x64_bf16 v[60:63], v[56:59], v[88:95], v18 abid:1\n"
        "s_mul_i32 s68, s31, 3\n"
        "ds_read_b128 v[52:55], v3\n"
        "v_cvt_pk_bf16_f32 v153, v32, v68\n"
        "v_cvt_pk_bf16_f32 v157, v33, v69\n"
        "v_cvt_pk_bf16_f32 v161, v34, v70\n"
        "v_cvt_pk_bf16_f32 v165, v35, v71\n"
        "v_mfma_f32_16x16x32_bf16 v[68:71], v[48:51], v[108:111], 0\n"
        "s_bitcmp1_b32 s31, 0\n"
        "v_mov_b64 v[32:33], 0\n"
        "v_mov_b64 v[34:35], 0\n"
        "s_waitcnt lgkmcnt(2)\n"
        "v_smfmac_f32_16x16x64_bf16 v[68:71], v[56:59], v[112:119], v18 abid:1\n"
        "s_cselect_b32 s38, 3, 6\n"
        "s_waitcnt lgkmcnt(1)\n"
        "v_mov_b32_dpp v67, v19 row_shr:4 row_mask:0xf bank_mask:0xf bound_ctrl:0\n"
        "v_smfmac_f32_16x16x64_bf16 v[20:23], v[36:39], v[40:47], v4\n"
        "s_add_u32 s68, s68, s38\n"
        "v_mov_b64 v[48:49], 0\n"
        "v_mov_b64 v[50:51], 0\n"
        "s_waitcnt lgkmcnt(0)\n"
        "v_smfmac_f32_16x16x64_bf16 v[20:23], v[52:55], v[76:83], v4 abid:1\n"
        "s_add_u32 s37, s35, 0\n"
        "s_cmp_lt_u32 s31, s14\n"
        "v_cvt_pk_bf16_f32 v154, v60, v68\n"
        "v_cvt_pk_bf16_f32 v158, v61, v69\n"
        "v_cvt_pk_bf16_f32 v162, v62, v70\n"
        "v_cvt_pk_bf16_f32 v166, v63, v71\n"
        "v_smfmac_f32_16x16x64_bf16 v[32:35], v[36:39], v[96:103], v4\n"
        "s_cselect_b32 s68, s68, 0\n"
        "v_add_f32 v20, v20, v21\n"
        "v_add_f32 v22, v22, v23\n"
        "v_smfmac_f32_16x16x64_bf16 v[32:35], v[52:55], v[88:95], v4 abid:1\n"
        "s_and_b32 s38, s31, 7\n"
        "ds_read_b64_tr_b16 v[78:79], v9 offset:43392\n"
        "v_smfmac_f32_16x16x64_bf16 v[48:51], v[36:39], v[104:111], v4\n"
        "buffer_store_dwordx3 v[152:154], v12, s[8:11], s37 offen sc0 sc1 nt\n"
        "s_cselect_b32 s30, 1, -7\n"
        "v_smfmac_f32_16x16x64_bf16 v[48:51], v[52:55], v[112:119], v4 abid:1\n"
        "s_mul_i32 s29, s30, 192\n"
        "ds_read_b64_tr_b16 v[90:91], v9 offset:43408\n"
        "v_add_f32 v32, v32, v33\n"
        "v_add_f32 v34, v34, v35\n"
        "v_cvt_pk_bf16_f32 v148, v20, v32\n"
        "v_mfma_f32_16x16x32_bf16 v[152:155], v[120:123], v[40:43], 0\n"
        "s_mul_i32 s30, s30, 392\n"
        "s_waitcnt lgkmcnt(1)\n"
        "v_smfmac_f32_16x16x64_bf16 v[152:155], v[64:67], v[76:83], v18\n"
        "s_add_u32 s37, s35, 0\n"
        "s_cmp_eq_u32 s69, s18\n"
        "ds_read_b64_tr_b16 v[114:115], v9 offset:43424\n"
        "v_add_f32 v48, v48, v49\n"
        "v_add_f32 v50, v50, v51\n"
        "v_cvt_pk_bf16_f32 v149, v48, v22\n"
        "v_cvt_pk_bf16_f32 v150, v34, v50\n"
        "v_mfma_f32_16x16x32_bf16 v[56:59], v[120:123], v[96:99], 0\n"
        "s_cselect_b32 s34, -7488, s70\n"
        "s_waitcnt lgkmcnt(1)\n"
        "v_smfmac_f32_16x16x64_bf16 v[56:59], v[64:67], v[88:95], v18\n"
        "s_add_u32 s69, s69, s34\n"
        "ds_read_b64_tr_b16 v[78:79], v9 offset:43440\n"
        "v_writelane_b32 v10, s69, 56\n"
        "v_add_u32 v11, s30, v11\n"
        "s_mov_b64 exec, s[54:55]\n"
        "v_add_u32 v16, s29, v16\n"
        "v_add_u32 v8, s34, v8\n"
        "s_mov_b64 exec, -1\n"
        "v_mfma_f32_16x16x32_bf16 v[72:75], v[120:123], v[104:107], 0\n"
        "buffer_store_dwordx3 v[156:158], v15, s[8:11], s37 offen offset:768 sc0 sc1 nt\n"
        "s_mov_b64 exec, s[50:51]\n"
        "v_add_u32 v5, s29, v5\n"
        "v_add_u32 v6, s34, v6\n"
        "s_mov_b64 exec, -1\n"
        "s_waitcnt lgkmcnt(1)\n"
        "v_smfmac_f32_16x16x64_bf16 v[72:75], v[64:67], v[112:119], v18\n"
        "ds_read_b64_tr_b16 v[90:91], v9 offset:43456\n"
        "v_mov_b32_dpp v31, v19 row_shr:4 row_mask:0xf bank_mask:0xf bound_ctrl:0\n"
        "v_mfma_f32_16x16x32_bf16 v[20:23], v[120:123], v[44:47], 0\n"
        "s_waitcnt lgkmcnt(1)\n"
        "v_smfmac_f32_16x16x64_bf16 v[20:23], v[64:67], v[76:83], v18 abid:1\n"
        "s_add_u32 s37, s35, 98112\n"
        "ds_read_b64_tr_b16 v[114:115], v9 offset:43472\n"
        "v_mfma_f32_16x16x32_bf16 v[84:87], v[120:123], v[100:103], 0\n"
        "s_waitcnt lgkmcnt(1)\n"
        "v_smfmac_f32_16x16x64_bf16 v[84:87], v[64:67], v[88:95], v18 abid:1\n"
        "ds_read_b64_tr_b16 v[78:79], v9 offset:46720\n"
        "v_mfma_f32_16x16x32_bf16 v[60:63], v[120:123], v[108:111], 0\n"
        "buffer_store_dwordx3 v[160:162], v12, s[8:11], s37 offen offset:192 sc0 sc1 nt\n"
        "ds_read_b32 v167, v0 offset:21600\n"
        "s_waitcnt lgkmcnt(2)\n"
        "v_smfmac_f32_16x16x64_bf16 v[60:63], v[64:67], v[112:119], v18 abid:1\n"
        "ds_read_b64_tr_b16 v[90:91], v9 offset:46736\n"
        "v_mfma_f32_16x16x32_bf16 v[68:71], v[24:27], v[40:43], 0\n"
        "s_waitcnt lgkmcnt(2)\n"
        "v_smfmac_f32_16x16x64_bf16 v[68:71], v[28:31], v[76:83], v18\n"
        "s_add_u32 s37, s35, 98112\n"
        "ds_read_b64_tr_b16 v[114:115], v9 offset:46752\n"
        "v_mfma_f32_16x16x32_bf16 v[36:39], v[24:27], v[96:99], 0\n"
        "s_waitcnt lgkmcnt(1)\n"
        "v_smfmac_f32_16x16x64_bf16 v[36:39], v[28:31], v[88:95], v18\n"
        "ds_read_b64_tr_b16 v[78:79], v9 offset:46768\n"
        "v_readfirstlane_b32 s40, v167\n"
        "v_mfma_f32_16x16x32_bf16 v[40:43], v[24:27], v[104:107], 0\n"
        "buffer_store_dwordx3 v[164:166], v14, s[8:11], s37 offen offset:960 sc0 sc1 nt\n"
        "s_add_u32 s37, s35, 588672\n"
        "s_add_u32 s46, s37, 1344\n"
        "s_cmp_lt_i32 s40, s68\n"
        "s_waitcnt lgkmcnt(1)\n"
        "v_smfmac_f32_16x16x64_bf16 v[40:43], v[28:31], v[112:119], v18\n"
        "buffer_store_dwordx3 v[148:150], v12, s[44:47], s37 offen offset:1152 sc0 sc1 nt\n"
        "ds_read_b64_tr_b16 v[90:91], v9 offset:46784\n"
        "v_mfma_f32_16x16x32_bf16 v[64:67], v[24:27], v[44:47], 0\n"
        "s_waitcnt lgkmcnt(1)\n"
        "v_smfmac_f32_16x16x64_bf16 v[64:67], v[28:31], v[76:83], v18 abid:1\n"
        "ds_read_b64_tr_b16 v[114:115], v9 offset:46800\n"
        "s_mov_b64 exec, 1\n"
        "ds_inc_u32 v0, v1 offset:21612\n"
        "s_mov_b64 exec, -1\n"
        "s_cbranch_scc1 L56B_%=\n"
        "L57B_%=:\n"
        "ds_read_b128 v[124:127], v7 offset:0\n"
        "ds_read_b128 v[132:135], v7 offset:3328\n"
        "ds_read_b128 v[128:131], v7 offset:64\n"
        "ds_read_b128 v[136:139], v7 offset:3392\n"
        "v_mfma_f32_16x16x32_bf16 v[104:107], v[24:27], v[100:103], 0\n"
        "s_waitcnt lgkmcnt(6)\n"
        "ds_read_b128 v[160:163], v7 offset:128\n"
        "ds_read_b128 v[52:55], v7 offset:3456\n"
        "s_mov_b32 s36, s35\n"
        "v_smfmac_f32_16x16x64_bf16 v[104:107], v[28:31], v[88:95], v18 abid:1\n"
        "s_mov_b32 s35, s39\n"
        "v_mfma_f32_16x16x32_bf16 v[88:91], v[24:27], v[108:111], 0\n"
        "s_waitcnt lgkmcnt(7)\n"
        "v_smfmac_f32_16x16x64_bf16 v[88:91], v[28:31], v[112:119], v18 abid:1\n"
        "s_setprio 0\n"
        "s_waitcnt lgkmcnt(0)\n"
        "s_nop 15\n"
        "s_branch CNEXTB_%=\n"
        :: "{v0}"(tid), "{s[0:1]}"(kap), "{s2}"(bid), "{s[4:5]}"(tab) : CLOBBERS);
}

extern "C" void kernel_launch(void* const* d_in, const int* in_sizes, int n_in, void* d_out, int out_size, void* d_ws, size_t ws_size, hipStream_t stream) {
    (void)in_sizes; (void)n_in; (void)out_size; (void)ws_size;
    (void)hipFuncSetAttribute((const void*)swin_kernel, hipFuncAttributeMaxDynamicSharedMemorySize, LDS_BYTES);
    swin_kernel<<<256, 512, LDS_BYTES, stream>>>((const char*)d_in[0], (const char*)d_in[1], (const char*)d_in[2], (const char*)d_in[3], (const char*)d_in[4], (char*)d_out, (const char*)d_ws);
}
```

```cpp
#define LTOK_ 208
#define FPAD 32
#define NPR 36
#define NWC 22
#define ASP 0
#define LK0 0
#define LK1 0
#define LK2 1
#define LK3 1
#include <hip/hip_runtime.h>
#include <hip/hip_bf16.h>

constexpr int TOKB = 192, ROWB = 511 * TOKB;
constexpr unsigned FRAMEB = 511u * ROWB;
constexpr int LTOK = LTOK_;
constexpr int LFRAME = 48 * LTOK + FPAD;
constexpr int LBUF = 4 * LFRAME;
constexpr int LSTASH = 10 * 4 * LTOK;
constexpr int LSTG = 6208;
constexpr int VST = 392, LEXT = 1536 + 8 * VST;
constexpr int WT = 3088;
constexpr int LDS_AUX = (54 - NPR) / 3 * WT, LDS_TAB = LDS_AUX + 1024, LDS_CNT = LDS_TAB + 2048, LDS_BUF = LDS_CNT + 128, LDS_STASH = LDS_BUF + 2 * LBUF,
              LDS_STG = LDS_STASH + LSTASH, LDS_SCRAP = LDS_STG + 4 * LSTG, LDS_EXT = LDS_SCRAP + 512, LDS_BYTES = 163840;
static_assert(NWC >= 18 && NPR % 3 == 0 && LDS_EXT + 4 * LEXT + 256 <= LDS_BYTES && NPR * 1024 <= LBUF, "LDS");
constexpr unsigned RAWM = 18432, RAW_T = LDS_BUF, RAW_TV = RAW_T + 2 * RAWM + 128, RAW_SV = (36 - NPR) / 3 * WT, RAW_SO = LDS_EXT, RAW_SQ = LDS_BUF + LBUF, RAW_SK = RAW_SQ + RAWM;
static_assert(RAW_SV + RAWM <= LDS_AUX && RAW_SO + RAWM <= LDS_BYTES && RAW_SK + RAWM <= LDS_STASH && RAW_TV + 2 * RAWM <= LDS_STASH && RAWM + 6 * WT <= 2 * RAWM + 128, "fold LDS");

constexpr int phi(int v) { return v ^ (v >> 1); }
struct Tab {
    unsigned img[3][64][4];
    unsigned p[4][7][64][4];
    unsigned c[4][6][64][4];
    unsigned d[4][7][64][4];
    static constexpr void job(unsigned (*q)[64][4], int lane, unsigned L, unsigned R, int mode, int tau, unsigned st0, unsigned st1) {
        const int i = lane & 15, g = lane >> 4;
        for (int a = 0; a < 2; ++a) {
            const unsigned ra = (8 * (i >> 2) + 4 * a + (i & 3)) * 192 + ((g ^ ((2 * (i >> 2) + a) & 3)) * 16);
            q[0][lane][a] = L + ra;
            q[0][lane][2 + a] = mode == 0 ? R + tau * 6144 + ra : mode == 2 ? R + tau * 6144 + (16 * a + i) * 192 + ((g ^ (i >> 2)) * 16)
                                          : R + tau * 64 + (8 * g + 4 * a + (i >> 2)) * 192 + (((i & 3) ^ ((2 * g + a) & 3)) * 16);
        }
        q[1][lane][0] = st0; q[1][lane][1] = st1; q[1][lane][2] = LDS_SCRAP + g * 32;
    }
    static constexpr unsigned scat(bool win, int lane, int tau, int tb) {
        const int i = lane & 15, c = 32 * tau + 8 * (i >> 2) + 4 * tb + (i & 3), r = c / 24, o = c % 24, q = win ? o / 6 : o & 3, t = win ? o % 6 : o >> 2;
        return (win ? RAW_SV : 3 * t < NPR - 18 ? LDS_BUF + 18 * 1024 : -(NPR - 18) / 3 * WT) + t * WT + ((lane & 48) + 4 * q + r) * 16;
    }
    constexpr Tab() : img{}, p{}, c{}, d{} {
        for (int e = 0; e < 48; ++e) img[0][e >> 2][e & 3] = 0x3f803f80u;
        img[0][12][0] = 0x7f800000u;
        for (int e = 0; e < 8; ++e) img[0][36 + 10 + e][e >> 1] = e & 1 ? 0x3f800000u : 0x3f80u;
        for (int k = 0; k < 4; ++k) for (int lane = 0; lane < 64; ++lane) {
            const int i = lane & 15, g = lane >> 4, rowv = 8 * (i >> 2) + (i & 3);
            unsigned vi[20] = {};
            {
                const int pp = i >> 2, t = i & 3;
                const int t1 = (k + 3 - LK1) & 3;
                if (t1 < 3) job(p[k], lane, RAW_TV, RAW_TV + RAWM, 1, t1, scat(false, lane, t1, 0), scat(false, lane, t1, 1));
                const unsigned wl = lane * 16, lm = pp == g ? ~0u : 0u, idp = LDS_AUX + 576 + (t == g ? 10 + pp : 0) * 16, sidx = i & 1 ? 0xeeeeddddu : 0x4444ddddu;
                unsigned vA[3] = {};
                for (int m = 0; m < 3; ++m) {
                    const int n = 64 * m + lane, tf = n / 96, q = n % 96 / 12, s = (n % 12) ^ tf;
                    const unsigned cc = tf * FRAMEB + (3 * (s & 3) + (s >> 2)) * 16 + 2048 - 1024 * m;
                    vA[m] = cc + q * TOKB;
                    img[1][lane][m] = (cc + 6 * ROWB + 6 * TOKB) | ((unsigned)(2 * (q & 3) + (q >> 2)) << 28);
                }
                const unsigned vA0 = vA[0], vA1 = vA[1], vA2 = vA[2], vB0 = vA0 + (ROWB - 7 * TOKB), vB1 = vA1 + (ROWB - 7 * TOKB), vB2 = vA2 + (ROWB - 7 * TOKB);
                const unsigned dA = LDS_BUF + t * LFRAME + pp * LTOK + g * 16, rA = LDS_BUF + t * LFRAME + (pp == g ? pp : 4 + g) * LTOK + 192;
                img[1][lane][3] = (2 * pp * (4 * LTOK) + t * LTOK + g * 16) | (((pp == g ? 2 * pp : 2 * g + 1) * (4 * LTOK) + t * LTOK + 192) << 16);
                const unsigned cd0 = k < 3 ? dA + k * 8 * LTOK : LDS_STASH + (img[1][lane][3] & 0xffffu), cr0 = k < 3 ? rA + k * 8 * LTOK : LDS_STASH + (img[1][lane][3] >> 16);
                const unsigned xr = LDS_STG + k * LSTG + (t >> 1) * 3104 + ((t & 1) * 8 + pp) * TOKB + (g ^ (t & 1)) * 16;
                const unsigned axp = LDS_AUX + (t > ASP ? 384 : 0);
                const unsigned pf = g * FRAMEB + (i >> 1) * TOKB + (i & 1) * 188 + 2048;
                vi[0] = xr; vi[1] = lm; vi[2] = rA; vi[3] = dA; vi[4] = vA0; vi[5] = vA1; vi[6] = vA2; vi[7] = vB0; vi[8] = vB1; vi[9] = vB2; vi[10] = sidx; vi[11] = cd0; vi[12] = cr0; vi[13] = wl; vi[14] = idp; vi[15] = pf; vi[16] = axp;
                for (int e = 0; e < 20; ++e) p[k][2 + (e >> 2)][lane][e & 3] = vi[e];
            }
            {
                const int t0 = (k + 3 - LK0) & 3, t2 = (k + 3 - LK2) & 3, t3 = (k + 3 - LK3) & 3;
                const unsigned s0 = LDS_BUF + t0 * 6144 + lane * 16, s3 = LDS_BUF + LBUF + t3 * 6144 + lane * 16;
                if (t0 < 3) job(c[k], lane, RAW_T, RAW_T + RAWM, 2, t0, s0, s0 + 3072); else job(c[k], lane, RAW_T, RAW_T + RAWM, 1, 0, LDS_SCRAP + rowv * 4, 0);
                if (t2 < 3) job(c[k] + 4, lane, RAW_SV, RAW_SO, 1, t2, scat(true, lane, t2, 0), scat(true, lane, t2, 1));
                if (t3 < 3) job(d[k], lane, RAW_SQ, RAW_SK, 0, t3, s3, s3 + 3072); else job(d[k], lane, RAW_SQ, RAW_SK, 1, 0, LDS_SCRAP + rowv * 4, 0);
                for (int m = 0; m < 3; ++m) {
                    const int n = 64 * m + lane, row = n / 12, pc = ((n % 12) ^ ((row >> 2) & 3)) * 16;
                    c[k][3][lane][m] = (24 * (row & 3) + 12 * ((row >> 2) & 1) + 2 * (row >> 3)) * 576 + pc;
                    c[k][2][lane][m] = row * 192 + pc;
                }
                const int dg = 16 * (2 * (g & 1) + (g >> 1)), pix = 4 * (i & 3) + phi(i >> 2);
                const unsigned wl = lane * 16, fr = LDS_BUF + k * LFRAME, ext = LDS_EXT + k * LEXT;
                const unsigned ta = fr + pix * LTOK + dg, zr = LDS_AUX + 384 + g * 16, t30 = i & 3 ? zr : LDS_STASH + k * LTOK + dg;
                const unsigned xa = fr + (4 * (i >> 2) + phi(g)) * LTOK + 96 * (i & 1) + 8 * ((i >> 1) & 1);
                const int sg = 8 * ((i >> 1) & 1) + 4 * (i >> 3) + 2 * ((i >> 2) & 1) + (i & 1);
                const unsigned tv = fr + (32 + 4 * (sg & 3) + phi(sg >> 2)) * LTOK + dg, c1f = (i >> 2) == 2 * g ? 0x3f800000u : 0u;
                const unsigned sidx = g < 2 ? 0x0e0e0404u | ((i >> 2) == 2 * g ? (i & 3) * 0x00100010u : (i >> 2) == 2 * g + 1 ? (i & 3) * 0x40004000u : 0u) : 0xeeee4444u;
                const unsigned ga0 = phi(g) * TOKB + 48 * (i & 3) + 12 * (i >> 2), wr = ROWB - 7 * TOKB;
                const unsigned ga1 = ga0 + (phi(g) >= 1 ? wr : 0), ga2 = ga0 + (phi(g) >= 2 ? wr : 0), ga3 = ga0 + (phi(g) >= 3 ? wr : 0);
                const unsigned pb = LDS_SCRAP + k * 128, pr0 = i < 4 && (i >> 1) == (g >> 1) ? pb + (i & 1) * 32 + (g & 1) * 16 : zr, pr1 = i == 0 || i == 2 ? pb + 64 + g * 16 : zr;
                const unsigned pw = g < 2 ? pb + ((i >> 1) & 1) * 32 + (i >> 3) * 16 + ((i >> 2) & 1) * 8 + g * 4 + (i & 1) * 2
                                          : pb + 64 + (i >> 2) * 16 + (g & 1) * 4 + ((i >> 1) & 1) * 8 + (i & 1) * 2 + (lane == 57 ? 6 : 0);
                const unsigned six2 = (i & 1 ? 0xeeeeu : 0x4444u) | ((i & 2 ? 0xeeee0000u : 0x44440000u) & (g < 2 ? 0x0f0f0000u : 0xffff0000u));
                const unsigned exs0 = i & 2 ? LDS_AUX + g * 16 : ext + (i & 1) * 64 + g * 16, t3s0 = i & 2 ? zr : LDS_STASH + k * LTOK + 64 * (g >> 1) + 16 * (g & 1) + 4 * (i & 1);
                const unsigned ex0 = i & 1 ? zr : i & 2 ? LDS_AUX + g * 16 : ext + g * 16, exv0 = ext + 1536 + i * 24;
                const unsigned rla0 = lane < 48 ? fr + (16 * g + pix) * LTOK + 192 : lane == 56 ? LDS_STASH + k * LTOK + 192 : LDS_AUX + 192;
                img[2][lane][0] = ((i & 7) * (4 * LTOK) + dg) | ((((4 * g + (i & 3)) & 7) * (4 * LTOK) + 96 * ((i >> 2) & 1) + 8 * (i >> 3)) << 16);
                img[2][lane][1] = ((i & 7) * 192u + g * 16) | ((1536u + i * 24 + (g & 1) * 4 * VST) << 16);
                img[2][lane][2] = ((fr + phi(g) * LTOK + 192 - xa) & 0xffffu) | ((fr + pix * LTOK + 192 - xa) << 16);
                img[2][lane][3] = (LDS_AUX + 576 + (10 + (i & 3)) * 16) | ((1536u + (i & 7) * VST + 384) << 16);
                vi[0] = wl; vi[1] = pw; vi[2] = pr0; vi[3] = pr1; vi[4] = six2; vi[5] = exs0; vi[6] = t3s0; vi[7] = ta; vi[8] = t30; vi[9] = xa; vi[10] = rla0; vi[11] = exv0; vi[12] = ga0; vi[13] = ga1; vi[14] = ga2; vi[15] = ga3; vi[16] = ex0; vi[17] = tv; vi[18] = sidx; vi[19] = c1f;
                for (int e = 0; e < 20; ++e) d[k][2 + (e >> 2)][lane][e & 3] = vi[e];
            }
        }
    }
};
static __device__ const Tab TAB __attribute__((aligned(128))) = Tab();

#define CLOBBERS "v1", "v2", "v3", "v4", "v5", "v6", "v7", "v8", "v9", "v10", "v11", "v12", "v13", "v14", "v15", "v16", "v17", "v18", "v19", "v20", "v21", "v22", "v23", "v24", "v25", "v26", "v27", "v28", "v29", "v30", "v31", "v32", "v33", "v34", "v35", "v36", "v37", "v38", "v39", "v40", "v41", "v42", "v43", "v44", "v45", "v46", "v47", "v48", "v49", "v50", "v51", "v52", "v53", "v54", "v55", "v56", "v57", "v58", "v59", "v60", "v61", "v62", "v63", "v64", "v65", "v66", "v67", "v68", "v69", "v70", "v71", "v72", "v73", "v74", "v75", "v76", "v77", "v78", "v79", "v80", "v81", "v82", "v83", "v84", "v85", "v86", "v87", "v88", "v89", "v90", "v91", "v92", "v93", "v94", "v95", "v96", "v97", "v98", "v99", "v100", "v101", "v102", "v103", "v104", "v105", "v106", "v107", "v108", "v109", "v110", "v111", "v112", "v113", "v114", "v115", "v116", "v117", "v118", "v119", "v120", "v121", "v122", "v123", "v124", "v125", "v126", "v127", "v128", "v129", "v130", "v131", "v132", "v133", "v134", "v135", "v136", "v137", "v138", "v139", "v140", "v141", "v142", "v143", "v144", "v145", "v146", "v147", "v148", "v149", "v150", "v151", "v152", "v153", "v154", "v155", "v156", "v157", "v158", "v159", "v160", "v161", "v162", "v163", "v164", "v165", "v166", "v167", "v168", "v169", "v170", "v171", "v172", "v173", "v174", "v175", "v176", "v177", "v178", "v179", "v180", "v181", "v182", "v183", "v184", "v185", "v186", "v187", "v188", "v189", "v190", "v191", "v192", "v193", "v194", "v195", "v196", "v197", "v198", "v199", "v200", "v201", "v202", "v203", "v204", "v205", "v206", "v207", "v208", "v209", "v210", "v211", "v212", "v213", "v214", "v215", "v216", "v217", "v218", "v219", "v220", "v221", "v222", "v223", "v224", "v225", "v226", "v227", "v228", "v229", "v230", "v231", "v232", "v233", "v234", "v235", "v236", "v237", "v238", "v239", "v240", "v241", "v242", "v243", "v244", "v245", "v246", "v247", "v248", "v249", "v250", "v251", "v252", "v253", "v254", "v255", "s3", "s6", "s7", "s8", "s9", "s10", "s11", "s12", "s13", "s14", "s15", "s16", "s17", "s18", "s19", "s20", "s21", "s22", "s23", "s24", "s25", "s26", "s27", "s28", "s29", "s30", "s31", "s34", "s35", "s36", "s37", "s38", "s39", "s40", "s41", "s42", "s43", "s44", "s45", "s46", "s47", "s48", "s49", "s50", "s51", "s52", "s53", "s54", "s55", "s56", "s57", "s58", "s59", "s60", "s61", "s62", "s63", "s64", "s65", "s66", "s67", "s68", "s69", "s70", "s71", "s72", "s73", "s74", "s75", "s76", "s77", "vcc", "scc", "memory"
__global__ __launch_bounds__(512, 2) void swin_kernel(const char* x, const char* wqkv_t, const char* wo_t, const char* wqkv_s, const char* wo_s, char* out, const char* ws) {
    const unsigned long long kap = (unsigned long long)__builtin_amdgcn_kernarg_segment_ptr(), tab = (unsigned long long)&TAB;
    const unsigned tid = threadIdx.x, bid = blockIdx.x;
    asm volatile(
        "s_load_dwordx8 s[44:51], s[0:1], 0x0\n"
        "s_load_dwordx4 s[52:55], s[0:1], 0x20\n"
        "s_load_dwordx2 s[56:57], s[0:1], 0x30\n"
        "v_readfirstlane_b32 s3, v0\n"
        "v_and_b32 v56, 63, v0\n"
        "v_lshlrev_b32 v57, 4, v56\n"
        "s_bfe_u32 s3, s3, 0x30006\n"
        "s_and_b32 s6, s3, 3\n"
        "s_mul_i32 s64, s3, 9216\n"
        "s_add_u32 s64, s64, 21728\n"
        "s_mov_b32 s27, 0x20000\n"
        "s_cmp_lt_u32 s3, 4\n"
        "s_cbranch_scc0 PPRO_%=\n"
        "s_mul_i32 s7, s6, 6144\n"
        "s_add_u32 s7, s7, 33792\n"
        "s_add_u32 s22, s4, s7\n"
        "s_addc_u32 s23, s5, 0\n"
        "global_load_dwordx4 v[240:243], v57, s[22:23] offset:-2048\n"
        "global_load_dwordx4 v[244:247], v57, s[22:23] offset:-1024\n"
        "global_load_dwordx4 v[36:39], v57, s[22:23] offset:1024\n"
        "global_load_dwordx4 v[48:51], v57, s[22:23] offset:2048\n"
        "global_load_dwordx4 v[52:55], v57, s[22:23] offset:3072\n"
        "s_mov_b32 s65, 13824\n"
        "v_add_u32 v24, 0, v56\n"
        "v_mul_u32_u24 v21, 171, v24\n"
        "v_lshrrev_b32 v21, 11, v21\n"
        "v_mad_i32_i24 v24, v21, -12, v24\n"
        "v_bfe_u32 v58, v21, 2, 2\n"
        "v_xor_b32 v58, v24, v58\n"
        "v_and_b32 v27, 7, v21\n"
        "v_mul_u32_u24 v27, 576, v27\n"
        "v_lshl_add_u32 v58, v58, 4, v27\n"
        "v_add_u32 v25, 64, v56\n"
        "v_mul_u32_u24 v22, 171, v25\n"
        "v_lshrrev_b32 v22, 11, v22\n"
        "v_mad_i32_i24 v25, v22, -12, v25\n"
        "v_bfe_u32 v59, v22, 2, 2\n"
        "v_xor_b32 v59, v25, v59\n"
        "v_and_b32 v27, 7, v22\n"
        "v_mul_u32_u24 v27, 576, v27\n"
        "v_lshl_add_u32 v59, v59, 4, v27\n"
        "v_lshrrev_b32 v27, 3, v22\n"
        "v_mad_u32_u24 v59, v27, s65, v59\n"
        "v_add_u32 v26, 128, v56\n"
        "v_mul_u32_u24 v23, 171, v26\n"
        "v_lshrrev_b32 v23, 11, v23\n"
        "v_mad_i32_i24 v26, v23, -12, v26\n"
        "v_bfe_u32 v60, v23, 2, 2\n"
        "v_xor_b32 v60, v26, v60\n"
        "v_and_b32 v27, 7, v23\n"
        "v_mul_u32_u24 v27, 576, v27\n"
        "v_lshl_add_u32 v60, v60, 4, v27\n"
        "v_add_u32 v60, s65, v60\n"
        "s_lshr_b32 s61, s6, 1\n"
        "s_mul_i32 s61, s61, 192\n"
        "s_mov_b32 s62, 27648\n"
        "s_mov_b32 s63, 4608\n"
        "s_add_u32 s7, s62, s63\n"
        "s_lshl_b32 s29, s63, 1\n"
        "s_add_u32 s60, s7, s63\n"
        "s_bitcmp1_b32 s6, 0\n"
        "s_cselect_b32 s28, s7, 0\n"
        "s_cselect_b32 s29, s29, s62\n"
        "s_cselect_b32 s60, s60, s63\n"
        "s_add_u32 s28, s28, s61\n"
        "s_add_u32 s29, s29, s61\n"
        "s_add_u32 s60, s60, s61\n"
        "s_mov_b32 s26, 55296\n"
        "s_waitcnt lgkmcnt(0)\n"
        "s_mov_b32 s24, s46\n"
        "s_and_b32 s25, s47, 0xffff\n"
        "s_add_u32 m0, s64, 0\n"
        "s_nop 0\n"
        "buffer_load_dwordx4 v58, s[24:27], s28 offen lds\n"
        "s_add_u32 m0, s64, 1024\n"
        "s_nop 0\n"
        "buffer_load_dwordx4 v59, s[24:27], s28 offen lds\n"
        "s_add_u32 m0, s64, 2048\n"
        "s_nop 0\n"
        "buffer_load_dwordx4 v60, s[24:27], s28 offen lds\n"
        "s_add_u32 m0, s64, 3072\n"
        "s_nop 0\n"
        "buffer_load_dwordx4 v58, s[24:27], s29 offen lds\n"
        "s_add_u32 m0, s64, 4096\n"
        "s_nop 0\n"
        "buffer_load_dwordx4 v59, s[24:27], s29 offen lds\n"
        "s_add_u32 m0, s64, 5120\n"
        "s_nop 0\n"
        "buffer_load_dwordx4 v60, s[24:27], s29 offen lds\n"
        "s_add_u32 m0, s64, 6144\n"
        "s_nop 0\n"
        "buffer_load_dwordx4 v58, s[24:27], s60 offen lds\n"
        "s_add_u32 m0, s64, 7168\n"
        "s_nop 0\n"
        "buffer_load_dwordx4 v59, s[24:27], s60 offen lds\n"
        "s_add_u32 m0, s64, 8192\n"
        "s_nop 0\n"
        "buffer_load_dwordx4 v60, s[24:27], s60 offen lds\n"
        "s_barrier\n"
        "s_setprio 1\n"
        "v_mov_b32 v250, 21600\n"
        "s_mov_b32 s41, 0x400000\n"
        "s_and_b32 s7, s2, 7\n"
        "s_lshl_b32 s7, s7, 5\n"
        "s_lshr_b32 s14, s2, 3\n"
        "s_add_u32 s7, s7, s14\n"
        "s_and_b32 s16, s7, 255\n"
        "s_sub_u32 s7, s7, s16\n"
        "s_mul_i32 s7, s7, 20\n"
        "s_add_u32 s7, s7, s16\n"
        "s_mov_b32 s14, 22\n"
        "s_mul_i32 s15, s7, 57457\n"
        "s_lshr_b32 s15, s15, 22\n"
        "s_mov_b32 s16, s7\n"
        "s_mul_i32 s15, s15, 588672\n"
        "s_mul_i32 s7, s16, 1344\n"
        "s_add_u32 s15, s15, s7\n"
        "s_mov_b32 s8, s54\n"
        "s_and_b32 s9, s55, 0xffff\n"
        "s_mov_b32 s10, 200540928\n"
        "s_mov_b32 s11, 0x20000\n"
        "s_mov_b64 s[12:13], s[54:55]\n"
        "s_mul_i32 s17, s6, 50135232\n"
        "s_mul_i32 s18, s6, 208\n"
        "s_add_u32 s18, s18, 109536\n"
        "s_mul_i32 s19, s6, 4672\n"
        "s_add_u32 s19, s19, 135520\n"
        "s_mul_i32 s7, s6, 7168\n"
        "s_add_u32 s7, s7, 59392\n"
        "s_add_u32 s22, s4, s7\n"
        "s_addc_u32 s23, s5, 0\n"
        "s_waitcnt vmcnt(0)\n"
        "s_mov_b64 exec, 1\n"
        "ds_inc_u32 v250, v250 offset:16\n"
        "s_mov_b64 exec, -1\n"
        "L69_%=:\n"
        "ds_read_b32 v251, v250 offset:16\n"
        "s_waitcnt lgkmcnt(0)\n"
        "v_readfirstlane_b32 s40, v251\n"
        "s_nop 3\n"
        "s_cmp_ge_i32 s40, 4\n"
        "s_cbranch_scc1 L70_%=\n"
        "s_nop 0\n"
        "s_sub_u32 s41, s41, 1\n"
        "s_cbranch_scc0 L69_%=\n"
        "s_endpgm\n"
        "L70_%=:\n"
        "s_cmp_eq_u32 s6, 0\n"
        "s_cbranch_scc1 L71_%=\n"
        "s_mov_b32 s34, 0x4162ccfd\n"
        "s_mov_b32 s35, 21624\n"
        "s_mov_b32 s36, 21624\n"
        "s_mov_b32 s37, 21628\n"
        "s_call_b64 s[30:31], FOLDM_%=\n"
        "s_branch L72_%=\n"
        "L71_%=:\n"
        "s_mov_b32 s34, 0xbe173353\n"
        "s_mov_b32 s35, 21656\n"
        "s_mov_b32 s36, 21656\n"
        "s_mov_b32 s37, 21624\n"
        "s_call_b64 s[30:31], FOLDR_%=\n"
        "s_cmp_lg_u32 s2, 0\n"
        "s_cbranch_scc1 L68_%=\n"
        "s_mov_b64 s[24:25], s[56:57]\n"
        "s_mov_b64 exec, 1\n"
        "v_mov_b32 v27, 0\n"
        "v_mov_b32 v28, 0\n"
        "global_atomic_swap v252, v27, v28, s[24:25] offset:0 sc0\n"
        "s_mov_b64 exec, -1\n"
        "s_waitcnt vmcnt(0)\n"
        "s_mov_b64 exec, 1\n"
        "v_mov_b32 v27, 0\n"
        "v_mov_b32 v28, 1\n"
        "global_atomic_add v252, v27, v28, s[24:25] offset:256 sc0\n"
        "s_mov_b64 exec, -1\n"
        "L68_%=:\n"
        "L72_%=:\n"
        "s_setprio 2\n"
        "s_waitcnt vmcnt(0)\n"
        "v_mov_b64 v[240:241], v[48:49]\n"
        "v_mov_b64 v[242:243], v[50:51]\n"
        "v_mov_b64 v[244:245], v[52:53]\n"
        "v_mov_b64 v[246:247], v[54:55]\n"
        "L73_%=:\n"
        "ds_read_b32 v251, v250 offset:32\n"
        "s_waitcnt lgkmcnt(0)\n"
        "v_readfirstlane_b32 s40, v251\n"
        "s_nop 3\n"
        "s_cmp_ge_i32 s40, 4\n"
        "s_cbranch_scc1 L74_%=\n"
        "s_nop 0\n"
        "s_sub_u32 s41, s41, 1\n"
        "s_cbranch_scc0 L73_%=\n"
        "s_endpgm\n"
        "L74_%=:\n"
        "s_cmp_eq_u32 s6, 1\n"
        "s_cbranch_scc1 L75_%=\n"
        "s_mov_b32 s34, 0x411da57f\n"
        "s_mov_b32 s35, 21640\n"
        "s_mov_b32 s36, 21640\n"
        "s_mov_b32 s37, 21652\n"
        "s_call_b64 s[30:31], FOLDW_%=\n"
        "s_branch L76_%=\n"
        "L75_%=:\n"
        "s_mov_b64 exec, 1\n"
        "ds_inc_u32 v250, v250 offset:40\n"
        "s_mov_b64 exec, -1\n"
        "s_mov_b32 s24, s50\n"
        "s_and_b32 s25, s51, 0xffff\n"
        "s_mov_b32 s26, 55296\n"
        "s_mov_b32 m0, 61792\n"
        "s_mov_b32 s28, 0\n"
        "buffer_load_dwordx4 v36, s[24:27], s28 offen lds\n"
        "s_mov_b32 m0, 62816\n"
        "s_nop 0\n"
        "buffer_load_dwordx4 v37, s[24:27], s28 offen lds\n"
        "s_mov_b32 m0, 63840\n"
        "s_nop 0\n"
        "buffer_load_dwordx4 v38, s[24:27], s28 offen lds\n"
        "s_mov_b32 m0, 64864\n"
        "s_mov_b32 s28, 576\n"
        "buffer_load_dwordx4 v36, s[24:27], s28 offen lds\n"
        "s_mov_b32 m0, 65888\n"
        "s_nop 0\n"
        "buffer_load_dwordx4 v37, s[24:27], s28 offen lds\n"
        "s_mov_b32 m0, 66912\n"
        "s_nop 0\n"
        "buffer_load_dwordx4 v38, s[24:27], s28 offen lds\n"
        "s_mov_b32 m0, 67936\n"
        "s_mov_b32 s28, 2304\n"
        "buffer_load_dwordx4 v36, s[24:27], s28 offen lds\n"
        "s_mov_b32 m0, 68960\n"
        "s_nop 0\n"
        "buffer_load_dwordx4 v37, s[24:27], s28 offen lds\n"
        "s_mov_b32 m0, 69984\n"
        "s_nop 0\n"
        "buffer_load_dwordx4 v38, s[24:27], s28 offen lds\n"
        "s_mov_b32 m0, 71008\n"
        "s_mov_b32 s28, 2880\n"
        "buffer_load_dwordx4 v36, s[24:27], s28 offen lds\n"
        "s_mov_b32 m0, 72032\n"
        "s_nop 0\n"
        "buffer_load_dwordx4 v37, s[24:27], s28 offen lds\n"
        "s_mov_b32 m0, 73056\n"
        "s_nop 0\n"
        "buffer_load_dwordx4 v38, s[24:27], s28 offen lds\n"
        "s_mov_b32 m0, 74080\n"
        "s_mov_b32 s28, 4608\n"
        "buffer_load_dwordx4 v36, s[24:27], s28 offen lds\n"
        "s_mov_b32 m0, 75104\n"
        "s_nop 0\n"
        "buffer_load_dwordx4 v37, s[24:27], s28 offen lds\n"
        "s_mov_b32 m0, 76128\n"
        "s_nop 0\n"
        "buffer_load_dwordx4 v38, s[24:27], s28 offen lds\n"
        "s_mov_b32 m0, 77152\n"
        "s_mov_b32 s28, 5184\n"
        "buffer_load_dwordx4 v36, s[24:27], s28 offen lds\n"
        "s_mov_b32 m0, 78176\n"
        "s_nop 0\n"
        "buffer_load_dwordx4 v37, s[24:27], s28 offen lds\n"
        "s_mov_b32 m0, 79200\n"
        "s_nop 0\n"
        "buffer_load_dwordx4 v38, s[24:27], s28 offen lds\n"
        "s_mov_b32 s24, s50\n"
        "s_and_b32 s25, s51, 0xffff\n"
        "s_mov_b32 s26, 55296\n"
        "s_mov_b32 m0, 80224\n"
        "s_mov_b32 s28, 192\n"
        "buffer_load_dwordx4 v36, s[24:27], s28 offen lds\n"
        "s_mov_b32 m0, 81248\n"
        "s_nop 0\n"
        "buffer_load_dwordx4 v37, s[24:27], s28 offen lds\n"
        "s_mov_b32 m0, 82272\n"
        "s_nop 0\n"
        "buffer_load_dwordx4 v38, s[24:27], s28 offen lds\n"
        "s_mov_b32 m0, 83296\n"
        "s_mov_b32 s28, 768\n"
        "buffer_load_dwordx4 v36, s[24:27], s28 offen lds\n"
        "s_mov_b32 m0, 84320\n"
        "s_nop 0\n"
        "buffer_load_dwordx4 v37, s[24:27], s28 offen lds\n"
        "s_mov_b32 m0, 85344\n"
        "s_nop 0\n"
        "buffer_load_dwordx4 v38, s[24:27], s28 offen lds\n"
        "s_mov_b32 m0, 86368\n"
        "s_mov_b32 s28, 2496\n"
        "buffer_load_dwordx4 v36, s[24:27], s28 offen lds\n"
        "s_mov_b32 m0, 87392\n"
        "s_nop 0\n"
        "buffer_load_dwordx4 v37, s[24:27], s28 offen lds\n"
        "s_mov_b32 m0, 88416\n"
        "s_nop 0\n"
        "buffer_load_dwordx4 v38, s[24:27], s28 offen lds\n"
        "s_mov_b32 m0, 89440\n"
        "s_mov_b32 s28, 3072\n"
        "buffer_load_dwordx4 v36, s[24:27], s28 offen lds\n"
        "s_mov_b32 m0, 90464\n"
        "s_nop 0\n"
        "buffer_load_dwordx4 v37, s[24:27], s28 offen lds\n"
        "s_mov_b32 m0, 91488\n"
        "s_nop 0\n"
        "buffer_load_dwordx4 v38, s[24:27], s28 offen lds\n"
        "s_mov_b32 m0, 92512\n"
        "s_mov_b32 s28, 4800\n"
        "buffer_load_dwordx4 v36, s[24:27], s28 offen lds\n"
        "s_mov_b32 m0, 93536\n"
        "s_nop 0\n"
        "buffer_load_dwordx4 v37, s[24:27], s28 offen lds\n"
        "s_mov_b32 m0, 94560\n"
        "s_nop 0\n"
        "buffer_load_dwordx4 v38, s[24:27], s28 offen lds\n"
        "s_mov_b32 m0, 95584\n"
        "s_mov_b32 s28, 5376\n"
        "buffer_load_dwordx4 v36, s[24:27], s28 offen lds\n"
        "s_mov_b32 m0, 96608\n"
        "s_nop 0\n"
        "buffer_load_dwordx4 v37, s[24:27], s28 offen lds\n"
        "s_mov_b32 m0, 97632\n"
        "s_nop 0\n"
        "buffer_load_dwordx4 v38, s[24:27], s28 offen lds\n"
        "global_load_dwordx4 v[40:43], v57, s[22:23] offset:-3072\n"
        "global_load_dwordx4 v[44:47], v57, s[22:23] offset:-2048\n"
        "global_load_dwordx4 v[0:3], v57, s[22:23] offset:-1024\n"
        "global_load_dwordx4 v[4:7], v57, s[22:23] offset:0\n"
        "global_load_dwordx4 v[8:11], v57, s[22:23] offset:1024\n"
        "global_load_dwordx4 v[12:15], v57, s[22:23] offset:2048\n"
        "global_load_dwordx4 v[16:19], v57, s[22:23] offset:3072\n"
        "L76_%=:\n"
        "s_setprio 2\n"
        "s_waitcnt vmcnt(0)\n"
        "v_mov_b64 v[240:241], v[40:41]\n"
        "v_mov_b64 v[242:243], v[42:43]\n"
        "v_mov_b64 v[244:245], v[44:45]\n"
        "v_mov_b64 v[246:247], v[46:47]\n"
        "s_cmp_eq_u32 s6, 1\n"
        "s_cbranch_scc1 L81_%=\n"
        "s_mov_b32 s34, 0x4162ccfd\n"
        "s_mov_b32 s35, 21648\n"
        "s_mov_b32 s36, 21648\n"
        "s_mov_b32 s37, 21652\n"
        "L77_%=:\n"
        "ds_read_b32 v251, v250 offset:44\n"
        "s_waitcnt lgkmcnt(0)\n"
        "v_readfirstlane_b32 s40, v251\n"
        "s_nop 3\n"
        "s_cmp_ge_i32 s40, 1\n"
        "s_cbranch_scc1 L78_%=\n"
        "s_nop 0\n"
        "s_sub_u32 s41, s41, 1\n"
        "s_cbranch_scc0 L77_%=\n"
        "s_endpgm\n"
        "L78_%=:\n"
        "s_call_b64 s[30:31], FOLDM_%=\n"
        "s_branch L82_%=\n"
        "L81_%=:\n"
        "s_mov_b64 exec, 1\n"
        "ds_inc_u32 v250, v250 offset:44\n"
        "s_mov_b64 exec, -1\n"
        "L79_%=:\n"
        "ds_read_b32 v251, v250 offset:40\n"
        "s_waitcnt lgkmcnt(0)\n"
        "v_readfirstlane_b32 s40, v251\n"
        "s_nop 3\n"
        "s_cmp_ge_i32 s40, 4\n"
        "s_cbranch_scc1 L80_%=\n"
        "s_nop 0\n"
        "s_sub_u32 s41, s41, 1\n"
        "s_cbranch_scc0 L79_%=\n"
        "s_endpgm\n"
        "L80_%=:\n"
        "s_mov_b32 s34, 0xbe173353\n"
        "s_mov_b32 s35, 21656\n"
        "s_mov_b32 s36, 21656\n"
        "s_mov_b32 s37, 21648\n"
        "s_call_b64 s[30:31], FOLDR_%=\n"
        "L82_%=:\n"
        "s_branch CONS_%=\n"
        "FOLDM_%=:\n"
        "ds_read_b128 v[88:91], v242\n"
        "ds_read_b128 v[92:95], v240\n"
        "ds_read_b128 v[180:183], v242 offset:64\n"
        "ds_read_b128 v[172:175], v240 offset:64\n"
        "ds_read_b128 v[184:187], v242 offset:128\n"
        "ds_read_b128 v[164:167], v240 offset:128\n"
        "ds_read_b128 v[168:171], v241\n"
        "ds_read_b128 v[100:103], v241 offset:64\n"
        "ds_read_b128 v[104:107], v241 offset:128\n"
        "ds_read_b128 v[140:143], v240 offset:6144\n"
        "ds_read_b128 v[132:135], v240 offset:6208\n"
        "ds_read_b128 v[96:99], v240 offset:6272\n"
        "ds_read_b128 v[136:139], v241 offset:6144\n"
        "ds_read_b128 v[128:131], v241 offset:6208\n"
        "ds_read_b128 v[108:111], v241 offset:6272\n"
        "s_waitcnt lgkmcnt(13)\n"
        "v_mfma_f32_16x16x32_bf16 v[64:67], v[92:95], v[88:91], 0\n"
        "s_waitcnt lgkmcnt(11)\n"
        "ds_read_b128 v[152:155], v240 offset:12288\n"
        "v_mfma_f32_16x16x32_bf16 v[64:67], v[172:175], v[180:183], v[64:67]\n"
        "s_waitcnt lgkmcnt(8)\n"
        "ds_read_b128 v[160:163], v240 offset:12352\n"
        "v_mfma_f32_16x16x32_bf16 v[64:67], v[164:167], v[184:187], v[64:67]\n"
        "ds_read_b128 v[112:115], v240 offset:12416\n"
        "v_mfma_f32_16x16x32_bf16 v[68:71], v[168:171], v[88:91], 0\n"
        "ds_read_b128 v[116:119], v241 offset:12288\n"
        "v_mfma_f32_16x16x32_bf16 v[68:71], v[100:103], v[180:183], v[68:71]\n"
        "s_waitcnt lgkmcnt(4)\n"
        "ds_read_b128 v[120:123], v241 offset:12352\n"
        "v_mfma_f32_16x16x32_bf16 v[68:71], v[104:107], v[184:187], v[68:71]\n"
        "ds_read_b128 v[124:127], v241 offset:12416\n"
        "v_mfma_f32_16x16x32_bf16 v[72:75], v[140:143], v[88:91], 0\n"
        "ds_read_b128 v[144:147], v243\n"
        "v_mfma_f32_16x16x32_bf16 v[72:75], v[132:135], v[180:183], v[72:75]\n"
        "ds_read_b128 v[148:151], v243 offset:64\n"
        "v_mfma_f32_16x16x32_bf16 v[72:75], v[96:99], v[184:187], v[72:75]\n"
        "ds_read_b128 v[156:159], v243 offset:128\n"
        "v_mfma_f32_16x16x32_bf16 v[76:79], v[136:139], v[88:91], 0\n"
        "v_add_f32 v176, v64, v68\n"
        "v_add_f32 v177, v65, v69\n"
        "v_add_f32 v178, v66, v70\n"
        "v_add_f32 v179, v67, v71\n"
        "v_mfma_f32_16x16x32_bf16 v[76:79], v[128:131], v[180:183], v[76:79]\n"
        "v_mfma_f32_16x16x32_bf16 v[76:79], v[108:111], v[184:187], v[76:79]\n"
        "s_waitcnt lgkmcnt(3)\n"
        "v_add_f32 v176, v176, v72\n"
        "v_add_f32 v177, v177, v73\n"
        "v_add_f32 v178, v178, v74\n"
        "v_add_f32 v179, v179, v75\n"
        "v_mfma_f32_16x16x32_bf16 v[80:83], v[152:155], v[88:91], 0\n"
        "v_mfma_f32_16x16x32_bf16 v[80:83], v[160:163], v[180:183], v[80:83]\n"
        "v_mfma_f32_16x16x32_bf16 v[80:83], v[112:115], v[184:187], v[80:83]\n"
        "v_add_f32 v176, v176, v76\n"
        "v_add_f32 v177, v177, v77\n"
        "v_add_f32 v178, v178, v78\n"
        "v_add_f32 v179, v179, v79\n"
        "v_mfma_f32_16x16x32_bf16 v[84:87], v[116:119], v[88:91], 0\n"
        "v_mfma_f32_16x16x32_bf16 v[84:87], v[120:123], v[180:183], v[84:87]\n"
        "v_mfma_f32_16x16x32_bf16 v[84:87], v[124:127], v[184:187], v[84:87]\n"
        "s_waitcnt lgkmcnt(0)\n"
        "v_add_f32 v176, v176, v80\n"
        "v_add_f32 v177, v177, v81\n"
        "v_add_f32 v178, v178, v82\n"
        "v_add_f32 v179, v179, v83\n"
        "v_mov_b32 v239, s35\n"
        "s_waitcnt vmcnt(6)\n"
        "s_mov_b64 exec, 1\n"
        "ds_inc_u32 v239, v239 offset:0\n"
        "s_mov_b64 exec, -1\n"
        "v_add_f32 v176, v176, v84\n"
        "v_add_f32 v177, v177, v85\n"
        "v_add_f32 v178, v178, v86\n"
        "v_add_f32 v179, v179, v87\n"
        "v_add_f32 v176, v176, v177\n"
        "v_add_f32 v178, v178, v179\n"
        "v_add_f32 v176, v176, v178\n"
        "v_mov_b32 v177, v176\n"
        "v_mfma_f32_16x16x32_bf16 v[88:91], v[92:95], v[144:147], 0\n"
        "v_mfma_f32_16x16x32_bf16 v[88:91], v[172:175], v[148:151], v[88:91]\n"
        "v_permlane32_swap_b32 v176, v177\n"
        "v_add_f32 v176, v176, v177\n"
        "v_mov_b32 v177, v176\n"
        "v_mfma_f32_16x16x32_bf16 v[88:91], v[164:167], v[156:159], v[88:91]\n"
        "v_mfma_f32_16x16x32_bf16 v[92:95], v[168:171], v[144:147], 0\n"
        "v_permlane16_swap_b32 v176, v177\n"
        "v_add_f32 v176, v176, v177\n"
        "v_mul_f32 v176, 0xbc2aaaab, v176\n"
        "v_mul_f32 v176, s34, v176\n"
        "v_fma_f32 v64, v64, s34, v176\n"
        "v_fma_f32 v65, v65, s34, v176\n"
        "v_fma_f32 v66, v66, s34, v176\n"
        "v_fma_f32 v67, v67, s34, v176\n"
        "v_fma_f32 v68, v68, s34, v176\n"
        "v_fma_f32 v69, v69, s34, v176\n"
        "v_fma_f32 v70, v70, s34, v176\n"
        "v_fma_f32 v71, v71, s34, v176\n"
        "v_fma_f32 v72, v72, s34, v176\n"
        "v_fma_f32 v73, v73, s34, v176\n"
        "v_fma_f32 v74, v74, s34, v176\n"
        "v_fma_f32 v75, v75, s34, v176\n"
        "v_fma_f32 v76, v76, s34, v176\n"
        "v_fma_f32 v77, v77, s34, v176\n"
        "v_fma_f32 v78, v78, s34, v176\n"
        "v_fma_f32 v79, v79, s34, v176\n"
        "v_fma_f32 v80, v80, s34, v176\n"
        "v_fma_f32 v81, v81, s34, v176\n"
        "v_fma_f32 v82, v82, s34, v176\n"
        "v_fma_f32 v83, v83, s34, v176\n"
        "v_fma_f32 v84, v84, s34, v176\n"
        "v_fma_f32 v85, v85, s34, v176\n"
        "v_fma_f32 v86, v86, s34, v176\n"
        "v_fma_f32 v87, v87, s34, v176\n"
        "v_mfma_f32_16x16x32_bf16 v[92:95], v[100:103], v[148:151], v[92:95]\n"
        "v_mfma_f32_16x16x32_bf16 v[92:95], v[104:107], v[156:159], v[92:95]\n"
        "v_mfma_f32_16x16x32_bf16 v[100:103], v[140:143], v[144:147], 0\n"
        "v_mfma_f32_16x16x32_bf16 v[100:103], v[132:135], v[148:151], v[100:103]\n"
        "v_mfma_f32_16x16x32_bf16 v[100:103], v[96:99], v[156:159], v[100:103]\n"
        "v_mfma_f32_16x16x32_bf16 v[104:107], v[136:139], v[144:147], 0\n"
        "v_mfma_f32_16x16x32_bf16 v[104:107], v[128:131], v[148:151], v[104:107]\n"
        "v_mfma_f32_16x16x32_bf16 v[104:107], v[108:111], v[156:159], v[104:107]\n"
        "v_mfma_f32_16x16x32_bf16 v[96:99], v[152:155], v[144:147], 0\n"
        "v_mfma_f32_16x16x32_bf16 v[96:99], v[160:163], v[148:151], v[96:99]\n"
        "v_add_f32 v128, v88, v92\n"
        "v_add_f32 v129, v89, v93\n"
        "v_add_f32 v130, v90, v94\n"
        "v_add_f32 v131, v91, v95\n"
        "v_add_f32 v128, v128, v100\n"
        "v_add_f32 v129, v129, v101\n"
        "v_add_f32 v130, v130, v102\n"
        "v_add_f32 v131, v131, v103\n"
        "v_add_f32 v128, v128, v104\n"
        "v_add_f32 v129, v129, v105\n"
        "v_add_f32 v130, v130, v106\n"
        "v_add_f32 v131, v131, v107\n"
        "v_mfma_f32_16x16x32_bf16 v[96:99], v[112:115], v[156:159], v[96:99]\n"
        "v_mfma_f32_16x16x32_bf16 v[108:111], v[116:119], v[144:147], 0\n"
        "v_mfma_f32_16x16x32_bf16 v[108:111], v[120:123], v[148:151], v[108:111]\n"
        "v_mfma_f32_16x16x32_bf16 v[108:111], v[124:127], v[156:159], v[108:111]\n"
        "v_mov_b32 v239, s37\n"
        "s_nop 3\n"
        "v_add_f32 v128, v128, v96\n"
        "v_add_f32 v129, v129, v97\n"
        "v_add_f32 v130, v130, v98\n"
        "v_add_f32 v131, v131, v99\n"
        "v_add_f32 v128, v128, v108\n"
        "v_add_f32 v129, v129, v109\n"
        "v_add_f32 v130, v130, v110\n"
        "v_add_f32 v131, v131, v111\n"
        "v_add_f32 v128, v128, v129\n"
        "v_add_f32 v130, v130, v131\n"
        "v_add_f32 v128, v128, v130\n"
        "v_mov_b32 v129, v128\n"
        "s_nop 1\n"
        "v_permlane32_swap_b32 v128, v129\n"
        "v_add_f32 v128, v128, v129\n"
        "v_mov_b32 v129, v128\n"
        "s_nop 1\n"
        "v_permlane16_swap_b32 v128, v129\n"
        "v_add_f32 v128, v128, v129\n"
        "v_mul_f32 v128, 0xbc2aaaab, v128\n"
        "v_mul_f32 v128, s34, v128\n"
        "v_fma_f32 v88, v88, s34, v128\n"
        "v_fma_f32 v89, v89, s34, v128\n"
        "v_fma_f32 v90, v90, s34, v128\n"
        "v_fma_f32 v91, v91, s34, v128\n"
        "v_fma_f32 v92, v92, s34, v128\n"
        "v_fma_f32 v93, v93, s34, v128\n"
        "v_fma_f32 v94, v94, s34, v128\n"
        "v_fma_f32 v95, v95, s34, v128\n"
        "v_fma_f32 v100, v100, s34, v128\n"
        "v_fma_f32 v101, v101, s34, v128\n"
        "v_fma_f32 v102, v102, s34, v128\n"
        "v_fma_f32 v103, v103, s34, v128\n"
        "v_fma_f32 v104, v104, s34, v128\n"
        "v_fma_f32 v105, v105, s34, v128\n"
        "v_fma_f32 v106, v106, s34, v128\n"
        "v_fma_f32 v107, v107, s34, v128\n"
        "v_fma_f32 v96, v96, s34, v128\n"
        "v_fma_f32 v97, v97, s34, v128\n"
        "v_fma_f32 v98, v98, s34, v128\n"
        "v_fma_f32 v99, v99, s34, v128\n"
        "v_fma_f32 v108, v108, s34, v128\n"
        "v_fma_f32 v109, v109, s34, v128\n"
        "v_fma_f32 v110, v110, s34, v128\n"
        "v_fma_f32 v111, v111, s34, v128\n"
        "v_mov_b32 v237, s36\n"
        "L62_%=:\n"
        "ds_read_b32 v238, v237 offset:0\n"
        "s_waitcnt lgkmcnt(0)\n"
        "v_readfirstlane_b32 s40, v238\n"
        "s_nop 3\n"
        "s_cmp_ge_i32 s40, 4\n"
        "s_cbranch_scc1 L63_%=\n"
        "s_nop 0\n"
        "s_sub_u32 s41, s41, 1\n"
        "s_cbranch_scc0 L62_%=\n"
        "s_endpgm\n"
        "L63_%=:\n"
        "ds_read_b128 v[132:135], v246\n"
        "ds_read_b128 v[128:131], v246 offset:16\n"
        "ds_read_b128 v[124:127], v246 offset:128\n"
        "ds_read_b128 v[120:123], v246 offset:144\n"
        "ds_read_b128 v[116:119], v246 offset:256\n"
        "ds_read_b128 v[112:115], v246 offset:272\n"
        "s_waitcnt lgkmcnt(5)\n"
        "v_add_f32 v64, v64, v132\n"
        "v_add_f32 v65, v65, v133\n"
        "v_add_f32 v66, v66, v134\n"
        "v_add_f32 v67, v67, v135\n"
        "s_waitcnt lgkmcnt(0)\n"
        "v_add_f32 v68, v68, v128\n"
        "v_add_f32 v69, v69, v129\n"
        "v_add_f32 v70, v70, v130\n"
        "v_add_f32 v71, v71, v131\n"
        "v_add_f32 v88, v88, v132\n"
        "v_add_f32 v89, v89, v133\n"
        "v_add_f32 v90, v90, v134\n"
        "v_add_f32 v91, v91, v135\n"
        "v_add_f32 v92, v92, v128\n"
        "v_add_f32 v93, v93, v129\n"
        "v_add_f32 v94, v94, v130\n"
        "v_add_f32 v95, v95, v131\n"
        "v_add_f32 v72, v72, v124\n"
        "v_add_f32 v73, v73, v125\n"
        "v_add_f32 v74, v74, v126\n"
        "v_add_f32 v75, v75, v127\n"
        "v_add_f32 v76, v76, v120\n"
        "v_add_f32 v77, v77, v121\n"
        "v_add_f32 v78, v78, v122\n"
        "v_add_f32 v79, v79, v123\n"
        "v_add_f32 v100, v100, v124\n"
        "v_add_f32 v101, v101, v125\n"
        "v_add_f32 v102, v102, v126\n"
        "v_add_f32 v103, v103, v127\n"
        "v_add_f32 v104, v104, v120\n"
        "v_add_f32 v105, v105, v121\n"
        "v_add_f32 v106, v106, v122\n"
        "v_add_f32 v107, v107, v123\n"
        "v_add_f32 v80, v80, v116\n"
        "v_add_f32 v81, v81, v117\n"
        "v_add_f32 v82, v82, v118\n"
        "v_add_f32 v83, v83, v119\n"
        "v_add_f32 v84, v84, v112\n"
        "v_add_f32 v85, v85, v113\n"
        "v_add_f32 v86, v86, v114\n"
        "v_add_f32 v87, v87, v115\n"
        "v_add_f32 v96, v96, v116\n"
        "v_add_f32 v97, v97, v117\n"
        "v_add_f32 v98, v98, v118\n"
        "v_add_f32 v99, v99, v119\n"
        "v_add_f32 v108, v108, v112\n"
        "v_add_f32 v109, v109, v113\n"
        "v_add_f32 v110, v110, v114\n"
        "v_add_f32 v111, v111, v115\n"
        "v_cvt_pk_bf16_f32 v112, v64, v65\n"
        "v_cvt_pk_bf16_f32 v113, v66, v67\n"
        "v_cvt_pk_bf16_f32 v114, v68, v69\n"
        "v_cvt_pk_bf16_f32 v115, v70, v71\n"
        "v_cvt_pk_bf16_f32 v64, v88, v89\n"
        "v_cvt_pk_bf16_f32 v65, v90, v91\n"
        "v_cvt_pk_bf16_f32 v66, v92, v93\n"
        "v_cvt_pk_bf16_f32 v67, v94, v95\n"
        "v_cvt_pk_bf16_f32 v68, v72, v73\n"
        "v_cvt_pk_bf16_f32 v69, v74, v75\n"
        "v_cvt_pk_bf16_f32 v70, v76, v77\n"
        "v_cvt_pk_bf16_f32 v71, v78, v79\n"
        "v_cvt_pk_bf16_f32 v72, v100, v101\n"
        "v_cvt_pk_bf16_f32 v73, v102, v103\n"
        "v_cvt_pk_bf16_f32 v74, v104, v105\n"
        "v_cvt_pk_bf16_f32 v75, v106, v107\n"
        "v_cvt_pk_bf16_f32 v76, v80, v81\n"
        "v_cvt_pk_bf16_f32 v77, v82, v83\n"
        "v_cvt_pk_bf16_f32 v78, v84, v85\n"
        "v_cvt_pk_bf16_f32 v79, v86, v87\n"
        "v_cvt_pk_bf16_f32 v80, v96, v97\n"
        "v_cvt_pk_bf16_f32 v81, v98, v99\n"
        "v_cvt_pk_bf16_f32 v82, v108, v109\n"
        "v_cvt_pk_bf16_f32 v83, v110, v111\n"
        "ds_write_b128 v244, v[112:115]\n"
        "ds_write_b128 v245, v[64:67]\n"
        "ds_write_b128 v244, v[68:71] offset:1024\n"
        "ds_write_b128 v245, v[72:75] offset:1024\n"
        "ds_write_b128 v244, v[76:79] offset:2048\n"
        "ds_write_b128 v245, v[80:83] offset:2048\n"
        "s_mov_b64 exec, 1\n"
        "ds_inc_u32 v239, v239 offset:0\n"
        "s_mov_b64 exec, -1\n"
        "s_setpc_b64 s[30:31]\n"
        "FOLDR_%=:\n"
        "ds_read_b64_tr_b16 v[84:85], v242 offset:0\n"
        "ds_read_b64_tr_b16 v[86:87], v243 offset:0\n"
        "ds_read_b64_tr_b16 v[92:93], v242 offset:6144\n"
        "ds_read_b64_tr_b16 v[94:95], v243 offset:6144\n"
        "ds_read_b64_tr_b16 v[80:81], v242 offset:12288\n"
        "ds_read_b64_tr_b16 v[82:83], v243 offset:12288\n"
        "ds_read_b64_tr_b16 v[76:77], v242 offset:8\n"
        "ds_read_b64_tr_b16 v[78:79], v243 offset:8\n"
        "ds_read_b64_tr_b16 v[68:69], v242 offset:6152\n"
        "ds_read_b64_tr_b16 v[70:71], v243 offset:6152\n"
        "ds_read_b64_tr_b16 v[72:73], v242 offset:12296\n"
        "ds_read_b64_tr_b16 v[74:75], v243 offset:12296\n"
        "v_mov_b32 v64, 0x3f803f80\n"
        "v_mov_b32 v65, 0x3f803f80\n"
        "v_mov_b32 v66, 0x3f803f80\n"
        "v_mov_b32 v67, 0x3f803f80\n"
        "ds_read_b64_tr_b16 v[88:89], v242 offset:64\n"
        "ds_read_b64_tr_b16 v[90:91], v243 offset:64\n"
        "s_waitcnt lgkmcnt(12)\n"
        "v_mfma_f32_16x16x32_bf16 v[104:107], v[84:87], v[64:67], 0\n"
        "s_waitcnt lgkmcnt(10)\n"
        "v_mfma_f32_16x16x32_bf16 v[104:107], v[92:95], v[64:67], v[104:107]\n"
        "s_waitcnt lgkmcnt(6)\n"
        "ds_read_b64_tr_b16 v[96:97], v242 offset:6208\n"
        "ds_read_b64_tr_b16 v[98:99], v243 offset:6208\n"
        "v_mfma_f32_16x16x32_bf16 v[104:107], v[80:83], v[64:67], v[104:107]\n"
        "ds_read_b64_tr_b16 v[92:93], v242 offset:12352\n"
        "ds_read_b64_tr_b16 v[94:95], v243 offset:12352\n"
        "v_mfma_f32_16x16x32_bf16 v[100:103], v[76:79], v[64:67], 0\n"
        "s_waitcnt lgkmcnt(6)\n"
        "ds_read_b64_tr_b16 v[80:81], v242 offset:72\n"
        "ds_read_b64_tr_b16 v[82:83], v243 offset:72\n"
        "v_mfma_f32_16x16x32_bf16 v[100:103], v[68:71], v[64:67], v[100:103]\n"
        "ds_read_b64_tr_b16 v[76:77], v242 offset:6216\n"
        "ds_read_b64_tr_b16 v[78:79], v243 offset:6216\n"
        "v_cvt_pk_bf16_f32 v68, v104, v105\n"
        "v_cvt_pk_bf16_f32 v69, v106, v107\n"
        "v_mfma_f32_16x16x32_bf16 v[100:103], v[72:75], v[64:67], v[100:103]\n"
        "s_waitcnt lgkmcnt(8)\n"
        "ds_read_b64_tr_b16 v[84:85], v242 offset:12360\n"
        "ds_read_b64_tr_b16 v[86:87], v243 offset:12360\n"
        "v_mfma_f32_16x16x32_bf16 v[104:107], v[88:91], v[64:67], 0\n"
        "ds_read_b64_tr_b16 v[72:73], v242 offset:128\n"
        "ds_read_b64_tr_b16 v[74:75], v243 offset:128\n"
        "s_waitcnt lgkmcnt(10)\n"
        "v_mfma_f32_16x16x32_bf16 v[104:107], v[96:99], v[64:67], v[104:107]\n"
        "ds_read_b64_tr_b16 v[88:89], v242 offset:6272\n"
        "ds_read_b64_tr_b16 v[90:91], v243 offset:6272\n"
        "v_cvt_pk_bf16_f32 v70, v100, v101\n"
        "v_cvt_pk_bf16_f32 v71, v102, v103\n"
        "s_waitcnt lgkmcnt(9)\n"
        "v_mfma_f32_16x16x32_bf16 v[104:107], v[92:95], v[64:67], v[104:107]\n"
        "s_waitcnt lgkmcnt(8)\n"
        "ds_read_b64_tr_b16 v[100:101], v242 offset:12416\n"
        "ds_read_b64_tr_b16 v[102:103], v243 offset:12416\n"
        "v_mfma_f32_16x16x32_bf16 v[108:111], v[80:83], v[64:67], 0\n"
        "s_waitcnt lgkmcnt(8)\n"
        "ds_read_b64_tr_b16 v[92:93], v242 offset:136\n"
        "ds_read_b64_tr_b16 v[94:95], v243 offset:136\n"
        "v_mfma_f32_16x16x32_bf16 v[108:111], v[76:79], v[64:67], v[108:111]\n"
        "ds_read_b64_tr_b16 v[96:97], v242 offset:6280\n"
        "ds_read_b64_tr_b16 v[98:99], v243 offset:6280\n"
        "v_cvt_pk_bf16_f32 v80, v104, v105\n"
        "v_cvt_pk_bf16_f32 v81, v106, v107\n"
        "s_waitcnt lgkmcnt(8)\n"
        "v_mfma_f32_16x16x32_bf16 v[108:111], v[84:87], v[64:67], v[108:111]\n"
        "ds_read_b64_tr_b16 v[76:77], v242 offset:12424\n"
        "ds_read_b64_tr_b16 v[78:79], v243 offset:12424\n"
        "v_mfma_f32_16x16x32_bf16 v[116:119], v[72:75], v[64:67], 0\n"
        "s_waitcnt lgkmcnt(8)\n"
        "ds_read_b128 v[72:75], v240\n"
        "v_mfma_f32_16x16x32_bf16 v[116:119], v[88:91], v[64:67], v[116:119]\n"
        "ds_read_b128 v[104:107], v241\n"
        "s_waitcnt lgkmcnt(8)\n"
        "v_mfma_f32_16x16x32_bf16 v[116:119], v[100:103], v[64:67], v[116:119]\n"
        "ds_read_b128 v[84:87], v240 offset:6144\n"
        "v_cvt_pk_bf16_f32 v82, v108, v109\n"
        "v_cvt_pk_bf16_f32 v83, v110, v111\n"
        "s_waitcnt lgkmcnt(7)\n"
        "v_mfma_f32_16x16x32_bf16 v[112:115], v[92:95], v[64:67], 0\n"
        "s_waitcnt lgkmcnt(5)\n"
        "ds_read_b128 v[92:95], v241 offset:6144\n"
        "v_mfma_f32_16x16x32_bf16 v[112:115], v[96:99], v[64:67], v[112:115]\n"
        "ds_read_b128 v[100:103], v240 offset:12288\n"
        "v_cvt_pk_bf16_f32 v88, v116, v117\n"
        "v_cvt_pk_bf16_f32 v89, v118, v119\n"
        "s_waitcnt lgkmcnt(5)\n"
        "v_mfma_f32_16x16x32_bf16 v[112:115], v[76:79], v[64:67], v[112:115]\n"
        "s_waitcnt lgkmcnt(4)\n"
        "ds_read_b128 v[116:119], v241 offset:12288\n"
        "v_mfma_f32_16x16x32_bf16 v[64:67], v[68:71], v[72:75], 0\n"
        "s_waitcnt lgkmcnt(4)\n"
        "ds_read_b128 v[120:123], v240 offset:64\n"
        "v_mfma_f32_16x16x32_bf16 v[72:75], v[68:71], v[104:107], 0\n"
        "ds_read_b128 v[108:111], v241 offset:64\n"
        "s_waitcnt lgkmcnt(5)\n"
        "v_mfma_f32_16x16x32_bf16 v[76:79], v[68:71], v[84:87], 0\n"
        "ds_read_b128 v[104:107], v240 offset:6208\n"
        "v_cvt_pk_bf16_f32 v90, v112, v113\n"
        "v_cvt_pk_bf16_f32 v91, v114, v115\n"
        "s_waitcnt lgkmcnt(5)\n"
        "v_mfma_f32_16x16x32_bf16 v[84:87], v[68:71], v[92:95], 0\n"
        "ds_read_b128 v[124:127], v241 offset:6208\n"
        "s_waitcnt lgkmcnt(5)\n"
        "v_mfma_f32_16x16x32_bf16 v[92:95], v[68:71], v[100:103], 0\n"
        "ds_read_b128 v[112:115], v240 offset:12352\n"
        "s_waitcnt lgkmcnt(5)\n"
        "v_mfma_f32_16x16x32_bf16 v[96:99], v[68:71], v[116:119], 0\n"
        "ds_read_b128 v[100:103], v241 offset:12352\n"
        "s_waitcnt lgkmcnt(5)\n"
        "v_mfma_f32_16x16x32_bf16 v[64:67], v[80:83], v[120:123], v[64:67]\n"
        "ds_read_b128 v[68:71], v240 offset:128\n"
        "s_waitcnt lgkmcnt(5)\n"
        "v_mfma_f32_16x16x32_bf16 v[72:75], v[80:83], v[108:111], v[72:75]\n"
        "ds_read_b128 v[116:119], v241 offset:128\n"
        "s_waitcnt lgkmcnt(5)\n"
        "v_mfma_f32_16x16x32_bf16 v[76:79], v[80:83], v[104:107], v[76:79]\n"
        "ds_read_b128 v[108:111], v240 offset:6272\n"
        "s_waitcnt lgkmcnt(5)\n"
        "v_mfma_f32_16x16x32_bf16 v[84:87], v[80:83], v[124:127], v[84:87]\n"
        "ds_read_b128 v[104:107], v241 offset:6272\n"
        "s_waitcnt lgkmcnt(5)\n"
        "v_mfma_f32_16x16x32_bf16 v[92:95], v[80:83], v[112:115], v[92:95]\n"
        "ds_read_b128 v[120:123], v240 offset:12416\n"
        "s_waitcnt lgkmcnt(5)\n"
        "v_mfma_f32_16x16x32_bf16 v[96:99], v[80:83], v[100:103], v[96:99]\n"
        "ds_read_b128 v[112:115], v241 offset:12416\n"
        "s_waitcnt lgkmcnt(5)\n"
        "v_mfma_f32_16x16x32_bf16 v[64:67], v[88:91], v[68:71], v[64:67]\n"
        "s_waitcnt lgkmcnt(4)\n"
        "v_mfma_f32_16x16x32_bf16 v[72:75], v[88:91], v[116:119], v[72:75]\n"
        "v_mov_b32 v239, s35\n"
        "s_waitcnt lgkmcnt(3)\n"
        "v_mfma_f32_16x16x32_bf16 v[76:79], v[88:91], v[108:111], v[76:79]\n"
        "s_waitcnt vmcnt(6)\n"
        "s_mov_b64 exec, 1\n"
        "ds_inc_u32 v239, v239 offset:0\n"
        "s_mov_b64 exec, -1\n"
        "s_waitcnt lgkmcnt(3)\n"
        "v_mfma_f32_16x16x32_bf16 v[84:87], v[88:91], v[104:107], v[84:87]\n"
        "v_add_f32 v238, v64, v72\n"
        "s_waitcnt lgkmcnt(2)\n"
        "v_mfma_f32_16x16x32_bf16 v[92:95], v[88:91], v[120:123], v[92:95]\n"
        "v_add_f32 v238, v238, v76\n"
        "s_waitcnt lgkmcnt(1)\n"
        "v_mfma_f32_16x16x32_bf16 v[96:99], v[88:91], v[112:115], v[96:99]\n"
        "v_mov_b32 v239, s37\n"
        "s_nop 0\n"
        "v_add_f32 v238, v238, v84\n"
        "s_nop 1\n"
        "v_add_f32 v238, v238, v92\n"
        "s_nop 1\n"
        "v_add_f32 v238, v238, v96\n"
        "s_nop 1\n"
        "v_add_f32_dpp v238, v238, v238 row_ror:8 row_mask:0xf bank_mask:0xf\n"
        "s_nop 1\n"
        "v_add_f32_dpp v238, v238, v238 row_ror:4 row_mask:0xf bank_mask:0xf\n"
        "s_nop 1\n"
        "v_add_f32_dpp v238, v238, v238 row_ror:2 row_mask:0xf bank_mask:0xf\n"
        "s_nop 1\n"
        "v_add_f32_dpp v238, v238, v238 row_ror:1 row_mask:0xf bank_mask:0xf\n"
        "v_mul_f32 v238, 0xbc2aaaab, v238\n"
        "v_mul_f32 v238, s34, v238\n"
        "v_fma_f32 v64, v64, s34, v238\n"
        "v_fma_f32 v72, v72, s34, v238\n"
        "v_fma_f32 v76, v76, s34, v238\n"
        "v_fma_f32 v84, v84, s34, v238\n"
        "v_fma_f32 v92, v92, s34, v238\n"
        "v_fma_f32 v96, v96, s34, v238\n"
        "ds_write_b32 v244, v64\n"
        "ds_write_b32 v244, v72 offset:16\n"
        "ds_write_b32 v244, v76 offset:128\n"
        "ds_write_b32 v244, v84 offset:144\n"
        "ds_write_b32 v244, v92 offset:256\n"
        "ds_write_b32 v244, v96 offset:272\n"
        "s_mov_b64 exec, 1\n"
        "ds_inc_u32 v239, v239 offset:0\n"
        "s_mov_b64 exec, -1\n"
        "s_setpc_b64 s[30:31]\n"
        "FOLDW_%=:\n"
        "ds_read_b64_tr_b16 v[64:65], v242 offset:0\n"
        "ds_read_b64_tr_b16 v[66:67], v243 offset:0\n"
        "ds_read_b128 v[172:175], v240\n"
        "ds_read_b64_tr_b16 v[180:181], v242 offset:6144\n"
        "ds_read_b64_tr_b16 v[182:183], v243 offset:6144\n"
        "ds_read_b128 v[120:123], v240 offset:64\n"
        "ds_read_b64_tr_b16 v[184:185], v242 offset:12288\n"
        "ds_read_b64_tr_b16 v[186:187], v243 offset:12288\n"
        "ds_read_b128 v[168:171], v240 offset:128\n"
        "ds_read_b128 v[160:163], v241\n"
        "ds_read_b128 v[148:151], v241 offset:64\n"
        "ds_read_b128 v[132:135], v241 offset:128\n"
        "ds_read_b128 v[124:127], v240 offset:6144\n"
        "ds_read_b128 v[104:107], v240 offset:6208\n"
        "ds_read_b128 v[92:95], v240 offset:6272\n"
        "ds_read_b128 v[96:99], v241 offset:6144\n"
        "ds_read_b128 v[88:91], v241 offset:6208\n"
        "ds_read_b128 v[84:87], v241 offset:6272\n"
        "s_waitcnt lgkmcnt(15)\n"
        "v_mfma_f32_16x16x32_bf16 v[136:139], v[172:175], v[64:67], 0\n"
        "ds_read_b128 v[100:103], v240 offset:12288\n"
        "s_waitcnt lgkmcnt(12)\n"
        "v_mfma_f32_16x16x32_bf16 v[136:139], v[120:123], v[180:183], v[136:139]\n"
        "s_waitcnt lgkmcnt(10)\n"
        "ds_read_b128 v[108:111], v240 offset:12352\n"
        "v_mfma_f32_16x16x32_bf16 v[136:139], v[168:171], v[184:187], v[136:139]\n"
        "s_waitcnt lgkmcnt(7)\n"
        "ds_read_b128 v[68:71], v240 offset:12416\n"
        "v_mfma_f32_16x16x32_bf16 v[140:143], v[160:163], v[64:67], 0\n"
        "ds_read_b128 v[72:75], v241 offset:12288\n"
        "v_mfma_f32_16x16x32_bf16 v[140:143], v[148:151], v[180:183], v[140:143]\n"
        "ds_read_b128 v[76:79], v241 offset:12352\n"
        "v_mfma_f32_16x16x32_bf16 v[140:143], v[132:135], v[184:187], v[140:143]\n"
        "ds_read_b128 v[80:83], v241 offset:12416\n"
        "v_mfma_f32_16x16x32_bf16 v[144:147], v[124:127], v[64:67], 0\n"
        "s_waitcnt lgkmcnt(6)\n"
        "ds_read_b64_tr_b16 v[112:113], v242 offset:8\n"
        "ds_read_b64_tr_b16 v[114:115], v243 offset:8\n"
        "v_mfma_f32_16x16x32_bf16 v[144:147], v[104:107], v[180:183], v[144:147]\n"
        "ds_read_b64_tr_b16 v[116:117], v242 offset:6152\n"
        "ds_read_b64_tr_b16 v[118:119], v243 offset:6152\n"
        "v_add_f32 v164, v136, v140\n"
        "v_add_f32 v165, v137, v141\n"
        "v_add_f32 v166, v138, v142\n"
        "v_add_f32 v167, v139, v143\n"
        "v_mfma_f32_16x16x32_bf16 v[144:147], v[92:95], v[184:187], v[144:147]\n"
        "ds_read_b64_tr_b16 v[128:129], v242 offset:12296\n"
        "ds_read_b64_tr_b16 v[130:131], v243 offset:12296\n"
        "v_mfma_f32_16x16x32_bf16 v[152:155], v[96:99], v[64:67], 0\n"
        "v_mfma_f32_16x16x32_bf16 v[152:155], v[88:91], v[180:183], v[152:155]\n"
        "v_mfma_f32_16x16x32_bf16 v[152:155], v[84:87], v[184:187], v[152:155]\n"
        "s_waitcnt lgkmcnt(6)\n"
        "v_mfma_f32_16x16x32_bf16 v[156:159], v[100:103], v[64:67], 0\n"
        "v_mfma_f32_16x16x32_bf16 v[156:159], v[108:111], v[180:183], v[156:159]\n"
        "v_add_f32 v164, v164, v144\n"
        "v_add_f32 v165, v165, v145\n"
        "v_add_f32 v166, v166, v146\n"
        "v_add_f32 v167, v167, v147\n"
        "v_mfma_f32_16x16x32_bf16 v[156:159], v[68:71], v[184:187], v[156:159]\n"
        "v_add_f32 v164, v164, v152\n"
        "v_add_f32 v165, v165, v153\n"
        "v_add_f32 v166, v166, v154\n"
        "v_add_f32 v167, v167, v155\n"
        "v_mfma_f32_16x16x32_bf16 v[176:179], v[72:75], v[64:67], 0\n"
        "v_mfma_f32_16x16x32_bf16 v[176:179], v[76:79], v[180:183], v[176:179]\n"
        "v_mfma_f32_16x16x32_bf16 v[176:179], v[80:83], v[184:187], v[176:179]\n"
        "s_waitcnt lgkmcnt(0)\n"
        "v_mov_b32 v239, s35\n"
        "v_add_f32 v164, v164, v156\n"
        "v_add_f32 v165, v165, v157\n"
        "v_add_f32 v166, v166, v158\n"
        "v_add_f32 v167, v167, v159\n"
        "s_waitcnt vmcnt(6)\n"
        "s_mov_b64 exec, 1\n"
        "ds_inc_u32 v239, v239 offset:0\n"
        "s_mov_b64 exec, -1\n"
        "v_add_f32 v164, v164, v176\n"
        "v_add_f32 v165, v165, v177\n"
        "v_mfma_f32_16x16x32_bf16 v[64:67], v[172:175], v[112:115], 0\n"
        "v_add_f32 v166, v166, v178\n"
        "v_add_f32 v167, v167, v179\n"
        "v_add_f32 v164, v164, v165\n"
        "v_add_f32 v166, v166, v167\n"
        "v_add_f32 v164, v164, v166\n"
        "v_mov_b32 v165, v164\n"
        "v_mfma_f32_16x16x32_bf16 v[64:67], v[120:123], v[116:119], v[64:67]\n"
        "v_mfma_f32_16x16x32_bf16 v[64:67], v[168:171], v[128:131], v[64:67]\n"
        "v_permlane32_swap_b32 v164, v165\n"
        "v_add_f32 v164, v164, v165\n"
        "v_mov_b32 v165, v164\n"
        "v_mfma_f32_16x16x32_bf16 v[120:123], v[160:163], v[112:115], 0\n"
        "global_load_dwordx4 v[40:43], v57, s[22:23] offset:-3072\n"
        "v_permlane16_swap_b32 v164, v165\n"
        "v_add_f32 v164, v164, v165\n"
        "v_mul_f32 v164, 0xbc2aaaab, v164\n"
        "v_mul_f32 v164, s34, v164\n"
        "v_fma_f32 v136, v136, s34, v164\n"
        "v_fma_f32 v137, v137, s34, v164\n"
        "v_fma_f32 v138, v138, s34, v164\n"
        "v_fma_f32 v139, v139, s34, v164\n"
        "v_fma_f32 v140, v140, s34, v164\n"
        "v_fma_f32 v141, v141, s34, v164\n"
        "v_mfma_f32_16x16x32_bf16 v[120:123], v[148:151], v[116:119], v[120:123]\n"
        "v_fma_f32 v142, v142, s34, v164\n"
        "v_fma_f32 v143, v143, s34, v164\n"
        "v_fma_f32 v144, v144, s34, v164\n"
        "v_fma_f32 v145, v145, s34, v164\n"
        "v_fma_f32 v146, v146, s34, v164\n"
        "v_fma_f32 v147, v147, s34, v164\n"
        "v_fma_f32 v152, v152, s34, v164\n"
        "v_fma_f32 v153, v153, s34, v164\n"
        "v_fma_f32 v154, v154, s34, v164\n"
        "v_fma_f32 v155, v155, s34, v164\n"
        "v_fma_f32 v156, v156, s34, v164\n"
        "v_fma_f32 v157, v157, s34, v164\n"
        "v_fma_f32 v158, v158, s34, v164\n"
        "v_fma_f32 v159, v159, s34, v164\n"
        "v_mfma_f32_16x16x32_bf16 v[120:123], v[132:135], v[128:131], v[120:123]\n"
        "global_load_dwordx4 v[44:47], v57, s[22:23] offset:-2048\n"
        "v_fma_f32 v176, v176, s34, v164\n"
        "v_fma_f32 v177, v177, s34, v164\n"
        "v_fma_f32 v178, v178, s34, v164\n"
        "v_fma_f32 v179, v179, s34, v164\n"
        "v_cvt_pk_bf16_f32 v132, v136, v137\n"
        "v_cvt_pk_bf16_f32 v133, v138, v139\n"
        "v_cvt_pk_bf16_f32 v134, v140, v141\n"
        "v_add_f32 v148, v64, v120\n"
        "v_add_f32 v149, v65, v121\n"
        "v_add_f32 v150, v66, v122\n"
        "v_add_f32 v151, v67, v123\n"
        "v_cvt_pk_bf16_f32 v135, v142, v143\n"
        "v_mfma_f32_16x16x32_bf16 v[140:143], v[124:127], v[112:115], 0\n"
        "v_cvt_pk_bf16_f32 v136, v144, v145\n"
        "v_cvt_pk_bf16_f32 v137, v146, v147\n"
        "v_cvt_pk_bf16_f32 v138, v152, v153\n"
        "v_cvt_pk_bf16_f32 v139, v154, v155\n"
        "v_cvt_pk_bf16_f32 v124, v156, v157\n"
        "v_cvt_pk_bf16_f32 v125, v158, v159\n"
        "v_cvt_pk_bf16_f32 v126, v176, v177\n"
        "v_cvt_pk_bf16_f32 v127, v178, v179\n"
        "v_mfma_f32_16x16x32_bf16 v[140:143], v[104:107], v[116:119], v[140:143]\n"
        "global_load_dwordx4 v[0:3], v57, s[22:23] offset:-1024\n"
        "v_mfma_f32_16x16x32_bf16 v[140:143], v[92:95], v[128:131], v[140:143]\n"
        "v_mfma_f32_16x16x32_bf16 v[104:107], v[96:99], v[112:115], 0\n"
        "global_load_dwordx4 v[4:7], v57, s[22:23] offset:0\n"
        "v_mfma_f32_16x16x32_bf16 v[104:107], v[88:91], v[116:119], v[104:107]\n"
        "v_mfma_f32_16x16x32_bf16 v[104:107], v[84:87], v[128:131], v[104:107]\n"
        "global_load_dwordx4 v[8:11], v57, s[22:23] offset:1024\n"
        "v_mfma_f32_16x16x32_bf16 v[84:87], v[100:103], v[112:115], 0\n"
        "v_mfma_f32_16x16x32_bf16 v[84:87], v[108:111], v[116:119], v[84:87]\n"
        "global_load_dwordx4 v[12:15], v57, s[22:23] offset:2048\n"
        "v_add_f32 v148, v148, v140\n"
        "v_add_f32 v149, v149, v141\n"
        "v_add_f32 v150, v150, v142\n"
        "v_add_f32 v151, v151, v143\n"
        "v_add_f32 v148, v148, v104\n"
        "v_add_f32 v149, v149, v105\n"
        "v_add_f32 v150, v150, v106\n"
        "v_add_f32 v151, v151, v107\n"
        "v_mfma_f32_16x16x32_bf16 v[84:87], v[68:71], v[128:131], v[84:87]\n"
        "v_mfma_f32_16x16x32_bf16 v[88:91], v[72:75], v[112:115], 0\n"
        "global_load_dwordx4 v[16:19], v57, s[22:23] offset:3072\n"
        "v_mfma_f32_16x16x32_bf16 v[88:91], v[76:79], v[116:119], v[88:91]\n"
        "v_mfma_f32_16x16x32_bf16 v[88:91], v[80:83], v[128:131], v[88:91]\n"
        "v_mov_b32 v239, s37\n"
        "s_nop 2\n"
        "v_add_f32 v148, v148, v84\n"
        "v_add_f32 v149, v149, v85\n"
        "v_add_f32 v150, v150, v86\n"
        "v_add_f32 v151, v151, v87\n"
        "v_add_f32 v148, v148, v88\n"
        "v_add_f32 v149, v149, v89\n"
        "v_add_f32 v150, v150, v90\n"
        "v_add_f32 v151, v151, v91\n"
        "v_add_f32 v148, v148, v149\n"
        "v_add_f32 v150, v150, v151\n"
        "v_add_f32 v148, v148, v150\n"
        "v_mov_b32 v149, v148\n"
        "s_nop 1\n"
        "v_permlane32_swap_b32 v148, v149\n"
        "v_add_f32 v148, v148, v149\n"
        "v_mov_b32 v149, v148\n"
        "s_nop 1\n"
        "v_permlane16_swap_b32 v148, v149\n"
        "v_add_f32 v148, v148, v149\n"
        "v_mul_f32 v148, 0xbc2aaaab, v148\n"
        "v_mul_f32 v148, s34, v148\n"
        "v_fma_f32 v64, v64, s34, v148\n"
        "v_fma_f32 v65, v65, s34, v148\n"
        "v_fma_f32 v66, v66, s34, v148\n"
        "v_fma_f32 v67, v67, s34, v148\n"
        "v_fma_f32 v120, v120, s34, v148\n"
        "v_fma_f32 v121, v121, s34, v148\n"
        "v_fma_f32 v122, v122, s34, v148\n"
        "v_fma_f32 v123, v123, s34, v148\n"
        "v_fma_f32 v140, v140, s34, v148\n"
        "v_fma_f32 v141, v141, s34, v148\n"
        "v_fma_f32 v142, v142, s34, v148\n"
        "v_fma_f32 v143, v143, s34, v148\n"
        "v_fma_f32 v104, v104, s34, v148\n"
        "v_fma_f32 v105, v105, s34, v148\n"
        "v_fma_f32 v106, v106, s34, v148\n"
        "v_fma_f32 v107, v107, s34, v148\n"
        "v_fma_f32 v84, v84, s34, v148\n"
        "v_fma_f32 v85, v85, s34, v148\n"
        "v_fma_f32 v86, v86, s34, v148\n"
        "v_fma_f32 v87, v87, s34, v148\n"
        "v_fma_f32 v88, v88, s34, v148\n"
        "v_fma_f32 v89, v89, s34, v148\n"
        "v_fma_f32 v90, v90, s34, v148\n"
        "v_fma_f32 v91, v91, s34, v148\n"
        "v_cvt_pk_bf16_f32 v68, v64, v65\n"
        "v_cvt_pk_bf16_f32 v69, v66, v67\n"
        "v_cvt_pk_bf16_f32 v70, v120, v121\n"
        "v_cvt_pk_bf16_f32 v71, v122, v123\n"
        "v_cvt_pk_bf16_f32 v64, v140, v141\n"
        "v_cvt_pk_bf16_f32 v65, v142, v143\n"
        "v_cvt_pk_bf16_f32 v66, v104, v105\n"
        "v_cvt_pk_bf16_f32 v67, v106, v107\n"
        "v_cvt_pk_bf16_f32 v72, v84, v85\n"
        "v_cvt_pk_bf16_f32 v73, v86, v87\n"
        "v_cvt_pk_bf16_f32 v74, v88, v89\n"
        "v_cvt_pk_bf16_f32 v75, v90, v91\n"
        "s_nop 1\n"
        "v_mov_b32 v238, s36\n"
        "L64_%=:\n"
        "ds_read_b32 v237, v238 offset:0\n"
        "s_waitcnt lgkmcnt(0)\n"
        "v_readfirstlane_b32 s40, v237\n"
        "s_nop 3\n"
        "s_cmp_ge_i32 s40, 4\n"
        "s_cbranch_scc1 L65_%=\n"
        "s_nop 0\n"
        "s_sub_u32 s41, s41, 1\n"
        "s_cbranch_scc0 L64_%=\n"
        "s_endpgm\n"
        "L65_%=:\n"
        "ds_write_b128 v244, v[132:135]\n"
        "ds_write_b128 v244, v[136:139] offset:1024\n"
        "ds_write_b128 v244, v[124:127] offset:2048\n"
        "ds_write_b128 v245, v[68:71]\n"
        "ds_write_b128 v245, v[64:67] offset:1024\n"
        "ds_write_b128 v245, v[72:75] offset:2048\n"
        "s_mov_b64 exec, 1\n"
        "ds_inc_u32 v239, v239 offset:0\n"
        "s_mov_b64 exec, -1\n"
        "s_setpc_b64 s[30:31]\n"
        "PPRO_%=:\n"
        "s_add_u32 s64, s64, 128\n"
        "s_cmp_lg_u32 s6, 0\n"
        "s_cbranch_scc1 L84_%=\n"
        "s_mov_b32 exec_hi, 0\n"
        "v_lshlrev_b32 v27, 2, v56\n"
        "v_add_u32 v27, 21600, v27\n"
        "v_mov_b32 v28, 0\n"
        "ds_write_b32 v27, v28\n"
        "v_add_u32 v27, 132608, v27\n"
        "ds_write2_b32 v27, v28, v28 offset1:32\n"
        "s_mov_b64 exec, 0x1fffff\n"
        "s_bfe_u32 s7, s2, 0x50003\n"
        "s_mul_i32 s7, s7, 2656\n"
        "v_lshlrev_b32 v27, 7, v56\n"
        "v_add_u32 v27, s7, v27\n"
        "global_load_dword v252, v27, s[4:5]\n"
        "s_mov_b64 exec, -1\n"
        "L84_%=:\n"
        "s_cmp_lt_u32 s6, 2\n"
        "s_cselect_b32 s65, 3, 1\n"
        "s_cselect_b32 s61, 384, 0\n"
        "s_cselect_b32 s62, 9, 1\n"
        "s_cselect_b32 s63, 3, 4\n"
        "s_cselect_b32 s26, 3, 1\n"
        "s_cselect_b32 s34, 9, 1\n"
        "s_cselect_b32 s70, 72, 1\n"
        "s_cselect_b32 s71, 36, 4\n"
        "s_cselect_b32 s72, 6, 8\n"
        "s_mul_i32 s70, s70, 192\n"
        "s_mul_i32 s71, s71, 192\n"
        "s_mul_i32 s72, s72, 192\n"
        "s_mul_i32 s65, s65, 192\n"
        "s_mul_i32 s62, s62, 3072\n"
        "s_mul_i32 s63, s63, 1536\n"
        "s_mul_i32 s26, s26, 18432\n"
        "s_mul_i32 s34, s34, 1536\n"
        "s_add_u32 s7, s62, s63\n"
        "s_lshl_b32 s29, s63, 1\n"
        "s_add_u32 s60, s7, s63\n"
        "s_bitcmp1_b32 s6, 0\n"
        "s_cselect_b32 s28, s7, 0\n"
        "s_cselect_b32 s29, s29, s62\n"
        "s_cselect_b32 s60, s60, s63\n"
        "s_add_u32 s28, s28, s61\n"
        "s_add_u32 s29, s29, s61\n"
        "s_add_u32 s60, s60, s61\n"
        "v_add_u32 v24, 0, v56\n"
        "v_mul_u32_u24 v21, 171, v24\n"
        "v_lshrrev_b32 v21, 11, v21\n"
        "v_mad_i32_i24 v24, v21, -12, v24\n"
        "v_bfe_u32 v58, v21, 2, 2\n"
        "v_xor_b32 v58, v24, v58\n"
        "v_and_b32 v27, 3, v21\n"
        "v_mul_u32_u24 v27, s70, v27\n"
        "v_lshl_add_u32 v32, v58, 4, v27\n"
        "v_bfe_u32 v27, v21, 2, 1\n"
        "v_mad_u32_u24 v32, v27, s71, v32\n"
        "v_lshrrev_b32 v27, 3, v21\n"
        "v_mad_u32_u24 v32, v27, s72, v32\n"
        "v_and_b32 v27, 7, v21\n"
        "v_mul_u32_u24 v27, s65, v27\n"
        "v_lshl_add_u32 v58, v58, 4, v27\n"
        "v_add_u32 v25, 64, v56\n"
        "v_mul_u32_u24 v22, 171, v25\n"
        "v_lshrrev_b32 v22, 11, v22\n"
        "v_mad_i32_i24 v25, v22, -12, v25\n"
        "v_bfe_u32 v59, v22, 2, 2\n"
        "v_xor_b32 v59, v25, v59\n"
        "v_and_b32 v27, 3, v22\n"
        "v_mul_u32_u24 v27, s70, v27\n"
        "v_lshl_add_u32 v33, v59, 4, v27\n"
        "v_bfe_u32 v27, v22, 2, 1\n"
        "v_mad_u32_u24 v33, v27, s71, v33\n"
        "v_lshrrev_b32 v27, 3, v22\n"
        "v_mad_u32_u24 v33, v27, s72, v33\n"
        "v_and_b32 v27, 7, v22\n"
        "v_mul_u32_u24 v27, s65, v27\n"
        "v_lshl_add_u32 v59, v59, 4, v27\n"
        "v_lshrrev_b32 v27, 3, v22\n"
        "v_mad_u32_u24 v59, v27, s34, v59\n"
        "v_add_u32 v26, 128, v56\n"
        "v_mul_u32_u24 v23, 171, v26\n"
        "v_lshrrev_b32 v23, 11, v23\n"
        "v_mad_i32_i24 v26, v23, -12, v26\n"
        "v_bfe_u32 v60, v23, 2, 2\n"
        "v_xor_b32 v60, v26, v60\n"
        "v_and_b32 v27, 3, v23\n"
        "v_mul_u32_u24 v27, s70, v27\n"
        "v_lshl_add_u32 v34, v60, 4, v27\n"
        "v_bfe_u32 v27, v23, 2, 1\n"
        "v_mad_u32_u24 v34, v27, s71, v34\n"
        "v_lshrrev_b32 v27, 3, v23\n"
        "v_mad_u32_u24 v34, v27, s72, v34\n"
        "v_and_b32 v27, 7, v23\n"
        "v_mul_u32_u24 v27, s65, v27\n"
        "v_lshl_add_u32 v60, v60, 4, v27\n"
        "v_add_u32 v60, s34, v60\n"
        "s_and_b32 s7, s2, 7\n"
        "s_lshl_b32 s7, s7, 5\n"
        "s_lshr_b32 s14, s2, 3\n"
        "s_add_u32 s7, s7, s14\n"
        "s_and_b32 s16, s7, 255\n"
        "s_sub_u32 s7, s7, s16\n"
        "s_mul_i32 s7, s7, 20\n"
        "s_add_u32 s7, s7, s16\n"
        "s_mov_b32 s14, 22\n"
        "s_mul_i32 s15, s7, 57457\n"
        "s_lshr_b32 s15, s15, 22\n"
        "s_mov_b32 s16, s7\n"
        "s_mul_i32 s15, s15, 588672\n"
        "s_mul_i32 s7, s16, 1344\n"
        "s_add_u32 s15, s15, s7\n"
        "s_mul_i32 s74, s6, 98112\n"
        "s_add_u32 s74, s74, s15\n"
        "s_mov_b32 s36, 96768\n"
        "s_mov_b32 s40, 599187\n"
        "s_cmp_eq_u32 s6, 3\n"
        "s_cselect_b32 s7, s16, s6\n"
        "s_cselect_b32 s36, 588672, s36\n"
        "s_cselect_b32 s74, 589824, s74\n"
        "s_cselect_b32 s37, 512, 1\n"
        "s_cselect_b32 s38, 256, 4\n"
        "s_cselect_b32 s39, 7, 1\n"
        "s_cselect_b32 s40, 57457, s40\n"
        "s_mul_i32 s39, s39, 192\n"
        "s_mov_b32 s35, 50135232\n"
        "v_mov_b32 v29, 2048\n"
        "v_bfe_u32 v27, v21, 2, 1\n"
        "v_and_b32 v21, 3, v21\n"
        "v_mul_u32_u24 v21, s37, v21\n"
        "v_mad_u32_u24 v21, v27, s38, v21\n"
        "v_add_u32 v21, s7, v21\n"
        "v_and_b32 v27, 3, v24\n"
        "v_mad_u32_u24 v29, v27, 48, v29\n"
        "v_lshrrev_b32 v27, 2, v24\n"
        "v_lshl_add_u32 v29, v27, 4, v29\n"
        "v_mul_u32_u24 v27, s40, v21\n"
        "v_mul_u32_u24 v21, s39, v21\n"
        "v_lshrrev_b32 v27, 22, v27\n"
        "v_mul_u32_u24 v27, s36, v27\n"
        "v_add3_u32 v29, v29, v21, v27\n"
        "v_lshrrev_b32 v27, 3, v22\n"
        "v_xor_b32 v25, v25, v27\n"
        "v_mul_lo_u32 v30, v27, s35\n"
        "v_add_u32 v30, 1024, v30\n"
        "v_bfe_u32 v27, v22, 2, 1\n"
        "v_and_b32 v22, 3, v22\n"
        "v_mul_u32_u24 v22, s37, v22\n"
        "v_mad_u32_u24 v22, v27, s38, v22\n"
        "v_add_u32 v22, s7, v22\n"
        "v_and_b32 v27, 3, v25\n"
        "v_mad_u32_u24 v30, v27, 48, v30\n"
        "v_lshrrev_b32 v27, 2, v25\n"
        "v_lshl_add_u32 v30, v27, 4, v30\n"
        "v_mul_u32_u24 v27, s40, v22\n"
        "v_mul_u32_u24 v22, s39, v22\n"
        "v_lshrrev_b32 v27, 22, v27\n"
        "v_mul_u32_u24 v27, s36, v27\n"
        "v_add3_u32 v30, v30, v22, v27\n"
        "v_mov_b32 v31, 50135232\n"
        "v_xor_b32 v26, 1, v26\n"
        "v_bfe_u32 v27, v23, 2, 1\n"
        "v_and_b32 v23, 3, v23\n"
        "v_mul_u32_u24 v23, s37, v23\n"
        "v_mad_u32_u24 v23, v27, s38, v23\n"
        "v_add_u32 v23, s7, v23\n"
        "v_and_b32 v27, 3, v26\n"
        "v_mad_u32_u24 v31, v27, 48, v31\n"
        "v_lshrrev_b32 v27, 2, v26\n"
        "v_lshl_add_u32 v31, v27, 4, v31\n"
        "v_mul_u32_u24 v27, s40, v23\n"
        "v_mul_u32_u24 v23, s39, v23\n"
        "v_lshrrev_b32 v27, 22, v27\n"
        "v_mul_u32_u24 v27, s36, v27\n"
        "v_add3_u32 v31, v31, v23, v27\n"
        "s_mul_i32 s17, s6, 6208\n"
        "s_add_u32 s17, s17, 110176\n"
        "s_mov_b32 s18, s6\n"
        "s_add_u32 s75, s74, 100270464\n"
        "v_mov_b32 v250, 21600\n"
        "s_mov_b32 s41, 0x400000\n"
        "s_waitcnt lgkmcnt(0)\n"
        "s_cmp_lg_u32 s6, 0\n"
        "s_cbranch_scc1 L85_%=\n"
        "s_bitcmp1_b32 s2, 7\n"
        "s_cselect_b64 s[8:9], s[50:51], s[46:47]\n"
        "s_cselect_b64 s[10:11], s[52:53], s[48:49]\n"
        "s_bfe_u32 s7, s2, 0x40003\n"
        "s_cmp_ge_u32 s7, 12\n"
        "s_cselect_b64 s[8:9], s[10:11], s[8:9]\n"
        "s_cselect_b32 s10, 12, 0\n"
        "s_sub_u32 s7, s7, s10\n"
        "s_mul_i32 s7, s7, 4608\n"
        "v_lshlrev_b32 v27, 7, v56\n"
        "v_add_u32 v27, s7, v27\n"
        "s_mov_b32 exec_hi, 0xf\n"
        "global_load_dword v252, v27, s[8:9]\n"
        "s_mov_b32 exec_hi, -1\n"
        "s_mov_b64 s[24:25], s[56:57]\n"
        "s_mov_b64 exec, 1\n"
        "v_mov_b32 v27, 0\n"
        "global_load_dword v253, v27, s[24:25] offset:256 sc1\n"
        "s_mov_b64 exec, -1\n"
        "L85_%=:\n"
        "s_cmp_lt_u32 s6, 2\n"
        "s_cselect_b64 s[24:25], s[46:47], s[48:49]\n"
        "s_and_b32 s25, s25, 0xffff\n"
        "s_sub_u32 s8, s44, 2048\n"
        "s_subb_u32 s9, s45, 0\n"
        "s_and_b32 s9, s9, 0xffff\n"
        "s_mov_b32 s10, 200542976\n"
        "s_mov_b32 s11, 0x20000\n"
        "s_mul_i32 s7, s6, 7168\n"
        "s_add_u32 s7, s7, 6144\n"
        "s_add_u32 s22, s4, s7\n"
        "s_addc_u32 s23, s5, 0\n"
        "s_sub_u32 s7, s6, 0\n"
        "s_and_b32 s7, s7, 3\n"
        "s_min_u32 s7, s7, 2\n"
        "s_lshl_b32 s7, s7, 10\n"
        "s_add_u32 s73, s7, 18528\n"
        "v_add_u32 v27, s7, v57\n"
        "s_barrier\n"
        "s_mov_b32 m0, s73\n"
        "s_nop 0\n"
        "global_load_lds_dwordx4 v27, s[4:5]\n"
        "global_load_dwordx4 v[240:243], v57, s[22:23] offset:-3072\n"
        "global_load_dwordx4 v[244:247], v57, s[22:23] offset:-2048\n"
        "s_add_u32 m0, s64, 0\n"
        "s_nop 0\n"
        "buffer_load_dwordx4 v58, s[24:27], s28 offen lds\n"
        "s_add_u32 m0, s64, 1024\n"
        "s_nop 0\n"
        "buffer_load_dwordx4 v59, s[24:27], s28 offen lds\n"
        "s_add_u32 m0, s64, 2048\n"
        "s_nop 0\n"
        "buffer_load_dwordx4 v60, s[24:27], s28 offen lds\n"
        "s_add_u32 m0, s64, 3072\n"
        "s_nop 0\n"
        "buffer_load_dwordx4 v58, s[24:27], s29 offen lds\n"
        "s_add_u32 m0, s64, 4096\n"
        "s_nop 0\n"
        "buffer_load_dwordx4 v59, s[24:27], s29 offen lds\n"
        "s_add_u32 m0, s64, 5120\n"
        "s_nop 0\n"
        "buffer_load_dwordx4 v60, s[24:27], s29 offen lds\n"
        "s_add_u32 m0, s64, 6144\n"
        "s_nop 0\n"
        "buffer_load_dwordx4 v58, s[24:27], s60 offen lds\n"
        "s_add_u32 m0, s64, 7168\n"
        "s_nop 0\n"
        "buffer_load_dwordx4 v59, s[24:27], s60 offen lds\n"
        "s_add_u32 m0, s64, 8192\n"
        "s_nop 0\n"
        "buffer_load_dwordx4 v60, s[24:27], s60 offen lds\n"
        "s_cmp_lt_u32 s6, 2\n"
        "s_cselect_b32 s62, 3, 16\n"
        "s_cselect_b32 s63, 12, 32\n"
        "s_cselect_b64 s[24:25], s[50:51], s[52:53]\n"
        "s_cselect_b32 s64, 0, 117088\n"
        "s_mul_i32 s62, s62, 192\n"
        "s_mul_i32 s63, s63, 192\n"
        "s_and_b32 s25, s25, 0xffff\n"
        "s_mul_i32 s7, s6, 9216\n"
        "s_add_u32 s64, s64, s7\n"
        "s_add_u32 s64, s64, 0\n"
        "s_add_u32 s7, s62, s63\n"
        "s_lshl_b32 s29, s63, 1\n"
        "s_add_u32 s60, s7, s63\n"
        "s_bitcmp1_b32 s6, 0\n"
        "s_cselect_b32 s28, s7, 0\n"
        "s_cselect_b32 s29, s29, s62\n"
        "s_cselect_b32 s60, s60, s63\n"
        "s_add_u32 s28, s28, s61\n"
        "s_add_u32 s29, s29, s61\n"
        "s_add_u32 s60, s60, s61\n"
        "s_add_u32 m0, s64, 0\n"
        "s_nop 0\n"
        "buffer_load_dwordx4 v32, s[24:27], s28 offen lds\n"
        "s_add_u32 m0, s64, 1024\n"
        "s_nop 0\n"
        "buffer_load_dwordx4 v33, s[24:27], s28 offen lds\n"
        "s_add_u32 m0, s64, 2048\n"
        "s_nop 0\n"
        "buffer_load_dwordx4 v34, s[24:27], s28 offen lds\n"
        "s_add_u32 m0, s64, 3072\n"
        "s_nop 0\n"
        "buffer_load_dwordx4 v32, s[24:27], s29 offen lds\n"
        "s_add_u32 m0, s64, 4096\n"
        "s_nop 0\n"
        "buffer_load_dwordx4 v33, s[24:27], s29 offen lds\n"
        "s_add_u32 m0, s64, 5120\n"
        "s_nop 0\n"
        "buffer_load_dwordx4 v34, s[24:27], s29 offen lds\n"
        "s_add_u32 m0, s64, 6144\n"
        "s_nop 0\n"
        "buffer_load_dwordx4 v32, s[24:27], s60 offen lds\n"
        "s_add_u32 m0, s64, 7168\n"
        "s_nop 0\n"
        "buffer_load_dwordx4 v33, s[24:27], s60 offen lds\n"
        "s_add_u32 m0, s64, 8192\n"
        "s_nop 0\n"
        "buffer_load_dwordx4 v34, s[24:27], s60 offen lds\n"
        "s_setprio 3\n"
        "s_waitcnt vmcnt(9)\n"
        "s_mov_b64 exec, 1\n"
        "ds_inc_u32 v250, v250 offset:20\n"
        "s_mov_b64 exec, -1\n"
        "L86_%=:\n"
        "ds_read_b32 v251, v250 offset:20\n"
        "s_waitcnt lgkmcnt(0)\n"
        "v_readfirstlane_b32 s40, v251\n"
        "s_nop 3\n"
        "s_cmp_ge_i32 s40, 4\n"
        "s_cbranch_scc1 L87_%=\n"
        "s_nop 0\n"
        "s_sub_u32 s41, s41, 1\n"
        "s_cbranch_scc0 L86_%=\n"
        "s_endpgm\n"
        "L87_%=:\n"
        "s_add_u32 m0, s17, 0\n"
        "s_nop 0\n"
        "buffer_load_dwordx4 v29, s[8:11], s74 offen lds\n"
        "buffer_load_dwordx4 v30, s[8:11], s74 offen offset:1024 lds\n"
        "buffer_load_dwordx4 v31, s[8:11], s74 offen offset:2048 lds\n"
        "s_add_u32 m0, s17, 3104\n"
        "s_nop 0\n"
        "buffer_load_dwordx4 v29, s[8:11], s75 offen lds\n"
        "buffer_load_dwordx4 v30, s[8:11], s75 offen offset:1024 lds\n"
        "buffer_load_dwordx4 v31, s[8:11], s75 offen offset:2048 lds\n"
        "s_cmp_eq_u32 s6, 0\n"
        "s_cbranch_scc1 L88_%=\n"
        "s_mov_b32 s34, 0x411cc471\n"
        "s_mov_b32 s35, 21632\n"
        "s_mov_b32 s36, 21624\n"
        "s_mov_b32 s37, 21628\n"
        "s_call_b64 s[30:31], FOLDW_%=\n"
        "s_branch L89_%=\n"
        "L88_%=:\n"
        "s_waitcnt vmcnt(6)\n"
        "s_mov_b64 exec, 1\n"
        "ds_inc_u32 v250, v250 offset:32\n"
        "s_mov_b64 exec, -1\n"
        "global_load_dwordx4 v[40:43], v57, s[22:23] offset:-3072\n"
        "global_load_dwordx4 v[44:47], v57, s[22:23] offset:-2048\n"
        "global_load_dwordx4 v[0:3], v57, s[22:23] offset:-1024\n"
        "global_load_dwordx4 v[4:7], v57, s[22:23] offset:0\n"
        "global_load_dwordx4 v[8:11], v57, s[22:23] offset:1024\n"
        "global_load_dwordx4 v[12:15], v57, s[22:23] offset:2048\n"
        "global_load_dwordx4 v[16:19], v57, s[22:23] offset:3072\n"
        "L89_%=:\n"
        "s_mov_b64 s[24:25], s[56:57]\n"
        "s_setprio 3\n"
        "s_mov_b32 s44, 0xf0000f\n"
        "s_mov_b32 s45, 0xf0000f00\n"
        "s_mov_b32 s46, 0xff000ff\n"
        "s_mov_b32 s47, 0xf00fff00\n"
        "s_mov_b32 s48, 0x22222222\n"
        "s_mov_b32 s49, 0x22222222\n"
        "s_mov_b32 s50, 0x44444444\n"
        "s_mov_b32 s51, 0x44444444\n"
        "s_mov_b32 s52, 0x88888888\n"
        "s_mov_b32 s53, 0x88888888\n"
        "s_mov_b64 vcc, s[44:45]\n"
        "s_mov_b32 s28, 0x3c2aaaab\n"
        "s_mov_b32 s29, 0x3a7ba882\n"
        "s_mov_b32 s30, 0\n"
        "s_mov_b32 s34, 0\n"
        "s_cmp_eq_u32 s18, 3\n"
        "s_cselect_b32 s31, 6, s18\n"
        "s_mov_b32 s36, s18\n"
        "s_mov_b32 s37, s18\n"
        "s_add_u32 s71, s17, 3104\n"
        "s_mov_b32 s68, 4\n"
        "s_mov_b32 s63, 0x07060302\n"
        "s_mov_b32 s74, 16\n"
        "s_mov_b32 s72, -1\n"
        "s_mov_b32 s41, 0x400000\n"
        "L23_%=:\n"
        "ds_read_b32 v251, v250 offset:28\n"
        "s_waitcnt lgkmcnt(0)\n"
        "v_readfirstlane_b32 s40, v251\n"
        "s_nop 3\n"
        "s_cmp_ge_i32 s40, 6\n"
        "s_cbranch_scc1 L24_%=\n"
        "s_nop 0\n"
        "s_sub_u32 s41, s41, 1\n"
        "s_cbranch_scc0 L23_%=\n"
        "s_endpgm\n"
        "L24_%=:\n"
        "s_waitcnt vmcnt(0)\n"
        "v_readfirstlane_b32 s26, v253\n"
        "ds_read_b128 v[16:19], v16\n"
        "ds_read_b64 v[94:95], v14\n"
        "ds_read_b64 v[86:87], v14\n"
        "ds_read_b64 v[28:29], v14\n"
        "ds_read_b64 v[90:91], v14\n"
        "v_mov_b32 v102, 21600\n"
        "v_add_u32 v20, 21728, v13\n"
        "ds_read_b128 v[112:115], v20 offset:0\n"
        "ds_read_b128 v[116:119], v20 offset:1024\n"
        "ds_read_b128 v[120:123], v20 offset:2048\n"
        "ds_read_b128 v[124:127], v20 offset:3072\n"
        "ds_read_b128 v[128:131], v20 offset:4096\n"
        "ds_read_b128 v[132:135], v20 offset:5120\n"
        "ds_read_b128 v[136:139], v20 offset:6144\n"
        "ds_read_b128 v[140:143], v20 offset:7168\n"
        "ds_read_b128 v[144:147], v20 offset:8192\n"
        "ds_read_b128 v[148:151], v20 offset:9216\n"
        "ds_read_b128 v[152:155], v20 offset:10240\n"
        "ds_read_b128 v[156:159], v20 offset:11264\n"
        "ds_read_b128 v[160:163], v20 offset:12288\n"
        "ds_read_b128 v[164:167], v20 offset:13312\n"
        "ds_read_b128 v[168:171], v20 offset:14336\n"
        "ds_read_b128 v[172:175], v20 offset:15360\n"
        "ds_read_b128 v[176:179], v20 offset:16384\n"
        "ds_read_b128 v[180:183], v20 offset:17408\n"
        "ds_read_b128 v[184:187], v20 offset:18432\n"
        "ds_read_b128 v[188:191], v20 offset:19456\n"
        "ds_read_b128 v[192:195], v20 offset:20480\n"
        "ds_read_b128 v[196:199], v20 offset:21520\n"
        "ds_read_b128 v[200:203], v20 offset:22544\n"
        "ds_read_b128 v[204:207], v20 offset:23568\n"
        "ds_read_b128 v[208:211], v20 offset:24608\n"
        "ds_read_b128 v[212:215], v20 offset:25632\n"
        "ds_read_b128 v[216:219], v20 offset:26656\n"
        "ds_read_b128 v[220:223], v20 offset:27696\n"
        "ds_read_b128 v[224:227], v20 offset:28720\n"
        "ds_read_b128 v[228:231], v20 offset:29744\n"
        "ds_read_b128 v[232:235], v20 offset:30784\n"
        "ds_read_b128 v[236:239], v20 offset:31808\n"
        "ds_read_b128 v[240:243], v20 offset:32832\n"
        "ds_read_b128 v[244:247], v20 offset:33872\n"
        "ds_read_b128 v[248:251], v20 offset:34896\n"
        "ds_read_b128 v[252:255], v20 offset:35920\n"
        "ds_read_b128 v[60:63], v0 offset:0\n"
        "ds_read_b128 v[66:69], v0 offset:64\n"
        "ds_read_b128 v[72:75], v0 offset:128\n"
        "ds_read_b128 v[42:45], v0 offset:768\n"
        "ds_read_b128 v[48:51], v0 offset:832\n"
        "ds_read_b128 v[54:57], v0 offset:896\n"
        "s_add_u32 s37, s37, 4\n"
        "s_cmp_ge_u32 s37, 49\n"
        "s_cselect_b32 s69, 49, 0\n"
        "s_sub_u32 s37, s37, s69\n"
        "s_sub_u32 s69, s37, 4\n"
        "s_cmp_lt_u32 s69, 4\n"
        "s_subb_u32 s36, s36, -4\n"
        "s_cmp_ge_u32 s36, 6\n"
        "s_cselect_b32 s69, 6, 0\n"
        "s_addc_u32 s34, s34, 0\n"
        "s_sub_u32 s36, s36, s69\n"
        "s_cmp_ge_u32 s34, s74\n"
        "s_cbranch_scc0 L27_%=\n"
        "s_nop 1\n"
        "v_mov_b32 v22, 21600\n"
        "L25_%=:\n"
        "ds_read_b32 v23, v22 offset:64\n"
        "s_waitcnt lgkmcnt(0)\n"
        "v_readfirstlane_b32 s40, v23\n"
        "s_nop 3\n"
        "s_cmp_ge_i32 s40, 1\n"
        "s_cbranch_scc1 L26_%=\n"
        "s_sleep 1\n"
        "s_sub_u32 s41, s41, 1\n"
        "s_cbranch_scc0 L25_%=\n"
        "s_endpgm\n"
        "L26_%=:\n"
        "v_mbcnt_lo_u32_b32 v23, -1, 0\n"
        "v_lshl_add_u32 v22, v23, 2, v22\n"
        "ds_read_b32 v23, v22 offset:68\n"
        "s_mov_b32 s14, s40\n"
        "s_mov_b32 s74, -1\n"
        "s_waitcnt lgkmcnt(0)\n"
        "v_readlane_b32 s22, v23, 0\n"
        "v_readlane_b32 s23, v23, 1\n"
        "s_nop 3\n"
        "L27_%=:\n"
        "s_mul_i32 s38, s34, 256\n"
        "s_add_u32 s38, s38, s16\n"
        "s_cmp_ge_u32 s34, 20\n"
        "s_cselect_b32 s38, s22, s38\n"
        "s_cmp_ge_u32 s34, 21\n"
        "s_cselect_b32 s38, s23, s38\n"
        "s_mul_hi_u32 s70, s38, 58835169\n"
        "s_mul_i32 s38, s38, 1344\n"
        "s_mul_i32 s70, s70, 588672\n"
        "s_add_u32 s38, s38, s70\n"
        "s_mul_i32 s69, s36, 98304\n"
        "s_add_u32 s38, s38, s69\n"
        "s_mov_b32 s35, s36\n"
        "s_mul_i32 s69, s36, -12\n"
        "s_add_u32 s69, s69, 60\n"
        "s_lshl_b64 s[54:55], -1, s69\n"
        "s_lshl_b64 s[56:57], s[54:55], 24\n"
        "s_ashr_i64 s[60:61], s[54:55], 8\n"
        "s_mov_b32 s58, s61\n"
        "s_mov_b32 s59, s56\n"
        "v_cndmask_b32_e64 v15, v4, v7, s[56:57]\n"
        "v_cndmask_b32_e64 v100, v5, v8, s[58:59]\n"
        "v_cndmask_b32_e64 v103, v6, v9, s[60:61]\n"
        "s_cmp_eq_u32 s37, 3\n"
        "s_cbranch_scc0 L28_%=\n"
        "s_nop 1\n"
        "ds_read_b32 v15, v13 offset:19552\n"
        "ds_read_b32 v100, v13 offset:19556\n"
        "ds_read_b32 v103, v13 offset:19560\n"
        "s_mov_b32 s35, 6\n"
        "s_mov_b32 s38, 0\n"
        "s_mov_b32 s69, 1344\n"
        "s_waitcnt lgkmcnt(0)\n"
        "v_lshrrev_b32 v22, 28, v15\n"
        "v_add_u32 v22, s34, v22\n"
        "v_and_b32 v15, 0x0fffffff, v15\n"
        "v_mul_u32_u24 v23, 256, v22\n"
        "v_add_u32 v23, s16, v23\n"
        "v_cmpx_le_u32 vcc, 20, v22\n"
        "v_mov_b32 v23, s22\n"
        "s_mov_b64 exec, -1\n"
        "v_cmpx_le_u32 vcc, 21, v22\n"
        "v_mov_b32 v23, s23\n"
        "s_mov_b64 exec, -1\n"
        "v_mad_u32_u24 v15, v23, s69, v15\n"
        "v_mul_u32_u24 v23, 57457, v23\n"
        "v_lshrrev_b32 v23, 22, v23\n"
        "v_mul_u32_u24 v23, 588672, v23\n"
        "v_add_u32 v15, v15, v23\n"
        "v_lshrrev_b32 v22, 28, v100\n"
        "v_add_u32 v22, s34, v22\n"
        "v_and_b32 v100, 0x0fffffff, v100\n"
        "v_mul_u32_u24 v23, 256, v22\n"
        "v_add_u32 v23, s16, v23\n"
        "v_cmpx_le_u32 vcc, 20, v22\n"
        "v_mov_b32 v23, s22\n"
        "s_mov_b64 exec, -1\n"
        "v_cmpx_le_u32 vcc, 21, v22\n"
        "v_mov_b32 v23, s23\n"
        "s_mov_b64 exec, -1\n"
        "v_mad_u32_u24 v100, v23, s69, v100\n"
        "v_mul_u32_u24 v23, 57457, v23\n"
        "v_lshrrev_b32 v23, 22, v23\n"
        "v_mul_u32_u24 v23, 588672, v23\n"
        "v_add_u32 v100, v100, v23\n"
        "v_lshrrev_b32 v22, 28, v103\n"
        "v_add_u32 v22, s34, v22\n"
        "v_and_b32 v103, 0x0fffffff, v103\n"
        "v_mul_u32_u24 v23, 256, v22\n"
        "v_add_u32 v23, s16, v23\n"
        "v_cmpx_le_u32 vcc, 20, v22\n"
        "v_mov_b32 v23, s22\n"
        "s_mov_b64 exec, -1\n"
        "v_cmpx_le_u32 vcc, 21, v22\n"
        "v_mov_b32 v23, s23\n"
        "s_mov_b64 exec, -1\n"
        "v_mad_u32_u24 v103, v23, s69, v103\n"
        "v_mul_u32_u24 v23, 57457, v23\n"
        "v_lshrrev_b32 v23, 22, v23\n"
        "v_mul_u32_u24 v23, 588672, v23\n"
        "v_add_u32 v103, v103, v23\n"
        "s_nop 4\n"
        "s_mov_b64 vcc, s[44:45]\n"
        "L28_%=:\n"
        "s_cmp_lt_u32 s34, s14\n"
        "s_cselect_b32 s38, s38, 0x80000000\n"
        "s_add_u32 s39, s38, 100270464\n"
        "s_waitcnt lgkmcnt(0)\n"
        "s_mov_b64 exec, 1\n"
        "ds_inc_u32 v102, v0 offset:8\n"
        "s_mov_b64 exec, -1\n"
        "s_cmp_eq_u32 s18, 0\n"
        "s_cselect_b32 s73, -20, 8\n"
        "s_setprio 1\n"
        "PFST_%=:\n"
        "v_mfma_f32_16x16x32_bf16 v[34:37], v[112:115], v[60:63], 0\n"
        "s_mov_b32 m0, s17\n"
        "v_mfma_f32_16x16x32_bf16 v[34:37], v[116:119], v[66:69], v[34:37]\n"
        "v_mfma_f32_16x16x32_bf16 v[34:37], v[120:123], v[72:75], v[34:37]\n"
        "buffer_load_dwordx4 v15, s[8:11], s38 offen lds\n"
        "v_mfma_f32_16x16x32_bf16 v[38:41], v[132:135], v[72:75], 0\n"
        "v_mfma_f32_16x16x32_bf16 v[38:41], v[128:131], v[66:69], v[38:41]\n"
        "s_setprio 3\n"
        "v_mfma_f32_16x16x32_bf16 v[38:41], v[124:127], v[60:63], v[38:41]\n"
        "buffer_load_dwordx4 v100, s[8:11], s38 offen offset:1024 lds\n"
        "v_mfma_f32_16x16x32_bf16 v[78:81], v[136:139], v[60:63], 0\n"
        "v_mfma_f32_16x16x32_bf16 v[78:81], v[140:143], v[66:69], v[78:81]\n"
        "v_mfma_f32_16x16x32_bf16 v[78:81], v[144:147], v[72:75], v[78:81]\n"
        "v_mfma_f32_16x16x32_bf16 v[82:85], v[156:159], v[72:75], 0\n"
        "v_mfma_f32_16x16x32_bf16 v[82:85], v[152:155], v[66:69], v[82:85]\n"
        "v_mfma_f32_16x16x32_bf16 v[82:85], v[148:151], v[60:63], v[82:85]\n"
        "buffer_load_dwordx4 v103, s[8:11], s38 offen offset:2048 lds\n"
        "s_mov_b32 m0, s71\n"
        "s_cmp_eq_u32 s72, s30\n"
        "v_mfma_f32_16x16x32_bf16 v[104:107], v[160:163], v[60:63], 0\n"
        "s_setprio 3\n"
        "s_cbranch_scc1 F5_%=\n"
        "s_setprio 1\n"
        "F5_%=:\n"
        "v_mfma_f32_16x16x32_bf16 v[104:107], v[164:167], v[66:69], v[104:107]\n"
        "v_mfma_f32_16x16x32_bf16 v[104:107], v[168:171], v[72:75], v[104:107]\n"
        "buffer_load_dwordx4 v15, s[8:11], s39 offen lds\n"
        "v_mfma_f32_16x16x32_bf16 v[108:111], v[180:183], v[72:75], 0\n"
        "v_mfma_f32_16x16x32_bf16 v[108:111], v[176:179], v[66:69], v[108:111]\n"
        "v_mfma_f32_16x16x32_bf16 v[108:111], v[172:175], v[60:63], v[108:111]\n"
        "buffer_load_dwordx4 v100, s[8:11], s39 offen offset:1024 lds\n"
        "v_mfma_f32_16x16x32_bf16 v[20:23], v[60:63], v[60:63], 0\n"
        "v_mfma_f32_16x16x32_bf16 v[20:23], v[66:69], v[66:69], v[20:23]\n"
        "v_mfma_f32_16x16x32_bf16 v[20:23], v[72:75], v[72:75], v[20:23]\n"
        "buffer_load_dwordx4 v103, s[8:11], s39 offen offset:2048 lds\n"
        "v_mov_b64 v[96:97], 0\n"
        "v_mov_b64 v[98:99], 0\n"
        "v_mfma_f32_16x16x32_bf16 v[24:27], v[42:45], v[42:45], 0\n"
        "v_mfma_f32_16x16x32_bf16 v[24:27], v[48:51], v[48:51], v[24:27]\n"
        "v_mfma_f32_16x16x32_bf16 v[24:27], v[54:57], v[54:57], v[24:27]\n"
        "v_smfmac_f32_16x16x64_bf16 v[96:99], v[60:63], v[34:41], v10\n"
        "s_nop 0\n"
        "v_cndmask_b32_e64 v93, v20, v21, s[48:49]\n"
        "v_smfmac_f32_16x16x64_bf16 v[96:99], v[66:69], v[78:85], v10\n"
        "v_cndmask_b32_e64 v93, v93, v22, s[50:51]\n"
        "v_smfmac_f32_16x16x64_bf16 v[96:99], v[72:75], v[104:111], v10\n"
        "v_mfma_f32_16x16x32_bf16 v[34:37], v[112:115], v[42:45], 0\n"
        "s_nop 0\n"
        "v_cndmask_b32_e64 v111, v24, v25, s[48:49]\n"
        "v_cndmask_b32_e64 v111, v111, v26, s[50:51]\n"
        "v_mfma_f32_16x16x32_bf16 v[34:37], v[116:119], v[48:51], v[34:37]\n"
        "s_and_b32 s69, s30, 1\n"
        "v_cndmask_b32_e64 v93, v93, v23, s[52:53]\n"
        "v_cndmask_b32_e64 v111, v111, v27, s[52:53]\n"
        "v_mfma_f32_16x16x32_bf16 v[34:37], v[120:123], v[54:57], v[34:37]\n"
        "s_lshl2_add_u32 s70, s69, 21600\n"
        "v_mov_b32 v102, s70\n"
        "v_cndmask_b32_dpp v93, v111, v93, vcc row_ror:4 row_mask:0xf bank_mask:0xf\n"
        "v_mfma_f32_16x16x32_bf16 v[38:41], v[132:135], v[54:57], 0\n"
        "s_mul_i32 s69, s69, 40064\n"
        "s_mul_i32 s70, s31, 1664\n"
        "v_mfma_f32_16x16x32_bf16 v[38:41], v[128:131], v[48:51], v[38:41]\n"
        "s_add_u32 s69, s69, s70\n"
        "v_add_u32 v11, s69, v3\n"
        "v_mfma_f32_16x16x32_bf16 v[38:41], v[124:127], v[42:45], v[38:41]\n"
        "v_add_u32 v12, s69, v2\n"
        "s_lshr_b32 s68, s30, 1\n"
        "v_mfma_f32_16x16x32_bf16 v[104:107], v[136:139], v[42:45], 0\n"
        "s_lshl2_add_u32 s68, s68, 4\n"
        "s_cmp_eq_u32 s31, 6\n"
        "s_cbranch_scc1 F8_%=\n"
        "F9_%=:\n"
        "v_mfma_f32_16x16x32_bf16 v[104:107], v[140:143], v[48:51], v[104:107]\n"
        "s_mov_b32 s72, s30\n"
        "s_mov_b32 s30, s34\n"
        "v_mfma_f32_16x16x32_bf16 v[104:107], v[144:147], v[54:57], v[104:107]\n"
        "s_mov_b32 s31, s35\n"
        "s_add_u32 s37, s37, 4\n"
        "v_mfma_f32_16x16x32_bf16 v[108:111], v[156:159], v[54:57], 0\n"
        "s_cmp_ge_u32 s37, 49\n"
        "s_cselect_b32 s69, 49, 0\n"
        "v_mfma_f32_16x16x32_bf16 v[108:111], v[152:155], v[48:51], v[108:111]\n"
        "s_sub_u32 s37, s37, s69\n"
        "s_sub_u32 s69, s37, 4\n"
        "s_cmp_lt_u32 s69, 4\n"
        "v_mfma_f32_16x16x32_bf16 v[108:111], v[148:151], v[42:45], v[108:111]\n"
        "s_subb_u32 s36, s36, -4\n"
        "s_cmp_ge_u32 s36, 6\n"
        "v_mfma_f32_16x16x32_bf16 v[76:79], v[160:163], v[42:45], 0\n"
        "s_cselect_b32 s69, 6, 0\n"
        "s_addc_u32 s34, s34, 0\n"
        "v_mfma_f32_16x16x32_bf16 v[76:79], v[164:167], v[48:51], v[76:79]\n"
        "s_sub_u32 s36, s36, s69\n"
        "s_cmp_ge_u32 s34, s74\n"
        "v_mfma_f32_16x16x32_bf16 v[76:79], v[168:171], v[54:57], v[76:79]\n"
        "s_cbranch_scc1 F10_%=\n"
        "F11_%=:\n"
        "s_mul_i32 s38, s34, 256\n"
        "v_mfma_f32_16x16x32_bf16 v[80:83], v[180:183], v[54:57], 0\n"
        "s_add_u32 s38, s38, s16\n"
        "s_cmp_ge_u32 s34, 20\n"
        "s_cselect_b32 s38, s22, s38\n"
        "v_mfma_f32_16x16x32_bf16 v[80:83], v[176:179], v[48:51], v[80:83]\n"
        "s_cmp_ge_u32 s34, 21\n"
        "s_cselect_b32 s38, s23, s38\n"
        "v_mfma_f32_16x16x32_bf16 v[80:83], v[172:175], v[42:45], v[80:83]\n"
        "s_mul_hi_u32 s70, s38, 58835169\n"
        "s_mul_i32 s38, s38, 1344\n"
        "v_mfma_f32_16x16x32_bf16 v[20:23], v[16:19], v[60:63], 0\n"
        "s_mul_i32 s70, s70, 588672\n"
        "s_add_u32 s38, s38, s70\n"
        "v_mfma_f32_16x16x32_bf16 v[20:23], v[16:19], v[66:69], v[20:23]\n"
        "s_mul_i32 s69, s36, 98304\n"
        "s_add_u32 s38, s38, s69\n"
        "s_mov_b32 s35, s36\n"
        "v_mfma_f32_16x16x32_bf16 v[20:23], v[16:19], v[72:75], v[20:23]\n"
        "s_mul_i32 s69, s36, -12\n"
        "v_mov_b64 v[30:31], 0\n"
        "v_mov_b64 v[32:33], 0\n"
        "v_mfma_f32_16x16x32_bf16 v[24:27], v[16:19], v[42:45], 0\n"
        "s_add_u32 s69, s69, 60\n"
        "s_lshl_b64 s[54:55], -1, s69\n"
        "s_lshl_b64 s[56:57], s[54:55], 24\n"
        "v_mfma_f32_16x16x32_bf16 v[24:27], v[16:19], v[48:51], v[24:27]\n"
        "s_ashr_i64 s[60:61], s[54:55], 8\n"
        "s_mov_b32 s58, s61\n"
        "v_mfma_f32_16x16x32_bf16 v[24:27], v[16:19], v[54:57], v[24:27]\n"
        "s_mov_b32 s59, s56\n"
        "v_cndmask_b32_e64 v15, v4, v7, s[56:57]\n"
        "v_smfmac_f32_16x16x64_bf16 v[30:33], v[42:45], v[34:41], v10\n"
        "v_cndmask_b32_e64 v100, v5, v8, s[58:59]\n"
        "v_smfmac_f32_16x16x64_bf16 v[30:33], v[48:51], v[104:111], v10\n"
        "v_cndmask_b32_e64 v103, v6, v9, s[60:61]\n"
        "v_smfmac_f32_16x16x64_bf16 v[30:33], v[54:57], v[76:83], v10\n"
        "s_cmp_eq_u32 s37, 3\n"
        "s_cbranch_scc1 F12_%=\n"
        "F13_%=:\n"
        "v_cndmask_b32_dpp v110, v24, v20, vcc row_ror:4 row_mask:0xf bank_mask:0xf\n"
        "v_mfma_f32_16x16x32_bf16 v[36:39], v[54:57], v[192:195], 0\n"
        "s_cmp_lt_u32 s34, s14\n"
        "v_mul_f32 v111, s28, v110\n"
        "v_fma_f32 v111, -v111, v110, v93\n"
        "v_mfma_f32_16x16x32_bf16 v[36:39], v[48:51], v[188:191], v[36:39]\n"
        "s_cselect_b32 s38, s38, 0x80000000\n"
        "s_add_u32 s39, s38, 100270464\n"
        "v_add_f32_e64 v111, s29, |v111|\n"
        "v_mfma_f32_16x16x32_bf16 v[36:39], v[42:45], v[184:187], v[36:39]\n"
        "v_rsq_f32 v84, v111\n"
        "v_cndmask_b32_dpp v104, v30, v96, vcc row_ror:4 row_mask:0xf bank_mask:0xf\n"
        "v_mfma_f32_16x16x32_bf16 v[24:27], v[72:75], v[192:195], 0\n"
        "v_cndmask_b32_e64 v84, 0, v84, s[46:47]\n"
        "v_cndmask_b32_dpp v105, v31, v97, vcc row_ror:4 row_mask:0xf bank_mask:0xf\n"
        "v_mfma_f32_16x16x32_bf16 v[24:27], v[66:69], v[188:191], v[24:27]\n"
        "v_cndmask_b32_dpp v85, v32, v98, vcc row_ror:4 row_mask:0xf bank_mask:0xf\n"
        "v_mul_f32_dpp v104, v84, v104 quad_perm:[0,0,0,0] row_mask:0xf bank_mask:0xf\n"
        "v_mfma_f32_16x16x32_bf16 v[24:27], v[60:63], v[184:187], v[24:27]\n"
        "v_mul_f32_dpp v105, v84, v105 quad_perm:[1,1,1,1] row_mask:0xf bank_mask:0xf\n"
        "v_mul_f32_dpp v85, v84, v85 quad_perm:[2,2,2,2] row_mask:0xf bank_mask:0xf\n"
        "v_mfma_f32_16x16x32_bf16 v[20:23], v[60:63], v[220:223], 0\n"
        "v_cndmask_b32_dpp v106, v33, v99, vcc row_ror:4 row_mask:0xf bank_mask:0xf\n"
        "v_max3_f32 v107, v104, v105, v85\n"
        "v_mfma_f32_16x16x32_bf16 v[20:23], v[66:69], v[224:227], v[20:23]\n"
        "v_mul_f32_dpp v106, v84, v106 quad_perm:[3,3,3,3] row_mask:0xf bank_mask:0xf\n"
        "v_max_f32 v107, v107, v106\n"
        "v_mfma_f32_16x16x32_bf16 v[20:23], v[72:75], v[228:231], v[20:23]\n"
        "v_mul_f32 v107, v107, v84\n"
        "v_fma_f32 v104, v104, v84, -v107\n"
        "v_fma_f32 v105, v105, v84, -v107\n"
        "v_mfma_f32_16x16x32_bf16 v[32:35], v[42:45], v[220:223], 0\n"
        "v_fma_f32 v85, v85, v84, -v107\n"
        "v_fma_f32 v106, v106, v84, -v107\n"
        "v_mfma_f32_16x16x32_bf16 v[32:35], v[48:51], v[224:227], v[32:35]\n"
        "v_exp_f32 v104, v104\n"
        "v_mfma_f32_16x16x32_bf16 v[32:35], v[54:57], v[228:231], v[32:35]\n"
        "v_exp_f32 v105, v105\n"
        "v_mfma_f32_16x16x32_bf16 v[108:111], v[54:57], v[204:207], 0\n"
        "v_exp_f32 v85, v85\n"
        "v_mfma_f32_16x16x32_bf16 v[108:111], v[48:51], v[200:203], v[108:111]\n"
        "v_exp_f32 v106, v106\n"
        "v_mfma_f32_16x16x32_bf16 v[108:111], v[42:45], v[196:199], v[108:111]\n"
        "v_add_f32 v107, v104, v105\n"
        "v_add_f32 v101, v85, v106\n"
        "v_add_f32 v107, v107, v101\n"
        "v_mfma_f32_16x16x32_bf16 v[80:83], v[72:75], v[204:207], 0\n"
        "v_rcp_f32 v107, v107\n"
        "v_mfma_f32_16x16x32_bf16 v[80:83], v[66:69], v[200:203], v[80:83]\n"
        "v_mul_f32_dpp v104, v84, v104 quad_perm:[0,0,0,0] row_mask:0xf bank_mask:0xf\n"
        "v_mul_f32_dpp v105, v84, v105 quad_perm:[1,1,1,1] row_mask:0xf bank_mask:0xf\n"
        "v_mfma_f32_16x16x32_bf16 v[80:83], v[60:63], v[196:199], v[80:83]\n"
        "v_mul_f32_dpp v85, v84, v85 quad_perm:[2,2,2,2] row_mask:0xf bank_mask:0xf\n"
        "v_mul_f32_dpp v106, v84, v106 quad_perm:[3,3,3,3] row_mask:0xf bank_mask:0xf\n"
        "v_mul_f32 v104, v104, v107\n"
        "v_mfma_f32_16x16x32_bf16 v[76:79], v[60:63], v[232:235], 0\n"
        "v_mul_f32 v105, v105, v107\n"
        "v_mul_f32 v85, v85, v107\n"
        "v_mfma_f32_16x16x32_bf16 v[76:79], v[66:69], v[236:239], v[76:79]\n"
        "v_mul_f32 v106, v106, v107\n"
        "v_cvt_pk_bf16_f32 v99, v104, v105\n"
        "v_mfma_f32_16x16x32_bf16 v[76:79], v[72:75], v[240:243], v[76:79]\n"
        "v_cvt_pk_bf16_f32 v101, v85, v106\n"
        "v_and_b32 v58, v1, v99\n"
        "v_and_b32 v59, v1, v101\n"
        "v_mfma_f32_16x16x32_bf16 v[104:107], v[42:45], v[232:235], 0\n"
        "v_cvt_pk_bf16_f32 v92, v24, v25\n"
        "v_cvt_pk_bf16_f32 v93, v26, v27\n"
        "v_mfma_f32_16x16x32_bf16 v[104:107], v[48:51], v[236:239], v[104:107]\n"
        "v_and_b32_dpp v40, v99, v1 row_ror:12 row_mask:0xf bank_mask:0xf\n"
        "v_and_b32_dpp v41, v101, v1 row_ror:12 row_mask:0xf bank_mask:0xf\n"
        "v_mfma_f32_16x16x32_bf16 v[104:107], v[54:57], v[240:243], v[104:107]\n"
        "v_cvt_pk_bf16_f32 v84, v36, v37\n"
        "v_cvt_pk_bf16_f32 v85, v38, v39\n"
        "v_mov_b64 v[70:71], v[58:59]\n"
        "v_mfma_f32_16x16x32_bf16 v[36:39], v[92:95], v[58:61], 0\n"
        "v_cvt_pk_bf16_f32 v96, v20, v21\n"
        "v_cvt_pk_bf16_f32 v97, v22, v23\n"
        "v_mfma_f32_16x16x32_bf16 v[22:25], v[84:87], v[40:43], 0\n"
        "v_mov_b64 v[52:53], v[40:41]\n"
        "v_cvt_pk_bf16_f32 v88, v32, v33\n"
        "v_cvt_pk_bf16_f32 v89, v34, v35\n"
        "v_mfma_f32_16x16x32_bf16 v[32:35], v[94:97], v[68:71], 0\n"
        "v_mov_b64 v[64:65], v[58:59]\n"
        "v_cvt_pk_bf16_f32 v30, v80, v81\n"
        "v_mfma_f32_16x16x32_bf16 v[96:99], v[86:89], v[50:53], 0\n"
        "v_cvt_pk_bf16_f32 v31, v82, v83\n"
        "v_mov_b64 v[46:47], v[40:41]\n"
        "v_mfma_f32_16x16x32_bf16 v[80:83], v[54:57], v[216:219], 0\n"
        "v_cvt_pk_bf16_f32 v92, v108, v109\n"
        "v_cvt_pk_bf16_f32 v93, v110, v111\n"
        "v_mfma_f32_16x16x32_bf16 v[80:83], v[48:51], v[212:215], v[80:83]\n"
        "v_cvt_pk_bf16_f32 v26, v76, v77\n"
        "v_cvt_pk_bf16_f32 v27, v78, v79\n"
        "v_mfma_f32_16x16x32_bf16 v[80:83], v[42:45], v[208:211], v[80:83]\n"
        "v_cvt_pk_bf16_f32 v88, v104, v105\n"
        "v_cvt_pk_bf16_f32 v89, v106, v107\n"
        "v_mfma_f32_16x16x32_bf16 v[108:111], v[72:75], v[216:219], 0\n"
        "v_mov_b64 v[76:77], v[58:59]\n"
        "v_mov_b64 v[58:59], v[40:41]\n"
        "v_mfma_f32_16x16x32_bf16 v[108:111], v[66:69], v[212:215], v[108:111]\n"
        "v_cvt_pk_bf16_f32 v104, v36, v37\n"
        "v_cvt_pk_bf16_f32 v105, v38, v39\n"
        "v_mfma_f32_16x16x32_bf16 v[108:111], v[60:63], v[208:211], v[108:111]\n"
        "v_cvt_pk_bf16_f32 v84, v80, v81\n"
        "v_cvt_pk_bf16_f32 v85, v82, v83\n"
        "v_mfma_f32_16x16x32_bf16 v[80:83], v[28:31], v[62:65], 0\n"
        "v_cvt_pk_bf16_f32 v106, v32, v33\n"
        "v_cvt_pk_bf16_f32 v107, v34, v35\n"
        "v_mfma_f32_16x16x32_bf16 v[32:35], v[90:93], v[44:47], 0\n"
        "v_cvt_pk_bf16_f32 v20, v22, v23\n"
        "v_cvt_pk_bf16_f32 v21, v24, v25\n"
        "v_mfma_f32_16x16x32_bf16 v[38:41], v[60:63], v[244:247], 0\n"
        "v_cvt_pk_bf16_f32 v92, v108, v109\n"
        "v_cvt_pk_bf16_f32 v93, v110, v111\n"
        "v_mfma_f32_16x16x32_bf16 v[38:41], v[66:69], v[248:251], v[38:41]\n"
        "v_cvt_pk_bf16_f32 v108, v80, v81\n"
        "v_cvt_pk_bf16_f32 v109, v82, v83\n"
        "v_mfma_f32_16x16x32_bf16 v[38:41], v[72:75], v[252:255], v[38:41]\n"
        "v_cvt_pk_bf16_f32 v22, v96, v97\n"
        "v_cvt_pk_bf16_f32 v23, v98, v99\n"
        "v_mfma_f32_16x16x32_bf16 v[80:83], v[42:45], v[244:247], 0\n"
        "v_cvt_pk_bf16_f32 v24, v32, v33\n"
        "v_cvt_pk_bf16_f32 v25, v34, v35\n"
        "v_mfma_f32_16x16x32_bf16 v[80:83], v[48:51], v[248:251], v[80:83]\n"
        "v_mfma_f32_16x16x32_bf16 v[80:83], v[54:57], v[252:255], v[80:83]\n"
        "v_mfma_f32_16x16x32_bf16 v[34:37], v[26:29], v[70:73], 0\n"
        "v_cvt_pk_bf16_f32 v96, v38, v39\n"
        "v_cvt_pk_bf16_f32 v97, v40, v41\n"
        "v_mfma_f32_16x16x32_bf16 v[38:41], v[88:91], v[52:55], 0\n"
        "v_mfma_f32_16x16x32_bf16 v[30:33], v[92:95], v[64:67], 0\n"
        "s_waitcnt vmcnt(0)\n"
        "ds_read_b128 v[60:63], v0\n"
        "ds_read_b128 v[42:45], v0 offset:768\n"
        "ds_read_b128 v[66:69], v0 offset:64\n"
        "v_cvt_pk_bf16_f32 v88, v80, v81\n"
        "v_mfma_f32_16x16x32_bf16 v[78:81], v[84:87], v[46:49], 0\n"
        "s_add_u32 s73, s73, 1\n"
        "ds_read_b128 v[48:51], v0 offset:832\n"
        "v_cvt_pk_bf16_f32 v89, v82, v83\n"
        "v_cvt_pk_bf16_f32 v110, v34, v35\n"
        "v_cvt_pk_bf16_f32 v111, v36, v37\n"
        "v_mfma_f32_16x16x32_bf16 v[82:85], v[94:97], v[74:77], 0\n"
        "s_cmp_lt_u32 s73, 4\n"
        "ds_read_b128 v[72:75], v0 offset:128\n"
        "v_cvt_pk_bf16_f32 v96, v30, v31\n"
        "v_cvt_pk_bf16_f32 v97, v32, v33\n"
        "v_cvt_pk_bf16_f32 v26, v38, v39\n"
        "v_mfma_f32_16x16x32_bf16 v[34:37], v[86:89], v[56:59], 0\n"
        "s_cbranch_scc1 F19_%=\n"
        "F20_%=:\n"
        "ds_read_b128 v[54:57], v0 offset:896\n"
        "v_cvt_pk_bf16_f32 v98, v82, v83\n"
        "v_cvt_pk_bf16_f32 v99, v84, v85\n"
        "v_cvt_pk_bf16_f32 v27, v40, v41\n"
        "v_mfma_f32_16x16x32_bf16 v[82:85], v[104:107], v[104:107], 0\n"
        "v_cvt_pk_bf16_f32 v30, v78, v79\n"
        "v_cvt_pk_bf16_f32 v31, v80, v81\n"
        "v_mfma_f32_16x16x32_bf16 v[82:85], v[108:111], v[108:111], v[82:85]\n"
        "v_cvt_pk_bf16_f32 v32, v34, v35\n"
        "v_cvt_pk_bf16_f32 v33, v36, v37\n"
        "v_mfma_f32_16x16x32_bf16 v[82:85], v[96:99], v[96:99], v[82:85]\n"
        "v_mfma_f32_16x16x32_bf16 v[76:79], v[20:23], v[20:23], 0\n"
        "v_mfma_f32_16x16x32_bf16 v[76:79], v[24:27], v[24:27], v[76:79]\n"
        "v_mfma_f32_16x16x32_bf16 v[76:79], v[30:33], v[30:33], v[76:79]\n"
        "s_cmp_lt_u32 s30, s14\n"
        "s_cmp_lg_u32 s72, 0\n"
        "s_cbranch_scc1 PTOP_%=\n"
        "s_setprio 3\n"
        "s_nop 7\n"
        "F21_%=:\n"
        "ds_read_b32 v101, v102 offset:8\n"
        "s_waitcnt lgkmcnt(0)\n"
        "v_readfirstlane_b32 s40, v101\n"
        "s_nop 3\n"
        "s_cmp_ge_i32 s40, s68\n"
        "s_cbranch_scc1 F22_%=\n"
        "s_sleep 1\n"
        "s_sub_u32 s41, s41, 1\n"
        "s_cbranch_scc0 F21_%=\n"
        "s_endpgm\n"
        "F22_%=:\n"
        "v_cndmask_b32_e64 v101, v82, v83, s[48:49]\n"
        "v_cndmask_b32_e64 v93, v76, v77, s[48:49]\n"
        "v_cndmask_b32_e64 v101, v101, v84, s[50:51]\n"
        "v_cndmask_b32_e64 v93, v93, v78, s[50:51]\n"
        "v_cndmask_b32_e64 v101, v101, v85, s[52:53]\n"
        "v_cndmask_b32_e64 v93, v93, v79, s[52:53]\n"
        "ds_write_b128 v11, v[104:107]\n"
        "ds_write_b128 v11, v[20:23] offset:832\n"
        "v_cndmask_b32_dpp v101, v93, v101, vcc row_ror:4 row_mask:0xf bank_mask:0xf\n"
        "ds_write_b128 v11, v[108:111] offset:64\n"
        "ds_write_b128 v11, v[24:27] offset:896\n"
        "ds_write_b128 v11, v[96:99] offset:128\n"
        "ds_write_b128 v11, v[30:33] offset:960\n"
        "s_mov_b64 exec, s[46:47]\n"
        "ds_write_b32 v12, v101\n"
        "s_mov_b64 exec, -1\n"
        "s_mov_b64 exec, 2\n"
        "ds_inc_u32 v102, v0 offset:0\n"
        "s_mov_b64 exec, -1\n"
        "s_setprio 3\n"
        "s_waitcnt lgkmcnt(0)\n"
        "s_branch PFST_%=\n"
        ".p2align 6\n"
        "s_nop 0\n"
        "s_nop 0\n"
        "s_nop 0\n"
        "s_nop 0\n"
        "PTOP_%=:\n"
        "s_waitcnt lgkmcnt(5)\n"
        "v_mfma_f32_16x16x32_bf16 v[34:37], v[112:115], v[60:63], 0\n"
        "s_mov_b32 m0, s17\n"
        "v_cndmask_b32_e64 v88, v82, v83, s[48:49]\n"
        "s_nop 1\n"
        "v_cndmask_b32_e64 v89, v76, v77, s[48:49]\n"
        "s_waitcnt lgkmcnt(3)\n"
        "v_mfma_f32_16x16x32_bf16 v[34:37], v[116:119], v[66:69], v[34:37]\n"
        "ds_read_b32 v101, v102 offset:8\n"
        "v_cndmask_b32_e64 v88, v88, v84, s[50:51]\n"
        "s_waitcnt lgkmcnt(2)\n"
        "v_mfma_f32_16x16x32_bf16 v[34:37], v[120:123], v[72:75], v[34:37]\n"
        "s_waitcnt lgkmcnt(1)\n"
        "buffer_load_dwordx4 v15, s[8:11], s38 offen lds\n"
        "v_mfma_f32_16x16x32_bf16 v[38:41], v[132:135], v[72:75], 0\n"
        "v_cndmask_b32_e64 v89, v89, v78, s[50:51]\n"
        "v_cndmask_b32_e64 v88, v88, v85, s[52:53]\n"
        "v_mfma_f32_16x16x32_bf16 v[38:41], v[128:131], v[66:69], v[38:41]\n"
        "s_setprio 3 \n"
        "v_cndmask_b32_e64 v89, v89, v79, s[52:53]\n"
        "v_mfma_f32_16x16x32_bf16 v[38:41], v[124:127], v[60:63], v[38:41]\n"
        "buffer_load_dwordx4 v100, s[8:11], s38 offen offset:1024 lds\n"
        "v_mfma_f32_16x16x32_bf16 v[78:81], v[136:139], v[60:63], 0\n"
        "s_waitcnt lgkmcnt(0)\n"
        "v_readfirstlane_b32 s40, v101\n"
        "v_cndmask_b32_dpp v88, v89, v88, vcc row_ror:4 row_mask:0xf bank_mask:0xf\n"
        "v_mfma_f32_16x16x32_bf16 v[78:81], v[140:143], v[66:69], v[78:81]\n"
        "s_cmp_lt_i32 s40, s68\n"
        "v_mfma_f32_16x16x32_bf16 v[78:81], v[144:147], v[72:75], v[78:81]\n"
        "s_cbranch_scc1 L3_%=\n"
        "L4_%=:\n"
        "ds_write_b128 v11, v[104:107]\n"
        "s_mov_b64 exec, s[46:47]\n"
        "ds_write_b32 v12, v88\n"
        "s_mov_b64 exec, -1\n"
        "v_mfma_f32_16x16x32_bf16 v[82:85], v[156:159], v[72:75], 0\n"
        "v_mfma_f32_16x16x32_bf16 v[82:85], v[152:155], v[66:69], v[82:85]\n"
        "v_mfma_f32_16x16x32_bf16 v[82:85], v[148:151], v[60:63], v[82:85]\n"
        "buffer_load_dwordx4 v103, s[8:11], s38 offen offset:2048 lds\n"
        "s_mov_b32 m0, s71\n"
        "ds_write_b128 v11, v[20:23] offset:832\n"
        "s_cmp_eq_u32 s72, s30\n"
        "v_mfma_f32_16x16x32_bf16 v[104:107], v[160:163], v[60:63], 0\n"
        "s_setprio 3\n"
        "s_cbranch_scc1 L5_%=\n"
        "s_setprio 1\n"
        "L5_%=:\n"
        "v_mfma_f32_16x16x32_bf16 v[104:107], v[164:167], v[66:69], v[104:107]\n"
        "v_mfma_f32_16x16x32_bf16 v[104:107], v[168:171], v[72:75], v[104:107]\n"
        "buffer_load_dwordx4 v15, s[8:11], s39 offen lds\n"
        "ds_write_b128 v11, v[108:111] offset:64\n"
        "v_mfma_f32_16x16x32_bf16 v[108:111], v[180:183], v[72:75], 0\n"
        "v_mfma_f32_16x16x32_bf16 v[108:111], v[176:179], v[66:69], v[108:111]\n"
        "v_mfma_f32_16x16x32_bf16 v[108:111], v[172:175], v[60:63], v[108:111]\n"
        "buffer_load_dwordx4 v100, s[8:11], s39 offen offset:1024 lds\n"
        "ds_write_b128 v11, v[24:27] offset:896\n"
        "v_mfma_f32_16x16x32_bf16 v[20:23], v[60:63], v[60:63], 0\n"
        "v_mfma_f32_16x16x32_bf16 v[20:23], v[66:69], v[66:69], v[20:23]\n"
        "v_mfma_f32_16x16x32_bf16 v[20:23], v[72:75], v[72:75], v[20:23]\n"
        "buffer_load_dwordx4 v103, s[8:11], s39 offen offset:2048 lds\n"
        "ds_write_b128 v11, v[96:99] offset:128\n"
        "v_mov_b64 v[96:97], 0\n"
        "v_mov_b64 v[98:99], 0\n"
        "v_mfma_f32_16x16x32_bf16 v[24:27], v[42:45], v[42:45], 0\n"
        "v_mfma_f32_16x16x32_bf16 v[24:27], v[48:51], v[48:51], v[24:27]\n"
        "v_mfma_f32_16x16x32_bf16 v[24:27], v[54:57], v[54:57], v[24:27]\n"
        "ds_write_b128 v11, v[30:33] offset:960\n"
        "v_smfmac_f32_16x16x64_bf16 v[96:99], v[60:63], v[34:41], v10\n"
        "v_cndmask_b32_e64 v93, v20, v21, s[48:49]\n"
        "v_smfmac_f32_16x16x64_bf16 v[96:99], v[66:69], v[78:85], v10\n"
        "v_cndmask_b32_e64 v93, v93, v22, s[50:51]\n"
        "v_smfmac_f32_16x16x64_bf16 v[96:99], v[72:75], v[104:111], v10\n"
        "s_mov_b64 exec, 2\n"
        "ds_inc_u32 v102, v0 offset:0\n"
        "s_mov_b64 exec, -1\n"
        "v_mfma_f32_16x16x32_bf16 v[34:37], v[112:115], v[42:45], 0\n"
        "v_cndmask_b32_e64 v111, v24, v25, s[48:49]\n"
        "v_cndmask_b32_e64 v111, v111, v26, s[50:51]\n"
        "v_mfma_f32_16x16x32_bf16 v[34:37], v[116:119], v[48:51], v[34:37]\n"
        "s_and_b32 s69, s30, 1\n"
        "v_cndmask_b32_e64 v93, v93, v23, s[52:53]\n"
        "v_cndmask_b32_e64 v111, v111, v27, s[52:53]\n"
        "v_mfma_f32_16x16x32_bf16 v[34:37], v[120:123], v[54:57], v[34:37]\n"
        "s_lshl2_add_u32 s70, s69, 21600\n"
        "v_mov_b32 v102, s70\n"
        "v_cndmask_b32_dpp v93, v111, v93, vcc row_ror:4 row_mask:0xf bank_mask:0xf\n"
        "v_mfma_f32_16x16x32_bf16 v[38:41], v[132:135], v[54:57], 0\n"
        "s_mul_i32 s69, s69, 40064\n"
        "s_mul_i32 s70, s31, 1664\n"
        "v_mfma_f32_16x16x32_bf16 v[38:41], v[128:131], v[48:51], v[38:41]\n"
        "s_add_u32 s69, s69, s70\n"
        "v_add_u32 v11, s69, v3\n"
        "v_mfma_f32_16x16x32_bf16 v[38:41], v[124:127], v[42:45], v[38:41]\n"
        "v_add_u32 v12, s69, v2\n"
        "s_lshr_b32 s68, s30, 1\n"
        "v_mfma_f32_16x16x32_bf16 v[104:107], v[136:139], v[42:45], 0\n"
        "s_lshl2_add_u32 s68, s68, 4\n"
        "s_cmp_eq_u32 s31, 6\n"
        "s_cbranch_scc1 L8_%=\n"
        "L9_%=:\n"
        "v_mfma_f32_16x16x32_bf16 v[104:107], v[140:143], v[48:51], v[104:107]\n"
        "s_mov_b32 s72, s30\n"
        "s_mov_b32 s30, s34\n"
        "v_mfma_f32_16x16x32_bf16 v[104:107], v[144:147], v[54:57], v[104:107]\n"
        "s_mov_b32 s31, s35\n"
        "s_add_u32 s37, s37, 4\n"
        "v_mfma_f32_16x16x32_bf16 v[108:111], v[156:159], v[54:57], 0\n"
        "s_cmp_ge_u32 s37, 49\n"
        "s_cselect_b32 s69, 49, 0\n"
        "v_mfma_f32_16x16x32_bf16 v[108:111], v[152:155], v[48:51], v[108:111]\n"
        "s_sub_u32 s37, s37, s69\n"
        "s_sub_u32 s69, s37, 4\n"
        "s_cmp_lt_u32 s69, 4\n"
        "v_mfma_f32_16x16x32_bf16 v[108:111], v[148:151], v[42:45], v[108:111]\n"
        "s_subb_u32 s36, s36, -4\n"
        "s_cmp_ge_u32 s36, 6\n"
        "v_mfma_f32_16x16x32_bf16 v[76:79], v[160:163], v[42:45], 0\n"
        "s_cselect_b32 s69, 6, 0\n"
        "s_addc_u32 s34, s34, 0\n"
        "v_mfma_f32_16x16x32_bf16 v[76:79], v[164:167], v[48:51], v[76:79]\n"
        "s_sub_u32 s36, s36, s69\n"
        "s_cmp_ge_u32 s34, s74\n"
        "v_mfma_f32_16x16x32_bf16 v[76:79], v[168:171], v[54:57], v[76:79]\n"
        "s_cbranch_scc1 L10_%=\n"
        "L11_%=:\n"
        "s_mul_i32 s38, s34, 256\n"
        "v_mfma_f32_16x16x32_bf16 v[80:83], v[180:183], v[54:57], 0\n"
        "s_add_u32 s38, s38, s16\n"
        "s_cmp_ge_u32 s34, 20\n"
        "s_cselect_b32 s38, s22, s38\n"
        "v_mfma_f32_16x16x32_bf16 v[80:83], v[176:179], v[48:51], v[80:83]\n"
        "s_cmp_ge_u32 s34, 21\n"
        "s_cselect_b32 s38, s23, s38\n"
        "v_mfma_f32_16x16x32_bf16 v[80:83], v[172:175], v[42:45], v[80:83]\n"
        "s_mul_hi_u32 s70, s38, 58835169\n"
        "s_mul_i32 s38, s38, 1344\n"
        "v_mfma_f32_16x16x32_bf16 v[20:23], v[16:19], v[60:63], 0\n"
        "s_mul_i32 s70, s70, 588672\n"
        "s_add_u32 s38, s38, s70\n"
        "v_mfma_f32_16x16x32_bf16 v[20:23], v[16:19], v[66:69], v[20:23]\n"
        "s_mul_i32 s69, s36, 98304\n"
        "s_add_u32 s38, s38, s69\n"
        "s_mov_b32 s35, s36\n"
        "v_mfma_f32_16x16x32_bf16 v[20:23], v[16:19], v[72:75], v[20:23]\n"
        "s_mul_i32 s69, s36, -12\n"
        "v_mov_b64 v[30:31], 0\n"
        "v_mov_b64 v[32:33], 0\n"
        "v_mfma_f32_16x16x32_bf16 v[24:27], v[16:19], v[42:45], 0\n"
        "s_add_u32 s69, s69, 60\n"
        "s_lshl_b64 s[54:55], -1, s69\n"
        "s_lshl_b64 s[56:57], s[54:55], 24\n"
        "v_mfma_f32_16x16x32_bf16 v[24:27], v[16:19], v[48:51], v[24:27]\n"
        "s_ashr_i64 s[60:61], s[54:55], 8\n"
        "s_mov_b32 s58, s61\n"
        "v_mfma_f32_16x16x32_bf16 v[24:27], v[16:19], v[54:57], v[24:27]\n"
        "s_mov_b32 s59, s56\n"
        "v_cndmask_b32_e64 v15, v4, v7, s[56:57]\n"
        "v_smfmac_f32_16x16x64_bf16 v[30:33], v[42:45], v[34:41], v10\n"
        "v_cndmask_b32_e64 v100, v5, v8, s[58:59]\n"
        "v_smfmac_f32_16x16x64_bf16 v[30:33], v[48:51], v[104:111], v10\n"
        "v_cndmask_b32_e64 v103, v6, v9, s[60:61]\n"
        "v_smfmac_f32_16x16x64_bf16 v[30:33], v[54:57], v[76:83], v10\n"
        "s_cmp_eq_u32 s37, 3\n"
        "s_cbranch_scc1 L12_%=\n"
        "L13_%=:\n"
        "v_cndmask_b32_dpp v110, v24, v20, vcc row_ror:4 row_mask:0xf bank_mask:0xf\n"
        "v_mfma_f32_16x16x32_bf16 v[36:39], v[54:57], v[192:195], 0\n"
        "s_cmp_lt_u32 s34, s14\n"
        "v_mul_f32 v111, s28, v110\n"
        "v_fma_f32 v111, -v111, v110, v93\n"
        "v_mfma_f32_16x16x32_bf16 v[36:39], v[48:51], v[188:191], v[36:39]\n"
        "s_cselect_b32 s38, s38, 0x80000000\n"
        "s_add_u32 s39, s38, 100270464\n"
        "v_add_f32_e64 v111, s29, |v111|\n"
        "v_mfma_f32_16x16x32_bf16 v[36:39], v[42:45], v[184:187], v[36:39]\n"
        "v_rsq_f32 v84, v111\n"
        "v_cndmask_b32_dpp v104, v30, v96, vcc row_ror:4 row_mask:0xf bank_mask:0xf\n"
        "v_mfma_f32_16x16x32_bf16 v[24:27], v[72:75], v[192:195], 0\n"
        "v_cndmask_b32_e64 v84, 0, v84, s[46:47]\n"
        "v_cndmask_b32_dpp v105, v31, v97, vcc row_ror:4 row_mask:0xf bank_mask:0xf\n"
        "v_mfma_f32_16x16x32_bf16 v[24:27], v[66:69], v[188:191], v[24:27]\n"
        "v_cndmask_b32_dpp v85, v32, v98, vcc row_ror:4 row_mask:0xf bank_mask:0xf\n"
        "v_mul_f32_dpp v104, v84, v104 quad_perm:[0,0,0,0] row_mask:0xf bank_mask:0xf\n"
        "v_mfma_f32_16x16x32_bf16 v[24:27], v[60:63], v[184:187], v[24:27]\n"
        "v_mul_f32_dpp v105, v84, v105 quad_perm:[1,1,1,1] row_mask:0xf bank_mask:0xf\n"
        "v_mul_f32_dpp v85, v84, v85 quad_perm:[2,2,2,2] row_mask:0xf bank_mask:0xf\n"
        "v_mfma_f32_16x16x32_bf16 v[20:23], v[60:63], v[220:223], 0\n"
        "v_cndmask_b32_dpp v106, v33, v99, vcc row_ror:4 row_mask:0xf bank_mask:0xf\n"
        "v_max3_f32 v107, v104, v105, v85\n"
        "v_mfma_f32_16x16x32_bf16 v[20:23], v[66:69], v[224:227], v[20:23]\n"
        "v_mul_f32_dpp v106, v84, v106 quad_perm:[3,3,3,3] row_mask:0xf bank_mask:0xf\n"
        "v_max_f32 v107, v107, v106\n"
        "v_mfma_f32_16x16x32_bf16 v[20:23], v[72:75], v[228:231], v[20:23]\n"
        "v_mul_f32 v107, v107, v84\n"
        "v_fma_f32 v104, v104, v84, -v107\n"
        "v_fma_f32 v105, v105, v84, -v107\n"
        "v_mfma_f32_16x16x32_bf16 v[32:35], v[42:45], v[220:223], 0\n"
        "v_fma_f32 v85, v85, v84, -v107\n"
        "v_fma_f32 v106, v106, v84, -v107\n"
        "v_mfma_f32_16x16x32_bf16 v[32:35], v[48:51], v[224:227], v[32:35]\n"
        "v_exp_f32 v104, v104\n"
        "v_mfma_f32_16x16x32_bf16 v[32:35], v[54:57], v[228:231], v[32:35]\n"
        "v_exp_f32 v105, v105\n"
        "v_mfma_f32_16x16x32_bf16 v[108:111], v[54:57], v[204:207], 0\n"
        "v_exp_f32 v85, v85\n"
        "v_mfma_f32_16x16x32_bf16 v[108:111], v[48:51], v[200:203], v[108:111]\n"
        "v_exp_f32 v106, v106\n"
        "v_mfma_f32_16x16x32_bf16 v[108:111], v[42:45], v[196:199], v[108:111]\n"
        "v_add_f32 v107, v104, v105\n"
        "v_add_f32 v101, v85, v106\n"
        "v_add_f32 v107, v107, v101\n"
        "v_mfma_f32_16x16x32_bf16 v[80:83], v[72:75], v[204:207], 0\n"
        "v_rcp_f32 v107, v107\n"
        "v_mfma_f32_16x16x32_bf16 v[80:83], v[66:69], v[200:203], v[80:83]\n"
        "v_mul_f32_dpp v104, v84, v104 quad_perm:[0,0,0,0] row_mask:0xf bank_mask:0xf\n"
        "v_mul_f32_dpp v105, v84, v105 quad_perm:[1,1,1,1] row_mask:0xf bank_mask:0xf\n"
        "v_mfma_f32_16x16x32_bf16 v[80:83], v[60:63], v[196:199], v[80:83]\n"
        "v_mul_f32_dpp v85, v84, v85 quad_perm:[2,2,2,2] row_mask:0xf bank_mask:0xf\n"
        "v_mul_f32_dpp v106, v84, v106 quad_perm:[3,3,3,3] row_mask:0xf bank_mask:0xf\n"
        "v_mul_f32 v104, v104, v107\n"
        "v_mfma_f32_16x16x32_bf16 v[76:79], v[60:63], v[232:235], 0\n"
        "v_mul_f32 v105, v105, v107\n"
        "v_mul_f32 v85, v85, v107\n"
        "v_mfma_f32_16x16x32_bf16 v[76:79], v[66:69], v[236:239], v[76:79]\n"
        "v_mul_f32 v106, v106, v107\n"
        "v_cvt_pk_bf16_f32 v99, v104, v105\n"
        "v_mfma_f32_16x16x32_bf16 v[76:79], v[72:75], v[240:243], v[76:79]\n"
        "v_cvt_pk_bf16_f32 v101, v85, v106\n"
        "v_and_b32 v58, v1, v99\n"
        "v_and_b32 v59, v1, v101\n"
        "v_mfma_f32_16x16x32_bf16 v[104:107], v[42:45], v[232:235], 0\n"
        "v_cvt_pk_bf16_f32 v92, v24, v25\n"
        "v_cvt_pk_bf16_f32 v93, v26, v27\n"
        "v_mfma_f32_16x16x32_bf16 v[104:107], v[48:51], v[236:239], v[104:107]\n"
        "v_and_b32_dpp v40, v99, v1 row_ror:12 row_mask:0xf bank_mask:0xf\n"
        "v_and_b32_dpp v41, v101, v1 row_ror:12 row_mask:0xf bank_mask:0xf\n"
        "v_mfma_f32_16x16x32_bf16 v[104:107], v[54:57], v[240:243], v[104:107]\n"
        "v_cvt_pk_bf16_f32 v84, v36, v37\n"
        "v_cvt_pk_bf16_f32 v85, v38, v39\n"
        "v_mov_b64 v[70:71], v[58:59]\n"
        "v_mfma_f32_16x16x32_bf16 v[36:39], v[92:95], v[58:61], 0\n"
        "v_cvt_pk_bf16_f32 v96, v20, v21\n"
        "v_cvt_pk_bf16_f32 v97, v22, v23\n"
        "v_mfma_f32_16x16x32_bf16 v[22:25], v[84:87], v[40:43], 0\n"
        "v_mov_b64 v[52:53], v[40:41]\n"
        "v_cvt_pk_bf16_f32 v88, v32, v33\n"
        "v_cvt_pk_bf16_f32 v89, v34, v35\n"
        "v_mfma_f32_16x16x32_bf16 v[32:35], v[94:97], v[68:71], 0\n"
        "v_mov_b64 v[64:65], v[58:59]\n"
        "v_cvt_pk_bf16_f32 v30, v80, v81\n"
        "v_mfma_f32_16x16x32_bf16 v[96:99], v[86:89], v[50:53], 0\n"
        "v_cvt_pk_bf16_f32 v31, v82, v83\n"
        "v_mov_b64 v[46:47], v[40:41]\n"
        "v_mfma_f32_16x16x32_bf16 v[80:83], v[54:57], v[216:219], 0\n"
        "v_cvt_pk_bf16_f32 v92, v108, v109\n"
        "v_cvt_pk_bf16_f32 v93, v110, v111\n"
        "v_mfma_f32_16x16x32_bf16 v[80:83], v[48:51], v[212:215], v[80:83]\n"
        "v_cvt_pk_bf16_f32 v26, v76, v77\n"
        "v_cvt_pk_bf16_f32 v27, v78, v79\n"
        "v_mfma_f32_16x16x32_bf16 v[80:83], v[42:45], v[208:211], v[80:83]\n"
        "v_cvt_pk_bf16_f32 v88, v104, v105\n"
        "v_cvt_pk_bf16_f32 v89, v106, v107\n"
        "v_mfma_f32_16x16x32_bf16 v[108:111], v[72:75], v[216:219], 0\n"
        "v_mov_b64 v[76:77], v[58:59]\n"
        "v_mov_b64 v[58:59], v[40:41]\n"
        "v_mfma_f32_16x16x32_bf16 v[108:111], v[66:69], v[212:215], v[108:111]\n"
        "v_cvt_pk_bf16_f32 v104, v36, v37\n"
        "v_cvt_pk_bf16_f32 v105, v38, v39\n"
        "v_mfma_f32_16x16x32_bf16 v[108:111], v[60:63], v[208:211], v[108:111]\n"
        "v_cvt_pk_bf16_f32 v84, v80, v81\n"
        "v_cvt_pk_bf16_f32 v85, v82, v83\n"
        "v_mfma_f32_16x16x32_bf16 v[80:83], v[28:31], v[62:65], 0\n"
        "v_cvt_pk_bf16_f32 v106, v32, v33\n"
        "v_cvt_pk_bf16_f32 v107, v34, v35\n"
        "v_mfma_f32_16x16x32_bf16 v[32:35], v[90:93], v[44:47], 0\n"
        "v_cvt_pk_bf16_f32 v20, v22, v23\n"
        "v_cvt_pk_bf16_f32 v21, v24, v25\n"
        "v_mfma_f32_16x16x32_bf16 v[38:41], v[60:63], v[244:247], 0\n"
        "v_cvt_pk_bf16_f32 v92, v108, v109\n"
        "v_cvt_pk_bf16_f32 v93, v110, v111\n"
        "v_mfma_f32_16x16x32_bf16 v[38:41], v[66:69], v[248:251], v[38:41]\n"
        "v_cvt_pk_bf16_f32 v108, v80, v81\n"
        "v_cvt_pk_bf16_f32 v109, v82, v83\n"
        "v_mfma_f32_16x16x32_bf16 v[38:41], v[72:75], v[252:255], v[38:41]\n"
        "v_cvt_pk_bf16_f32 v22, v96, v97\n"
        "v_cvt_pk_bf16_f32 v23, v98, v99\n"
        "v_mfma_f32_16x16x32_bf16 v[80:83], v[42:45], v[244:247], 0\n"
        "v_cvt_pk_bf16_f32 v24, v32, v33\n"
        "v_cvt_pk_bf16_f32 v25, v34, v35\n"
        "v_mfma_f32_16x16x32_bf16 v[80:83], v[48:51], v[248:251], v[80:83]\n"
        "v_mfma_f32_16x16x32_bf16 v[80:83], v[54:57], v[252:255], v[80:83]\n"
        "v_mfma_f32_16x16x32_bf16 v[34:37], v[26:29], v[70:73], 0\n"
        "v_cvt_pk_bf16_f32 v96, v38, v39\n"
        "v_cvt_pk_bf16_f32 v97, v40, v41\n"
        "v_mfma_f32_16x16x32_bf16 v[38:41], v[88:91], v[52:55], 0\n"
        "v_mfma_f32_16x16x32_bf16 v[30:33], v[92:95], v[64:67], 0\n"
        "s_waitcnt vmcnt(0)\n"
        "ds_read_b128 v[60:63], v0\n"
        "ds_read_b128 v[42:45], v0 offset:768\n"
        "ds_read_b128 v[66:69], v0 offset:64\n"
        "v_cvt_pk_bf16_f32 v88, v80, v81\n"
        "v_mfma_f32_16x16x32_bf16 v[78:81], v[84:87], v[46:49], 0\n"
        "s_add_u32 s73, s73, 1\n"
        "ds_read_b128 v[48:51], v0 offset:832\n"
        "v_cvt_pk_bf16_f32 v89, v82, v83\n"
        "v_cvt_pk_bf16_f32 v110, v34, v35\n"
        "v_cvt_pk_bf16_f32 v111, v36, v37\n"
        "v_mfma_f32_16x16x32_bf16 v[82:85], v[94:97], v[74:77], 0\n"
        "s_cmp_lt_u32 s73, 4\n"
        "ds_read_b128 v[72:75], v0 offset:128\n"
        "v_cvt_pk_bf16_f32 v96, v30, v31\n"
        "v_cvt_pk_bf16_f32 v97, v32, v33\n"
        "v_cvt_pk_bf16_f32 v26, v38, v39\n"
        "v_mfma_f32_16x16x32_bf16 v[34:37], v[86:89], v[56:59], 0\n"
        "s_cbranch_scc1 L19_%=\n"
        "L20_%=:\n"
        "ds_read_b128 v[54:57], v0 offset:896\n"
        "v_cvt_pk_bf16_f32 v98, v82, v83\n"
        "v_cvt_pk_bf16_f32 v99, v84, v85\n"
        "v_cvt_pk_bf16_f32 v27, v40, v41\n"
        "v_mfma_f32_16x16x32_bf16 v[82:85], v[104:107], v[104:107], 0\n"
        "v_cvt_pk_bf16_f32 v30, v78, v79\n"
        "v_cvt_pk_bf16_f32 v31, v80, v81\n"
        "v_mfma_f32_16x16x32_bf16 v[82:85], v[108:111], v[108:111], v[82:85]\n"
        "v_cvt_pk_bf16_f32 v32, v34, v35\n"
        "v_cvt_pk_bf16_f32 v33, v36, v37\n"
        "v_mfma_f32_16x16x32_bf16 v[82:85], v[96:99], v[96:99], v[82:85]\n"
        "v_mfma_f32_16x16x32_bf16 v[76:79], v[20:23], v[20:23], 0\n"
        "v_mfma_f32_16x16x32_bf16 v[76:79], v[24:27], v[24:27], v[76:79]\n"
        "v_mfma_f32_16x16x32_bf16 v[76:79], v[30:33], v[30:33], v[76:79]\n"
        "s_cmp_lt_u32 s30, s14\n"
        "s_cbranch_scc1 PTOP_%=\n"
        "s_waitcnt lgkmcnt(0)\n"
        "s_nop 15\n"
        "L21_%=:\n"
        "ds_read_b32 v101, v102 offset:8\n"
        "s_waitcnt lgkmcnt(0)\n"
        "v_readfirstlane_b32 s40, v101\n"
        "s_nop 3\n"
        "s_cmp_ge_i32 s40, s68\n"
        "s_cbranch_scc1 L22_%=\n"
        "s_sleep 1\n"
        "s_sub_u32 s41, s41, 1\n"
        "s_cbranch_scc0 L21_%=\n"
        "s_endpgm\n"
        "L22_%=:\n"
        "v_cndmask_b32_e64 v101, v82, v83, s[48:49]\n"
        "v_cndmask_b32_e64 v93, v76, v77, s[48:49]\n"
        "v_cndmask_b32_e64 v101, v101, v84, s[50:51]\n"
        "v_cndmask_b32_e64 v93, v93, v78, s[50:51]\n"
        "v_cndmask_b32_e64 v101, v101, v85, s[52:53]\n"
        "v_cndmask_b32_e64 v93, v93, v79, s[52:53]\n"
        "ds_write_b128 v11, v[104:107]\n"
        "ds_write_b128 v11, v[20:23] offset:832\n"
        "v_cndmask_b32_dpp v101, v93, v101, vcc row_ror:4 row_mask:0xf bank_mask:0xf\n"
        "ds_write_b128 v11, v[108:111] offset:64\n"
        "ds_write_b128 v11, v[24:27] offset:896\n"
        "ds_write_b128 v11, v[96:99] offset:128\n"
        "ds_write_b128 v11, v[30:33] offset:960\n"
        "s_mov_b64 exec, s[46:47]\n"
        "ds_write_b32 v12, v101\n"
        "s_mov_b64 exec, -1\n"
        "s_mov_b64 exec, 2\n"
        "ds_inc_u32 v102, v0 offset:0\n"
        "s_mov_b64 exec, -1\n"
        "s_or_b32 s40, s18, s2\n"
        "s_cbranch_scc1 L31_%=\n"
        "L30_%=:\n"
        "s_mov_b64 exec, 1\n"
        "v_mov_b32 v20, 0\n"
        "v_mov_b32 v21, 1\n"
        "global_atomic_add v22, v20, v21, s[24:25] offset:256 sc0\n"
        "s_mov_b64 exec, -1\n"
        "s_mov_b64 exec, 1\n"
        "v_mov_b32 v20, 0\n"
        "v_mov_b32 v21, 0\n"
        "global_atomic_add v22, v20, v21, s[24:25] offset:0 sc0\n"
        "s_mov_b64 exec, -1\n"
        "s_waitcnt vmcnt(0)\n"
        "v_readfirstlane_b32 s40, v22\n"
        "s_nop 3\n"
        "s_cmp_ge_u32 s40, 512\n"
        "s_cbranch_scc1 L31_%=\n"
        "s_sleep 8\n"
        "s_sub_u32 s41, s41, 64\n"
        "s_cbranch_scc0 L30_%=\n"
        "L31_%=:\n"
        "s_endpgm\n"
        "L3_%=:\n"
        "s_nop 7\n"
        "L1_%=:\n"
        "ds_read_b32 v101, v102 offset:8\n"
        "s_waitcnt lgkmcnt(0)\n"
        "v_readfirstlane_b32 s40, v101\n"
        "s_nop 3\n"
        "s_cmp_ge_i32 s40, s68\n"
        "s_cbranch_scc1 L2_%=\n"
        "s_sleep 1\n"
        "s_sub_u32 s41, s41, 1\n"
        "s_cbranch_scc0 L1_%=\n"
        "s_endpgm\n"
        "L2_%=:\n"
        "s_nop 1\n"
        "s_branch L4_%=\n"
        "L8_%=:\n"
        "s_nop 7\n"
        "ds_read_u16 v11, v13 offset:19564\n"
        "ds_read_u16 v12, v13 offset:19566\n"
        "s_cmp_ge_u32 s30, 10\n"
        "s_cselect_b32 s69, 10, 0\n"
        "s_cmp_ge_u32 s30, 20\n"
        "s_cselect_b32 s69, 20, s69\n"
        "s_sub_u32 s69, s30, s69\n"
        "s_mul_i32 s69, s69, 832\n"
        "s_waitcnt lgkmcnt(0)\n"
        "v_add_u32 v11, s69, v11\n"
        "v_subrev_u32 v101, 8320, v11\n"
        "v_min_u32 v11, v11, v101\n"
        "v_add_u32 v11, 101856, v11\n"
        "v_add_u32 v12, s69, v12\n"
        "v_subrev_u32 v101, 8320, v12\n"
        "v_min_u32 v12, v12, v101\n"
        "v_add_u32 v12, 101856, v12\n"
        "s_mov_b32 s69, 21660\n"
        "v_writelane_b32 v102, s69, 1\n"
        "s_nop 1\n"
        "s_branch L9_%=\n"
        "L10_%=:\n"
        "s_nop 7\n"
        "v_mov_b32 v101, 21600\n"
        "L6_%=:\n"
        "ds_read_b32 v92, v101 offset:64\n"
        "s_waitcnt lgkmcnt(0)\n"
        "v_readfirstlane_b32 s40, v92\n"
        "s_nop 3\n"
        "s_cmp_ge_i32 s40, 1\n"
        "s_cbranch_scc1 L7_%=\n"
        "s_sleep 1\n"
        "s_sub_u32 s41, s41, 1\n"
        "s_cbranch_scc0 L6_%=\n"
        "s_endpgm\n"
        "L7_%=:\n"
        "v_mbcnt_lo_u32_b32 v92, -1, 0\n"
        "v_lshl_add_u32 v101, v92, 2, v101\n"
        "ds_read_b32 v92, v101 offset:68\n"
        "s_mov_b32 s14, s40\n"
        "s_mov_b32 s74, -1\n"
        "s_waitcnt lgkmcnt(0)\n"
        "v_readlane_b32 s22, v92, 0\n"
        "v_readlane_b32 s23, v92, 1\n"
        "s_nop 3\n"
        "s_nop 1\n"
        "s_branch L11_%=\n"
        "L12_%=:\n"
        "s_nop 7\n"
        "ds_read_b32 v15, v13 offset:19552\n"
        "ds_read_b32 v100, v13 offset:19556\n"
        "ds_read_b32 v103, v13 offset:19560\n"
        "s_mov_b32 s35, 6\n"
        "s_mov_b32 s38, 0\n"
        "s_mov_b32 s69, 1344\n"
        "s_waitcnt lgkmcnt(0)\n"
        "v_lshrrev_b32 v101, 28, v15\n"
        "v_add_u32 v101, s34, v101\n"
        "v_and_b32 v15, 0x0fffffff, v15\n"
        "v_mul_u32_u24 v92, 256, v101\n"
        "v_add_u32 v92, s16, v92\n"
        "v_cmpx_le_u32 vcc, 20, v101\n"
        "v_mov_b32 v92, s22\n"
        "s_mov_b64 exec, -1\n"
        "v_cmpx_le_u32 vcc, 21, v101\n"
        "v_mov_b32 v92, s23\n"
        "s_mov_b64 exec, -1\n"
        "v_mad_u32_u24 v15, v92, s69, v15\n"
        "v_mul_u32_u24 v92, 57457, v92\n"
        "v_lshrrev_b32 v92, 22, v92\n"
        "v_mul_u32_u24 v92, 588672, v92\n"
        "v_add_u32 v15, v15, v92\n"
        "v_lshrrev_b32 v101, 28, v100\n"
        "v_add_u32 v101, s34, v101\n"
        "v_and_b32 v100, 0x0fffffff, v100\n"
        "v_mul_u32_u24 v92, 256, v101\n"
        "v_add_u32 v92, s16, v92\n"
        "v_cmpx_le_u32 vcc, 20, v101\n"
        "v_mov_b32 v92, s22\n"
        "s_mov_b64 exec, -1\n"
        "v_cmpx_le_u32 vcc, 21, v101\n"
        "v_mov_b32 v92, s23\n"
        "s_mov_b64 exec, -1\n"
        "v_mad_u32_u24 v100, v92, s69, v100\n"
        "v_mul_u32_u24 v92, 57457, v92\n"
        "v_lshrrev_b32 v92, 22, v92\n"
        "v_mul_u32_u24 v92, 588672, v92\n"
        "v_add_u32 v100, v100, v92\n"
        "v_lshrrev_b32 v101, 28, v103\n"
        "v_add_u32 v101, s34, v101\n"
        "v_and_b32 v103, 0x0fffffff, v103\n"
        "v_mul_u32_u24 v92, 256, v101\n"
        "v_add_u32 v92, s16, v92\n"
        "v_cmpx_le_u32 vcc, 20, v101\n"
        "v_mov_b32 v92, s22\n"
        "s_mov_b64 exec, -1\n"
        "v_cmpx_le_u32 vcc, 21, v101\n"
        "v_mov_b32 v92, s23\n"
        "s_mov_b64 exec, -1\n"
        "v_mad_u32_u24 v103, v92, s69, v103\n"
        "v_mul_u32_u24 v92, 57457, v92\n"
        "v_lshrrev_b32 v92, 22, v92\n"
        "v_mul_u32_u24 v92, 588672, v92\n"
        "v_add_u32 v103, v103, v92\n"
        "s_nop 4\n"
        "s_mov_b64 vcc, s[44:45]\n"
        "s_nop 1\n"
        "s_branch L13_%=\n"
        "L19_%=:\n"
        "s_nop 7\n"
        "v_readfirstlane_b32 s62, v14\n"
        "s_cmp_eq_u32 s73, 1\n"
        "s_cbranch_scc1 L14_%=\n"
        "s_cmp_ge_u32 s73, 2\n"
        "s_cbranch_scc1 L15_%=\n"
        "s_mov_b32 s27, 20\n"
        "s_cmp_lg_u32 s2, 0\n"
        "s_cbranch_scc1 L18_%=\n"
        "s_not_b32 s26, s26\n"
        "s_mov_b64 exec, 1\n"
        "v_mov_b32 v101, 0\n"
        "v_mov_b32 v92, 1\n"
        "global_atomic_add v14, v101, v92, s[24:25] offset:256 sc0\n"
        "s_mov_b64 exec, -1\n"
        "s_branch L16_%=\n"
        "L18_%=:\n"
        "s_mov_b64 exec, 1\n"
        "v_mov_b32 v101, 0\n"
        "global_load_dword v14, v101, s[24:25] offset:256 sc1\n"
        "s_mov_b64 exec, -1\n"
        "s_branch L16_%=\n"
        "L14_%=:\n"
        "s_cmp_lg_u32 s62, s26\n"
        "s_cbranch_scc1 L17_%=\n"
        "s_sleep 8\n"
        "s_mov_b64 exec, 1\n"
        "v_mov_b32 v101, 0\n"
        "global_load_dword v14, v101, s[24:25] offset:256 sc1\n"
        "s_mov_b64 exec, -1\n"
        "s_waitcnt vmcnt(0)\n"
        "v_readfirstlane_b32 s62, v14\n"
        "s_sub_u32 s41, s41, 64\n"
        "s_cbranch_scc0 L14_%=\n"
        "s_endpgm\n"
        "L15_%=:\n"
        "s_cmp_lt_u32 s62, 209\n"
        "s_addc_u32 s27, s27, 0\n"
        "s_min_u32 s62, s62, 208\n"
        "s_add_u32 s62, s62, 5120\n"
        "s_lshl2_add_u32 s64, s73, 21600\n"
        "v_mov_b32 v101, s64\n"
        "v_mov_b32 v92, s62\n"
        "ds_write_b32 v101, v92 offset:60\n"
        "s_waitcnt lgkmcnt(0)\n"
        "s_cmp_lt_u32 s73, 3\n"
        "s_cbranch_scc1 L17_%=\n"
        "v_mov_b32 v92, s27\n"
        "ds_write_b32 v101, v92 offset:52\n"
        "s_waitcnt lgkmcnt(0)\n"
        "s_branch L16_%=\n"
        "L17_%=:\n"
        "s_mov_b64 exec, 1\n"
        "v_mov_b32 v101, 0\n"
        "v_mov_b32 v92, 1\n"
        "global_atomic_add v14, v101, v92, s[24:25] offset:0 sc0\n"
        "s_mov_b64 exec, -1\n"
        "L16_%=:\n"
        "s_nop 1\n"
        "s_branch L20_%=\n"
        "F8_%=:\n"
        "s_nop 7\n"
        "ds_read_u16 v11, v13 offset:19564\n"
        "ds_read_u16 v12, v13 offset:19566\n"
        "s_cmp_ge_u32 s30, 10\n"
        "s_cselect_b32 s69, 10, 0\n"
        "s_cmp_ge_u32 s30, 20\n"
        "s_cselect_b32 s69, 20, s69\n"
        "s_sub_u32 s69, s30, s69\n"
        "s_mul_i32 s69, s69, 832\n"
        "s_waitcnt lgkmcnt(0)\n"
        "v_add_u32 v11, s69, v11\n"
        "v_subrev_u32 v101, 8320, v11\n"
        "v_min_u32 v11, v11, v101\n"
        "v_add_u32 v11, 101856, v11\n"
        "v_add_u32 v12, s69, v12\n"
        "v_subrev_u32 v101, 8320, v12\n"
        "v_min_u32 v12, v12, v101\n"
        "v_add_u32 v12, 101856, v12\n"
        "s_mov_b32 s69, 21660\n"
        "v_writelane_b32 v102, s69, 1\n"
        "s_nop 1\n"
        "s_branch F9_%=\n"
        "F10_%=:\n"
        "s_nop 7\n"
        "v_mov_b32 v101, 21600\n"
        "F6_%=:\n"
        "ds_read_b32 v92, v101 offset:64\n"
        "s_waitcnt lgkmcnt(0)\n"
        "v_readfirstlane_b32 s40, v92\n"
        "s_nop 3\n"
        "s_cmp_ge_i32 s40, 1\n"
        "s_cbranch_scc1 F7_%=\n"
        "s_sleep 1\n"
        "s_sub_u32 s41, s41, 1\n"
        "s_cbranch_scc0 F6_%=\n"
        "s_endpgm\n"
        "F7_%=:\n"
        "v_mbcnt_lo_u32_b32 v92, -1, 0\n"
        "v_lshl_add_u32 v101, v92, 2, v101\n"
        "ds_read_b32 v92, v101 offset:68\n"
        "s_mov_b32 s14, s40\n"
        "s_mov_b32 s74, -1\n"
        "s_waitcnt lgkmcnt(0)\n"
        "v_readlane_b32 s22, v92, 0\n"
        "v_readlane_b32 s23, v92, 1\n"
        "s_nop 3\n"
        "s_nop 1\n"
        "s_branch F11_%=\n"
        "F12_%=:\n"
        "s_nop 7\n"
        "ds_read_b32 v15, v13 offset:19552\n"
        "ds_read_b32 v100, v13 offset:19556\n"
        "ds_read_b32 v103, v13 offset:19560\n"
        "s_mov_b32 s35, 6\n"
        "s_mov_b32 s38, 0\n"
        "s_mov_b32 s69, 1344\n"
        "s_waitcnt lgkmcnt(0)\n"
        "v_lshrrev_b32 v101, 28, v15\n"
        "v_add_u32 v101, s34, v101\n"
        "v_and_b32 v15, 0x0fffffff, v15\n"
        "v_mul_u32_u24 v92, 256, v101\n"
        "v_add_u32 v92, s16, v92\n"
        "v_cmpx_le_u32 vcc, 20, v101\n"
        "v_mov_b32 v92, s22\n"
        "s_mov_b64 exec, -1\n"
        "v_cmpx_le_u32 vcc, 21, v101\n"
        "v_mov_b32 v92, s23\n"
        "s_mov_b64 exec, -1\n"
        "v_mad_u32_u24 v15, v92, s69, v15\n"
        "v_mul_u32_u24 v92, 57457, v92\n"
        "v_lshrrev_b32 v92, 22, v92\n"
        "v_mul_u32_u24 v92, 588672, v92\n"
        "v_add_u32 v15, v15, v92\n"
        "v_lshrrev_b32 v101, 28, v100\n"
        "v_add_u32 v101, s34, v101\n"
        "v_and_b32 v100, 0x0fffffff, v100\n"
        "v_mul_u32_u24 v92, 256, v101\n"
        "v_add_u32 v92, s16, v92\n"
        "v_cmpx_le_u32 vcc, 20, v101\n"
        "v_mov_b32 v92, s22\n"
        "s_mov_b64 exec, -1\n"
        "v_cmpx_le_u32 vcc, 21, v101\n"
        "v_mov_b32 v92, s23\n"
        "s_mov_b64 exec, -1\n"
        "v_mad_u32_u24 v100, v92, s69, v100\n"
        "v_mul_u32_u24 v92, 57457, v92\n"
        "v_lshrrev_b32 v92, 22, v92\n"
        "v_mul_u32_u24 v92, 588672, v92\n"
        "v_add_u32 v100, v100, v92\n"
        "v_lshrrev_b32 v101, 28, v103\n"
        "v_add_u32 v101, s34, v101\n"
        "v_and_b32 v103, 0x0fffffff, v103\n"
        "v_mul_u32_u24 v92, 256, v101\n"
        "v_add_u32 v92, s16, v92\n"
        "v_cmpx_le_u32 vcc, 20, v101\n"
        "v_mov_b32 v92, s22\n"
        "s_mov_b64 exec, -1\n"
        "v_cmpx_le_u32 vcc, 21, v101\n"
        "v_mov_b32 v92, s23\n"
        "s_mov_b64 exec, -1\n"
        "v_mad_u32_u24 v103, v92, s69, v103\n"
        "v_mul_u32_u24 v92, 57457, v92\n"
        "v_lshrrev_b32 v92, 22, v92\n"
        "v_mul_u32_u24 v92, 588672, v92\n"
        "v_add_u32 v103, v103, v92\n"
        "s_nop 4\n"
        "s_mov_b64 vcc, s[44:45]\n"
        "s_nop 1\n"
        "s_branch F13_%=\n"
        "F19_%=:\n"
        "s_nop 7\n"
        "v_readfirstlane_b32 s62, v14\n"
        "s_cmp_eq_u32 s73, 1\n"
        "s_cbranch_scc1 F14_%=\n"
        "s_cmp_ge_u32 s73, 2\n"
        "s_cbranch_scc1 F15_%=\n"
        "s_mov_b32 s27, 20\n"
        "s_cmp_lg_u32 s2, 0\n"
        "s_cbranch_scc1 F18_%=\n"
        "s_not_b32 s26, s26\n"
        "s_mov_b64 exec, 1\n"
        "v_mov_b32 v101, 0\n"
        "v_mov_b32 v92, 1\n"
        "global_atomic_add v14, v101, v92, s[24:25] offset:256 sc0\n"
        "s_mov_b64 exec, -1\n"
        "s_branch F16_%=\n"
        "F18_%=:\n"
        "s_mov_b64 exec, 1\n"
        "v_mov_b32 v101, 0\n"
        "global_load_dword v14, v101, s[24:25] offset:256 sc1\n"
        "s_mov_b64 exec, -1\n"
        "s_branch F16_%=\n"
        "F14_%=:\n"
        "s_cmp_lg_u32 s62, s26\n"
        "s_cbranch_scc1 F17_%=\n"
        "s_sleep 8\n"
        "s_mov_b64 exec, 1\n"
        "v_mov_b32 v101, 0\n"
        "global_load_dword v14, v101, s[24:25] offset:256 sc1\n"
        "s_mov_b64 exec, -1\n"
        "s_waitcnt vmcnt(0)\n"
        "v_readfirstlane_b32 s62, v14\n"
        "s_sub_u32 s41, s41, 64\n"
        "s_cbranch_scc0 F14_%=\n"
        "s_endpgm\n"
        "F15_%=:\n"
        "s_cmp_lt_u32 s62, 209\n"
        "s_addc_u32 s27, s27, 0\n"
        "s_min_u32 s62, s62, 208\n"
        "s_add_u32 s62, s62, 5120\n"
        "s_lshl2_add_u32 s64, s73, 21600\n"
        "v_mov_b32 v101, s64\n"
        "v_mov_b32 v92, s62\n"
        "ds_write_b32 v101, v92 offset:60\n"
        "s_waitcnt lgkmcnt(0)\n"
        "s_cmp_lt_u32 s73, 3\n"
        "s_cbranch_scc1 F17_%=\n"
        "v_mov_b32 v92, s27\n"
        "ds_write_b32 v101, v92 offset:52\n"
        "s_waitcnt lgkmcnt(0)\n"
        "s_branch F16_%=\n"
        "F17_%=:\n"
        "s_mov_b64 exec, 1\n"
        "v_mov_b32 v101, 0\n"
        "v_mov_b32 v92, 1\n"
        "global_atomic_add v14, v101, v92, s[24:25] offset:0 sc0\n"
        "s_mov_b64 exec, -1\n"
        "F16_%=:\n"
        "s_nop 1\n"
        "s_branch F20_%=\n"
        "CONS_%=:\n"
        "s_mov_b32 s41, 0x400000\n"
        "s_mov_b32 s48, 0xffffffff\n"
        "s_mov_b32 s49, 0xffff0000\n"
        "s_mov_b32 s50, 0x33333333\n"
        "s_mov_b32 s51, 0x33333333\n"
        "s_mov_b32 s52, 0x3c2aaaab\n"
        "s_mov_b32 s53, 0x3a7ba882\n"
        "s_mov_b32 s56, 0x0\n"
        "s_mov_b32 s57, 0xfeff0000\n"
        "s_mov_b32 s54, 0x11111111\n"
        "s_mov_b32 s55, 0x11111111\n"
        "s_mov_b32 s63, 0x07060302\n"
        "s_mov_b64 s[44:45], s[12:13]\n"
        "s_and_b32 s45, s45, 0xffff\n"
        "s_mov_b32 s47, 0x20000\n"
        "s_mov_b32 s70, 832\n"
        "s_mov_b32 s71, 17\n"
        "s_sub_u32 s69, s18, 7488\n"
        "s_mov_b32 s28, 0xffff0000\n"
        "s_mov_b32 s31, 0\n"
        "s_add_u32 s35, s15, s17\n"
        "s_mov_b32 s36, 0x20000000\n"
        "L58_%=:\n"
        "ds_read_b32 v251, v250 offset:52\n"
        "s_waitcnt lgkmcnt(0)\n"
        "v_readfirstlane_b32 s40, v251\n"
        "s_nop 3\n"
        "s_cmp_ge_i32 s40, 6\n"
        "s_cbranch_scc1 L59_%=\n"
        "s_nop 0\n"
        "s_sub_u32 s41, s41, 1\n"
        "s_cbranch_scc0 L58_%=\n"
        "s_endpgm\n"
        "L59_%=:\n"
        "s_waitcnt vmcnt(0)\n"
        "s_setprio 2\n"
        "v_add_u32 v24, 2, v1\n"
        "v_mov_b32 v25, 0x3f80\n"
        "s_mov_b32 exec_lo, 0\n"
        "s_mov_b32 exec_hi, 0x1000000\n"
        "ds_write_b16 v24, v25\n"
        "s_mov_b64 exec, -1\n"
        "v_add_u32 v24, 61792, v0\n"
        "ds_read_b128 v[168:171], v24 offset:0\n"
        "ds_read_b128 v[172:175], v24 offset:1024\n"
        "ds_read_b128 v[176:179], v24 offset:2048\n"
        "ds_read_b128 v[180:183], v24 offset:3072\n"
        "ds_read_b128 v[184:187], v24 offset:4096\n"
        "ds_read_b128 v[188:191], v24 offset:5120\n"
        "ds_read_b128 v[192:195], v24 offset:6144\n"
        "ds_read_b128 v[196:199], v24 offset:7168\n"
        "ds_read_b128 v[200:203], v24 offset:8192\n"
        "ds_read_b128 v[204:207], v24 offset:9216\n"
        "ds_read_b128 v[208:211], v24 offset:10240\n"
        "ds_read_b128 v[212:215], v24 offset:11264\n"
        "ds_read_b128 v[216:219], v24 offset:12288\n"
        "ds_read_b128 v[220:223], v24 offset:13312\n"
        "ds_read_b128 v[224:227], v24 offset:14336\n"
        "ds_read_b128 v[228:231], v24 offset:15360\n"
        "ds_read_b128 v[232:235], v24 offset:16384\n"
        "ds_read_b128 v[236:239], v24 offset:17408\n"
        "ds_read_b128 v[240:243], v0 offset:0\n"
        "ds_read_b128 v[244:247], v0 offset:1024\n"
        "ds_read_b128 v[248:251], v0 offset:2048\n"
        "ds_read_b128 v[252:255], v0 offset:3088\n"
        "v_mov_b32 v25, 21600\n"
        "s_waitcnt lgkmcnt(0)\n"
        "s_mov_b64 exec, 1\n"
        "ds_inc_u32 v25, v1 offset:12\n"
        "s_mov_b64 exec, -1\n"
        "s_call_b64 s[64:65], CGRQ_%=\n"
        "s_setprio 0\n"
        "L60_%=:\n"
        "ds_read_b32 v24, v0 offset:21600\n"
        "s_waitcnt lgkmcnt(0)\n"
        "v_readfirstlane_b32 s40, v24\n"
        "s_nop 3\n"
        "s_cmp_ge_i32 s40, 6\n"
        "s_cbranch_scc1 L61_%=\n"
        "s_nop 0\n"
        "s_sub_u32 s41, s41, 1\n"
        "s_cbranch_scc0 L60_%=\n"
        "s_endpgm\n"
        "L61_%=:\n"
        "ds_read_b128 v[124:127], v7 offset:0\n"
        "ds_read_b128 v[132:135], v7 offset:3328\n"
        "ds_read_b128 v[128:131], v7 offset:64\n"
        "ds_read_b128 v[136:139], v7 offset:3392\n"
        "ds_read_b128 v[160:163], v7 offset:128\n"
        "ds_read_b128 v[52:55], v7 offset:3456\n"
        "s_waitcnt lgkmcnt(0)\n"
        "s_branch CTOP_%=\n"
        ".p2align 6\n"
        "s_nop 0\n"
        "s_nop 0\n"
        "s_nop 0\n"
        "s_nop 0\n"
        "s_nop 0\n"
        "s_nop 0\n"
        "s_nop 0\n"
        "s_nop 0\n"
        "s_nop 0\n"
        "s_nop 0\n"
        "s_nop 0\n"
        "s_nop 0\n"
        "s_nop 0\n"
        "s_nop 0\n"
        "s_nop 0\n"
        "CFULL_%=:\n"
        "s_call_b64 s[64:65], CGRP_%=\n"
        "CTOP_%=:\n"
        "s_waitcnt lgkmcnt(5)\n"
        "v_mfma_f32_16x16x32_bf16 v[24:27], v[168:171], v[124:127], 0\n"
        "v_cvt_pk_bf16_f32 v156, v152, v56\n"
        "v_cvt_pk_bf16_f32 v157, v72, v20\n"
        "s_waitcnt lgkmcnt(3)\n"
        "v_mfma_f32_16x16x32_bf16 v[24:27], v[172:175], v[128:131], v[24:27]\n"
        "ds_read_b128 v[44:47], v16 offset:128\n"
        "v_cvt_pk_bf16_f32 v158, v84, v60\n"
        "s_waitcnt lgkmcnt(2)\n"
        "v_mfma_f32_16x16x32_bf16 v[24:27], v[176:179], v[160:163], v[24:27]\n"
        "ds_read_b128 v[48:51], v5\n"
        "v_cvt_pk_bf16_f32 v164, v153, v57\n"
        "v_mfma_f32_16x16x32_bf16 v[32:35], v[188:191], v[160:163], 0\n"
        "v_cvt_pk_bf16_f32 v165, v73, v21\n"
        "v_cvt_pk_bf16_f32 v166, v85, v61\n"
        "v_mfma_f32_16x16x32_bf16 v[32:35], v[184:187], v[128:131], v[32:35]\n"
        "v_cvt_pk_bf16_f32 v146, v154, v58\n"
        "v_cvt_pk_bf16_f32 v147, v74, v22\n"
        "v_mfma_f32_16x16x32_bf16 v[32:35], v[180:183], v[124:127], v[32:35]\n"
        "s_add_u32 s37, s36, 196224\n"
        "ds_read_b128 v[92:95], v7 offset:6656\n"
        "ds_read_b128 v[96:99], v7 offset:6720\n"
        "ds_read_b128 v[116:119], v7 offset:6784\n"
        "v_mfma_f32_16x16x32_bf16 v[76:79], v[192:195], v[124:127], 0\n"
        "v_perm_b32 v80, v25, v24, s63\n"
        "v_perm_b32 v81, v27, v26, s63\n"
        "v_mfma_f32_16x16x32_bf16 v[76:79], v[196:199], v[128:131], v[76:79]\n"
        "v_perm_b32 v82, v33, v32, s63\n"
        "v_perm_b32 v83, v35, v34, s63\n"
        "v_mfma_f32_16x16x32_bf16 v[76:79], v[200:203], v[160:163], v[76:79]\n"
        "buffer_store_dwordx3 v[156:158], v12, s[8:11], s37 offen offset:384 sc0 sc1 nt\n"
        "v_cvt_pk_bf16_f32 v148, v86, v62\n"
        "v_cvt_pk_bf16_f32 v150, v155, v59\n"
        "v_cvt_pk_bf16_f32 v151, v75, v23\n"
        "v_cvt_pk_bf16_f32 v152, v87, v63\n"
        "s_waitcnt lgkmcnt(3)\n"
        "v_mfma_f32_16x16x32_bf16 v[32:35], v[44:47], v[160:163], 0\n"
        "v_cvt_pk_bf16_f32 v72, v68, v36\n"
        "v_perm_b32 v84, v77, v76, s63\n"
        "v_smfmac_f32_16x16x64_bf16 v[32:35], v[48:51], v[124:131], v4\n"
        "s_add_u32 s37, s36, 196224\n"
        "v_perm_b32 v85, v79, v78, s63\n"
        "v_mfma_f32_16x16x32_bf16 v[24:27], v[44:47], v[52:55], 0\n"
        "v_cvt_pk_bf16_f32 v73, v40, v64\n"
        "v_cvt_pk_bf16_f32 v74, v104, v88\n"
        "v_smfmac_f32_16x16x64_bf16 v[24:27], v[48:51], v[132:139], v4\n"
        "s_waitcnt lgkmcnt(0)\n"
        "s_setprio 2\n"
        "v_cvt_pk_bf16_f32 v156, v69, v37\n"
        "v_cvt_pk_bf16_f32 v157, v41, v65\n"
        "v_mfma_f32_16x16x32_bf16 v[100:103], v[44:47], v[116:119], 0\n"
        "v_cvt_pk_bf16_f32 v158, v105, v89\n"
        "v_cvt_pk_bf16_f32 v76, v70, v38\n"
        "v_smfmac_f32_16x16x64_bf16 v[100:103], v[48:51], v[92:99], v4\n"
        "buffer_store_dwordx3 v[164:166], v13, s[8:11], s37 offen offset:1152 sc0 sc1 nt\n"
        "v_cvt_pk_bf16_f32 v77, v42, v66\n"
        "v_cvt_pk_bf16_f32 v78, v106, v90\n"
        "v_cvt_pk_bf16_f32 v88, v71, v39\n"
        "v_cvt_pk_bf16_f32 v89, v43, v67\n"
        "v_cvt_pk_bf16_f32 v90, v107, v91\n"
        "v_mfma_f32_16x16x32_bf16 v[20:23], v[212:215], v[160:163], 0\n"
        "s_add_u32 s37, s36, 294336\n"
        "ds_read_b64 v[154:155], v11 offset:192\n"
        "v_mfma_f32_16x16x32_bf16 v[20:23], v[208:211], v[128:131], v[20:23]\n"
        "ds_read_b32 v153, v10\n"
        "v_mfma_f32_16x16x32_bf16 v[20:23], v[204:207], v[124:127], v[20:23]\n"
        "v_mfma_f32_16x16x32_bf16 v[28:31], v[216:219], v[124:127], 0\n"
        "v_mfma_f32_16x16x32_bf16 v[28:31], v[220:223], v[128:131], v[28:31]\n"
        "v_mfma_f32_16x16x32_bf16 v[28:31], v[224:227], v[160:163], v[28:31]\n"
        "buffer_store_dwordx3 v[146:148], v12, s[8:11], s37 offen offset:576 sc0 sc1 nt\n"
        "s_add_u32 s37, s36, 392448\n"
        "s_waitcnt lgkmcnt(1)\n"
        "v_add_f32_dpp v155, v34, v35 quad_perm:[0,1,2,3] row_mask:0x1 bank_mask:0xf\n"
        "v_add_f32_dpp v154, v32, v33 quad_perm:[0,1,2,3] row_mask:0x1 bank_mask:0xf\n"
        "v_perm_b32 v86, v21, v20, s63\n"
        "v_mfma_f32_16x16x32_bf16 v[44:47], v[236:239], v[160:163], 0\n"
        "v_perm_b32 v87, v23, v22, s63\n"
        "v_perm_b32 v20, v29, v28, s63\n"
        "v_mfma_f32_16x16x32_bf16 v[44:47], v[232:235], v[128:131], v[44:47]\n"
        "v_perm_b32 v21, v31, v30, s63\n"
        "v_add_f32_dpp v155, v26, v27 quad_perm:[0,1,2,3] row_mask:0x2 bank_mask:0xf\n"
        "v_mfma_f32_16x16x32_bf16 v[44:47], v[228:231], v[124:127], v[44:47]\n"
        "buffer_store_dwordx3 v[150:152], v12, s[8:11], s37 offen sc0 sc1 nt\n"
        "v_add_f32_dpp v154, v24, v25 quad_perm:[0,1,2,3] row_mask:0x2 bank_mask:0xf\n"
        "v_add_f32_dpp v155, v102, v103 quad_perm:[0,1,2,3] row_mask:0x4 bank_mask:0xf\n"
        "v_mul_f32 v165, s52, v155\n"
        "v_add_f32_dpp v154, v100, v101 quad_perm:[0,1,2,3] row_mask:0x4 bank_mask:0xf\n"
        "s_waitcnt lgkmcnt(0)\n"
        "v_fma_f32 v165, -v165, v155, v153\n"
        "v_add_f32_e64 v165, s53, |v165|\n"
        "v_perm_b32 v23, v47, v46, s63\n"
        "v_perm_b32 v22, v45, v44, s63\n"
        "v_mfma_f32_16x16x32_bf16 v[100:103], v[124:127], v[80:83], 0\n"
        "v_rsq_f32 v153, v165\n"
        "v_mfma_f32_16x16x32_bf16 v[100:103], v[128:131], v[84:87], v[100:103]\n"
        "s_add_u32 s37, s36, 392448\n"
        "ds_read_b128 v[104:107], v8 offset:128\n"
        "v_and_b32 v56, s28, v153\n"
        "v_mfma_f32_16x16x32_bf16 v[100:103], v[160:163], v[20:23], v[100:103]\n"
        "ds_read2_b32 v[108:109], v6 offset0:0 offset1:2\n"
        "ds_read2_b32 v[110:111], v6 offset0:8 offset1:10\n"
        "v_mfma_f32_16x16x32_bf16 v[144:147], v[52:55], v[20:23], 0\n"
        "v_and_b32 v164, s28, v153\n"
        "v_sub_f32 v167, v153, v56\n"
        "v_mfma_f32_16x16x32_bf16 v[144:147], v[136:139], v[84:87], v[144:147]\n"
        "v_cvt_pk_bf16_f32 v57, v167, v56\n"
        "v_cvt_pk_bf16_f32 v165, v164, v167\n"
        "v_mfma_f32_16x16x32_bf16 v[144:147], v[132:135], v[80:83], v[144:147]\n"
        "buffer_store_dwordx3 v[72:74], v15, s[8:11], s37 offen offset:768 sc0 sc1 nt\n"
        "v_mul_f32 v154, v154, v153\n"
        "v_readlane_b32 s58, v153, 56\n"
        "s_mov_b64 exec, s[56:57]\n"
        "v_mov_b32 v154, 0xff800000\n"
        "s_mov_b64 exec, -1\n"
        "v_mfma_f32_16x16x4_4b_bf16 v[32:47], v[56:57], v[164:165], 0 blgp:4\n"
        "v_mul_f32 v154, s58, v154\n"
        "v_exp_f32 v154, v154\n"
        "v_mfma_f32_16x16x32_bf16 v[24:27], v[92:95], v[80:83], 0\n"
        "s_add_u32 s37, s36, 490560\n"
        "v_add_f32_dpp v167, v154, v154 row_ror:8 row_mask:0xf bank_mask:0xf\n"
        "v_mul_f32 v154, v154, v153\n"
        "v_mfma_f32_16x16x32_bf16 v[24:27], v[96:99], v[84:87], v[24:27]\n"
        "v_add_f32_dpp v167, v167, v167 row_ror:4 row_mask:0xf bank_mask:0xf\n"
        "v_mfma_f32_16x16x32_bf16 v[24:27], v[116:119], v[20:23], v[24:27]\n"
        "s_waitcnt lgkmcnt(0)\n"
        "v_add_f32_dpp v167, v167, v167 row_ror:2 row_mask:0xf bank_mask:0xf\n"
        "v_mfma_f32_16x16x32_bf16 v[148:151], v[104:107], v[20:23], 0\n"
        "v_mul_f32 v100, v100, v32\n"
        "v_mul_f32 v101, v101, v33\n"
        "v_smfmac_f32_16x16x64_bf16 v[148:151], v[108:111], v[80:87], v4\n"
        "buffer_store_dwordx3 v[156:158], v12, s[8:11], s37 offen offset:192 sc0 sc1 nt\n"
        "v_mul_f32 v102, v102, v34\n"
        "v_mul_f32 v103, v103, v35\n"
        "v_exp_f32 v100, v100\n"
        "v_mul_f32 v144, v144, v36\n"
        "v_mfma_f32_16x16x32_bf16 v[20:23], v[168:171], v[132:135], 0\n"
        "s_add_u32 s37, s36, 490560\n"
        "v_exp_f32 v101, v101\n"
        "v_mfma_f32_16x16x32_bf16 v[20:23], v[172:175], v[136:139], v[20:23]\n"
        "v_exp_f32 v102, v102\n"
        "v_mfma_f32_16x16x32_bf16 v[20:23], v[176:179], v[52:55], v[20:23]\n"
        "v_exp_f32 v103, v103\n"
        "v_add_f32 v151, v100, v101\n"
        "v_mfma_f32_16x16x32_bf16 v[28:31], v[188:191], v[52:55], 0\n"
        "v_add_f32 v151, v151, v102\n"
        "v_add_f32 v151, v151, v103\n"
        "v_mfma_f32_16x16x32_bf16 v[28:31], v[184:187], v[136:139], v[28:31]\n"
        "v_mul_f32 v145, v145, v37\n"
        "v_mul_f32 v146, v146, v38\n"
        "v_mfma_f32_16x16x32_bf16 v[28:31], v[180:183], v[132:135], v[28:31]\n"
        "buffer_store_dwordx3 v[76:78], v14, s[8:11], s37 offen offset:960 sc0 sc1 nt\n"
        "v_perm_b32 v68, v21, v20, s63\n"
        "v_perm_b32 v69, v23, v22, s63\n"
        "v_mul_f32 v147, v147, v39\n"
        "v_exp_f32 v144, v144\n"
        "v_mfma_f32_16x16x32_bf16 v[20:23], v[192:195], v[132:135], 0\n"
        "s_add_u32 s37, s36, 588672\n"
        "v_exp_f32 v145, v145\n"
        "v_mfma_f32_16x16x32_bf16 v[20:23], v[196:199], v[136:139], v[20:23]\n"
        "v_perm_b32 v70, v29, v28, s63\n"
        "v_perm_b32 v71, v31, v30, s63\n"
        "v_mfma_f32_16x16x32_bf16 v[20:23], v[200:203], v[52:55], v[20:23]\n"
        "buffer_store_dwordx3 v[88:90], v12, s[8:11], s37 offen offset:384 sc0 sc1 nt\n"
        "v_exp_f32 v146, v146\n"
        "v_exp_f32 v147, v147\n"
        "v_add_f32 v151, v151, v144\n"
        "v_add_f32 v151, v151, v145\n"
        "v_mfma_f32_16x16x32_bf16 v[28:31], v[212:215], v[52:55], 0\n"
        "v_add_f32 v151, v151, v146\n"
        "v_add_f32 v151, v151, v147\n"
        "v_mfma_f32_16x16x32_bf16 v[28:31], v[208:211], v[136:139], v[28:31]\n"
        "v_perm_b32 v72, v21, v20, s63\n"
        "v_perm_b32 v73, v23, v22, s63\n"
        "v_mfma_f32_16x16x32_bf16 v[28:31], v[204:207], v[132:135], v[28:31]\n"
        "v_mul_f32 v24, v24, v40\n"
        "v_mul_f32 v25, v25, v41\n"
        "v_mul_f32 v26, v26, v42\n"
        "v_mfma_f32_16x16x32_bf16 v[20:23], v[216:219], v[132:135], 0\n"
        "v_mul_f32 v27, v27, v43\n"
        "v_add_f32 v148, v148, v149\n"
        "v_mfma_f32_16x16x32_bf16 v[20:23], v[220:223], v[136:139], v[20:23]\n"
        "v_exp_f32 v24, v24\n"
        "v_mfma_f32_16x16x32_bf16 v[20:23], v[224:227], v[52:55], v[20:23]\n"
        "v_perm_b32 v74, v29, v28, s63\n"
        "v_perm_b32 v75, v31, v30, s63\n"
        "v_add_f32 v151, v151, v24\n"
        "v_mfma_f32_16x16x32_bf16 v[64:67], v[236:239], v[52:55], 0\n"
        "v_exp_f32 v25, v25\n"
        "v_mfma_f32_16x16x32_bf16 v[64:67], v[232:235], v[136:139], v[64:67]\n"
        "v_exp_f32 v26, v26\n"
        "v_mfma_f32_16x16x32_bf16 v[64:67], v[228:231], v[132:135], v[64:67]\n"
        "v_perm_b32 v48, v21, v20, s63\n"
        "v_perm_b32 v49, v23, v22, s63\n"
        "v_mfma_f32_16x16x4_4b_bf16 v[28:43], v[56:57], v[164:165], 0 blgp:5\n"
        "v_exp_f32 v27, v27\n"
        "v_add_f32 v151, v151, v25\n"
        "v_add_f32 v151, v151, v26\n"
        "v_mul_f32 v148, v148, v44\n"
        "v_exp_f32 v148, v148\n"
        "v_perm_b32 v50, v65, v64, s63\n"
        "v_perm_b32 v51, v67, v66, s63\n"
        "v_mfma_f32_16x16x32_bf16 v[58:61], v[124:127], v[68:71], 0\n"
        "v_add_f32 v151, v151, v27\n"
        "v_add_f32_dpp v167, v167, v167 row_ror:1 row_mask:0xf bank_mask:0xf\n"
        "v_mfma_f32_16x16x32_bf16 v[58:61], v[128:131], v[72:75], v[58:61]\n"
        "v_add_f32_dpp v151, v148, v151 quad_perm:[0,1,2,3] row_mask:0x4 bank_mask:0xf\n"
        "v_mfma_f32_16x16x32_bf16 v[58:61], v[160:163], v[48:51], v[58:61]\n"
        "v_mfma_f32_16x16x32_bf16 v[112:115], v[52:55], v[48:51], 0\n"
        "v_permlane32_swap_b32 v151, v167\n"
        "v_mfma_f32_16x16x32_bf16 v[112:115], v[136:139], v[72:75], v[112:115]\n"
        "v_add_f32 v151, v151, v167\n"
        "v_mov_b32 v167, v151\n"
        "v_mfma_f32_16x16x32_bf16 v[112:115], v[132:135], v[68:71], v[112:115]\n"
        "v_mfma_f32_16x16x32_bf16 v[20:23], v[92:95], v[68:71], 0\n"
        "v_permlane16_swap_b32 v151, v167\n"
        "v_mfma_f32_16x16x32_bf16 v[20:23], v[96:99], v[72:75], v[20:23]\n"
        "v_add_f32 v151, v151, v167\n"
        "v_mul_f32 v58, v58, v28\n"
        "v_mfma_f32_16x16x32_bf16 v[20:23], v[116:119], v[48:51], v[20:23]\n"
        "v_rcp_f32 v123, v151\n"
        "v_mul_f32 v59, v59, v29\n"
        "v_and_b32 v158, s28, v123\n"
        "v_mfma_f32_16x16x32_bf16 v[62:65], v[104:107], v[48:51], 0\n"
        "v_sub_f32 v152, v123, v158\n"
        "v_cvt_pk_bf16_f32 v159, v158, v152\n"
        "v_smfmac_f32_16x16x64_bf16 v[62:65], v[108:111], v[68:75], v4\n"
        "v_min3_f32 v167, v151, v123, v153\n"
        "v_mfma_f32_16x16x32_bf16 v[48:51], v[168:171], v[92:95], 0\n"
        "v_mul_f32 v60, v60, v30\n"
        "v_mul_f32 v61, v61, v31\n"
        "v_mfma_f32_16x16x32_bf16 v[48:51], v[172:175], v[96:99], v[48:51]\n"
        "v_exp_f32 v58, v58\n"
        "v_mfma_f32_16x16x32_bf16 v[48:51], v[176:179], v[116:119], v[48:51]\n"
        "v_exp_f32 v59, v59\n"
        "v_exp_f32 v60, v60\n"
        "v_mfma_f32_16x16x32_bf16 v[68:71], v[188:191], v[116:119], 0\n"
        "v_exp_f32 v61, v61\n"
        "v_mfma_f32_16x16x32_bf16 v[68:71], v[184:187], v[96:99], v[68:71]\n"
        "v_add_f32 v155, v58, v59\n"
        "v_add_f32 v155, v155, v60\n"
        "v_mfma_f32_16x16x32_bf16 v[68:71], v[180:183], v[92:95], v[68:71]\n"
        "v_perm_b32 v44, v49, v48, s63\n"
        "v_perm_b32 v45, v51, v50, s63\n"
        "v_add_f32 v155, v155, v61\n"
        "v_mfma_f32_16x16x32_bf16 v[28:31], v[192:195], v[92:95], 0\n"
        "v_mul_f32 v112, v112, v32\n"
        "v_mul_f32 v113, v113, v33\n"
        "v_mfma_f32_16x16x32_bf16 v[28:31], v[196:199], v[96:99], v[28:31]\n"
        "v_mul_f32 v114, v114, v34\n"
        "v_perm_b32 v46, v69, v68, s63\n"
        "v_mfma_f32_16x16x32_bf16 v[28:31], v[200:203], v[116:119], v[28:31]\n"
        "v_perm_b32 v47, v71, v70, s63\n"
        "v_mul_f32 v115, v115, v35\n"
        "v_exp_f32 v112, v112\n"
        "v_mfma_f32_16x16x32_bf16 v[68:71], v[212:215], v[116:119], 0\n"
        "v_exp_f32 v113, v113\n"
        "v_mfma_f32_16x16x32_bf16 v[68:71], v[208:211], v[96:99], v[68:71]\n"
        "v_exp_f32 v114, v114\n"
        "v_mfma_f32_16x16x32_bf16 v[68:71], v[204:207], v[92:95], v[68:71]\n"
        "v_perm_b32 v48, v29, v28, s63\n"
        "v_perm_b32 v49, v31, v30, s63\n"
        "v_add_f32 v155, v155, v112\n"
        "v_mfma_f32_16x16x32_bf16 v[64:67], v[216:219], v[92:95], 0\n"
        "v_exp_f32 v115, v115\n"
        "v_mfma_f32_16x16x32_bf16 v[64:67], v[220:223], v[96:99], v[64:67]\n"
        "v_add_f32 v155, v155, v113\n"
        "v_add_f32 v155, v155, v114\n"
        "v_mfma_f32_16x16x32_bf16 v[64:67], v[224:227], v[116:119], v[64:67]\n"
        "v_perm_b32 v50, v69, v68, s63\n"
        "v_perm_b32 v51, v71, v70, s63\n"
        "v_add_f32 v155, v155, v115\n"
        "v_mul_f32 v20, v20, v36\n"
        "v_mfma_f32_16x16x32_bf16 v[28:31], v[236:239], v[116:119], 0\n"
        "v_mul_f32 v21, v21, v37\n"
        "v_mul_f32 v22, v22, v38\n"
        "v_mfma_f32_16x16x32_bf16 v[28:31], v[232:235], v[96:99], v[28:31]\n"
        "v_perm_b32 v150, v65, v64, s63\n"
        "v_perm_b32 v151, v67, v66, s63\n"
        "v_mfma_f32_16x16x32_bf16 v[28:31], v[228:231], v[92:95], v[28:31]\n"
        "v_exp_f32 v20, v20\n"
        "v_mfma_f32_16x16x4_4b_bf16 v[76:91], v[56:57], v[158:159], 0 blgp:4\n"
        "v_exp_f32 v21, v21\n"
        "v_mul_f32 v23, v23, v39\n"
        "v_exp_f32 v22, v22\n"
        "v_exp_f32 v23, v23\n"
        "v_add_f32 v155, v155, v20\n"
        "v_add_f32 v155, v155, v21\n"
        "v_perm_b32 v152, v29, v28, s63\n"
        "v_perm_b32 v153, v31, v30, s63\n"
        "v_mfma_f32_16x16x32_bf16 v[36:39], v[124:127], v[44:47], 0\n"
        "v_add_f32 v155, v155, v22\n"
        "v_add_f32 v155, v155, v23\n"
        "v_mfma_f32_16x16x32_bf16 v[36:39], v[128:131], v[48:51], v[36:39]\n"
        "v_mul_f32 v100, v100, v76\n"
        "v_mul_f32 v101, v101, v77\n"
        "v_mfma_f32_16x16x32_bf16 v[36:39], v[160:163], v[150:153], v[36:39]\n"
        "v_mul_f32 v102, v102, v78\n"
        "v_mul_f32 v103, v103, v79\n"
        "v_mul_f32 v144, v144, v80\n"
        "v_mul_f32 v145, v145, v81\n"
        "v_mfma_f32_16x16x32_bf16 v[140:143], v[52:55], v[150:153], 0\n"
        "v_mul_f32 v146, v146, v82\n"
        "v_mul_f32 v147, v147, v83\n"
        "v_mfma_f32_16x16x32_bf16 v[140:143], v[136:139], v[48:51], v[140:143]\n"
        "v_mul_f32 v24, v24, v84\n"
        "v_mul_f32 v25, v25, v85\n"
        "v_mfma_f32_16x16x32_bf16 v[140:143], v[132:135], v[44:47], v[140:143]\n"
        "v_add_f32 v62, v62, v63\n"
        "v_mul_f32 v62, v62, v40\n"
        "v_mul_f32 v26, v26, v86\n"
        "v_mfma_f32_16x16x4_4b_bf16 v[64:79], v[56:57], v[164:165], 0 blgp:6\n"
        "v_exp_f32 v62, v62\n"
        "v_mul_f32 v27, v27, v87\n"
        "v_fma_f32 v148, v148, v88, v19\n"
        "v_add_f32_dpp v155, v62, v155 quad_perm:[0,1,2,3] row_mask:0x4 bank_mask:0xf\n"
        "v_perm_b32 v40, v101, v100, s63\n"
        "v_perm_b32 v41, v103, v102, s63\n"
        "v_perm_b32 v42, v145, v144, s63\n"
        "v_perm_b32 v43, v147, v146, s63\n"
        "v_perm_b32 v164, v25, v24, s63\n"
        "v_mfma_f32_16x16x32_bf16 v[156:159], v[92:95], v[44:47], 0\n"
        "v_perm_b32 v166, v27, v26, s63\n"
        "v_perm_b32 v165, 0, v148, s63\n"
        "v_mfma_f32_16x16x32_bf16 v[156:159], v[96:99], v[48:51], v[156:159]\n"
        "v_mul_f32 v36, v36, v64\n"
        "v_mul_f32 v37, v37, v65\n"
        "v_mfma_f32_16x16x32_bf16 v[156:159], v[116:119], v[150:153], v[156:159]\n"
        "v_mul_f32 v38, v38, v66\n"
        "v_mul_f32 v39, v39, v67\n"
        "v_mul_f32 v140, v140, v68\n"
        "v_mfma_f32_16x16x32_bf16 v[92:95], v[104:107], v[150:153], 0\n"
        "ds_read_b128 v[88:91], v17 offset:0\n"
        "v_mul_f32 v141, v141, v69\n"
        "v_smfmac_f32_16x16x64_bf16 v[92:95], v[108:111], v[44:51], v4\n"
        "ds_read_b128 v[84:87], v17 offset:64\n"
        "ds_read_b128 v[150:153], v17 offset:128\n"
        "v_mfma_f32_16x16x32_bf16 v[48:51], v[124:127], v[240:243], 0\n"
        "v_exp_f32 v36, v36\n"
        "v_mfma_f32_16x16x32_bf16 v[48:51], v[128:131], v[244:247], v[48:51]\n"
        "ds_read_b128 v[80:83], v0 offset:5136\n"
        "v_mul_f32 v142, v142, v70\n"
        "v_mfma_f32_16x16x32_bf16 v[48:51], v[160:163], v[248:251], v[48:51]\n"
        "ds_read_b128 v[32:35], v0 offset:4112\n"
        "v_mul_f32 v143, v143, v71\n"
        "v_mfma_f32_16x16x32_bf16 v[24:27], v[52:55], v[248:251], 0\n"
        "v_exp_f32 v37, v37\n"
        "v_mfma_f32_16x16x32_bf16 v[24:27], v[136:139], v[244:247], v[24:27]\n"
        "v_exp_f32 v38, v38\n"
        "v_mfma_f32_16x16x32_bf16 v[24:27], v[132:135], v[240:243], v[24:27]\n"
        "s_waitcnt lgkmcnt(2)\n"
        "v_perm_b32 v44, v49, v48, s63\n"
        "v_perm_b32 v45, v51, v50, s63\n"
        "v_add_f32 v145, v36, v37\n"
        "v_mfma_f32_16x16x32_bf16 v[104:107], v[88:91], v[240:243], 0\n"
        "v_exp_f32 v39, v39\n"
        "v_mfma_f32_16x16x32_bf16 v[104:107], v[84:87], v[244:247], v[104:107]\n"
        "v_add_f32 v145, v145, v38\n"
        "v_perm_b32 v46, v25, v24, s63\n"
        "v_mfma_f32_16x16x32_bf16 v[104:107], v[150:153], v[248:251], v[104:107]\n"
        "s_waitcnt lgkmcnt(0)\n"
        "v_perm_b32 v47, v27, v26, s63\n"
        "v_add_f32 v145, v145, v39\n"
        "v_exp_f32 v140, v140\n"
        "v_mfma_f32_16x16x32_bf16 v[64:67], v[150:153], v[80:83], 0\n"
        "v_exp_f32 v141, v141\n"
        "v_mfma_f32_16x16x32_bf16 v[64:67], v[84:87], v[32:35], v[64:67]\n"
        "ds_read_b128 v[146:149], v0 offset:6176\n"
        "v_perm_b32 v116, v105, v104, s63\n"
        "v_mfma_f32_16x16x32_bf16 v[64:67], v[88:91], v[252:255], v[64:67]\n"
        "ds_read_b128 v[24:27], v0 offset:7200\n"
        "ds_read_b128 v[108:111], v0 offset:8224\n"
        "v_mfma_f32_16x16x32_bf16 v[100:103], v[132:135], v[252:255], 0\n"
        "v_perm_b32 v120, v107, v106, s63\n"
        "v_add_f32 v145, v145, v140\n"
        "v_mfma_f32_16x16x32_bf16 v[100:103], v[136:139], v[32:35], v[100:103]\n"
        "v_exp_f32 v142, v142\n"
        "v_mfma_f32_16x16x32_bf16 v[100:103], v[52:55], v[80:83], v[100:103]\n"
        "v_perm_b32 v94, v65, v64, s63\n"
        "v_perm_b32 v98, v67, v66, s63\n"
        "v_add_f32 v145, v145, v141\n"
        "v_mfma_f32_16x16x32_bf16 v[68:71], v[160:163], v[80:83], 0\n"
        "v_exp_f32 v143, v143\n"
        "v_mfma_f32_16x16x32_bf16 v[68:71], v[128:131], v[32:35], v[68:71]\n"
        "v_add_f32 v145, v145, v142\n"
        "v_add_f32 v145, v145, v143\n"
        "v_mfma_f32_16x16x32_bf16 v[68:71], v[124:127], v[252:255], v[68:71]\n"
        "s_waitcnt lgkmcnt(0)\n"
        "v_perm_b32 v106, v101, v100, s63\n"
        "v_perm_b32 v107, v103, v102, s63\n"
        "v_mul_f32 v156, v156, v72\n"
        "v_mul_f32 v157, v157, v73\n"
        "v_mfma_f32_16x16x32_bf16 v[32:35], v[124:127], v[146:149], 0\n"
        "v_mul_f32 v158, v158, v74\n"
        "v_mul_f32 v159, v159, v75\n"
        "v_mfma_f32_16x16x32_bf16 v[32:35], v[128:131], v[24:27], v[32:35]\n"
        "ds_read_b128 v[48:51], v0 offset:11312\n"
        "v_perm_b32 v104, v69, v68, s63\n"
        "v_mfma_f32_16x16x32_bf16 v[32:35], v[160:163], v[108:111], v[32:35]\n"
        "ds_read_b128 v[80:83], v0 offset:10288\n"
        "ds_read_b128 v[64:67], v0 offset:9264\n"
        "v_mfma_f32_16x16x32_bf16 v[100:103], v[52:55], v[108:111], 0\n"
        "v_perm_b32 v105, v71, v70, s63\n"
        "v_add_f32 v92, v92, v93\n"
        "v_mfma_f32_16x16x32_bf16 v[100:103], v[136:139], v[24:27], v[100:103]\n"
        "v_exp_f32 v156, v156\n"
        "v_mfma_f32_16x16x32_bf16 v[100:103], v[132:135], v[146:149], v[100:103]\n"
        "v_perm_b32 v28, v33, v32, s63\n"
        "v_perm_b32 v29, v35, v34, s63\n"
        "v_add_f32 v145, v145, v156\n"
        "v_mfma_f32_16x16x32_bf16 v[70:73], v[88:91], v[146:149], 0\n"
        "v_exp_f32 v157, v157\n"
        "v_mfma_f32_16x16x32_bf16 v[70:73], v[84:87], v[24:27], v[70:73]\n"
        "v_exp_f32 v158, v158\n"
        "v_mfma_f32_16x16x32_bf16 v[70:73], v[150:153], v[108:111], v[70:73]\n"
        "s_waitcnt lgkmcnt(0)\n"
        "v_perm_b32 v30, v101, v100, s63\n"
        "v_perm_b32 v31, v103, v102, s63\n"
        "v_exp_f32 v159, v159\n"
        "v_mfma_f32_16x16x32_bf16 v[32:35], v[150:153], v[48:51], 0\n"
        "v_add_f32 v145, v145, v157\n"
        "v_add_f32 v145, v145, v158\n"
        "v_mfma_f32_16x16x32_bf16 v[32:35], v[84:87], v[80:83], v[32:35]\n"
        "ds_read_b128 v[100:103], v0 offset:12352\n"
        "v_perm_b32 v144, v71, v70, s63\n"
        "v_mfma_f32_16x16x32_bf16 v[32:35], v[88:91], v[64:67], v[32:35]\n"
        "ds_read_b128 v[108:111], v0 offset:13376\n"
        "ds_read_b128 v[24:27], v0 offset:14400\n"
        "v_mfma_f32_16x16x32_bf16 v[68:71], v[132:135], v[64:67], 0\n"
        "v_perm_b32 v148, v73, v72, s63\n"
        "v_mul_f32 v92, v92, v76\n"
        "v_mfma_f32_16x16x32_bf16 v[68:71], v[136:139], v[80:83], v[68:71]\n"
        "v_add_f32 v145, v145, v159\n"
        "v_readlane_b32 s59, v123, 32\n"
        "v_mfma_f32_16x16x32_bf16 v[68:71], v[52:55], v[48:51], v[68:71]\n"
        "v_perm_b32 v117, v33, v32, s63\n"
        "v_perm_b32 v121, v35, v34, s63\n"
        "v_exp_f32 v92, v92\n"
        "v_mfma_f32_16x16x32_bf16 v[72:75], v[160:163], v[48:51], 0\n"
        "v_mul_f32 v154, s59, v154\n"
        "v_add_f32_dpp v145, v92, v145 quad_perm:[0,1,2,3] row_mask:0x4 bank_mask:0xf\n"
        "v_mfma_f32_16x16x32_bf16 v[72:75], v[128:131], v[80:83], v[72:75]\n"
        "v_perm_b32 v154, 0, v154, s63\n"
        "v_perm_b32 v50, v69, v68, s63\n"
        "v_mfma_f32_16x16x32_bf16 v[72:75], v[124:127], v[64:67], v[72:75]\n"
        "s_waitcnt lgkmcnt(0)\n"
        "v_perm_b32 v51, v71, v70, s63\n"
        "v_permlane32_swap_b32 v155, v145\n"
        "v_mfma_f32_16x16x32_bf16 v[80:83], v[124:127], v[100:103], 0\n"
        "v_add_f32 v155, v155, v145\n"
        "v_mov_b32 v149, v155\n"
        "v_mfma_f32_16x16x32_bf16 v[80:83], v[128:131], v[108:111], v[80:83]\n"
        "ds_read_b128 v[32:35], v0 offset:17488\n"
        "v_perm_b32 v48, v73, v72, s63\n"
        "v_mfma_f32_16x16x32_bf16 v[80:83], v[160:163], v[24:27], v[80:83]\n"
        "ds_read_b128 v[68:71], v0 offset:16464\n"
        "ds_read_b128 v[64:67], v0 offset:15440\n"
        "v_mfma_f32_16x16x32_bf16 v[76:79], v[52:55], v[24:27], 0\n"
        "v_perm_b32 v49, v75, v74, s63\n"
        "v_mfma_f32_16x16x32_bf16 v[76:79], v[136:139], v[108:111], v[76:79]\n"
        "v_permlane16_swap_b32 v155, v149\n"
        "v_mfma_f32_16x16x32_bf16 v[76:79], v[132:135], v[100:103], v[76:79]\n"
        "v_add_f32 v155, v155, v149\n"
        "v_rcp_f32 v147, v155\n"
        "v_mfma_f32_16x16x32_bf16 v[72:75], v[88:91], v[100:103], 0\n"
        "ds_read2_b32 v[122:123], v11 offset0:0 offset1:3\n"
        "v_min3_f32 v167, v167, v155, v147\n"
        "v_mfma_f32_16x16x32_bf16 v[72:75], v[84:87], v[108:111], v[72:75]\n"
        "ds_read_b64_tr_b16 v[118:119], v9 offset:0\n"
        "v_and_b32 v102, s28, v147\n"
        "v_mfma_f32_16x16x32_bf16 v[72:75], v[150:153], v[24:27], v[72:75]\n"
        "s_waitcnt lgkmcnt(2)\n"
        "ds_read2_b32 v[100:101], v11 offset0:1 offset1:4\n"
        "v_sub_f32 v149, v147, v102\n"
        "v_cvt_pk_bf16_f32 v103, v102, v149\n"
        "v_perm_b32 v108, v81, v80, s63\n"
        "v_mfma_f32_16x16x32_bf16 v[24:27], v[150:153], v[32:35], 0\n"
        "ds_read_b64_tr_b16 v[96:97], v9 offset:16\n"
        "v_perm_b32 v109, v83, v82, s63\n"
        "v_mfma_f32_16x16x32_bf16 v[24:27], v[84:87], v[68:71], v[24:27]\n"
        "ds_read2_b32 v[150:151], v11 offset0:2 offset1:5\n"
        "v_perm_b32 v110, v77, v76, s63\n"
        "v_mfma_f32_16x16x32_bf16 v[24:27], v[88:91], v[64:67], v[24:27]\n"
        "ds_read_b64_tr_b16 v[146:147], v9 offset:32\n"
        "v_perm_b32 v111, v79, v78, s63\n"
        "v_perm_b32 v95, v73, v72, s63\n"
        "v_mfma_f32_16x16x4_4b_bf16 v[76:91], v[56:57], v[102:103], 0 blgp:4\n"
        "v_perm_b32 v99, v75, v74, s63\n"
        "v_cmp_nle_f32 vcc, 0x2b800000, v167\n"
        "v_mov_b32_dpp v167, v19 row_shr:4 row_mask:0xf bank_mask:0xf bound_ctrl:0\n"
        "ds_write_b16 v1, v154\n"
        "v_perm_b32 v145, v25, v24, s63\n"
        "v_perm_b32 v149, v27, v26, s63\n"
        "s_andn2_b64 vcc, vcc, s[56:57]\n"
        "s_cbranch_scc1 CREDOA_%=\n"
        "v_mfma_f32_16x16x32_bf16 v[24:27], v[132:135], v[64:67], 0\n"
        "s_mov_b32 exec_hi, 0xffff\n"
        "v_add_u32 v10, 40064, v10\n"
        "s_mov_b32 exec_hi, -1\n"
        "v_mfma_f32_16x16x32_bf16 v[24:27], v[136:139], v[68:71], v[24:27]\n"
        "v_mul_f32 v58, v58, v76\n"
        "v_mul_f32 v59, v59, v77\n"
        "v_mfma_f32_16x16x32_bf16 v[24:27], v[52:55], v[32:35], v[24:27]\n"
        "s_add_u32 s31, s31, 1\n"
        "v_mul_f32 v60, v60, v78\n"
        "v_mul_f32 v61, v61, v79\n"
        "v_mul_f32 v112, v112, v80\n"
        "v_mul_f32 v113, v113, v81\n"
        "v_mfma_f32_16x16x32_bf16 v[52:55], v[160:163], v[32:35], 0\n"
        "s_cmp_ge_u32 s31, s71\n"
        "v_mul_f32 v114, v114, v82\n"
        "v_mul_f32 v115, v115, v83\n"
        "v_mfma_f32_16x16x32_bf16 v[52:55], v[128:131], v[68:71], v[52:55]\n"
        "s_cbranch_scc1 L34A_%=\n"
        "L35A_%=:\n"
        "v_mul_f32 v20, v20, v84\n"
        "v_perm_b32 v34, v25, v24, s63\n"
        "v_mfma_f32_16x16x32_bf16 v[52:55], v[124:127], v[64:67], v[52:55]\n"
        "s_add_u32 s16, s16, 256\n"
        "v_perm_b32 v35, v27, v26, s63\n"
        "v_mul_f32 v21, v21, v85\n"
        "v_mfma_f32_16x16x32_bf16 v[124:127], v[40:43], v[44:47], 0\n"
        "s_waitcnt lgkmcnt(0)\n"
        "v_mul_f32 v22, v22, v86\n"
        "v_mul_f32 v23, v23, v87\n"
        "v_smfmac_f32_16x16x64_bf16 v[124:127], v[164:167], v[116:123], v18\n"
        "s_cmp_ge_u32 s31, 20\n"
        "s_cselect_b32 s39, s22, s16\n"
        "ds_read_b64_tr_b16 v[118:119], v9 offset:48\n"
        "v_mfma_f32_16x16x4_4b_bf16 v[72:87], v[56:57], v[102:103], 0 blgp:6\n"
        "v_perm_b32 v32, v53, v52, s63\n"
        "v_perm_b32 v33, v55, v54, s63\n"
        "v_fma_f32 v62, v62, v88, v19\n"
        "v_perm_b32 v68, v59, v58, s63\n"
        "v_perm_b32 v69, v61, v60, s63\n"
        "v_perm_b32 v70, v113, v112, s63\n"
        "v_perm_b32 v71, v115, v114, s63\n"
        "v_perm_b32 v24, v21, v20, s63\n"
        "v_perm_b32 v26, v23, v22, s63\n"
        "v_mfma_f32_16x16x32_bf16 v[88:91], v[40:43], v[104:107], 0\n"
        "s_cmp_ge_u32 s31, 21\n"
        "v_perm_b32 v25, 0, v62, s63\n"
        "v_mul_f32 v36, v36, v72\n"
        "v_smfmac_f32_16x16x64_bf16 v[88:91], v[164:167], v[94:101], v18\n"
        "s_cselect_b32 s39, s23, s39\n"
        "ds_read_b64_tr_b16 v[96:97], v9 offset:64\n"
        "v_mfma_f32_16x16x32_bf16 v[56:59], v[40:43], v[28:31], 0\n"
        "s_mul_hi_u32 s38, s39, 58835169\n"
        "ds_read_b128 v[128:131], v2\n"
        "v_mul_f32 v37, v37, v73\n"
        "v_smfmac_f32_16x16x64_bf16 v[56:59], v[164:167], v[144:151], v18\n"
        "s_mul_i32 s39, s39, 1344\n"
        "ds_read_b64_tr_b16 v[146:147], v9 offset:80\n"
        "ds_read_b128 v[52:55], v3\n"
        "v_mfma_f32_16x16x32_bf16 v[112:115], v[40:43], v[48:51], 0\n"
        "s_waitcnt lgkmcnt(4)\n"
        "v_mul_f32 v38, v38, v74\n"
        "v_mul_f32 v39, v39, v75\n"
        "v_smfmac_f32_16x16x64_bf16 v[112:115], v[164:167], v[116:123], v18 abid:1\n"
        "s_mul_i32 s38, s38, 588672\n"
        "s_add_u32 s39, s39, s38\n"
        "v_mul_f32 v140, v140, v76\n"
        "v_cvt_pk_bf16_f32 v152, v124, v88\n"
        "v_mul_f32 v141, v141, v77\n"
        "v_mfma_f32_16x16x32_bf16 v[72:75], v[40:43], v[108:111], 0\n"
        "s_add_u32 s39, s39, s17\n"
        "v_mov_b64 v[60:61], 0\n"
        "s_waitcnt lgkmcnt(3)\n"
        "v_smfmac_f32_16x16x64_bf16 v[72:75], v[164:167], v[94:101], v18 abid:1\n"
        "s_mul_i32 s68, s31, 3\n"
        "v_mov_b64 v[62:63], 0\n"
        "v_cvt_pk_bf16_f32 v153, v56, v112\n"
        "v_mfma_f32_16x16x32_bf16 v[64:67], v[40:43], v[32:35], 0\n"
        "s_waitcnt lgkmcnt(1)\n"
        "v_mov_b64 v[20:21], 0\n"
        "v_mov_b64 v[22:23], 0\n"
        "v_smfmac_f32_16x16x64_bf16 v[64:67], v[164:167], v[144:151], v18 abid:1\n"
        "s_waitcnt lgkmcnt(0)\n"
        "s_bitcmp1_b32 s31, 0\n"
        "v_mov_b32_dpp v27, v19 row_shr:4 row_mask:0xf bank_mask:0xf bound_ctrl:0\n"
        "s_cselect_b32 s38, 3, 6\n"
        "v_smfmac_f32_16x16x64_bf16 v[60:63], v[128:131], v[44:51], v4\n"
        "s_add_u32 s68, s68, s38\n"
        "v_mov_b64 v[40:41], 0\n"
        "v_smfmac_f32_16x16x64_bf16 v[60:63], v[52:55], v[116:123], v4 abid:1\n"
        "s_add_u32 s37, s35, 0\n"
        "s_cmp_lt_u32 s31, s14\n"
        "ds_read_b64_tr_b16 v[118:119], v9 offset:3328\n"
        "v_mov_b64 v[42:43], 0\n"
        "v_smfmac_f32_16x16x64_bf16 v[20:23], v[128:131], v[104:111], v4\n"
        "s_cselect_b32 s68, s68, 0\n"
        "v_cvt_pk_bf16_f32 v154, v72, v64\n"
        "v_smfmac_f32_16x16x64_bf16 v[20:23], v[52:55], v[94:101], v4 abid:1\n"
        "s_and_b32 s38, s31, 7\n"
        "ds_read_b64_tr_b16 v[96:97], v9 offset:3344\n"
        "v_cvt_pk_bf16_f32 v160, v125, v89\n"
        "v_smfmac_f32_16x16x64_bf16 v[40:43], v[128:131], v[28:35], v4\n"
        "s_cselect_b32 s30, 1, -7\n"
        "v_cvt_pk_bf16_f32 v88, v126, v90\n"
        "v_smfmac_f32_16x16x64_bf16 v[40:43], v[52:55], v[144:151], v4 abid:1\n"
        "buffer_store_dwordx3 v[152:154], v12, s[8:11], s37 offen sc0 sc1 nt\n"
        "ds_read_b64_tr_b16 v[146:147], v9 offset:3360\n"
        "v_cvt_pk_bf16_f32 v164, v127, v91\n"
        "v_cvt_pk_bf16_f32 v161, v57, v113\n"
        "v_cvt_pk_bf16_f32 v89, v58, v114\n"
        "v_cvt_pk_bf16_f32 v165, v59, v115\n"
        "v_cvt_pk_bf16_f32 v162, v73, v65\n"
        "v_mfma_f32_16x16x32_bf16 v[152:155], v[68:71], v[44:47], 0\n"
        "s_waitcnt lgkmcnt(2)\n"
        "v_cvt_pk_bf16_f32 v90, v74, v66\n"
        "v_cvt_pk_bf16_f32 v166, v75, v67\n"
        "v_smfmac_f32_16x16x64_bf16 v[152:155], v[24:27], v[116:123], v18\n"
        "s_mul_i32 s29, s30, 192\n"
        "s_mul_i32 s30, s30, 392\n"
        "ds_read_b64_tr_b16 v[118:119], v9 offset:3376\n"
        "v_add_f32 v60, v60, v61\n"
        "v_add_f32 v62, v62, v63\n"
        "v_add_f32 v20, v20, v21\n"
        "v_mfma_f32_16x16x32_bf16 v[56:59], v[68:71], v[104:107], 0\n"
        "s_waitcnt lgkmcnt(2)\n"
        "v_add_f32 v22, v22, v23\n"
        "v_cvt_pk_bf16_f32 v112, v60, v20\n"
        "v_smfmac_f32_16x16x64_bf16 v[56:59], v[24:27], v[94:101], v18\n"
        "s_add_u32 s37, s35, 0\n"
        "s_cmp_eq_u32 s69, s18\n"
        "ds_read_b64_tr_b16 v[96:97], v9 offset:3392\n"
        "v_add_f32 v40, v40, v41\n"
        "v_add_f32 v42, v42, v43\n"
        "v_cvt_pk_bf16_f32 v113, v40, v62\n"
        "v_mfma_f32_16x16x32_bf16 v[72:75], v[68:71], v[28:31], 0\n"
        "s_waitcnt lgkmcnt(2)\n"
        "v_cvt_pk_bf16_f32 v114, v22, v42\n"
        "v_mul_f32 v142, v142, v78\n"
        "v_smfmac_f32_16x16x64_bf16 v[72:75], v[24:27], v[144:151], v18\n"
        "buffer_store_dwordx3 v[160:162], v15, s[8:11], s37 offen offset:768 sc0 sc1 nt\n"
        "ds_read_b64_tr_b16 v[146:147], v9 offset:3408\n"
        "s_cselect_b32 s34, -7488, s70\n"
        "v_mul_f32 v143, v143, v79\n"
        "v_mul_f32 v156, v156, v80\n"
        "v_mul_f32 v157, v157, v81\n"
        "v_mul_f32 v158, v158, v82\n"
        "v_mfma_f32_16x16x32_bf16 v[20:23], v[68:71], v[48:51], 0\n"
        "s_waitcnt lgkmcnt(2)\n"
        "s_add_u32 s69, s69, s34\n"
        "v_mul_f32 v159, v159, v83\n"
        "v_smfmac_f32_16x16x64_bf16 v[20:23], v[24:27], v[116:123], v18 abid:1\n"
        "s_add_u32 s37, s35, 98112\n"
        "ds_read_b64_tr_b16 v[118:119], v9 offset:6656\n"
        "v_fma_f32 v92, v92, v84, v19\n"
        "v_mfma_f32_16x16x32_bf16 v[84:87], v[68:71], v[108:111], 0\n"
        "s_waitcnt lgkmcnt(2)\n"
        "v_perm_b32 v80, v37, v36, s63\n"
        "v_perm_b32 v81, v39, v38, s63\n"
        "v_smfmac_f32_16x16x64_bf16 v[84:87], v[24:27], v[94:101], v18 abid:1\n"
        "ds_read_b64_tr_b16 v[96:97], v9 offset:6672\n"
        "v_perm_b32 v82, v141, v140, s63\n"
        "v_mfma_f32_16x16x32_bf16 v[60:63], v[68:71], v[32:35], 0\n"
        "s_waitcnt lgkmcnt(2)\n"
        "v_perm_b32 v83, v143, v142, s63\n"
        "v_perm_b32 v76, v157, v156, s63\n"
        "v_smfmac_f32_16x16x64_bf16 v[60:63], v[24:27], v[144:151], v18 abid:1\n"
        "buffer_store_dwordx3 v[88:90], v12, s[8:11], s37 offen offset:192 sc0 sc1 nt\n"
        "ds_read_b64_tr_b16 v[146:147], v9 offset:6688\n"
        "ds_read_b32 v167, v0 offset:21604\n"
        "v_perm_b32 v78, v159, v158, s63\n"
        "v_perm_b32 v77, 0, v92, s63\n"
        "v_mov_b32_dpp v79, v19 row_shr:4 row_mask:0xf bank_mask:0xf bound_ctrl:0\n"
        "v_mfma_f32_16x16x32_bf16 v[68:71], v[80:83], v[44:47], 0\n"
        "s_waitcnt lgkmcnt(3)\n"
        "v_writelane_b32 v10, s69, 56\n"
        "v_add_u32 v11, s30, v11\n"
        "v_smfmac_f32_16x16x64_bf16 v[68:71], v[76:79], v[116:123], v18\n"
        "s_add_u32 s37, s35, 98112\n"
        "ds_read_b64_tr_b16 v[118:119], v9 offset:6704\n"
        "v_mfma_f32_16x16x32_bf16 v[36:39], v[80:83], v[104:107], 0\n"
        "s_waitcnt lgkmcnt(3)\n"
        "v_smfmac_f32_16x16x64_bf16 v[36:39], v[76:79], v[94:101], v18\n"
        "ds_read_b64_tr_b16 v[96:97], v9 offset:6720\n"
        "v_mfma_f32_16x16x32_bf16 v[40:43], v[80:83], v[28:31], 0\n"
        "s_waitcnt lgkmcnt(3)\n"
        "s_waitcnt lgkmcnt(2)\n"
        "v_readfirstlane_b32 s40, v167\n"
        "v_smfmac_f32_16x16x64_bf16 v[40:43], v[76:79], v[144:151], v18\n"
        "buffer_store_dwordx3 v[164:166], v14, s[8:11], s37 offen offset:960 sc0 sc1 nt\n"
        "ds_read_b64_tr_b16 v[146:147], v9 offset:6736\n"
        "s_add_u32 s37, s35, 588672\n"
        "s_add_u32 s46, s37, 1344\n"
        "s_cmp_lt_i32 s40, s68\n"
        "s_mov_b64 exec, 1\n"
        "ds_inc_u32 v0, v1 offset:21608\n"
        "s_mov_b64 exec, -1\n"
        "s_cbranch_scc1 L38A_%=\n"
        "L39A_%=:\n"
        "ds_read_b128 v[124:127], v7 offset:40064\n"
        "v_mfma_f32_16x16x32_bf16 v[64:67], v[80:83], v[48:51], 0\n"
        "s_waitcnt lgkmcnt(4)\n"
        "ds_read_b128 v[132:135], v7 offset:43392\n"
        "v_smfmac_f32_16x16x64_bf16 v[64:67], v[76:79], v[116:123], v18 abid:1\n"
        "buffer_store_dwordx3 v[112:114], v12, s[44:47], s37 offen offset:1152 sc0 sc1 nt\n"
        "ds_read_b128 v[128:131], v7 offset:40128\n"
        "ds_read_b128 v[136:139], v7 offset:43456\n"
        "ds_read_b128 v[160:163], v7 offset:40192\n"
        "ds_read_b128 v[52:55], v7 offset:43520\n"
        "v_mfma_f32_16x16x32_bf16 v[104:107], v[80:83], v[108:111], 0\n"
        "s_waitcnt lgkmcnt(7)\n"
        "s_mov_b32 s36, s35\n"
        "v_smfmac_f32_16x16x64_bf16 v[104:107], v[76:79], v[94:101], v18 abid:1\n"
        "s_mov_b64 exec, s[54:55]\n"
        "v_add_u32 v16, s29, v16\n"
        "v_add_u32 v8, s34, v8\n"
        "s_mov_b64 exec, -1\n"
        "v_mfma_f32_16x16x32_bf16 v[88:91], v[80:83], v[32:35], 0\n"
        "s_mov_b32 s35, s39\n"
        "v_smfmac_f32_16x16x64_bf16 v[88:91], v[76:79], v[144:151], v18 abid:1\n"
        "s_mov_b64 exec, s[50:51]\n"
        "v_add_u32 v5, s29, v5\n"
        "v_add_u32 v6, s34, v6\n"
        "s_mov_b64 exec, -1\n"
        "s_setprio 0\n"
        "CNEXTA_%=:\n"
        "s_cmp_ge_u32 s31, s14\n"
        "s_cbranch_scc1 CEND_%=\n"
        "s_waitcnt lgkmcnt(5)\n"
        "v_mfma_f32_16x16x32_bf16 v[24:27], v[168:171], v[124:127], 0\n"
        "v_cvt_pk_bf16_f32 v156, v152, v56\n"
        "v_cvt_pk_bf16_f32 v157, v72, v20\n"
        "s_waitcnt lgkmcnt(3)\n"
        "v_mfma_f32_16x16x32_bf16 v[24:27], v[172:175], v[128:131], v[24:27]\n"
        "ds_read_b128 v[44:47], v16 offset:128\n"
        "v_cvt_pk_bf16_f32 v158, v84, v60\n"
        "s_waitcnt lgkmcnt(2)\n"
        "v_mfma_f32_16x16x32_bf16 v[24:27], v[176:179], v[160:163], v[24:27]\n"
        "ds_read_b128 v[48:51], v5\n"
        "v_cvt_pk_bf16_f32 v164, v153, v57\n"
        "v_mfma_f32_16x16x32_bf16 v[32:35], v[188:191], v[160:163], 0\n"
        "v_cvt_pk_bf16_f32 v165, v73, v21\n"
        "v_cvt_pk_bf16_f32 v166, v85, v61\n"
        "v_mfma_f32_16x16x32_bf16 v[32:35], v[184:187], v[128:131], v[32:35]\n"
        "v_cvt_pk_bf16_f32 v146, v154, v58\n"
        "v_cvt_pk_bf16_f32 v147, v74, v22\n"
        "v_mfma_f32_16x16x32_bf16 v[32:35], v[180:183], v[124:127], v[32:35]\n"
        "s_add_u32 s37, s36, 196224\n"
        "ds_read_b128 v[92:95], v7 offset:46720\n"
        "ds_read_b128 v[96:99], v7 offset:46784\n"
        "ds_read_b128 v[116:119], v7 offset:46848\n"
        "v_mfma_f32_16x16x32_bf16 v[76:79], v[192:195], v[124:127], 0\n"
        "v_perm_b32 v80, v25, v24, s63\n"
        "v_perm_b32 v81, v27, v26, s63\n"
        "v_mfma_f32_16x16x32_bf16 v[76:79], v[196:199], v[128:131], v[76:79]\n"
        "v_perm_b32 v82, v33, v32, s63\n"
        "v_perm_b32 v83, v35, v34, s63\n"
        "v_mfma_f32_16x16x32_bf16 v[76:79], v[200:203], v[160:163], v[76:79]\n"
        "buffer_store_dwordx3 v[156:158], v12, s[8:11], s37 offen offset:384 sc0 sc1 nt\n"
        "v_cvt_pk_bf16_f32 v148, v86, v62\n"
        "v_cvt_pk_bf16_f32 v150, v155, v59\n"
        "v_cvt_pk_bf16_f32 v151, v75, v23\n"
        "v_cvt_pk_bf16_f32 v152, v87, v63\n"
        "s_waitcnt lgkmcnt(3)\n"
        "v_mfma_f32_16x16x32_bf16 v[32:35], v[44:47], v[160:163], 0\n"
        "v_cvt_pk_bf16_f32 v72, v68, v36\n"
        "v_perm_b32 v84, v77, v76, s63\n"
        "v_smfmac_f32_16x16x64_bf16 v[32:35], v[48:51], v[124:131], v4\n"
        "s_add_u32 s37, s36, 196224\n"
        "v_perm_b32 v85, v79, v78, s63\n"
        "v_mfma_f32_16x16x32_bf16 v[24:27], v[44:47], v[52:55], 0\n"
        "v_cvt_pk_bf16_f32 v73, v40, v64\n"
        "v_cvt_pk_bf16_f32 v74, v104, v88\n"
        "v_smfmac_f32_16x16x64_bf16 v[24:27], v[48:51], v[132:139], v4\n"
        "s_waitcnt lgkmcnt(0)\n"
        "s_setprio 2\n"
        "v_cvt_pk_bf16_f32 v156, v69, v37\n"
        "v_cvt_pk_bf16_f32 v157, v41, v65\n"
        "v_mfma_f32_16x16x32_bf16 v[100:103], v[44:47], v[116:119], 0\n"
        "v_cvt_pk_bf16_f32 v158, v105, v89\n"
        "v_cvt_pk_bf16_f32 v76, v70, v38\n"
        "v_smfmac_f32_16x16x64_bf16 v[100:103], v[48:51], v[92:99], v4\n"
        "buffer_store_dwordx3 v[164:166], v13, s[8:11], s37 offen offset:1152 sc0 sc1 nt\n"
        "v_cvt_pk_bf16_f32 v77, v42, v66\n"
        "v_cvt_pk_bf16_f32 v78, v106, v90\n"
        "v_cvt_pk_bf16_f32 v88, v71, v39\n"
        "v_cvt_pk_bf16_f32 v89, v43, v67\n"
        "v_cvt_pk_bf16_f32 v90, v107, v91\n"
        "v_mfma_f32_16x16x32_bf16 v[20:23], v[212:215], v[160:163], 0\n"
        "s_add_u32 s37, s36, 294336\n"
        "ds_read_b64 v[154:155], v11 offset:192\n"
        "v_mfma_f32_16x16x32_bf16 v[20:23], v[208:211], v[128:131], v[20:23]\n"
        "ds_read_b32 v153, v10\n"
        "v_mfma_f32_16x16x32_bf16 v[20:23], v[204:207], v[124:127], v[20:23]\n"
        "v_mfma_f32_16x16x32_bf16 v[28:31], v[216:219], v[124:127], 0\n"
        "v_mfma_f32_16x16x32_bf16 v[28:31], v[220:223], v[128:131], v[28:31]\n"
        "v_mfma_f32_16x16x32_bf16 v[28:31], v[224:227], v[160:163], v[28:31]\n"
        "buffer_store_dwordx3 v[146:148], v12, s[8:11], s37 offen offset:576 sc0 sc1 nt\n"
        "s_add_u32 s37, s36, 392448\n"
        "s_waitcnt lgkmcnt(1)\n"
        "v_add_f32_dpp v155, v34, v35 quad_perm:[0,1,2,3] row_mask:0x1 bank_mask:0xf\n"
        "v_add_f32_dpp v154, v32, v33 quad_perm:[0,1,2,3] row_mask:0x1 bank_mask:0xf\n"
        "v_perm_b32 v86, v21, v20, s63\n"
        "v_mfma_f32_16x16x32_bf16 v[44:47], v[236:239], v[160:163], 0\n"
        "v_perm_b32 v87, v23, v22, s63\n"
        "v_perm_b32 v20, v29, v28, s63\n"
        "v_mfma_f32_16x16x32_bf16 v[44:47], v[232:235], v[128:131], v[44:47]\n"
        "v_perm_b32 v21, v31, v30, s63\n"
        "v_add_f32_dpp v155, v26, v27 quad_perm:[0,1,2,3] row_mask:0x2 bank_mask:0xf\n"
        "v_mfma_f32_16x16x32_bf16 v[44:47], v[228:231], v[124:127], v[44:47]\n"
        "buffer_store_dwordx3 v[150:152], v12, s[8:11], s37 offen sc0 sc1 nt\n"
        "v_add_f32_dpp v154, v24, v25 quad_perm:[0,1,2,3] row_mask:0x2 bank_mask:0xf\n"
        "v_add_f32_dpp v155, v102, v103 quad_perm:[0,1,2,3] row_mask:0x4 bank_mask:0xf\n"
        "v_mul_f32 v165, s52, v155\n"
        "v_add_f32_dpp v154, v100, v101 quad_perm:[0,1,2,3] row_mask:0x4 bank_mask:0xf\n"
        "s_waitcnt lgkmcnt(0)\n"
        "v_fma_f32 v165, -v165, v155, v153\n"
        "v_add_f32_e64 v165, s53, |v165|\n"
        "v_perm_b32 v23, v47, v46, s63\n"
        "v_perm_b32 v22, v45, v44, s63\n"
        "v_mfma_f32_16x16x32_bf16 v[100:103], v[124:127], v[80:83], 0\n"
        "v_rsq_f32 v153, v165\n"
        "v_mfma_f32_16x16x32_bf16 v[100:103], v[128:131], v[84:87], v[100:103]\n"
        "s_add_u32 s37, s36, 392448\n"
        "ds_read_b128 v[104:107], v8 offset:128\n"
        "v_and_b32 v56, s28, v153\n"
        "v_mfma_f32_16x16x32_bf16 v[100:103], v[160:163], v[20:23], v[100:103]\n"
        "ds_read2_b32 v[108:109], v6 offset0:0 offset1:2\n"
        "ds_read2_b32 v[110:111], v6 offset0:8 offset1:10\n"
        "v_mfma_f32_16x16x32_bf16 v[144:147], v[52:55], v[20:23], 0\n"
        "v_and_b32 v164, s28, v153\n"
        "v_sub_f32 v167, v153, v56\n"
        "v_mfma_f32_16x16x32_bf16 v[144:147], v[136:139], v[84:87], v[144:147]\n"
        "v_cvt_pk_bf16_f32 v57, v167, v56\n"
        "v_cvt_pk_bf16_f32 v165, v164, v167\n"
        "v_mfma_f32_16x16x32_bf16 v[144:147], v[132:135], v[80:83], v[144:147]\n"
        "buffer_store_dwordx3 v[72:74], v15, s[8:11], s37 offen offset:768 sc0 sc1 nt\n"
        "v_mul_f32 v154, v154, v153\n"
        "v_readlane_b32 s58, v153, 56\n"
        "s_mov_b64 exec, s[56:57]\n"
        "v_mov_b32 v154, 0xff800000\n"
        "s_mov_b64 exec, -1\n"
        "v_mfma_f32_16x16x4_4b_bf16 v[32:47], v[56:57], v[164:165], 0 blgp:4\n"
        "v_mul_f32 v154, s58, v154\n"
        "v_exp_f32 v154, v154\n"
        "v_mfma_f32_16x16x32_bf16 v[24:27], v[92:95], v[80:83], 0\n"
        "s_add_u32 s37, s36, 490560\n"
        "v_add_f32_dpp v167, v154, v154 row_ror:8 row_mask:0xf bank_mask:0xf\n"
        "v_mul_f32 v154, v154, v153\n"
        "v_mfma_f32_16x16x32_bf16 v[24:27], v[96:99], v[84:87], v[24:27]\n"
        "v_add_f32_dpp v167, v167, v167 row_ror:4 row_mask:0xf bank_mask:0xf\n"
        "v_mfma_f32_16x16x32_bf16 v[24:27], v[116:119], v[20:23], v[24:27]\n"
        "s_waitcnt lgkmcnt(0)\n"
        "v_add_f32_dpp v167, v167, v167 row_ror:2 row_mask:0xf bank_mask:0xf\n"
        "v_mfma_f32_16x16x32_bf16 v[148:151], v[104:107], v[20:23], 0\n"
        "v_mul_f32 v100, v100, v32\n"
        "v_mul_f32 v101, v101, v33\n"
        "v_smfmac_f32_16x16x64_bf16 v[148:151], v[108:111], v[80:87], v4\n"
        "buffer_store_dwordx3 v[156:158], v12, s[8:11], s37 offen offset:192 sc0 sc1 nt\n"
        "v_mul_f32 v102, v102, v34\n"
        "v_mul_f32 v103, v103, v35\n"
        "v_exp_f32 v100, v100\n"
        "v_mul_f32 v144, v144, v36\n"
        "v_mfma_f32_16x16x32_bf16 v[20:23], v[168:171], v[132:135], 0\n"
        "s_add_u32 s37, s36, 490560\n"
        "v_exp_f32 v101, v101\n"
        "v_mfma_f32_16x16x32_bf16 v[20:23], v[172:175], v[136:139], v[20:23]\n"
        "v_exp_f32 v102, v102\n"
        "v_mfma_f32_16x16x32_bf16 v[20:23], v[176:179], v[52:55], v[20:23]\n"
        "v_exp_f32 v103, v103\n"
        "v_add_f32 v151, v100, v101\n"
        "v_mfma_f32_16x16x32_bf16 v[28:31], v[188:191], v[52:55], 0\n"
        "v_add_f32 v151, v151, v102\n"
        "v_add_f32 v151, v151, v103\n"
        "v_mfma_f32_16x16x32_bf16 v[28:31], v[184:187], v[136:139], v[28:31]\n"
        "v_mul_f32 v145, v145, v37\n"
        "v_mul_f32 v146, v146, v38\n"
        "v_mfma_f32_16x16x32_bf16 v[28:31], v[180:183], v[132:135], v[28:31]\n"
        "buffer_store_dwordx3 v[76:78], v14, s[8:11], s37 offen offset:960 sc0 sc1 nt\n"
        "v_perm_b32 v68, v21, v20, s63\n"
        "v_perm_b32 v69, v23, v22, s63\n"
        "v_mul_f32 v147, v147, v39\n"
        "v_exp_f32 v144, v144\n"
        "v_mfma_f32_16x16x32_bf16 v[20:23], v[192:195], v[132:135], 0\n"
        "s_add_u32 s37, s36, 588672\n"
        "v_exp_f32 v145, v145\n"
        "v_mfma_f32_16x16x32_bf16 v[20:23], v[196:199], v[136:139], v[20:23]\n"
        "v_perm_b32 v70, v29, v28, s63\n"
        "v_perm_b32 v71, v31, v30, s63\n"
        "v_mfma_f32_16x16x32_bf16 v[20:23], v[200:203], v[52:55], v[20:23]\n"
        "buffer_store_dwordx3 v[88:90], v12, s[8:11], s37 offen offset:384 sc0 sc1 nt\n"
        "v_exp_f32 v146, v146\n"
        "v_exp_f32 v147, v147\n"
        "v_add_f32 v151, v151, v144\n"
        "v_add_f32 v151, v151, v145\n"
        "v_mfma_f32_16x16x32_bf16 v[28:31], v[212:215], v[52:55], 0\n"
        "v_add_f32 v151, v151, v146\n"
        "v_add_f32 v151, v151, v147\n"
        "v_mfma_f32_16x16x32_bf16 v[28:31], v[208:211], v[136:139], v[28:31]\n"
        "v_perm_b32 v72, v21, v20, s63\n"
        "v_perm_b32 v73, v23, v22, s63\n"
        "v_mfma_f32_16x16x32_bf16 v[28:31], v[204:207], v[132:135], v[28:31]\n"
        "v_mul_f32 v24, v24, v40\n"
        "v_mul_f32 v25, v25, v41\n"
        "v_mul_f32 v26, v26, v42\n"
        "v_mfma_f32_16x16x32_bf16 v[20:23], v[216:219], v[132:135], 0\n"
        "v_mul_f32 v27, v27, v43\n"
        "v_add_f32 v148, v148, v149\n"
        "v_mfma_f32_16x16x32_bf16 v[20:23], v[220:223], v[136:139], v[20:23]\n"
        "v_exp_f32 v24, v24\n"
        "v_mfma_f32_16x16x32_bf16 v[20:23], v[224:227], v[52:55], v[20:23]\n"
        "v_perm_b32 v74, v29, v28, s63\n"
        "v_perm_b32 v75, v31, v30, s63\n"
        "v_add_f32 v151, v151, v24\n"
        "v_mfma_f32_16x16x32_bf16 v[64:67], v[236:239], v[52:55], 0\n"
        "v_exp_f32 v25, v25\n"
        "v_mfma_f32_16x16x32_bf16 v[64:67], v[232:235], v[136:139], v[64:67]\n"
        "v_exp_f32 v26, v26\n"
        "v_mfma_f32_16x16x32_bf16 v[64:67], v[228:231], v[132:135], v[64:67]\n"
        "v_perm_b32 v48, v21, v20, s63\n"
        "v_perm_b32 v49, v23, v22, s63\n"
        "v_mfma_f32_16x16x4_4b_bf16 v[28:43], v[56:57], v[164:165], 0 blgp:5\n"
        "v_exp_f32 v27, v27\n"
        "v_add_f32 v151, v151, v25\n"
        "v_add_f32 v151, v151, v26\n"
        "v_mul_f32 v148, v148, v44\n"
        "v_exp_f32 v148, v148\n"
        "v_perm_b32 v50, v65, v64, s63\n"
        "v_perm_b32 v51, v67, v66, s63\n"
        "v_mfma_f32_16x16x32_bf16 v[58:61], v[124:127], v[68:71], 0\n"
        "v_add_f32 v151, v151, v27\n"
        "v_add_f32_dpp v167, v167, v167 row_ror:1 row_mask:0xf bank_mask:0xf\n"
        "v_mfma_f32_16x16x32_bf16 v[58:61], v[128:131], v[72:75], v[58:61]\n"
        "v_add_f32_dpp v151, v148, v151 quad_perm:[0,1,2,3] row_mask:0x4 bank_mask:0xf\n"
        "v_mfma_f32_16x16x32_bf16 v[58:61], v[160:163], v[48:51], v[58:61]\n"
        "v_mfma_f32_16x16x32_bf16 v[112:115], v[52:55], v[48:51], 0\n"
        "v_permlane32_swap_b32 v151, v167\n"
        "v_mfma_f32_16x16x32_bf16 v[112:115], v[136:139], v[72:75], v[112:115]\n"
        "v_add_f32 v151, v151, v167\n"
        "v_mov_b32 v167, v151\n"
        "v_mfma_f32_16x16x32_bf16 v[112:115], v[132:135], v[68:71], v[112:115]\n"
        "v_mfma_f32_16x16x32_bf16 v[20:23], v[92:95], v[68:71], 0\n"
        "v_permlane16_swap_b32 v151, v167\n"
        "v_mfma_f32_16x16x32_bf16 v[20:23], v[96:99], v[72:75], v[20:23]\n"
        "v_add_f32 v151, v151, v167\n"
        "v_mul_f32 v58, v58, v28\n"
        "v_mfma_f32_16x16x32_bf16 v[20:23], v[116:119], v[48:51], v[20:23]\n"
        "v_rcp_f32 v123, v151\n"
        "v_mul_f32 v59, v59, v29\n"
        "v_and_b32 v158, s28, v123\n"
        "v_mfma_f32_16x16x32_bf16 v[62:65], v[104:107], v[48:51], 0\n"
        "v_sub_f32 v152, v123, v158\n"
        "v_cvt_pk_bf16_f32 v159, v158, v152\n"
        "v_smfmac_f32_16x16x64_bf16 v[62:65], v[108:111], v[68:75], v4\n"
        "v_min3_f32 v167, v151, v123, v153\n"
        "v_mfma_f32_16x16x32_bf16 v[48:51], v[168:171], v[92:95], 0\n"
        "v_mul_f32 v60, v60, v30\n"
        "v_mul_f32 v61, v61, v31\n"
        "v_mfma_f32_16x16x32_bf16 v[48:51], v[172:175], v[96:99], v[48:51]\n"
        "v_exp_f32 v58, v58\n"
        "v_mfma_f32_16x16x32_bf16 v[48:51], v[176:179], v[116:119], v[48:51]\n"
        "v_exp_f32 v59, v59\n"
        "v_exp_f32 v60, v60\n"
        "v_mfma_f32_16x16x32_bf16 v[68:71], v[188:191], v[116:119], 0\n"
        "v_exp_f32 v61, v61\n"
        "v_mfma_f32_16x16x32_bf16 v[68:71], v[184:187], v[96:99], v[68:71]\n"
        "v_add_f32 v155, v58, v59\n"
        "v_add_f32 v155, v155, v60\n"
        "v_mfma_f32_16x16x32_bf16 v[68:71], v[180:183], v[92:95], v[68:71]\n"
        "v_perm_b32 v44, v49, v48, s63\n"
        "v_perm_b32 v45, v51, v50, s63\n"
        "v_add_f32 v155, v155, v61\n"
        "v_mfma_f32_16x16x32_bf16 v[28:31], v[192:195], v[92:95], 0\n"
        "v_mul_f32 v112, v112, v32\n"
        "v_mul_f32 v113, v113, v33\n"
        "v_mfma_f32_16x16x32_bf16 v[28:31], v[196:199], v[96:99], v[28:31]\n"
        "v_mul_f32 v114, v114, v34\n"
        "v_perm_b32 v46, v69, v68, s63\n"
        "v_mfma_f32_16x16x32_bf16 v[28:31], v[200:203], v[116:119], v[28:31]\n"
        "v_perm_b32 v47, v71, v70, s63\n"
        "v_mul_f32 v115, v115, v35\n"
        "v_exp_f32 v112, v112\n"
        "v_mfma_f32_16x16x32_bf16 v[68:71], v[212:215], v[116:119], 0\n"
        "v_exp_f32 v113, v113\n"
        "v_mfma_f32_16x16x32_bf16 v[68:71], v[208:211], v[96:99], v[68:71]\n"
        "v_exp_f32 v114, v114\n"
        "v_mfma_f32_16x16x32_bf16 v[68:71], v[204:207], v[92:95], v[68:71]\n"
        "v_perm_b32 v48, v29, v28, s63\n"
        "v_perm_b32 v49, v31, v30, s63\n"
        "v_add_f32 v155, v155, v112\n"
        "v_mfma_f32_16x16x32_bf16 v[64:67], v[216:219], v[92:95], 0\n"
        "v_exp_f32 v115, v115\n"
        "v_mfma_f32_16x16x32_bf16 v[64:67], v[220:223], v[96:99], v[64:67]\n"
        "v_add_f32 v155, v155, v113\n"
        "v_add_f32 v155, v155, v114\n"
        "v_mfma_f32_16x16x32_bf16 v[64:67], v[224:227], v[116:119], v[64:67]\n"
        "v_perm_b32 v50, v69, v68, s63\n"
        "v_perm_b32 v51, v71, v70, s63\n"
        "v_add_f32 v155, v155, v115\n"
        "v_mul_f32 v20, v20, v36\n"
        "v_mfma_f32_16x16x32_bf16 v[28:31], v[236:239], v[116:119], 0\n"
        "v_mul_f32 v21, v21, v37\n"
        "v_mul_f32 v22, v22, v38\n"
        "v_mfma_f32_16x16x32_bf16 v[28:31], v[232:235], v[96:99], v[28:31]\n"
        "v_perm_b32 v150, v65, v64, s63\n"
        "v_perm_b32 v151, v67, v66, s63\n"
        "v_mfma_f32_16x16x32_bf16 v[28:31], v[228:231], v[92:95], v[28:31]\n"
        "v_exp_f32 v20, v20\n"
        "v_mfma_f32_16x16x4_4b_bf16 v[76:91], v[56:57], v[158:159], 0 blgp:4\n"
        "v_exp_f32 v21, v21\n"
        "v_mul_f32 v23, v23, v39\n"
        "v_exp_f32 v22, v22\n"
        "v_exp_f32 v23, v23\n"
        "v_add_f32 v155, v155, v20\n"
        "v_add_f32 v155, v155, v21\n"
        "v_perm_b32 v152, v29, v28, s63\n"
        "v_perm_b32 v153, v31, v30, s63\n"
        "v_mfma_f32_16x16x32_bf16 v[36:39], v[124:127], v[44:47], 0\n"
        "v_add_f32 v155, v155, v22\n"
        "v_add_f32 v155, v155, v23\n"
        "v_mfma_f32_16x16x32_bf16 v[36:39], v[128:131], v[48:51], v[36:39]\n"
        "v_mul_f32 v100, v100, v76\n"
        "v_mul_f32 v101, v101, v77\n"
        "v_mfma_f32_16x16x32_bf16 v[36:39], v[160:163], v[150:153], v[36:39]\n"
        "v_mul_f32 v102, v102, v78\n"
        "v_mul_f32 v103, v103, v79\n"
        "v_mul_f32 v144, v144, v80\n"
        "v_mul_f32 v145, v145, v81\n"
        "v_mfma_f32_16x16x32_bf16 v[140:143], v[52:55], v[150:153], 0\n"
        "v_mul_f32 v146, v146, v82\n"
        "v_mul_f32 v147, v147, v83\n"
        "v_mfma_f32_16x16x32_bf16 v[140:143], v[136:139], v[48:51], v[140:143]\n"
        "v_mul_f32 v24, v24, v84\n"
        "v_mul_f32 v25, v25, v85\n"
        "v_mfma_f32_16x16x32_bf16 v[140:143], v[132:135], v[44:47], v[140:143]\n"
        "v_add_f32 v62, v62, v63\n"
        "v_mul_f32 v62, v62, v40\n"
        "v_mul_f32 v26, v26, v86\n"
        "v_mfma_f32_16x16x4_4b_bf16 v[64:79], v[56:57], v[164:165], 0 blgp:6\n"
        "v_exp_f32 v62, v62\n"
        "v_mul_f32 v27, v27, v87\n"
        "v_fma_f32 v148, v148, v88, v19\n"
        "v_add_f32_dpp v155, v62, v155 quad_perm:[0,1,2,3] row_mask:0x4 bank_mask:0xf\n"
        "v_perm_b32 v40, v101, v100, s63\n"
        "v_perm_b32 v41, v103, v102, s63\n"
        "v_perm_b32 v42, v145, v144, s63\n"
        "v_perm_b32 v43, v147, v146, s63\n"
        "v_perm_b32 v164, v25, v24, s63\n"
        "v_mfma_f32_16x16x32_bf16 v[156:159], v[92:95], v[44:47], 0\n"
        "v_perm_b32 v166, v27, v26, s63\n"
        "v_perm_b32 v165, 0, v148, s63\n"
        "v_mfma_f32_16x16x32_bf16 v[156:159], v[96:99], v[48:51], v[156:159]\n"
        "v_mul_f32 v36, v36, v64\n"
        "v_mul_f32 v37, v37, v65\n"
        "v_mfma_f32_16x16x32_bf16 v[156:159], v[116:119], v[150:153], v[156:159]\n"
        "v_mul_f32 v38, v38, v66\n"
        "v_mul_f32 v39, v39, v67\n"
        "v_mul_f32 v140, v140, v68\n"
        "v_mfma_f32_16x16x32_bf16 v[92:95], v[104:107], v[150:153], 0\n"
        "ds_read_b128 v[88:91], v17 offset:40064\n"
        "v_mul_f32 v141, v141, v69\n"
        "v_smfmac_f32_16x16x64_bf16 v[92:95], v[108:111], v[44:51], v4\n"
        "ds_read_b128 v[84:87], v17 offset:40128\n"
        "ds_read_b128 v[150:153], v17 offset:40192\n"
        "v_mfma_f32_16x16x32_bf16 v[48:51], v[124:127], v[240:243], 0\n"
        "v_exp_f32 v36, v36\n"
        "v_mfma_f32_16x16x32_bf16 v[48:51], v[128:131], v[244:247], v[48:51]\n"
        "ds_read_b128 v[80:83], v0 offset:5136\n"
        "v_mul_f32 v142, v142, v70\n"
        "v_mfma_f32_16x16x32_bf16 v[48:51], v[160:163], v[248:251], v[48:51]\n"
        "ds_read_b128 v[32:35], v0 offset:4112\n"
        "v_mul_f32 v143, v143, v71\n"
        "v_mfma_f32_16x16x32_bf16 v[24:27], v[52:55], v[248:251], 0\n"
        "v_exp_f32 v37, v37\n"
        "v_mfma_f32_16x16x32_bf16 v[24:27], v[136:139], v[244:247], v[24:27]\n"
        "v_exp_f32 v38, v38\n"
        "v_mfma_f32_16x16x32_bf16 v[24:27], v[132:135], v[240:243], v[24:27]\n"
        "s_waitcnt lgkmcnt(2)\n"
        "v_perm_b32 v44, v49, v48, s63\n"
        "v_perm_b32 v45, v51, v50, s63\n"
        "v_add_f32 v145, v36, v37\n"
        "v_mfma_f32_16x16x32_bf16 v[104:107], v[88:91], v[240:243], 0\n"
        "v_exp_f32 v39, v39\n"
        "v_mfma_f32_16x16x32_bf16 v[104:107], v[84:87], v[244:247], v[104:107]\n"
        "v_add_f32 v145, v145, v38\n"
        "v_perm_b32 v46, v25, v24, s63\n"
        "v_mfma_f32_16x16x32_bf16 v[104:107], v[150:153], v[248:251], v[104:107]\n"
        "s_waitcnt lgkmcnt(0)\n"
        "v_perm_b32 v47, v27, v26, s63\n"
        "v_add_f32 v145, v145, v39\n"
        "v_exp_f32 v140, v140\n"
        "v_mfma_f32_16x16x32_bf16 v[64:67], v[150:153], v[80:83], 0\n"
        "v_exp_f32 v141, v141\n"
        "v_mfma_f32_16x16x32_bf16 v[64:67], v[84:87], v[32:35], v[64:67]\n"
        "ds_read_b128 v[146:149], v0 offset:6176\n"
        "v_perm_b32 v116, v105, v104, s63\n"
        "v_mfma_f32_16x16x32_bf16 v[64:67], v[88:91], v[252:255], v[64:67]\n"
        "ds_read_b128 v[24:27], v0 offset:7200\n"
        "ds_read_b128 v[108:111], v0 offset:8224\n"
        "v_mfma_f32_16x16x32_bf16 v[100:103], v[132:135], v[252:255], 0\n"
        "v_perm_b32 v120, v107, v106, s63\n"
        "v_add_f32 v145, v145, v140\n"
        "v_mfma_f32_16x16x32_bf16 v[100:103], v[136:139], v[32:35], v[100:103]\n"
        "v_exp_f32 v142, v142\n"
        "v_mfma_f32_16x16x32_bf16 v[100:103], v[52:55], v[80:83], v[100:103]\n"
        "v_perm_b32 v94, v65, v64, s63\n"
        "v_perm_b32 v98, v67, v66, s63\n"
        "v_add_f32 v145, v145, v141\n"
        "v_mfma_f32_16x16x32_bf16 v[68:71], v[160:163], v[80:83], 0\n"
        "v_exp_f32 v143, v143\n"
        "v_mfma_f32_16x16x32_bf16 v[68:71], v[128:131], v[32:35], v[68:71]\n"
        "v_add_f32 v145, v145, v142\n"
        "v_add_f32 v145, v145, v143\n"
        "v_mfma_f32_16x16x32_bf16 v[68:71], v[124:127], v[252:255], v[68:71]\n"
        "s_waitcnt lgkmcnt(0)\n"
        "v_perm_b32 v106, v101, v100, s63\n"
        "v_perm_b32 v107, v103, v102, s63\n"
        "v_mul_f32 v156, v156, v72\n"
        "v_mul_f32 v157, v157, v73\n"
        "v_mfma_f32_16x16x32_bf16 v[32:35], v[124:127], v[146:149], 0\n"
        "v_mul_f32 v158, v158, v74\n"
        "v_mul_f32 v159, v159, v75\n"
        "v_mfma_f32_16x16x32_bf16 v[32:35], v[128:131], v[24:27], v[32:35]\n"
        "ds_read_b128 v[48:51], v0 offset:11312\n"
        "v_perm_b32 v104, v69, v68, s63\n"
        "v_mfma_f32_16x16x32_bf16 v[32:35], v[160:163], v[108:111], v[32:35]\n"
        "ds_read_b128 v[80:83], v0 offset:10288\n"
        "ds_read_b128 v[64:67], v0 offset:9264\n"
        "v_mfma_f32_16x16x32_bf16 v[100:103], v[52:55], v[108:111], 0\n"
        "v_perm_b32 v105, v71, v70, s63\n"
        "v_add_f32 v92, v92, v93\n"
        "v_mfma_f32_16x16x32_bf16 v[100:103], v[136:139], v[24:27], v[100:103]\n"
        "v_exp_f32 v156, v156\n"
        "v_mfma_f32_16x16x32_bf16 v[100:103], v[132:135], v[146:149], v[100:103]\n"
        "v_perm_b32 v28, v33, v32, s63\n"
        "v_perm_b32 v29, v35, v34, s63\n"
        "v_add_f32 v145, v145, v156\n"
        "v_mfma_f32_16x16x32_bf16 v[70:73], v[88:91], v[146:149], 0\n"
        "v_exp_f32 v157, v157\n"
        "v_mfma_f32_16x16x32_bf16 v[70:73], v[84:87], v[24:27], v[70:73]\n"
        "v_exp_f32 v158, v158\n"
        "v_mfma_f32_16x16x32_bf16 v[70:73], v[150:153], v[108:111], v[70:73]\n"
        "s_waitcnt lgkmcnt(0)\n"
        "v_perm_b32 v30, v101, v100, s63\n"
        "v_perm_b32 v31, v103, v102, s63\n"
        "v_exp_f32 v159, v159\n"
        "v_mfma_f32_16x16x32_bf16 v[32:35], v[150:153], v[48:51], 0\n"
        "v_add_f32 v145, v145, v157\n"
        "v_add_f32 v145, v145, v158\n"
        "v_mfma_f32_16x16x32_bf16 v[32:35], v[84:87], v[80:83], v[32:35]\n"
        "ds_read_b128 v[100:103], v0 offset:12352\n"
        "v_perm_b32 v144, v71, v70, s63\n"
        "v_mfma_f32_16x16x32_bf16 v[32:35], v[88:91], v[64:67], v[32:35]\n"
        "ds_read_b128 v[108:111], v0 offset:13376\n"
        "ds_read_b128 v[24:27], v0 offset:14400\n"
        "v_mfma_f32_16x16x32_bf16 v[68:71], v[132:135], v[64:67], 0\n"
        "v_perm_b32 v148, v73, v72, s63\n"
        "v_mul_f32 v92, v92, v76\n"
        "v_mfma_f32_16x16x32_bf16 v[68:71], v[136:139], v[80:83], v[68:71]\n"
        "v_add_f32 v145, v145, v159\n"
        "v_readlane_b32 s59, v123, 32\n"
        "v_mfma_f32_16x16x32_bf16 v[68:71], v[52:55], v[48:51], v[68:71]\n"
        "v_perm_b32 v117, v33, v32, s63\n"
        "v_perm_b32 v121, v35, v34, s63\n"
        "v_exp_f32 v92, v92\n"
        "v_mfma_f32_16x16x32_bf16 v[72:75], v[160:163], v[48:51], 0\n"
        "v_mul_f32 v154, s59, v154\n"
        "v_add_f32_dpp v145, v92, v145 quad_perm:[0,1,2,3] row_mask:0x4 bank_mask:0xf\n"
        "v_mfma_f32_16x16x32_bf16 v[72:75], v[128:131], v[80:83], v[72:75]\n"
        "v_perm_b32 v154, 0, v154, s63\n"
        "v_perm_b32 v50, v69, v68, s63\n"
        "v_mfma_f32_16x16x32_bf16 v[72:75], v[124:127], v[64:67], v[72:75]\n"
        "s_waitcnt lgkmcnt(0)\n"
        "v_perm_b32 v51, v71, v70, s63\n"
        "v_permlane32_swap_b32 v155, v145\n"
        "v_mfma_f32_16x16x32_bf16 v[80:83], v[124:127], v[100:103], 0\n"
        "v_add_f32 v155, v155, v145\n"
        "v_mov_b32 v149, v155\n"
        "v_mfma_f32_16x16x32_bf16 v[80:83], v[128:131], v[108:111], v[80:83]\n"
        "ds_read_b128 v[32:35], v0 offset:17488\n"
        "v_perm_b32 v48, v73, v72, s63\n"
        "v_mfma_f32_16x16x32_bf16 v[80:83], v[160:163], v[24:27], v[80:83]\n"
        "ds_read_b128 v[68:71], v0 offset:16464\n"
        "ds_read_b128 v[64:67], v0 offset:15440\n"
        "v_mfma_f32_16x16x32_bf16 v[76:79], v[52:55], v[24:27], 0\n"
        "v_perm_b32 v49, v75, v74, s63\n"
        "v_mfma_f32_16x16x32_bf16 v[76:79], v[136:139], v[108:111], v[76:79]\n"
        "v_permlane16_swap_b32 v155, v149\n"
        "v_mfma_f32_16x16x32_bf16 v[76:79], v[132:135], v[100:103], v[76:79]\n"
        "v_add_f32 v155, v155, v149\n"
        "v_rcp_f32 v147, v155\n"
        "v_mfma_f32_16x16x32_bf16 v[72:75], v[88:91], v[100:103], 0\n"
        "ds_read2_b32 v[122:123], v11 offset0:0 offset1:3\n"
        "v_min3_f32 v167, v167, v155, v147\n"
        "v_mfma_f32_16x16x32_bf16 v[72:75], v[84:87], v[108:111], v[72:75]\n"
        "ds_read_b64_tr_b16 v[118:119], v9 offset:40064\n"
        "v_and_b32 v102, s28, v147\n"
        "v_mfma_f32_16x16x32_bf16 v[72:75], v[150:153], v[24:27], v[72:75]\n"
        "s_waitcnt lgkmcnt(2)\n"
        "ds_read2_b32 v[100:101], v11 offset0:1 offset1:4\n"
        "v_sub_f32 v149, v147, v102\n"
        "v_cvt_pk_bf16_f32 v103, v102, v149\n"
        "v_perm_b32 v108, v81, v80, s63\n"
        "v_mfma_f32_16x16x32_bf16 v[24:27], v[150:153], v[32:35], 0\n"
        "ds_read_b64_tr_b16 v[96:97], v9 offset:40080\n"
        "v_perm_b32 v109, v83, v82, s63\n"
        "v_mfma_f32_16x16x32_bf16 v[24:27], v[84:87], v[68:71], v[24:27]\n"
        "ds_read2_b32 v[150:151], v11 offset0:2 offset1:5\n"
        "v_perm_b32 v110, v77, v76, s63\n"
        "v_mfma_f32_16x16x32_bf16 v[24:27], v[88:91], v[64:67], v[24:27]\n"
        "ds_read_b64_tr_b16 v[146:147], v9 offset:40096\n"
        "v_perm_b32 v111, v79, v78, s63\n"
        "v_perm_b32 v95, v73, v72, s63\n"
        "v_mfma_f32_16x16x4_4b_bf16 v[76:91], v[56:57], v[102:103], 0 blgp:4\n"
        "v_perm_b32 v99, v75, v74, s63\n"
        "v_cmp_nle_f32 vcc, 0x2b800000, v167\n"
        "v_mov_b32_dpp v167, v19 row_shr:4 row_mask:0xf bank_mask:0xf bound_ctrl:0\n"
        "ds_write_b16 v1, v154\n"
        "v_perm_b32 v145, v25, v24, s63\n"
        "v_perm_b32 v149, v27, v26, s63\n"
        "s_andn2_b64 vcc, vcc, s[56:57]\n"
        "s_cbranch_scc1 CREDOB_%=\n"
        "v_mfma_f32_16x16x32_bf16 v[24:27], v[132:135], v[64:67], 0\n"
        "s_mov_b32 exec_hi, 0xffff\n"
        "v_add_u32 v10, -40064, v10\n"
        "s_mov_b32 exec_hi, -1\n"
        "v_mfma_f32_16x16x32_bf16 v[24:27], v[136:139], v[68:71], v[24:27]\n"
        "v_mul_f32 v58, v58, v76\n"
        "v_mul_f32 v59, v59, v77\n"
        "v_mfma_f32_16x16x32_bf16 v[24:27], v[52:55], v[32:35], v[24:27]\n"
        "s_add_u32 s31, s31, 1\n"
        "v_mul_f32 v60, v60, v78\n"
        "v_mul_f32 v61, v61, v79\n"
        "v_mul_f32 v112, v112, v80\n"
        "v_mul_f32 v113, v113, v81\n"
        "v_mfma_f32_16x16x32_bf16 v[52:55], v[160:163], v[32:35], 0\n"
        "s_cmp_ge_u32 s31, s71\n"
        "v_mul_f32 v114, v114, v82\n"
        "v_mul_f32 v115, v115, v83\n"
        "v_mfma_f32_16x16x32_bf16 v[52:55], v[128:131], v[68:71], v[52:55]\n"
        "s_cbranch_scc1 L34B_%=\n"
        "L35B_%=:\n"
        "v_mul_f32 v20, v20, v84\n"
        "v_perm_b32 v34, v25, v24, s63\n"
        "v_mfma_f32_16x16x32_bf16 v[52:55], v[124:127], v[64:67], v[52:55]\n"
        "s_add_u32 s16, s16, 256\n"
        "v_perm_b32 v35, v27, v26, s63\n"
        "v_mul_f32 v21, v21, v85\n"
        "v_mfma_f32_16x16x32_bf16 v[124:127], v[40:43], v[44:47], 0\n"
        "s_waitcnt lgkmcnt(0)\n"
        "v_mul_f32 v22, v22, v86\n"
        "v_mul_f32 v23, v23, v87\n"
        "v_smfmac_f32_16x16x64_bf16 v[124:127], v[164:167], v[116:123], v18\n"
        "s_cmp_ge_u32 s31, 20\n"
        "s_cselect_b32 s39, s22, s16\n"
        "ds_read_b64_tr_b16 v[118:119], v9 offset:40112\n"
        "v_mfma_f32_16x16x4_4b_bf16 v[72:87], v[56:57], v[102:103], 0 blgp:6\n"
        "v_perm_b32 v32, v53, v52, s63\n"
        "v_perm_b32 v33, v55, v54, s63\n"
        "v_fma_f32 v62, v62, v88, v19\n"
        "v_perm_b32 v68, v59, v58, s63\n"
        "v_perm_b32 v69, v61, v60, s63\n"
        "v_perm_b32 v70, v113, v112, s63\n"
        "v_perm_b32 v71, v115, v114, s63\n"
        "v_perm_b32 v24, v21, v20, s63\n"
        "v_perm_b32 v26, v23, v22, s63\n"
        "v_mfma_f32_16x16x32_bf16 v[88:91], v[40:43], v[104:107], 0\n"
        "s_cmp_ge_u32 s31, 21\n"
        "v_perm_b32 v25, 0, v62, s63\n"
        "v_mul_f32 v36, v36, v72\n"
        "v_smfmac_f32_16x16x64_bf16 v[88:91], v[164:167], v[94:101], v18\n"
        "s_cselect_b32 s39, s23, s39\n"
        "ds_read_b64_tr_b16 v[96:97], v9 offset:40128\n"
        "v_mfma_f32_16x16x32_bf16 v[56:59], v[40:43], v[28:31], 0\n"
        "s_mul_hi_u32 s38, s39, 58835169\n"
        "ds_read_b128 v[128:131], v2\n"
        "v_mul_f32 v37, v37, v73\n"
        "v_smfmac_f32_16x16x64_bf16 v[56:59], v[164:167], v[144:151], v18\n"
        "s_mul_i32 s39, s39, 1344\n"
        "ds_read_b64_tr_b16 v[146:147], v9 offset:40144\n"
        "ds_read_b128 v[52:55], v3\n"
        "v_mfma_f32_16x16x32_bf16 v[112:115], v[40:43], v[48:51], 0\n"
        "s_waitcnt lgkmcnt(4)\n"
        "v_mul_f32 v38, v38, v74\n"
        "v_mul_f32 v39, v39, v75\n"
        "v_smfmac_f32_16x16x64_bf16 v[112:115], v[164:167], v[116:123], v18 abid:1\n"
        "s_mul_i32 s38, s38, 588672\n"
        "s_add_u32 s39, s39, s38\n"
        "v_mul_f32 v140, v140, v76\n"
        "v_cvt_pk_bf16_f32 v152, v124, v88\n"
        "v_mul_f32 v141, v141, v77\n"
        "v_mfma_f32_16x16x32_bf16 v[72:75], v[40:43], v[108:111], 0\n"
        "s_add_u32 s39, s39, s17\n"
        "v_mov_b64 v[60:61], 0\n"
        "s_waitcnt lgkmcnt(3)\n"
        "v_smfmac_f32_16x16x64_bf16 v[72:75], v[164:167], v[94:101], v18 abid:1\n"
        "s_mul_i32 s68, s31, 3\n"
        "v_mov_b64 v[62:63], 0\n"
        "v_cvt_pk_bf16_f32 v153, v56, v112\n"
        "v_mfma_f32_16x16x32_bf16 v[64:67], v[40:43], v[32:35], 0\n"
        "s_waitcnt lgkmcnt(1)\n"
        "v_mov_b64 v[20:21], 0\n"
        "v_mov_b64 v[22:23], 0\n"
        "v_smfmac_f32_16x16x64_bf16 v[64:67], v[164:167], v[144:151], v18 abid:1\n"
        "s_waitcnt lgkmcnt(0)\n"
        "s_bitcmp1_b32 s31, 0\n"
        "v_mov_b32_dpp v27, v19 row_shr:4 row_mask:0xf bank_mask:0xf bound_ctrl:0\n"
        "s_cselect_b32 s38, 3, 6\n"
        "v_smfmac_f32_16x16x64_bf16 v[60:63], v[128:131], v[44:51], v4\n"
        "s_add_u32 s68, s68, s38\n"
        "v_mov_b64 v[40:41], 0\n"
        "v_smfmac_f32_16x16x64_bf16 v[60:63], v[52:55], v[116:123], v4 abid:1\n"
        "s_add_u32 s37, s35, 0\n"
        "s_cmp_lt_u32 s31, s14\n"
        "ds_read_b64_tr_b16 v[118:119], v9 offset:43392\n"
        "v_mov_b64 v[42:43], 0\n"
        "v_smfmac_f32_16x16x64_bf16 v[20:23], v[128:131], v[104:111], v4\n"
        "s_cselect_b32 s68, s68, 0\n"
        "v_cvt_pk_bf16_f32 v154, v72, v64\n"
        "v_smfmac_f32_16x16x64_bf16 v[20:23], v[52:55], v[94:101], v4 abid:1\n"
        "s_and_b32 s38, s31, 7\n"
        "ds_read_b64_tr_b16 v[96:97], v9 offset:43408\n"
        "v_cvt_pk_bf16_f32 v160, v125, v89\n"
        "v_smfmac_f32_16x16x64_bf16 v[40:43], v[128:131], v[28:35], v4\n"
        "s_cselect_b32 s30, 1, -7\n"
        "v_cvt_pk_bf16_f32 v88, v126, v90\n"
        "v_smfmac_f32_16x16x64_bf16 v[40:43], v[52:55], v[144:151], v4 abid:1\n"
        "buffer_store_dwordx3 v[152:154], v12, s[8:11], s37 offen sc0 sc1 nt\n"
        "ds_read_b64_tr_b16 v[146:147], v9 offset:43424\n"
        "v_cvt_pk_bf16_f32 v164, v127, v91\n"
        "v_cvt_pk_bf16_f32 v161, v57, v113\n"
        "v_cvt_pk_bf16_f32 v89, v58, v114\n"
        "v_cvt_pk_bf16_f32 v165, v59, v115\n"
        "v_cvt_pk_bf16_f32 v162, v73, v65\n"
        "v_mfma_f32_16x16x32_bf16 v[152:155], v[68:71], v[44:47], 0\n"
        "s_waitcnt lgkmcnt(2)\n"
        "v_cvt_pk_bf16_f32 v90, v74, v66\n"
        "v_cvt_pk_bf16_f32 v166, v75, v67\n"
        "v_smfmac_f32_16x16x64_bf16 v[152:155], v[24:27], v[116:123], v18\n"
        "s_mul_i32 s29, s30, 192\n"
        "s_mul_i32 s30, s30, 392\n"
        "ds_read_b64_tr_b16 v[118:119], v9 offset:43440\n"
        "v_add_f32 v60, v60, v61\n"
        "v_add_f32 v62, v62, v63\n"
        "v_add_f32 v20, v20, v21\n"
        "v_mfma_f32_16x16x32_bf16 v[56:59], v[68:71], v[104:107], 0\n"
        "s_waitcnt lgkmcnt(2)\n"
        "v_add_f32 v22, v22, v23\n"
        "v_cvt_pk_bf16_f32 v112, v60, v20\n"
        "v_smfmac_f32_16x16x64_bf16 v[56:59], v[24:27], v[94:101], v18\n"
        "s_add_u32 s37, s35, 0\n"
        "s_cmp_eq_u32 s69, s18\n"
        "ds_read_b64_tr_b16 v[96:97], v9 offset:43456\n"
        "v_add_f32 v40, v40, v41\n"
        "v_add_f32 v42, v42, v43\n"
        "v_cvt_pk_bf16_f32 v113, v40, v62\n"
        "v_mfma_f32_16x16x32_bf16 v[72:75], v[68:71], v[28:31], 0\n"
        "s_waitcnt lgkmcnt(2)\n"
        "v_cvt_pk_bf16_f32 v114, v22, v42\n"
        "v_mul_f32 v142, v142, v78\n"
        "v_smfmac_f32_16x16x64_bf16 v[72:75], v[24:27], v[144:151], v18\n"
        "buffer_store_dwordx3 v[160:162], v15, s[8:11], s37 offen offset:768 sc0 sc1 nt\n"
        "ds_read_b64_tr_b16 v[146:147], v9 offset:43472\n"
        "s_cselect_b32 s34, -7488, s70\n"
        "v_mul_f32 v143, v143, v79\n"
        "v_mul_f32 v156, v156, v80\n"
        "v_mul_f32 v157, v157, v81\n"
        "v_mul_f32 v158, v158, v82\n"
        "v_mfma_f32_16x16x32_bf16 v[20:23], v[68:71], v[48:51], 0\n"
        "s_waitcnt lgkmcnt(2)\n"
        "s_add_u32 s69, s69, s34\n"
        "v_mul_f32 v159, v159, v83\n"
        "v_smfmac_f32_16x16x64_bf16 v[20:23], v[24:27], v[116:123], v18 abid:1\n"
        "s_add_u32 s37, s35, 98112\n"
        "ds_read_b64_tr_b16 v[118:119], v9 offset:46720\n"
        "v_fma_f32 v92, v92, v84, v19\n"
        "v_mfma_f32_16x16x32_bf16 v[84:87], v[68:71], v[108:111], 0\n"
        "s_waitcnt lgkmcnt(2)\n"
        "v_perm_b32 v80, v37, v36, s63\n"
        "v_perm_b32 v81, v39, v38, s63\n"
        "v_smfmac_f32_16x16x64_bf16 v[84:87], v[24:27], v[94:101], v18 abid:1\n"
        "ds_read_b64_tr_b16 v[96:97], v9 offset:46736\n"
        "v_perm_b32 v82, v141, v140, s63\n"
        "v_mfma_f32_16x16x32_bf16 v[60:63], v[68:71], v[32:35], 0\n"
        "s_waitcnt lgkmcnt(2)\n"
        "v_perm_b32 v83, v143, v142, s63\n"
        "v_perm_b32 v76, v157, v156, s63\n"
        "v_smfmac_f32_16x16x64_bf16 v[60:63], v[24:27], v[144:151], v18 abid:1\n"
        "buffer_store_dwordx3 v[88:90], v12, s[8:11], s37 offen offset:192 sc0 sc1 nt\n"
        "ds_read_b64_tr_b16 v[146:147], v9 offset:46752\n"
        "ds_read_b32 v167, v0 offset:21600\n"
        "v_perm_b32 v78, v159, v158, s63\n"
        "v_perm_b32 v77, 0, v92, s63\n"
        "v_mov_b32_dpp v79, v19 row_shr:4 row_mask:0xf bank_mask:0xf bound_ctrl:0\n"
        "v_mfma_f32_16x16x32_bf16 v[68:71], v[80:83], v[44:47], 0\n"
        "s_waitcnt lgkmcnt(3)\n"
        "v_writelane_b32 v10, s69, 56\n"
        "v_add_u32 v11, s30, v11\n"
        "v_smfmac_f32_16x16x64_bf16 v[68:71], v[76:79], v[116:123], v18\n"
        "s_add_u32 s37, s35, 98112\n"
        "ds_read_b64_tr_b16 v[118:119], v9 offset:46768\n"
        "v_mfma_f32_16x16x32_bf16 v[36:39], v[80:83], v[104:107], 0\n"
        "s_waitcnt lgkmcnt(3)\n"
        "v_smfmac_f32_16x16x64_bf16 v[36:39], v[76:79], v[94:101], v18\n"
        "ds_read_b64_tr_b16 v[96:97], v9 offset:46784\n"
        "v_mfma_f32_16x16x32_bf16 v[40:43], v[80:83], v[28:31], 0\n"
        "s_waitcnt lgkmcnt(3)\n"
        "s_waitcnt lgkmcnt(2)\n"
        "v_readfirstlane_b32 s40, v167\n"
        "v_smfmac_f32_16x16x64_bf16 v[40:43], v[76:79], v[144:151], v18\n"
        "buffer_store_dwordx3 v[164:166], v14, s[8:11], s37 offen offset:960 sc0 sc1 nt\n"
        "ds_read_b64_tr_b16 v[146:147], v9 offset:46800\n"
        "s_add_u32 s37, s35, 588672\n"
        "s_add_u32 s46, s37, 1344\n"
        "s_cmp_lt_i32 s40, s68\n"
        "s_mov_b64 exec, 1\n"
        "ds_inc_u32 v0, v1 offset:21612\n"
        "s_mov_b64 exec, -1\n"
        "s_cbranch_scc1 L38B_%=\n"
        "L39B_%=:\n"
        "ds_read_b128 v[124:127], v7 offset:0\n"
        "v_mfma_f32_16x16x32_bf16 v[64:67], v[80:83], v[48:51], 0\n"
        "s_waitcnt lgkmcnt(4)\n"
        "ds_read_b128 v[132:135], v7 offset:3328\n"
        "v_smfmac_f32_16x16x64_bf16 v[64:67], v[76:79], v[116:123], v18 abid:1\n"
        "buffer_store_dwordx3 v[112:114], v12, s[44:47], s37 offen offset:1152 sc0 sc1 nt\n"
        "ds_read_b128 v[128:131], v7 offset:64\n"
        "ds_read_b128 v[136:139], v7 offset:3392\n"
        "ds_read_b128 v[160:163], v7 offset:128\n"
        "ds_read_b128 v[52:55], v7 offset:3456\n"
        "v_mfma_f32_16x16x32_bf16 v[104:107], v[80:83], v[108:111], 0\n"
        "s_waitcnt lgkmcnt(7)\n"
        "s_mov_b32 s36, s35\n"
        "v_smfmac_f32_16x16x64_bf16 v[104:107], v[76:79], v[94:101], v18 abid:1\n"
        "s_mov_b64 exec, s[54:55]\n"
        "v_add_u32 v16, s29, v16\n"
        "v_add_u32 v8, s34, v8\n"
        "s_mov_b64 exec, -1\n"
        "v_mfma_f32_16x16x32_bf16 v[88:91], v[80:83], v[32:35], 0\n"
        "s_mov_b32 s35, s39\n"
        "v_smfmac_f32_16x16x64_bf16 v[88:91], v[76:79], v[144:151], v18 abid:1\n"
        "s_mov_b64 exec, s[50:51]\n"
        "v_add_u32 v5, s29, v5\n"
        "v_add_u32 v6, s34, v6\n"
        "s_mov_b64 exec, -1\n"
        "s_setprio 0\n"
        "CNEXTB_%=:\n"
        "s_cmp_ge_u32 s31, s14\n"
        "s_cbranch_scc1 CEND_%=\n"
        "s_cmp_gt_i32 s29, 0\n"
        "s_cbranch_scc1 CTOP_%=\n"
        "s_branch CFULL_%=\n"
        "CEND_%=:\n"
        "s_waitcnt lgkmcnt(0)\n"
        "s_nop 15\n"
        "v_cvt_pk_bf16_f32 v248, v152, v56\n"
        "v_cvt_pk_bf16_f32 v249, v72, v20\n"
        "v_cvt_pk_bf16_f32 v250, v84, v60\n"
        "s_add_u32 s37, s36, 196224\n"
        "buffer_store_dwordx3 v[248:250], v12, s[8:11], s37 offen offset:384 sc0 sc1 nt\n"
        "v_cvt_pk_bf16_f32 v252, v153, v57\n"
        "v_cvt_pk_bf16_f32 v253, v73, v21\n"
        "v_cvt_pk_bf16_f32 v254, v85, v61\n"
        "s_add_u32 s37, s36, 196224\n"
        "buffer_store_dwordx3 v[252:254], v13, s[8:11], s37 offen offset:1152 sc0 sc1 nt\n"
        "v_cvt_pk_bf16_f32 v248, v154, v58\n"
        "v_cvt_pk_bf16_f32 v249, v74, v22\n"
        "v_cvt_pk_bf16_f32 v250, v86, v62\n"
        "s_add_u32 s37, s36, 294336\n"
        "buffer_store_dwordx3 v[248:250], v12, s[8:11], s37 offen offset:576 sc0 sc1 nt\n"
        "v_cvt_pk_bf16_f32 v252, v155, v59\n"
        "v_cvt_pk_bf16_f32 v253, v75, v23\n"
        "v_cvt_pk_bf16_f32 v254, v87, v63\n"
        "s_add_u32 s37, s36, 392448\n"
        "buffer_store_dwordx3 v[252:254], v12, s[8:11], s37 offen sc0 sc1 nt\n"
        "v_cvt_pk_bf16_f32 v248, v68, v36\n"
        "v_cvt_pk_bf16_f32 v249, v40, v64\n"
        "v_cvt_pk_bf16_f32 v250, v104, v88\n"
        "s_add_u32 s37, s36, 392448\n"
        "v_cvt_pk_bf16_f32 v252, v69, v37\n"
        "v_cvt_pk_bf16_f32 v253, v41, v65\n"
        "v_cvt_pk_bf16_f32 v254, v105, v89\n"
        "buffer_store_dwordx3 v[248:250], v15, s[8:11], s37 offen offset:768 sc0 sc1 nt\n"
        "s_add_u32 s37, s36, 490560\n"
        "v_cvt_pk_bf16_f32 v246, v70, v38\n"
        "v_cvt_pk_bf16_f32 v247, v42, v66\n"
        "v_cvt_pk_bf16_f32 v248, v106, v90\n"
        "buffer_store_dwordx3 v[252:254], v12, s[8:11], s37 offen offset:192 sc0 sc1 nt\n"
        "s_add_u32 s37, s36, 490560\n"
        "buffer_store_dwordx3 v[246:248], v14, s[8:11], s37 offen offset:960 sc0 sc1 nt\n"
        "s_add_u32 s37, s36, 588672\n"
        "v_cvt_pk_bf16_f32 v252, v71, v39\n"
        "v_cvt_pk_bf16_f32 v253, v43, v67\n"
        "v_cvt_pk_bf16_f32 v254, v107, v91\n"
        "buffer_store_dwordx3 v[252:254], v12, s[8:11], s37 offen offset:384 sc0 sc1 nt\n"
        "s_nop 1\n"
        "s_endpgm\n"
        "L34A_%=:\n"
        "s_nop 7\n"
        "v_mov_b32 v153, 21600\n"
        "L32A_%=:\n"
        "ds_read_b32 v163, v153 offset:64\n"
        "s_waitcnt lgkmcnt(0)\n"
        "v_readfirstlane_b32 s40, v163\n"
        "s_nop 3\n"
        "s_cmp_ge_i32 s40, 1\n"
        "s_cbranch_scc1 L33A_%=\n"
        "s_sleep 1\n"
        "s_sub_u32 s41, s41, 1\n"
        "s_cbranch_scc0 L32A_%=\n"
        "s_endpgm\n"
        "L33A_%=:\n"
        "v_mbcnt_lo_u32_b32 v163, -1, 0\n"
        "v_lshl_add_u32 v153, v163, 2, v153\n"
        "ds_read_b32 v163, v153 offset:68\n"
        "s_mov_b32 s14, s40\n"
        "s_mov_b32 s71, -1\n"
        "s_waitcnt lgkmcnt(0)\n"
        "v_readlane_b32 s22, v163, 0\n"
        "v_readlane_b32 s23, v163, 1\n"
        "s_nop 3\n"
        "s_nop 1\n"
        "s_branch L35A_%=\n"
        "L38A_%=:\n"
        "s_nop 7\n"
        "L36A_%=:\n"
        "ds_read_b32 v167, v0 offset:21604\n"
        "s_waitcnt lgkmcnt(0)\n"
        "v_readfirstlane_b32 s40, v167\n"
        "s_nop 3\n"
        "s_cmp_ge_i32 s40, s68\n"
        "s_cbranch_scc1 L37A_%=\n"
        "s_sleep 1\n"
        "s_sub_u32 s41, s41, 1\n"
        "s_cbranch_scc0 L36A_%=\n"
        "s_endpgm\n"
        "L37A_%=:\n"
        "s_nop 1\n"
        "s_branch L39A_%=\n"
        "L34B_%=:\n"
        "s_nop 7\n"
        "v_mov_b32 v153, 21600\n"
        "L32B_%=:\n"
        "ds_read_b32 v163, v153 offset:64\n"
        "s_waitcnt lgkmcnt(0)\n"
        "v_readfirstlane_b32 s40, v163\n"
        "s_nop 3\n"
        "s_cmp_ge_i32 s40, 1\n"
        "s_cbranch_scc1 L33B_%=\n"
        "s_sleep 1\n"
        "s_sub_u32 s41, s41, 1\n"
        "s_cbranch_scc0 L32B_%=\n"
        "s_endpgm\n"
        "L33B_%=:\n"
        "v_mbcnt_lo_u32_b32 v163, -1, 0\n"
        "v_lshl_add_u32 v153, v163, 2, v153\n"
        "ds_read_b32 v163, v153 offset:68\n"
        "s_mov_b32 s14, s40\n"
        "s_mov_b32 s71, -1\n"
        "s_waitcnt lgkmcnt(0)\n"
        "v_readlane_b32 s22, v163, 0\n"
        "v_readlane_b32 s23, v163, 1\n"
        "s_nop 3\n"
        "s_nop 1\n"
        "s_branch L35B_%=\n"
        "L38B_%=:\n"
        "s_nop 7\n"
        "L36B_%=:\n"
        "ds_read_b32 v167, v0 offset:21600\n"
        "s_waitcnt lgkmcnt(0)\n"
        "v_readfirstlane_b32 s40, v167\n"
        "s_nop 3\n"
        "s_cmp_ge_i32 s40, s68\n"
        "s_cbranch_scc1 L37B_%=\n"
        "s_sleep 1\n"
        "s_sub_u32 s41, s41, 1\n"
        "s_cbranch_scc0 L36B_%=\n"
        "s_endpgm\n"
        "L37B_%=:\n"
        "s_nop 1\n"
        "s_branch L39B_%=\n"
        "CGRP_%=:\n"
        "s_setprio 3\n"
        "CGRQ_%=:\n"
        "s_waitcnt lgkmcnt(0)\n"
        "s_nop 6\n"
        "ds_read_u16 v166, v0 offset:20576\n"
        "s_lshr_b32 s61, s31, 3\n"
        "s_add_u32 s61, s61, 1\n"
        "L40_%=:\n"
        "ds_read_b32 v167, v0 offset:21660\n"
        "s_waitcnt lgkmcnt(0)\n"
        "v_readfirstlane_b32 s40, v167\n"
        "s_nop 3\n"
        "s_cmp_ge_i32 s40, s61\n"
        "s_cbranch_scc1 L41_%=\n"
        "s_sleep 1\n"
        "s_sub_u32 s41, s41, 1\n"
        "s_cbranch_scc0 L40_%=\n"
        "s_endpgm\n"
        "L41_%=:\n"
        "s_sub_u32 s60, s69, 102048\n"
        "v_add_u32 v166, s60, v166\n"
        "v_subrev_u32 v165, 8320, v166\n"
        "v_min_u32 v166, v166, v165\n"
        "v_add_u32 v166, 101856, v166\n"
        "s_mov_b32 exec_lo, 0xff00ff00\n"
        "s_mov_b32 exec_hi, 0xff00ff00\n"
        "v_mov_b32 v166, 18912\n"
        "s_mov_b64 exec, -1\n"
        "ds_read_b128 v[28:31], v166 offset:64\n"
        "ds_read_b128 v[24:27], v166 offset:128\n"
        "ds_read_b128 v[32:35], v166\n"
        "s_waitcnt lgkmcnt(1)\n"
        "s_waitcnt lgkmcnt(0)\n"
        "v_mfma_f32_16x16x32_bf16 v[44:47], v[168:171], v[32:35], 0\n"
        "ds_read_u16 v162, v0 offset:20578\n"
        "v_lshrrev_b32 v157, 8, v0\n"
        "v_mfma_f32_16x16x32_bf16 v[44:47], v[172:175], v[28:31], v[44:47]\n"
        "ds_read_u16 v166, v0 offset:20580\n"
        "v_mfma_f32_16x16x32_bf16 v[44:47], v[176:179], v[24:27], v[44:47]\n"
        "ds_read_u16 v167, v0 offset:20582\n"
        "ds_read_u16 v163, v0 offset:20588\n"
        "ds_read_u16 v151, v0 offset:20590\n"
        "v_mfma_f32_16x16x32_bf16 v[76:79], v[180:183], v[32:35], 0\n"
        "v_mfma_f32_16x16x32_bf16 v[76:79], v[184:187], v[28:31], v[76:79]\n"
        "v_mfma_f32_16x16x32_bf16 v[76:79], v[188:191], v[24:27], v[76:79]\n"
        "v_mfma_f32_16x16x32_bf16 v[48:51], v[192:195], v[32:35], 0\n"
        "s_waitcnt lgkmcnt(4)\n"
        "v_add_u32 v162, s60, v162\n"
        "v_subrev_u32 v165, 8320, v162\n"
        "v_mfma_f32_16x16x32_bf16 v[48:51], v[196:199], v[28:31], v[48:51]\n"
        "v_min_u32 v162, v162, v165\n"
        "v_add_u32 v162, 101856, v162\n"
        "v_mfma_f32_16x16x32_bf16 v[48:51], v[200:203], v[24:27], v[48:51]\n"
        "s_mov_b32 exec_lo, 0\n"
        "v_mov_b32 v162, 18912\n"
        "s_mov_b32 exec_lo, -1\n"
        "ds_read_b64 v[160:161], v162\n"
        "v_perm_b32 v52, v45, v44, s63\n"
        "v_perm_b32 v53, v47, v46, s63\n"
        "v_perm_b32 v54, v77, v76, s63\n"
        "v_perm_b32 v55, v79, v78, s63\n"
        "v_mfma_f32_16x16x32_bf16 v[44:47], v[204:207], v[32:35], 0\n"
        "v_perm_b32 v80, v49, v48, s63\n"
        "v_perm_b32 v81, v51, v50, s63\n"
        "v_mfma_f32_16x16x32_bf16 v[44:47], v[208:211], v[28:31], v[44:47]\n"
        "s_waitcnt lgkmcnt(1)\n"
        "ds_read_b64 v[164:165], v163\n"
        "v_add_u32 v166, s19, v166\n"
        "v_mfma_f32_16x16x32_bf16 v[44:47], v[212:215], v[24:27], v[44:47]\n"
        "ds_read_b64 v[158:159], v162 offset:16\n"
        "v_mad_i32_i24 v157, v157, -8, v166\n"
        "v_add_u32 v151, s19, v151\n"
        "v_add_u32 v167, s19, v167\n"
        "v_mfma_f32_16x16x32_bf16 v[48:51], v[216:219], v[32:35], 0\n"
        "ds_read_b64 v[148:149], v162 offset:32\n"
        "v_mfma_f32_16x16x32_bf16 v[48:51], v[220:223], v[28:31], v[48:51]\n"
        "v_mfma_f32_16x16x32_bf16 v[48:51], v[224:227], v[24:27], v[48:51]\n"
        "s_mov_b32 exec_lo, 0x00ff00ff\n"
        "s_mov_b32 exec_hi, 0x00ff00ff\n"
        "ds_write2_b32 v157, v52, v54 offset0:0 offset1:1\n"
        "ds_write2_b32 v157, v53, v55 offset0:16 offset1:17\n"
        "s_mov_b64 exec, -1\n"
        "v_mfma_f32_16x16x32_bf16 v[76:79], v[228:231], v[32:35], 0\n"
        "v_perm_b32 v82, v45, v44, s63\n"
        "v_perm_b32 v83, v47, v46, s63\n"
        "v_mfma_f32_16x16x32_bf16 v[76:79], v[232:235], v[28:31], v[76:79]\n"
        "v_perm_b32 v44, v49, v48, s63\n"
        "v_perm_b32 v45, v51, v50, s63\n"
        "v_mfma_f32_16x16x32_bf16 v[76:79], v[236:239], v[24:27], v[76:79]\n"
        "s_waitcnt lgkmcnt(2)\n"
        "s_mov_b32 exec_lo, 0x00ff00ff\n"
        "s_mov_b32 exec_hi, 0x00ff00ff\n"
        "ds_write2_b32 v157, v80, v82 offset0:8 offset1:9\n"
        "ds_write2_b32 v157, v81, v83 offset0:24 offset1:25\n"
        "s_mov_b64 exec, -1\n"
        "v_mfma_f32_4x4x4_16b_bf16 v[100:103], v[160:161], v[164:165], 0\n"
        "v_mfma_f32_4x4x4_16b_bf16 v[92:95], v[158:159], v[164:165], 0\n"
        "v_mfma_f32_4x4x4_16b_bf16 v[48:51], v[148:149], v[164:165], 0\n"
        "v_mov_b32 v96, 0x3f803f80\n"
        "v_mov_b32 v97, 0x3f803f80\n"
        "v_mov_b32 v98, 0x3f803f80\n"
        "v_mov_b32 v99, 0x3f803f80\n"
        "v_mfma_f32_16x16x32_bf16 v[124:127], v[32:35], v[240:243], 0\n"
        "v_perm_b32 v46, v77, v76, s63\n"
        "v_perm_b32 v47, v79, v78, s63\n"
        "v_mfma_f32_16x16x32_bf16 v[124:127], v[28:31], v[244:247], v[124:127]\n"
        "ds_read_b128 v[120:123], v0 offset:4112\n"
        "v_mfma_f32_16x16x32_bf16 v[124:127], v[24:27], v[248:251], v[124:127]\n"
        "ds_read_b128 v[108:111], v0 offset:5136\n"
        "v_mfma_f32_16x16x32_bf16 v[76:79], v[96:99], v[32:35], 0\n"
        "v_mfma_f32_16x16x32_bf16 v[76:79], v[96:99], v[28:31], v[76:79]\n"
        "ds_read_b128 v[116:119], v0 offset:6176\n"
        "v_mfma_f32_16x16x32_bf16 v[76:79], v[96:99], v[24:27], v[76:79]\n"
        "ds_read_b128 v[112:115], v0 offset:7200\n"
        "ds_read_b128 v[96:99], v0 offset:8224\n"
        "ds_read_b64 v[160:161], v162 offset:48\n"
        "v_perm_b32 v142, v100, v124, s63\n"
        "v_perm_b32 v144, v101, v125, s63\n"
        "v_perm_b32 v158, v102, v126, s63\n"
        "v_perm_b32 v156, v103, v127, s63\n"
        "v_mfma_f32_16x16x32_bf16 v[128:131], v[32:35], v[52:55], 0\n"
        "v_mfma_f32_16x16x32_bf16 v[128:131], v[28:31], v[80:83], v[128:131]\n"
        "ds_read_b64 v[148:149], v162 offset:64\n"
        "v_mfma_f32_16x16x32_bf16 v[128:131], v[24:27], v[44:47], v[128:131]\n"
        "s_waitcnt lgkmcnt(5)\n"
        "v_mfma_f32_16x16x32_bf16 v[80:83], v[32:35], v[252:255], 0\n"
        "ds_read_b64 v[146:147], v162 offset:80\n"
        "v_mfma_f32_16x16x32_bf16 v[80:83], v[28:31], v[120:123], v[80:83]\n"
        "ds_read_b128 v[52:55], v0 offset:9264\n"
        "v_mfma_f32_16x16x32_bf16 v[80:83], v[24:27], v[108:111], v[80:83]\n"
        "s_waitcnt lgkmcnt(3)\n"
        "ds_read_b128 v[108:111], v0 offset:10288\n"
        "ds_read_b128 v[100:103], v0 offset:11312\n"
        "s_mov_b32 vcc_lo, 0x22222222\n"
        "v_cndmask_b32 v141, v128, v129, vcc\n"
        "s_mov_b32 vcc_lo, 0x44444444\n"
        "v_cndmask_b32 v141, v141, v130, vcc\n"
        "v_mfma_f32_16x16x32_bf16 v[124:127], v[32:35], v[116:119], 0\n"
        "s_mov_b32 vcc_lo, 0x88888888\n"
        "v_cndmask_b32 v141, v141, v131, vcc\n"
        "v_perm_b32 v143, v92, v80, s63\n"
        "v_mfma_f32_16x16x32_bf16 v[124:127], v[28:31], v[112:115], v[124:127]\n"
        "ds_read_b128 v[132:135], v0 offset:12352\n"
        "v_perm_b32 v145, v93, v81, s63\n"
        "v_mfma_f32_16x16x32_bf16 v[124:127], v[24:27], v[96:99], v[124:127]\n"
        "ds_read_b128 v[136:139], v0 offset:13376\n"
        "ds_read_b128 v[128:131], v0 offset:14400\n"
        "v_perm_b32 v159, v94, v82, s63\n"
        "v_perm_b32 v157, v95, v83, s63\n"
        "v_mfma_f32_4x4x4_16b_bf16 v[112:115], v[160:161], v[164:165], 0\n"
        "s_waitcnt lgkmcnt(5)\n"
        "v_mfma_f32_4x4x4_16b_bf16 v[80:83], v[148:149], v[164:165], 0\n"
        "ds_read_b128 v[120:123], v0 offset:15440\n"
        "ds_read_b128 v[116:119], v0 offset:16464\n"
        "ds_read_b128 v[96:99], v0 offset:17488\n"
        "v_mfma_f32_4x4x4_16b_bf16 v[92:95], v[146:147], v[164:165], 0\n"
        "v_perm_b32 v150, v48, v124, s63\n"
        "v_perm_b32 v148, v49, v125, s63\n"
        "v_perm_b32 v164, v50, v126, s63\n"
        "s_waitcnt lgkmcnt(6)\n"
        "v_mfma_f32_16x16x32_bf16 v[160:163], v[32:35], v[52:55], 0\n"
        "v_perm_b32 v146, v51, v127, s63\n"
        "v_mfma_f32_16x16x32_bf16 v[160:163], v[28:31], v[108:111], v[160:163]\n"
        "v_mfma_f32_16x16x32_bf16 v[160:163], v[24:27], v[100:103], v[160:163]\n"
        "s_waitcnt lgkmcnt(3)\n"
        "s_mov_b64 exec, 0x00f0000f\n"
        "ds_write2_b32 v151, v141, v76 offset0:0 offset1:1\n"
        "s_mov_b64 exec, -1\n"
        "v_mfma_f32_16x16x32_bf16 v[48:51], v[32:35], v[132:135], 0\n"
        "ds_read_b128 v[124:127], v7\n"
        "v_mfma_f32_16x16x32_bf16 v[48:51], v[28:31], v[136:139], v[48:51]\n"
        "ds_read_b128 v[132:135], v7 offset:3328\n"
        "v_perm_b32 v151, v112, v160, s63\n"
        "v_mfma_f32_16x16x32_bf16 v[48:51], v[24:27], v[128:131], v[48:51]\n"
        "s_waitcnt lgkmcnt(3)\n"
        "ds_read_b128 v[128:131], v7 offset:64\n"
        "v_perm_b32 v149, v113, v161, s63\n"
        "v_perm_b32 v165, v114, v162, s63\n"
        "v_perm_b32 v147, v115, v163, s63\n"
        "v_mfma_f32_16x16x32_bf16 v[76:79], v[32:35], v[120:123], 0\n"
        "ds_read_b128 v[136:139], v7 offset:3392\n"
        "v_mfma_f32_16x16x32_bf16 v[76:79], v[28:31], v[116:119], v[76:79]\n"
        "ds_read_b128 v[160:163], v7 offset:128\n"
        "v_perm_b32 v120, v80, v48, s63\n"
        "v_mfma_f32_16x16x32_bf16 v[76:79], v[24:27], v[96:99], v[76:79]\n"
        "s_mov_b32 exec_lo, 0x00ff00ff\n"
        "s_mov_b32 exec_hi, 0x00ff00ff\n"
        "ds_write_b128 v166, v[44:47] offset:128\n"
        "s_mov_b64 exec, -1\n"
        "ds_read_b128 v[52:55], v7 offset:3456\n"
        "v_perm_b32 v122, v81, v49, s63\n"
        "v_perm_b32 v140, v82, v50, s63\n"
        "v_perm_b32 v118, v83, v51, s63\n"
        "v_perm_b32 v121, v92, v76, s63\n"
        "v_perm_b32 v123, v93, v77, s63\n"
        "v_perm_b32 v141, v94, v78, s63\n"
        "v_perm_b32 v119, v95, v79, s63\n"
        "s_mov_b32 exec_hi, 0\n"
        "ds_write2_b64 v167, v[142:143], v[144:145] offset0:0 offset1:49\n"
        "ds_write2_b64 v167, v[158:159], v[156:157] offset0:98 offset1:147\n"
        "s_mov_b32 exec_hi, -1\n"
        "s_mov_b32 exec_hi, 0\n"
        "ds_write2_b64 v167, v[150:151], v[148:149] offset0:1 offset1:50\n"
        "ds_write2_b64 v167, v[164:165], v[146:147] offset0:99 offset1:148\n"
        "s_mov_b32 exec_hi, -1\n"
        "s_mov_b32 exec_hi, 0\n"
        "ds_write2_b64 v167, v[120:121], v[122:123] offset0:2 offset1:51\n"
        "ds_write2_b64 v167, v[140:141], v[118:119] offset0:100 offset1:149\n"
        "s_mov_b32 exec_hi, -1\n"
        "s_waitcnt lgkmcnt(0)\n"
        "s_nop 6\n"
        "s_setprio 0\n"
        "s_setpc_b64 s[64:65]\n"
        "L52A_%=:\n"
        "s_nop 7\n"
        "v_mov_b32 v167, 21600\n"
        "L50A_%=:\n"
        "ds_read_b32 v166, v167 offset:64\n"
        "s_waitcnt lgkmcnt(0)\n"
        "v_readfirstlane_b32 s40, v166\n"
        "s_nop 3\n"
        "s_cmp_ge_i32 s40, 1\n"
        "s_cbranch_scc1 L51A_%=\n"
        "s_sleep 1\n"
        "s_sub_u32 s41, s41, 1\n"
        "s_cbranch_scc0 L50A_%=\n"
        "s_endpgm\n"
        "L51A_%=:\n"
        "v_mbcnt_lo_u32_b32 v166, -1, 0\n"
        "v_lshl_add_u32 v167, v166, 2, v167\n"
        "ds_read_b32 v166, v167 offset:68\n"
        "s_mov_b32 s14, s40\n"
        "s_mov_b32 s71, -1\n"
        "s_waitcnt lgkmcnt(0)\n"
        "v_readlane_b32 s22, v166, 0\n"
        "v_readlane_b32 s23, v166, 1\n"
        "s_nop 3\n"
        "s_nop 1\n"
        "s_branch L53A_%=\n"
        "L56A_%=:\n"
        "s_nop 7\n"
        "L54A_%=:\n"
        "ds_read_b32 v167, v0 offset:21604\n"
        "s_waitcnt lgkmcnt(0)\n"
        "v_readfirstlane_b32 s40, v167\n"
        "s_nop 3\n"
        "s_cmp_ge_i32 s40, s68\n"
        "s_cbranch_scc1 L55A_%=\n"
        "s_sleep 1\n"
        "s_sub_u32 s41, s41, 1\n"
        "s_cbranch_scc0 L54A_%=\n"
        "s_endpgm\n"
        "L55A_%=:\n"
        "s_nop 1\n"
        "s_branch L57A_%=\n"
        "L52B_%=:\n"
        "s_nop 7\n"
        "v_mov_b32 v167, 21600\n"
        "L50B_%=:\n"
        "ds_read_b32 v166, v167 offset:64\n"
        "s_waitcnt lgkmcnt(0)\n"
        "v_readfirstlane_b32 s40, v166\n"
        "s_nop 3\n"
        "s_cmp_ge_i32 s40, 1\n"
        "s_cbranch_scc1 L51B_%=\n"
        "s_sleep 1\n"
        "s_sub_u32 s41, s41, 1\n"
        "s_cbranch_scc0 L50B_%=\n"
        "s_endpgm\n"
        "L51B_%=:\n"
        "v_mbcnt_lo_u32_b32 v166, -1, 0\n"
        "v_lshl_add_u32 v167, v166, 2, v167\n"
        "ds_read_b32 v166, v167 offset:68\n"
        "s_mov_b32 s14, s40\n"
        "s_mov_b32 s71, -1\n"
        "s_waitcnt lgkmcnt(0)\n"
        "v_readlane_b32 s22, v166, 0\n"
        "v_readlane_b32 s23, v166, 1\n"
        "s_nop 3\n"
        "s_nop 1\n"
        "s_branch L53B_%=\n"
        "L56B_%=:\n"
        "s_nop 7\n"
        "L54B_%=:\n"
        "ds_read_b32 v167, v0 offset:21600\n"
        "s_waitcnt lgkmcnt(0)\n"
        "v_readfirstlane_b32 s40, v167\n"
        "s_nop 3\n"
        "s_cmp_ge_i32 s40, s68\n"
        "s_cbranch_scc1 L55B_%=\n"
        "s_sleep 1\n"
        "s_sub_u32 s41, s41, 1\n"
        "s_cbranch_scc0 L54B_%=\n"
        "s_endpgm\n"
        "L55B_%=:\n"
        "s_nop 1\n"
        "s_branch L57B_%=\n"
        "CREDOA_%=:\n"
        "s_waitcnt lgkmcnt(0)\n"
        "s_nop 15\n"
        "s_mov_b32 s36, 0x20000000\n"
        "ds_read_b128 v[124:127], v7 offset:0\n"
        "ds_read_b128 v[132:135], v7 offset:3328\n"
        "ds_read_b128 v[128:131], v7 offset:64\n"
        "ds_read_b128 v[136:139], v7 offset:3392\n"
        "ds_read_b128 v[160:163], v7 offset:128\n"
        "ds_read_b128 v[52:55], v7 offset:3456\n"
        "s_waitcnt lgkmcnt(0)\n"
        "s_branch MTOPA_%=\n"
        "CREDOB_%=:\n"
        "s_waitcnt lgkmcnt(0)\n"
        "s_nop 15\n"
        "s_mov_b32 s36, 0x20000000\n"
        "ds_read_b128 v[124:127], v7 offset:40064\n"
        "ds_read_b128 v[132:135], v7 offset:43392\n"
        "ds_read_b128 v[128:131], v7 offset:40128\n"
        "ds_read_b128 v[136:139], v7 offset:43456\n"
        "ds_read_b128 v[160:163], v7 offset:40192\n"
        "ds_read_b128 v[52:55], v7 offset:43520\n"
        "s_waitcnt lgkmcnt(0)\n"
        "s_branch MTOPB_%=\n"
        "MTOPA_%=:\n"
        "s_waitcnt lgkmcnt(5)\n"
        "v_mfma_f32_16x16x32_bf16 v[24:27], v[168:171], v[124:127], 0\n"
        "v_cvt_pk_bf16_f32 v112, v152, v56\n"
        "v_cvt_pk_bf16_f32 v113, v72, v20\n"
        "v_cvt_pk_bf16_f32 v114, v84, v60\n"
        "v_cvt_pk_bf16_f32 v116, v153, v57\n"
        "v_cvt_pk_bf16_f32 v117, v73, v21\n"
        "v_cvt_pk_bf16_f32 v118, v85, v61\n"
        "v_cvt_pk_bf16_f32 v120, v154, v58\n"
        "v_cvt_pk_bf16_f32 v121, v74, v22\n"
        "s_waitcnt lgkmcnt(3)\n"
        "v_mfma_f32_16x16x32_bf16 v[24:27], v[172:175], v[128:131], v[24:27]\n"
        "v_cvt_pk_bf16_f32 v122, v86, v62\n"
        "v_cvt_pk_bf16_f32 v140, v155, v59\n"
        "v_cvt_pk_bf16_f32 v141, v75, v23\n"
        "v_cvt_pk_bf16_f32 v142, v87, v63\n"
        "v_cvt_pk_bf16_f32 v144, v68, v36\n"
        "v_cvt_pk_bf16_f32 v145, v40, v64\n"
        "v_cvt_pk_bf16_f32 v146, v104, v88\n"
        "v_cvt_pk_bf16_f32 v148, v69, v37\n"
        "s_waitcnt lgkmcnt(1)\n"
        "v_mfma_f32_16x16x32_bf16 v[24:27], v[176:179], v[160:163], v[24:27]\n"
        "v_cvt_pk_bf16_f32 v149, v41, v65\n"
        "v_cvt_pk_bf16_f32 v150, v105, v89\n"
        "v_cvt_pk_bf16_f32 v152, v70, v38\n"
        "v_cvt_pk_bf16_f32 v153, v42, v66\n"
        "v_cvt_pk_bf16_f32 v154, v106, v90\n"
        "v_cvt_pk_bf16_f32 v156, v71, v39\n"
        "v_cvt_pk_bf16_f32 v157, v43, v67\n"
        "v_cvt_pk_bf16_f32 v158, v107, v91\n"
        "v_mfma_f32_16x16x32_bf16 v[32:35], v[188:191], v[160:163], 0\n"
        "v_perm_b32 v40, v25, v24, s63\n"
        "v_perm_b32 v41, v27, v26, s63\n"
        "v_mfma_f32_16x16x32_bf16 v[32:35], v[184:187], v[128:131], v[32:35]\n"
        "v_mfma_f32_16x16x32_bf16 v[32:35], v[180:183], v[124:127], v[32:35]\n"
        "s_add_u32 s37, s36, 196224\n"
        "ds_read_b128 v[28:31], v16 offset:128\n"
        "ds_read_b32 v159, v10\n"
        "v_mfma_f32_16x16x32_bf16 v[24:27], v[192:195], v[124:127], 0\n"
        "ds_read_b128 v[20:23], v5\n"
        "v_mfma_f32_16x16x32_bf16 v[24:27], v[196:199], v[128:131], v[24:27]\n"
        "ds_read_b64 v[164:165], v11 offset:192\n"
        "v_mfma_f32_16x16x32_bf16 v[24:27], v[200:203], v[160:163], v[24:27]\n"
        "buffer_store_dwordx3 v[112:114], v12, s[8:11], s37 offen offset:384 sc0 sc1 nt\n"
        "v_perm_b32 v42, v33, v32, s63\n"
        "v_perm_b32 v43, v35, v34, s63\n"
        "s_waitcnt lgkmcnt(3)\n"
        "v_mfma_f32_16x16x32_bf16 v[48:51], v[28:31], v[160:163], 0\n"
        "ds_read_b128 v[60:63], v7 offset:6784\n"
        "ds_read_i16 v167, v0 offset:20584\n"
        "ds_read_i16 v155, v0 offset:20586\n"
        "v_perm_b32 v44, v25, v24, s63\n"
        "v_perm_b32 v45, v27, v26, s63\n"
        "s_waitcnt lgkmcnt(3)\n"
        "v_smfmac_f32_16x16x64_bf16 v[48:51], v[20:23], v[124:131], v4\n"
        "s_add_u32 s37, s36, 196224\n"
        "ds_read_b128 v[32:35], v7 offset:6656\n"
        "ds_read_b128 v[36:39], v7 offset:6720\n"
        "v_mfma_f32_16x16x32_bf16 v[56:59], v[28:31], v[52:55], 0\n"
        "v_smfmac_f32_16x16x64_bf16 v[56:59], v[20:23], v[132:139], v4\n"
        "s_setprio 2\n"
        "s_waitcnt lgkmcnt(4)\n"
        "v_mfma_f32_16x16x32_bf16 v[24:27], v[28:31], v[60:63], 0\n"
        "buffer_store_dwordx3 v[116:118], v13, s[8:11], s37 offen offset:1152 sc0 sc1 nt\n"
        "v_add_f32_dpp v165, v50, v51 quad_perm:[0,1,2,3] row_mask:0x1 bank_mask:0xf\n"
        "s_waitcnt lgkmcnt(2)\n"
        "v_add_u32 v167, v9, v167\n"
        "v_add_f32_dpp v164, v48, v49 quad_perm:[0,1,2,3] row_mask:0x1 bank_mask:0xf\n"
        "v_add_f32_dpp v165, v58, v59 quad_perm:[0,1,2,3] row_mask:0x2 bank_mask:0xf\n"
        "v_add_u32 v155, v9, v155\n"
        "v_add_f32_dpp v164, v56, v57 quad_perm:[0,1,2,3] row_mask:0x2 bank_mask:0xf\n"
        "s_waitcnt lgkmcnt(0)\n"
        "v_smfmac_f32_16x16x64_bf16 v[24:27], v[20:23], v[32:39], v4\n"
        "v_mfma_f32_16x16x32_bf16 v[28:31], v[212:215], v[160:163], 0\n"
        "s_add_u32 s37, s36, 294336\n"
        "v_mfma_f32_16x16x32_bf16 v[28:31], v[208:211], v[128:131], v[28:31]\n"
        "v_mfma_f32_16x16x32_bf16 v[28:31], v[204:207], v[124:127], v[28:31]\n"
        "v_mfma_f32_16x16x32_bf16 v[20:23], v[216:219], v[124:127], 0\n"
        "buffer_store_dwordx3 v[120:122], v12, s[8:11], s37 offen offset:576 sc0 sc1 nt\n"
        "v_mfma_f32_16x16x32_bf16 v[20:23], v[220:223], v[128:131], v[20:23]\n"
        "v_mfma_f32_16x16x32_bf16 v[20:23], v[224:227], v[160:163], v[20:23]\n"
        "s_add_u32 s37, s36, 392448\n"
        "v_add_f32_dpp v165, v26, v27 quad_perm:[0,1,2,3] row_mask:0x4 bank_mask:0xf\n"
        "v_mul_f32 v166, s52, v165\n"
        "v_fma_f32 v166, -v166, v165, v159\n"
        "v_perm_b32 v46, v29, v28, s63\n"
        "v_perm_b32 v47, v31, v30, s63\n"
        "v_add_f32_e64 v166, s53, |v166|\n"
        "v_rsq_f32 v159, v166\n"
        "v_mfma_f32_16x16x32_bf16 v[56:59], v[236:239], v[160:163], 0\n"
        "s_mov_b32 exec_hi, 0xffff\n"
        "ds_write_b32 v10, v159\n"
        "s_mov_b32 exec_hi, -1\n"
        "v_perm_b32 v48, v21, v20, s63\n"
        "v_perm_b32 v49, v23, v22, s63\n"
        "ds_read_b32 v20, v167 offset:0\n"
        "v_add_f32_dpp v164, v24, v25 quad_perm:[0,1,2,3] row_mask:0x4 bank_mask:0xf\n"
        "v_mfma_f32_16x16x32_bf16 v[56:59], v[232:235], v[128:131], v[56:59]\n"
        "s_mov_b32 exec_hi, 0xffff\n"
        "v_add_u32 v10, 40064, v10\n"
        "s_mov_b32 exec_hi, -1\n"
        "ds_read_b32 v21, v167 offset:832\n"
        "ds_read_b32 v22, v167 offset:1664\n"
        "ds_read_b32 v23, v167 offset:2496\n"
        "ds_read_b32 v24, v167 offset:3328\n"
        "v_mul_f32 v164, v164, v159\n"
        "v_mfma_f32_16x16x32_bf16 v[56:59], v[228:231], v[124:127], v[56:59]\n"
        "buffer_store_dwordx3 v[140:142], v12, s[8:11], s37 offen sc0 sc1 nt\n"
        "ds_read_b32 v25, v167 offset:4160\n"
        "ds_read_b32 v26, v167 offset:4992\n"
        "ds_read_b32 v27, v167 offset:5824\n"
        "ds_read_b32 v28, v167 offset:6656\n"
        "ds_read_b32 v29, v167 offset:7488\n"
        "ds_read_b32 v30, v167 offset:8320\n"
        "ds_read_b32 v31, v167 offset:9152\n"
        "v_perm_b32 v50, v57, v56, s63\n"
        "v_perm_b32 v51, v59, v58, s63\n"
        "v_readlane_b32 s58, v159, 56\n"
        "v_mfma_f32_16x16x32_bf16 v[56:59], v[124:127], v[40:43], 0\n"
        "s_mov_b64 exec, s[56:57]\n"
        "v_mov_b32 v164, 0xff800000\n"
        "s_mov_b64 exec, -1\n"
        "ds_read_b32 v165, v155 offset:0\n"
        "ds_read_b32 v166, v155 offset:3328\n"
        "v_max_f32_dpp v151, v164, v164 row_ror:8 row_mask:0xf bank_mask:0xf\n"
        "v_mfma_f32_16x16x32_bf16 v[56:59], v[128:131], v[44:47], v[56:59]\n"
        "s_add_u32 s37, s36, 392448\n"
        "v_max_f32_dpp v151, v151, v151 row_ror:4 row_mask:0xf bank_mask:0xf\n"
        "ds_read_b32 v167, v155 offset:6656\n"
        "v_mfma_f32_16x16x32_bf16 v[56:59], v[160:163], v[48:51], v[56:59]\n"
        "v_max_f32_dpp v151, v151, v151 row_ror:2 row_mask:0xf bank_mask:0xf\n"
        "v_mfma_f32_16x16x32_bf16 v[72:75], v[52:55], v[48:51], 0\n"
        "v_mfma_f32_16x16x32_bf16 v[72:75], v[136:139], v[44:47], v[72:75]\n"
        "buffer_store_dwordx3 v[144:146], v15, s[8:11], s37 offen offset:768 sc0 sc1 nt\n"
        "v_max_f32_dpp v151, v151, v151 row_ror:1 row_mask:0xf bank_mask:0xf\n"
        "v_mfma_f32_16x16x32_bf16 v[72:75], v[132:135], v[40:43], v[72:75]\n"
        "ds_read_b128 v[88:91], v8 offset:128\n"
        "v_mfma_f32_16x16x32_bf16 v[68:71], v[32:35], v[40:43], 0\n"
        "s_add_u32 s37, s36, 490560\n"
        "ds_read2_b32 v[92:93], v6 offset0:0 offset1:2\n"
        "ds_read2_b32 v[94:95], v6 offset0:8 offset1:10\n"
        "s_waitcnt lgkmcnt(3)\n"
        "v_mul_f32 v56, v56, v20\n"
        "v_mul_f32 v57, v57, v21\n"
        "v_mul_f32 v58, v58, v22\n"
        "v_mul_f32 v59, v59, v23\n"
        "v_mul_f32 v72, v72, v24\n"
        "v_mfma_f32_16x16x32_bf16 v[68:71], v[36:39], v[44:47], v[68:71]\n"
        "v_max3_f32 v155, v56, v57, v58\n"
        "v_mul_f32 v73, v73, v25\n"
        "v_mul_f32 v74, v74, v26\n"
        "v_max3_f32 v155, v155, v59, v72\n"
        "v_mul_f32 v75, v75, v27\n"
        "v_max3_f32 v155, v155, v73, v74\n"
        "v_mfma_f32_16x16x32_bf16 v[68:71], v[60:63], v[48:51], v[68:71]\n"
        "s_waitcnt lgkmcnt(2)\n"
        "v_mfma_f32_16x16x32_bf16 v[76:79], v[88:91], v[48:51], 0\n"
        "buffer_store_dwordx3 v[148:150], v12, s[8:11], s37 offen offset:192 sc0 sc1 nt\n"
        "s_waitcnt lgkmcnt(0)\n"
        "v_smfmac_f32_16x16x64_bf16 v[76:79], v[92:95], v[40:47], v4\n"
        "v_mfma_f32_16x16x32_bf16 v[48:51], v[168:171], v[132:135], 0\n"
        "s_add_u32 s37, s36, 490560\n"
        "v_mfma_f32_16x16x32_bf16 v[48:51], v[172:175], v[136:139], v[48:51]\n"
        "v_mul_f32 v68, v68, v28\n"
        "v_mul_f32 v69, v69, v29\n"
        "v_mul_f32 v70, v70, v30\n"
        "v_max3_f32 v155, v155, v75, v68\n"
        "v_mul_f32 v71, v71, v31\n"
        "v_add_f32 v76, v76, v77\n"
        "v_mul_f32 v76, s58, v76\n"
        "v_max3_f32 v155, v155, v69, v70\n"
        "v_mfma_f32_16x16x32_bf16 v[48:51], v[176:179], v[52:55], v[48:51]\n"
        "s_mov_b64 exec, s[48:49]\n"
        "v_mov_b32 v76, 0xff800000\n"
        "s_mov_b64 exec, -1\n"
        "v_max3_f32 v155, v155, v71, v76\n"
        "v_mfma_f32_16x16x32_bf16 v[44:47], v[188:191], v[52:55], 0\n"
        "buffer_store_dwordx3 v[152:154], v14, s[8:11], s37 offen offset:960 sc0 sc1 nt\n"
        "v_permlane32_swap_b32 v155, v151\n"
        "v_max_f32 v155, v155, v151\n"
        "v_perm_b32 v40, v49, v48, s63\n"
        "v_perm_b32 v41, v51, v50, s63\n"
        "v_mov_b32 v154, v155\n"
        "v_mfma_f32_16x16x32_bf16 v[44:47], v[184:187], v[136:139], v[44:47]\n"
        "v_mfma_f32_16x16x32_bf16 v[44:47], v[180:183], v[132:135], v[44:47]\n"
        "s_add_u32 s37, s36, 588672\n"
        "v_permlane16_swap_b32 v155, v154\n"
        "v_max_f32 v155, v155, v154\n"
        "v_mov_b32 v151, v155\n"
        "v_mfma_f32_16x16x32_bf16 v[64:67], v[192:195], v[132:135], 0\n"
        "v_mfma_f32_16x16x32_bf16 v[64:67], v[196:199], v[136:139], v[64:67]\n"
        "v_permlane32_swap_b32 v155, v151\n"
        "v_mul_f32 v155, v155, v165\n"
        "v_perm_b32 v42, v45, v44, s63\n"
        "v_perm_b32 v43, v47, v46, s63\n"
        "v_fma_f32 v56, v56, v165, -v155\n"
        "v_fma_f32 v57, v57, v165, -v155\n"
        "v_fma_f32 v58, v58, v165, -v155\n"
        "v_mfma_f32_16x16x32_bf16 v[64:67], v[200:203], v[52:55], v[64:67]\n"
        "buffer_store_dwordx3 v[156:158], v12, s[8:11], s37 offen offset:384 sc0 sc1 nt\n"
        "v_exp_f32 v56, v56\n"
        "v_exp_f32 v57, v57\n"
        "v_exp_f32 v58, v58\n"
        "v_fma_f32 v59, v59, v165, -v155\n"
        "v_add_f32 v158, v56, v57\n"
        "v_exp_f32 v59, v59\n"
        "v_mfma_f32_16x16x32_bf16 v[48:51], v[212:215], v[52:55], 0\n"
        "v_perm_b32 v44, v65, v64, s63\n"
        "v_perm_b32 v45, v67, v66, s63\n"
        "v_fma_f32 v72, v72, v165, -v155\n"
        "v_add_f32 v158, v158, v58\n"
        "v_exp_f32 v72, v72\n"
        "v_fma_f32 v73, v73, v165, -v155\n"
        "v_add_f32 v158, v158, v59\n"
        "v_mfma_f32_16x16x32_bf16 v[48:51], v[208:211], v[136:139], v[48:51]\n"
        "v_exp_f32 v73, v73\n"
        "v_fma_f32 v74, v74, v165, -v155\n"
        "v_add_f32 v158, v158, v72\n"
        "v_exp_f32 v74, v74\n"
        "v_fma_f32 v75, v75, v165, -v155\n"
        "v_sub_f32 v164, v164, v151\n"
        "v_mfma_f32_16x16x32_bf16 v[48:51], v[204:207], v[132:135], v[48:51]\n"
        "v_add_f32 v158, v158, v73\n"
        "v_exp_f32 v75, v75\n"
        "v_fma_f32 v68, v68, v165, -v155\n"
        "v_mul_f32 v164, s58, v164\n"
        "v_add_f32 v158, v158, v74\n"
        "v_exp_f32 v68, v68\n"
        "v_mfma_f32_16x16x32_bf16 v[64:67], v[216:219], v[132:135], 0\n"
        "v_fma_f32 v69, v69, v165, -v155\n"
        "v_perm_b32 v46, v49, v48, s63\n"
        "v_perm_b32 v47, v51, v50, s63\n"
        "v_exp_f32 v164, v164\n"
        "v_add_f32 v158, v158, v75\n"
        "v_exp_f32 v69, v69\n"
        "v_mfma_f32_16x16x32_bf16 v[64:67], v[220:223], v[136:139], v[64:67]\n"
        "v_fma_f32 v70, v70, v165, -v155\n"
        "v_add_f32 v158, v158, v68\n"
        "v_exp_f32 v70, v70\n"
        "v_fma_f32 v71, v71, v165, -v155\n"
        "v_add_f32_dpp v157, v164, v164 row_ror:8 row_mask:0xf bank_mask:0xf\n"
        "v_add_f32 v158, v158, v69\n"
        "v_fma_f32 v76, v76, v165, -v155\n"
        "v_mfma_f32_16x16x32_bf16 v[64:67], v[224:227], v[52:55], v[64:67]\n"
        "v_exp_f32 v71, v71\n"
        "v_add_f32_dpp v157, v157, v157 row_ror:4 row_mask:0xf bank_mask:0xf\n"
        "v_add_f32 v158, v158, v70\n"
        "v_exp_f32 v76, v76\n"
        "v_add_f32_dpp v157, v157, v157 row_ror:2 row_mask:0xf bank_mask:0xf\n"
        "v_add_f32 v158, v158, v71\n"
        "v_mfma_f32_16x16x32_bf16 v[48:51], v[236:239], v[52:55], 0\n"
        "v_add_f32_dpp v157, v157, v157 row_ror:1 row_mask:0xf bank_mask:0xf\n"
        "v_perm_b32 v80, v65, v64, s63\n"
        "v_perm_b32 v81, v67, v66, s63\n"
        "v_add_f32 v158, v158, v76\n"
        "v_mul_f32 v56, v56, v20\n"
        "v_mul_f32 v57, v57, v21\n"
        "v_permlane32_swap_b32 v158, v157\n"
        "v_mfma_f32_16x16x32_bf16 v[48:51], v[232:235], v[136:139], v[48:51]\n"
        "v_add_f32 v158, v158, v157\n"
        "v_mov_b32 v165, v158\n"
        "v_mul_f32 v58, v58, v22\n"
        "v_mul_f32 v59, v59, v23\n"
        "v_permlane16_swap_b32 v158, v165\n"
        "v_add_f32 v158, v158, v165\n"
        "v_mul_f32 v72, v72, v24\n"
        "v_mfma_f32_16x16x32_bf16 v[48:51], v[228:231], v[132:135], v[48:51]\n"
        "v_rcp_f32 v158, v158\n"
        "v_mul_f32 v73, v73, v25\n"
        "v_mov_b32 v157, v158\n"
        "v_mul_f32 v74, v74, v26\n"
        "v_mul_f32 v75, v75, v27\n"
        "v_permlane32_swap_b32 v158, v157\n"
        "v_mfma_f32_16x16x32_bf16 v[64:67], v[124:127], v[40:43], 0\n"
        "v_mul_f32 v56, v56, v158\n"
        "v_perm_b32 v82, v49, v48, s63\n"
        "v_perm_b32 v83, v51, v50, s63\n"
        "v_mul_f32 v57, v57, v158\n"
        "v_mul_f32 v58, v58, v158\n"
        "v_mul_f32 v59, v59, v158\n"
        "v_mul_f32 v72, v72, v158\n"
        "v_mul_f32 v73, v73, v158\n"
        "v_mfma_f32_16x16x32_bf16 v[64:67], v[128:131], v[44:47], v[64:67]\n"
        "v_mul_f32 v74, v74, v158\n"
        "v_mul_f32 v75, v75, v158\n"
        "v_perm_b32 v48, v57, v56, s63\n"
        "v_perm_b32 v49, v59, v58, s63\n"
        "v_perm_b32 v50, v73, v72, s63\n"
        "v_perm_b32 v51, v75, v74, s63\n"
        "v_mul_f32 v68, v68, v28\n"
        "v_mul_f32 v69, v69, v29\n"
        "v_mfma_f32_16x16x32_bf16 v[64:67], v[160:163], v[80:83], v[64:67]\n"
        "v_mul_f32 v70, v70, v30\n"
        "v_mul_f32 v71, v71, v31\n"
        "v_mul_f32 v76, s58, v76\n"
        "v_mul_f32 v68, v68, v158\n"
        "v_mul_f32 v69, v69, v158\n"
        "v_mul_f32 v70, v70, v158\n"
        "v_mul_f32 v71, v71, v158\n"
        "v_fma_f32 v76, v76, v158, v19\n"
        "v_mfma_f32_16x16x32_bf16 v[84:87], v[52:55], v[80:83], 0\n"
        "v_mul_f32 v64, v64, v20\n"
        "v_mul_f32 v65, v65, v21\n"
        "v_mul_f32 v66, v66, v22\n"
        "v_mul_f32 v67, v67, v23\n"
        "v_max3_f32 v165, v64, v65, v66\n"
        "v_perm_b32 v56, v69, v68, s63\n"
        "v_perm_b32 v58, v71, v70, s63\n"
        "v_perm_b32 v57, 0, v76, s63\n"
        "v_mfma_f32_16x16x32_bf16 v[84:87], v[136:139], v[44:47], v[84:87]\n"
        "v_mul_f32 v164, v164, v159\n"
        "v_mul_f32 v164, v164, v157\n"
        "v_perm_b32 v164, 0, v164, s63\n"
        "ds_write_b16 v1, v164\n"
        "v_mfma_f32_16x16x32_bf16 v[84:87], v[132:135], v[40:43], v[84:87]\n"
        "v_mfma_f32_16x16x32_bf16 v[68:71], v[32:35], v[40:43], 0\n"
        "v_mfma_f32_16x16x32_bf16 v[68:71], v[36:39], v[44:47], v[68:71]\n"
        "v_mfma_f32_16x16x32_bf16 v[68:71], v[60:63], v[80:83], v[68:71]\n"
        "v_mfma_f32_16x16x32_bf16 v[72:75], v[88:91], v[80:83], 0\n"
        "v_smfmac_f32_16x16x64_bf16 v[72:75], v[92:95], v[40:47], v4\n"
        "v_mfma_f32_16x16x32_bf16 v[80:83], v[168:171], v[32:35], 0\n"
        "v_mfma_f32_16x16x32_bf16 v[80:83], v[172:175], v[36:39], v[80:83]\n"
        "v_mfma_f32_16x16x32_bf16 v[80:83], v[176:179], v[60:63], v[80:83]\n"
        "v_mul_f32 v84, v84, v24\n"
        "v_mul_f32 v85, v85, v25\n"
        "v_mul_f32 v86, v86, v26\n"
        "v_max3_f32 v165, v165, v67, v84\n"
        "v_mul_f32 v87, v87, v27\n"
        "v_add_f32 v72, v72, v73\n"
        "v_mul_f32 v68, v68, v28\n"
        "v_mul_f32 v72, s58, v72\n"
        "v_mfma_f32_16x16x32_bf16 v[40:43], v[188:191], v[60:63], 0\n"
        "s_mov_b64 exec, s[48:49]\n"
        "v_mov_b32 v72, 0xff800000\n"
        "s_mov_b64 exec, -1\n"
        "v_perm_b32 v76, v81, v80, s63\n"
        "v_perm_b32 v77, v83, v82, s63\n"
        "v_max3_f32 v165, v165, v85, v86\n"
        "v_mul_f32 v69, v69, v29\n"
        "v_mul_f32 v70, v70, v30\n"
        "v_max3_f32 v165, v165, v87, v68\n"
        "v_mfma_f32_16x16x32_bf16 v[40:43], v[184:187], v[36:39], v[40:43]\n"
        "v_mul_f32 v71, v71, v31\n"
        "v_max3_f32 v165, v165, v69, v70\n"
        "v_max3_f32 v165, v165, v71, v72\n"
        "v_mfma_f32_16x16x32_bf16 v[40:43], v[180:183], v[32:35], v[40:43]\n"
        "v_mfma_f32_16x16x32_bf16 v[100:103], v[192:195], v[32:35], 0\n"
        "v_mfma_f32_16x16x32_bf16 v[100:103], v[196:199], v[36:39], v[100:103]\n"
        "v_mfma_f32_16x16x32_bf16 v[100:103], v[200:203], v[60:63], v[100:103]\n"
        "v_mfma_f32_16x16x32_bf16 v[96:99], v[212:215], v[60:63], 0\n"
        "v_mfma_f32_16x16x32_bf16 v[96:99], v[208:211], v[36:39], v[96:99]\n"
        "v_mfma_f32_16x16x32_bf16 v[96:99], v[204:207], v[32:35], v[96:99]\n"
        "v_mfma_f32_16x16x32_bf16 v[44:47], v[216:219], v[32:35], 0\n"
        "v_mfma_f32_16x16x32_bf16 v[44:47], v[220:223], v[36:39], v[44:47]\n"
        "v_perm_b32 v78, v41, v40, s63\n"
        "v_perm_b32 v79, v43, v42, s63\n"
        "v_mfma_f32_16x16x32_bf16 v[44:47], v[224:227], v[60:63], v[44:47]\n"
        "v_perm_b32 v80, v101, v100, s63\n"
        "v_perm_b32 v81, v103, v102, s63\n"
        "v_mfma_f32_16x16x32_bf16 v[40:43], v[236:239], v[60:63], 0\n"
        "v_perm_b32 v82, v97, v96, s63\n"
        "v_perm_b32 v83, v99, v98, s63\n"
        "v_mfma_f32_16x16x32_bf16 v[40:43], v[232:235], v[36:39], v[40:43]\n"
        "v_mfma_f32_16x16x32_bf16 v[40:43], v[228:231], v[32:35], v[40:43]\n"
        "v_mfma_f32_16x16x32_bf16 v[108:111], v[124:127], v[76:79], 0\n"
        "v_perm_b32 v96, v45, v44, s63\n"
        "v_perm_b32 v97, v47, v46, s63\n"
        "v_mfma_f32_16x16x32_bf16 v[108:111], v[128:131], v[80:83], v[108:111]\n"
        "s_nop 3\n"
        "v_perm_b32 v98, v41, v40, s63\n"
        "v_perm_b32 v99, v43, v42, s63\n"
        "s_nop 1\n"
        "v_mfma_f32_16x16x32_bf16 v[108:111], v[160:163], v[96:99], v[108:111]\n"
        "v_mfma_f32_16x16x32_bf16 v[140:143], v[52:55], v[96:99], 0\n"
        "v_mfma_f32_16x16x32_bf16 v[140:143], v[136:139], v[80:83], v[140:143]\n"
        "v_mfma_f32_16x16x32_bf16 v[140:143], v[132:135], v[76:79], v[140:143]\n"
        "v_mfma_f32_16x16x32_bf16 v[100:103], v[32:35], v[76:79], 0\n"
        "v_mfma_f32_16x16x32_bf16 v[100:103], v[36:39], v[80:83], v[100:103]\n"
        "v_mfma_f32_16x16x32_bf16 v[100:103], v[60:63], v[96:99], v[100:103]\n"
        "v_mfma_f32_16x16x32_bf16 v[44:47], v[88:91], v[96:99], 0\n"
        "v_smfmac_f32_16x16x64_bf16 v[44:47], v[92:95], v[76:83], v4\n"
        "v_mul_f32 v108, v108, v20\n"
        "v_mul_f32 v109, v109, v21\n"
        "v_mul_f32 v110, v110, v22\n"
        "v_mul_f32 v111, v111, v23\n"
        "v_mul_f32 v140, v140, v24\n"
        "v_max3_f32 v159, v108, v109, v110\n"
        "v_mul_f32 v141, v141, v25\n"
        "v_mfma_f32_16x16x32_bf16 v[60:63], v[124:127], v[240:243], 0\n"
        "v_add_f32 v44, v44, v45\n"
        "v_mul_f32 v142, v142, v26\n"
        "v_max3_f32 v159, v159, v111, v140\n"
        "v_mul_f32 v143, v143, v27\n"
        "v_mul_f32 v100, v100, v28\n"
        "v_mul_f32 v44, s58, v44\n"
        "v_max3_f32 v159, v159, v141, v142\n"
        "v_mul_f32 v101, v101, v29\n"
        "v_mfma_f32_16x16x32_bf16 v[60:63], v[128:131], v[244:247], v[60:63]\n"
        "s_mov_b64 exec, s[48:49]\n"
        "v_mov_b32 v44, 0xff800000\n"
        "s_mov_b64 exec, -1\n"
        "v_mul_f32 v102, v102, v30\n"
        "v_max3_f32 v159, v159, v143, v100\n"
        "v_mul_f32 v103, v103, v31\n"
        "v_max3_f32 v159, v159, v101, v102\n"
        "v_max3_f32 v159, v159, v103, v44\n"
        "v_mfma_f32_16x16x32_bf16 v[60:63], v[160:163], v[248:251], v[60:63]\n"
        "ds_read_b128 v[32:35], v17 offset:0\n"
        "v_permlane32_swap_b32 v165, v159\n"
        "v_max_f32 v165, v165, v159\n"
        "v_mov_b32 v164, v165\n"
        "v_mfma_f32_16x16x32_bf16 v[76:79], v[52:55], v[248:251], 0\n"
        "ds_read_b128 v[36:39], v17 offset:64\n"
        "v_permlane16_swap_b32 v165, v164\n"
        "v_max_f32 v165, v165, v164\n"
        "v_perm_b32 v40, v61, v60, s63\n"
        "v_perm_b32 v41, v63, v62, s63\n"
        "v_mov_b32 v159, v165\n"
        "v_mfma_f32_16x16x32_bf16 v[76:79], v[136:139], v[244:247], v[76:79]\n"
        "ds_read_b128 v[60:63], v17 offset:128\n"
        "v_permlane32_swap_b32 v165, v159\n"
        "v_mul_f32 v165, v165, v166\n"
        "v_mul_f32 v159, v159, v167\n"
        "v_fma_f32 v64, v64, v166, -v165\n"
        "v_fma_f32 v65, v65, v166, -v165\n"
        "v_fma_f32 v108, v108, v167, -v159\n"
        "v_mfma_f32_16x16x32_bf16 v[76:79], v[132:135], v[240:243], v[76:79]\n"
        "s_waitcnt lgkmcnt(2)\n"
        "ds_read_b128 v[96:99], v0 offset:5136\n"
        "v_fma_f32 v109, v109, v167, -v159\n"
        "v_exp_f32 v64, v64\n"
        "v_exp_f32 v65, v65\n"
        "v_fma_f32 v66, v66, v166, -v165\n"
        "v_fma_f32 v110, v110, v167, -v159\n"
        "v_mfma_f32_16x16x32_bf16 v[80:83], v[32:35], v[240:243], 0\n"
        "s_waitcnt lgkmcnt(2)\n"
        "ds_read_b128 v[104:107], v0 offset:4112\n"
        "v_perm_b32 v42, v77, v76, s63\n"
        "v_perm_b32 v43, v79, v78, s63\n"
        "v_exp_f32 v108, v108\n"
        "v_exp_f32 v109, v109\n"
        "v_fma_f32 v67, v67, v166, -v165\n"
        "v_mfma_f32_16x16x32_bf16 v[80:83], v[36:39], v[244:247], v[80:83]\n"
        "s_waitcnt lgkmcnt(2)\n"
        "v_exp_f32 v66, v66\n"
        "v_exp_f32 v110, v110\n"
        "v_fma_f32 v111, v111, v167, -v159\n"
        "v_add_f32 v95, v64, v65\n"
        "v_exp_f32 v67, v67\n"
        "v_mfma_f32_16x16x32_bf16 v[80:83], v[60:63], v[248:251], v[80:83]\n"
        "s_waitcnt lgkmcnt(1)\n"
        "v_fma_f32 v84, v84, v166, -v165\n"
        "v_add_f32 v164, v108, v109\n"
        "v_exp_f32 v111, v111\n"
        "v_fma_f32 v140, v140, v167, -v159\n"
        "v_add_f32 v95, v95, v66\n"
        "v_exp_f32 v84, v84\n"
        "v_mfma_f32_16x16x32_bf16 v[112:115], v[60:63], v[96:99], 0\n"
        "s_waitcnt lgkmcnt(0)\n"
        "v_perm_b32 v76, v81, v80, s63\n"
        "v_perm_b32 v80, v83, v82, s63\n"
        "v_fma_f32 v85, v85, v166, -v165\n"
        "v_add_f32 v164, v164, v110\n"
        "v_exp_f32 v140, v140\n"
        "v_fma_f32 v141, v141, v167, -v159\n"
        "v_add_f32 v95, v95, v67\n"
        "v_mfma_f32_16x16x32_bf16 v[112:115], v[36:39], v[104:107], v[112:115]\n"
        "v_exp_f32 v85, v85\n"
        "v_fma_f32 v86, v86, v166, -v165\n"
        "v_add_f32 v164, v164, v111\n"
        "v_exp_f32 v141, v141\n"
        "v_fma_f32 v142, v142, v167, -v159\n"
        "v_add_f32 v95, v95, v84\n"
        "v_mfma_f32_16x16x32_bf16 v[112:115], v[32:35], v[252:255], v[112:115]\n"
        "v_exp_f32 v86, v86\n"
        "v_fma_f32 v87, v87, v166, -v165\n"
        "v_add_f32 v164, v164, v140\n"
        "v_exp_f32 v142, v142\n"
        "v_fma_f32 v143, v143, v167, -v159\n"
        "v_add_f32 v95, v95, v85\n"
        "v_mfma_f32_16x16x32_bf16 v[120:123], v[132:135], v[252:255], 0\n"
        "v_exp_f32 v87, v87\n"
        "v_perm_b32 v88, v113, v112, s63\n"
        "v_perm_b32 v92, v115, v114, s63\n"
        "v_fma_f32 v68, v68, v166, -v165\n"
        "v_add_f32 v164, v164, v141\n"
        "v_exp_f32 v143, v143\n"
        "v_mfma_f32_16x16x32_bf16 v[120:123], v[136:139], v[104:107], v[120:123]\n"
        "v_fma_f32 v100, v100, v167, -v159\n"
        "v_add_f32 v95, v95, v86\n"
        "v_exp_f32 v68, v68\n"
        "v_fma_f32 v69, v69, v166, -v165\n"
        "v_add_f32 v164, v164, v142\n"
        "v_exp_f32 v100, v100\n"
        "v_mfma_f32_16x16x32_bf16 v[120:123], v[52:55], v[96:99], v[120:123]\n"
        "ds_read_b128 v[144:147], v0 offset:6176\n"
        "v_fma_f32 v101, v101, v167, -v159\n"
        "v_add_f32 v95, v95, v87\n"
        "v_exp_f32 v69, v69\n"
        "v_fma_f32 v70, v70, v166, -v165\n"
        "v_add_f32 v164, v164, v143\n"
        "v_fma_f32 v102, v102, v167, -v159\n"
        "v_mfma_f32_16x16x32_bf16 v[148:151], v[160:163], v[96:99], 0\n"
        "ds_read_b128 v[116:119], v0 offset:7200\n"
        "v_perm_b32 v98, v121, v120, s63\n"
        "v_perm_b32 v99, v123, v122, s63\n"
        "v_exp_f32 v101, v101\n"
        "v_add_f32 v95, v95, v68\n"
        "v_exp_f32 v70, v70\n"
        "v_mfma_f32_16x16x32_bf16 v[148:151], v[128:131], v[104:107], v[148:151]\n"
        "ds_read_b128 v[112:115], v0 offset:8224\n"
        "v_fma_f32 v71, v71, v166, -v165\n"
        "v_add_f32 v164, v164, v100\n"
        "v_exp_f32 v102, v102\n"
        "v_fma_f32 v103, v103, v167, -v159\n"
        "v_add_f32 v95, v95, v69\n"
        "v_fma_f32 v72, v72, v166, -v165\n"
        "v_mfma_f32_16x16x32_bf16 v[148:151], v[124:127], v[252:255], v[148:151]\n"
        "s_waitcnt lgkmcnt(2)\n"
        "v_exp_f32 v71, v71\n"
        "v_add_f32 v164, v164, v101\n"
        "v_exp_f32 v103, v103\n"
        "v_fma_f32 v44, v44, v167, -v159\n"
        "v_add_f32 v95, v95, v70\n"
        "v_add_f32 v164, v164, v102\n"
        "v_mfma_f32_16x16x32_bf16 v[120:123], v[124:127], v[144:147], 0\n"
        "s_waitcnt lgkmcnt(1)\n"
        "v_perm_b32 v96, v149, v148, s63\n"
        "v_perm_b32 v97, v151, v150, s63\n"
        "v_exp_f32 v72, v72\n"
        "v_exp_f32 v44, v44\n"
        "v_add_f32 v95, v95, v71\n"
        "v_add_f32 v164, v164, v103\n"
        "v_mfma_f32_16x16x32_bf16 v[120:123], v[128:131], v[116:119], v[120:123]\n"
        "s_waitcnt lgkmcnt(0)\n"
        "v_add_f32 v95, v95, v72\n"
        "v_add_f32 v164, v164, v44\n"
        "v_mul_f32 v64, v64, v20\n"
        "v_mul_f32 v65, v65, v21\n"
        "v_permlane32_swap_b32 v95, v164\n"
        "v_add_f32 v95, v95, v164\n"
        "v_mov_b32 v167, v95\n"
        "v_mfma_f32_16x16x32_bf16 v[120:123], v[160:163], v[112:115], v[120:123]\n"
        "v_mul_f32 v66, v66, v22\n"
        "v_permlane16_swap_b32 v95, v167\n"
        "v_add_f32 v95, v95, v167\n"
        "v_rcp_f32 v95, v95\n"
        "v_mul_f32 v67, v67, v23\n"
        "v_mov_b32 v164, v95\n"
        "v_mfma_f32_16x16x32_bf16 v[156:159], v[52:55], v[112:115], 0\n"
        "v_mul_f32 v84, v84, v24\n"
        "v_perm_b32 v104, v121, v120, s63\n"
        "v_perm_b32 v105, v123, v122, s63\n"
        "v_permlane32_swap_b32 v95, v164\n"
        "v_mul_f32 v85, v85, v25\n"
        "v_mul_f32 v86, v86, v26\n"
        "v_mul_f32 v87, v87, v27\n"
        "v_mfma_f32_16x16x32_bf16 v[156:159], v[136:139], v[116:119], v[156:159]\n"
        "v_mul_f32 v64, v64, v95\n"
        "v_mul_f32 v65, v65, v95\n"
        "v_mul_f32 v66, v66, v95\n"
        "v_mul_f32 v67, v67, v95\n"
        "v_mul_f32 v84, v84, v95\n"
        "v_mul_f32 v85, v85, v95\n"
        "v_mul_f32 v86, v86, v95\n"
        "v_mul_f32 v87, v87, v95\n"
        "v_mfma_f32_16x16x32_bf16 v[156:159], v[132:135], v[144:147], v[156:159]\n"
        "ds_read_b128 v[148:151], v0 offset:11312\n"
        "v_perm_b32 v120, v65, v64, s63\n"
        "v_perm_b32 v121, v67, v66, s63\n"
        "v_perm_b32 v122, v85, v84, s63\n"
        "v_perm_b32 v123, v87, v86, s63\n"
        "v_mul_f32 v68, v68, v28\n"
        "v_mul_f32 v69, v69, v29\n"
        "v_mul_f32 v70, v70, v30\n"
        "v_mfma_f32_16x16x32_bf16 v[152:155], v[32:35], v[144:147], 0\n"
        "ds_read_b128 v[84:87], v0 offset:10288\n"
        "v_perm_b32 v106, v157, v156, s63\n"
        "v_perm_b32 v107, v159, v158, s63\n"
        "v_mul_f32 v71, v71, v31\n"
        "v_mul_f32 v72, s58, v72\n"
        "v_mul_f32 v68, v68, v95\n"
        "v_mul_f32 v69, v69, v95\n"
        "v_mul_f32 v70, v70, v95\n"
        "v_mfma_f32_16x16x32_bf16 v[152:155], v[36:39], v[116:119], v[152:155]\n"
        "ds_read_b128 v[144:147], v0 offset:9264\n"
        "v_mul_f32 v71, v71, v95\n"
        "v_fma_f32 v72, v72, v95, v19\n"
        "v_perm_b32 v64, v69, v68, s63\n"
        "v_perm_b32 v66, v71, v70, s63\n"
        "v_perm_b32 v65, 0, v72, s63\n"
        "v_mul_f32 v108, v108, v20\n"
        "v_mul_f32 v109, v109, v21\n"
        "v_mfma_f32_16x16x32_bf16 v[152:155], v[60:63], v[112:115], v[152:155]\n"
        "s_waitcnt lgkmcnt(2)\n"
        "v_mul_f32 v110, v110, v22\n"
        "v_mul_f32 v111, v111, v23\n"
        "v_mul_f32 v140, v140, v24\n"
        "v_mul_f32 v141, v141, v25\n"
        "v_mul_f32 v142, v142, v26\n"
        "v_mul_f32 v143, v143, v27\n"
        "v_mul_f32 v108, v108, v164\n"
        "v_perm_b32 v112, v153, v152, s63\n"
        "v_mfma_f32_16x16x32_bf16 v[20:23], v[60:63], v[148:151], 0\n"
        "s_waitcnt lgkmcnt(1)\n"
        "v_perm_b32 v116, v155, v154, s63\n"
        "v_mul_f32 v109, v109, v164\n"
        "v_mul_f32 v110, v110, v164\n"
        "v_mul_f32 v111, v111, v164\n"
        "v_mul_f32 v140, v140, v164\n"
        "v_mul_f32 v141, v141, v164\n"
        "v_mul_f32 v142, v142, v164\n"
        "v_mul_f32 v143, v143, v164\n"
        "v_mfma_f32_16x16x32_bf16 v[20:23], v[36:39], v[84:87], v[20:23]\n"
        "s_waitcnt lgkmcnt(0)\n"
        "v_perm_b32 v24, v109, v108, s63\n"
        "v_perm_b32 v25, v111, v110, s63\n"
        "v_perm_b32 v26, v141, v140, s63\n"
        "v_perm_b32 v27, v143, v142, s63\n"
        "v_mul_f32 v100, v100, v28\n"
        "v_mul_f32 v101, v101, v29\n"
        "v_mul_f32 v102, v102, v30\n"
        "v_mul_f32 v103, v103, v31\n"
        "v_mfma_f32_16x16x32_bf16 v[20:23], v[32:35], v[144:147], v[20:23]\n"
        "v_mul_f32 v44, s58, v44\n"
        "v_mul_f32 v100, v100, v164\n"
        "v_mul_f32 v101, v101, v164\n"
        "v_mul_f32 v102, v102, v164\n"
        "v_mul_f32 v103, v103, v164\n"
        "v_fma_f32 v44, v44, v164, v19\n"
        "v_perm_b32 v28, v101, v100, s63\n"
        "v_perm_b32 v30, v103, v102, s63\n"
        "v_mfma_f32_16x16x32_bf16 v[68:71], v[132:135], v[144:147], 0\n"
        "v_perm_b32 v77, v21, v20, s63\n"
        "v_perm_b32 v81, v23, v22, s63\n"
        "v_perm_b32 v29, 0, v44, s63\n"
        "v_mfma_f32_16x16x32_bf16 v[68:71], v[136:139], v[84:87], v[68:71]\n"
        "v_mfma_f32_16x16x32_bf16 v[68:71], v[52:55], v[148:151], v[68:71]\n"
        "ds_read_b128 v[20:23], v0 offset:12352\n"
        "v_mfma_f32_16x16x32_bf16 v[72:75], v[160:163], v[148:151], 0\n"
        "ds_read_b128 v[108:111], v0 offset:13376\n"
        "v_mfma_f32_16x16x32_bf16 v[72:75], v[128:131], v[84:87], v[72:75]\n"
        "ds_read_b128 v[140:143], v0 offset:14400\n"
        "v_mfma_f32_16x16x32_bf16 v[72:75], v[124:127], v[144:147], v[72:75]\n"
        "s_waitcnt lgkmcnt(2)\n"
        "v_mfma_f32_16x16x32_bf16 v[144:147], v[124:127], v[20:23], 0\n"
        "v_perm_b32 v46, v69, v68, s63\n"
        "v_perm_b32 v47, v71, v70, s63\n"
        "s_waitcnt lgkmcnt(1)\n"
        "v_mfma_f32_16x16x32_bf16 v[144:147], v[128:131], v[108:111], v[144:147]\n"
        "s_waitcnt lgkmcnt(0)\n"
        "v_mfma_f32_16x16x32_bf16 v[144:147], v[160:163], v[140:143], v[144:147]\n"
        "v_perm_b32 v44, v73, v72, s63\n"
        "v_perm_b32 v45, v75, v74, s63\n"
        "v_mfma_f32_16x16x32_bf16 v[148:151], v[52:55], v[140:143], 0\n"
        "v_mfma_f32_16x16x32_bf16 v[148:151], v[136:139], v[108:111], v[148:151]\n"
        "v_mfma_f32_16x16x32_bf16 v[148:151], v[132:135], v[20:23], v[148:151]\n"
        "ds_read_b128 v[72:75], v0 offset:17488\n"
        "v_mfma_f32_16x16x32_bf16 v[84:87], v[32:35], v[20:23], 0\n"
        "ds_read_b128 v[68:71], v0 offset:16464\n"
        "v_perm_b32 v100, v145, v144, s63\n"
        "v_perm_b32 v101, v147, v146, s63\n"
        "v_mfma_f32_16x16x32_bf16 v[84:87], v[36:39], v[108:111], v[84:87]\n"
        "ds_read_b128 v[20:23], v0 offset:15440\n"
        "v_mfma_f32_16x16x32_bf16 v[84:87], v[60:63], v[140:143], v[84:87]\n"
        "v_perm_b32 v102, v149, v148, s63\n"
        "v_perm_b32 v103, v151, v150, s63\n"
        "s_waitcnt lgkmcnt(2)\n"
        "v_mfma_f32_16x16x32_bf16 v[108:111], v[60:63], v[72:75], 0\n"
        "s_waitcnt lgkmcnt(1)\n"
        "v_mfma_f32_16x16x32_bf16 v[108:111], v[36:39], v[68:71], v[108:111]\n"
        "s_waitcnt lgkmcnt(0)\n"
        "v_mfma_f32_16x16x32_bf16 v[108:111], v[32:35], v[20:23], v[108:111]\n"
        "ds_read2_b32 v[82:83], v11 offset0:0 offset1:3\n"
        "v_perm_b32 v89, v85, v84, s63\n"
        "v_perm_b32 v93, v87, v86, s63\n"
        "v_mov_b32_dpp v59, v19 row_shr:4 row_mask:0xf bank_mask:0xf bound_ctrl:0\n"
        "v_mfma_f32_16x16x32_bf16 v[36:39], v[132:135], v[20:23], 0\n"
        "ds_read_b64_tr_b16 v[78:79], v9 offset:0\n"
        "v_mfma_f32_16x16x32_bf16 v[36:39], v[136:139], v[68:71], v[36:39]\n"
        "ds_read2_b32 v[94:95], v11 offset0:1 offset1:4\n"
        "v_perm_b32 v113, v109, v108, s63\n"
        "v_perm_b32 v117, v111, v110, s63\n"
        "v_mfma_f32_16x16x32_bf16 v[36:39], v[52:55], v[72:75], v[36:39]\n"
        "s_add_u32 s31, s31, 1\n"
        "ds_read_b64_tr_b16 v[90:91], v9 offset:16\n"
        "v_mfma_f32_16x16x32_bf16 v[32:35], v[160:163], v[72:75], 0\n"
        "s_cmp_ge_u32 s31, s71\n"
        "ds_read2_b32 v[118:119], v11 offset0:2 offset1:5\n"
        "v_mfma_f32_16x16x32_bf16 v[32:35], v[128:131], v[68:71], v[32:35]\n"
        "s_cbranch_scc1 L52A_%=\n"
        "L53A_%=:\n"
        "ds_read_b64_tr_b16 v[114:115], v9 offset:32\n"
        "v_perm_b32 v110, v37, v36, s63\n"
        "v_perm_b32 v111, v39, v38, s63\n"
        "v_mfma_f32_16x16x32_bf16 v[32:35], v[124:127], v[20:23], v[32:35]\n"
        "s_add_u32 s16, s16, 256\n"
        "v_mfma_f32_16x16x32_bf16 v[20:23], v[48:51], v[40:43], 0\n"
        "s_waitcnt lgkmcnt(4)\n"
        "s_cmp_ge_u32 s31, 20\n"
        "v_smfmac_f32_16x16x64_bf16 v[20:23], v[56:59], v[76:83], v18\n"
        "s_cselect_b32 s39, s22, s16\n"
        "v_mfma_f32_16x16x32_bf16 v[36:39], v[48:51], v[96:99], 0\n"
        "s_waitcnt lgkmcnt(2)\n"
        "v_perm_b32 v108, v33, v32, s63\n"
        "v_perm_b32 v109, v35, v34, s63\n"
        "s_cmp_ge_u32 s31, 21\n"
        "v_smfmac_f32_16x16x64_bf16 v[36:39], v[56:59], v[88:95], v18\n"
        "s_cselect_b32 s39, s23, s39\n"
        "ds_read_b64_tr_b16 v[78:79], v9 offset:48\n"
        "v_mfma_f32_16x16x32_bf16 v[32:35], v[48:51], v[104:107], 0\n"
        "s_waitcnt lgkmcnt(1)\n"
        "s_mul_hi_u32 s38, s39, 58835169\n"
        "v_smfmac_f32_16x16x64_bf16 v[32:35], v[56:59], v[112:119], v18\n"
        "s_mul_i32 s39, s39, 1344\n"
        "ds_read_b64_tr_b16 v[90:91], v9 offset:64\n"
        "v_cvt_pk_bf16_f32 v152, v20, v36\n"
        "v_cvt_pk_bf16_f32 v156, v21, v37\n"
        "v_cvt_pk_bf16_f32 v160, v22, v38\n"
        "v_cvt_pk_bf16_f32 v164, v23, v39\n"
        "v_mfma_f32_16x16x32_bf16 v[68:71], v[48:51], v[44:47], 0\n"
        "s_mul_i32 s38, s38, 588672\n"
        "s_waitcnt lgkmcnt(1)\n"
        "v_smfmac_f32_16x16x64_bf16 v[68:71], v[56:59], v[76:83], v18 abid:1\n"
        "s_add_u32 s39, s39, s38\n"
        "ds_read_b64_tr_b16 v[114:115], v9 offset:80\n"
        "v_mfma_f32_16x16x32_bf16 v[60:63], v[48:51], v[100:103], 0\n"
        "s_add_u32 s39, s39, s17\n"
        "ds_read_b128 v[36:39], v2\n"
        "v_mov_b64 v[20:21], 0\n"
        "v_mov_b64 v[22:23], 0\n"
        "s_waitcnt lgkmcnt(2)\n"
        "v_smfmac_f32_16x16x64_bf16 v[60:63], v[56:59], v[88:95], v18 abid:1\n"
        "s_mul_i32 s68, s31, 3\n"
        "ds_read_b128 v[52:55], v3\n"
        "v_cvt_pk_bf16_f32 v153, v32, v68\n"
        "v_cvt_pk_bf16_f32 v157, v33, v69\n"
        "v_cvt_pk_bf16_f32 v161, v34, v70\n"
        "v_cvt_pk_bf16_f32 v165, v35, v71\n"
        "v_mfma_f32_16x16x32_bf16 v[68:71], v[48:51], v[108:111], 0\n"
        "s_bitcmp1_b32 s31, 0\n"
        "v_mov_b64 v[32:33], 0\n"
        "v_mov_b64 v[34:35], 0\n"
        "s_waitcnt lgkmcnt(2)\n"
        "v_smfmac_f32_16x16x64_bf16 v[68:71], v[56:59], v[112:119], v18 abid:1\n"
        "s_cselect_b32 s38, 3, 6\n"
        "s_waitcnt lgkmcnt(1)\n"
        "v_mov_b32_dpp v67, v19 row_shr:4 row_mask:0xf bank_mask:0xf bound_ctrl:0\n"
        "v_smfmac_f32_16x16x64_bf16 v[20:23], v[36:39], v[40:47], v4\n"
        "s_add_u32 s68, s68, s38\n"
        "v_mov_b64 v[48:49], 0\n"
        "v_mov_b64 v[50:51], 0\n"
        "s_waitcnt lgkmcnt(0)\n"
        "v_smfmac_f32_16x16x64_bf16 v[20:23], v[52:55], v[76:83], v4 abid:1\n"
        "s_add_u32 s37, s35, 0\n"
        "s_cmp_lt_u32 s31, s14\n"
        "v_cvt_pk_bf16_f32 v154, v60, v68\n"
        "v_cvt_pk_bf16_f32 v158, v61, v69\n"
        "v_cvt_pk_bf16_f32 v162, v62, v70\n"
        "v_cvt_pk_bf16_f32 v166, v63, v71\n"
        "v_smfmac_f32_16x16x64_bf16 v[32:35], v[36:39], v[96:103], v4\n"
        "s_cselect_b32 s68, s68, 0\n"
        "v_add_f32 v20, v20, v21\n"
        "v_add_f32 v22, v22, v23\n"
        "v_smfmac_f32_16x16x64_bf16 v[32:35], v[52:55], v[88:95], v4 abid:1\n"
        "s_and_b32 s38, s31, 7\n"
        "ds_read_b64_tr_b16 v[78:79], v9 offset:3328\n"
        "v_smfmac_f32_16x16x64_bf16 v[48:51], v[36:39], v[104:111], v4\n"
        "buffer_store_dwordx3 v[152:154], v12, s[8:11], s37 offen sc0 sc1 nt\n"
        "s_cselect_b32 s30, 1, -7\n"
        "v_smfmac_f32_16x16x64_bf16 v[48:51], v[52:55], v[112:119], v4 abid:1\n"
        "s_mul_i32 s29, s30, 192\n"
        "ds_read_b64_tr_b16 v[90:91], v9 offset:3344\n"
        "v_add_f32 v32, v32, v33\n"
        "v_add_f32 v34, v34, v35\n"
        "v_cvt_pk_bf16_f32 v148, v20, v32\n"
        "v_mfma_f32_16x16x32_bf16 v[152:155], v[120:123], v[40:43], 0\n"
        "s_mul_i32 s30, s30, 392\n"
        "s_waitcnt lgkmcnt(1)\n"
        "v_smfmac_f32_16x16x64_bf16 v[152:155], v[64:67], v[76:83], v18\n"
        "s_add_u32 s37, s35, 0\n"
        "s_cmp_eq_u32 s69, s18\n"
        "ds_read_b64_tr_b16 v[114:115], v9 offset:3360\n"
        "v_add_f32 v48, v48, v49\n"
        "v_add_f32 v50, v50, v51\n"
        "v_cvt_pk_bf16_f32 v149, v48, v22\n"
        "v_cvt_pk_bf16_f32 v150, v34, v50\n"
        "v_mfma_f32_16x16x32_bf16 v[56:59], v[120:123], v[96:99], 0\n"
        "s_cselect_b32 s34, -7488, s70\n"
        "s_waitcnt lgkmcnt(1)\n"
        "v_smfmac_f32_16x16x64_bf16 v[56:59], v[64:67], v[88:95], v18\n"
        "s_add_u32 s69, s69, s34\n"
        "ds_read_b64_tr_b16 v[78:79], v9 offset:3376\n"
        "v_writelane_b32 v10, s69, 56\n"
        "v_add_u32 v11, s30, v11\n"
        "s_mov_b64 exec, s[54:55]\n"
        "v_add_u32 v16, s29, v16\n"
        "v_add_u32 v8, s34, v8\n"
        "s_mov_b64 exec, -1\n"
        "v_mfma_f32_16x16x32_bf16 v[72:75], v[120:123], v[104:107], 0\n"
        "buffer_store_dwordx3 v[156:158], v15, s[8:11], s37 offen offset:768 sc0 sc1 nt\n"
        "s_mov_b64 exec, s[50:51]\n"
        "v_add_u32 v5, s29, v5\n"
        "v_add_u32 v6, s34, v6\n"
        "s_mov_b64 exec, -1\n"
        "s_waitcnt lgkmcnt(1)\n"
        "v_smfmac_f32_16x16x64_bf16 v[72:75], v[64:67], v[112:119], v18\n"
        "ds_read_b64_tr_b16 v[90:91], v9 offset:3392\n"
        "v_mov_b32_dpp v31, v19 row_shr:4 row_mask:0xf bank_mask:0xf bound_ctrl:0\n"
        "v_mfma_f32_16x16x32_bf16 v[20:23], v[120:123], v[44:47], 0\n"
        "s_waitcnt lgkmcnt(1)\n"
        "v_smfmac_f32_16x16x64_bf16 v[20:23], v[64:67], v[76:83], v18 abid:1\n"
        "s_add_u32 s37, s35, 98112\n"
        "ds_read_b64_tr_b16 v[114:115], v9 offset:3408\n"
        "v_mfma_f32_16x16x32_bf16 v[84:87], v[120:123], v[100:103], 0\n"
        "s_waitcnt lgkmcnt(1)\n"
        "v_smfmac_f32_16x16x64_bf16 v[84:87], v[64:67], v[88:95], v18 abid:1\n"
        "ds_read_b64_tr_b16 v[78:79], v9 offset:6656\n"
        "v_mfma_f32_16x16x32_bf16 v[60:63], v[120:123], v[108:111], 0\n"
        "buffer_store_dwordx3 v[160:162], v12, s[8:11], s37 offen offset:192 sc0 sc1 nt\n"
        "ds_read_b32 v167, v0 offset:21604\n"
        "s_waitcnt lgkmcnt(2)\n"
        "v_smfmac_f32_16x16x64_bf16 v[60:63], v[64:67], v[112:119], v18 abid:1\n"
        "ds_read_b64_tr_b16 v[90:91], v9 offset:6672\n"
        "v_mfma_f32_16x16x32_bf16 v[68:71], v[24:27], v[40:43], 0\n"
        "s_waitcnt lgkmcnt(2)\n"
        "v_smfmac_f32_16x16x64_bf16 v[68:71], v[28:31], v[76:83], v18\n"
        "s_add_u32 s37, s35, 98112\n"
        "ds_read_b64_tr_b16 v[114:115], v9 offset:6688\n"
        "v_mfma_f32_16x16x32_bf16 v[36:39], v[24:27], v[96:99], 0\n"
        "s_waitcnt lgkmcnt(1)\n"
        "v_smfmac_f32_16x16x64_bf16 v[36:39], v[28:31], v[88:95], v18\n"
        "ds_read_b64_tr_b16 v[78:79], v9 offset:6704\n"
        "v_readfirstlane_b32 s40, v167\n"
        "v_mfma_f32_16x16x32_bf16 v[40:43], v[24:27], v[104:107], 0\n"
        "buffer_store_dwordx3 v[164:166], v14, s[8:11], s37 offen offset:960 sc0 sc1 nt\n"
        "s_add_u32 s37, s35, 588672\n"
        "s_add_u32 s46, s37, 1344\n"
        "s_cmp_lt_i32 s40, s68\n"
        "s_waitcnt lgkmcnt(1)\n"
        "v_smfmac_f32_16x16x64_bf16 v[40:43], v[28:31], v[112:119], v18\n"
        "buffer_store_dwordx3 v[148:150], v12, s[44:47], s37 offen offset:1152 sc0 sc1 nt\n"
        "ds_read_b64_tr_b16 v[90:91], v9 offset:6720\n"
        "v_mfma_f32_16x16x32_bf16 v[64:67], v[24:27], v[44:47], 0\n"
        "s_waitcnt lgkmcnt(1)\n"
        "v_smfmac_f32_16x16x64_bf16 v[64:67], v[28:31], v[76:83], v18 abid:1\n"
        "ds_read_b64_tr_b16 v[114:115], v9 offset:6736\n"
        "s_mov_b64 exec, 1\n"
        "ds_inc_u32 v0, v1 offset:21608\n"
        "s_mov_b64 exec, -1\n"
        "s_cbranch_scc1 L56A_%=\n"
        "L57A_%=:\n"
        "ds_read_b128 v[124:127], v7 offset:40064\n"
        "ds_read_b128 v[132:135], v7 offset:43392\n"
        "ds_read_b128 v[128:131], v7 offset:40128\n"
        "ds_read_b128 v[136:139], v7 offset:43456\n"
        "v_mfma_f32_16x16x32_bf16 v[104:107], v[24:27], v[100:103], 0\n"
        "s_waitcnt lgkmcnt(6)\n"
        "ds_read_b128 v[160:163], v7 offset:40192\n"
        "ds_read_b128 v[52:55], v7 offset:43520\n"
        "s_mov_b32 s36, s35\n"
        "v_smfmac_f32_16x16x64_bf16 v[104:107], v[28:31], v[88:95], v18 abid:1\n"
        "s_mov_b32 s35, s39\n"
        "v_mfma_f32_16x16x32_bf16 v[88:91], v[24:27], v[108:111], 0\n"
        "s_waitcnt lgkmcnt(7)\n"
        "v_smfmac_f32_16x16x64_bf16 v[88:91], v[28:31], v[112:119], v18 abid:1\n"
        "s_setprio 0\n"
        "s_waitcnt lgkmcnt(0)\n"
        "s_nop 15\n"
        "s_branch CNEXTA_%=\n"
        "MTOPB_%=:\n"
        "s_waitcnt lgkmcnt(5)\n"
        "v_mfma_f32_16x16x32_bf16 v[24:27], v[168:171], v[124:127], 0\n"
        "v_cvt_pk_bf16_f32 v112, v152, v56\n"
        "v_cvt_pk_bf16_f32 v113, v72, v20\n"
        "v_cvt_pk_bf16_f32 v114, v84, v60\n"
        "v_cvt_pk_bf16_f32 v116, v153, v57\n"
        "v_cvt_pk_bf16_f32 v117, v73, v21\n"
        "v_cvt_pk_bf16_f32 v118, v85, v61\n"
        "v_cvt_pk_bf16_f32 v120, v154, v58\n"
        "v_cvt_pk_bf16_f32 v121, v74, v22\n"
        "s_waitcnt lgkmcnt(3)\n"
        "v_mfma_f32_16x16x32_bf16 v[24:27], v[172:175], v[128:131], v[24:27]\n"
        "v_cvt_pk_bf16_f32 v122, v86, v62\n"
        "v_cvt_pk_bf16_f32 v140, v155, v59\n"
        "v_cvt_pk_bf16_f32 v141, v75, v23\n"
        "v_cvt_pk_bf16_f32 v142, v87, v63\n"
        "v_cvt_pk_bf16_f32 v144, v68, v36\n"
        "v_cvt_pk_bf16_f32 v145, v40, v64\n"
        "v_cvt_pk_bf16_f32 v146, v104, v88\n"
        "v_cvt_pk_bf16_f32 v148, v69, v37\n"
        "s_waitcnt lgkmcnt(1)\n"
        "v_mfma_f32_16x16x32_bf16 v[24:27], v[176:179], v[160:163], v[24:27]\n"
        "v_cvt_pk_bf16_f32 v149, v41, v65\n"
        "v_cvt_pk_bf16_f32 v150, v105, v89\n"
        "v_cvt_pk_bf16_f32 v152, v70, v38\n"
        "v_cvt_pk_bf16_f32 v153, v42, v66\n"
        "v_cvt_pk_bf16_f32 v154, v106, v90\n"
        "v_cvt_pk_bf16_f32 v156, v71, v39\n"
        "v_cvt_pk_bf16_f32 v157, v43, v67\n"
        "v_cvt_pk_bf16_f32 v158, v107, v91\n"
        "v_mfma_f32_16x16x32_bf16 v[32:35], v[188:191], v[160:163], 0\n"
        "v_perm_b32 v40, v25, v24, s63\n"
        "v_perm_b32 v41, v27, v26, s63\n"
        "v_mfma_f32_16x16x32_bf16 v[32:35], v[184:187], v[128:131], v[32:35]\n"
        "v_mfma_f32_16x16x32_bf16 v[32:35], v[180:183], v[124:127], v[32:35]\n"
        "s_add_u32 s37, s36, 196224\n"
        "ds_read_b128 v[28:31], v16 offset:128\n"
        "ds_read_b32 v159, v10\n"
        "v_mfma_f32_16x16x32_bf16 v[24:27], v[192:195], v[124:127], 0\n"
        "ds_read_b128 v[20:23], v5\n"
        "v_mfma_f32_16x16x32_bf16 v[24:27], v[196:199], v[128:131], v[24:27]\n"
        "ds_read_b64 v[164:165], v11 offset:192\n"
        "v_mfma_f32_16x16x32_bf16 v[24:27], v[200:203], v[160:163], v[24:27]\n"
        "buffer_store_dwordx3 v[112:114], v12, s[8:11], s37 offen offset:384 sc0 sc1 nt\n"
        "v_perm_b32 v42, v33, v32, s63\n"
        "v_perm_b32 v43, v35, v34, s63\n"
        "s_waitcnt lgkmcnt(3)\n"
        "v_mfma_f32_16x16x32_bf16 v[48:51], v[28:31], v[160:163], 0\n"
        "ds_read_b128 v[60:63], v7 offset:46848\n"
        "ds_read_i16 v167, v0 offset:20584\n"
        "ds_read_i16 v155, v0 offset:20586\n"
        "v_perm_b32 v44, v25, v24, s63\n"
        "v_perm_b32 v45, v27, v26, s63\n"
        "s_waitcnt lgkmcnt(3)\n"
        "v_smfmac_f32_16x16x64_bf16 v[48:51], v[20:23], v[124:131], v4\n"
        "s_add_u32 s37, s36, 196224\n"
        "ds_read_b128 v[32:35], v7 offset:46720\n"
        "ds_read_b128 v[36:39], v7 offset:46784\n"
        "v_mfma_f32_16x16x32_bf16 v[56:59], v[28:31], v[52:55], 0\n"
        "v_smfmac_f32_16x16x64_bf16 v[56:59], v[20:23], v[132:139], v4\n"
        "s_setprio 2\n"
        "s_waitcnt lgkmcnt(4)\n"
        "v_mfma_f32_16x16x32_bf16 v[24:27], v[28:31], v[60:63], 0\n"
        "buffer_store_dwordx3 v[116:118], v13, s[8:11], s37 offen offset:1152 sc0 sc1 nt\n"
        "v_add_f32_dpp v165, v50, v51 quad_perm:[0,1,2,3] row_mask:0x1 bank_mask:0xf\n"
        "s_waitcnt lgkmcnt(2)\n"
        "v_add_u32 v167, v9, v167\n"
        "v_add_f32_dpp v164, v48, v49 quad_perm:[0,1,2,3] row_mask:0x1 bank_mask:0xf\n"
        "v_add_f32_dpp v165, v58, v59 quad_perm:[0,1,2,3] row_mask:0x2 bank_mask:0xf\n"
        "v_add_u32 v155, v9, v155\n"
        "v_add_f32_dpp v164, v56, v57 quad_perm:[0,1,2,3] row_mask:0x2 bank_mask:0xf\n"
        "s_waitcnt lgkmcnt(0)\n"
        "v_smfmac_f32_16x16x64_bf16 v[24:27], v[20:23], v[32:39], v4\n"
        "v_mfma_f32_16x16x32_bf16 v[28:31], v[212:215], v[160:163], 0\n"
        "s_add_u32 s37, s36, 294336\n"
        "v_mfma_f32_16x16x32_bf16 v[28:31], v[208:211], v[128:131], v[28:31]\n"
        "v_mfma_f32_16x16x32_bf16 v[28:31], v[204:207], v[124:127], v[28:31]\n"
        "v_mfma_f32_16x16x32_bf16 v[20:23], v[216:219], v[124:127], 0\n"
        "buffer_store_dwordx3 v[120:122], v12, s[8:11], s37 offen offset:576 sc0 sc1 nt\n"
        "v_mfma_f32_16x16x32_bf16 v[20:23], v[220:223], v[128:131], v[20:23]\n"
        "v_mfma_f32_16x16x32_bf16 v[20:23], v[224:227], v[160:163], v[20:23]\n"
        "s_add_u32 s37, s36, 392448\n"
        "v_add_f32_dpp v165, v26, v27 quad_perm:[0,1,2,3] row_mask:0x4 bank_mask:0xf\n"
        "v_mul_f32 v166, s52, v165\n"
        "v_fma_f32 v166, -v166, v165, v159\n"
        "v_perm_b32 v46, v29, v28, s63\n"
        "v_perm_b32 v47, v31, v30, s63\n"
        "v_add_f32_e64 v166, s53, |v166|\n"
        "v_rsq_f32 v159, v166\n"
        "v_mfma_f32_16x16x32_bf16 v[56:59], v[236:239], v[160:163], 0\n"
        "s_mov_b32 exec_hi, 0xffff\n"
        "ds_write_b32 v10, v159\n"
        "s_mov_b32 exec_hi, -1\n"
        "v_perm_b32 v48, v21, v20, s63\n"
        "v_perm_b32 v49, v23, v22, s63\n"
        "ds_read_b32 v20, v167 offset:40064\n"
        "v_add_f32_dpp v164, v24, v25 quad_perm:[0,1,2,3] row_mask:0x4 bank_mask:0xf\n"
        "v_mfma_f32_16x16x32_bf16 v[56:59], v[232:235], v[128:131], v[56:59]\n"
        "s_mov_b32 exec_hi, 0xffff\n"
        "v_add_u32 v10, -40064, v10\n"
        "s_mov_b32 exec_hi, -1\n"
        "ds_read_b32 v21, v167 offset:40896\n"
        "ds_read_b32 v22, v167 offset:41728\n"
        "ds_read_b32 v23, v167 offset:42560\n"
        "ds_read_b32 v24, v167 offset:43392\n"
        "v_mul_f32 v164, v164, v159\n"
        "v_mfma_f32_16x16x32_bf16 v[56:59], v[228:231], v[124:127], v[56:59]\n"
        "buffer_store_dwordx3 v[140:142], v12, s[8:11], s37 offen sc0 sc1 nt\n"
        "ds_read_b32 v25, v167 offset:44224\n"
        "ds_read_b32 v26, v167 offset:45056\n"
        "ds_read_b32 v27, v167 offset:45888\n"
        "ds_read_b32 v28, v167 offset:46720\n"
        "ds_read_b32 v29, v167 offset:47552\n"
        "ds_read_b32 v30, v167 offset:48384\n"
        "ds_read_b32 v31, v167 offset:49216\n"
        "v_perm_b32 v50, v57, v56, s63\n"
        "v_perm_b32 v51, v59, v58, s63\n"
        "v_readlane_b32 s58, v159, 56\n"
        "v_mfma_f32_16x16x32_bf16 v[56:59], v[124:127], v[40:43], 0\n"
        "s_mov_b64 exec, s[56:57]\n"
        "v_mov_b32 v164, 0xff800000\n"
        "s_mov_b64 exec, -1\n"
        "ds_read_b32 v165, v155 offset:40064\n"
        "ds_read_b32 v166, v155 offset:43392\n"
        "v_max_f32_dpp v151, v164, v164 row_ror:8 row_mask:0xf bank_mask:0xf\n"
        "v_mfma_f32_16x16x32_bf16 v[56:59], v[128:131], v[44:47], v[56:59]\n"
        "s_add_u32 s37, s36, 392448\n"
        "v_max_f32_dpp v151, v151, v151 row_ror:4 row_mask:0xf bank_mask:0xf\n"
        "ds_read_b32 v167, v155 offset:46720\n"
        "v_mfma_f32_16x16x32_bf16 v[56:59], v[160:163], v[48:51], v[56:59]\n"
        "v_max_f32_dpp v151, v151, v151 row_ror:2 row_mask:0xf bank_mask:0xf\n"
        "v_mfma_f32_16x16x32_bf16 v[72:75], v[52:55], v[48:51], 0\n"
        "v_mfma_f32_16x16x32_bf16 v[72:75], v[136:139], v[44:47], v[72:75]\n"
        "buffer_store_dwordx3 v[144:146], v15, s[8:11], s37 offen offset:768 sc0 sc1 nt\n"
        "v_max_f32_dpp v151, v151, v151 row_ror:1 row_mask:0xf bank_mask:0xf\n"
        "v_mfma_f32_16x16x32_bf16 v[72:75], v[132:135], v[40:43], v[72:75]\n"
        "ds_read_b128 v[88:91], v8 offset:128\n"
        "v_mfma_f32_16x16x32_bf16 v[68:71], v[32:35], v[40:43], 0\n"
        "s_add_u32 s37, s36, 490560\n"
        "ds_read2_b32 v[92:93], v6 offset0:0 offset1:2\n"
        "ds_read2_b32 v[94:95], v6 offset0:8 offset1:10\n"
        "s_waitcnt lgkmcnt(3)\n"
        "v_mul_f32 v56, v56, v20\n"
        "v_mul_f32 v57, v57, v21\n"
        "v_mul_f32 v58, v58, v22\n"
        "v_mul_f32 v59, v59, v23\n"
        "v_mul_f32 v72, v72, v24\n"
        "v_mfma_f32_16x16x32_bf16 v[68:71], v[36:39], v[44:47], v[68:71]\n"
        "v_max3_f32 v155, v56, v57, v58\n"
        "v_mul_f32 v73, v73, v25\n"
        "v_mul_f32 v74, v74, v26\n"
        "v_max3_f32 v155, v155, v59, v72\n"
        "v_mul_f32 v75, v75, v27\n"
        "v_max3_f32 v155, v155, v73, v74\n"
        "v_mfma_f32_16x16x32_bf16 v[68:71], v[60:63], v[48:51], v[68:71]\n"
        "s_waitcnt lgkmcnt(2)\n"
        "v_mfma_f32_16x16x32_bf16 v[76:79], v[88:91], v[48:51], 0\n"
        "buffer_store_dwordx3 v[148:150], v12, s[8:11], s37 offen offset:192 sc0 sc1 nt\n"
        "s_waitcnt lgkmcnt(0)\n"
        "v_smfmac_f32_16x16x64_bf16 v[76:79], v[92:95], v[40:47], v4\n"
        "v_mfma_f32_16x16x32_bf16 v[48:51], v[168:171], v[132:135], 0\n"
        "s_add_u32 s37, s36, 490560\n"
        "v_mfma_f32_16x16x32_bf16 v[48:51], v[172:175], v[136:139], v[48:51]\n"
        "v_mul_f32 v68, v68, v28\n"
        "v_mul_f32 v69, v69, v29\n"
        "v_mul_f32 v70, v70, v30\n"
        "v_max3_f32 v155, v155, v75, v68\n"
        "v_mul_f32 v71, v71, v31\n"
        "v_add_f32 v76, v76, v77\n"
        "v_mul_f32 v76, s58, v76\n"
        "v_max3_f32 v155, v155, v69, v70\n"
        "v_mfma_f32_16x16x32_bf16 v[48:51], v[176:179], v[52:55], v[48:51]\n"
        "s_mov_b64 exec, s[48:49]\n"
        "v_mov_b32 v76, 0xff800000\n"
        "s_mov_b64 exec, -1\n"
        "v_max3_f32 v155, v155, v71, v76\n"
        "v_mfma_f32_16x16x32_bf16 v[44:47], v[188:191], v[52:55], 0\n"
        "buffer_store_dwordx3 v[152:154], v14, s[8:11], s37 offen offset:960 sc0 sc1 nt\n"
        "v_permlane32_swap_b32 v155, v151\n"
        "v_max_f32 v155, v155, v151\n"
        "v_perm_b32 v40, v49, v48, s63\n"
        "v_perm_b32 v41, v51, v50, s63\n"
        "v_mov_b32 v154, v155\n"
        "v_mfma_f32_16x16x32_bf16 v[44:47], v[184:187], v[136:139], v[44:47]\n"
        "v_mfma_f32_16x16x32_bf16 v[44:47], v[180:183], v[132:135], v[44:47]\n"
        "s_add_u32 s37, s36, 588672\n"
        "v_permlane16_swap_b32 v155, v154\n"
        "v_max_f32 v155, v155, v154\n"
        "v_mov_b32 v151, v155\n"
        "v_mfma_f32_16x16x32_bf16 v[64:67], v[192:195], v[132:135], 0\n"
        "v_mfma_f32_16x16x32_bf16 v[64:67], v[196:199], v[136:139], v[64:67]\n"
        "v_permlane32_swap_b32 v155, v151\n"
        "v_mul_f32 v155, v155, v165\n"
        "v_perm_b32 v42, v45, v44, s63\n"
        "v_perm_b32 v43, v47, v46, s63\n"
        "v_fma_f32 v56, v56, v165, -v155\n"
        "v_fma_f32 v57, v57, v165, -v155\n"
        "v_fma_f32 v58, v58, v165, -v155\n"
        "v_mfma_f32_16x16x32_bf16 v[64:67], v[200:203], v[52:55], v[64:67]\n"
        "buffer_store_dwordx3 v[156:158], v12, s[8:11], s37 offen offset:384 sc0 sc1 nt\n"
        "v_exp_f32 v56, v56\n"
        "v_exp_f32 v57, v57\n"
        "v_exp_f32 v58, v58\n"
        "v_fma_f32 v59, v59, v165, -v155\n"
        "v_add_f32 v158, v56, v57\n"
        "v_exp_f32 v59, v59\n"
        "v_mfma_f32_16x16x32_bf16 v[48:51], v[212:215], v[52:55], 0\n"
        "v_perm_b32 v44, v65, v64, s63\n"
        "v_perm_b32 v45, v67, v66, s63\n"
        "v_fma_f32 v72, v72, v165, -v155\n"
        "v_add_f32 v158, v158, v58\n"
        "v_exp_f32 v72, v72\n"
        "v_fma_f32 v73, v73, v165, -v155\n"
        "v_add_f32 v158, v158, v59\n"
        "v_mfma_f32_16x16x32_bf16 v[48:51], v[208:211], v[136:139], v[48:51]\n"
        "v_exp_f32 v73, v73\n"
        "v_fma_f32 v74, v74, v165, -v155\n"
        "v_add_f32 v158, v158, v72\n"
        "v_exp_f32 v74, v74\n"
        "v_fma_f32 v75, v75, v165, -v155\n"
        "v_sub_f32 v164, v164, v151\n"
        "v_mfma_f32_16x16x32_bf16 v[48:51], v[204:207], v[132:135], v[48:51]\n"
        "v_add_f32 v158, v158, v73\n"
        "v_exp_f32 v75, v75\n"
        "v_fma_f32 v68, v68, v165, -v155\n"
        "v_mul_f32 v164, s58, v164\n"
        "v_add_f32 v158, v158, v74\n"
        "v_exp_f32 v68, v68\n"
        "v_mfma_f32_16x16x32_bf16 v[64:67], v[216:219], v[132:135], 0\n"
        "v_fma_f32 v69, v69, v165, -v155\n"
        "v_perm_b32 v46, v49, v48, s63\n"
        "v_perm_b32 v47, v51, v50, s63\n"
        "v_exp_f32 v164, v164\n"
        "v_add_f32 v158, v158, v75\n"
        "v_exp_f32 v69, v69\n"
        "v_mfma_f32_16x16x32_bf16 v[64:67], v[220:223], v[136:139], v[64:67]\n"
        "v_fma_f32 v70, v70, v165, -v155\n"
        "v_add_f32 v158, v158, v68\n"
        "v_exp_f32 v70, v70\n"
        "v_fma_f32 v71, v71, v165, -v155\n"
        "v_add_f32_dpp v157, v164, v164 row_ror:8 row_mask:0xf bank_mask:0xf\n"
        "v_add_f32 v158, v158, v69\n"
        "v_fma_f32 v76, v76, v165, -v155\n"
        "v_mfma_f32_16x16x32_bf16 v[64:67], v[224:227], v[52:55], v[64:67]\n"
        "v_exp_f32 v71, v71\n"
        "v_add_f32_dpp v157, v157, v157 row_ror:4 row_mask:0xf bank_mask:0xf\n"
        "v_add_f32 v158, v158, v70\n"
        "v_exp_f32 v76, v76\n"
        "v_add_f32_dpp v157, v157, v157 row_ror:2 row_mask:0xf bank_mask:0xf\n"
        "v_add_f32 v158, v158, v71\n"
        "v_mfma_f32_16x16x32_bf16 v[48:51], v[236:239], v[52:55], 0\n"
        "v_add_f32_dpp v157, v157, v157 row_ror:1 row_mask:0xf bank_mask:0xf\n"
        "v_perm_b32 v80, v65, v64, s63\n"
        "v_perm_b32 v81, v67, v66, s63\n"
        "v_add_f32 v158, v158, v76\n"
        "v_mul_f32 v56, v56, v20\n"
        "v_mul_f32 v57, v57, v21\n"
        "v_permlane32_swap_b32 v158, v157\n"
        "v_mfma_f32_16x16x32_bf16 v[48:51], v[232:235], v[136:139], v[48:51]\n"
        "v_add_f32 v158, v158, v157\n"
        "v_mov_b32 v165, v158\n"
        "v_mul_f32 v58, v58, v22\n"
        "v_mul_f32 v59, v59, v23\n"
        "v_permlane16_swap_b32 v158, v165\n"
        "v_add_f32 v158, v158, v165\n"
        "v_mul_f32 v72, v72, v24\n"
        "v_mfma_f32_16x16x32_bf16 v[48:51], v[228:231], v[132:135], v[48:51]\n"
        "v_rcp_f32 v158, v158\n"
        "v_mul_f32 v73, v73, v25\n"
        "v_mov_b32 v157, v158\n"
        "v_mul_f32 v74, v74, v26\n"
        "v_mul_f32 v75, v75, v27\n"
        "v_permlane32_swap_b32 v158, v157\n"
        "v_mfma_f32_16x16x32_bf16 v[64:67], v[124:127], v[40:43], 0\n"
        "v_mul_f32 v56, v56, v158\n"
        "v_perm_b32 v82, v49, v48, s63\n"
        "v_perm_b32 v83, v51, v50, s63\n"
        "v_mul_f32 v57, v57, v158\n"
        "v_mul_f32 v58, v58, v158\n"
        "v_mul_f32 v59, v59, v158\n"
        "v_mul_f32 v72, v72, v158\n"
        "v_mul_f32 v73, v73, v158\n"
        "v_mfma_f32_16x16x32_bf16 v[64:67], v[128:131], v[44:47], v[64:67]\n"
        "v_mul_f32 v74, v74, v158\n"
        "v_mul_f32 v75, v75, v158\n"
        "v_perm_b32 v48, v57, v56, s63\n"
        "v_perm_b32 v49, v59, v58, s63\n"
        "v_perm_b32 v50, v73, v72, s63\n"
        "v_perm_b32 v51, v75, v74, s63\n"
        "v_mul_f32 v68, v68, v28\n"
        "v_mul_f32 v69, v69, v29\n"
        "v_mfma_f32_16x16x32_bf16 v[64:67], v[160:163], v[80:83], v[64:67]\n"
        "v_mul_f32 v70, v70, v30\n"
        "v_mul_f32 v71, v71, v31\n"
        "v_mul_f32 v76, s58, v76\n"
        "v_mul_f32 v68, v68, v158\n"
        "v_mul_f32 v69, v69, v158\n"
        "v_mul_f32 v70, v70, v158\n"
        "v_mul_f32 v71, v71, v158\n"
        "v_fma_f32 v76, v76, v158, v19\n"
        "v_mfma_f32_16x16x32_bf16 v[84:87], v[52:55], v[80:83], 0\n"
        "v_mul_f32 v64, v64, v20\n"
        "v_mul_f32 v65, v65, v21\n"
        "v_mul_f32 v66, v66, v22\n"
        "v_mul_f32 v67, v67, v23\n"
        "v_max3_f32 v165, v64, v65, v66\n"
        "v_perm_b32 v56, v69, v68, s63\n"
        "v_perm_b32 v58, v71, v70, s63\n"
        "v_perm_b32 v57, 0, v76, s63\n"
        "v_mfma_f32_16x16x32_bf16 v[84:87], v[136:139], v[44:47], v[84:87]\n"
        "v_mul_f32 v164, v164, v159\n"
        "v_mul_f32 v164, v164, v157\n"
        "v_perm_b32 v164, 0, v164, s63\n"
        "ds_write_b16 v1, v164\n"
        "v_mfma_f32_16x16x32_bf16 v[84:87], v[132:135], v[40:43], v[84:87]\n"
        "v_mfma_f32_16x16x32_bf16 v[68:71], v[32:35], v[40:43], 0\n"
        "v_mfma_f32_16x16x32_bf16 v[68:71], v[36:39], v[44:47], v[68:71]\n"
        "v_mfma_f32_16x16x32_bf16 v[68:71], v[60:63], v[80:83], v[68:71]\n"
        "v_mfma_f32_16x16x32_bf16 v[72:75], v[88:91], v[80:83], 0\n"
        "v_smfmac_f32_16x16x64_bf16 v[72:75], v[92:95], v[40:47], v4\n"
        "v_mfma_f32_16x16x32_bf16 v[80:83], v[168:171], v[32:35], 0\n"
        "v_mfma_f32_16x16x32_bf16 v[80:83], v[172:175], v[36:39], v[80:83]\n"
        "v_mfma_f32_16x16x32_bf16 v[80:83], v[176:179], v[60:63], v[80:83]\n"
        "v_mul_f32 v84, v84, v24\n"
        "v_mul_f32 v85, v85, v25\n"
        "v_mul_f32 v86, v86, v26\n"
        "v_max3_f32 v165, v165, v67, v84\n"
        "v_mul_f32 v87, v87, v27\n"
        "v_add_f32 v72, v72, v73\n"
        "v_mul_f32 v68, v68, v28\n"
        "v_mul_f32 v72, s58, v72\n"
        "v_mfma_f32_16x16x32_bf16 v[40:43], v[188:191], v[60:63], 0\n"
        "s_mov_b64 exec, s[48:49]\n"
        "v_mov_b32 v72, 0xff800000\n"
        "s_mov_b64 exec, -1\n"
        "v_perm_b32 v76, v81, v80, s63\n"
        "v_perm_b32 v77, v83, v82, s63\n"
        "v_max3_f32 v165, v165, v85, v86\n"
        "v_mul_f32 v69, v69, v29\n"
        "v_mul_f32 v70, v70, v30\n"
        "v_max3_f32 v165, v165, v87, v68\n"
        "v_mfma_f32_16x16x32_bf16 v[40:43], v[184:187], v[36:39], v[40:43]\n"
        "v_mul_f32 v71, v71, v31\n"
        "v_max3_f32 v165, v165, v69, v70\n"
        "v_max3_f32 v165, v165, v71, v72\n"
        "v_mfma_f32_16x16x32_bf16 v[40:43], v[180:183], v[32:35], v[40:43]\n"
        "v_mfma_f32_16x16x32_bf16 v[100:103], v[192:195], v[32:35], 0\n"
        "v_mfma_f32_16x16x32_bf16 v[100:103], v[196:199], v[36:39], v[100:103]\n"
        "v_mfma_f32_16x16x32_bf16 v[100:103], v[200:203], v[60:63], v[100:103]\n"
        "v_mfma_f32_16x16x32_bf16 v[96:99], v[212:215], v[60:63], 0\n"
        "v_mfma_f32_16x16x32_bf16 v[96:99], v[208:211], v[36:39], v[96:99]\n"
        "v_mfma_f32_16x16x32_bf16 v[96:99], v[204:207], v[32:35], v[96:99]\n"
        "v_mfma_f32_16x16x32_bf16 v[44:47], v[216:219], v[32:35], 0\n"
        "v_mfma_f32_16x16x32_bf16 v[44:47], v[220:223], v[36:39], v[44:47]\n"
        "v_perm_b32 v78, v41, v40, s63\n"
        "v_perm_b32 v79, v43, v42, s63\n"
        "v_mfma_f32_16x16x32_bf16 v[44:47], v[224:227], v[60:63], v[44:47]\n"
        "v_perm_b32 v80, v101, v100, s63\n"
        "v_perm_b32 v81, v103, v102, s63\n"
        "v_mfma_f32_16x16x32_bf16 v[40:43], v[236:239], v[60:63], 0\n"
        "v_perm_b32 v82, v97, v96, s63\n"
        "v_perm_b32 v83, v99, v98, s63\n"
        "v_mfma_f32_16x16x32_bf16 v[40:43], v[232:235], v[36:39], v[40:43]\n"
        "v_mfma_f32_16x16x32_bf16 v[40:43], v[228:231], v[32:35], v[40:43]\n"
        "v_mfma_f32_16x16x32_bf16 v[108:111], v[124:127], v[76:79], 0\n"
        "v_perm_b32 v96, v45, v44, s63\n"
        "v_perm_b32 v97, v47, v46, s63\n"
        "v_mfma_f32_16x16x32_bf16 v[108:111], v[128:131], v[80:83], v[108:111]\n"
        "s_nop 3\n"
        "v_perm_b32 v98, v41, v40, s63\n"
        "v_perm_b32 v99, v43, v42, s63\n"
        "s_nop 1\n"
        "v_mfma_f32_16x16x32_bf16 v[108:111], v[160:163], v[96:99], v[108:111]\n"
        "v_mfma_f32_16x16x32_bf16 v[140:143], v[52:55], v[96:99], 0\n"
        "v_mfma_f32_16x16x32_bf16 v[140:143], v[136:139], v[80:83], v[140:143]\n"
        "v_mfma_f32_16x16x32_bf16 v[140:143], v[132:135], v[76:79], v[140:143]\n"
        "v_mfma_f32_16x16x32_bf16 v[100:103], v[32:35], v[76:79], 0\n"
        "v_mfma_f32_16x16x32_bf16 v[100:103], v[36:39], v[80:83], v[100:103]\n"
        "v_mfma_f32_16x16x32_bf16 v[100:103], v[60:63], v[96:99], v[100:103]\n"
        "v_mfma_f32_16x16x32_bf16 v[44:47], v[88:91], v[96:99], 0\n"
        "v_smfmac_f32_16x16x64_bf16 v[44:47], v[92:95], v[76:83], v4\n"
        "v_mul_f32 v108, v108, v20\n"
        "v_mul_f32 v109, v109, v21\n"
        "v_mul_f32 v110, v110, v22\n"
        "v_mul_f32 v111, v111, v23\n"
        "v_mul_f32 v140, v140, v24\n"
        "v_max3_f32 v159, v108, v109, v110\n"
        "v_mul_f32 v141, v141, v25\n"
        "v_mfma_f32_16x16x32_bf16 v[60:63], v[124:127], v[240:243], 0\n"
        "v_add_f32 v44, v44, v45\n"
        "v_mul_f32 v142, v142, v26\n"
        "v_max3_f32 v159, v159, v111, v140\n"
        "v_mul_f32 v143, v143, v27\n"
        "v_mul_f32 v100, v100, v28\n"
        "v_mul_f32 v44, s58, v44\n"
        "v_max3_f32 v159, v159, v141, v142\n"
        "v_mul_f32 v101, v101, v29\n"
        "v_mfma_f32_16x16x32_bf16 v[60:63], v[128:131], v[244:247], v[60:63]\n"
        "s_mov_b64 exec, s[48:49]\n"
        "v_mov_b32 v44, 0xff800000\n"
        "s_mov_b64 exec, -1\n"
        "v_mul_f32 v102, v102, v30\n"
        "v_max3_f32 v159, v159, v143, v100\n"
        "v_mul_f32 v103, v103, v31\n"
        "v_max3_f32 v159, v159, v101, v102\n"
        "v_max3_f32 v159, v159, v103, v44\n"
        "v_mfma_f32_16x16x32_bf16 v[60:63], v[160:163], v[248:251], v[60:63]\n"
        "ds_read_b128 v[32:35], v17 offset:40064\n"
        "v_permlane32_swap_b32 v165, v159\n"
        "v_max_f32 v165, v165, v159\n"
        "v_mov_b32 v164, v165\n"
        "v_mfma_f32_16x16x32_bf16 v[76:79], v[52:55], v[248:251], 0\n"
        "ds_read_b128 v[36:39], v17 offset:40128\n"
        "v_permlane16_swap_b32 v165, v164\n"
        "v_max_f32 v165, v165, v164\n"
        "v_perm_b32 v40, v61, v60, s63\n"
        "v_perm_b32 v41, v63, v62, s63\n"
        "v_mov_b32 v159, v165\n"
        "v_mfma_f32_16x16x32_bf16 v[76:79], v[136:139], v[244:247], v[76:79]\n"
        "ds_read_b128 v[60:63], v17 offset:40192\n"
        "v_permlane32_swap_b32 v165, v159\n"
        "v_mul_f32 v165, v165, v166\n"
        "v_mul_f32 v159, v159, v167\n"
        "v_fma_f32 v64, v64, v166, -v165\n"
        "v_fma_f32 v65, v65, v166, -v165\n"
        "v_fma_f32 v108, v108, v167, -v159\n"
        "v_mfma_f32_16x16x32_bf16 v[76:79], v[132:135], v[240:243], v[76:79]\n"
        "s_waitcnt lgkmcnt(2)\n"
        "ds_read_b128 v[96:99], v0 offset:5136\n"
        "v_fma_f32 v109, v109, v167, -v159\n"
        "v_exp_f32 v64, v64\n"
        "v_exp_f32 v65, v65\n"
        "v_fma_f32 v66, v66, v166, -v165\n"
        "v_fma_f32 v110, v110, v167, -v159\n"
        "v_mfma_f32_16x16x32_bf16 v[80:83], v[32:35], v[240:243], 0\n"
        "s_waitcnt lgkmcnt(2)\n"
        "ds_read_b128 v[104:107], v0 offset:4112\n"
        "v_perm_b32 v42, v77, v76, s63\n"
        "v_perm_b32 v43, v79, v78, s63\n"
        "v_exp_f32 v108, v108\n"
        "v_exp_f32 v109, v109\n"
        "v_fma_f32 v67, v67, v166, -v165\n"
        "v_mfma_f32_16x16x32_bf16 v[80:83], v[36:39], v[244:247], v[80:83]\n"
        "s_waitcnt lgkmcnt(2)\n"
        "v_exp_f32 v66, v66\n"
        "v_exp_f32 v110, v110\n"
        "v_fma_f32 v111, v111, v167, -v159\n"
        "v_add_f32 v95, v64, v65\n"
        "v_exp_f32 v67, v67\n"
        "v_mfma_f32_16x16x32_bf16 v[80:83], v[60:63], v[248:251], v[80:83]\n"
        "s_waitcnt lgkmcnt(1)\n"
        "v_fma_f32 v84, v84, v166, -v165\n"
        "v_add_f32 v164, v108, v109\n"
        "v_exp_f32 v111, v111\n"
        "v_fma_f32 v140, v140, v167, -v159\n"
        "v_add_f32 v95, v95, v66\n"
        "v_exp_f32 v84, v84\n"
        "v_mfma_f32_16x16x32_bf16 v[112:115], v[60:63], v[96:99], 0\n"
        "s_waitcnt lgkmcnt(0)\n"
        "v_perm_b32 v76, v81, v80, s63\n"
        "v_perm_b32 v80, v83, v82, s63\n"
        "v_fma_f32 v85, v85, v166, -v165\n"
        "v_add_f32 v164, v164, v110\n"
        "v_exp_f32 v140, v140\n"
        "v_fma_f32 v141, v141, v167, -v159\n"
        "v_add_f32 v95, v95, v67\n"
        "v_mfma_f32_16x16x32_bf16 v[112:115], v[36:39], v[104:107], v[112:115]\n"
        "v_exp_f32 v85, v85\n"
        "v_fma_f32 v86, v86, v166, -v165\n"
        "v_add_f32 v164, v164, v111\n"
        "v_exp_f32 v141, v141\n"
        "v_fma_f32 v142, v142, v167, -v159\n"
        "v_add_f32 v95, v95, v84\n"
        "v_mfma_f32_16x16x32_bf16 v[112:115], v[32:35], v[252:255], v[112:115]\n"
        "v_exp_f32 v86, v86\n"
        "v_fma_f32 v87, v87, v166, -v165\n"
        "v_add_f32 v164, v164, v140\n"
        "v_exp_f32 v142, v142\n"
        "v_fma_f32 v143, v143, v167, -v159\n"
        "v_add_f32 v95, v95, v85\n"
        "v_mfma_f32_16x16x32_bf16 v[120:123], v[132:135], v[252:255], 0\n"
        "v_exp_f32 v87, v87\n"
        "v_perm_b32 v88, v113, v112, s63\n"
        "v_perm_b32 v92, v115, v114, s63\n"
        "v_fma_f32 v68, v68, v166, -v165\n"
        "v_add_f32 v164, v164, v141\n"
        "v_exp_f32 v143, v143\n"
        "v_mfma_f32_16x16x32_bf16 v[120:123], v[136:139], v[104:107], v[120:123]\n"
        "v_fma_f32 v100, v100, v167, -v159\n"
        "v_add_f32 v95, v95, v86\n"
        "v_exp_f32 v68, v68\n"
        "v_fma_f32 v69, v69, v166, -v165\n"
        "v_add_f32 v164, v164, v142\n"
        "v_exp_f32 v100, v100\n"
        "v_mfma_f32_16x16x32_bf16 v[120:123], v[52:55], v[96:99], v[120:123]\n"
        "ds_read_b128 v[144:147], v0 offset:6176\n"
        "v_fma_f32 v101, v101, v167, -v159\n"
        "v_add_f32 v95, v95, v87\n"
        "v_exp_f32 v69, v69\n"
        "v_fma_f32 v70, v70, v166, -v165\n"
        "v_add_f32 v164, v164, v143\n"
        "v_fma_f32 v102, v102, v167, -v159\n"
        "v_mfma_f32_16x16x32_bf16 v[148:151], v[160:163], v[96:99], 0\n"
        "ds_read_b128 v[116:119], v0 offset:7200\n"
        "v_perm_b32 v98, v121, v120, s63\n"
        "v_perm_b32 v99, v123, v122, s63\n"
        "v_exp_f32 v101, v101\n"
        "v_add_f32 v95, v95, v68\n"
        "v_exp_f32 v70, v70\n"
        "v_mfma_f32_16x16x32_bf16 v[148:151], v[128:131], v[104:107], v[148:151]\n"
        "ds_read_b128 v[112:115], v0 offset:8224\n"
        "v_fma_f32 v71, v71, v166, -v165\n"
        "v_add_f32 v164, v164, v100\n"
        "v_exp_f32 v102, v102\n"
        "v_fma_f32 v103, v103, v167, -v159\n"
        "v_add_f32 v95, v95, v69\n"
        "v_fma_f32 v72, v72, v166, -v165\n"
        "v_mfma_f32_16x16x32_bf16 v[148:151], v[124:127], v[252:255], v[148:151]\n"
        "s_waitcnt lgkmcnt(2)\n"
        "v_exp_f32 v71, v71\n"
        "v_add_f32 v164, v164, v101\n"
        "v_exp_f32 v103, v103\n"
        "v_fma_f32 v44, v44, v167, -v159\n"
        "v_add_f32 v95, v95, v70\n"
        "v_add_f32 v164, v164, v102\n"
        "v_mfma_f32_16x16x32_bf16 v[120:123], v[124:127], v[144:147], 0\n"
        "s_waitcnt lgkmcnt(1)\n"
        "v_perm_b32 v96, v149, v148, s63\n"
        "v_perm_b32 v97, v151, v150, s63\n"
        "v_exp_f32 v72, v72\n"
        "v_exp_f32 v44, v44\n"
        "v_add_f32 v95, v95, v71\n"
        "v_add_f32 v164, v164, v103\n"
        "v_mfma_f32_16x16x32_bf16 v[120:123], v[128:131], v[116:119], v[120:123]\n"
        "s_waitcnt lgkmcnt(0)\n"
        "v_add_f32 v95, v95, v72\n"
        "v_add_f32 v164, v164, v44\n"
        "v_mul_f32 v64, v64, v20\n"
        "v_mul_f32 v65, v65, v21\n"
        "v_permlane32_swap_b32 v95, v164\n"
        "v_add_f32 v95, v95, v164\n"
        "v_mov_b32 v167, v95\n"
        "v_mfma_f32_16x16x32_bf16 v[120:123], v[160:163], v[112:115], v[120:123]\n"
        "v_mul_f32 v66, v66, v22\n"
        "v_permlane16_swap_b32 v95, v167\n"
        "v_add_f32 v95, v95, v167\n"
        "v_rcp_f32 v95, v95\n"
        "v_mul_f32 v67, v67, v23\n"
        "v_mov_b32 v164, v95\n"
        "v_mfma_f32_16x16x32_bf16 v[156:159], v[52:55], v[112:115], 0\n"
        "v_mul_f32 v84, v84, v24\n"
        "v_perm_b32 v104, v121, v120, s63\n"
        "v_perm_b32 v105, v123, v122, s63\n"
        "v_permlane32_swap_b32 v95, v164\n"
        "v_mul_f32 v85, v85, v25\n"
        "v_mul_f32 v86, v86, v26\n"
        "v_mul_f32 v87, v87, v27\n"
        "v_mfma_f32_16x16x32_bf16 v[156:159], v[136:139], v[116:119], v[156:159]\n"
        "v_mul_f32 v64, v64, v95\n"
        "v_mul_f32 v65, v65, v95\n"
        "v_mul_f32 v66, v66, v95\n"
        "v_mul_f32 v67, v67, v95\n"
        "v_mul_f32 v84, v84, v95\n"
        "v_mul_f32 v85, v85, v95\n"
        "v_mul_f32 v86, v86, v95\n"
        "v_mul_f32 v87, v87, v95\n"
        "v_mfma_f32_16x16x32_bf16 v[156:159], v[132:135], v[144:147], v[156:159]\n"
        "ds_read_b128 v[148:151], v0 offset:11312\n"
        "v_perm_b32 v120, v65, v64, s63\n"
        "v_perm_b32 v121, v67, v66, s63\n"
        "v_perm_b32 v122, v85, v84, s63\n"
        "v_perm_b32 v123, v87, v86, s63\n"
        "v_mul_f32 v68, v68, v28\n"
        "v_mul_f32 v69, v69, v29\n"
        "v_mul_f32 v70, v70, v30\n"
        "v_mfma_f32_16x16x32_bf16 v[152:155], v[32:35], v[144:147], 0\n"
        "ds_read_b128 v[84:87], v0 offset:10288\n"
        "v_perm_b32 v106, v157, v156, s63\n"
        "v_perm_b32 v107, v159, v158, s63\n"
        "v_mul_f32 v71, v71, v31\n"
        "v_mul_f32 v72, s58, v72\n"
        "v_mul_f32 v68, v68, v95\n"
        "v_mul_f32 v69, v69, v95\n"
        "v_mul_f32 v70, v70, v95\n"
        "v_mfma_f32_16x16x32_bf16 v[152:155], v[36:39], v[116:119], v[152:155]\n"
        "ds_read_b128 v[144:147], v0 offset:9264\n"
        "v_mul_f32 v71, v71, v95\n"
        "v_fma_f32 v72, v72, v95, v19\n"
        "v_perm_b32 v64, v69, v68, s63\n"
        "v_perm_b32 v66, v71, v70, s63\n"
        "v_perm_b32 v65, 0, v72, s63\n"
        "v_mul_f32 v108, v108, v20\n"
        "v_mul_f32 v109, v109, v21\n"
        "v_mfma_f32_16x16x32_bf16 v[152:155], v[60:63], v[112:115], v[152:155]\n"
        "s_waitcnt lgkmcnt(2)\n"
        "v_mul_f32 v110, v110, v22\n"
        "v_mul_f32 v111, v111, v23\n"
        "v_mul_f32 v140, v140, v24\n"
        "v_mul_f32 v141, v141, v25\n"
        "v_mul_f32 v142, v142, v26\n"
        "v_mul_f32 v143, v143, v27\n"
        "v_mul_f32 v108, v108, v164\n"
        "v_perm_b32 v112, v153, v152, s63\n"
        "v_mfma_f32_16x16x32_bf16 v[20:23], v[60:63], v[148:151], 0\n"
        "s_waitcnt lgkmcnt(1)\n"
        "v_perm_b32 v116, v155, v154, s63\n"
        "v_mul_f32 v109, v109, v164\n"
        "v_mul_f32 v110, v110, v164\n"
        "v_mul_f32 v111, v111, v164\n"
        "v_mul_f32 v140, v140, v164\n"
        "v_mul_f32 v141, v141, v164\n"
        "v_mul_f32 v142, v142, v164\n"
        "v_mul_f32 v143, v143, v164\n"
        "v_mfma_f32_16x16x32_bf16 v[20:23], v[36:39], v[84:87], v[20:23]\n"
        "s_waitcnt lgkmcnt(0)\n"
        "v_perm_b32 v24, v109, v108, s63\n"
        "v_perm_b32 v25, v111, v110, s63\n"
        "v_perm_b32 v26, v141, v140, s63\n"
        "v_perm_b32 v27, v143, v142, s63\n"
        "v_mul_f32 v100, v100, v28\n"
        "v_mul_f32 v101, v101, v29\n"
        "v_mul_f32 v102, v102, v30\n"
        "v_mul_f32 v103, v103, v31\n"
        "v_mfma_f32_16x16x32_bf16 v[20:23], v[32:35], v[144:147], v[20:23]\n"
        "v_mul_f32 v44, s58, v44\n"
        "v_mul_f32 v100, v100, v164\n"
        "v_mul_f32 v101, v101, v164\n"
        "v_mul_f32 v102, v102, v164\n"
        "v_mul_f32 v103, v103, v164\n"
        "v_fma_f32 v44, v44, v164, v19\n"
        "v_perm_b32 v28, v101, v100, s63\n"
        "v_perm_b32 v30, v103, v102, s63\n"
        "v_mfma_f32_16x16x32_bf16 v[68:71], v[132:135], v[144:147], 0\n"
        "v_perm_b32 v77, v21, v20, s63\n"
        "v_perm_b32 v81, v23, v22, s63\n"
        "v_perm_b32 v29, 0, v44, s63\n"
        "v_mfma_f32_16x16x32_bf16 v[68:71], v[136:139], v[84:87], v[68:71]\n"
        "v_mfma_f32_16x16x32_bf16 v[68:71], v[52:55], v[148:151], v[68:71]\n"
        "ds_read_b128 v[20:23], v0 offset:12352\n"
        "v_mfma_f32_16x16x32_bf16 v[72:75], v[160:163], v[148:151], 0\n"
        "ds_read_b128 v[108:111], v0 offset:13376\n"
        "v_mfma_f32_16x16x32_bf16 v[72:75], v[128:131], v[84:87], v[72:75]\n"
        "ds_read_b128 v[140:143], v0 offset:14400\n"
        "v_mfma_f32_16x16x32_bf16 v[72:75], v[124:127], v[144:147], v[72:75]\n"
        "s_waitcnt lgkmcnt(2)\n"
        "v_mfma_f32_16x16x32_bf16 v[144:147], v[124:127], v[20:23], 0\n"
        "v_perm_b32 v46, v69, v68, s63\n"
        "v_perm_b32 v47, v71, v70, s63\n"
        "s_waitcnt lgkmcnt(1)\n"
        "v_mfma_f32_16x16x32_bf16 v[144:147], v[128:131], v[108:111], v[144:147]\n"
        "s_waitcnt lgkmcnt(0)\n"
        "v_mfma_f32_16x16x32_bf16 v[144:147], v[160:163], v[140:143], v[144:147]\n"
        "v_perm_b32 v44, v73, v72, s63\n"
        "v_perm_b32 v45, v75, v74, s63\n"
        "v_mfma_f32_16x16x32_bf16 v[148:151], v[52:55], v[140:143], 0\n"
        "v_mfma_f32_16x16x32_bf16 v[148:151], v[136:139], v[108:111], v[148:151]\n"
        "v_mfma_f32_16x16x32_bf16 v[148:151], v[132:135], v[20:23], v[148:151]\n"
        "ds_read_b128 v[72:75], v0 offset:17488\n"
        "v_mfma_f32_16x16x32_bf16 v[84:87], v[32:35], v[20:23], 0\n"
        "ds_read_b128 v[68:71], v0 offset:16464\n"
        "v_perm_b32 v100, v145, v144, s63\n"
        "v_perm_b32 v101, v147, v146, s63\n"
        "v_mfma_f32_16x16x32_bf16 v[84:87], v[36:39], v[108:111], v[84:87]\n"
        "ds_read_b128 v[20:23], v0 offset:15440\n"
        "v_mfma_f32_16x16x32_bf16 v[84:87], v[60:63], v[140:143], v[84:87]\n"
        "v_perm_b32 v102, v149, v148, s63\n"
        "v_perm_b32 v103, v151, v150, s63\n"
        "s_waitcnt lgkmcnt(2)\n"
        "v_mfma_f32_16x16x32_bf16 v[108:111], v[60:63], v[72:75], 0\n"
        "s_waitcnt lgkmcnt(1)\n"
        "v_mfma_f32_16x16x32_bf16 v[108:111], v[36:39], v[68:71], v[108:111]\n"
        "s_waitcnt lgkmcnt(0)\n"
        "v_mfma_f32_16x16x32_bf16 v[108:111], v[32:35], v[20:23], v[108:111]\n"
        "ds_read2_b32 v[82:83], v11 offset0:0 offset1:3\n"
        "v_perm_b32 v89, v85, v84, s63\n"
        "v_perm_b32 v93, v87, v86, s63\n"
        "v_mov_b32_dpp v59, v19 row_shr:4 row_mask:0xf bank_mask:0xf bound_ctrl:0\n"
        "v_mfma_f32_16x16x32_bf16 v[36:39], v[132:135], v[20:23], 0\n"
        "ds_read_b64_tr_b16 v[78:79], v9 offset:40064\n"
        "v_mfma_f32_16x16x32_bf16 v[36:39], v[136:139], v[68:71], v[36:39]\n"
        "ds_read2_b32 v[94:95], v11 offset0:1 offset1:4\n"
        "v_perm_b32 v113, v109, v108, s63\n"
        "v_perm_b32 v117, v111, v110, s63\n"
        "v_mfma_f32_16x16x32_bf16 v[36:39], v[52:55], v[72:75], v[36:39]\n"
        "s_add_u32 s31, s31, 1\n"
        "ds_read_b64_tr_b16 v[90:91], v9 offset:40080\n"
        "v_mfma_f32_16x16x32_bf16 v[32:35], v[160:163], v[72:75], 0\n"
        "s_cmp_ge_u32 s31, s71\n"
        "ds_read2_b32 v[118:119], v11 offset0:2 offset1:5\n"
        "v_mfma_f32_16x16x32_bf16 v[32:35], v[128:131], v[68:71], v[32:35]\n"
        "s_cbranch_scc1 L52B_%=\n"
        "L53B_%=:\n"
        "ds_read_b64_tr_b16 v[114:115], v9 offset:40096\n"
        "v_perm_b32 v110, v37, v36, s63\n"
        "v_perm_b32 v111, v39, v38, s63\n"
        "v_mfma_f32_16x16x32_bf16 v[32:35], v[124:127], v[20:23], v[32:35]\n"
        "s_add_u32 s16, s16, 256\n"
        "v_mfma_f32_16x16x32_bf16 v[20:23], v[48:51], v[40:43], 0\n"
        "s_waitcnt lgkmcnt(4)\n"
        "s_cmp_ge_u32 s31, 20\n"
        "v_smfmac_f32_16x16x64_bf16 v[20:23], v[56:59], v[76:83], v18\n"
        "s_cselect_b32 s39, s22, s16\n"
        "v_mfma_f32_16x16x32_bf16 v[36:39], v[48:51], v[96:99], 0\n"
        "s_waitcnt lgkmcnt(2)\n"
        "v_perm_b32 v108, v33, v32, s63\n"
        "v_perm_b32 v109, v35, v34, s63\n"
        "s_cmp_ge_u32 s31, 21\n"
        "v_smfmac_f32_16x16x64_bf16 v[36:39], v[56:59], v[88:95], v18\n"
        "s_cselect_b32 s39, s23, s39\n"
        "ds_read_b64_tr_b16 v[78:79], v9 offset:40112\n"
        "v_mfma_f32_16x16x32_bf16 v[32:35], v[48:51], v[104:107], 0\n"
        "s_waitcnt lgkmcnt(1)\n"
        "s_mul_hi_u32 s38, s39, 58835169\n"
        "v_smfmac_f32_16x16x64_bf16 v[32:35], v[56:59], v[112:119], v18\n"
        "s_mul_i32 s39, s39, 1344\n"
        "ds_read_b64_tr_b16 v[90:91], v9 offset:40128\n"
        "v_cvt_pk_bf16_f32 v152, v20, v36\n"
        "v_cvt_pk_bf16_f32 v156, v21, v37\n"
        "v_cvt_pk_bf16_f32 v160, v22, v38\n"
        "v_cvt_pk_bf16_f32 v164, v23, v39\n"
        "v_mfma_f32_16x16x32_bf16 v[68:71], v[48:51], v[44:47], 0\n"
        "s_mul_i32 s38, s38, 588672\n"
        "s_waitcnt lgkmcnt(1)\n"
        "v_smfmac_f32_16x16x64_bf16 v[68:71], v[56:59], v[76:83], v18 abid:1\n"
        "s_add_u32 s39, s39, s38\n"
        "ds_read_b64_tr_b16 v[114:115], v9 offset:40144\n"
        "v_mfma_f32_16x16x32_bf16 v[60:63], v[48:51], v[100:103], 0\n"
        "s_add_u32 s39, s39, s17\n"
        "ds_read_b128 v[36:39], v2\n"
        "v_mov_b64 v[20:21], 0\n"
        "v_mov_b64 v[22:23], 0\n"
        "s_waitcnt lgkmcnt(2)\n"
        "v_smfmac_f32_16x16x64_bf16 v[60:63], v[56:59], v[88:95], v18 abid:1\n"
        "s_mul_i32 s68, s31, 3\n"
        "ds_read_b128 v[52:55], v3\n"
        "v_cvt_pk_bf16_f32 v153, v32, v68\n"
        "v_cvt_pk_bf16_f32 v157, v33, v69\n"
        "v_cvt_pk_bf16_f32 v161, v34, v70\n"
        "v_cvt_pk_bf16_f32 v165, v35, v71\n"
        "v_mfma_f32_16x16x32_bf16 v[68:71], v[48:51], v[108:111], 0\n"
        "s_bitcmp1_b32 s31, 0\n"
        "v_mov_b64 v[32:33], 0\n"
        "v_mov_b64 v[34:35], 0\n"
        "s_waitcnt lgkmcnt(2)\n"
        "v_smfmac_f32_16x16x64_bf16 v[68:71], v[56:59], v[112:119], v18 abid:1\n"
        "s_cselect_b32 s38, 3, 6\n"
        "s_waitcnt lgkmcnt(1)\n"
        "v_mov_b32_dpp v67, v19 row_shr:4 row_mask:0xf bank_mask:0xf bound_ctrl:0\n"
        "v_smfmac_f32_16x16x64_bf16 v[20:23], v[36:39], v[40:47], v4\n"
        "s_add_u32 s68, s68, s38\n"
        "v_mov_b64 v[48:49], 0\n"
        "v_mov_b64 v[50:51], 0\n"
        "s_waitcnt lgkmcnt(0)\n"
        "v_smfmac_f32_16x16x64_bf16 v[20:23], v[52:55], v[76:83], v4 abid:1\n"
        "s_add_u32 s37, s35, 0\n"
        "s_cmp_lt_u32 s31, s14\n"
        "v_cvt_pk_bf16_f32 v154, v60, v68\n"
        "v_cvt_pk_bf16_f32 v158, v61, v69\n"
        "v_cvt_pk_bf16_f32 v162, v62, v70\n"
        "v_cvt_pk_bf16_f32 v166, v63, v71\n"
        "v_smfmac_f32_16x16x64_bf16 v[32:35], v[36:39], v[96:103], v4\n"
        "s_cselect_b32 s68, s68, 0\n"
        "v_add_f32 v20, v20, v21\n"
        "v_add_f32 v22, v22, v23\n"
        "v_smfmac_f32_16x16x64_bf16 v[32:35], v[52:55], v[88:95], v4 abid:1\n"
        "s_and_b32 s38, s31, 7\n"
        "ds_read_b64_tr_b16 v[78:79], v9 offset:43392\n"
        "v_smfmac_f32_16x16x64_bf16 v[48:51], v[36:39], v[104:111], v4\n"
        "buffer_store_dwordx3 v[152:154], v12, s[8:11], s37 offen sc0 sc1 nt\n"
        "s_cselect_b32 s30, 1, -7\n"
        "v_smfmac_f32_16x16x64_bf16 v[48:51], v[52:55], v[112:119], v4 abid:1\n"
        "s_mul_i32 s29, s30, 192\n"
        "ds_read_b64_tr_b16 v[90:91], v9 offset:43408\n"
        "v_add_f32 v32, v32, v33\n"
        "v_add_f32 v34, v34, v35\n"
        "v_cvt_pk_bf16_f32 v148, v20, v32\n"
        "v_mfma_f32_16x16x32_bf16 v[152:155], v[120:123], v[40:43], 0\n"
        "s_mul_i32 s30, s30, 392\n"
        "s_waitcnt lgkmcnt(1)\n"
        "v_smfmac_f32_16x16x64_bf16 v[152:155], v[64:67], v[76:83], v18\n"
        "s_add_u32 s37, s35, 0\n"
        "s_cmp_eq_u32 s69, s18\n"
        "ds_read_b64_tr_b16 v[114:115], v9 offset:43424\n"
        "v_add_f32 v48, v48, v49\n"
        "v_add_f32 v50, v50, v51\n"
        "v_cvt_pk_bf16_f32 v149, v48, v22\n"
        "v_cvt_pk_bf16_f32 v150, v34, v50\n"
        "v_mfma_f32_16x16x32_bf16 v[56:59], v[120:123], v[96:99], 0\n"
        "s_cselect_b32 s34, -7488, s70\n"
        "s_waitcnt lgkmcnt(1)\n"
        "v_smfmac_f32_16x16x64_bf16 v[56:59], v[64:67], v[88:95], v18\n"
        "s_add_u32 s69, s69, s34\n"
        "ds_read_b64_tr_b16 v[78:79], v9 offset:43440\n"
        "v_writelane_b32 v10, s69, 56\n"
        "v_add_u32 v11, s30, v11\n"
        "s_mov_b64 exec, s[54:55]\n"
        "v_add_u32 v16, s29, v16\n"
        "v_add_u32 v8, s34, v8\n"
        "s_mov_b64 exec, -1\n"
        "v_mfma_f32_16x16x32_bf16 v[72:75], v[120:123], v[104:107], 0\n"
        "buffer_store_dwordx3 v[156:158], v15, s[8:11], s37 offen offset:768 sc0 sc1 nt\n"
        "s_mov_b64 exec, s[50:51]\n"
        "v_add_u32 v5, s29, v5\n"
        "v_add_u32 v6, s34, v6\n"
        "s_mov_b64 exec, -1\n"
        "s_waitcnt lgkmcnt(1)\n"
        "v_smfmac_f32_16x16x64_bf16 v[72:75], v[64:67], v[112:119], v18\n"
        "ds_read_b64_tr_b16 v[90:91], v9 offset:43456\n"
        "v_mov_b32_dpp v31, v19 row_shr:4 row_mask:0xf bank_mask:0xf bound_ctrl:0\n"
        "v_mfma_f32_16x16x32_bf16 v[20:23], v[120:123], v[44:47], 0\n"
        "s_waitcnt lgkmcnt(1)\n"
        "v_smfmac_f32_16x16x64_bf16 v[20:23], v[64:67], v[76:83], v18 abid:1\n"
        "s_add_u32 s37, s35, 98112\n"
        "ds_read_b64_tr_b16 v[114:115], v9 offset:43472\n"
        "v_mfma_f32_16x16x32_bf16 v[84:87], v[120:123], v[100:103], 0\n"
        "s_waitcnt lgkmcnt(1)\n"
        "v_smfmac_f32_16x16x64_bf16 v[84:87], v[64:67], v[88:95], v18 abid:1\n"
        "ds_read_b64_tr_b16 v[78:79], v9 offset:46720\n"
        "v_mfma_f32_16x16x32_bf16 v[60:63], v[120:123], v[108:111], 0\n"
        "buffer_store_dwordx3 v[160:162], v12, s[8:11], s37 offen offset:192 sc0 sc1 nt\n"
        "ds_read_b32 v167, v0 offset:21600\n"
        "s_waitcnt lgkmcnt(2)\n"
        "v_smfmac_f32_16x16x64_bf16 v[60:63], v[64:67], v[112:119], v18 abid:1\n"
        "ds_read_b64_tr_b16 v[90:91], v9 offset:46736\n"
        "v_mfma_f32_16x16x32_bf16 v[68:71], v[24:27], v[40:43], 0\n"
        "s_waitcnt lgkmcnt(2)\n"
        "v_smfmac_f32_16x16x64_bf16 v[68:71], v[28:31], v[76:83], v18\n"
        "s_add_u32 s37, s35, 98112\n"
        "ds_read_b64_tr_b16 v[114:115], v9 offset:46752\n"
        "v_mfma_f32_16x16x32_bf16 v[36:39], v[24:27], v[96:99], 0\n"
        "s_waitcnt lgkmcnt(1)\n"
        "v_smfmac_f32_16x16x64_bf16 v[36:39], v[28:31], v[88:95], v18\n"
        "ds_read_b64_tr_b16 v[78:79], v9 offset:46768\n"
        "v_readfirstlane_b32 s40, v167\n"
        "v_mfma_f32_16x16x32_bf16 v[40:43], v[24:27], v[104:107], 0\n"
        "buffer_store_dwordx3 v[164:166], v14, s[8:11], s37 offen offset:960 sc0 sc1 nt\n"
        "s_add_u32 s37, s35, 588672\n"
        "s_add_u32 s46, s37, 1344\n"
        "s_cmp_lt_i32 s40, s68\n"
        "s_waitcnt lgkmcnt(1)\n"
        "v_smfmac_f32_16x16x64_bf16 v[40:43], v[28:31], v[112:119], v18\n"
        "buffer_store_dwordx3 v[148:150], v12, s[44:47], s37 offen offset:1152 sc0 sc1 nt\n"
        "ds_read_b64_tr_b16 v[90:91], v9 offset:46784\n"
        "v_mfma_f32_16x16x32_bf16 v[64:67], v[24:27], v[44:47], 0\n"
        "s_waitcnt lgkmcnt(1)\n"
        "v_smfmac_f32_16x16x64_bf16 v[64:67], v[28:31], v[76:83], v18 abid:1\n"
        "ds_read_b64_tr_b16 v[114:115], v9 offset:46800\n"
        "s_mov_b64 exec, 1\n"
        "ds_inc_u32 v0, v1 offset:21612\n"
        "s_mov_b64 exec, -1\n"
        "s_cbranch_scc1 L56B_%=\n"
        "L57B_%=:\n"
        "ds_read_b128 v[124:127], v7 offset:0\n"
        "ds_read_b128 v[132:135], v7 offset:3328\n"
        "ds_read_b128 v[128:131], v7 offset:64\n"
        "ds_read_b128 v[136:139], v7 offset:3392\n"
        "v_mfma_f32_16x16x32_bf16 v[104:107], v[24:27], v[100:103], 0\n"
        "s_waitcnt lgkmcnt(6)\n"
        "ds_read_b128 v[160:163], v7 offset:128\n"
        "ds_read_b128 v[52:55], v7 offset:3456\n"
        "s_mov_b32 s36, s35\n"
        "v_smfmac_f32_16x16x64_bf16 v[104:107], v[28:31], v[88:95], v18 abid:1\n"
        "s_mov_b32 s35, s39\n"
        "v_mfma_f32_16x16x32_bf16 v[88:91], v[24:27], v[108:111], 0\n"
        "s_waitcnt lgkmcnt(7)\n"
        "v_smfmac_f32_16x16x64_bf16 v[88:91], v[28:31], v[112:119], v18 abid:1\n"
        "s_setprio 0\n"
        "s_waitcnt lgkmcnt(0)\n"
        "s_nop 15\n"
        "s_branch CNEXTB_%=\n"
        :: "{v0}"(tid), "{s[0:1]}"(kap), "{s2}"(bid), "{s[4:5]}"(tab) : CLOBBERS);
}

extern "C" void kernel_launch(void* const* d_in, const int* in_sizes, int n_in, void* d_out, int out_size, void* d_ws, size_t ws_size, hipStream_t stream) {
    (void)in_sizes; (void)n_in; (void)out_size; (void)ws_size;
    (void)hipFuncSetAttribute((const void*)swin_kernel, hipFuncAttributeMaxDynamicSharedMemorySize, LDS_BYTES);
    swin_kernel<<<256, 512, LDS_BYTES, stream>>>((const char*)d_in[0], (const char*)d_in[1], (const char*)d_in[2], (const char*)d_in[3], (const char*)d_in[4], (char*)d_out, (const char*)d_ws);
}
```

```cpp
#define LTOK_ 208
#define FPAD 32
#define NPR 36
#define NWC 22
#define ASP 0
#define LK0 0
#define LK1 0
#define LK2 1
#define LK3 1
#include <hip/hip_runtime.h>
#include <hip/hip_bf16.h>

constexpr int TOKB = 192, ROWB = 511 * TOKB;
constexpr unsigned FRAMEB = 511u * ROWB;
constexpr int LTOK = LTOK_;
constexpr int LFRAME = 48 * LTOK + FPAD;
constexpr int LBUF = 4 * LFRAME;
constexpr int LSTASH = 10 * 4 * LTOK;
constexpr int LSTG = 6208;
constexpr int VST = 392, LEXT = 1536 + 8 * VST;
constexpr int WT = 3088;
constexpr int LDS_AUX = (54 - NPR) / 3 * WT, LDS_TAB = LDS_AUX + 1024, LDS_CNT = LDS_TAB + 2048, LDS_BUF = LDS_CNT + 128, LDS_STASH = LDS_BUF + 2 * LBUF,
              LDS_STG = LDS_STASH + LSTASH, LDS_SCRAP = LDS_STG + 4 * LSTG, LDS_EXT = LDS_SCRAP + 512, LDS_BYTES = 163840;
static_assert(NWC >= 18 && NPR % 3 == 0 && LDS_EXT + 4 * LEXT + 256 <= LDS_BYTES && NPR * 1024 <= LBUF, "LDS");
constexpr unsigned RAWM = 18432, RAW_T = LDS_BUF, RAW_TV = RAW_T + 2 * RAWM + 128, RAW_SV = (36 - NPR) / 3 * WT;
static_assert(RAW_TV + 2 * RAWM <= LDS_STASH && RAWM + 6 * WT <= 2 * RAWM + 128, "fold LDS");

constexpr int phi(int v) { return v ^ (v >> 1); }
struct Tab {
    unsigned img[3][64][4];
    unsigned p[4][7][64][4];
    unsigned c[4][2][64][4];
    unsigned d[4][7][64][4];
    unsigned e[8][2][64][4];
    static constexpr void job(unsigned (*q)[64][4], int lane, unsigned L, unsigned R, int mode, int tau, unsigned st0, unsigned st1) {
        const int i = lane & 15, g = lane >> 4;
        for (int a = 0; a < 2; ++a) {
            const unsigned ra = (8 * (i >> 2) + 4 * a + (i & 3)) * 192 + ((g ^ ((2 * (i >> 2) + a) & 3)) * 16);
            q[0][lane][a] = L + ra;
            q[0][lane][2 + a] = mode == 0 ? R + tau * 6144 + ra : mode == 2 ? R + tau * 6144 + (16 * a + i) * 192 + ((g ^ (i >> 2)) * 16)
                                          : R + tau * 64 + (8 * g + 4 * a + (i >> 2)) * 192 + (((i & 3) ^ ((2 * g + a) & 3)) * 16);
        }
        q[1][lane][0] = st0; q[1][lane][1] = st1; q[1][lane][2] = LDS_SCRAP + g * 32;
    }
    static constexpr unsigned scat(bool win, int lane, int tau, int tb) {
        const int i = lane & 15, c = 32 * tau + 8 * (i >> 2) + 4 * tb + (i & 3), r = c / 24, o = c % 24, q = win ? o / 6 : o & 3, t = win ? o % 6 : o >> 2;
        return (win ? RAW_SV : 3 * t < NPR - 18 ? LDS_BUF + 18 * 1024 : -(NPR - 18) / 3 * WT) + t * WT + ((lane & 48) + 4 * q + r) * 16;
    }
    constexpr Tab() : img{}, p{}, c{}, d{}, e{} {
        for (int z = 0; z < 48; ++z) img[0][z >> 2][z & 3] = 0x3f803f80u;
        img[0][12][0] = 0x7f800000u;
        for (int z = 0; z < 8; ++z) img[0][36 + 10 + z][z >> 1] = z & 1 ? 0x3f800000u : 0x3f80u;
        for (int k = 0; k < 4; ++k) for (int lane = 0; lane < 64; ++lane) {
            const int i = lane & 15, g = lane >> 4, rowv = 8 * (i >> 2) + (i & 3);
            unsigned vi[20] = {};
            {
                const int pp = i >> 2, t = i & 3;
                const int t1 = (k + 3 - LK1) & 3;
                if (t1 < 3) job(p[k], lane, RAW_TV, RAW_TV + RAWM, 1, t1, scat(false, lane, t1, 0), scat(false, lane, t1, 1));
                const unsigned wl = lane * 16, lm = pp == g ? ~0u : 0u, idp = LDS_AUX + 576 + (t == g ? 10 + pp : 0) * 16, sidx = i & 1 ? 0xeeeeddddu : 0x4444ddddu;
                unsigned vA[3] = {};
                for (int m = 0; m < 3; ++m) {
                    const int n = 64 * m + lane, tf = n / 96, q = n % 96 / 12, s = (n % 12) ^ tf;
                    const unsigned cc = tf * FRAMEB + (3 * (s & 3) + (s >> 2)) * 16 + 2048 - 1024 * m;
                    vA[m] = cc + q * TOKB;
                    img[1][lane][m] = (cc + 6 * ROWB + 6 * TOKB) | ((unsigned)(2 * (q & 3) + (q >> 2)) << 28);
                }
                const unsigned vA0 = vA[0], vA1 = vA[1], vA2 = vA[2], vB0 = vA0 + (ROWB - 7 * TOKB), vB1 = vA1 + (ROWB - 7 * TOKB), vB2 = vA2 + (ROWB - 7 * TOKB);
                const unsigned dA = LDS_BUF + t * LFRAME + pp * LTOK + g * 16, rA = LDS_BUF + t * LFRAME + (pp == g ? pp : 4 + g) * LTOK + 192;
                img[1][lane][3] = (2 * pp * (4 * LTOK) + t * LTOK + g * 16) | (((pp == g ? 2 * pp : 2 * g + 1) * (4 * LTOK) + t * LTOK + 192) << 16);
                const unsigned cd0 = k < 3 ? dA + k * 8 * LTOK : LDS_STASH + (img[1][lane][3] & 0xffffu), cr0 = k < 3 ? rA + k * 8 * LTOK : LDS_STASH + (img[1][lane][3] >> 16);
                const unsigned xr = LDS_STG + k * LSTG + (t >> 1) * 3104 + ((t & 1) * 8 + pp) * TOKB + (g ^ (t & 1)) * 16;
                const unsigned axp = LDS_AUX + (t > ASP ? 384 : 0);
                const unsigned pf = g * FRAMEB + (i >> 1) * TOKB + (i & 1) * 188 + 2048;
                vi[0] = xr; vi[1] = lm; vi[2] = rA; vi[3] = dA; vi[4] = vA0; vi[5] = vA1; vi[6] = vA2; vi[7] = vB0; vi[8] = vB1; vi[9] = vB2; vi[10] = sidx; vi[11] = cd0; vi[12] = cr0; vi[13] = wl; vi[14] = idp; vi[15] = pf; vi[16] = axp;
                for (int z = 0; z < 20; ++z) p[k][2 + (z >> 2)][lane][z & 3] = vi[z];
            }
            {
                const int t0 = (k + 3 - LK0) & 3;
                const unsigned s0 = LDS_BUF + t0 * 6144 + lane * 16, s3 = LDS_BUF + k * 2 * WT + lane * 16, rs = RAW_T + 128;
                if (t0 < 3) job(c[k], lane, RAW_T, RAW_T + RAWM, 2, t0, s0, s0 + 3072); else job(c[k], lane, RAW_T, RAW_T + RAWM, 1, 0, LDS_SCRAP + rowv * 4, 0);
                if (k < 3) job(e[k], lane, rs, rs + RAWM, 0, k, s3, s3 + WT); else job(e[k], lane, rs, rs + RAWM, 1, 0, LDS_SCRAP + rowv * 4, 0);
                if (k > 0) job(e[4 + k], lane, RAW_TV, RAW_TV + RAWM, 1, k - 1, scat(true, lane, k - 1, 0), scat(true, lane, k - 1, 1));
                const int dg = 16 * (2 * (g & 1) + (g >> 1)), pix = 4 * (i & 3) + phi(i >> 2);
                const unsigned wl = lane * 16, fr = LDS_BUF + k * LFRAME, ext = LDS_EXT + k * LEXT;
                const unsigned ta = fr + pix * LTOK + dg, zr = LDS_AUX + 384 + g * 16, t30 = i & 3 ? zr : LDS_STASH + k * LTOK + dg;
                const unsigned xa = fr + (4 * (i >> 2) + phi(g)) * LTOK + 96 * (i & 1) + 8 * ((i >> 1) & 1);
                const int sg = 8 * ((i >> 1) & 1) + 4 * (i >> 3) + 2 * ((i >> 2) & 1) + (i & 1);
                const unsigned tv = fr + (32 + 4 * (sg & 3) + phi(sg >> 2)) * LTOK + dg, c1f = (i >> 2) == 2 * g ? 0x3f800000u : 0u;
                const unsigned sidx = g < 2 ? 0x0e0e0404u | ((i >> 2) == 2 * g ? (i & 3) * 0x00100010u : (i >> 2) == 2 * g + 1 ? (i & 3) * 0x40004000u : 0u) : 0xeeee4444u;
                const unsigned ga0 = phi(g) * TOKB + 48 * (i & 3) + 12 * (i >> 2), wr = ROWB - 7 * TOKB;
                const unsigned ga1 = ga0 + (phi(g) >= 1 ? wr : 0), ga2 = ga0 + (phi(g) >= 2 ? wr : 0), ga3 = ga0 + (phi(g) >= 3 ? wr : 0);
                const unsigned pb = LDS_SCRAP + k * 128, pr0 = i < 4 && (i >> 1) == (g >> 1) ? pb + (i & 1) * 32 + (g & 1) * 16 : zr, pr1 = i == 0 || i == 2 ? pb + 64 + g * 16 : zr;
                const unsigned pw = g < 2 ? pb + ((i >> 1) & 1) * 32 + (i >> 3) * 16 + ((i >> 2) & 1) * 8 + g * 4 + (i & 1) * 2
                                          : pb + 64 + (i >> 2) * 16 + (g & 1) * 4 + ((i >> 1) & 1) * 8 + (i & 1) * 2 + (lane == 57 ? 6 : 0);
                const unsigned six2 = (i & 1 ? 0xeeeeu : 0x4444u) | ((i & 2 ? 0xeeee0000u : 0x44440000u) & (g < 2 ? 0x0f0f0000u : 0xffff0000u));
                const unsigned exs0 = i & 2 ? LDS_AUX + g * 16 : ext + (i & 1) * 64 + g * 16, t3s0 = i & 2 ? zr : LDS_STASH + k * LTOK + 64 * (g >> 1) + 16 * (g & 1) + 4 * (i & 1);
                const unsigned ex0 = i & 1 ? zr : i & 2 ? LDS_AUX + g * 16 : ext + g * 16, exv0 = ext + 1536 + i * 24;
                const unsigned rla0 = lane < 48 ? fr + (16 * g + pix) * LTOK + 192 : lane == 56 ? LDS_STASH + k * LTOK + 192 : LDS_AUX + 192;
                img[2][lane][0] = ((i & 7) * (4 * LTOK) + dg) | ((((4 * g + (i & 3)) & 7) * (4 * LTOK) + 96 * ((i >> 2) & 1) + 8 * (i >> 3)) << 16);
                img[2][lane][1] = ((i & 7) * 192u + g * 16) | ((1536u + i * 24 + (g & 1) * 4 * VST) << 16);
                img[2][lane][2] = ((fr + phi(g) * LTOK + 192 - xa) & 0xffffu) | ((fr + pix * LTOK + 192 - xa) << 16);
                img[2][lane][3] = (LDS_AUX + 576 + (10 + (i & 3)) * 16) | ((1536u + (i & 7) * VST + 384) << 16);
                vi[0] = wl; vi[1] = pw; vi[2] = pr0; vi[3] = pr1; vi[4] = six2; vi[5] = exs0; vi[6] = t3s0; vi[7] = ta; vi[8] = t30; vi[9] = xa; vi[10] = rla0; vi[11] = exv0; vi[12] = ga0; vi[13] = ga1; vi[14] = ga2; vi[15] = ga3; vi[16] = ex0; vi[17] = tv; vi[18] = sidx; vi[19] = c1f;
                for (int z = 0; z < 20; ++z) d[k][2 + (z >> 2)][lane][z & 3] = vi[z];
            }
        }
    }
};
static __device__ const Tab TAB __attribute__((aligned(128))) = Tab();

#define CLOBBERS "v1", "v2", "v3", "v4", "v5", "v6", "v7", "v8", "v9", "v10", "v11", "v12", "v13", "v14", "v15", "v16", "v17", "v18", "v19", "v20", "v21", "v22", "v23", "v24", "v25", "v26", "v27", "v28", "v29", "v30", "v31", "v32", "v33", "v34", "v35", "v36", "v37", "v38", "v39", "v40", "v41", "v42", "v43", "v44", "v45", "v46", "v47", "v48", "v49", "v50", "v51", "v52", "v53", "v54", "v55", "v56", "v57", "v58", "v59", "v60", "v61", "v62", "v63", "v64", "v65", "v66", "v67", "v68", "v69", "v70", "v71", "v72", "v73", "v74", "v75", "v76", "v77", "v78", "v79", "v80", "v81", "v82", "v83", "v84", "v85", "v86", "v87", "v88", "v89", "v90", "v91", "v92", "v93", "v94", "v95", "v96", "v97", "v98", "v99", "v100", "v101", "v102", "v103", "v104", "v105", "v106", "v107", "v108", "v109", "v110", "v111", "v112", "v113", "v114", "v115", "v116", "v117", "v118", "v119", "v120", "v121", "v122", "v123", "v124", "v125", "v126", "v127", "v128", "v129", "v130", "v131", "v132", "v133", "v134", "v135", "v136", "v137", "v138", "v139", "v140", "v141", "v142", "v143", "v144", "v145", "v146", "v147", "v148", "v149", "v150", "v151", "v152", "v153", "v154", "v155", "v156", "v157", "v158", "v159", "v160", "v161", "v162", "v163", "v164", "v165", "v166", "v167", "v168", "v169", "v170", "v171", "v172", "v173", "v174", "v175", "v176", "v177", "v178", "v179", "v180", "v181", "v182", "v183", "v184", "v185", "v186", "v187", "v188", "v189", "v190", "v191", "v192", "v193", "v194", "v195", "v196", "v197", "v198", "v199", "v200", "v201", "v202", "v203", "v204", "v205", "v206", "v207", "v208", "v209", "v210", "v211", "v212", "v213", "v214", "v215", "v216", "v217", "v218", "v219", "v220", "v221", "v222", "v223", "v224", "v225", "v226", "v227", "v228", "v229", "v230", "v231", "v232", "v233", "v234", "v235", "v236", "v237", "v238", "v239", "v240", "v241", "v242", "v243", "v244", "v245", "v246", "v247", "v248", "v249", "v250", "v251", "v252", "v253", "v254", "v255", "s3", "s6", "s7", "s8", "s9", "s10", "s11", "s12", "s13", "s14", "s15", "s16", "s17", "s18", "s19", "s20", "s21", "s22", "s23", "s24", "s25", "s26", "s27", "s28", "s29", "s30", "s31", "s34", "s35", "s36", "s37", "s38", "s39", "s40", "s41", "s42", "s43", "s44", "s45", "s46", "s47", "s48", "s49", "s50", "s51", "s52", "s53", "s54", "s55", "s56", "s57", "s58", "s59", "s60", "s61", "s62", "s63", "s64", "s65", "s66", "s67", "s68", "s69", "s70", "s71", "s72", "s73", "s74", "s75", "s76", "s77", "vcc", "scc", "memory"
__global__ __launch_bounds__(512, 2) void swin_kernel(const char* x, const char* wqkv_t, const char* wo_t, const char* wqkv_s, const char* wo_s, char* out, const char* ws) {
    const unsigned long long kap = (unsigned long long)__builtin_amdgcn_kernarg_segment_ptr(), tab = (unsigned long long)&TAB;
    const unsigned tid = threadIdx.x, bid = blockIdx.x;
    asm volatile(
        "s_load_dwordx8 s[44:51], s[0:1], 0x0\n"
        "s_load_dwordx4 s[52:55], s[0:1], 0x20\n"
        "s_load_dwordx2 s[56:57], s[0:1], 0x30\n"
        "v_readfirstlane_b32 s3, v0\n"
        "v_and_b32 v56, 63, v0\n"
        "v_lshlrev_b32 v57, 4, v56\n"
        "s_bfe_u32 s3, s3, 0x30006\n"
        "s_and_b32 s6, s3, 3\n"
        "s_cmp_eq_u32 s2, 0\n"
        "s_cbranch_scc1 MPRO_%=\n"
        "PRO_%=:\n"
        "s_mul_i32 s64, s3, 9216\n"
        "s_add_u32 s64, s64, 21728\n"
        "s_mov_b32 s27, 0x20000\n"
        "s_cmp_lt_u32 s3, 4\n"
        "s_cbranch_scc0 PPRO_%=\n"
        "s_mul_i32 s7, s6, 2048\n"
        "s_add_u32 s7, s7, 31744\n"
        "s_add_u32 s22, s4, s7\n"
        "s_addc_u32 s23, s5, 0\n"
        "global_load_dwordx4 v[240:243], v57, s[22:23] offset:0\n"
        "global_load_dwordx4 v[244:247], v57, s[22:23] offset:1024\n"
        "s_mov_b32 s65, 13824\n"
        "v_add_u32 v24, 0, v56\n"
        "v_mul_u32_u24 v21, 171, v24\n"
        "v_lshrrev_b32 v21, 11, v21\n"
        "v_mad_i32_i24 v24, v21, -12, v24\n"
        "v_bfe_u32 v58, v21, 2, 2\n"
        "v_xor_b32 v58, v24, v58\n"
        "v_and_b32 v27, 7, v21\n"
        "v_mul_u32_u24 v27, 576, v27\n"
        "v_lshl_add_u32 v58, v58, 4, v27\n"
        "v_add_u32 v25, 64, v56\n"
        "v_mul_u32_u24 v22, 171, v25\n"
        "v_lshrrev_b32 v22, 11, v22\n"
        "v_mad_i32_i24 v25, v22, -12, v25\n"
        "v_bfe_u32 v59, v22, 2, 2\n"
        "v_xor_b32 v59, v25, v59\n"
        "v_and_b32 v27, 7, v22\n"
        "v_mul_u32_u24 v27, 576, v27\n"
        "v_lshl_add_u32 v59, v59, 4, v27\n"
        "v_lshrrev_b32 v27, 3, v22\n"
        "v_mad_u32_u24 v59, v27, s65, v59\n"
        "v_add_u32 v26, 128, v56\n"
        "v_mul_u32_u24 v23, 171, v26\n"
        "v_lshrrev_b32 v23, 11, v23\n"
        "v_mad_i32_i24 v26, v23, -12, v26\n"
        "v_bfe_u32 v60, v23, 2, 2\n"
        "v_xor_b32 v60, v26, v60\n"
        "v_and_b32 v27, 7, v23\n"
        "v_mul_u32_u24 v27, 576, v27\n"
        "v_lshl_add_u32 v60, v60, 4, v27\n"
        "v_add_u32 v60, s65, v60\n"
        "s_lshr_b32 s61, s6, 1\n"
        "s_mul_i32 s61, s61, 192\n"
        "s_mov_b32 s62, 27648\n"
        "s_mov_b32 s63, 4608\n"
        "s_add_u32 s7, s62, s63\n"
        "s_lshl_b32 s29, s63, 1\n"
        "s_add_u32 s60, s7, s63\n"
        "s_bitcmp1_b32 s6, 0\n"
        "s_cselect_b32 s28, s7, 0\n"
        "s_cselect_b32 s29, s29, s62\n"
        "s_cselect_b32 s60, s60, s63\n"
        "s_add_u32 s28, s28, s61\n"
        "s_add_u32 s29, s29, s61\n"
        "s_add_u32 s60, s60, s61\n"
        "s_mov_b32 s26, 55296\n"
        "s_waitcnt lgkmcnt(0)\n"
        "s_mov_b32 s24, s46\n"
        "s_and_b32 s25, s47, 0xffff\n"
        "s_add_u32 m0, s64, 0\n"
        "s_nop 0\n"
        "buffer_load_dwordx4 v58, s[24:27], s28 offen lds\n"
        "s_add_u32 m0, s64, 1024\n"
        "s_nop 0\n"
        "buffer_load_dwordx4 v59, s[24:27], s28 offen lds\n"
        "s_add_u32 m0, s64, 2048\n"
        "s_nop 0\n"
        "buffer_load_dwordx4 v60, s[24:27], s28 offen lds\n"
        "s_add_u32 m0, s64, 3072\n"
        "s_nop 0\n"
        "buffer_load_dwordx4 v58, s[24:27], s29 offen lds\n"
        "s_add_u32 m0, s64, 4096\n"
        "s_nop 0\n"
        "buffer_load_dwordx4 v59, s[24:27], s29 offen lds\n"
        "s_add_u32 m0, s64, 5120\n"
        "s_nop 0\n"
        "buffer_load_dwordx4 v60, s[24:27], s29 offen lds\n"
        "s_add_u32 m0, s64, 6144\n"
        "s_nop 0\n"
        "buffer_load_dwordx4 v58, s[24:27], s60 offen lds\n"
        "s_add_u32 m0, s64, 7168\n"
        "s_nop 0\n"
        "buffer_load_dwordx4 v59, s[24:27], s60 offen lds\n"
        "s_add_u32 m0, s64, 8192\n"
        "s_nop 0\n"
        "buffer_load_dwordx4 v60, s[24:27], s60 offen lds\n"
        "s_mov_b64 s[24:25], s[56:57]\n"
        "s_mov_b64 exec, 1\n"
        "v_bfe_u32 v27, s2, 3, 5\n"
        "v_lshlrev_b32 v27, 7, v27\n"
        "global_load_dword v253, v27, s[24:25] offset:256 sc1\n"
        "s_mov_b64 exec, -1\n"
        "s_barrier\n"
        "s_setprio 1\n"
        "v_mov_b32 v250, 21600\n"
        "s_mov_b32 s41, 0x400000\n"
        "s_and_b32 s7, s2, 7\n"
        "s_lshl_b32 s7, s7, 5\n"
        "s_lshr_b32 s14, s2, 3\n"
        "s_add_u32 s7, s7, s14\n"
        "s_sub_u32 s7, 255, s7\n"
        "s_and_b32 s16, s7, 255\n"
        "s_sub_u32 s7, s7, s16\n"
        "s_mul_i32 s7, s7, 20\n"
        "s_add_u32 s7, s7, s16\n"
        "s_mov_b32 s14, 22\n"
        "s_cmp_eq_u32 s2, 0\n"
        "s_cselect_b32 s43, 19, 20\n"
        "s_mul_i32 s15, s7, 57457\n"
        "s_lshr_b32 s15, s15, 22\n"
        "s_mov_b32 s16, s7\n"
        "s_mul_i32 s15, s15, 588672\n"
        "s_mul_i32 s7, s16, 1344\n"
        "s_add_u32 s15, s15, s7\n"
        "s_mov_b32 s8, s54\n"
        "s_and_b32 s9, s55, 0xffff\n"
        "s_mov_b32 s10, 200540928\n"
        "s_mov_b32 s11, 0x20000\n"
        "s_mov_b64 s[12:13], s[54:55]\n"
        "s_mul_i32 s17, s6, 50135232\n"
        "s_mul_i32 s18, s6, 208\n"
        "s_add_u32 s18, s18, 109536\n"
        "s_mul_i32 s19, s6, 4672\n"
        "s_add_u32 s19, s19, 135520\n"
        "s_mul_i32 s7, s6, 7168\n"
        "s_add_u32 s7, s7, 43008\n"
        "s_add_u32 s22, s4, s7\n"
        "s_addc_u32 s23, s5, 0\n"
        "s_waitcnt vmcnt(0)\n"
        "s_mov_b64 exec, 1\n"
        "ds_inc_u32 v250, v250 offset:16\n"
        "s_mov_b64 exec, -1\n"
        "L68_%=:\n"
        "ds_read_b32 v251, v250 offset:16\n"
        "s_waitcnt lgkmcnt(0)\n"
        "v_readfirstlane_b32 s40, v251\n"
        "s_nop 3\n"
        "s_cmp_ge_i32 s40, 4\n"
        "s_cbranch_scc1 L69_%=\n"
        "s_nop 0\n"
        "s_sub_u32 s41, s41, 1\n"
        "s_cbranch_scc0 L68_%=\n"
        "s_endpgm\n"
        "L69_%=:\n"
        "s_cmp_eq_u32 s6, 0\n"
        "s_cbranch_scc1 L72_%=\n"
        "s_mov_b32 s34, 0x4162ccfd\n"
        "s_mov_b32 s35, 21624\n"
        "s_mov_b32 s36, 21624\n"
        "s_mov_b32 s37, 21628\n"
        "s_call_b64 s[30:31], FOLDM_%=\n"
        "s_branch L73_%=\n"
        "L72_%=:\n"
        "s_mov_b32 s34, 0xbe173353\n"
        "s_mov_b32 s35, 21656\n"
        "s_mov_b32 s36, 21656\n"
        "s_mov_b32 s37, 21624\n"
        "s_call_b64 s[30:31], FOLDR_%=\n"
        "L73_%=:\n"
        "s_setprio 2\n"
        "global_load_dwordx4 v[0:3], v57, s[22:23] offset:-1024\n"
        "global_load_dwordx4 v[4:7], v57, s[22:23] offset:0\n"
        "global_load_dwordx4 v[8:11], v57, s[22:23] offset:1024\n"
        "global_load_dwordx4 v[12:15], v57, s[22:23] offset:2048\n"
        "global_load_dwordx4 v[16:19], v57, s[22:23] offset:3072\n"
        "s_cmp_lg_u32 s6, 0\n"
        "s_cbranch_scc1 PY_%=\n"
        "v_readfirstlane_b32 s26, v253\n"
        "s_cmp_eq_u32 s2, 0\n"
        "s_cbranch_scc1 L71_%=\n"
        "L70_%=:\n"
        "s_mov_b64 exec, 1\n"
        "v_bfe_u32 v27, s2, 3, 5\n"
        "v_lshlrev_b32 v27, 7, v27\n"
        "global_load_dword v253, v27, s[24:25] offset:256 sc1\n"
        "s_mov_b64 exec, -1\n"
        "s_waitcnt vmcnt(0)\n"
        "v_readfirstlane_b32 s7, v253\n"
        "s_nop 3\n"
        "s_cmp_lg_u32 s7, s26\n"
        "s_cbranch_scc1 L71_%=\n"
        "s_sleep 2\n"
        "s_sub_u32 s41, s41, 64\n"
        "s_cbranch_scc0 L70_%=\n"
        "s_endpgm\n"
        "L71_%=:\n"
        "s_mov_b64 exec, 1\n"
        "ds_inc_u32 v250, v250 offset:36\n"
        "s_mov_b64 exec, -1\n"
        "s_branch PX_%=\n"
        "PY_%=:\n"
        "PX_%=:\n"
        "s_mul_i32 s7, s6, 9216\n"
        "s_add_u32 s7, s7, 12288\n"
        "s_add_u32 s70, s56, s7\n"
        "s_addc_u32 s71, s57, 0\n"
        "v_add_u32 v28, 4096, v57\n"
        "L74_%=:\n"
        "ds_read_b32 v251, v250 offset:32\n"
        "s_waitcnt lgkmcnt(0)\n"
        "v_readfirstlane_b32 s40, v251\n"
        "s_nop 3\n"
        "s_cmp_ge_i32 s40, 4\n"
        "s_cbranch_scc1 L75_%=\n"
        "s_nop 0\n"
        "s_sub_u32 s41, s41, 1\n"
        "s_cbranch_scc0 L74_%=\n"
        "s_endpgm\n"
        "L75_%=:\n"
        "L76_%=:\n"
        "ds_read_b32 v251, v250 offset:36\n"
        "s_waitcnt lgkmcnt(0)\n"
        "v_readfirstlane_b32 s40, v251\n"
        "s_nop 3\n"
        "s_cmp_ge_i32 s40, 1\n"
        "s_cbranch_scc1 L77_%=\n"
        "s_sleep 1\n"
        "s_sub_u32 s41, s41, 1\n"
        "s_cbranch_scc0 L76_%=\n"
        "s_endpgm\n"
        "L77_%=:\n"
        "global_load_dwordx4 v[64:67], v57, s[70:71] offset:-4096 sc1\n"
        "global_load_dwordx4 v[68:71], v57, s[70:71] offset:-3072 sc1\n"
        "global_load_dwordx4 v[72:75], v57, s[70:71] offset:-2048 sc1\n"
        "global_load_dwordx4 v[76:79], v57, s[70:71] offset:-1024 sc1\n"
        "global_load_dwordx4 v[80:83], v57, s[70:71] offset:0 sc1\n"
        "global_load_dwordx4 v[84:87], v57, s[70:71] offset:1024 sc1\n"
        "global_load_dwordx4 v[88:91], v57, s[70:71] offset:2048 sc1\n"
        "global_load_dwordx4 v[92:95], v57, s[70:71] offset:3072 sc1\n"
        "global_load_dwordx4 v[96:99], v28, s[70:71] sc1\n"
        "s_cmp_lt_u32 s6, 2\n"
        "s_cselect_b32 s7, 80320, 0\n"
        "s_mul_i32 s28, s6, 9264\n"
        "s_add_u32 s7, s7, s28\n"
        "s_add_u32 s7, s7, -18528\n"
        "v_add_u32 v27, s7, v57\n"
        "s_waitcnt vmcnt(8)\n"
        "ds_write_b128 v27, v[64:67] offset:0\n"
        "s_waitcnt vmcnt(7)\n"
        "ds_write_b128 v27, v[68:71] offset:1024\n"
        "s_waitcnt vmcnt(6)\n"
        "ds_write_b128 v27, v[72:75] offset:2048\n"
        "s_waitcnt vmcnt(5)\n"
        "ds_write_b128 v27, v[76:79] offset:3088\n"
        "s_waitcnt vmcnt(4)\n"
        "ds_write_b128 v27, v[80:83] offset:4112\n"
        "s_waitcnt vmcnt(3)\n"
        "ds_write_b128 v27, v[84:87] offset:5136\n"
        "s_waitcnt vmcnt(2)\n"
        "ds_write_b128 v27, v[88:91] offset:6176\n"
        "s_waitcnt vmcnt(1)\n"
        "ds_write_b128 v27, v[92:95] offset:7200\n"
        "s_waitcnt vmcnt(0)\n"
        "ds_write_b128 v27, v[96:99] offset:8224\n"
        "s_mov_b64 exec, 1\n"
        "ds_inc_u32 v250, v250 offset:52\n"
        "s_mov_b64 exec, -1\n"
        "s_branch CONS_%=\n"
        "FOLDM_%=:\n"
        "ds_read_b128 v[88:91], v242\n"
        "ds_read_b128 v[92:95], v240\n"
        "ds_read_b128 v[180:183], v242 offset:64\n"
        "ds_read_b128 v[172:175], v240 offset:64\n"
        "ds_read_b128 v[184:187], v242 offset:128\n"
        "ds_read_b128 v[164:167], v240 offset:128\n"
        "ds_read_b128 v[168:171], v241\n"
        "ds_read_b128 v[100:103], v241 offset:64\n"
        "ds_read_b128 v[104:107], v241 offset:128\n"
        "ds_read_b128 v[140:143], v240 offset:6144\n"
        "ds_read_b128 v[132:135], v240 offset:6208\n"
        "ds_read_b128 v[96:99], v240 offset:6272\n"
        "ds_read_b128 v[136:139], v241 offset:6144\n"
        "ds_read_b128 v[128:131], v241 offset:6208\n"
        "ds_read_b128 v[108:111], v241 offset:6272\n"
        "s_waitcnt lgkmcnt(13)\n"
        "v_mfma_f32_16x16x32_bf16 v[64:67], v[92:95], v[88:91], 0\n"
        "s_waitcnt lgkmcnt(11)\n"
        "ds_read_b128 v[152:155], v240 offset:12288\n"
        "v_mfma_f32_16x16x32_bf16 v[64:67], v[172:175], v[180:183], v[64:67]\n"
        "s_waitcnt lgkmcnt(8)\n"
        "ds_read_b128 v[160:163], v240 offset:12352\n"
        "v_mfma_f32_16x16x32_bf16 v[64:67], v[164:167], v[184:187], v[64:67]\n"
        "ds_read_b128 v[112:115], v240 offset:12416\n"
        "v_mfma_f32_16x16x32_bf16 v[68:71], v[168:171], v[88:91], 0\n"
        "ds_read_b128 v[116:119], v241 offset:12288\n"
        "v_mfma_f32_16x16x32_bf16 v[68:71], v[100:103], v[180:183], v[68:71]\n"
        "s_waitcnt lgkmcnt(4)\n"
        "ds_read_b128 v[120:123], v241 offset:12352\n"
        "v_mfma_f32_16x16x32_bf16 v[68:71], v[104:107], v[184:187], v[68:71]\n"
        "ds_read_b128 v[124:127], v241 offset:12416\n"
        "v_mfma_f32_16x16x32_bf16 v[72:75], v[140:143], v[88:91], 0\n"
        "ds_read_b128 v[144:147], v243\n"
        "v_mfma_f32_16x16x32_bf16 v[72:75], v[132:135], v[180:183], v[72:75]\n"
        "ds_read_b128 v[148:151], v243 offset:64\n"
        "v_mfma_f32_16x16x32_bf16 v[72:75], v[96:99], v[184:187], v[72:75]\n"
        "ds_read_b128 v[156:159], v243 offset:128\n"
        "v_mfma_f32_16x16x32_bf16 v[76:79], v[136:139], v[88:91], 0\n"
        "v_add_f32 v176, v64, v68\n"
        "v_add_f32 v177, v65, v69\n"
        "v_add_f32 v178, v66, v70\n"
        "v_add_f32 v179, v67, v71\n"
        "v_mfma_f32_16x16x32_bf16 v[76:79], v[128:131], v[180:183], v[76:79]\n"
        "v_mfma_f32_16x16x32_bf16 v[76:79], v[108:111], v[184:187], v[76:79]\n"
        "s_waitcnt lgkmcnt(3)\n"
        "v_add_f32 v176, v176, v72\n"
        "v_add_f32 v177, v177, v73\n"
        "v_add_f32 v178, v178, v74\n"
        "v_add_f32 v179, v179, v75\n"
        "v_mfma_f32_16x16x32_bf16 v[80:83], v[152:155], v[88:91], 0\n"
        "v_mfma_f32_16x16x32_bf16 v[80:83], v[160:163], v[180:183], v[80:83]\n"
        "v_mfma_f32_16x16x32_bf16 v[80:83], v[112:115], v[184:187], v[80:83]\n"
        "v_add_f32 v176, v176, v76\n"
        "v_add_f32 v177, v177, v77\n"
        "v_add_f32 v178, v178, v78\n"
        "v_add_f32 v179, v179, v79\n"
        "v_mfma_f32_16x16x32_bf16 v[84:87], v[116:119], v[88:91], 0\n"
        "v_mfma_f32_16x16x32_bf16 v[84:87], v[120:123], v[180:183], v[84:87]\n"
        "v_mfma_f32_16x16x32_bf16 v[84:87], v[124:127], v[184:187], v[84:87]\n"
        "s_waitcnt lgkmcnt(0)\n"
        "v_add_f32 v176, v176, v80\n"
        "v_add_f32 v177, v177, v81\n"
        "v_add_f32 v178, v178, v82\n"
        "v_add_f32 v179, v179, v83\n"
        "v_mov_b32 v239, s35\n"
        "s_mov_b64 exec, 1\n"
        "ds_inc_u32 v239, v239 offset:0\n"
        "s_mov_b64 exec, -1\n"
        "v_add_f32 v176, v176, v84\n"
        "v_add_f32 v177, v177, v85\n"
        "v_add_f32 v178, v178, v86\n"
        "v_add_f32 v179, v179, v87\n"
        "v_add_f32 v176, v176, v177\n"
        "v_add_f32 v178, v178, v179\n"
        "v_add_f32 v176, v176, v178\n"
        "v_mov_b32 v177, v176\n"
        "v_mfma_f32_16x16x32_bf16 v[88:91], v[92:95], v[144:147], 0\n"
        "v_mfma_f32_16x16x32_bf16 v[88:91], v[172:175], v[148:151], v[88:91]\n"
        "v_permlane32_swap_b32 v176, v177\n"
        "v_add_f32 v176, v176, v177\n"
        "v_mov_b32 v177, v176\n"
        "v_mfma_f32_16x16x32_bf16 v[88:91], v[164:167], v[156:159], v[88:91]\n"
        "v_mfma_f32_16x16x32_bf16 v[92:95], v[168:171], v[144:147], 0\n"
        "v_permlane16_swap_b32 v176, v177\n"
        "v_add_f32 v176, v176, v177\n"
        "v_mul_f32 v176, 0xbc2aaaab, v176\n"
        "v_mul_f32 v176, s34, v176\n"
        "v_fma_f32 v64, v64, s34, v176\n"
        "v_fma_f32 v65, v65, s34, v176\n"
        "v_fma_f32 v66, v66, s34, v176\n"
        "v_fma_f32 v67, v67, s34, v176\n"
        "v_fma_f32 v68, v68, s34, v176\n"
        "v_fma_f32 v69, v69, s34, v176\n"
        "v_fma_f32 v70, v70, s34, v176\n"
        "v_fma_f32 v71, v71, s34, v176\n"
        "v_fma_f32 v72, v72, s34, v176\n"
        "v_fma_f32 v73, v73, s34, v176\n"
        "v_fma_f32 v74, v74, s34, v176\n"
        "v_fma_f32 v75, v75, s34, v176\n"
        "v_fma_f32 v76, v76, s34, v176\n"
        "v_fma_f32 v77, v77, s34, v176\n"
        "v_fma_f32 v78, v78, s34, v176\n"
        "v_fma_f32 v79, v79, s34, v176\n"
        "v_fma_f32 v80, v80, s34, v176\n"
        "v_fma_f32 v81, v81, s34, v176\n"
        "v_fma_f32 v82, v82, s34, v176\n"
        "v_fma_f32 v83, v83, s34, v176\n"
        "v_fma_f32 v84, v84, s34, v176\n"
        "v_fma_f32 v85, v85, s34, v176\n"
        "v_fma_f32 v86, v86, s34, v176\n"
        "v_fma_f32 v87, v87, s34, v176\n"
        "v_mfma_f32_16x16x32_bf16 v[92:95], v[100:103], v[148:151], v[92:95]\n"
        "v_mfma_f32_16x16x32_bf16 v[92:95], v[104:107], v[156:159], v[92:95]\n"
        "v_mfma_f32_16x16x32_bf16 v[100:103], v[140:143], v[144:147], 0\n"
        "v_mfma_f32_16x16x32_bf16 v[100:103], v[132:135], v[148:151], v[100:103]\n"
        "v_mfma_f32_16x16x32_bf16 v[100:103], v[96:99], v[156:159], v[100:103]\n"
        "v_mfma_f32_16x16x32_bf16 v[104:107], v[136:139], v[144:147], 0\n"
        "v_mfma_f32_16x16x32_bf16 v[104:107], v[128:131], v[148:151], v[104:107]\n"
        "v_mfma_f32_16x16x32_bf16 v[104:107], v[108:111], v[156:159], v[104:107]\n"
        "v_mfma_f32_16x16x32_bf16 v[96:99], v[152:155], v[144:147], 0\n"
        "v_mfma_f32_16x16x32_bf16 v[96:99], v[160:163], v[148:151], v[96:99]\n"
        "v_add_f32 v128, v88, v92\n"
        "v_add_f32 v129, v89, v93\n"
        "v_add_f32 v130, v90, v94\n"
        "v_add_f32 v131, v91, v95\n"
        "v_add_f32 v128, v128, v100\n"
        "v_add_f32 v129, v129, v101\n"
        "v_add_f32 v130, v130, v102\n"
        "v_add_f32 v131, v131, v103\n"
        "v_add_f32 v128, v128, v104\n"
        "v_add_f32 v129, v129, v105\n"
        "v_add_f32 v130, v130, v106\n"
        "v_add_f32 v131, v131, v107\n"
        "v_mfma_f32_16x16x32_bf16 v[96:99], v[112:115], v[156:159], v[96:99]\n"
        "v_mfma_f32_16x16x32_bf16 v[108:111], v[116:119], v[144:147], 0\n"
        "v_mfma_f32_16x16x32_bf16 v[108:111], v[120:123], v[148:151], v[108:111]\n"
        "v_mfma_f32_16x16x32_bf16 v[108:111], v[124:127], v[156:159], v[108:111]\n"
        "v_mov_b32 v239, s37\n"
        "s_nop 3\n"
        "v_add_f32 v128, v128, v96\n"
        "v_add_f32 v129, v129, v97\n"
        "v_add_f32 v130, v130, v98\n"
        "v_add_f32 v131, v131, v99\n"
        "v_add_f32 v128, v128, v108\n"
        "v_add_f32 v129, v129, v109\n"
        "v_add_f32 v130, v130, v110\n"
        "v_add_f32 v131, v131, v111\n"
        "v_add_f32 v128, v128, v129\n"
        "v_add_f32 v130, v130, v131\n"
        "v_add_f32 v128, v128, v130\n"
        "v_mov_b32 v129, v128\n"
        "s_nop 1\n"
        "v_permlane32_swap_b32 v128, v129\n"
        "v_add_f32 v128, v128, v129\n"
        "v_mov_b32 v129, v128\n"
        "s_nop 1\n"
        "v_permlane16_swap_b32 v128, v129\n"
        "v_add_f32 v128, v128, v129\n"
        "v_mul_f32 v128, 0xbc2aaaab, v128\n"
        "v_mul_f32 v128, s34, v128\n"
        "v_fma_f32 v88, v88, s34, v128\n"
        "v_fma_f32 v89, v89, s34, v128\n"
        "v_fma_f32 v90, v90, s34, v128\n"
        "v_fma_f32 v91, v91, s34, v128\n"
        "v_fma_f32 v92, v92, s34, v128\n"
        "v_fma_f32 v93, v93, s34, v128\n"
        "v_fma_f32 v94, v94, s34, v128\n"
        "v_fma_f32 v95, v95, s34, v128\n"
        "v_fma_f32 v100, v100, s34, v128\n"
        "v_fma_f32 v101, v101, s34, v128\n"
        "v_fma_f32 v102, v102, s34, v128\n"
        "v_fma_f32 v103, v103, s34, v128\n"
        "v_fma_f32 v104, v104, s34, v128\n"
        "v_fma_f32 v105, v105, s34, v128\n"
        "v_fma_f32 v106, v106, s34, v128\n"
        "v_fma_f32 v107, v107, s34, v128\n"
        "v_fma_f32 v96, v96, s34, v128\n"
        "v_fma_f32 v97, v97, s34, v128\n"
        "v_fma_f32 v98, v98, s34, v128\n"
        "v_fma_f32 v99, v99, s34, v128\n"
        "v_fma_f32 v108, v108, s34, v128\n"
        "v_fma_f32 v109, v109, s34, v128\n"
        "v_fma_f32 v110, v110, s34, v128\n"
        "v_fma_f32 v111, v111, s34, v128\n"
        "v_mov_b32 v237, s36\n"
        "L62_%=:\n"
        "ds_read_b32 v238, v237 offset:0\n"
        "s_waitcnt lgkmcnt(0)\n"
        "v_readfirstlane_b32 s40, v238\n"
        "s_nop 3\n"
        "s_cmp_ge_i32 s40, 4\n"
        "s_cbranch_scc1 L63_%=\n"
        "s_nop 0\n"
        "s_sub_u32 s41, s41, 1\n"
        "s_cbranch_scc0 L62_%=\n"
        "s_endpgm\n"
        "L63_%=:\n"
        "ds_read_b128 v[132:135], v246\n"
        "ds_read_b128 v[128:131], v246 offset:16\n"
        "ds_read_b128 v[124:127], v246 offset:128\n"
        "ds_read_b128 v[120:123], v246 offset:144\n"
        "ds_read_b128 v[116:119], v246 offset:256\n"
        "ds_read_b128 v[112:115], v246 offset:272\n"
        "s_waitcnt lgkmcnt(5)\n"
        "v_add_f32 v64, v64, v132\n"
        "v_add_f32 v65, v65, v133\n"
        "v_add_f32 v66, v66, v134\n"
        "v_add_f32 v67, v67, v135\n"
        "s_waitcnt lgkmcnt(0)\n"
        "v_add_f32 v68, v68, v128\n"
        "v_add_f32 v69, v69, v129\n"
        "v_add_f32 v70, v70, v130\n"
        "v_add_f32 v71, v71, v131\n"
        "v_add_f32 v88, v88, v132\n"
        "v_add_f32 v89, v89, v133\n"
        "v_add_f32 v90, v90, v134\n"
        "v_add_f32 v91, v91, v135\n"
        "v_add_f32 v92, v92, v128\n"
        "v_add_f32 v93, v93, v129\n"
        "v_add_f32 v94, v94, v130\n"
        "v_add_f32 v95, v95, v131\n"
        "v_add_f32 v72, v72, v124\n"
        "v_add_f32 v73, v73, v125\n"
        "v_add_f32 v74, v74, v126\n"
        "v_add_f32 v75, v75, v127\n"
        "v_add_f32 v76, v76, v120\n"
        "v_add_f32 v77, v77, v121\n"
        "v_add_f32 v78, v78, v122\n"
        "v_add_f32 v79, v79, v123\n"
        "v_add_f32 v100, v100, v124\n"
        "v_add_f32 v101, v101, v125\n"
        "v_add_f32 v102, v102, v126\n"
        "v_add_f32 v103, v103, v127\n"
        "v_add_f32 v104, v104, v120\n"
        "v_add_f32 v105, v105, v121\n"
        "v_add_f32 v106, v106, v122\n"
        "v_add_f32 v107, v107, v123\n"
        "v_add_f32 v80, v80, v116\n"
        "v_add_f32 v81, v81, v117\n"
        "v_add_f32 v82, v82, v118\n"
        "v_add_f32 v83, v83, v119\n"
        "v_add_f32 v84, v84, v112\n"
        "v_add_f32 v85, v85, v113\n"
        "v_add_f32 v86, v86, v114\n"
        "v_add_f32 v87, v87, v115\n"
        "v_add_f32 v96, v96, v116\n"
        "v_add_f32 v97, v97, v117\n"
        "v_add_f32 v98, v98, v118\n"
        "v_add_f32 v99, v99, v119\n"
        "v_add_f32 v108, v108, v112\n"
        "v_add_f32 v109, v109, v113\n"
        "v_add_f32 v110, v110, v114\n"
        "v_add_f32 v111, v111, v115\n"
        "v_cvt_pk_bf16_f32 v112, v64, v65\n"
        "v_cvt_pk_bf16_f32 v113, v66, v67\n"
        "v_cvt_pk_bf16_f32 v114, v68, v69\n"
        "v_cvt_pk_bf16_f32 v115, v70, v71\n"
        "v_cvt_pk_bf16_f32 v64, v88, v89\n"
        "v_cvt_pk_bf16_f32 v65, v90, v91\n"
        "v_cvt_pk_bf16_f32 v66, v92, v93\n"
        "v_cvt_pk_bf16_f32 v67, v94, v95\n"
        "v_cvt_pk_bf16_f32 v68, v72, v73\n"
        "v_cvt_pk_bf16_f32 v69, v74, v75\n"
        "v_cvt_pk_bf16_f32 v70, v76, v77\n"
        "v_cvt_pk_bf16_f32 v71, v78, v79\n"
        "v_cvt_pk_bf16_f32 v72, v100, v101\n"
        "v_cvt_pk_bf16_f32 v73, v102, v103\n"
        "v_cvt_pk_bf16_f32 v74, v104, v105\n"
        "v_cvt_pk_bf16_f32 v75, v106, v107\n"
        "v_cvt_pk_bf16_f32 v76, v80, v81\n"
        "v_cvt_pk_bf16_f32 v77, v82, v83\n"
        "v_cvt_pk_bf16_f32 v78, v84, v85\n"
        "v_cvt_pk_bf16_f32 v79, v86, v87\n"
        "v_cvt_pk_bf16_f32 v80, v96, v97\n"
        "v_cvt_pk_bf16_f32 v81, v98, v99\n"
        "v_cvt_pk_bf16_f32 v82, v108, v109\n"
        "v_cvt_pk_bf16_f32 v83, v110, v111\n"
        "ds_write_b128 v244, v[112:115]\n"
        "ds_write_b128 v245, v[64:67]\n"
        "ds_write_b128 v244, v[68:71] offset:1024\n"
        "ds_write_b128 v245, v[72:75] offset:1024\n"
        "ds_write_b128 v244, v[76:79] offset:2048\n"
        "ds_write_b128 v245, v[80:83] offset:2048\n"
        "s_mov_b64 exec, 1\n"
        "ds_inc_u32 v239, v239 offset:0\n"
        "s_mov_b64 exec, -1\n"
        "s_setpc_b64 s[30:31]\n"
        "FOLDR_%=:\n"
        "ds_read_b64_tr_b16 v[84:85], v242 offset:0\n"
        "ds_read_b64_tr_b16 v[86:87], v243 offset:0\n"
        "ds_read_b64_tr_b16 v[92:93], v242 offset:6144\n"
        "ds_read_b64_tr_b16 v[94:95], v243 offset:6144\n"
        "ds_read_b64_tr_b16 v[80:81], v242 offset:12288\n"
        "ds_read_b64_tr_b16 v[82:83], v243 offset:12288\n"
        "ds_read_b64_tr_b16 v[76:77], v242 offset:8\n"
        "ds_read_b64_tr_b16 v[78:79], v243 offset:8\n"
        "ds_read_b64_tr_b16 v[68:69], v242 offset:6152\n"
        "ds_read_b64_tr_b16 v[70:71], v243 offset:6152\n"
        "ds_read_b64_tr_b16 v[72:73], v242 offset:12296\n"
        "ds_read_b64_tr_b16 v[74:75], v243 offset:12296\n"
        "v_mov_b32 v64, 0x3f803f80\n"
        "v_mov_b32 v65, 0x3f803f80\n"
        "v_mov_b32 v66, 0x3f803f80\n"
        "v_mov_b32 v67, 0x3f803f80\n"
        "ds_read_b64_tr_b16 v[88:89], v242 offset:64\n"
        "ds_read_b64_tr_b16 v[90:91], v243 offset:64\n"
        "s_waitcnt lgkmcnt(12)\n"
        "v_mfma_f32_16x16x32_bf16 v[104:107], v[84:87], v[64:67], 0\n"
        "s_waitcnt lgkmcnt(10)\n"
        "v_mfma_f32_16x16x32_bf16 v[104:107], v[92:95], v[64:67], v[104:107]\n"
        "s_waitcnt lgkmcnt(6)\n"
        "ds_read_b64_tr_b16 v[96:97], v242 offset:6208\n"
        "ds_read_b64_tr_b16 v[98:99], v243 offset:6208\n"
        "v_mfma_f32_16x16x32_bf16 v[104:107], v[80:83], v[64:67], v[104:107]\n"
        "ds_read_b64_tr_b16 v[92:93], v242 offset:12352\n"
        "ds_read_b64_tr_b16 v[94:95], v243 offset:12352\n"
        "v_mfma_f32_16x16x32_bf16 v[100:103], v[76:79], v[64:67], 0\n"
        "s_waitcnt lgkmcnt(6)\n"
        "ds_read_b64_tr_b16 v[80:81], v242 offset:72\n"
        "ds_read_b64_tr_b16 v[82:83], v243 offset:72\n"
        "v_mfma_f32_16x16x32_bf16 v[100:103], v[68:71], v[64:67], v[100:103]\n"
        "ds_read_b64_tr_b16 v[76:77], v242 offset:6216\n"
        "ds_read_b64_tr_b16 v[78:79], v243 offset:6216\n"
        "v_cvt_pk_bf16_f32 v68, v104, v105\n"
        "v_cvt_pk_bf16_f32 v69, v106, v107\n"
        "v_mfma_f32_16x16x32_bf16 v[100:103], v[72:75], v[64:67], v[100:103]\n"
        "s_waitcnt lgkmcnt(8)\n"
        "ds_read_b64_tr_b16 v[84:85], v242 offset:12360\n"
        "ds_read_b64_tr_b16 v[86:87], v243 offset:12360\n"
        "v_mfma_f32_16x16x32_bf16 v[104:107], v[88:91], v[64:67], 0\n"
        "ds_read_b64_tr_b16 v[72:73], v242 offset:128\n"
        "ds_read_b64_tr_b16 v[74:75], v243 offset:128\n"
        "s_waitcnt lgkmcnt(10)\n"
        "v_mfma_f32_16x16x32_bf16 v[104:107], v[96:99], v[64:67], v[104:107]\n"
        "ds_read_b64_tr_b16 v[88:89], v242 offset:6272\n"
        "ds_read_b64_tr_b16 v[90:91], v243 offset:6272\n"
        "v_cvt_pk_bf16_f32 v70, v100, v101\n"
        "v_cvt_pk_bf16_f32 v71, v102, v103\n"
        "s_waitcnt lgkmcnt(9)\n"
        "v_mfma_f32_16x16x32_bf16 v[104:107], v[92:95], v[64:67], v[104:107]\n"
        "s_waitcnt lgkmcnt(8)\n"
        "ds_read_b64_tr_b16 v[100:101], v242 offset:12416\n"
        "ds_read_b64_tr_b16 v[102:103], v243 offset:12416\n"
        "v_mfma_f32_16x16x32_bf16 v[108:111], v[80:83], v[64:67], 0\n"
        "s_waitcnt lgkmcnt(8)\n"
        "ds_read_b64_tr_b16 v[92:93], v242 offset:136\n"
        "ds_read_b64_tr_b16 v[94:95], v243 offset:136\n"
        "v_mfma_f32_16x16x32_bf16 v[108:111], v[76:79], v[64:67], v[108:111]\n"
        "ds_read_b64_tr_b16 v[96:97], v242 offset:6280\n"
        "ds_read_b64_tr_b16 v[98:99], v243 offset:6280\n"
        "v_cvt_pk_bf16_f32 v80, v104, v105\n"
        "v_cvt_pk_bf16_f32 v81, v106, v107\n"
        "s_waitcnt lgkmcnt(8)\n"
        "v_mfma_f32_16x16x32_bf16 v[108:111], v[84:87], v[64:67], v[108:111]\n"
        "ds_read_b64_tr_b16 v[76:77], v242 offset:12424\n"
        "ds_read_b64_tr_b16 v[78:79], v243 offset:12424\n"
        "v_mfma_f32_16x16x32_bf16 v[116:119], v[72:75], v[64:67], 0\n"
        "s_waitcnt lgkmcnt(8)\n"
        "ds_read_b128 v[72:75], v240\n"
        "v_mfma_f32_16x16x32_bf16 v[116:119], v[88:91], v[64:67], v[116:119]\n"
        "ds_read_b128 v[104:107], v241\n"
        "s_waitcnt lgkmcnt(8)\n"
        "v_mfma_f32_16x16x32_bf16 v[116:119], v[100:103], v[64:67], v[116:119]\n"
        "ds_read_b128 v[84:87], v240 offset:6144\n"
        "v_cvt_pk_bf16_f32 v82, v108, v109\n"
        "v_cvt_pk_bf16_f32 v83, v110, v111\n"
        "s_waitcnt lgkmcnt(7)\n"
        "v_mfma_f32_16x16x32_bf16 v[112:115], v[92:95], v[64:67], 0\n"
        "s_waitcnt lgkmcnt(5)\n"
        "ds_read_b128 v[92:95], v241 offset:6144\n"
        "v_mfma_f32_16x16x32_bf16 v[112:115], v[96:99], v[64:67], v[112:115]\n"
        "ds_read_b128 v[100:103], v240 offset:12288\n"
        "v_cvt_pk_bf16_f32 v88, v116, v117\n"
        "v_cvt_pk_bf16_f32 v89, v118, v119\n"
        "s_waitcnt lgkmcnt(5)\n"
        "v_mfma_f32_16x16x32_bf16 v[112:115], v[76:79], v[64:67], v[112:115]\n"
        "s_waitcnt lgkmcnt(4)\n"
        "ds_read_b128 v[116:119], v241 offset:12288\n"
        "v_mfma_f32_16x16x32_bf16 v[64:67], v[68:71], v[72:75], 0\n"
        "s_waitcnt lgkmcnt(4)\n"
        "ds_read_b128 v[120:123], v240 offset:64\n"
        "v_mfma_f32_16x16x32_bf16 v[72:75], v[68:71], v[104:107], 0\n"
        "ds_read_b128 v[108:111], v241 offset:64\n"
        "s_waitcnt lgkmcnt(5)\n"
        "v_mfma_f32_16x16x32_bf16 v[76:79], v[68:71], v[84:87], 0\n"
        "ds_read_b128 v[104:107], v240 offset:6208\n"
        "v_cvt_pk_bf16_f32 v90, v112, v113\n"
        "v_cvt_pk_bf16_f32 v91, v114, v115\n"
        "s_waitcnt lgkmcnt(5)\n"
        "v_mfma_f32_16x16x32_bf16 v[84:87], v[68:71], v[92:95], 0\n"
        "ds_read_b128 v[124:127], v241 offset:6208\n"
        "s_waitcnt lgkmcnt(5)\n"
        "v_mfma_f32_16x16x32_bf16 v[92:95], v[68:71], v[100:103], 0\n"
        "ds_read_b128 v[112:115], v240 offset:12352\n"
        "s_waitcnt lgkmcnt(5)\n"
        "v_mfma_f32_16x16x32_bf16 v[96:99], v[68:71], v[116:119], 0\n"
        "ds_read_b128 v[100:103], v241 offset:12352\n"
        "s_waitcnt lgkmcnt(5)\n"
        "v_mfma_f32_16x16x32_bf16 v[64:67], v[80:83], v[120:123], v[64:67]\n"
        "ds_read_b128 v[68:71], v240 offset:128\n"
        "s_waitcnt lgkmcnt(5)\n"
        "v_mfma_f32_16x16x32_bf16 v[72:75], v[80:83], v[108:111], v[72:75]\n"
        "ds_read_b128 v[116:119], v241 offset:128\n"
        "s_waitcnt lgkmcnt(5)\n"
        "v_mfma_f32_16x16x32_bf16 v[76:79], v[80:83], v[104:107], v[76:79]\n"
        "ds_read_b128 v[108:111], v240 offset:6272\n"
        "s_waitcnt lgkmcnt(5)\n"
        "v_mfma_f32_16x16x32_bf16 v[84:87], v[80:83], v[124:127], v[84:87]\n"
        "ds_read_b128 v[104:107], v241 offset:6272\n"
        "s_waitcnt lgkmcnt(5)\n"
        "v_mfma_f32_16x16x32_bf16 v[92:95], v[80:83], v[112:115], v[92:95]\n"
        "ds_read_b128 v[120:123], v240 offset:12416\n"
        "s_waitcnt lgkmcnt(5)\n"
        "v_mfma_f32_16x16x32_bf16 v[96:99], v[80:83], v[100:103], v[96:99]\n"
        "ds_read_b128 v[112:115], v241 offset:12416\n"
        "s_waitcnt lgkmcnt(5)\n"
        "v_mfma_f32_16x16x32_bf16 v[64:67], v[88:91], v[68:71], v[64:67]\n"
        "s_waitcnt lgkmcnt(4)\n"
        "v_mfma_f32_16x16x32_bf16 v[72:75], v[88:91], v[116:119], v[72:75]\n"
        "v_mov_b32 v239, s35\n"
        "s_waitcnt lgkmcnt(3)\n"
        "v_mfma_f32_16x16x32_bf16 v[76:79], v[88:91], v[108:111], v[76:79]\n"
        "s_mov_b64 exec, 1\n"
        "ds_inc_u32 v239, v239 offset:0\n"
        "s_mov_b64 exec, -1\n"
        "s_waitcnt lgkmcnt(3)\n"
        "v_mfma_f32_16x16x32_bf16 v[84:87], v[88:91], v[104:107], v[84:87]\n"
        "v_add_f32 v238, v64, v72\n"
        "s_waitcnt lgkmcnt(2)\n"
        "v_mfma_f32_16x16x32_bf16 v[92:95], v[88:91], v[120:123], v[92:95]\n"
        "v_add_f32 v238, v238, v76\n"
        "s_waitcnt lgkmcnt(1)\n"
        "v_mfma_f32_16x16x32_bf16 v[96:99], v[88:91], v[112:115], v[96:99]\n"
        "v_mov_b32 v239, s37\n"
        "s_nop 0\n"
        "v_add_f32 v238, v238, v84\n"
        "s_nop 1\n"
        "v_add_f32 v238, v238, v92\n"
        "s_nop 1\n"
        "v_add_f32 v238, v238, v96\n"
        "s_nop 1\n"
        "v_add_f32_dpp v238, v238, v238 row_ror:8 row_mask:0xf bank_mask:0xf\n"
        "s_nop 1\n"
        "v_add_f32_dpp v238, v238, v238 row_ror:4 row_mask:0xf bank_mask:0xf\n"
        "s_nop 1\n"
        "v_add_f32_dpp v238, v238, v238 row_ror:2 row_mask:0xf bank_mask:0xf\n"
        "s_nop 1\n"
        "v_add_f32_dpp v238, v238, v238 row_ror:1 row_mask:0xf bank_mask:0xf\n"
        "v_mul_f32 v238, 0xbc2aaaab, v238\n"
        "v_mul_f32 v238, s34, v238\n"
        "v_fma_f32 v64, v64, s34, v238\n"
        "v_fma_f32 v72, v72, s34, v238\n"
        "v_fma_f32 v76, v76, s34, v238\n"
        "v_fma_f32 v84, v84, s34, v238\n"
        "v_fma_f32 v92, v92, s34, v238\n"
        "v_fma_f32 v96, v96, s34, v238\n"
        "ds_write_b32 v244, v64\n"
        "ds_write_b32 v244, v72 offset:16\n"
        "ds_write_b32 v244, v76 offset:128\n"
        "ds_write_b32 v244, v84 offset:144\n"
        "ds_write_b32 v244, v92 offset:256\n"
        "ds_write_b32 v244, v96 offset:272\n"
        "s_mov_b64 exec, 1\n"
        "ds_inc_u32 v239, v239 offset:0\n"
        "s_mov_b64 exec, -1\n"
        "s_setpc_b64 s[30:31]\n"
        "FOLDW_%=:\n"
        "ds_read_b64_tr_b16 v[68:69], v242 offset:0\n"
        "ds_read_b64_tr_b16 v[70:71], v243 offset:0\n"
        "ds_read_b128 v[172:175], v240\n"
        "ds_read_b64_tr_b16 v[180:181], v242 offset:6144\n"
        "ds_read_b64_tr_b16 v[182:183], v243 offset:6144\n"
        "ds_read_b128 v[120:123], v240 offset:64\n"
        "ds_read_b64_tr_b16 v[184:185], v242 offset:12288\n"
        "ds_read_b64_tr_b16 v[186:187], v243 offset:12288\n"
        "ds_read_b128 v[168:171], v240 offset:128\n"
        "ds_read_b128 v[160:163], v241\n"
        "ds_read_b128 v[148:151], v241 offset:64\n"
        "ds_read_b128 v[132:135], v241 offset:128\n"
        "ds_read_b128 v[124:127], v240 offset:6144\n"
        "ds_read_b128 v[104:107], v240 offset:6208\n"
        "ds_read_b128 v[92:95], v240 offset:6272\n"
        "ds_read_b128 v[96:99], v241 offset:6144\n"
        "ds_read_b128 v[88:91], v241 offset:6208\n"
        "ds_read_b128 v[84:87], v241 offset:6272\n"
        "s_waitcnt lgkmcnt(15)\n"
        "v_mfma_f32_16x16x32_bf16 v[136:139], v[172:175], v[68:71], 0\n"
        "ds_read_b128 v[100:103], v240 offset:12288\n"
        "s_waitcnt lgkmcnt(12)\n"
        "v_mfma_f32_16x16x32_bf16 v[136:139], v[120:123], v[180:183], v[136:139]\n"
        "s_waitcnt lgkmcnt(10)\n"
        "ds_read_b128 v[108:111], v240 offset:12352\n"
        "v_mfma_f32_16x16x32_bf16 v[136:139], v[168:171], v[184:187], v[136:139]\n"
        "s_waitcnt lgkmcnt(7)\n"
        "ds_read_b128 v[64:67], v240 offset:12416\n"
        "v_mfma_f32_16x16x32_bf16 v[140:143], v[160:163], v[68:71], 0\n"
        "ds_read_b128 v[72:75], v241 offset:12288\n"
        "v_mfma_f32_16x16x32_bf16 v[140:143], v[148:151], v[180:183], v[140:143]\n"
        "ds_read_b128 v[76:79], v241 offset:12352\n"
        "v_mfma_f32_16x16x32_bf16 v[140:143], v[132:135], v[184:187], v[140:143]\n"
        "ds_read_b128 v[80:83], v241 offset:12416\n"
        "v_mfma_f32_16x16x32_bf16 v[144:147], v[124:127], v[68:71], 0\n"
        "s_waitcnt lgkmcnt(6)\n"
        "ds_read_b64_tr_b16 v[112:113], v242 offset:8\n"
        "ds_read_b64_tr_b16 v[114:115], v243 offset:8\n"
        "v_mfma_f32_16x16x32_bf16 v[144:147], v[104:107], v[180:183], v[144:147]\n"
        "ds_read_b64_tr_b16 v[116:117], v242 offset:6152\n"
        "ds_read_b64_tr_b16 v[118:119], v243 offset:6152\n"
        "v_add_f32 v164, v136, v140\n"
        "v_add_f32 v165, v137, v141\n"
        "v_add_f32 v166, v138, v142\n"
        "v_add_f32 v167, v139, v143\n"
        "v_mfma_f32_16x16x32_bf16 v[144:147], v[92:95], v[184:187], v[144:147]\n"
        "ds_read_b64_tr_b16 v[128:129], v242 offset:12296\n"
        "ds_read_b64_tr_b16 v[130:131], v243 offset:12296\n"
        "v_mfma_f32_16x16x32_bf16 v[152:155], v[96:99], v[68:71], 0\n"
        "v_mfma_f32_16x16x32_bf16 v[152:155], v[88:91], v[180:183], v[152:155]\n"
        "v_mfma_f32_16x16x32_bf16 v[152:155], v[84:87], v[184:187], v[152:155]\n"
        "s_waitcnt lgkmcnt(6)\n"
        "v_mfma_f32_16x16x32_bf16 v[156:159], v[100:103], v[68:71], 0\n"
        "v_mfma_f32_16x16x32_bf16 v[156:159], v[108:111], v[180:183], v[156:159]\n"
        "v_add_f32 v164, v164, v144\n"
        "v_add_f32 v165, v165, v145\n"
        "v_add_f32 v166, v166, v146\n"
        "v_add_f32 v167, v167, v147\n"
        "v_mfma_f32_16x16x32_bf16 v[156:159], v[64:67], v[184:187], v[156:159]\n"
        "v_add_f32 v164, v164, v152\n"
        "v_add_f32 v165, v165, v153\n"
        "v_add_f32 v166, v166, v154\n"
        "v_add_f32 v167, v167, v155\n"
        "v_mfma_f32_16x16x32_bf16 v[176:179], v[72:75], v[68:71], 0\n"
        "v_mfma_f32_16x16x32_bf16 v[176:179], v[76:79], v[180:183], v[176:179]\n"
        "v_mfma_f32_16x16x32_bf16 v[176:179], v[80:83], v[184:187], v[176:179]\n"
        "s_waitcnt lgkmcnt(0)\n"
        "v_mov_b32 v239, s35\n"
        "v_add_f32 v164, v164, v156\n"
        "v_add_f32 v165, v165, v157\n"
        "v_add_f32 v166, v166, v158\n"
        "v_add_f32 v167, v167, v159\n"
        "s_mov_b64 exec, 1\n"
        "ds_inc_u32 v239, v239 offset:0\n"
        "s_mov_b64 exec, -1\n"
        "v_add_f32 v164, v164, v176\n"
        "v_add_f32 v165, v165, v177\n"
        "v_add_f32 v166, v166, v178\n"
        "v_mfma_f32_16x16x32_bf16 v[68:71], v[172:175], v[112:115], 0\n"
        "v_add_f32 v167, v167, v179\n"
        "v_add_f32 v164, v164, v165\n"
        "v_add_f32 v166, v166, v167\n"
        "v_add_f32 v164, v164, v166\n"
        "v_mov_b32 v165, v164\n"
        "v_mfma_f32_16x16x32_bf16 v[68:71], v[120:123], v[116:119], v[68:71]\n"
        "v_mfma_f32_16x16x32_bf16 v[68:71], v[168:171], v[128:131], v[68:71]\n"
        "v_permlane32_swap_b32 v164, v165\n"
        "v_add_f32 v164, v164, v165\n"
        "v_mov_b32 v165, v164\n"
        "v_mfma_f32_16x16x32_bf16 v[120:123], v[160:163], v[112:115], 0\n"
        "global_load_dwordx4 v[40:43], v57, s[22:23] offset:-3072\n"
        "v_permlane16_swap_b32 v164, v165\n"
        "v_add_f32 v164, v164, v165\n"
        "v_mul_f32 v164, 0xbc2aaaab, v164\n"
        "v_mul_f32 v164, s34, v164\n"
        "v_fma_f32 v136, v136, s34, v164\n"
        "v_fma_f32 v137, v137, s34, v164\n"
        "v_fma_f32 v138, v138, s34, v164\n"
        "v_fma_f32 v139, v139, s34, v164\n"
        "v_fma_f32 v140, v140, s34, v164\n"
        "v_mfma_f32_16x16x32_bf16 v[120:123], v[148:151], v[116:119], v[120:123]\n"
        "v_fma_f32 v141, v141, s34, v164\n"
        "v_fma_f32 v142, v142, s34, v164\n"
        "v_fma_f32 v143, v143, s34, v164\n"
        "v_fma_f32 v144, v144, s34, v164\n"
        "v_fma_f32 v145, v145, s34, v164\n"
        "v_fma_f32 v146, v146, s34, v164\n"
        "v_fma_f32 v147, v147, s34, v164\n"
        "v_fma_f32 v152, v152, s34, v164\n"
        "v_fma_f32 v153, v153, s34, v164\n"
        "v_fma_f32 v154, v154, s34, v164\n"
        "v_fma_f32 v155, v155, s34, v164\n"
        "v_fma_f32 v156, v156, s34, v164\n"
        "v_fma_f32 v157, v157, s34, v164\n"
        "v_fma_f32 v158, v158, s34, v164\n"
        "v_mfma_f32_16x16x32_bf16 v[120:123], v[132:135], v[128:131], v[120:123]\n"
        "global_load_dwordx4 v[44:47], v57, s[22:23] offset:-2048\n"
        "v_fma_f32 v159, v159, s34, v164\n"
        "v_fma_f32 v176, v176, s34, v164\n"
        "v_fma_f32 v177, v177, s34, v164\n"
        "v_fma_f32 v178, v178, s34, v164\n"
        "v_fma_f32 v179, v179, s34, v164\n"
        "v_cvt_pk_bf16_f32 v132, v136, v137\n"
        "v_cvt_pk_bf16_f32 v133, v138, v139\n"
        "v_add_f32 v160, v68, v120\n"
        "v_add_f32 v161, v69, v121\n"
        "v_add_f32 v162, v70, v122\n"
        "v_add_f32 v163, v71, v123\n"
        "v_cvt_pk_bf16_f32 v134, v140, v141\n"
        "v_mfma_f32_16x16x32_bf16 v[148:151], v[124:127], v[112:115], 0\n"
        "v_cvt_pk_bf16_f32 v135, v142, v143\n"
        "v_cvt_pk_bf16_f32 v124, v144, v145\n"
        "v_cvt_pk_bf16_f32 v125, v146, v147\n"
        "v_cvt_pk_bf16_f32 v126, v152, v153\n"
        "v_cvt_pk_bf16_f32 v127, v154, v155\n"
        "v_cvt_pk_bf16_f32 v136, v156, v157\n"
        "v_cvt_pk_bf16_f32 v137, v158, v159\n"
        "v_cvt_pk_bf16_f32 v138, v176, v177\n"
        "v_cvt_pk_bf16_f32 v139, v178, v179\n"
        "v_mfma_f32_16x16x32_bf16 v[148:151], v[104:107], v[116:119], v[148:151]\n"
        "global_load_dwordx4 v[0:3], v57, s[22:23] offset:-1024\n"
        "v_mfma_f32_16x16x32_bf16 v[148:151], v[92:95], v[128:131], v[148:151]\n"
        "v_mfma_f32_16x16x32_bf16 v[104:107], v[96:99], v[112:115], 0\n"
        "global_load_dwordx4 v[4:7], v57, s[22:23] offset:0\n"
        "v_mfma_f32_16x16x32_bf16 v[104:107], v[88:91], v[116:119], v[104:107]\n"
        "v_mfma_f32_16x16x32_bf16 v[104:107], v[84:87], v[128:131], v[104:107]\n"
        "global_load_dwordx4 v[8:11], v57, s[22:23] offset:1024\n"
        "v_mfma_f32_16x16x32_bf16 v[84:87], v[100:103], v[112:115], 0\n"
        "v_mfma_f32_16x16x32_bf16 v[84:87], v[108:111], v[116:119], v[84:87]\n"
        "global_load_dwordx4 v[12:15], v57, s[22:23] offset:2048\n"
        "v_add_f32 v160, v160, v148\n"
        "v_add_f32 v161, v161, v149\n"
        "v_add_f32 v162, v162, v150\n"
        "v_add_f32 v163, v163, v151\n"
        "v_add_f32 v160, v160, v104\n"
        "v_add_f32 v161, v161, v105\n"
        "v_add_f32 v162, v162, v106\n"
        "v_add_f32 v163, v163, v107\n"
        "v_mfma_f32_16x16x32_bf16 v[84:87], v[64:67], v[128:131], v[84:87]\n"
        "v_mfma_f32_16x16x32_bf16 v[88:91], v[72:75], v[112:115], 0\n"
        "global_load_dwordx4 v[16:19], v57, s[22:23] offset:3072\n"
        "v_mfma_f32_16x16x32_bf16 v[88:91], v[76:79], v[116:119], v[88:91]\n"
        "v_mfma_f32_16x16x32_bf16 v[88:91], v[80:83], v[128:131], v[88:91]\n"
        "v_mov_b32 v239, s37\n"
        "s_nop 2\n"
        "v_add_f32 v160, v160, v84\n"
        "v_add_f32 v161, v161, v85\n"
        "v_add_f32 v162, v162, v86\n"
        "v_add_f32 v163, v163, v87\n"
        "v_add_f32 v160, v160, v88\n"
        "v_add_f32 v161, v161, v89\n"
        "v_add_f32 v162, v162, v90\n"
        "v_add_f32 v163, v163, v91\n"
        "v_add_f32 v160, v160, v161\n"
        "v_add_f32 v162, v162, v163\n"
        "v_add_f32 v160, v160, v162\n"
        "v_mov_b32 v161, v160\n"
        "s_nop 1\n"
        "v_permlane32_swap_b32 v160, v161\n"
        "v_add_f32 v160, v160, v161\n"
        "v_mov_b32 v161, v160\n"
        "s_nop 1\n"
        "v_permlane16_swap_b32 v160, v161\n"
        "v_add_f32 v160, v160, v161\n"
        "v_mul_f32 v160, 0xbc2aaaab, v160\n"
        "v_mul_f32 v160, s34, v160\n"
        "v_fma_f32 v68, v68, s34, v160\n"
        "v_fma_f32 v69, v69, s34, v160\n"
        "v_fma_f32 v70, v70, s34, v160\n"
        "v_fma_f32 v71, v71, s34, v160\n"
        "v_fma_f32 v120, v120, s34, v160\n"
        "v_fma_f32 v121, v121, s34, v160\n"
        "v_fma_f32 v122, v122, s34, v160\n"
        "v_fma_f32 v123, v123, s34, v160\n"
        "v_fma_f32 v148, v148, s34, v160\n"
        "v_fma_f32 v149, v149, s34, v160\n"
        "v_fma_f32 v150, v150, s34, v160\n"
        "v_fma_f32 v151, v151, s34, v160\n"
        "v_fma_f32 v104, v104, s34, v160\n"
        "v_fma_f32 v105, v105, s34, v160\n"
        "v_fma_f32 v106, v106, s34, v160\n"
        "v_fma_f32 v107, v107, s34, v160\n"
        "v_fma_f32 v84, v84, s34, v160\n"
        "v_fma_f32 v85, v85, s34, v160\n"
        "v_fma_f32 v86, v86, s34, v160\n"
        "v_fma_f32 v87, v87, s34, v160\n"
        "v_fma_f32 v88, v88, s34, v160\n"
        "v_fma_f32 v89, v89, s34, v160\n"
        "v_fma_f32 v90, v90, s34, v160\n"
        "v_fma_f32 v91, v91, s34, v160\n"
        "v_cvt_pk_bf16_f32 v64, v68, v69\n"
        "v_cvt_pk_bf16_f32 v65, v70, v71\n"
        "v_cvt_pk_bf16_f32 v66, v120, v121\n"
        "v_cvt_pk_bf16_f32 v67, v122, v123\n"
        "v_cvt_pk_bf16_f32 v68, v148, v149\n"
        "v_cvt_pk_bf16_f32 v69, v150, v151\n"
        "v_cvt_pk_bf16_f32 v70, v104, v105\n"
        "v_cvt_pk_bf16_f32 v71, v106, v107\n"
        "v_cvt_pk_bf16_f32 v72, v84, v85\n"
        "v_cvt_pk_bf16_f32 v73, v86, v87\n"
        "v_cvt_pk_bf16_f32 v74, v88, v89\n"
        "v_cvt_pk_bf16_f32 v75, v90, v91\n"
        "s_nop 1\n"
        "v_mov_b32 v238, s36\n"
        "L64_%=:\n"
        "ds_read_b32 v237, v238 offset:0\n"
        "s_waitcnt lgkmcnt(0)\n"
        "v_readfirstlane_b32 s40, v237\n"
        "s_nop 3\n"
        "s_cmp_ge_i32 s40, 4\n"
        "s_cbranch_scc1 L65_%=\n"
        "s_nop 0\n"
        "s_sub_u32 s41, s41, 1\n"
        "s_cbranch_scc0 L64_%=\n"
        "s_endpgm\n"
        "L65_%=:\n"
        "ds_write_b128 v244, v[132:135]\n"
        "ds_write_b128 v244, v[124:127] offset:1024\n"
        "ds_write_b128 v244, v[136:139] offset:2048\n"
        "ds_write_b128 v245, v[64:67]\n"
        "ds_write_b128 v245, v[68:71] offset:1024\n"
        "ds_write_b128 v245, v[72:75] offset:2048\n"
        "s_mov_b64 exec, 1\n"
        "ds_inc_u32 v239, v239 offset:0\n"
        "s_mov_b64 exec, -1\n"
        "s_setpc_b64 s[30:31]\n"
        "PPRO_%=:\n"
        "s_add_u32 s64, s64, 128\n"
        "s_cmp_lg_u32 s6, 0\n"
        "s_cbranch_scc1 L93_%=\n"
        "s_mov_b32 exec_hi, 0\n"
        "v_lshlrev_b32 v27, 2, v56\n"
        "v_add_u32 v27, 21600, v27\n"
        "v_mov_b32 v28, 0\n"
        "ds_write_b32 v27, v28\n"
        "v_add_u32 v27, 132608, v27\n"
        "ds_write2_b32 v27, v28, v28 offset1:32\n"
        "s_mov_b64 exec, 0x1ffff\n"
        "s_bfe_u32 s7, s2, 0x50003\n"
        "s_mul_i32 s7, s7, 2144\n"
        "v_lshlrev_b32 v27, 7, v56\n"
        "v_add_u32 v27, s7, v27\n"
        "global_load_dword v252, v27, s[4:5]\n"
        "s_mov_b64 exec, -1\n"
        "L93_%=:\n"
        "s_cmp_lt_u32 s6, 2\n"
        "s_cselect_b32 s65, 3, 1\n"
        "s_cselect_b32 s61, 384, 0\n"
        "s_cselect_b32 s62, 9, 1\n"
        "s_cselect_b32 s63, 3, 4\n"
        "s_cselect_b32 s26, 3, 1\n"
        "s_cselect_b32 s34, 9, 1\n"
        "s_mul_i32 s65, s65, 192\n"
        "s_mul_i32 s62, s62, 3072\n"
        "s_mul_i32 s63, s63, 1536\n"
        "s_mul_i32 s26, s26, 18432\n"
        "s_mul_i32 s34, s34, 1536\n"
        "s_add_u32 s7, s62, s63\n"
        "s_lshl_b32 s29, s63, 1\n"
        "s_add_u32 s60, s7, s63\n"
        "s_bitcmp1_b32 s6, 0\n"
        "s_cselect_b32 s28, s7, 0\n"
        "s_cselect_b32 s29, s29, s62\n"
        "s_cselect_b32 s60, s60, s63\n"
        "s_add_u32 s28, s28, s61\n"
        "s_add_u32 s29, s29, s61\n"
        "s_add_u32 s60, s60, s61\n"
        "v_add_u32 v24, 0, v56\n"
        "v_mul_u32_u24 v21, 171, v24\n"
        "v_lshrrev_b32 v21, 11, v21\n"
        "v_mad_i32_i24 v24, v21, -12, v24\n"
        "v_bfe_u32 v58, v21, 2, 2\n"
        "v_xor_b32 v58, v24, v58\n"
        "v_and_b32 v27, 7, v21\n"
        "v_mul_u32_u24 v27, s65, v27\n"
        "v_lshl_add_u32 v58, v58, 4, v27\n"
        "v_add_u32 v25, 64, v56\n"
        "v_mul_u32_u24 v22, 171, v25\n"
        "v_lshrrev_b32 v22, 11, v22\n"
        "v_mad_i32_i24 v25, v22, -12, v25\n"
        "v_bfe_u32 v59, v22, 2, 2\n"
        "v_xor_b32 v59, v25, v59\n"
        "v_and_b32 v27, 7, v22\n"
        "v_mul_u32_u24 v27, s65, v27\n"
        "v_lshl_add_u32 v59, v59, 4, v27\n"
        "v_lshrrev_b32 v27, 3, v22\n"
        "v_mad_u32_u24 v59, v27, s34, v59\n"
        "v_add_u32 v26, 128, v56\n"
        "v_mul_u32_u24 v23, 171, v26\n"
        "v_lshrrev_b32 v23, 11, v23\n"
        "v_mad_i32_i24 v26, v23, -12, v26\n"
        "v_bfe_u32 v60, v23, 2, 2\n"
        "v_xor_b32 v60, v26, v60\n"
        "v_and_b32 v27, 7, v23\n"
        "v_mul_u32_u24 v27, s65, v27\n"
        "v_lshl_add_u32 v60, v60, 4, v27\n"
        "v_add_u32 v60, s34, v60\n"
        "s_and_b32 s7, s2, 7\n"
        "s_lshl_b32 s7, s7, 5\n"
        "s_lshr_b32 s14, s2, 3\n"
        "s_add_u32 s7, s7, s14\n"
        "s_sub_u32 s7, 255, s7\n"
        "s_and_b32 s16, s7, 255\n"
        "s_sub_u32 s7, s7, s16\n"
        "s_mul_i32 s7, s7, 20\n"
        "s_add_u32 s7, s7, s16\n"
        "s_mov_b32 s14, 22\n"
        "s_cmp_eq_u32 s2, 0\n"
        "s_cselect_b32 s43, 19, 20\n"
        "s_mul_i32 s15, s7, 57457\n"
        "s_lshr_b32 s15, s15, 22\n"
        "s_mov_b32 s16, s7\n"
        "s_mul_i32 s15, s15, 588672\n"
        "s_mul_i32 s7, s16, 1344\n"
        "s_add_u32 s15, s15, s7\n"
        "s_mul_i32 s74, s6, 98112\n"
        "s_add_u32 s74, s74, s15\n"
        "s_mov_b32 s36, 96768\n"
        "s_mov_b32 s40, 599187\n"
        "s_cmp_eq_u32 s6, 3\n"
        "s_cselect_b32 s7, s16, s6\n"
        "s_cselect_b32 s36, 588672, s36\n"
        "s_cselect_b32 s74, 589824, s74\n"
        "s_cselect_b32 s37, 512, 1\n"
        "s_cselect_b32 s38, 256, 4\n"
        "s_cselect_b32 s39, 7, 1\n"
        "s_cselect_b32 s40, 57457, s40\n"
        "s_mul_i32 s39, s39, 192\n"
        "s_mov_b32 s35, 50135232\n"
        "v_mov_b32 v29, 2048\n"
        "v_bfe_u32 v27, v21, 2, 1\n"
        "v_and_b32 v21, 3, v21\n"
        "v_mul_u32_u24 v21, s37, v21\n"
        "v_mad_u32_u24 v21, v27, s38, v21\n"
        "v_add_u32 v21, s7, v21\n"
        "v_and_b32 v27, 3, v24\n"
        "v_mad_u32_u24 v29, v27, 48, v29\n"
        "v_lshrrev_b32 v27, 2, v24\n"
        "v_lshl_add_u32 v29, v27, 4, v29\n"
        "v_mul_u32_u24 v27, s40, v21\n"
        "v_mul_u32_u24 v21, s39, v21\n"
        "v_lshrrev_b32 v27, 22, v27\n"
        "v_mul_u32_u24 v27, s36, v27\n"
        "v_add3_u32 v29, v29, v21, v27\n"
        "v_lshrrev_b32 v27, 3, v22\n"
        "v_xor_b32 v25, v25, v27\n"
        "v_mul_lo_u32 v30, v27, s35\n"
        "v_add_u32 v30, 1024, v30\n"
        "v_bfe_u32 v27, v22, 2, 1\n"
        "v_and_b32 v22, 3, v22\n"
        "v_mul_u32_u24 v22, s37, v22\n"
        "v_mad_u32_u24 v22, v27, s38, v22\n"
        "v_add_u32 v22, s7, v22\n"
        "v_and_b32 v27, 3, v25\n"
        "v_mad_u32_u24 v30, v27, 48, v30\n"
        "v_lshrrev_b32 v27, 2, v25\n"
        "v_lshl_add_u32 v30, v27, 4, v30\n"
        "v_mul_u32_u24 v27, s40, v22\n"
        "v_mul_u32_u24 v22, s39, v22\n"
        "v_lshrrev_b32 v27, 22, v27\n"
        "v_mul_u32_u24 v27, s36, v27\n"
        "v_add3_u32 v30, v30, v22, v27\n"
        "v_mov_b32 v31, 50135232\n"
        "v_xor_b32 v26, 1, v26\n"
        "v_bfe_u32 v27, v23, 2, 1\n"
        "v_and_b32 v23, 3, v23\n"
        "v_mul_u32_u24 v23, s37, v23\n"
        "v_mad_u32_u24 v23, v27, s38, v23\n"
        "v_add_u32 v23, s7, v23\n"
        "v_and_b32 v27, 3, v26\n"
        "v_mad_u32_u24 v31, v27, 48, v31\n"
        "v_lshrrev_b32 v27, 2, v26\n"
        "v_lshl_add_u32 v31, v27, 4, v31\n"
        "v_mul_u32_u24 v27, s40, v23\n"
        "v_mul_u32_u24 v23, s39, v23\n"
        "v_lshrrev_b32 v27, 22, v27\n"
        "v_mul_u32_u24 v27, s36, v27\n"
        "v_add3_u32 v31, v31, v23, v27\n"
        "s_mul_i32 s17, s6, 6208\n"
        "s_add_u32 s17, s17, 110176\n"
        "s_mov_b32 s18, s6\n"
        "s_add_u32 s75, s74, 100270464\n"
        "v_mov_b32 v250, 21600\n"
        "s_mov_b32 s41, 0x400000\n"
        "s_waitcnt lgkmcnt(0)\n"
        "s_cmp_lg_u32 s6, 0\n"
        "s_cbranch_scc1 L94_%=\n"
        "s_bitcmp1_b32 s2, 7\n"
        "s_cselect_b64 s[8:9], s[46:47], s[50:51]\n"
        "s_cselect_b64 s[10:11], s[48:49], s[52:53]\n"
        "s_bfe_u32 s7, s2, 0x40003\n"
        "s_cmp_ge_u32 s7, 12\n"
        "s_cselect_b64 s[8:9], s[10:11], s[8:9]\n"
        "s_cselect_b32 s10, 12, 0\n"
        "s_sub_u32 s7, s7, s10\n"
        "s_mul_i32 s7, s7, 4608\n"
        "v_lshlrev_b32 v27, 7, v56\n"
        "v_add_u32 v27, s7, v27\n"
        "s_mov_b32 exec_hi, 0xf\n"
        "global_load_dword v252, v27, s[8:9]\n"
        "s_mov_b64 s[24:25], s[56:57]\n"
        "s_mov_b64 exec, 1\n"
        "v_bfe_u32 v27, s2, 3, 5\n"
        "v_lshlrev_b32 v27, 7, v27\n"
        "global_load_dword v253, v27, s[24:25] offset:256 sc1\n"
        "s_mov_b64 exec, -1\n"
        "L94_%=:\n"
        "s_cmp_lt_u32 s6, 2\n"
        "s_cselect_b64 s[24:25], s[46:47], s[48:49]\n"
        "s_and_b32 s25, s25, 0xffff\n"
        "s_sub_u32 s8, s44, 2048\n"
        "s_subb_u32 s9, s45, 0\n"
        "s_and_b32 s9, s9, 0xffff\n"
        "s_mov_b32 s10, 200542976\n"
        "s_mov_b32 s11, 0x20000\n"
        "s_mul_i32 s7, s6, 7168\n"
        "s_add_u32 s7, s7, 6144\n"
        "s_add_u32 s22, s4, s7\n"
        "s_addc_u32 s23, s5, 0\n"
        "s_sub_u32 s7, s6, 0\n"
        "s_and_b32 s7, s7, 3\n"
        "s_min_u32 s7, s7, 2\n"
        "s_lshl_b32 s7, s7, 10\n"
        "s_add_u32 s73, s7, 18528\n"
        "v_add_u32 v27, s7, v57\n"
        "s_barrier\n"
        "s_mov_b32 m0, s73\n"
        "s_nop 0\n"
        "global_load_lds_dwordx4 v27, s[4:5]\n"
        "global_load_dwordx4 v[240:243], v57, s[22:23] offset:-3072\n"
        "global_load_dwordx4 v[244:247], v57, s[22:23] offset:-2048\n"
        "s_add_u32 m0, s64, 0\n"
        "s_nop 0\n"
        "buffer_load_dwordx4 v58, s[24:27], s28 offen lds\n"
        "s_add_u32 m0, s64, 1024\n"
        "s_nop 0\n"
        "buffer_load_dwordx4 v59, s[24:27], s28 offen lds\n"
        "s_add_u32 m0, s64, 2048\n"
        "s_nop 0\n"
        "buffer_load_dwordx4 v60, s[24:27], s28 offen lds\n"
        "s_add_u32 m0, s64, 3072\n"
        "s_nop 0\n"
        "buffer_load_dwordx4 v58, s[24:27], s29 offen lds\n"
        "s_add_u32 m0, s64, 4096\n"
        "s_nop 0\n"
        "buffer_load_dwordx4 v59, s[24:27], s29 offen lds\n"
        "s_add_u32 m0, s64, 5120\n"
        "s_nop 0\n"
        "buffer_load_dwordx4 v60, s[24:27], s29 offen lds\n"
        "s_add_u32 m0, s64, 6144\n"
        "s_nop 0\n"
        "buffer_load_dwordx4 v58, s[24:27], s60 offen lds\n"
        "s_add_u32 m0, s64, 7168\n"
        "s_nop 0\n"
        "buffer_load_dwordx4 v59, s[24:27], s60 offen lds\n"
        "s_add_u32 m0, s64, 8192\n"
        "s_nop 0\n"
        "buffer_load_dwordx4 v60, s[24:27], s60 offen lds\n"
        "s_setprio 3\n"
        "s_waitcnt vmcnt(0)\n"
        "s_mov_b64 exec, 1\n"
        "ds_inc_u32 v250, v250 offset:20\n"
        "s_mov_b64 exec, -1\n"
        "L95_%=:\n"
        "ds_read_b32 v251, v250 offset:20\n"
        "s_waitcnt lgkmcnt(0)\n"
        "v_readfirstlane_b32 s40, v251\n"
        "s_nop 3\n"
        "s_cmp_ge_i32 s40, 4\n"
        "s_cbranch_scc1 L96_%=\n"
        "s_nop 0\n"
        "s_sub_u32 s41, s41, 1\n"
        "s_cbranch_scc0 L95_%=\n"
        "s_endpgm\n"
        "L96_%=:\n"
        "s_add_u32 m0, s17, 0\n"
        "s_nop 0\n"
        "buffer_load_dwordx4 v29, s[8:11], s74 offen lds\n"
        "buffer_load_dwordx4 v30, s[8:11], s74 offen offset:1024 lds\n"
        "buffer_load_dwordx4 v31, s[8:11], s74 offen offset:2048 lds\n"
        "s_add_u32 m0, s17, 3104\n"
        "s_nop 0\n"
        "buffer_load_dwordx4 v29, s[8:11], s75 offen lds\n"
        "buffer_load_dwordx4 v30, s[8:11], s75 offen offset:1024 lds\n"
        "buffer_load_dwordx4 v31, s[8:11], s75 offen offset:2048 lds\n"
        "s_cmp_eq_u32 s6, 0\n"
        "s_cbranch_scc1 L97_%=\n"
        "s_mov_b32 s34, 0x411cc471\n"
        "s_mov_b32 s35, 21632\n"
        "s_mov_b32 s36, 21624\n"
        "s_mov_b32 s37, 21628\n"
        "s_call_b64 s[30:31], FOLDW_%=\n"
        "s_branch L98_%=\n"
        "L97_%=:\n"
        "s_mov_b64 exec, 1\n"
        "ds_inc_u32 v250, v250 offset:32\n"
        "s_mov_b64 exec, -1\n"
        "global_load_dwordx4 v[40:43], v57, s[22:23] offset:-3072\n"
        "global_load_dwordx4 v[44:47], v57, s[22:23] offset:-2048\n"
        "global_load_dwordx4 v[0:3], v57, s[22:23] offset:-1024\n"
        "global_load_dwordx4 v[4:7], v57, s[22:23] offset:0\n"
        "global_load_dwordx4 v[8:11], v57, s[22:23] offset:1024\n"
        "global_load_dwordx4 v[12:15], v57, s[22:23] offset:2048\n"
        "global_load_dwordx4 v[16:19], v57, s[22:23] offset:3072\n"
        "L98_%=:\n"
        "s_mov_b64 s[24:25], s[56:57]\n"
        "s_setprio 3\n"
        "s_mov_b32 s44, 0xf0000f\n"
        "s_mov_b32 s45, 0xf0000f00\n"
        "s_mov_b32 s46, 0xff000ff\n"
        "s_mov_b32 s47, 0xf00fff00\n"
        "s_mov_b32 s48, 0x22222222\n"
        "s_mov_b32 s49, 0x22222222\n"
        "s_mov_b32 s50, 0x44444444\n"
        "s_mov_b32 s51, 0x44444444\n"
        "s_mov_b32 s52, 0x88888888\n"
        "s_mov_b32 s53, 0x88888888\n"
        "s_mov_b64 vcc, s[44:45]\n"
        "s_mov_b32 s28, 0x3c2aaaab\n"
        "s_mov_b32 s29, 0x3a7ba882\n"
        "s_mov_b32 s30, 0\n"
        "s_mov_b32 s34, 0\n"
        "s_cmp_eq_u32 s18, 3\n"
        "s_cselect_b32 s31, 6, s18\n"
        "s_mov_b32 s36, s18\n"
        "s_mov_b32 s37, s18\n"
        "s_add_u32 s71, s17, 3104\n"
        "s_mov_b32 s68, 4\n"
        "s_mov_b32 s63, 0x07060302\n"
        "s_mov_b32 s74, 16\n"
        "s_mov_b32 s72, -1\n"
        "s_mov_b32 s41, 0x400000\n"
        "L23_%=:\n"
        "ds_read_b32 v251, v250 offset:28\n"
        "s_waitcnt lgkmcnt(0)\n"
        "v_readfirstlane_b32 s40, v251\n"
        "s_nop 3\n"
        "s_cmp_ge_i32 s40, 6\n"
        "s_cbranch_scc1 L24_%=\n"
        "s_nop 0\n"
        "s_sub_u32 s41, s41, 1\n"
        "s_cbranch_scc0 L23_%=\n"
        "s_endpgm\n"
        "L24_%=:\n"
        "s_waitcnt vmcnt(0)\n"
        "v_readfirstlane_b32 s26, v253\n"
        "ds_read_b128 v[16:19], v16\n"
        "ds_read_b64 v[94:95], v14\n"
        "ds_read_b64 v[86:87], v14\n"
        "ds_read_b64 v[28:29], v14\n"
        "ds_read_b64 v[90:91], v14\n"
        "v_mov_b32 v102, 21600\n"
        "v_add_u32 v20, 21728, v13\n"
        "ds_read_b128 v[112:115], v20 offset:0\n"
        "ds_read_b128 v[116:119], v20 offset:1024\n"
        "ds_read_b128 v[120:123], v20 offset:2048\n"
        "ds_read_b128 v[124:127], v20 offset:3072\n"
        "ds_read_b128 v[128:131], v20 offset:4096\n"
        "ds_read_b128 v[132:135], v20 offset:5120\n"
        "ds_read_b128 v[136:139], v20 offset:6144\n"
        "ds_read_b128 v[140:143], v20 offset:7168\n"
        "ds_read_b128 v[144:147], v20 offset:8192\n"
        "ds_read_b128 v[148:151], v20 offset:9216\n"
        "ds_read_b128 v[152:155], v20 offset:10240\n"
        "ds_read_b128 v[156:159], v20 offset:11264\n"
        "ds_read_b128 v[160:163], v20 offset:12288\n"
        "ds_read_b128 v[164:167], v20 offset:13312\n"
        "ds_read_b128 v[168:171], v20 offset:14336\n"
        "ds_read_b128 v[172:175], v20 offset:15360\n"
        "ds_read_b128 v[176:179], v20 offset:16384\n"
        "ds_read_b128 v[180:183], v20 offset:17408\n"
        "ds_read_b128 v[184:187], v20 offset:18432\n"
        "ds_read_b128 v[188:191], v20 offset:19456\n"
        "ds_read_b128 v[192:195], v20 offset:20480\n"
        "ds_read_b128 v[196:199], v20 offset:21520\n"
        "ds_read_b128 v[200:203], v20 offset:22544\n"
        "ds_read_b128 v[204:207], v20 offset:23568\n"
        "ds_read_b128 v[208:211], v20 offset:24608\n"
        "ds_read_b128 v[212:215], v20 offset:25632\n"
        "ds_read_b128 v[216:219], v20 offset:26656\n"
        "ds_read_b128 v[220:223], v20 offset:27696\n"
        "ds_read_b128 v[224:227], v20 offset:28720\n"
        "ds_read_b128 v[228:231], v20 offset:29744\n"
        "ds_read_b128 v[232:235], v20 offset:30784\n"
        "ds_read_b128 v[236:239], v20 offset:31808\n"
        "ds_read_b128 v[240:243], v20 offset:32832\n"
        "ds_read_b128 v[244:247], v20 offset:33872\n"
        "ds_read_b128 v[248:251], v20 offset:34896\n"
        "ds_read_b128 v[252:255], v20 offset:35920\n"
        "ds_read_b128 v[60:63], v0 offset:0\n"
        "ds_read_b128 v[66:69], v0 offset:64\n"
        "ds_read_b128 v[72:75], v0 offset:128\n"
        "ds_read_b128 v[42:45], v0 offset:768\n"
        "ds_read_b128 v[48:51], v0 offset:832\n"
        "ds_read_b128 v[54:57], v0 offset:896\n"
        "s_add_u32 s37, s37, 4\n"
        "s_cmp_ge_u32 s37, 49\n"
        "s_cselect_b32 s69, 49, 0\n"
        "s_sub_u32 s37, s37, s69\n"
        "s_sub_u32 s69, s37, 4\n"
        "s_cmp_lt_u32 s69, 4\n"
        "s_subb_u32 s36, s36, -4\n"
        "s_cmp_ge_u32 s36, 6\n"
        "s_cselect_b32 s69, 6, 0\n"
        "s_addc_u32 s34, s34, 0\n"
        "s_sub_u32 s36, s36, s69\n"
        "s_cmp_ge_u32 s34, s74\n"
        "s_cbranch_scc0 L27_%=\n"
        "s_nop 1\n"
        "v_mov_b32 v22, 21600\n"
        "L25_%=:\n"
        "ds_read_b32 v23, v22 offset:64\n"
        "s_waitcnt lgkmcnt(0)\n"
        "v_readfirstlane_b32 s40, v23\n"
        "s_nop 3\n"
        "s_cmp_ge_i32 s40, 1\n"
        "s_cbranch_scc1 L26_%=\n"
        "s_sleep 1\n"
        "s_sub_u32 s41, s41, 1\n"
        "s_cbranch_scc0 L25_%=\n"
        "s_endpgm\n"
        "L26_%=:\n"
        "v_mbcnt_lo_u32_b32 v23, -1, 0\n"
        "v_lshl_add_u32 v22, v23, 2, v22\n"
        "ds_read_b32 v23, v22 offset:68\n"
        "s_mov_b32 s14, s40\n"
        "s_mov_b32 s74, -1\n"
        "s_waitcnt lgkmcnt(0)\n"
        "v_readlane_b32 s22, v23, 0\n"
        "v_readlane_b32 s23, v23, 1\n"
        "s_nop 3\n"
        "L27_%=:\n"
        "s_mul_i32 s38, s34, 256\n"
        "s_add_u32 s38, s38, s16\n"
        "s_cmp_ge_u32 s34, s43\n"
        "s_cselect_b32 s38, s22, s38\n"
        "s_cmp_gt_u32 s34, s43\n"
        "s_cselect_b32 s38, s23, s38\n"
        "s_mul_hi_u32 s70, s38, 58835169\n"
        "s_mul_i32 s38, s38, 1344\n"
        "s_mul_i32 s70, s70, 588672\n"
        "s_add_u32 s38, s38, s70\n"
        "s_mul_i32 s69, s36, 98304\n"
        "s_add_u32 s38, s38, s69\n"
        "s_mov_b32 s35, s36\n"
        "s_mul_i32 s69, s36, -12\n"
        "s_add_u32 s69, s69, 60\n"
        "s_lshl_b64 s[54:55], -1, s69\n"
        "s_lshl_b64 s[56:57], s[54:55], 24\n"
        "s_ashr_i64 s[60:61], s[54:55], 8\n"
        "s_mov_b32 s58, s61\n"
        "s_mov_b32 s59, s56\n"
        "v_cndmask_b32_e64 v15, v4, v7, s[56:57]\n"
        "v_cndmask_b32_e64 v100, v5, v8, s[58:59]\n"
        "v_cndmask_b32_e64 v103, v6, v9, s[60:61]\n"
        "s_cmp_eq_u32 s37, 3\n"
        "s_cbranch_scc0 L28_%=\n"
        "s_nop 1\n"
        "ds_read_b32 v15, v13 offset:19552\n"
        "ds_read_b32 v100, v13 offset:19556\n"
        "ds_read_b32 v103, v13 offset:19560\n"
        "s_mov_b32 s35, 6\n"
        "s_mov_b32 s38, 0\n"
        "s_mov_b32 s69, 1344\n"
        "s_waitcnt lgkmcnt(0)\n"
        "v_lshrrev_b32 v22, 28, v15\n"
        "v_add_u32 v22, s34, v22\n"
        "v_and_b32 v15, 0x0fffffff, v15\n"
        "v_mul_u32_u24 v23, 256, v22\n"
        "v_add_u32 v23, s16, v23\n"
        "v_cmpx_le_u32 vcc, s43, v22\n"
        "v_mov_b32 v23, s22\n"
        "s_mov_b64 exec, -1\n"
        "v_cmpx_lt_u32 vcc, s43, v22\n"
        "v_mov_b32 v23, s23\n"
        "s_mov_b64 exec, -1\n"
        "v_mad_u32_u24 v15, v23, s69, v15\n"
        "v_mul_u32_u24 v23, 57457, v23\n"
        "v_lshrrev_b32 v23, 22, v23\n"
        "v_mul_u32_u24 v23, 588672, v23\n"
        "v_add_u32 v15, v15, v23\n"
        "v_lshrrev_b32 v22, 28, v100\n"
        "v_add_u32 v22, s34, v22\n"
        "v_and_b32 v100, 0x0fffffff, v100\n"
        "v_mul_u32_u24 v23, 256, v22\n"
        "v_add_u32 v23, s16, v23\n"
        "v_cmpx_le_u32 vcc, s43, v22\n"
        "v_mov_b32 v23, s22\n"
        "s_mov_b64 exec, -1\n"
        "v_cmpx_lt_u32 vcc, s43, v22\n"
        "v_mov_b32 v23, s23\n"
        "s_mov_b64 exec, -1\n"
        "v_mad_u32_u24 v100, v23, s69, v100\n"
        "v_mul_u32_u24 v23, 57457, v23\n"
        "v_lshrrev_b32 v23, 22, v23\n"
        "v_mul_u32_u24 v23, 588672, v23\n"
        "v_add_u32 v100, v100, v23\n"
        "v_lshrrev_b32 v22, 28, v103\n"
        "v_add_u32 v22, s34, v22\n"
        "v_and_b32 v103, 0x0fffffff, v103\n"
        "v_mul_u32_u24 v23, 256, v22\n"
        "v_add_u32 v23, s16, v23\n"
        "v_cmpx_le_u32 vcc, s43, v22\n"
        "v_mov_b32 v23, s22\n"
        "s_mov_b64 exec, -1\n"
        "v_cmpx_lt_u32 vcc, s43, v22\n"
        "v_mov_b32 v23, s23\n"
        "s_mov_b64 exec, -1\n"
        "v_mad_u32_u24 v103, v23, s69, v103\n"
        "v_mul_u32_u24 v23, 57457, v23\n"
        "v_lshrrev_b32 v23, 22, v23\n"
        "v_mul_u32_u24 v23, 588672, v23\n"
        "v_add_u32 v103, v103, v23\n"
        "s_nop 4\n"
        "s_mov_b64 vcc, s[44:45]\n"
        "L28_%=:\n"
        "s_cmp_lt_u32 s34, s14\n"
        "s_cselect_b32 s38, s38, 0x80000000\n"
        "s_add_u32 s39, s38, 100270464\n"
        "s_waitcnt lgkmcnt(0)\n"
        "s_mov_b64 exec, 1\n"
        "ds_inc_u32 v102, v0 offset:8\n"
        "s_mov_b64 exec, -1\n"
        "s_cmp_eq_u32 s18, 0\n"
        "s_cselect_b32 s73, -20, 8\n"
        "s_setprio 1\n"
        "PFST_%=:\n"
        "v_mfma_f32_16x16x32_bf16 v[34:37], v[112:115], v[60:63], 0\n"
        "s_mov_b32 m0, s17\n"
        "v_mfma_f32_16x16x32_bf16 v[34:37], v[116:119], v[66:69], v[34:37]\n"
        "v_mfma_f32_16x16x32_bf16 v[34:37], v[120:123], v[72:75], v[34:37]\n"
        "buffer_load_dwordx4 v15, s[8:11], s38 offen lds\n"
        "v_mfma_f32_16x16x32_bf16 v[38:41], v[132:135], v[72:75], 0\n"
        "v_mfma_f32_16x16x32_bf16 v[38:41], v[128:131], v[66:69], v[38:41]\n"
        "s_setprio 3\n"
        "v_mfma_f32_16x16x32_bf16 v[38:41], v[124:127], v[60:63], v[38:41]\n"
        "buffer_load_dwordx4 v100, s[8:11], s38 offen offset:1024 lds\n"
        "v_mfma_f32_16x16x32_bf16 v[78:81], v[136:139], v[60:63], 0\n"
        "v_mfma_f32_16x16x32_bf16 v[78:81], v[140:143], v[66:69], v[78:81]\n"
        "v_mfma_f32_16x16x32_bf16 v[78:81], v[144:147], v[72:75], v[78:81]\n"
        "v_mfma_f32_16x16x32_bf16 v[82:85], v[156:159], v[72:75], 0\n"
        "v_mfma_f32_16x16x32_bf16 v[82:85], v[152:155], v[66:69], v[82:85]\n"
        "v_mfma_f32_16x16x32_bf16 v[82:85], v[148:151], v[60:63], v[82:85]\n"
        "buffer_load_dwordx4 v103, s[8:11], s38 offen offset:2048 lds\n"
        "s_mov_b32 m0, s71\n"
        "s_cmp_eq_u32 s72, s30\n"
        "v_mfma_f32_16x16x32_bf16 v[104:107], v[160:163], v[60:63], 0\n"
        "s_setprio 3\n"
        "s_cbranch_scc1 F5_%=\n"
        "s_setprio 1\n"
        "F5_%=:\n"
        "v_mfma_f32_16x16x32_bf16 v[104:107], v[164:167], v[66:69], v[104:107]\n"
        "v_mfma_f32_16x16x32_bf16 v[104:107], v[168:171], v[72:75], v[104:107]\n"
        "buffer_load_dwordx4 v15, s[8:11], s39 offen lds\n"
        "v_mfma_f32_16x16x32_bf16 v[108:111], v[180:183], v[72:75], 0\n"
        "v_mfma_f32_16x16x32_bf16 v[108:111], v[176:179], v[66:69], v[108:111]\n"
        "v_mfma_f32_16x16x32_bf16 v[108:111], v[172:175], v[60:63], v[108:111]\n"
        "buffer_load_dwordx4 v100, s[8:11], s39 offen offset:1024 lds\n"
        "v_mfma_f32_16x16x32_bf16 v[20:23], v[60:63], v[60:63], 0\n"
        "v_mfma_f32_16x16x32_bf16 v[20:23], v[66:69], v[66:69], v[20:23]\n"
        "v_mfma_f32_16x16x32_bf16 v[20:23], v[72:75], v[72:75], v[20:23]\n"
        "buffer_load_dwordx4 v103, s[8:11], s39 offen offset:2048 lds\n"
        "v_mov_b64 v[96:97], 0\n"
        "v_mov_b64 v[98:99], 0\n"
        "v_mfma_f32_16x16x32_bf16 v[24:27], v[42:45], v[42:45], 0\n"
        "v_mfma_f32_16x16x32_bf16 v[24:27], v[48:51], v[48:51], v[24:27]\n"
        "v_mfma_f32_16x16x32_bf16 v[24:27], v[54:57], v[54:57], v[24:27]\n"
        "v_smfmac_f32_16x16x64_bf16 v[96:99], v[60:63], v[34:41], v10\n"
        "s_nop 0\n"
        "v_cndmask_b32_e64 v93, v20, v21, s[48:49]\n"
        "v_smfmac_f32_16x16x64_bf16 v[96:99], v[66:69], v[78:85], v10\n"
        "v_cndmask_b32_e64 v93, v93, v22, s[50:51]\n"
        "v_smfmac_f32_16x16x64_bf16 v[96:99], v[72:75], v[104:111], v10\n"
        "v_mfma_f32_16x16x32_bf16 v[34:37], v[112:115], v[42:45], 0\n"
        "s_nop 0\n"
        "v_cndmask_b32_e64 v111, v24, v25, s[48:49]\n"
        "v_cndmask_b32_e64 v111, v111, v26, s[50:51]\n"
        "v_mfma_f32_16x16x32_bf16 v[34:37], v[116:119], v[48:51], v[34:37]\n"
        "s_and_b32 s69, s30, 1\n"
        "v_cndmask_b32_e64 v93, v93, v23, s[52:53]\n"
        "v_cndmask_b32_e64 v111, v111, v27, s[52:53]\n"
        "v_mfma_f32_16x16x32_bf16 v[34:37], v[120:123], v[54:57], v[34:37]\n"
        "s_lshl2_add_u32 s70, s69, 21600\n"
        "v_mov_b32 v102, s70\n"
        "v_cndmask_b32_dpp v93, v111, v93, vcc row_ror:4 row_mask:0xf bank_mask:0xf\n"
        "v_mfma_f32_16x16x32_bf16 v[38:41], v[132:135], v[54:57], 0\n"
        "s_mul_i32 s69, s69, 40064\n"
        "s_mul_i32 s70, s31, 1664\n"
        "v_mfma_f32_16x16x32_bf16 v[38:41], v[128:131], v[48:51], v[38:41]\n"
        "s_add_u32 s69, s69, s70\n"
        "v_add_u32 v11, s69, v3\n"
        "v_mfma_f32_16x16x32_bf16 v[38:41], v[124:127], v[42:45], v[38:41]\n"
        "v_add_u32 v12, s69, v2\n"
        "s_lshr_b32 s68, s30, 1\n"
        "v_mfma_f32_16x16x32_bf16 v[104:107], v[136:139], v[42:45], 0\n"
        "s_lshl2_add_u32 s68, s68, 4\n"
        "s_cmp_eq_u32 s31, 6\n"
        "s_cbranch_scc1 F8_%=\n"
        "F9_%=:\n"
        "v_mfma_f32_16x16x32_bf16 v[104:107], v[140:143], v[48:51], v[104:107]\n"
        "s_mov_b32 s72, s30\n"
        "s_mov_b32 s30, s34\n"
        "v_mfma_f32_16x16x32_bf16 v[104:107], v[144:147], v[54:57], v[104:107]\n"
        "s_mov_b32 s31, s35\n"
        "s_add_u32 s37, s37, 4\n"
        "v_mfma_f32_16x16x32_bf16 v[108:111], v[156:159], v[54:57], 0\n"
        "s_cmp_ge_u32 s37, 49\n"
        "s_cselect_b32 s69, 49, 0\n"
        "v_mfma_f32_16x16x32_bf16 v[108:111], v[152:155], v[48:51], v[108:111]\n"
        "s_sub_u32 s37, s37, s69\n"
        "s_sub_u32 s69, s37, 4\n"
        "s_cmp_lt_u32 s69, 4\n"
        "v_mfma_f32_16x16x32_bf16 v[108:111], v[148:151], v[42:45], v[108:111]\n"
        "s_subb_u32 s36, s36, -4\n"
        "s_cmp_ge_u32 s36, 6\n"
        "v_mfma_f32_16x16x32_bf16 v[76:79], v[160:163], v[42:45], 0\n"
        "s_cselect_b32 s69, 6, 0\n"
        "s_addc_u32 s34, s34, 0\n"
        "v_mfma_f32_16x16x32_bf16 v[76:79], v[164:167], v[48:51], v[76:79]\n"
        "s_sub_u32 s36, s36, s69\n"
        "s_cmp_ge_u32 s34, s74\n"
        "v_mfma_f32_16x16x32_bf16 v[76:79], v[168:171], v[54:57], v[76:79]\n"
        "s_cbranch_scc1 F10_%=\n"
        "F11_%=:\n"
        "s_mul_i32 s38, s34, 256\n"
        "v_mfma_f32_16x16x32_bf16 v[80:83], v[180:183], v[54:57], 0\n"
        "s_add_u32 s38, s38, s16\n"
        "s_cmp_ge_u32 s34, s43\n"
        "s_cselect_b32 s38, s22, s38\n"
        "v_mfma_f32_16x16x32_bf16 v[80:83], v[176:179], v[48:51], v[80:83]\n"
        "s_cmp_gt_u32 s34, s43\n"
        "s_cselect_b32 s38, s23, s38\n"
        "v_mfma_f32_16x16x32_bf16 v[80:83], v[172:175], v[42:45], v[80:83]\n"
        "s_mul_hi_u32 s70, s38, 58835169\n"
        "s_mul_i32 s38, s38, 1344\n"
        "v_mfma_f32_16x16x32_bf16 v[20:23], v[16:19], v[60:63], 0\n"
        "s_mul_i32 s70, s70, 588672\n"
        "s_add_u32 s38, s38, s70\n"
        "v_mfma_f32_16x16x32_bf16 v[20:23], v[16:19], v[66:69], v[20:23]\n"
        "s_mul_i32 s69, s36, 98304\n"
        "s_add_u32 s38, s38, s69\n"
        "s_mov_b32 s35, s36\n"
        "v_mfma_f32_16x16x32_bf16 v[20:23], v[16:19], v[72:75], v[20:23]\n"
        "s_mul_i32 s69, s36, -12\n"
        "v_mov_b64 v[30:31], 0\n"
        "v_mov_b64 v[32:33], 0\n"
        "v_mfma_f32_16x16x32_bf16 v[24:27], v[16:19], v[42:45], 0\n"
        "s_add_u32 s69, s69, 60\n"
        "s_lshl_b64 s[54:55], -1, s69\n"
        "s_lshl_b64 s[56:57], s[54:55], 24\n"
        "v_mfma_f32_16x16x32_bf16 v[24:27], v[16:19], v[48:51], v[24:27]\n"
        "s_ashr_i64 s[60:61], s[54:55], 8\n"
        "s_mov_b32 s58, s61\n"
        "v_mfma_f32_16x16x32_bf16 v[24:27], v[16:19], v[54:57], v[24:27]\n"
        "s_mov_b32 s59, s56\n"
        "v_cndmask_b32_e64 v15, v4, v7, s[56:57]\n"
        "v_smfmac_f32_16x16x64_bf16 v[30:33], v[42:45], v[34:41], v10\n"
        "v_cndmask_b32_e64 v100, v5, v8, s[58:59]\n"
        "v_smfmac_f32_16x16x64_bf16 v[30:33], v[48:51], v[104:111], v10\n"
        "v_cndmask_b32_e64 v103, v6, v9, s[60:61]\n"
        "v_smfmac_f32_16x16x64_bf16 v[30:33], v[54:57], v[76:83], v10\n"
        "s_cmp_eq_u32 s37, 3\n"
        "s_cbranch_scc1 F12_%=\n"
        "F13_%=:\n"
        "v_cndmask_b32_dpp v110, v24, v20, vcc row_ror:4 row_mask:0xf bank_mask:0xf\n"
        "v_mfma_f32_16x16x32_bf16 v[36:39], v[54:57], v[192:195], 0\n"
        "s_cmp_lt_u32 s34, s14\n"
        "v_mul_f32 v111, s28, v110\n"
        "v_fma_f32 v111, -v111, v110, v93\n"
        "v_mfma_f32_16x16x32_bf16 v[36:39], v[48:51], v[188:191], v[36:39]\n"
        "s_cselect_b32 s38, s38, 0x80000000\n"
        "s_add_u32 s39, s38, 100270464\n"
        "v_add_f32_e64 v111, s29, |v111|\n"
        "v_mfma_f32_16x16x32_bf16 v[36:39], v[42:45], v[184:187], v[36:39]\n"
        "v_rsq_f32 v84, v111\n"
        "v_cndmask_b32_dpp v104, v30, v96, vcc row_ror:4 row_mask:0xf bank_mask:0xf\n"
        "v_mfma_f32_16x16x32_bf16 v[24:27], v[72:75], v[192:195], 0\n"
        "v_cndmask_b32_e64 v84, 0, v84, s[46:47]\n"
        "v_cndmask_b32_dpp v105, v31, v97, vcc row_ror:4 row_mask:0xf bank_mask:0xf\n"
        "v_mfma_f32_16x16x32_bf16 v[24:27], v[66:69], v[188:191], v[24:27]\n"
        "v_cndmask_b32_dpp v85, v32, v98, vcc row_ror:4 row_mask:0xf bank_mask:0xf\n"
        "v_mul_f32_dpp v104, v84, v104 quad_perm:[0,0,0,0] row_mask:0xf bank_mask:0xf\n"
        "v_mfma_f32_16x16x32_bf16 v[24:27], v[60:63], v[184:187], v[24:27]\n"
        "v_mul_f32_dpp v105, v84, v105 quad_perm:[1,1,1,1] row_mask:0xf bank_mask:0xf\n"
        "v_mul_f32_dpp v85, v84, v85 quad_perm:[2,2,2,2] row_mask:0xf bank_mask:0xf\n"
        "v_mfma_f32_16x16x32_bf16 v[20:23], v[60:63], v[220:223], 0\n"
        "v_cndmask_b32_dpp v106, v33, v99, vcc row_ror:4 row_mask:0xf bank_mask:0xf\n"
        "v_max3_f32 v107, v104, v105, v85\n"
        "v_mfma_f32_16x16x32_bf16 v[20:23], v[66:69], v[224:227], v[20:23]\n"
        "v_mul_f32_dpp v106, v84, v106 quad_perm:[3,3,3,3] row_mask:0xf bank_mask:0xf\n"
        "v_max_f32 v107, v107, v106\n"
        "v_mfma_f32_16x16x32_bf16 v[20:23], v[72:75], v[228:231], v[20:23]\n"
        "v_mul_f32 v107, v107, v84\n"
        "v_fma_f32 v104, v104, v84, -v107\n"
        "v_fma_f32 v105, v105, v84, -v107\n"
        "v_mfma_f32_16x16x32_bf16 v[32:35], v[42:45], v[220:223], 0\n"
        "v_fma_f32 v85, v85, v84, -v107\n"
        "v_fma_f32 v106, v106, v84, -v107\n"
        "v_mfma_f32_16x16x32_bf16 v[32:35], v[48:51], v[224:227], v[32:35]\n"
        "v_exp_f32 v104, v104\n"
        "v_mfma_f32_16x16x32_bf16 v[32:35], v[54:57], v[228:231], v[32:35]\n"
        "v_exp_f32 v105, v105\n"
        "v_mfma_f32_16x16x32_bf16 v[108:111], v[54:57], v[204:207], 0\n"
        "v_exp_f32 v85, v85\n"
        "v_mfma_f32_16x16x32_bf16 v[108:111], v[48:51], v[200:203], v[108:111]\n"
        "v_exp_f32 v106, v106\n"
        "v_mfma_f32_16x16x32_bf16 v[108:111], v[42:45], v[196:199], v[108:111]\n"
        "v_add_f32 v107, v104, v105\n"
        "v_add_f32 v101, v85, v106\n"
        "v_add_f32 v107, v107, v101\n"
        "v_mfma_f32_16x16x32_bf16 v[80:83], v[72:75], v[204:207], 0\n"
        "v_rcp_f32 v107, v107\n"
        "v_mfma_f32_16x16x32_bf16 v[80:83], v[66:69], v[200:203], v[80:83]\n"
        "v_mul_f32_dpp v104, v84, v104 quad_perm:[0,0,0,0] row_mask:0xf bank_mask:0xf\n"
        "v_mul_f32_dpp v105, v84, v105 quad_perm:[1,1,1,1] row_mask:0xf bank_mask:0xf\n"
        "v_mfma_f32_16x16x32_bf16 v[80:83], v[60:63], v[196:199], v[80:83]\n"
        "v_mul_f32_dpp v85, v84, v85 quad_perm:[2,2,2,2] row_mask:0xf bank_mask:0xf\n"
        "v_mul_f32_dpp v106, v84, v106 quad_perm:[3,3,3,3] row_mask:0xf bank_mask:0xf\n"
        "v_mul_f32 v104, v104, v107\n"
        "v_mfma_f32_16x16x32_bf16 v[76:79], v[60:63], v[232:235], 0\n"
        "v_mul_f32 v105, v105, v107\n"
        "v_mul_f32 v85, v85, v107\n"
        "v_mfma_f32_16x16x32_bf16 v[76:79], v[66:69], v[236:239], v[76:79]\n"
        "v_mul_f32 v106, v106, v107\n"
        "v_cvt_pk_bf16_f32 v99, v104, v105\n"
        "v_mfma_f32_16x16x32_bf16 v[76:79], v[72:75], v[240:243], v[76:79]\n"
        "v_cvt_pk_bf16_f32 v101, v85, v106\n"
        "v_and_b32 v58, v1, v99\n"
        "v_and_b32 v59, v1, v101\n"
        "v_mfma_f32_16x16x32_bf16 v[104:107], v[42:45], v[232:235], 0\n"
        "v_cvt_pk_bf16_f32 v92, v24, v25\n"
        "v_cvt_pk_bf16_f32 v93, v26, v27\n"
        "v_mfma_f32_16x16x32_bf16 v[104:107], v[48:51], v[236:239], v[104:107]\n"
        "v_and_b32_dpp v40, v99, v1 row_ror:12 row_mask:0xf bank_mask:0xf\n"
        "v_and_b32_dpp v41, v101, v1 row_ror:12 row_mask:0xf bank_mask:0xf\n"
        "v_mfma_f32_16x16x32_bf16 v[104:107], v[54:57], v[240:243], v[104:107]\n"
        "v_cvt_pk_bf16_f32 v84, v36, v37\n"
        "v_cvt_pk_bf16_f32 v85, v38, v39\n"
        "v_mov_b64 v[70:71], v[58:59]\n"
        "v_mfma_f32_16x16x32_bf16 v[36:39], v[92:95], v[58:61], 0\n"
        "v_cvt_pk_bf16_f32 v96, v20, v21\n"
        "v_cvt_pk_bf16_f32 v97, v22, v23\n"
        "v_mfma_f32_16x16x32_bf16 v[22:25], v[84:87], v[40:43], 0\n"
        "v_mov_b64 v[52:53], v[40:41]\n"
        "v_cvt_pk_bf16_f32 v88, v32, v33\n"
        "v_cvt_pk_bf16_f32 v89, v34, v35\n"
        "v_mfma_f32_16x16x32_bf16 v[32:35], v[94:97], v[68:71], 0\n"
        "v_mov_b64 v[64:65], v[58:59]\n"
        "v_cvt_pk_bf16_f32 v30, v80, v81\n"
        "v_mfma_f32_16x16x32_bf16 v[96:99], v[86:89], v[50:53], 0\n"
        "v_cvt_pk_bf16_f32 v31, v82, v83\n"
        "v_mov_b64 v[46:47], v[40:41]\n"
        "v_mfma_f32_16x16x32_bf16 v[80:83], v[54:57], v[216:219], 0\n"
        "v_cvt_pk_bf16_f32 v92, v108, v109\n"
        "v_cvt_pk_bf16_f32 v93, v110, v111\n"
        "v_mfma_f32_16x16x32_bf16 v[80:83], v[48:51], v[212:215], v[80:83]\n"
        "v_cvt_pk_bf16_f32 v26, v76, v77\n"
        "v_cvt_pk_bf16_f32 v27, v78, v79\n"
        "v_mfma_f32_16x16x32_bf16 v[80:83], v[42:45], v[208:211], v[80:83]\n"
        "v_cvt_pk_bf16_f32 v88, v104, v105\n"
        "v_cvt_pk_bf16_f32 v89, v106, v107\n"
        "v_mfma_f32_16x16x32_bf16 v[108:111], v[72:75], v[216:219], 0\n"
        "v_mov_b64 v[76:77], v[58:59]\n"
        "v_mov_b64 v[58:59], v[40:41]\n"
        "v_mfma_f32_16x16x32_bf16 v[108:111], v[66:69], v[212:215], v[108:111]\n"
        "v_cvt_pk_bf16_f32 v104, v36, v37\n"
        "v_cvt_pk_bf16_f32 v105, v38, v39\n"
        "v_mfma_f32_16x16x32_bf16 v[108:111], v[60:63], v[208:211], v[108:111]\n"
        "v_cvt_pk_bf16_f32 v84, v80, v81\n"
        "v_cvt_pk_bf16_f32 v85, v82, v83\n"
        "v_mfma_f32_16x16x32_bf16 v[80:83], v[28:31], v[62:65], 0\n"
        "v_cvt_pk_bf16_f32 v106, v32, v33\n"
        "v_cvt_pk_bf16_f32 v107, v34, v35\n"
        "v_mfma_f32_16x16x32_bf16 v[32:35], v[90:93], v[44:47], 0\n"
        "v_cvt_pk_bf16_f32 v20, v22, v23\n"
        "v_cvt_pk_bf16_f32 v21, v24, v25\n"
        "v_mfma_f32_16x16x32_bf16 v[38:41], v[60:63], v[244:247], 0\n"
        "v_cvt_pk_bf16_f32 v92, v108, v109\n"
        "v_cvt_pk_bf16_f32 v93, v110, v111\n"
        "v_mfma_f32_16x16x32_bf16 v[38:41], v[66:69], v[248:251], v[38:41]\n"
        "v_cvt_pk_bf16_f32 v108, v80, v81\n"
        "v_cvt_pk_bf16_f32 v109, v82, v83\n"
        "v_mfma_f32_16x16x32_bf16 v[38:41], v[72:75], v[252:255], v[38:41]\n"
        "v_cvt_pk_bf16_f32 v22, v96, v97\n"
        "v_cvt_pk_bf16_f32 v23, v98, v99\n"
        "v_mfma_f32_16x16x32_bf16 v[80:83], v[42:45], v[244:247], 0\n"
        "v_cvt_pk_bf16_f32 v24, v32, v33\n"
        "v_cvt_pk_bf16_f32 v25, v34, v35\n"
        "v_mfma_f32_16x16x32_bf16 v[80:83], v[48:51], v[248:251], v[80:83]\n"
        "v_mfma_f32_16x16x32_bf16 v[80:83], v[54:57], v[252:255], v[80:83]\n"
        "v_mfma_f32_16x16x32_bf16 v[34:37], v[26:29], v[70:73], 0\n"
        "v_cvt_pk_bf16_f32 v96, v38, v39\n"
        "v_cvt_pk_bf16_f32 v97, v40, v41\n"
        "v_mfma_f32_16x16x32_bf16 v[38:41], v[88:91], v[52:55], 0\n"
        "v_mfma_f32_16x16x32_bf16 v[30:33], v[92:95], v[64:67], 0\n"
        "s_waitcnt vmcnt(0)\n"
        "ds_read_b128 v[60:63], v0\n"
        "ds_read_b128 v[42:45], v0 offset:768\n"
        "ds_read_b128 v[66:69], v0 offset:64\n"
        "v_cvt_pk_bf16_f32 v88, v80, v81\n"
        "v_mfma_f32_16x16x32_bf16 v[78:81], v[84:87], v[46:49], 0\n"
        "s_add_u32 s73, s73, 1\n"
        "ds_read_b128 v[48:51], v0 offset:832\n"
        "v_cvt_pk_bf16_f32 v89, v82, v83\n"
        "v_cvt_pk_bf16_f32 v110, v34, v35\n"
        "v_cvt_pk_bf16_f32 v111, v36, v37\n"
        "v_mfma_f32_16x16x32_bf16 v[82:85], v[94:97], v[74:77], 0\n"
        "s_cmp_lt_u32 s73, 4\n"
        "ds_read_b128 v[72:75], v0 offset:128\n"
        "v_cvt_pk_bf16_f32 v96, v30, v31\n"
        "v_cvt_pk_bf16_f32 v97, v32, v33\n"
        "v_cvt_pk_bf16_f32 v26, v38, v39\n"
        "v_mfma_f32_16x16x32_bf16 v[34:37], v[86:89], v[56:59], 0\n"
        "s_cbranch_scc1 F19_%=\n"
        "F20_%=:\n"
        "ds_read_b128 v[54:57], v0 offset:896\n"
        "v_cvt_pk_bf16_f32 v98, v82, v83\n"
        "v_cvt_pk_bf16_f32 v99, v84, v85\n"
        "v_cvt_pk_bf16_f32 v27, v40, v41\n"
        "v_mfma_f32_16x16x32_bf16 v[82:85], v[104:107], v[104:107], 0\n"
        "v_cvt_pk_bf16_f32 v30, v78, v79\n"
        "v_cvt_pk_bf16_f32 v31, v80, v81\n"
        "v_mfma_f32_16x16x32_bf16 v[82:85], v[108:111], v[108:111], v[82:85]\n"
        "v_cvt_pk_bf16_f32 v32, v34, v35\n"
        "v_cvt_pk_bf16_f32 v33, v36, v37\n"
        "v_mfma_f32_16x16x32_bf16 v[82:85], v[96:99], v[96:99], v[82:85]\n"
        "v_mfma_f32_16x16x32_bf16 v[76:79], v[20:23], v[20:23], 0\n"
        "v_mfma_f32_16x16x32_bf16 v[76:79], v[24:27], v[24:27], v[76:79]\n"
        "v_mfma_f32_16x16x32_bf16 v[76:79], v[30:33], v[30:33], v[76:79]\n"
        "s_cmp_lt_u32 s30, s14\n"
        "s_cmp_lg_u32 s72, 0\n"
        "s_cbranch_scc1 PTOP_%=\n"
        "s_setprio 3\n"
        "s_nop 7\n"
        "F21_%=:\n"
        "ds_read_b32 v101, v102 offset:8\n"
        "s_waitcnt lgkmcnt(0)\n"
        "v_readfirstlane_b32 s40, v101\n"
        "s_nop 3\n"
        "s_cmp_ge_i32 s40, s68\n"
        "s_cbranch_scc1 F22_%=\n"
        "s_sleep 1\n"
        "s_sub_u32 s41, s41, 1\n"
        "s_cbranch_scc0 F21_%=\n"
        "s_endpgm\n"
        "F22_%=:\n"
        "v_cndmask_b32_e64 v101, v82, v83, s[48:49]\n"
        "v_cndmask_b32_e64 v93, v76, v77, s[48:49]\n"
        "v_cndmask_b32_e64 v101, v101, v84, s[50:51]\n"
        "v_cndmask_b32_e64 v93, v93, v78, s[50:51]\n"
        "v_cndmask_b32_e64 v101, v101, v85, s[52:53]\n"
        "v_cndmask_b32_e64 v93, v93, v79, s[52:53]\n"
        "ds_write_b128 v11, v[104:107]\n"
        "ds_write_b128 v11, v[20:23] offset:832\n"
        "v_cndmask_b32_dpp v101, v93, v101, vcc row_ror:4 row_mask:0xf bank_mask:0xf\n"
        "ds_write_b128 v11, v[108:111] offset:64\n"
        "ds_write_b128 v11, v[24:27] offset:896\n"
        "ds_write_b128 v11, v[96:99] offset:128\n"
        "ds_write_b128 v11, v[30:33] offset:960\n"
        "s_mov_b64 exec, s[46:47]\n"
        "ds_write_b32 v12, v101\n"
        "s_mov_b64 exec, -1\n"
        "s_mov_b64 exec, 2\n"
        "ds_inc_u32 v102, v0 offset:0\n"
        "s_mov_b64 exec, -1\n"
        "s_setprio 3\n"
        "s_waitcnt lgkmcnt(0)\n"
        "s_branch PFST_%=\n"
        ".p2align 6\n"
        "s_nop 0\n"
        "s_nop 0\n"
        "s_nop 0\n"
        "s_nop 0\n"
        "PTOP_%=:\n"
        "s_waitcnt lgkmcnt(5)\n"
        "v_mfma_f32_16x16x32_bf16 v[34:37], v[112:115], v[60:63], 0\n"
        "s_mov_b32 m0, s17\n"
        "v_cndmask_b32_e64 v88, v82, v83, s[48:49]\n"
        "s_nop 1\n"
        "v_cndmask_b32_e64 v89, v76, v77, s[48:49]\n"
        "s_waitcnt lgkmcnt(3)\n"
        "v_mfma_f32_16x16x32_bf16 v[34:37], v[116:119], v[66:69], v[34:37]\n"
        "ds_read_b32 v101, v102 offset:8\n"
        "v_cndmask_b32_e64 v88, v88, v84, s[50:51]\n"
        "s_waitcnt lgkmcnt(2)\n"
        "v_mfma_f32_16x16x32_bf16 v[34:37], v[120:123], v[72:75], v[34:37]\n"
        "s_waitcnt lgkmcnt(1)\n"
        "buffer_load_dwordx4 v15, s[8:11], s38 offen lds\n"
        "v_mfma_f32_16x16x32_bf16 v[38:41], v[132:135], v[72:75], 0\n"
        "v_cndmask_b32_e64 v89, v89, v78, s[50:51]\n"
        "v_cndmask_b32_e64 v88, v88, v85, s[52:53]\n"
        "v_mfma_f32_16x16x32_bf16 v[38:41], v[128:131], v[66:69], v[38:41]\n"
        "s_setprio 3 \n"
        "v_cndmask_b32_e64 v89, v89, v79, s[52:53]\n"
        "v_mfma_f32_16x16x32_bf16 v[38:41], v[124:127], v[60:63], v[38:41]\n"
        "buffer_load_dwordx4 v100, s[8:11], s38 offen offset:1024 lds\n"
        "v_mfma_f32_16x16x32_bf16 v[78:81], v[136:139], v[60:63], 0\n"
        "s_waitcnt lgkmcnt(0)\n"
        "v_readfirstlane_b32 s40, v101\n"
        "v_cndmask_b32_dpp v88, v89, v88, vcc row_ror:4 row_mask:0xf bank_mask:0xf\n"
        "v_mfma_f32_16x16x32_bf16 v[78:81], v[140:143], v[66:69], v[78:81]\n"
        "s_cmp_lt_i32 s40, s68\n"
        "v_mfma_f32_16x16x32_bf16 v[78:81], v[144:147], v[72:75], v[78:81]\n"
        "s_cbranch_scc1 L3_%=\n"
        "L4_%=:\n"
        "ds_write_b128 v11, v[104:107]\n"
        "s_mov_b64 exec, s[46:47]\n"
        "ds_write_b32 v12, v88\n"
        "s_mov_b64 exec, -1\n"
        "v_mfma_f32_16x16x32_bf16 v[82:85], v[156:159], v[72:75], 0\n"
        "v_mfma_f32_16x16x32_bf16 v[82:85], v[152:155], v[66:69], v[82:85]\n"
        "v_mfma_f32_16x16x32_bf16 v[82:85], v[148:151], v[60:63], v[82:85]\n"
        "buffer_load_dwordx4 v103, s[8:11], s38 offen offset:2048 lds\n"
        "s_mov_b32 m0, s71\n"
        "ds_write_b128 v11, v[20:23] offset:832\n"
        "s_cmp_eq_u32 s72, s30\n"
        "v_mfma_f32_16x16x32_bf16 v[104:107], v[160:163], v[60:63], 0\n"
        "s_setprio 3\n"
        "s_cbranch_scc1 L5_%=\n"
        "s_setprio 1\n"
        "L5_%=:\n"
        "v_mfma_f32_16x16x32_bf16 v[104:107], v[164:167], v[66:69], v[104:107]\n"
        "v_mfma_f32_16x16x32_bf16 v[104:107], v[168:171], v[72:75], v[104:107]\n"
        "buffer_load_dwordx4 v15, s[8:11], s39 offen lds\n"
        "ds_write_b128 v11, v[108:111] offset:64\n"
        "v_mfma_f32_16x16x32_bf16 v[108:111], v[180:183], v[72:75], 0\n"
        "v_mfma_f32_16x16x32_bf16 v[108:111], v[176:179], v[66:69], v[108:111]\n"
        "v_mfma_f32_16x16x32_bf16 v[108:111], v[172:175], v[60:63], v[108:111]\n"
        "buffer_load_dwordx4 v100, s[8:11], s39 offen offset:1024 lds\n"
        "ds_write_b128 v11, v[24:27] offset:896\n"
        "v_mfma_f32_16x16x32_bf16 v[20:23], v[60:63], v[60:63], 0\n"
        "v_mfma_f32_16x16x32_bf16 v[20:23], v[66:69], v[66:69], v[20:23]\n"
        "v_mfma_f32_16x16x32_bf16 v[20:23], v[72:75], v[72:75], v[20:23]\n"
        "buffer_load_dwordx4 v103, s[8:11], s39 offen offset:2048 lds\n"
        "ds_write_b128 v11, v[96:99] offset:128\n"
        "v_mov_b64 v[96:97], 0\n"
        "v_mov_b64 v[98:99], 0\n"
        "v_mfma_f32_16x16x32_bf16 v[24:27], v[42:45], v[42:45], 0\n"
        "v_mfma_f32_16x16x32_bf16 v[24:27], v[48:51], v[48:51], v[24:27]\n"
        "v_mfma_f32_16x16x32_bf16 v[24:27], v[54:57], v[54:57], v[24:27]\n"
        "ds_write_b128 v11, v[30:33] offset:960\n"
        "v_smfmac_f32_16x16x64_bf16 v[96:99], v[60:63], v[34:41], v10\n"
        "v_cndmask_b32_e64 v93, v20, v21, s[48:49]\n"
        "v_smfmac_f32_16x16x64_bf16 v[96:99], v[66:69], v[78:85], v10\n"
        "v_cndmask_b32_e64 v93, v93, v22, s[50:51]\n"
        "v_smfmac_f32_16x16x64_bf16 v[96:99], v[72:75], v[104:111], v10\n"
        "s_mov_b64 exec, 2\n"
        "ds_inc_u32 v102, v0 offset:0\n"
        "s_mov_b64 exec, -1\n"
        "v_mfma_f32_16x16x32_bf16 v[34:37], v[112:115], v[42:45], 0\n"
        "v_cndmask_b32_e64 v111, v24, v25, s[48:49]\n"
        "v_cndmask_b32_e64 v111, v111, v26, s[50:51]\n"
        "v_mfma_f32_16x16x32_bf16 v[34:37], v[116:119], v[48:51], v[34:37]\n"
        "s_and_b32 s69, s30, 1\n"
        "v_cndmask_b32_e64 v93, v93, v23, s[52:53]\n"
        "v_cndmask_b32_e64 v111, v111, v27, s[52:53]\n"
        "v_mfma_f32_16x16x32_bf16 v[34:37], v[120:123], v[54:57], v[34:37]\n"
        "s_lshl2_add_u32 s70, s69, 21600\n"
        "v_mov_b32 v102, s70\n"
        "v_cndmask_b32_dpp v93, v111, v93, vcc row_ror:4 row_mask:0xf bank_mask:0xf\n"
        "v_mfma_f32_16x16x32_bf16 v[38:41], v[132:135], v[54:57], 0\n"
        "s_mul_i32 s69, s69, 40064\n"
        "s_mul_i32 s70, s31, 1664\n"
        "v_mfma_f32_16x16x32_bf16 v[38:41], v[128:131], v[48:51], v[38:41]\n"
        "s_add_u32 s69, s69, s70\n"
        "v_add_u32 v11, s69, v3\n"
        "v_mfma_f32_16x16x32_bf16 v[38:41], v[124:127], v[42:45], v[38:41]\n"
        "v_add_u32 v12, s69, v2\n"
        "s_lshr_b32 s68, s30, 1\n"
        "v_mfma_f32_16x16x32_bf16 v[104:107], v[136:139], v[42:45], 0\n"
        "s_lshl2_add_u32 s68, s68, 4\n"
        "s_cmp_eq_u32 s31, 6\n"
        "s_cbranch_scc1 L8_%=\n"
        "L9_%=:\n"
        "v_mfma_f32_16x16x32_bf16 v[104:107], v[140:143], v[48:51], v[104:107]\n"
        "s_mov_b32 s72, s30\n"
        "s_mov_b32 s30, s34\n"
        "v_mfma_f32_16x16x32_bf16 v[104:107], v[144:147], v[54:57], v[104:107]\n"
        "s_mov_b32 s31, s35\n"
        "s_add_u32 s37, s37, 4\n"
        "v_mfma_f32_16x16x32_bf16 v[108:111], v[156:159], v[54:57], 0\n"
        "s_cmp_ge_u32 s37, 49\n"
        "s_cselect_b32 s69, 49, 0\n"
        "v_mfma_f32_16x16x32_bf16 v[108:111], v[152:155], v[48:51], v[108:111]\n"
        "s_sub_u32 s37, s37, s69\n"
        "s_sub_u32 s69, s37, 4\n"
        "s_cmp_lt_u32 s69, 4\n"
        "v_mfma_f32_16x16x32_bf16 v[108:111], v[148:151], v[42:45], v[108:111]\n"
        "s_subb_u32 s36, s36, -4\n"
        "s_cmp_ge_u32 s36, 6\n"
        "v_mfma_f32_16x16x32_bf16 v[76:79], v[160:163], v[42:45], 0\n"
        "s_cselect_b32 s69, 6, 0\n"
        "s_addc_u32 s34, s34, 0\n"
        "v_mfma_f32_16x16x32_bf16 v[76:79], v[164:167], v[48:51], v[76:79]\n"
        "s_sub_u32 s36, s36, s69\n"
        "s_cmp_ge_u32 s34, s74\n"
        "v_mfma_f32_16x16x32_bf16 v[76:79], v[168:171], v[54:57], v[76:79]\n"
        "s_cbranch_scc1 L10_%=\n"
        "L11_%=:\n"
        "s_mul_i32 s38, s34, 256\n"
        "v_mfma_f32_16x16x32_bf16 v[80:83], v[180:183], v[54:57], 0\n"
        "s_add_u32 s38, s38, s16\n"
        "s_cmp_ge_u32 s34, s43\n"
        "s_cselect_b32 s38, s22, s38\n"
        "v_mfma_f32_16x16x32_bf16 v[80:83], v[176:179], v[48:51], v[80:83]\n"
        "s_cmp_gt_u32 s34, s43\n"
        "s_cselect_b32 s38, s23, s38\n"
        "v_mfma_f32_16x16x32_bf16 v[80:83], v[172:175], v[42:45], v[80:83]\n"
        "s_mul_hi_u32 s70, s38, 58835169\n"
        "s_mul_i32 s38, s38, 1344\n"
        "v_mfma_f32_16x16x32_bf16 v[20:23], v[16:19], v[60:63], 0\n"
        "s_mul_i32 s70, s70, 588672\n"
        "s_add_u32 s38, s38, s70\n"
        "v_mfma_f32_16x16x32_bf16 v[20:23], v[16:19], v[66:69], v[20:23]\n"
        "s_mul_i32 s69, s36, 98304\n"
        "s_add_u32 s38, s38, s69\n"
        "s_mov_b32 s35, s36\n"
        "v_mfma_f32_16x16x32_bf16 v[20:23], v[16:19], v[72:75], v[20:23]\n"
        "s_mul_i32 s69, s36, -12\n"
        "v_mov_b64 v[30:31], 0\n"
        "v_mov_b64 v[32:33], 0\n"
        "v_mfma_f32_16x16x32_bf16 v[24:27], v[16:19], v[42:45], 0\n"
        "s_add_u32 s69, s69, 60\n"
        "s_lshl_b64 s[54:55], -1, s69\n"
        "s_lshl_b64 s[56:57], s[54:55], 24\n"
        "v_mfma_f32_16x16x32_bf16 v[24:27], v[16:19], v[48:51], v[24:27]\n"
        "s_ashr_i64 s[60:61], s[54:55], 8\n"
        "s_mov_b32 s58, s61\n"
        "v_mfma_f32_16x16x32_bf16 v[24:27], v[16:19], v[54:57], v[24:27]\n"
        "s_mov_b32 s59, s56\n"
        "v_cndmask_b32_e64 v15, v4, v7, s[56:57]\n"
        "v_smfmac_f32_16x16x64_bf16 v[30:33], v[42:45], v[34:41], v10\n"
        "v_cndmask_b32_e64 v100, v5, v8, s[58:59]\n"
        "v_smfmac_f32_16x16x64_bf16 v[30:33], v[48:51], v[104:111], v10\n"
        "v_cndmask_b32_e64 v103, v6, v9, s[60:61]\n"
        "v_smfmac_f32_16x16x64_bf16 v[30:33], v[54:57], v[76:83], v10\n"
        "s_cmp_eq_u32 s37, 3\n"
        "s_cbranch_scc1 L12_%=\n"
        "L13_%=:\n"
        "v_cndmask_b32_dpp v110, v24, v20, vcc row_ror:4 row_mask:0xf bank_mask:0xf\n"
        "v_mfma_f32_16x16x32_bf16 v[36:39], v[54:57], v[192:195], 0\n"
        "s_cmp_lt_u32 s34, s14\n"
        "v_mul_f32 v111, s28, v110\n"
        "v_fma_f32 v111, -v111, v110, v93\n"
        "v_mfma_f32_16x16x32_bf16 v[36:39], v[48:51], v[188:191], v[36:39]\n"
        "s_cselect_b32 s38, s38, 0x80000000\n"
        "s_add_u32 s39, s38, 100270464\n"
        "v_add_f32_e64 v111, s29, |v111|\n"
        "v_mfma_f32_16x16x32_bf16 v[36:39], v[42:45], v[184:187], v[36:39]\n"
        "v_rsq_f32 v84, v111\n"
        "v_cndmask_b32_dpp v104, v30, v96, vcc row_ror:4 row_mask:0xf bank_mask:0xf\n"
        "v_mfma_f32_16x16x32_bf16 v[24:27], v[72:75], v[192:195], 0\n"
        "v_cndmask_b32_e64 v84, 0, v84, s[46:47]\n"
        "v_cndmask_b32_dpp v105, v31, v97, vcc row_ror:4 row_mask:0xf bank_mask:0xf\n"
        "v_mfma_f32_16x16x32_bf16 v[24:27], v[66:69], v[188:191], v[24:27]\n"
        "v_cndmask_b32_dpp v85, v32, v98, vcc row_ror:4 row_mask:0xf bank_mask:0xf\n"
        "v_mul_f32_dpp v104, v84, v104 quad_perm:[0,0,0,0] row_mask:0xf bank_mask:0xf\n"
        "v_mfma_f32_16x16x32_bf16 v[24:27], v[60:63], v[184:187], v[24:27]\n"
        "v_mul_f32_dpp v105, v84, v105 quad_perm:[1,1,1,1] row_mask:0xf bank_mask:0xf\n"
        "v_mul_f32_dpp v85, v84, v85 quad_perm:[2,2,2,2] row_mask:0xf bank_mask:0xf\n"
        "v_mfma_f32_16x16x32_bf16 v[20:23], v[60:63], v[220:223], 0\n"
        "v_cndmask_b32_dpp v106, v33, v99, vcc row_ror:4 row_mask:0xf bank_mask:0xf\n"
        "v_max3_f32 v107, v104, v105, v85\n"
        "v_mfma_f32_16x16x32_bf16 v[20:23], v[66:69], v[224:227], v[20:23]\n"
        "v_mul_f32_dpp v106, v84, v106 quad_perm:[3,3,3,3] row_mask:0xf bank_mask:0xf\n"
        "v_max_f32 v107, v107, v106\n"
        "v_mfma_f32_16x16x32_bf16 v[20:23], v[72:75], v[228:231], v[20:23]\n"
        "v_mul_f32 v107, v107, v84\n"
        "v_fma_f32 v104, v104, v84, -v107\n"
        "v_fma_f32 v105, v105, v84, -v107\n"
        "v_mfma_f32_16x16x32_bf16 v[32:35], v[42:45], v[220:223], 0\n"
        "v_fma_f32 v85, v85, v84, -v107\n"
        "v_fma_f32 v106, v106, v84, -v107\n"
        "v_mfma_f32_16x16x32_bf16 v[32:35], v[48:51], v[224:227], v[32:35]\n"
        "v_exp_f32 v104, v104\n"
        "v_mfma_f32_16x16x32_bf16 v[32:35], v[54:57], v[228:231], v[32:35]\n"
        "v_exp_f32 v105, v105\n"
        "v_mfma_f32_16x16x32_bf16 v[108:111], v[54:57], v[204:207], 0\n"
        "v_exp_f32 v85, v85\n"
        "v_mfma_f32_16x16x32_bf16 v[108:111], v[48:51], v[200:203], v[108:111]\n"
        "v_exp_f32 v106, v106\n"
        "v_mfma_f32_16x16x32_bf16 v[108:111], v[42:45], v[196:199], v[108:111]\n"
        "v_add_f32 v107, v104, v105\n"
        "v_add_f32 v101, v85, v106\n"
        "v_add_f32 v107, v107, v101\n"
        "v_mfma_f32_16x16x32_bf16 v[80:83], v[72:75], v[204:207], 0\n"
        "v_rcp_f32 v107, v107\n"
        "v_mfma_f32_16x16x32_bf16 v[80:83], v[66:69], v[200:203], v[80:83]\n"
        "v_mul_f32_dpp v104, v84, v104 quad_perm:[0,0,0,0] row_mask:0xf bank_mask:0xf\n"
        "v_mul_f32_dpp v105, v84, v105 quad_perm:[1,1,1,1] row_mask:0xf bank_mask:0xf\n"
        "v_mfma_f32_16x16x32_bf16 v[80:83], v[60:63], v[196:199], v[80:83]\n"
        "v_mul_f32_dpp v85, v84, v85 quad_perm:[2,2,2,2] row_mask:0xf bank_mask:0xf\n"
        "v_mul_f32_dpp v106, v84, v106 quad_perm:[3,3,3,3] row_mask:0xf bank_mask:0xf\n"
        "v_mul_f32 v104, v104, v107\n"
        "v_mfma_f32_16x16x32_bf16 v[76:79], v[60:63], v[232:235], 0\n"
        "v_mul_f32 v105, v105, v107\n"
        "v_mul_f32 v85, v85, v107\n"
        "v_mfma_f32_16x16x32_bf16 v[76:79], v[66:69], v[236:239], v[76:79]\n"
        "v_mul_f32 v106, v106, v107\n"
        "v_cvt_pk_bf16_f32 v99, v104, v105\n"
        "v_mfma_f32_16x16x32_bf16 v[76:79], v[72:75], v[240:243], v[76:79]\n"
        "v_cvt_pk_bf16_f32 v101, v85, v106\n"
        "v_and_b32 v58, v1, v99\n"
        "v_and_b32 v59, v1, v101\n"
        "v_mfma_f32_16x16x32_bf16 v[104:107], v[42:45], v[232:235], 0\n"
        "v_cvt_pk_bf16_f32 v92, v24, v25\n"
        "v_cvt_pk_bf16_f32 v93, v26, v27\n"
        "v_mfma_f32_16x16x32_bf16 v[104:107], v[48:51], v[236:239], v[104:107]\n"
        "v_and_b32_dpp v40, v99, v1 row_ror:12 row_mask:0xf bank_mask:0xf\n"
        "v_and_b32_dpp v41, v101, v1 row_ror:12 row_mask:0xf bank_mask:0xf\n"
        "v_mfma_f32_16x16x32_bf16 v[104:107], v[54:57], v[240:243], v[104:107]\n"
        "v_cvt_pk_bf16_f32 v84, v36, v37\n"
        "v_cvt_pk_bf16_f32 v85, v38, v39\n"
        "v_mov_b64 v[70:71], v[58:59]\n"
        "v_mfma_f32_16x16x32_bf16 v[36:39], v[92:95], v[58:61], 0\n"
        "v_cvt_pk_bf16_f32 v96, v20, v21\n"
        "v_cvt_pk_bf16_f32 v97, v22, v23\n"
        "v_mfma_f32_16x16x32_bf16 v[22:25], v[84:87], v[40:43], 0\n"
        "v_mov_b64 v[52:53], v[40:41]\n"
        "v_cvt_pk_bf16_f32 v88, v32, v33\n"
        "v_cvt_pk_bf16_f32 v89, v34, v35\n"
        "v_mfma_f32_16x16x32_bf16 v[32:35], v[94:97], v[68:71], 0\n"
        "v_mov_b64 v[64:65], v[58:59]\n"
        "v_cvt_pk_bf16_f32 v30, v80, v81\n"
        "v_mfma_f32_16x16x32_bf16 v[96:99], v[86:89], v[50:53], 0\n"
        "v_cvt_pk_bf16_f32 v31, v82, v83\n"
        "v_mov_b64 v[46:47], v[40:41]\n"
        "v_mfma_f32_16x16x32_bf16 v[80:83], v[54:57], v[216:219], 0\n"
        "v_cvt_pk_bf16_f32 v92, v108, v109\n"
        "v_cvt_pk_bf16_f32 v93, v110, v111\n"
        "v_mfma_f32_16x16x32_bf16 v[80:83], v[48:51], v[212:215], v[80:83]\n"
        "v_cvt_pk_bf16_f32 v26, v76, v77\n"
        "v_cvt_pk_bf16_f32 v27, v78, v79\n"
        "v_mfma_f32_16x16x32_bf16 v[80:83], v[42:45], v[208:211], v[80:83]\n"
        "v_cvt_pk_bf16_f32 v88, v104, v105\n"
        "v_cvt_pk_bf16_f32 v89, v106, v107\n"
        "v_mfma_f32_16x16x32_bf16 v[108:111], v[72:75], v[216:219], 0\n"
        "v_mov_b64 v[76:77], v[58:59]\n"
        "v_mov_b64 v[58:59], v[40:41]\n"
        "v_mfma_f32_16x16x32_bf16 v[108:111], v[66:69], v[212:215], v[108:111]\n"
        "v_cvt_pk_bf16_f32 v104, v36, v37\n"
        "v_cvt_pk_bf16_f32 v105, v38, v39\n"
        "v_mfma_f32_16x16x32_bf16 v[108:111], v[60:63], v[208:211], v[108:111]\n"
        "v_cvt_pk_bf16_f32 v84, v80, v81\n"
        "v_cvt_pk_bf16_f32 v85, v82, v83\n"
        "v_mfma_f32_16x16x32_bf16 v[80:83], v[28:31], v[62:65], 0\n"
        "v_cvt_pk_bf16_f32 v106, v32, v33\n"
        "v_cvt_pk_bf16_f32 v107, v34, v35\n"
        "v_mfma_f32_16x16x32_bf16 v[32:35], v[90:93], v[44:47], 0\n"
        "v_cvt_pk_bf16_f32 v20, v22, v23\n"
        "v_cvt_pk_bf16_f32 v21, v24, v25\n"
        "v_mfma_f32_16x16x32_bf16 v[38:41], v[60:63], v[244:247], 0\n"
        "v_cvt_pk_bf16_f32 v92, v108, v109\n"
        "v_cvt_pk_bf16_f32 v93, v110, v111\n"
        "v_mfma_f32_16x16x32_bf16 v[38:41], v[66:69], v[248:251], v[38:41]\n"
        "v_cvt_pk_bf16_f32 v108, v80, v81\n"
        "v_cvt_pk_bf16_f32 v109, v82, v83\n"
        "v_mfma_f32_16x16x32_bf16 v[38:41], v[72:75], v[252:255], v[38:41]\n"
        "v_cvt_pk_bf16_f32 v22, v96, v97\n"
        "v_cvt_pk_bf16_f32 v23, v98, v99\n"
        "v_mfma_f32_16x16x32_bf16 v[80:83], v[42:45], v[244:247], 0\n"
        "v_cvt_pk_bf16_f32 v24, v32, v33\n"
        "v_cvt_pk_bf16_f32 v25, v34, v35\n"
        "v_mfma_f32_16x16x32_bf16 v[80:83], v[48:51], v[248:251], v[80:83]\n"
        "v_mfma_f32_16x16x32_bf16 v[80:83], v[54:57], v[252:255], v[80:83]\n"
        "v_mfma_f32_16x16x32_bf16 v[34:37], v[26:29], v[70:73], 0\n"
        "v_cvt_pk_bf16_f32 v96, v38, v39\n"
        "v_cvt_pk_bf16_f32 v97, v40, v41\n"
        "v_mfma_f32_16x16x32_bf16 v[38:41], v[88:91], v[52:55], 0\n"
        "v_mfma_f32_16x16x32_bf16 v[30:33], v[92:95], v[64:67], 0\n"
        "s_waitcnt vmcnt(0)\n"
        "ds_read_b128 v[60:63], v0\n"
        "ds_read_b128 v[42:45], v0 offset:768\n"
        "ds_read_b128 v[66:69], v0 offset:64\n"
        "v_cvt_pk_bf16_f32 v88, v80, v81\n"
        "v_mfma_f32_16x16x32_bf16 v[78:81], v[84:87], v[46:49], 0\n"
        "s_add_u32 s73, s73, 1\n"
        "ds_read_b128 v[48:51], v0 offset:832\n"
        "v_cvt_pk_bf16_f32 v89, v82, v83\n"
        "v_cvt_pk_bf16_f32 v110, v34, v35\n"
        "v_cvt_pk_bf16_f32 v111, v36, v37\n"
        "v_mfma_f32_16x16x32_bf16 v[82:85], v[94:97], v[74:77], 0\n"
        "s_cmp_lt_u32 s73, 4\n"
        "ds_read_b128 v[72:75], v0 offset:128\n"
        "v_cvt_pk_bf16_f32 v96, v30, v31\n"
        "v_cvt_pk_bf16_f32 v97, v32, v33\n"
        "v_cvt_pk_bf16_f32 v26, v38, v39\n"
        "v_mfma_f32_16x16x32_bf16 v[34:37], v[86:89], v[56:59], 0\n"
        "s_cbranch_scc1 L19_%=\n"
        "L20_%=:\n"
        "ds_read_b128 v[54:57], v0 offset:896\n"
        "v_cvt_pk_bf16_f32 v98, v82, v83\n"
        "v_cvt_pk_bf16_f32 v99, v84, v85\n"
        "v_cvt_pk_bf16_f32 v27, v40, v41\n"
        "v_mfma_f32_16x16x32_bf16 v[82:85], v[104:107], v[104:107], 0\n"
        "v_cvt_pk_bf16_f32 v30, v78, v79\n"
        "v_cvt_pk_bf16_f32 v31, v80, v81\n"
        "v_mfma_f32_16x16x32_bf16 v[82:85], v[108:111], v[108:111], v[82:85]\n"
        "v_cvt_pk_bf16_f32 v32, v34, v35\n"
        "v_cvt_pk_bf16_f32 v33, v36, v37\n"
        "v_mfma_f32_16x16x32_bf16 v[82:85], v[96:99], v[96:99], v[82:85]\n"
        "v_mfma_f32_16x16x32_bf16 v[76:79], v[20:23], v[20:23], 0\n"
        "v_mfma_f32_16x16x32_bf16 v[76:79], v[24:27], v[24:27], v[76:79]\n"
        "v_mfma_f32_16x16x32_bf16 v[76:79], v[30:33], v[30:33], v[76:79]\n"
        "s_cmp_lt_u32 s30, s14\n"
        "s_cbranch_scc1 PTOP_%=\n"
        "s_waitcnt lgkmcnt(0)\n"
        "s_nop 15\n"
        "L21_%=:\n"
        "ds_read_b32 v101, v102 offset:8\n"
        "s_waitcnt lgkmcnt(0)\n"
        "v_readfirstlane_b32 s40, v101\n"
        "s_nop 3\n"
        "s_cmp_ge_i32 s40, s68\n"
        "s_cbranch_scc1 L22_%=\n"
        "s_sleep 1\n"
        "s_sub_u32 s41, s41, 1\n"
        "s_cbranch_scc0 L21_%=\n"
        "s_endpgm\n"
        "L22_%=:\n"
        "v_cndmask_b32_e64 v101, v82, v83, s[48:49]\n"
        "v_cndmask_b32_e64 v93, v76, v77, s[48:49]\n"
        "v_cndmask_b32_e64 v101, v101, v84, s[50:51]\n"
        "v_cndmask_b32_e64 v93, v93, v78, s[50:51]\n"
        "v_cndmask_b32_e64 v101, v101, v85, s[52:53]\n"
        "v_cndmask_b32_e64 v93, v93, v79, s[52:53]\n"
        "ds_write_b128 v11, v[104:107]\n"
        "ds_write_b128 v11, v[20:23] offset:832\n"
        "v_cndmask_b32_dpp v101, v93, v101, vcc row_ror:4 row_mask:0xf bank_mask:0xf\n"
        "ds_write_b128 v11, v[108:111] offset:64\n"
        "ds_write_b128 v11, v[24:27] offset:896\n"
        "ds_write_b128 v11, v[96:99] offset:128\n"
        "ds_write_b128 v11, v[30:33] offset:960\n"
        "s_mov_b64 exec, s[46:47]\n"
        "ds_write_b32 v12, v101\n"
        "s_mov_b64 exec, -1\n"
        "s_mov_b64 exec, 2\n"
        "ds_inc_u32 v102, v0 offset:0\n"
        "s_mov_b64 exec, -1\n"
        "s_xor_b32 s40, s2, 0\n"
        "s_or_b32 s40, s18, s40\n"
        "s_cbranch_scc1 L31_%=\n"
        "L30_%=:\n"
        "s_mov_b32 exec_hi, 0\n"
        "v_mbcnt_lo_u32_b32 v20, -1, 0\n"
        "v_lshlrev_b32 v20, 7, v20\n"
        "v_mov_b32 v21, 1\n"
        "global_atomic_add v20, v21, s[24:25] offset:256\n"
        "s_mov_b32 exec_hi, -1\n"
        "s_mov_b64 exec, 1\n"
        "v_mov_b32 v20, 0\n"
        "v_mov_b32 v21, 0\n"
        "global_atomic_add v22, v20, v21, s[24:25] offset:0 sc0\n"
        "s_mov_b64 exec, -1\n"
        "s_waitcnt vmcnt(0)\n"
        "v_readfirstlane_b32 s40, v22\n"
        "s_nop 3\n"
        "s_cmp_ge_u32 s40, 512\n"
        "s_cbranch_scc1 L31_%=\n"
        "s_sleep 8\n"
        "s_sub_u32 s41, s41, 64\n"
        "s_cbranch_scc0 L30_%=\n"
        "L31_%=:\n"
        "s_endpgm\n"
        "L3_%=:\n"
        "s_nop 7\n"
        "L1_%=:\n"
        "ds_read_b32 v101, v102 offset:8\n"
        "s_waitcnt lgkmcnt(0)\n"
        "v_readfirstlane_b32 s40, v101\n"
        "s_nop 3\n"
        "s_cmp_ge_i32 s40, s68\n"
        "s_cbranch_scc1 L2_%=\n"
        "s_sleep 1\n"
        "s_sub_u32 s41, s41, 1\n"
        "s_cbranch_scc0 L1_%=\n"
        "s_endpgm\n"
        "L2_%=:\n"
        "s_nop 1\n"
        "s_branch L4_%=\n"
        "L8_%=:\n"
        "s_nop 7\n"
        "ds_read_u16 v11, v13 offset:19564\n"
        "ds_read_u16 v12, v13 offset:19566\n"
        "s_cmp_ge_u32 s30, 10\n"
        "s_cselect_b32 s69, 10, 0\n"
        "s_cmp_ge_u32 s30, 20\n"
        "s_cselect_b32 s69, 20, s69\n"
        "s_sub_u32 s69, s30, s69\n"
        "s_mul_i32 s69, s69, 832\n"
        "s_waitcnt lgkmcnt(0)\n"
        "v_add_u32 v11, s69, v11\n"
        "v_subrev_u32 v101, 8320, v11\n"
        "v_min_u32 v11, v11, v101\n"
        "v_add_u32 v11, 101856, v11\n"
        "v_add_u32 v12, s69, v12\n"
        "v_subrev_u32 v101, 8320, v12\n"
        "v_min_u32 v12, v12, v101\n"
        "v_add_u32 v12, 101856, v12\n"
        "s_mov_b32 s69, 21660\n"
        "v_writelane_b32 v102, s69, 1\n"
        "s_nop 1\n"
        "s_branch L9_%=\n"
        "L10_%=:\n"
        "s_nop 7\n"
        "v_mov_b32 v101, 21600\n"
        "L6_%=:\n"
        "ds_read_b32 v92, v101 offset:64\n"
        "s_waitcnt lgkmcnt(0)\n"
        "v_readfirstlane_b32 s40, v92\n"
        "s_nop 3\n"
        "s_cmp_ge_i32 s40, 1\n"
        "s_cbranch_scc1 L7_%=\n"
        "s_sleep 1\n"
        "s_sub_u32 s41, s41, 1\n"
        "s_cbranch_scc0 L6_%=\n"
        "s_endpgm\n"
        "L7_%=:\n"
        "v_mbcnt_lo_u32_b32 v92, -1, 0\n"
        "v_lshl_add_u32 v101, v92, 2, v101\n"
        "ds_read_b32 v92, v101 offset:68\n"
        "s_mov_b32 s14, s40\n"
        "s_mov_b32 s74, -1\n"
        "s_waitcnt lgkmcnt(0)\n"
        "v_readlane_b32 s22, v92, 0\n"
        "v_readlane_b32 s23, v92, 1\n"
        "s_nop 3\n"
        "s_nop 1\n"
        "s_branch L11_%=\n"
        "L12_%=:\n"
        "s_nop 7\n"
        "ds_read_b32 v15, v13 offset:19552\n"
        "ds_read_b32 v100, v13 offset:19556\n"
        "ds_read_b32 v103, v13 offset:19560\n"
        "s_mov_b32 s35, 6\n"
        "s_mov_b32 s38, 0\n"
        "s_mov_b32 s69, 1344\n"
        "s_waitcnt lgkmcnt(0)\n"
        "v_lshrrev_b32 v101, 28, v15\n"
        "v_add_u32 v101, s34, v101\n"
        "v_and_b32 v15, 0x0fffffff, v15\n"
        "v_mul_u32_u24 v92, 256, v101\n"
        "v_add_u32 v92, s16, v92\n"
        "v_cmpx_le_u32 vcc, s43, v101\n"
        "v_mov_b32 v92, s22\n"
        "s_mov_b64 exec, -1\n"
        "v_cmpx_lt_u32 vcc, s43, v101\n"
        "v_mov_b32 v92, s23\n"
        "s_mov_b64 exec, -1\n"
        "v_mad_u32_u24 v15, v92, s69, v15\n"
        "v_mul_u32_u24 v92, 57457, v92\n"
        "v_lshrrev_b32 v92, 22, v92\n"
        "v_mul_u32_u24 v92, 588672, v92\n"
        "v_add_u32 v15, v15, v92\n"
        "v_lshrrev_b32 v101, 28, v100\n"
        "v_add_u32 v101, s34, v101\n"
        "v_and_b32 v100, 0x0fffffff, v100\n"
        "v_mul_u32_u24 v92, 256, v101\n"
        "v_add_u32 v92, s16, v92\n"
        "v_cmpx_le_u32 vcc, s43, v101\n"
        "v_mov_b32 v92, s22\n"
        "s_mov_b64 exec, -1\n"
        "v_cmpx_lt_u32 vcc, s43, v101\n"
        "v_mov_b32 v92, s23\n"
        "s_mov_b64 exec, -1\n"
        "v_mad_u32_u24 v100, v92, s69, v100\n"
        "v_mul_u32_u24 v92, 57457, v92\n"
        "v_lshrrev_b32 v92, 22, v92\n"
        "v_mul_u32_u24 v92, 588672, v92\n"
        "v_add_u32 v100, v100, v92\n"
        "v_lshrrev_b32 v101, 28, v103\n"
        "v_add_u32 v101, s34, v101\n"
        "v_and_b32 v103, 0x0fffffff, v103\n"
        "v_mul_u32_u24 v92, 256, v101\n"
        "v_add_u32 v92, s16, v92\n"
        "v_cmpx_le_u32 vcc, s43, v101\n"
        "v_mov_b32 v92, s22\n"
        "s_mov_b64 exec, -1\n"
        "v_cmpx_lt_u32 vcc, s43, v101\n"
        "v_mov_b32 v92, s23\n"
        "s_mov_b64 exec, -1\n"
        "v_mad_u32_u24 v103, v92, s69, v103\n"
        "v_mul_u32_u24 v92, 57457, v92\n"
        "v_lshrrev_b32 v92, 22, v92\n"
        "v_mul_u32_u24 v92, 588672, v92\n"
        "v_add_u32 v103, v103, v92\n"
        "s_nop 4\n"
        "s_mov_b64 vcc, s[44:45]\n"
        "s_nop 1\n"
        "s_branch L13_%=\n"
        "L19_%=:\n"
        "s_nop 7\n"
        "v_readfirstlane_b32 s62, v14\n"
        "s_cmp_eq_u32 s73, 1\n"
        "s_cbranch_scc1 L14_%=\n"
        "s_cmp_ge_u32 s73, 2\n"
        "s_cbranch_scc1 L15_%=\n"
        "s_mov_b32 s27, s43\n"
        "s_cmp_lg_u32 s2, 0\n"
        "s_cbranch_scc1 L18_%=\n"
        "s_not_b32 s26, s26\n"
        "s_mov_b32 exec_hi, 0\n"
        "v_mbcnt_lo_u32_b32 v101, -1, 0\n"
        "v_lshlrev_b32 v101, 7, v101\n"
        "v_mov_b32 v92, 1\n"
        "global_atomic_add v101, v92, s[24:25] offset:256\n"
        "s_mov_b32 exec_hi, -1\n"
        "L18_%=:\n"
        "s_mov_b64 exec, 1\n"
        "v_bfe_u32 v101, s2, 3, 5\n"
        "v_lshlrev_b32 v101, 7, v101\n"
        "global_load_dword v14, v101, s[24:25] offset:256 sc1\n"
        "s_mov_b64 exec, -1\n"
        "s_branch L16_%=\n"
        "L14_%=:\n"
        "s_cmp_lg_u32 s62, s26\n"
        "s_cbranch_scc1 L17_%=\n"
        "s_sleep 8\n"
        "s_mov_b64 exec, 1\n"
        "v_bfe_u32 v101, s2, 3, 5\n"
        "v_lshlrev_b32 v101, 7, v101\n"
        "global_load_dword v14, v101, s[24:25] offset:256 sc1\n"
        "s_mov_b64 exec, -1\n"
        "s_waitcnt vmcnt(0)\n"
        "v_readfirstlane_b32 s62, v14\n"
        "s_sub_u32 s41, s41, 64\n"
        "s_cbranch_scc0 L14_%=\n"
        "s_endpgm\n"
        "L15_%=:\n"
        "s_cmp_lt_u32 s62, 210\n"
        "s_addc_u32 s27, s27, 0\n"
        "s_min_u32 s62, s62, 209\n"
        "s_add_u32 s62, s62, 5119\n"
        "s_lshl2_add_u32 s64, s73, 21600\n"
        "v_mov_b32 v101, s64\n"
        "v_mov_b32 v92, s62\n"
        "ds_write_b32 v101, v92 offset:60\n"
        "s_waitcnt lgkmcnt(0)\n"
        "s_cmp_lt_u32 s73, 3\n"
        "s_cbranch_scc1 L17_%=\n"
        "v_mov_b32 v92, s27\n"
        "ds_write_b32 v101, v92 offset:52\n"
        "s_waitcnt lgkmcnt(0)\n"
        "s_branch L16_%=\n"
        "L17_%=:\n"
        "s_mov_b64 exec, 1\n"
        "v_mov_b32 v101, 0\n"
        "v_mov_b32 v92, 1\n"
        "global_atomic_add v14, v101, v92, s[24:25] offset:0 sc0\n"
        "s_mov_b64 exec, -1\n"
        "L16_%=:\n"
        "s_nop 1\n"
        "s_branch L20_%=\n"
        "F8_%=:\n"
        "s_nop 7\n"
        "ds_read_u16 v11, v13 offset:19564\n"
        "ds_read_u16 v12, v13 offset:19566\n"
        "s_cmp_ge_u32 s30, 10\n"
        "s_cselect_b32 s69, 10, 0\n"
        "s_cmp_ge_u32 s30, 20\n"
        "s_cselect_b32 s69, 20, s69\n"
        "s_sub_u32 s69, s30, s69\n"
        "s_mul_i32 s69, s69, 832\n"
        "s_waitcnt lgkmcnt(0)\n"
        "v_add_u32 v11, s69, v11\n"
        "v_subrev_u32 v101, 8320, v11\n"
        "v_min_u32 v11, v11, v101\n"
        "v_add_u32 v11, 101856, v11\n"
        "v_add_u32 v12, s69, v12\n"
        "v_subrev_u32 v101, 8320, v12\n"
        "v_min_u32 v12, v12, v101\n"
        "v_add_u32 v12, 101856, v12\n"
        "s_mov_b32 s69, 21660\n"
        "v_writelane_b32 v102, s69, 1\n"
        "s_nop 1\n"
        "s_branch F9_%=\n"
        "F10_%=:\n"
        "s_nop 7\n"
        "v_mov_b32 v101, 21600\n"
        "F6_%=:\n"
        "ds_read_b32 v92, v101 offset:64\n"
        "s_waitcnt lgkmcnt(0)\n"
        "v_readfirstlane_b32 s40, v92\n"
        "s_nop 3\n"
        "s_cmp_ge_i32 s40, 1\n"
        "s_cbranch_scc1 F7_%=\n"
        "s_sleep 1\n"
        "s_sub_u32 s41, s41, 1\n"
        "s_cbranch_scc0 F6_%=\n"
        "s_endpgm\n"
        "F7_%=:\n"
        "v_mbcnt_lo_u32_b32 v92, -1, 0\n"
        "v_lshl_add_u32 v101, v92, 2, v101\n"
        "ds_read_b32 v92, v101 offset:68\n"
        "s_mov_b32 s14, s40\n"
        "s_mov_b32 s74, -1\n"
        "s_waitcnt lgkmcnt(0)\n"
        "v_readlane_b32 s22, v92, 0\n"
        "v_readlane_b32 s23, v92, 1\n"
        "s_nop 3\n"
        "s_nop 1\n"
        "s_branch F11_%=\n"
        "F12_%=:\n"
        "s_nop 7\n"
        "ds_read_b32 v15, v13 offset:19552\n"
        "ds_read_b32 v100, v13 offset:19556\n"
        "ds_read_b32 v103, v13 offset:19560\n"
        "s_mov_b32 s35, 6\n"
        "s_mov_b32 s38, 0\n"
        "s_mov_b32 s69, 1344\n"
        "s_waitcnt lgkmcnt(0)\n"
        "v_lshrrev_b32 v101, 28, v15\n"
        "v_add_u32 v101, s34, v101\n"
        "v_and_b32 v15, 0x0fffffff, v15\n"
        "v_mul_u32_u24 v92, 256, v101\n"
        "v_add_u32 v92, s16, v92\n"
        "v_cmpx_le_u32 vcc, s43, v101\n"
        "v_mov_b32 v92, s22\n"
        "s_mov_b64 exec, -1\n"
        "v_cmpx_lt_u32 vcc, s43, v101\n"
        "v_mov_b32 v92, s23\n"
        "s_mov_b64 exec, -1\n"
        "v_mad_u32_u24 v15, v92, s69, v15\n"
        "v_mul_u32_u24 v92, 57457, v92\n"
        "v_lshrrev_b32 v92, 22, v92\n"
        "v_mul_u32_u24 v92, 588672, v92\n"
        "v_add_u32 v15, v15, v92\n"
        "v_lshrrev_b32 v101, 28, v100\n"
        "v_add_u32 v101, s34, v101\n"
        "v_and_b32 v100, 0x0fffffff, v100\n"
        "v_mul_u32_u24 v92, 256, v101\n"
        "v_add_u32 v92, s16, v92\n"
        "v_cmpx_le_u32 vcc, s43, v101\n"
        "v_mov_b32 v92, s22\n"
        "s_mov_b64 exec, -1\n"
        "v_cmpx_lt_u32 vcc, s43, v101\n"
        "v_mov_b32 v92, s23\n"
        "s_mov_b64 exec, -1\n"
        "v_mad_u32_u24 v100, v92, s69, v100\n"
        "v_mul_u32_u24 v92, 57457, v92\n"
        "v_lshrrev_b32 v92, 22, v92\n"
        "v_mul_u32_u24 v92, 588672, v92\n"
        "v_add_u32 v100, v100, v92\n"
        "v_lshrrev_b32 v101, 28, v103\n"
        "v_add_u32 v101, s34, v101\n"
        "v_and_b32 v103, 0x0fffffff, v103\n"
        "v_mul_u32_u24 v92, 256, v101\n"
        "v_add_u32 v92, s16, v92\n"
        "v_cmpx_le_u32 vcc, s43, v101\n"
        "v_mov_b32 v92, s22\n"
        "s_mov_b64 exec, -1\n"
        "v_cmpx_lt_u32 vcc, s43, v101\n"
        "v_mov_b32 v92, s23\n"
        "s_mov_b64 exec, -1\n"
        "v_mad_u32_u24 v103, v92, s69, v103\n"
        "v_mul_u32_u24 v92, 57457, v92\n"
        "v_lshrrev_b32 v92, 22, v92\n"
        "v_mul_u32_u24 v92, 588672, v92\n"
        "v_add_u32 v103, v103, v92\n"
        "s_nop 4\n"
        "s_mov_b64 vcc, s[44:45]\n"
        "s_nop 1\n"
        "s_branch F13_%=\n"
        "F19_%=:\n"
        "s_nop 7\n"
        "v_readfirstlane_b32 s62, v14\n"
        "s_cmp_eq_u32 s73, 1\n"
        "s_cbranch_scc1 F14_%=\n"
        "s_cmp_ge_u32 s73, 2\n"
        "s_cbranch_scc1 F15_%=\n"
        "s_mov_b32 s27, s43\n"
        "s_cmp_lg_u32 s2, 0\n"
        "s_cbranch_scc1 F18_%=\n"
        "s_not_b32 s26, s26\n"
        "s_mov_b32 exec_hi, 0\n"
        "v_mbcnt_lo_u32_b32 v101, -1, 0\n"
        "v_lshlrev_b32 v101, 7, v101\n"
        "v_mov_b32 v92, 1\n"
        "global_atomic_add v101, v92, s[24:25] offset:256\n"
        "s_mov_b32 exec_hi, -1\n"
        "F18_%=:\n"
        "s_mov_b64 exec, 1\n"
        "v_bfe_u32 v101, s2, 3, 5\n"
        "v_lshlrev_b32 v101, 7, v101\n"
        "global_load_dword v14, v101, s[24:25] offset:256 sc1\n"
        "s_mov_b64 exec, -1\n"
        "s_branch F16_%=\n"
        "F14_%=:\n"
        "s_cmp_lg_u32 s62, s26\n"
        "s_cbranch_scc1 F17_%=\n"
        "s_sleep 8\n"
        "s_mov_b64 exec, 1\n"
        "v_bfe_u32 v101, s2, 3, 5\n"
        "v_lshlrev_b32 v101, 7, v101\n"
        "global_load_dword v14, v101, s[24:25] offset:256 sc1\n"
        "s_mov_b64 exec, -1\n"
        "s_waitcnt vmcnt(0)\n"
        "v_readfirstlane_b32 s62, v14\n"
        "s_sub_u32 s41, s41, 64\n"
        "s_cbranch_scc0 F14_%=\n"
        "s_endpgm\n"
        "F15_%=:\n"
        "s_cmp_lt_u32 s62, 210\n"
        "s_addc_u32 s27, s27, 0\n"
        "s_min_u32 s62, s62, 209\n"
        "s_add_u32 s62, s62, 5119\n"
        "s_lshl2_add_u32 s64, s73, 21600\n"
        "v_mov_b32 v101, s64\n"
        "v_mov_b32 v92, s62\n"
        "ds_write_b32 v101, v92 offset:60\n"
        "s_waitcnt lgkmcnt(0)\n"
        "s_cmp_lt_u32 s73, 3\n"
        "s_cbranch_scc1 F17_%=\n"
        "v_mov_b32 v92, s27\n"
        "ds_write_b32 v101, v92 offset:52\n"
        "s_waitcnt lgkmcnt(0)\n"
        "s_branch F16_%=\n"
        "F17_%=:\n"
        "s_mov_b64 exec, 1\n"
        "v_mov_b32 v101, 0\n"
        "v_mov_b32 v92, 1\n"
        "global_atomic_add v14, v101, v92, s[24:25] offset:0 sc0\n"
        "s_mov_b64 exec, -1\n"
        "F16_%=:\n"
        "s_nop 1\n"
        "s_branch F20_%=\n"
        "CONS_%=:\n"
        "s_mov_b32 s41, 0x400000\n"
        "s_mov_b32 s48, 0xffffffff\n"
        "s_mov_b32 s49, 0xffff0000\n"
        "s_mov_b32 s50, 0x33333333\n"
        "s_mov_b32 s51, 0x33333333\n"
        "s_mov_b32 s52, 0x3c2aaaab\n"
        "s_mov_b32 s53, 0x3a7ba882\n"
        "s_mov_b32 s56, 0x0\n"
        "s_mov_b32 s57, 0xfeff0000\n"
        "s_mov_b32 s54, 0x11111111\n"
        "s_mov_b32 s55, 0x11111111\n"
        "s_mov_b32 s63, 0x07060302\n"
        "s_mov_b64 s[44:45], s[12:13]\n"
        "s_and_b32 s45, s45, 0xffff\n"
        "s_mov_b32 s47, 0x20000\n"
        "s_mov_b32 s70, 832\n"
        "s_mov_b32 s71, 17\n"
        "s_sub_u32 s69, s18, 7488\n"
        "s_mov_b32 s28, 0xffff0000\n"
        "s_mov_b32 s31, 0\n"
        "s_add_u32 s35, s15, s17\n"
        "s_mov_b32 s36, 0x20000000\n"
        "L58_%=:\n"
        "ds_read_b32 v251, v250 offset:52\n"
        "s_waitcnt lgkmcnt(0)\n"
        "v_readfirstlane_b32 s40, v251\n"
        "s_nop 3\n"
        "s_cmp_ge_i32 s40, 4\n"
        "s_cbranch_scc1 L59_%=\n"
        "s_nop 0\n"
        "s_sub_u32 s41, s41, 1\n"
        "s_cbranch_scc0 L58_%=\n"
        "s_endpgm\n"
        "L59_%=:\n"
        "s_waitcnt vmcnt(0)\n"
        "s_setprio 2\n"
        "v_add_u32 v24, 2, v1\n"
        "v_mov_b32 v25, 0x3f80\n"
        "s_mov_b32 exec_lo, 0\n"
        "s_mov_b32 exec_hi, 0x1000000\n"
        "ds_write_b16 v24, v25\n"
        "s_mov_b64 exec, -1\n"
        "v_add_u32 v24, 61792, v0\n"
        "ds_read_b128 v[168:171], v24 offset:0\n"
        "ds_read_b128 v[172:175], v24 offset:1024\n"
        "ds_read_b128 v[176:179], v24 offset:2048\n"
        "ds_read_b128 v[180:183], v24 offset:3088\n"
        "ds_read_b128 v[184:187], v24 offset:4112\n"
        "ds_read_b128 v[188:191], v24 offset:5136\n"
        "ds_read_b128 v[192:195], v24 offset:6176\n"
        "ds_read_b128 v[196:199], v24 offset:7200\n"
        "ds_read_b128 v[200:203], v24 offset:8224\n"
        "ds_read_b128 v[204:207], v24 offset:9264\n"
        "ds_read_b128 v[208:211], v24 offset:10288\n"
        "ds_read_b128 v[212:215], v24 offset:11312\n"
        "ds_read_b128 v[216:219], v24 offset:12352\n"
        "ds_read_b128 v[220:223], v24 offset:13376\n"
        "ds_read_b128 v[224:227], v24 offset:14400\n"
        "ds_read_b128 v[228:231], v24 offset:15440\n"
        "ds_read_b128 v[232:235], v24 offset:16464\n"
        "ds_read_b128 v[236:239], v24 offset:17488\n"
        "ds_read_b128 v[240:243], v0 offset:0\n"
        "ds_read_b128 v[244:247], v0 offset:1024\n"
        "ds_read_b128 v[248:251], v0 offset:2048\n"
        "ds_read_b128 v[252:255], v0 offset:3088\n"
        "v_mov_b32 v25, 21600\n"
        "s_waitcnt lgkmcnt(0)\n"
        "s_mov_b64 exec, 1\n"
        "ds_inc_u32 v25, v1 offset:12\n"
        "s_mov_b64 exec, -1\n"
        "s_call_b64 s[64:65], CGRQ_%=\n"
        "s_setprio 0\n"
        "L60_%=:\n"
        "ds_read_b32 v24, v0 offset:21600\n"
        "s_waitcnt lgkmcnt(0)\n"
        "v_readfirstlane_b32 s40, v24\n"
        "s_nop 3\n"
        "s_cmp_ge_i32 s40, 6\n"
        "s_cbranch_scc1 L61_%=\n"
        "s_nop 0\n"
        "s_sub_u32 s41, s41, 1\n"
        "s_cbranch_scc0 L60_%=\n"
        "s_endpgm\n"
        "L61_%=:\n"
        "ds_read_b128 v[124:127], v7 offset:0\n"
        "ds_read_b128 v[132:135], v7 offset:3328\n"
        "ds_read_b128 v[128:131], v7 offset:64\n"
        "ds_read_b128 v[136:139], v7 offset:3392\n"
        "ds_read_b128 v[160:163], v7 offset:128\n"
        "ds_read_b128 v[52:55], v7 offset:3456\n"
        "s_waitcnt lgkmcnt(0)\n"
        "s_branch CTOP_%=\n"
        ".p2align 6\n"
        "s_nop 0\n"
        "s_nop 0\n"
        "s_nop 0\n"
        "s_nop 0\n"
        "s_nop 0\n"
        "s_nop 0\n"
        "s_nop 0\n"
        "s_nop 0\n"
        "s_nop 0\n"
        "s_nop 0\n"
        "s_nop 0\n"
        "s_nop 0\n"
        "s_nop 0\n"
        "s_nop 0\n"
        "s_nop 0\n"
        "CFULL_%=:\n"
        "s_call_b64 s[64:65], CGRP_%=\n"
        "CTOP_%=:\n"
        "s_waitcnt lgkmcnt(5)\n"
        "v_mfma_f32_16x16x32_bf16 v[24:27], v[168:171], v[124:127], 0\n"
        "v_cvt_pk_bf16_f32 v156, v152, v56\n"
        "v_cvt_pk_bf16_f32 v157, v72, v20\n"
        "s_waitcnt lgkmcnt(3)\n"
        "v_mfma_f32_16x16x32_bf16 v[24:27], v[172:175], v[128:131], v[24:27]\n"
        "ds_read_b128 v[44:47], v16 offset:128\n"
        "v_cvt_pk_bf16_f32 v158, v84, v60\n"
        "s_waitcnt lgkmcnt(2)\n"
        "v_mfma_f32_16x16x32_bf16 v[24:27], v[176:179], v[160:163], v[24:27]\n"
        "ds_read_b128 v[48:51], v5\n"
        "v_cvt_pk_bf16_f32 v164, v153, v57\n"
        "v_mfma_f32_16x16x32_bf16 v[32:35], v[188:191], v[160:163], 0\n"
        "v_cvt_pk_bf16_f32 v165, v73, v21\n"
        "v_cvt_pk_bf16_f32 v166, v85, v61\n"
        "v_mfma_f32_16x16x32_bf16 v[32:35], v[184:187], v[128:131], v[32:35]\n"
        "v_cvt_pk_bf16_f32 v146, v154, v58\n"
        "v_cvt_pk_bf16_f32 v147, v74, v22\n"
        "v_mfma_f32_16x16x32_bf16 v[32:35], v[180:183], v[124:127], v[32:35]\n"
        "s_add_u32 s37, s36, 196224\n"
        "ds_read_b128 v[92:95], v7 offset:6656\n"
        "ds_read_b128 v[96:99], v7 offset:6720\n"
        "ds_read_b128 v[116:119], v7 offset:6784\n"
        "v_mfma_f32_16x16x32_bf16 v[76:79], v[192:195], v[124:127], 0\n"
        "v_perm_b32 v80, v25, v24, s63\n"
        "v_perm_b32 v81, v27, v26, s63\n"
        "v_mfma_f32_16x16x32_bf16 v[76:79], v[196:199], v[128:131], v[76:79]\n"
        "v_perm_b32 v82, v33, v32, s63\n"
        "v_perm_b32 v83, v35, v34, s63\n"
        "v_mfma_f32_16x16x32_bf16 v[76:79], v[200:203], v[160:163], v[76:79]\n"
        "buffer_store_dwordx3 v[156:158], v12, s[8:11], s37 offen offset:384 sc0 sc1 nt\n"
        "v_cvt_pk_bf16_f32 v148, v86, v62\n"
        "v_cvt_pk_bf16_f32 v150, v155, v59\n"
        "v_cvt_pk_bf16_f32 v151, v75, v23\n"
        "v_cvt_pk_bf16_f32 v152, v87, v63\n"
        "s_waitcnt lgkmcnt(3)\n"
        "v_mfma_f32_16x16x32_bf16 v[32:35], v[44:47], v[160:163], 0\n"
        "v_cvt_pk_bf16_f32 v72, v68, v36\n"
        "v_perm_b32 v84, v77, v76, s63\n"
        "v_smfmac_f32_16x16x64_bf16 v[32:35], v[48:51], v[124:131], v4\n"
        "s_add_u32 s37, s36, 196224\n"
        "v_perm_b32 v85, v79, v78, s63\n"
        "v_mfma_f32_16x16x32_bf16 v[24:27], v[44:47], v[52:55], 0\n"
        "v_cvt_pk_bf16_f32 v73, v40, v64\n"
        "v_cvt_pk_bf16_f32 v74, v104, v88\n"
        "v_smfmac_f32_16x16x64_bf16 v[24:27], v[48:51], v[132:139], v4\n"
        "s_waitcnt lgkmcnt(0)\n"
        "s_setprio 2\n"
        "v_cvt_pk_bf16_f32 v156, v69, v37\n"
        "v_cvt_pk_bf16_f32 v157, v41, v65\n"
        "v_mfma_f32_16x16x32_bf16 v[100:103], v[44:47], v[116:119], 0\n"
        "v_cvt_pk_bf16_f32 v158, v105, v89\n"
        "v_cvt_pk_bf16_f32 v76, v70, v38\n"
        "v_smfmac_f32_16x16x64_bf16 v[100:103], v[48:51], v[92:99], v4\n"
        "buffer_store_dwordx3 v[164:166], v13, s[8:11], s37 offen offset:1152 sc0 sc1 nt\n"
        "v_cvt_pk_bf16_f32 v77, v42, v66\n"
        "v_cvt_pk_bf16_f32 v78, v106, v90\n"
        "v_cvt_pk_bf16_f32 v88, v71, v39\n"
        "v_cvt_pk_bf16_f32 v89, v43, v67\n"
        "v_cvt_pk_bf16_f32 v90, v107, v91\n"
        "v_mfma_f32_16x16x32_bf16 v[20:23], v[212:215], v[160:163], 0\n"
        "s_add_u32 s37, s36, 294336\n"
        "ds_read_b64 v[154:155], v11 offset:192\n"
        "v_mfma_f32_16x16x32_bf16 v[20:23], v[208:211], v[128:131], v[20:23]\n"
        "ds_read_b32 v153, v10\n"
        "v_mfma_f32_16x16x32_bf16 v[20:23], v[204:207], v[124:127], v[20:23]\n"
        "v_mfma_f32_16x16x32_bf16 v[28:31], v[216:219], v[124:127], 0\n"
        "v_mfma_f32_16x16x32_bf16 v[28:31], v[220:223], v[128:131], v[28:31]\n"
        "v_mfma_f32_16x16x32_bf16 v[28:31], v[224:227], v[160:163], v[28:31]\n"
        "buffer_store_dwordx3 v[146:148], v12, s[8:11], s37 offen offset:576 sc0 sc1 nt\n"
        "s_add_u32 s37, s36, 392448\n"
        "s_waitcnt lgkmcnt(1)\n"
        "v_add_f32_dpp v155, v34, v35 quad_perm:[0,1,2,3] row_mask:0x1 bank_mask:0xf\n"
        "v_add_f32_dpp v154, v32, v33 quad_perm:[0,1,2,3] row_mask:0x1 bank_mask:0xf\n"
        "v_perm_b32 v86, v21, v20, s63\n"
        "v_mfma_f32_16x16x32_bf16 v[44:47], v[236:239], v[160:163], 0\n"
        "v_perm_b32 v87, v23, v22, s63\n"
        "v_perm_b32 v20, v29, v28, s63\n"
        "v_mfma_f32_16x16x32_bf16 v[44:47], v[232:235], v[128:131], v[44:47]\n"
        "v_perm_b32 v21, v31, v30, s63\n"
        "v_add_f32_dpp v155, v26, v27 quad_perm:[0,1,2,3] row_mask:0x2 bank_mask:0xf\n"
        "v_mfma_f32_16x16x32_bf16 v[44:47], v[228:231], v[124:127], v[44:47]\n"
        "buffer_store_dwordx3 v[150:152], v12, s[8:11], s37 offen sc0 sc1 nt\n"
        "v_add_f32_dpp v154, v24, v25 quad_perm:[0,1,2,3] row_mask:0x2 bank_mask:0xf\n"
        "v_add_f32_dpp v155, v102, v103 quad_perm:[0,1,2,3] row_mask:0x4 bank_mask:0xf\n"
        "v_mul_f32 v165, s52, v155\n"
        "v_add_f32_dpp v154, v100, v101 quad_perm:[0,1,2,3] row_mask:0x4 bank_mask:0xf\n"
        "s_waitcnt lgkmcnt(0)\n"
        "v_fma_f32 v165, -v165, v155, v153\n"
        "v_add_f32_e64 v165, s53, |v165|\n"
        "v_perm_b32 v23, v47, v46, s63\n"
        "v_perm_b32 v22, v45, v44, s63\n"
        "v_mfma_f32_16x16x32_bf16 v[100:103], v[124:127], v[80:83], 0\n"
        "v_rsq_f32 v153, v165\n"
        "v_mfma_f32_16x16x32_bf16 v[100:103], v[128:131], v[84:87], v[100:103]\n"
        "s_add_u32 s37, s36, 392448\n"
        "ds_read_b128 v[104:107], v8 offset:128\n"
        "v_and_b32 v56, s28, v153\n"
        "v_mfma_f32_16x16x32_bf16 v[100:103], v[160:163], v[20:23], v[100:103]\n"
        "ds_read2_b32 v[108:109], v6 offset0:0 offset1:2\n"
        "ds_read2_b32 v[110:111], v6 offset0:8 offset1:10\n"
        "v_mfma_f32_16x16x32_bf16 v[144:147], v[52:55], v[20:23], 0\n"
        "v_and_b32 v164, s28, v153\n"
        "v_sub_f32 v167, v153, v56\n"
        "v_mfma_f32_16x16x32_bf16 v[144:147], v[136:139], v[84:87], v[144:147]\n"
        "v_cvt_pk_bf16_f32 v57, v167, v56\n"
        "v_cvt_pk_bf16_f32 v165, v164, v167\n"
        "v_mfma_f32_16x16x32_bf16 v[144:147], v[132:135], v[80:83], v[144:147]\n"
        "buffer_store_dwordx3 v[72:74], v15, s[8:11], s37 offen offset:768 sc0 sc1 nt\n"
        "v_mul_f32 v154, v154, v153\n"
        "v_readlane_b32 s58, v153, 56\n"
        "s_mov_b64 exec, s[56:57]\n"
        "v_mov_b32 v154, 0xff800000\n"
        "s_mov_b64 exec, -1\n"
        "v_mfma_f32_16x16x4_4b_bf16 v[32:47], v[56:57], v[164:165], 0 blgp:4\n"
        "v_mul_f32 v154, s58, v154\n"
        "v_exp_f32 v154, v154\n"
        "v_mfma_f32_16x16x32_bf16 v[24:27], v[92:95], v[80:83], 0\n"
        "s_add_u32 s37, s36, 490560\n"
        "v_add_f32_dpp v167, v154, v154 row_ror:8 row_mask:0xf bank_mask:0xf\n"
        "v_mul_f32 v154, v154, v153\n"
        "v_mfma_f32_16x16x32_bf16 v[24:27], v[96:99], v[84:87], v[24:27]\n"
        "v_add_f32_dpp v167, v167, v167 row_ror:4 row_mask:0xf bank_mask:0xf\n"
        "v_mfma_f32_16x16x32_bf16 v[24:27], v[116:119], v[20:23], v[24:27]\n"
        "s_waitcnt lgkmcnt(0)\n"
        "v_add_f32_dpp v167, v167, v167 row_ror:2 row_mask:0xf bank_mask:0xf\n"
        "v_mfma_f32_16x16x32_bf16 v[148:151], v[104:107], v[20:23], 0\n"
        "v_mul_f32 v100, v100, v32\n"
        "v_mul_f32 v101, v101, v33\n"
        "v_smfmac_f32_16x16x64_bf16 v[148:151], v[108:111], v[80:87], v4\n"
        "buffer_store_dwordx3 v[156:158], v12, s[8:11], s37 offen offset:192 sc0 sc1 nt\n"
        "v_mul_f32 v102, v102, v34\n"
        "v_mul_f32 v103, v103, v35\n"
        "v_exp_f32 v100, v100\n"
        "v_mul_f32 v144, v144, v36\n"
        "v_mfma_f32_16x16x32_bf16 v[20:23], v[168:171], v[132:135], 0\n"
        "s_add_u32 s37, s36, 490560\n"
        "v_exp_f32 v101, v101\n"
        "v_mfma_f32_16x16x32_bf16 v[20:23], v[172:175], v[136:139], v[20:23]\n"
        "v_exp_f32 v102, v102\n"
        "v_mfma_f32_16x16x32_bf16 v[20:23], v[176:179], v[52:55], v[20:23]\n"
        "v_exp_f32 v103, v103\n"
        "v_add_f32 v151, v100, v101\n"
        "v_mfma_f32_16x16x32_bf16 v[28:31], v[188:191], v[52:55], 0\n"
        "v_add_f32 v151, v151, v102\n"
        "v_add_f32 v151, v151, v103\n"
        "v_mfma_f32_16x16x32_bf16 v[28:31], v[184:187], v[136:139], v[28:31]\n"
        "v_mul_f32 v145, v145, v37\n"
        "v_mul_f32 v146, v146, v38\n"
        "v_mfma_f32_16x16x32_bf16 v[28:31], v[180:183], v[132:135], v[28:31]\n"
        "buffer_store_dwordx3 v[76:78], v14, s[8:11], s37 offen offset:960 sc0 sc1 nt\n"
        "v_perm_b32 v68, v21, v20, s63\n"
        "v_perm_b32 v69, v23, v22, s63\n"
        "v_mul_f32 v147, v147, v39\n"
        "v_exp_f32 v144, v144\n"
        "v_mfma_f32_16x16x32_bf16 v[20:23], v[192:195], v[132:135], 0\n"
        "s_add_u32 s37, s36, 588672\n"
        "v_exp_f32 v145, v145\n"
        "v_mfma_f32_16x16x32_bf16 v[20:23], v[196:199], v[136:139], v[20:23]\n"
        "v_perm_b32 v70, v29, v28, s63\n"
        "v_perm_b32 v71, v31, v30, s63\n"
        "v_mfma_f32_16x16x32_bf16 v[20:23], v[200:203], v[52:55], v[20:23]\n"
        "buffer_store_dwordx3 v[88:90], v12, s[8:11], s37 offen offset:384 sc0 sc1 nt\n"
        "v_exp_f32 v146, v146\n"
        "v_exp_f32 v147, v147\n"
        "v_add_f32 v151, v151, v144\n"
        "v_add_f32 v151, v151, v145\n"
        "v_mfma_f32_16x16x32_bf16 v[28:31], v[212:215], v[52:55], 0\n"
        "v_add_f32 v151, v151, v146\n"
        "v_add_f32 v151, v151, v147\n"
        "v_mfma_f32_16x16x32_bf16 v[28:31], v[208:211], v[136:139], v[28:31]\n"
        "v_perm_b32 v72, v21, v20, s63\n"
        "v_perm_b32 v73, v23, v22, s63\n"
        "v_mfma_f32_16x16x32_bf16 v[28:31], v[204:207], v[132:135], v[28:31]\n"
        "v_mul_f32 v24, v24, v40\n"
        "v_mul_f32 v25, v25, v41\n"
        "v_mul_f32 v26, v26, v42\n"
        "v_mfma_f32_16x16x32_bf16 v[20:23], v[216:219], v[132:135], 0\n"
        "v_mul_f32 v27, v27, v43\n"
        "v_add_f32 v148, v148, v149\n"
        "v_mfma_f32_16x16x32_bf16 v[20:23], v[220:223], v[136:139], v[20:23]\n"
        "v_exp_f32 v24, v24\n"
        "v_mfma_f32_16x16x32_bf16 v[20:23], v[224:227], v[52:55], v[20:23]\n"
        "v_perm_b32 v74, v29, v28, s63\n"
        "v_perm_b32 v75, v31, v30, s63\n"
        "v_add_f32 v151, v151, v24\n"
        "v_mfma_f32_16x16x32_bf16 v[64:67], v[236:239], v[52:55], 0\n"
        "v_exp_f32 v25, v25\n"
        "v_mfma_f32_16x16x32_bf16 v[64:67], v[232:235], v[136:139], v[64:67]\n"
        "v_exp_f32 v26, v26\n"
        "v_mfma_f32_16x16x32_bf16 v[64:67], v[228:231], v[132:135], v[64:67]\n"
        "v_perm_b32 v48, v21, v20, s63\n"
        "v_perm_b32 v49, v23, v22, s63\n"
        "v_mfma_f32_16x16x4_4b_bf16 v[28:43], v[56:57], v[164:165], 0 blgp:5\n"
        "v_exp_f32 v27, v27\n"
        "v_add_f32 v151, v151, v25\n"
        "v_add_f32 v151, v151, v26\n"
        "v_mul_f32 v148, v148, v44\n"
        "v_exp_f32 v148, v148\n"
        "v_perm_b32 v50, v65, v64, s63\n"
        "v_perm_b32 v51, v67, v66, s63\n"
        "v_mfma_f32_16x16x32_bf16 v[58:61], v[124:127], v[68:71], 0\n"
        "v_add_f32 v151, v151, v27\n"
        "v_add_f32_dpp v167, v167, v167 row_ror:1 row_mask:0xf bank_mask:0xf\n"
        "v_mfma_f32_16x16x32_bf16 v[58:61], v[128:131], v[72:75], v[58:61]\n"
        "v_add_f32_dpp v151, v148, v151 quad_perm:[0,1,2,3] row_mask:0x4 bank_mask:0xf\n"
        "v_mfma_f32_16x16x32_bf16 v[58:61], v[160:163], v[48:51], v[58:61]\n"
        "v_mfma_f32_16x16x32_bf16 v[112:115], v[52:55], v[48:51], 0\n"
        "v_permlane32_swap_b32 v151, v167\n"
        "v_mfma_f32_16x16x32_bf16 v[112:115], v[136:139], v[72:75], v[112:115]\n"
        "v_add_f32 v151, v151, v167\n"
        "v_mov_b32 v167, v151\n"
        "v_mfma_f32_16x16x32_bf16 v[112:115], v[132:135], v[68:71], v[112:115]\n"
        "v_mfma_f32_16x16x32_bf16 v[20:23], v[92:95], v[68:71], 0\n"
        "v_permlane16_swap_b32 v151, v167\n"
        "v_mfma_f32_16x16x32_bf16 v[20:23], v[96:99], v[72:75], v[20:23]\n"
        "v_add_f32 v151, v151, v167\n"
        "v_mul_f32 v58, v58, v28\n"
        "v_mfma_f32_16x16x32_bf16 v[20:23], v[116:119], v[48:51], v[20:23]\n"
        "v_rcp_f32 v123, v151\n"
        "v_mul_f32 v59, v59, v29\n"
        "v_and_b32 v158, s28, v123\n"
        "v_mfma_f32_16x16x32_bf16 v[62:65], v[104:107], v[48:51], 0\n"
        "v_sub_f32 v152, v123, v158\n"
        "v_cvt_pk_bf16_f32 v159, v158, v152\n"
        "v_smfmac_f32_16x16x64_bf16 v[62:65], v[108:111], v[68:75], v4\n"
        "v_min3_f32 v167, v151, v123, v153\n"
        "v_mfma_f32_16x16x32_bf16 v[48:51], v[168:171], v[92:95], 0\n"
        "v_mul_f32 v60, v60, v30\n"
        "v_mul_f32 v61, v61, v31\n"
        "v_mfma_f32_16x16x32_bf16 v[48:51], v[172:175], v[96:99], v[48:51]\n"
        "v_exp_f32 v58, v58\n"
        "v_mfma_f32_16x16x32_bf16 v[48:51], v[176:179], v[116:119], v[48:51]\n"
        "v_exp_f32 v59, v59\n"
        "v_exp_f32 v60, v60\n"
        "v_mfma_f32_16x16x32_bf16 v[68:71], v[188:191], v[116:119], 0\n"
        "v_exp_f32 v61, v61\n"
        "v_mfma_f32_16x16x32_bf16 v[68:71], v[184:187], v[96:99], v[68:71]\n"
        "v_add_f32 v155, v58, v59\n"
        "v_add_f32 v155, v155, v60\n"
        "v_mfma_f32_16x16x32_bf16 v[68:71], v[180:183], v[92:95], v[68:71]\n"
        "v_perm_b32 v44, v49, v48, s63\n"
        "v_perm_b32 v45, v51, v50, s63\n"
        "v_add_f32 v155, v155, v61\n"
        "v_mfma_f32_16x16x32_bf16 v[28:31], v[192:195], v[92:95], 0\n"
        "v_mul_f32 v112, v112, v32\n"
        "v_mul_f32 v113, v113, v33\n"
        "v_mfma_f32_16x16x32_bf16 v[28:31], v[196:199], v[96:99], v[28:31]\n"
        "v_mul_f32 v114, v114, v34\n"
        "v_perm_b32 v46, v69, v68, s63\n"
        "v_mfma_f32_16x16x32_bf16 v[28:31], v[200:203], v[116:119], v[28:31]\n"
        "v_perm_b32 v47, v71, v70, s63\n"
        "v_mul_f32 v115, v115, v35\n"
        "v_exp_f32 v112, v112\n"
        "v_mfma_f32_16x16x32_bf16 v[68:71], v[212:215], v[116:119], 0\n"
        "v_exp_f32 v113, v113\n"
        "v_mfma_f32_16x16x32_bf16 v[68:71], v[208:211], v[96:99], v[68:71]\n"
        "v_exp_f32 v114, v114\n"
        "v_mfma_f32_16x16x32_bf16 v[68:71], v[204:207], v[92:95], v[68:71]\n"
        "v_perm_b32 v48, v29, v28, s63\n"
        "v_perm_b32 v49, v31, v30, s63\n"
        "v_add_f32 v155, v155, v112\n"
        "v_mfma_f32_16x16x32_bf16 v[64:67], v[216:219], v[92:95], 0\n"
        "v_exp_f32 v115, v115\n"
        "v_mfma_f32_16x16x32_bf16 v[64:67], v[220:223], v[96:99], v[64:67]\n"
        "v_add_f32 v155, v155, v113\n"
        "v_add_f32 v155, v155, v114\n"
        "v_mfma_f32_16x16x32_bf16 v[64:67], v[224:227], v[116:119], v[64:67]\n"
        "v_perm_b32 v50, v69, v68, s63\n"
        "v_perm_b32 v51, v71, v70, s63\n"
        "v_add_f32 v155, v155, v115\n"
        "v_mul_f32 v20, v20, v36\n"
        "v_mfma_f32_16x16x32_bf16 v[28:31], v[236:239], v[116:119], 0\n"
        "v_mul_f32 v21, v21, v37\n"
        "v_mul_f32 v22, v22, v38\n"
        "v_mfma_f32_16x16x32_bf16 v[28:31], v[232:235], v[96:99], v[28:31]\n"
        "v_perm_b32 v150, v65, v64, s63\n"
        "v_perm_b32 v151, v67, v66, s63\n"
        "v_mfma_f32_16x16x32_bf16 v[28:31], v[228:231], v[92:95], v[28:31]\n"
        "v_exp_f32 v20, v20\n"
        "v_mfma_f32_16x16x4_4b_bf16 v[76:91], v[56:57], v[158:159], 0 blgp:4\n"
        "v_exp_f32 v21, v21\n"
        "v_mul_f32 v23, v23, v39\n"
        "v_exp_f32 v22, v22\n"
        "v_exp_f32 v23, v23\n"
        "v_add_f32 v155, v155, v20\n"
        "v_add_f32 v155, v155, v21\n"
        "v_perm_b32 v152, v29, v28, s63\n"
        "v_perm_b32 v153, v31, v30, s63\n"
        "v_mfma_f32_16x16x32_bf16 v[36:39], v[124:127], v[44:47], 0\n"
        "v_add_f32 v155, v155, v22\n"
        "v_add_f32 v155, v155, v23\n"
        "v_mfma_f32_16x16x32_bf16 v[36:39], v[128:131], v[48:51], v[36:39]\n"
        "v_mul_f32 v100, v100, v76\n"
        "v_mul_f32 v101, v101, v77\n"
        "v_mfma_f32_16x16x32_bf16 v[36:39], v[160:163], v[150:153], v[36:39]\n"
        "v_mul_f32 v102, v102, v78\n"
        "v_mul_f32 v103, v103, v79\n"
        "v_mul_f32 v144, v144, v80\n"
        "v_mul_f32 v145, v145, v81\n"
        "v_mfma_f32_16x16x32_bf16 v[140:143], v[52:55], v[150:153], 0\n"
        "v_mul_f32 v146, v146, v82\n"
        "v_mul_f32 v147, v147, v83\n"
        "v_mfma_f32_16x16x32_bf16 v[140:143], v[136:139], v[48:51], v[140:143]\n"
        "v_mul_f32 v24, v24, v84\n"
        "v_mul_f32 v25, v25, v85\n"
        "v_mfma_f32_16x16x32_bf16 v[140:143], v[132:135], v[44:47], v[140:143]\n"
        "v_add_f32 v62, v62, v63\n"
        "v_mul_f32 v62, v62, v40\n"
        "v_mul_f32 v26, v26, v86\n"
        "v_mfma_f32_16x16x4_4b_bf16 v[64:79], v[56:57], v[164:165], 0 blgp:6\n"
        "v_exp_f32 v62, v62\n"
        "v_mul_f32 v27, v27, v87\n"
        "v_fma_f32 v148, v148, v88, v19\n"
        "v_add_f32_dpp v155, v62, v155 quad_perm:[0,1,2,3] row_mask:0x4 bank_mask:0xf\n"
        "v_perm_b32 v40, v101, v100, s63\n"
        "v_perm_b32 v41, v103, v102, s63\n"
        "v_perm_b32 v42, v145, v144, s63\n"
        "v_perm_b32 v43, v147, v146, s63\n"
        "v_perm_b32 v164, v25, v24, s63\n"
        "v_mfma_f32_16x16x32_bf16 v[156:159], v[92:95], v[44:47], 0\n"
        "v_perm_b32 v166, v27, v26, s63\n"
        "v_perm_b32 v165, 0, v148, s63\n"
        "v_mfma_f32_16x16x32_bf16 v[156:159], v[96:99], v[48:51], v[156:159]\n"
        "v_mul_f32 v36, v36, v64\n"
        "v_mul_f32 v37, v37, v65\n"
        "v_mfma_f32_16x16x32_bf16 v[156:159], v[116:119], v[150:153], v[156:159]\n"
        "v_mul_f32 v38, v38, v66\n"
        "v_mul_f32 v39, v39, v67\n"
        "v_mul_f32 v140, v140, v68\n"
        "v_mfma_f32_16x16x32_bf16 v[92:95], v[104:107], v[150:153], 0\n"
        "ds_read_b128 v[88:91], v17 offset:0\n"
        "v_mul_f32 v141, v141, v69\n"
        "v_smfmac_f32_16x16x64_bf16 v[92:95], v[108:111], v[44:51], v4\n"
        "ds_read_b128 v[84:87], v17 offset:64\n"
        "ds_read_b128 v[150:153], v17 offset:128\n"
        "v_mfma_f32_16x16x32_bf16 v[48:51], v[124:127], v[240:243], 0\n"
        "v_exp_f32 v36, v36\n"
        "v_mfma_f32_16x16x32_bf16 v[48:51], v[128:131], v[244:247], v[48:51]\n"
        "ds_read_b128 v[80:83], v0 offset:5136\n"
        "v_mul_f32 v142, v142, v70\n"
        "v_mfma_f32_16x16x32_bf16 v[48:51], v[160:163], v[248:251], v[48:51]\n"
        "ds_read_b128 v[32:35], v0 offset:4112\n"
        "v_mul_f32 v143, v143, v71\n"
        "v_mfma_f32_16x16x32_bf16 v[24:27], v[52:55], v[248:251], 0\n"
        "v_exp_f32 v37, v37\n"
        "v_mfma_f32_16x16x32_bf16 v[24:27], v[136:139], v[244:247], v[24:27]\n"
        "v_exp_f32 v38, v38\n"
        "v_mfma_f32_16x16x32_bf16 v[24:27], v[132:135], v[240:243], v[24:27]\n"
        "s_waitcnt lgkmcnt(2)\n"
        "v_perm_b32 v44, v49, v48, s63\n"
        "v_perm_b32 v45, v51, v50, s63\n"
        "v_add_f32 v145, v36, v37\n"
        "v_mfma_f32_16x16x32_bf16 v[104:107], v[88:91], v[240:243], 0\n"
        "v_exp_f32 v39, v39\n"
        "v_mfma_f32_16x16x32_bf16 v[104:107], v[84:87], v[244:247], v[104:107]\n"
        "v_add_f32 v145, v145, v38\n"
        "v_perm_b32 v46, v25, v24, s63\n"
        "v_mfma_f32_16x16x32_bf16 v[104:107], v[150:153], v[248:251], v[104:107]\n"
        "s_waitcnt lgkmcnt(0)\n"
        "v_perm_b32 v47, v27, v26, s63\n"
        "v_add_f32 v145, v145, v39\n"
        "v_exp_f32 v140, v140\n"
        "v_mfma_f32_16x16x32_bf16 v[64:67], v[150:153], v[80:83], 0\n"
        "v_exp_f32 v141, v141\n"
        "v_mfma_f32_16x16x32_bf16 v[64:67], v[84:87], v[32:35], v[64:67]\n"
        "ds_read_b128 v[146:149], v0 offset:6176\n"
        "v_perm_b32 v116, v105, v104, s63\n"
        "v_mfma_f32_16x16x32_bf16 v[64:67], v[88:91], v[252:255], v[64:67]\n"
        "ds_read_b128 v[24:27], v0 offset:7200\n"
        "ds_read_b128 v[108:111], v0 offset:8224\n"
        "v_mfma_f32_16x16x32_bf16 v[100:103], v[132:135], v[252:255], 0\n"
        "v_perm_b32 v120, v107, v106, s63\n"
        "v_add_f32 v145, v145, v140\n"
        "v_mfma_f32_16x16x32_bf16 v[100:103], v[136:139], v[32:35], v[100:103]\n"
        "v_exp_f32 v142, v142\n"
        "v_mfma_f32_16x16x32_bf16 v[100:103], v[52:55], v[80:83], v[100:103]\n"
        "v_perm_b32 v94, v65, v64, s63\n"
        "v_perm_b32 v98, v67, v66, s63\n"
        "v_add_f32 v145, v145, v141\n"
        "v_mfma_f32_16x16x32_bf16 v[68:71], v[160:163], v[80:83], 0\n"
        "v_exp_f32 v143, v143\n"
        "v_mfma_f32_16x16x32_bf16 v[68:71], v[128:131], v[32:35], v[68:71]\n"
        "v_add_f32 v145, v145, v142\n"
        "v_add_f32 v145, v145, v143\n"
        "v_mfma_f32_16x16x32_bf16 v[68:71], v[124:127], v[252:255], v[68:71]\n"
        "s_waitcnt lgkmcnt(0)\n"
        "v_perm_b32 v106, v101, v100, s63\n"
        "v_perm_b32 v107, v103, v102, s63\n"
        "v_mul_f32 v156, v156, v72\n"
        "v_mul_f32 v157, v157, v73\n"
        "v_mfma_f32_16x16x32_bf16 v[32:35], v[124:127], v[146:149], 0\n"
        "v_mul_f32 v158, v158, v74\n"
        "v_mul_f32 v159, v159, v75\n"
        "v_mfma_f32_16x16x32_bf16 v[32:35], v[128:131], v[24:27], v[32:35]\n"
        "ds_read_b128 v[48:51], v0 offset:11312\n"
        "v_perm_b32 v104, v69, v68, s63\n"
        "v_mfma_f32_16x16x32_bf16 v[32:35], v[160:163], v[108:111], v[32:35]\n"
        "ds_read_b128 v[80:83], v0 offset:10288\n"
        "ds_read_b128 v[64:67], v0 offset:9264\n"
        "v_mfma_f32_16x16x32_bf16 v[100:103], v[52:55], v[108:111], 0\n"
        "v_perm_b32 v105, v71, v70, s63\n"
        "v_add_f32 v92, v92, v93\n"
        "v_mfma_f32_16x16x32_bf16 v[100:103], v[136:139], v[24:27], v[100:103]\n"
        "v_exp_f32 v156, v156\n"
        "v_mfma_f32_16x16x32_bf16 v[100:103], v[132:135], v[146:149], v[100:103]\n"
        "v_perm_b32 v28, v33, v32, s63\n"
        "v_perm_b32 v29, v35, v34, s63\n"
        "v_add_f32 v145, v145, v156\n"
        "v_mfma_f32_16x16x32_bf16 v[70:73], v[88:91], v[146:149], 0\n"
        "v_exp_f32 v157, v157\n"
        "v_mfma_f32_16x16x32_bf16 v[70:73], v[84:87], v[24:27], v[70:73]\n"
        "v_exp_f32 v158, v158\n"
        "v_mfma_f32_16x16x32_bf16 v[70:73], v[150:153], v[108:111], v[70:73]\n"
        "s_waitcnt lgkmcnt(0)\n"
        "v_perm_b32 v30, v101, v100, s63\n"
        "v_perm_b32 v31, v103, v102, s63\n"
        "v_exp_f32 v159, v159\n"
        "v_mfma_f32_16x16x32_bf16 v[32:35], v[150:153], v[48:51], 0\n"
        "v_add_f32 v145, v145, v157\n"
        "v_add_f32 v145, v145, v158\n"
        "v_mfma_f32_16x16x32_bf16 v[32:35], v[84:87], v[80:83], v[32:35]\n"
        "ds_read_b128 v[100:103], v0 offset:12352\n"
        "v_perm_b32 v144, v71, v70, s63\n"
        "v_mfma_f32_16x16x32_bf16 v[32:35], v[88:91], v[64:67], v[32:35]\n"
        "ds_read_b128 v[108:111], v0 offset:13376\n"
        "ds_read_b128 v[24:27], v0 offset:14400\n"
        "v_mfma_f32_16x16x32_bf16 v[68:71], v[132:135], v[64:67], 0\n"
        "v_perm_b32 v148, v73, v72, s63\n"
        "v_mul_f32 v92, v92, v76\n"
        "v_mfma_f32_16x16x32_bf16 v[68:71], v[136:139], v[80:83], v[68:71]\n"
        "v_add_f32 v145, v145, v159\n"
        "v_readlane_b32 s59, v123, 32\n"
        "v_mfma_f32_16x16x32_bf16 v[68:71], v[52:55], v[48:51], v[68:71]\n"
        "v_perm_b32 v117, v33, v32, s63\n"
        "v_perm_b32 v121, v35, v34, s63\n"
        "v_exp_f32 v92, v92\n"
        "v_mfma_f32_16x16x32_bf16 v[72:75], v[160:163], v[48:51], 0\n"
        "v_mul_f32 v154, s59, v154\n"
        "v_add_f32_dpp v145, v92, v145 quad_perm:[0,1,2,3] row_mask:0x4 bank_mask:0xf\n"
        "v_mfma_f32_16x16x32_bf16 v[72:75], v[128:131], v[80:83], v[72:75]\n"
        "v_perm_b32 v154, 0, v154, s63\n"
        "v_perm_b32 v50, v69, v68, s63\n"
        "v_mfma_f32_16x16x32_bf16 v[72:75], v[124:127], v[64:67], v[72:75]\n"
        "s_waitcnt lgkmcnt(0)\n"
        "v_perm_b32 v51, v71, v70, s63\n"
        "v_permlane32_swap_b32 v155, v145\n"
        "v_mfma_f32_16x16x32_bf16 v[80:83], v[124:127], v[100:103], 0\n"
        "v_add_f32 v155, v155, v145\n"
        "v_mov_b32 v149, v155\n"
        "v_mfma_f32_16x16x32_bf16 v[80:83], v[128:131], v[108:111], v[80:83]\n"
        "ds_read_b128 v[32:35], v0 offset:17488\n"
        "v_perm_b32 v48, v73, v72, s63\n"
        "v_mfma_f32_16x16x32_bf16 v[80:83], v[160:163], v[24:27], v[80:83]\n"
        "ds_read_b128 v[68:71], v0 offset:16464\n"
        "ds_read_b128 v[64:67], v0 offset:15440\n"
        "v_mfma_f32_16x16x32_bf16 v[76:79], v[52:55], v[24:27], 0\n"
        "v_perm_b32 v49, v75, v74, s63\n"
        "v_mfma_f32_16x16x32_bf16 v[76:79], v[136:139], v[108:111], v[76:79]\n"
        "v_permlane16_swap_b32 v155, v149\n"
        "v_mfma_f32_16x16x32_bf16 v[76:79], v[132:135], v[100:103], v[76:79]\n"
        "v_add_f32 v155, v155, v149\n"
        "v_rcp_f32 v147, v155\n"
        "v_mfma_f32_16x16x32_bf16 v[72:75], v[88:91], v[100:103], 0\n"
        "ds_read2_b32 v[122:123], v11 offset0:0 offset1:3\n"
        "v_min3_f32 v167, v167, v155, v147\n"
        "v_mfma_f32_16x16x32_bf16 v[72:75], v[84:87], v[108:111], v[72:75]\n"
        "ds_read_b64_tr_b16 v[118:119], v9 offset:0\n"
        "v_and_b32 v102, s28, v147\n"
        "v_mfma_f32_16x16x32_bf16 v[72:75], v[150:153], v[24:27], v[72:75]\n"
        "s_waitcnt lgkmcnt(2)\n"
        "ds_read2_b32 v[100:101], v11 offset0:1 offset1:4\n"
        "v_sub_f32 v149, v147, v102\n"
        "v_cvt_pk_bf16_f32 v103, v102, v149\n"
        "v_perm_b32 v108, v81, v80, s63\n"
        "v_mfma_f32_16x16x32_bf16 v[24:27], v[150:153], v[32:35], 0\n"
        "ds_read_b64_tr_b16 v[96:97], v9 offset:16\n"
        "v_perm_b32 v109, v83, v82, s63\n"
        "v_mfma_f32_16x16x32_bf16 v[24:27], v[84:87], v[68:71], v[24:27]\n"
        "ds_read2_b32 v[150:151], v11 offset0:2 offset1:5\n"
        "v_perm_b32 v110, v77, v76, s63\n"
        "v_mfma_f32_16x16x32_bf16 v[24:27], v[88:91], v[64:67], v[24:27]\n"
        "ds_read_b64_tr_b16 v[146:147], v9 offset:32\n"
        "v_perm_b32 v111, v79, v78, s63\n"
        "v_perm_b32 v95, v73, v72, s63\n"
        "v_mfma_f32_16x16x4_4b_bf16 v[76:91], v[56:57], v[102:103], 0 blgp:4\n"
        "v_perm_b32 v99, v75, v74, s63\n"
        "v_cmp_nle_f32 vcc, 0x2b800000, v167\n"
        "v_mov_b32_dpp v167, v19 row_shr:4 row_mask:0xf bank_mask:0xf bound_ctrl:0\n"
        "ds_write_b16 v1, v154\n"
        "v_perm_b32 v145, v25, v24, s63\n"
        "v_perm_b32 v149, v27, v26, s63\n"
        "s_andn2_b64 vcc, vcc, s[56:57]\n"
        "s_cbranch_scc1 CREDOA_%=\n"
        "v_mfma_f32_16x16x32_bf16 v[24:27], v[132:135], v[64:67], 0\n"
        "s_mov_b32 exec_hi, 0xffff\n"
        "v_add_u32 v10, 40064, v10\n"
        "s_mov_b32 exec_hi, -1\n"
        "v_mfma_f32_16x16x32_bf16 v[24:27], v[136:139], v[68:71], v[24:27]\n"
        "v_mul_f32 v58, v58, v76\n"
        "v_mul_f32 v59, v59, v77\n"
        "v_mfma_f32_16x16x32_bf16 v[24:27], v[52:55], v[32:35], v[24:27]\n"
        "s_add_u32 s31, s31, 1\n"
        "v_mul_f32 v60, v60, v78\n"
        "v_mul_f32 v61, v61, v79\n"
        "v_mul_f32 v112, v112, v80\n"
        "v_mul_f32 v113, v113, v81\n"
        "v_mfma_f32_16x16x32_bf16 v[52:55], v[160:163], v[32:35], 0\n"
        "s_cmp_ge_u32 s31, s71\n"
        "v_mul_f32 v114, v114, v82\n"
        "v_mul_f32 v115, v115, v83\n"
        "v_mfma_f32_16x16x32_bf16 v[52:55], v[128:131], v[68:71], v[52:55]\n"
        "s_cbranch_scc1 L34A_%=\n"
        "L35A_%=:\n"
        "v_mul_f32 v20, v20, v84\n"
        "v_perm_b32 v34, v25, v24, s63\n"
        "v_mfma_f32_16x16x32_bf16 v[52:55], v[124:127], v[64:67], v[52:55]\n"
        "s_add_u32 s16, s16, 256\n"
        "v_perm_b32 v35, v27, v26, s63\n"
        "v_mul_f32 v21, v21, v85\n"
        "v_mfma_f32_16x16x32_bf16 v[124:127], v[40:43], v[44:47], 0\n"
        "s_waitcnt lgkmcnt(0)\n"
        "v_mul_f32 v22, v22, v86\n"
        "v_mul_f32 v23, v23, v87\n"
        "v_smfmac_f32_16x16x64_bf16 v[124:127], v[164:167], v[116:123], v18\n"
        "s_cmp_ge_u32 s31, s43\n"
        "s_cselect_b32 s39, s22, s16\n"
        "ds_read_b64_tr_b16 v[118:119], v9 offset:48\n"
        "v_mfma_f32_16x16x4_4b_bf16 v[72:87], v[56:57], v[102:103], 0 blgp:6\n"
        "v_perm_b32 v32, v53, v52, s63\n"
        "v_perm_b32 v33, v55, v54, s63\n"
        "v_fma_f32 v62, v62, v88, v19\n"
        "v_perm_b32 v68, v59, v58, s63\n"
        "v_perm_b32 v69, v61, v60, s63\n"
        "v_perm_b32 v70, v113, v112, s63\n"
        "v_perm_b32 v71, v115, v114, s63\n"
        "v_perm_b32 v24, v21, v20, s63\n"
        "v_perm_b32 v26, v23, v22, s63\n"
        "v_mfma_f32_16x16x32_bf16 v[88:91], v[40:43], v[104:107], 0\n"
        "s_cmp_gt_u32 s31, s43\n"
        "v_perm_b32 v25, 0, v62, s63\n"
        "v_mul_f32 v36, v36, v72\n"
        "v_smfmac_f32_16x16x64_bf16 v[88:91], v[164:167], v[94:101], v18\n"
        "s_cselect_b32 s39, s23, s39\n"
        "ds_read_b64_tr_b16 v[96:97], v9 offset:64\n"
        "v_mfma_f32_16x16x32_bf16 v[56:59], v[40:43], v[28:31], 0\n"
        "s_mul_hi_u32 s38, s39, 58835169\n"
        "ds_read_b128 v[128:131], v2\n"
        "v_mul_f32 v37, v37, v73\n"
        "v_smfmac_f32_16x16x64_bf16 v[56:59], v[164:167], v[144:151], v18\n"
        "s_mul_i32 s39, s39, 1344\n"
        "ds_read_b64_tr_b16 v[146:147], v9 offset:80\n"
        "ds_read_b128 v[52:55], v3\n"
        "v_mfma_f32_16x16x32_bf16 v[112:115], v[40:43], v[48:51], 0\n"
        "s_waitcnt lgkmcnt(4)\n"
        "v_mul_f32 v38, v38, v74\n"
        "v_mul_f32 v39, v39, v75\n"
        "v_smfmac_f32_16x16x64_bf16 v[112:115], v[164:167], v[116:123], v18 abid:1\n"
        "s_mul_i32 s38, s38, 588672\n"
        "s_add_u32 s39, s39, s38\n"
        "v_mul_f32 v140, v140, v76\n"
        "v_cvt_pk_bf16_f32 v152, v124, v88\n"
        "v_mul_f32 v141, v141, v77\n"
        "v_mfma_f32_16x16x32_bf16 v[72:75], v[40:43], v[108:111], 0\n"
        "s_add_u32 s39, s39, s17\n"
        "v_mov_b64 v[60:61], 0\n"
        "s_waitcnt lgkmcnt(3)\n"
        "v_smfmac_f32_16x16x64_bf16 v[72:75], v[164:167], v[94:101], v18 abid:1\n"
        "s_mul_i32 s68, s31, 3\n"
        "v_mov_b64 v[62:63], 0\n"
        "v_cvt_pk_bf16_f32 v153, v56, v112\n"
        "v_mfma_f32_16x16x32_bf16 v[64:67], v[40:43], v[32:35], 0\n"
        "s_waitcnt lgkmcnt(1)\n"
        "v_mov_b64 v[20:21], 0\n"
        "v_mov_b64 v[22:23], 0\n"
        "v_smfmac_f32_16x16x64_bf16 v[64:67], v[164:167], v[144:151], v18 abid:1\n"
        "s_waitcnt lgkmcnt(0)\n"
        "s_bitcmp1_b32 s31, 0\n"
        "v_mov_b32_dpp v27, v19 row_shr:4 row_mask:0xf bank_mask:0xf bound_ctrl:0\n"
        "s_cselect_b32 s38, 3, 6\n"
        "v_smfmac_f32_16x16x64_bf16 v[60:63], v[128:131], v[44:51], v4\n"
        "s_add_u32 s68, s68, s38\n"
        "v_mov_b64 v[40:41], 0\n"
        "v_smfmac_f32_16x16x64_bf16 v[60:63], v[52:55], v[116:123], v4 abid:1\n"
        "s_add_u32 s37, s35, 0\n"
        "s_cmp_lt_u32 s31, s14\n"
        "ds_read_b64_tr_b16 v[118:119], v9 offset:3328\n"
        "v_mov_b64 v[42:43], 0\n"
        "v_smfmac_f32_16x16x64_bf16 v[20:23], v[128:131], v[104:111], v4\n"
        "s_cselect_b32 s68, s68, 0\n"
        "v_cvt_pk_bf16_f32 v154, v72, v64\n"
        "v_smfmac_f32_16x16x64_bf16 v[20:23], v[52:55], v[94:101], v4 abid:1\n"
        "s_and_b32 s38, s31, 7\n"
        "ds_read_b64_tr_b16 v[96:97], v9 offset:3344\n"
        "v_cvt_pk_bf16_f32 v160, v125, v89\n"
        "v_smfmac_f32_16x16x64_bf16 v[40:43], v[128:131], v[28:35], v4\n"
        "s_cselect_b32 s30, 1, -7\n"
        "v_cvt_pk_bf16_f32 v88, v126, v90\n"
        "v_smfmac_f32_16x16x64_bf16 v[40:43], v[52:55], v[144:151], v4 abid:1\n"
        "buffer_store_dwordx3 v[152:154], v12, s[8:11], s37 offen sc0 sc1 nt\n"
        "ds_read_b64_tr_b16 v[146:147], v9 offset:3360\n"
        "v_cvt_pk_bf16_f32 v164, v127, v91\n"
        "v_cvt_pk_bf16_f32 v161, v57, v113\n"
        "v_cvt_pk_bf16_f32 v89, v58, v114\n"
        "v_cvt_pk_bf16_f32 v165, v59, v115\n"
        "v_cvt_pk_bf16_f32 v162, v73, v65\n"
        "v_mfma_f32_16x16x32_bf16 v[152:155], v[68:71], v[44:47], 0\n"
        "s_waitcnt lgkmcnt(2)\n"
        "v_cvt_pk_bf16_f32 v90, v74, v66\n"
        "v_cvt_pk_bf16_f32 v166, v75, v67\n"
        "v_smfmac_f32_16x16x64_bf16 v[152:155], v[24:27], v[116:123], v18\n"
        "s_mul_i32 s29, s30, 192\n"
        "s_mul_i32 s30, s30, 392\n"
        "ds_read_b64_tr_b16 v[118:119], v9 offset:3376\n"
        "v_add_f32 v60, v60, v61\n"
        "v_add_f32 v62, v62, v63\n"
        "v_add_f32 v20, v20, v21\n"
        "v_mfma_f32_16x16x32_bf16 v[56:59], v[68:71], v[104:107], 0\n"
        "s_waitcnt lgkmcnt(2)\n"
        "v_add_f32 v22, v22, v23\n"
        "v_cvt_pk_bf16_f32 v112, v60, v20\n"
        "v_smfmac_f32_16x16x64_bf16 v[56:59], v[24:27], v[94:101], v18\n"
        "s_add_u32 s37, s35, 0\n"
        "s_cmp_eq_u32 s69, s18\n"
        "ds_read_b64_tr_b16 v[96:97], v9 offset:3392\n"
        "v_add_f32 v40, v40, v41\n"
        "v_add_f32 v42, v42, v43\n"
        "v_cvt_pk_bf16_f32 v113, v40, v62\n"
        "v_mfma_f32_16x16x32_bf16 v[72:75], v[68:71], v[28:31], 0\n"
        "s_waitcnt lgkmcnt(2)\n"
        "v_cvt_pk_bf16_f32 v114, v22, v42\n"
        "v_mul_f32 v142, v142, v78\n"
        "v_smfmac_f32_16x16x64_bf16 v[72:75], v[24:27], v[144:151], v18\n"
        "buffer_store_dwordx3 v[160:162], v15, s[8:11], s37 offen offset:768 sc0 sc1 nt\n"
        "ds_read_b64_tr_b16 v[146:147], v9 offset:3408\n"
        "s_cselect_b32 s34, -7488, s70\n"
        "v_mul_f32 v143, v143, v79\n"
        "v_mul_f32 v156, v156, v80\n"
        "v_mul_f32 v157, v157, v81\n"
        "v_mul_f32 v158, v158, v82\n"
        "v_mfma_f32_16x16x32_bf16 v[20:23], v[68:71], v[48:51], 0\n"
        "s_waitcnt lgkmcnt(2)\n"
        "s_add_u32 s69, s69, s34\n"
        "v_mul_f32 v159, v159, v83\n"
        "v_smfmac_f32_16x16x64_bf16 v[20:23], v[24:27], v[116:123], v18 abid:1\n"
        "s_add_u32 s37, s35, 98112\n"
        "ds_read_b64_tr_b16 v[118:119], v9 offset:6656\n"
        "v_fma_f32 v92, v92, v84, v19\n"
        "v_mfma_f32_16x16x32_bf16 v[84:87], v[68:71], v[108:111], 0\n"
        "s_waitcnt lgkmcnt(2)\n"
        "v_perm_b32 v80, v37, v36, s63\n"
        "v_perm_b32 v81, v39, v38, s63\n"
        "v_smfmac_f32_16x16x64_bf16 v[84:87], v[24:27], v[94:101], v18 abid:1\n"
        "ds_read_b64_tr_b16 v[96:97], v9 offset:6672\n"
        "v_perm_b32 v82, v141, v140, s63\n"
        "v_mfma_f32_16x16x32_bf16 v[60:63], v[68:71], v[32:35], 0\n"
        "s_waitcnt lgkmcnt(2)\n"
        "v_perm_b32 v83, v143, v142, s63\n"
        "v_perm_b32 v76, v157, v156, s63\n"
        "v_smfmac_f32_16x16x64_bf16 v[60:63], v[24:27], v[144:151], v18 abid:1\n"
        "buffer_store_dwordx3 v[88:90], v12, s[8:11], s37 offen offset:192 sc0 sc1 nt\n"
        "ds_read_b64_tr_b16 v[146:147], v9 offset:6688\n"
        "ds_read_b32 v167, v0 offset:21604\n"
        "v_perm_b32 v78, v159, v158, s63\n"
        "v_perm_b32 v77, 0, v92, s63\n"
        "v_mov_b32_dpp v79, v19 row_shr:4 row_mask:0xf bank_mask:0xf bound_ctrl:0\n"
        "v_mfma_f32_16x16x32_bf16 v[68:71], v[80:83], v[44:47], 0\n"
        "s_waitcnt lgkmcnt(3)\n"
        "v_writelane_b32 v10, s69, 56\n"
        "v_add_u32 v11, s30, v11\n"
        "v_smfmac_f32_16x16x64_bf16 v[68:71], v[76:79], v[116:123], v18\n"
        "s_add_u32 s37, s35, 98112\n"
        "ds_read_b64_tr_b16 v[118:119], v9 offset:6704\n"
        "v_mfma_f32_16x16x32_bf16 v[36:39], v[80:83], v[104:107], 0\n"
        "s_waitcnt lgkmcnt(3)\n"
        "v_smfmac_f32_16x16x64_bf16 v[36:39], v[76:79], v[94:101], v18\n"
        "ds_read_b64_tr_b16 v[96:97], v9 offset:6720\n"
        "v_mfma_f32_16x16x32_bf16 v[40:43], v[80:83], v[28:31], 0\n"
        "s_waitcnt lgkmcnt(3)\n"
        "s_waitcnt lgkmcnt(2)\n"
        "v_readfirstlane_b32 s40, v167\n"
        "v_smfmac_f32_16x16x64_bf16 v[40:43], v[76:79], v[144:151], v18\n"
        "buffer_store_dwordx3 v[164:166], v14, s[8:11], s37 offen offset:960 sc0 sc1 nt\n"
        "ds_read_b64_tr_b16 v[146:147], v9 offset:6736\n"
        "s_add_u32 s37, s35, 588672\n"
        "s_add_u32 s46, s37, 1344\n"
        "s_cmp_lt_i32 s40, s68\n"
        "s_mov_b64 exec, 1\n"
        "ds_inc_u32 v0, v1 offset:21608\n"
        "s_mov_b64 exec, -1\n"
        "s_cbranch_scc1 L38A_%=\n"
        "L39A_%=:\n"
        "ds_read_b128 v[124:127], v7 offset:40064\n"
        "v_mfma_f32_16x16x32_bf16 v[64:67], v[80:83], v[48:51], 0\n"
        "s_waitcnt lgkmcnt(4)\n"
        "ds_read_b128 v[132:135], v7 offset:43392\n"
        "v_smfmac_f32_16x16x64_bf16 v[64:67], v[76:79], v[116:123], v18 abid:1\n"
        "buffer_store_dwordx3 v[112:114], v12, s[44:47], s37 offen offset:1152 sc0 sc1 nt\n"
        "ds_read_b128 v[128:131], v7 offset:40128\n"
        "ds_read_b128 v[136:139], v7 offset:43456\n"
        "ds_read_b128 v[160:163], v7 offset:40192\n"
        "ds_read_b128 v[52:55], v7 offset:43520\n"
        "v_mfma_f32_16x16x32_bf16 v[104:107], v[80:83], v[108:111], 0\n"
        "s_waitcnt lgkmcnt(7)\n"
        "s_mov_b32 s36, s35\n"
        "v_smfmac_f32_16x16x64_bf16 v[104:107], v[76:79], v[94:101], v18 abid:1\n"
        "s_mov_b64 exec, s[54:55]\n"
        "v_add_u32 v16, s29, v16\n"
        "v_add_u32 v8, s34, v8\n"
        "s_mov_b64 exec, -1\n"
        "v_mfma_f32_16x16x32_bf16 v[88:91], v[80:83], v[32:35], 0\n"
        "s_mov_b32 s35, s39\n"
        "v_smfmac_f32_16x16x64_bf16 v[88:91], v[76:79], v[144:151], v18 abid:1\n"
        "s_mov_b64 exec, s[50:51]\n"
        "v_add_u32 v5, s29, v5\n"
        "v_add_u32 v6, s34, v6\n"
        "s_mov_b64 exec, -1\n"
        "s_setprio 0\n"
        "CNEXTA_%=:\n"
        "s_cmp_ge_u32 s31, s14\n"
        "s_cbranch_scc1 CEND_%=\n"
        "s_waitcnt lgkmcnt(5)\n"
        "v_mfma_f32_16x16x32_bf16 v[24:27], v[168:171], v[124:127], 0\n"
        "v_cvt_pk_bf16_f32 v156, v152, v56\n"
        "v_cvt_pk_bf16_f32 v157, v72, v20\n"
        "s_waitcnt lgkmcnt(3)\n"
        "v_mfma_f32_16x16x32_bf16 v[24:27], v[172:175], v[128:131], v[24:27]\n"
        "ds_read_b128 v[44:47], v16 offset:128\n"
        "v_cvt_pk_bf16_f32 v158, v84, v60\n"
        "s_waitcnt lgkmcnt(2)\n"
        "v_mfma_f32_16x16x32_bf16 v[24:27], v[176:179], v[160:163], v[24:27]\n"
        "ds_read_b128 v[48:51], v5\n"
        "v_cvt_pk_bf16_f32 v164, v153, v57\n"
        "v_mfma_f32_16x16x32_bf16 v[32:35], v[188:191], v[160:163], 0\n"
        "v_cvt_pk_bf16_f32 v165, v73, v21\n"
        "v_cvt_pk_bf16_f32 v166, v85, v61\n"
        "v_mfma_f32_16x16x32_bf16 v[32:35], v[184:187], v[128:131], v[32:35]\n"
        "v_cvt_pk_bf16_f32 v146, v154, v58\n"
        "v_cvt_pk_bf16_f32 v147, v74, v22\n"
        "v_mfma_f32_16x16x32_bf16 v[32:35], v[180:183], v[124:127], v[32:35]\n"
        "s_add_u32 s37, s36, 196224\n"
        "ds_read_b128 v[92:95], v7 offset:46720\n"
        "ds_read_b128 v[96:99], v7 offset:46784\n"
        "ds_read_b128 v[116:119], v7 offset:46848\n"
        "v_mfma_f32_16x16x32_bf16 v[76:79], v[192:195], v[124:127], 0\n"
        "v_perm_b32 v80, v25, v24, s63\n"
        "v_perm_b32 v81, v27, v26, s63\n"
        "v_mfma_f32_16x16x32_bf16 v[76:79], v[196:199], v[128:131], v[76:79]\n"
        "v_perm_b32 v82, v33, v32, s63\n"
        "v_perm_b32 v83, v35, v34, s63\n"
        "v_mfma_f32_16x16x32_bf16 v[76:79], v[200:203], v[160:163], v[76:79]\n"
        "buffer_store_dwordx3 v[156:158], v12, s[8:11], s37 offen offset:384 sc0 sc1 nt\n"
        "v_cvt_pk_bf16_f32 v148, v86, v62\n"
        "v_cvt_pk_bf16_f32 v150, v155, v59\n"
        "v_cvt_pk_bf16_f32 v151, v75, v23\n"
        "v_cvt_pk_bf16_f32 v152, v87, v63\n"
        "s_waitcnt lgkmcnt(3)\n"
        "v_mfma_f32_16x16x32_bf16 v[32:35], v[44:47], v[160:163], 0\n"
        "v_cvt_pk_bf16_f32 v72, v68, v36\n"
        "v_perm_b32 v84, v77, v76, s63\n"
        "v_smfmac_f32_16x16x64_bf16 v[32:35], v[48:51], v[124:131], v4\n"
        "s_add_u32 s37, s36, 196224\n"
        "v_perm_b32 v85, v79, v78, s63\n"
        "v_mfma_f32_16x16x32_bf16 v[24:27], v[44:47], v[52:55], 0\n"
        "v_cvt_pk_bf16_f32 v73, v40, v64\n"
        "v_cvt_pk_bf16_f32 v74, v104, v88\n"
        "v_smfmac_f32_16x16x64_bf16 v[24:27], v[48:51], v[132:139], v4\n"
        "s_waitcnt lgkmcnt(0)\n"
        "s_setprio 2\n"
        "v_cvt_pk_bf16_f32 v156, v69, v37\n"
        "v_cvt_pk_bf16_f32 v157, v41, v65\n"
        "v_mfma_f32_16x16x32_bf16 v[100:103], v[44:47], v[116:119], 0\n"
        "v_cvt_pk_bf16_f32 v158, v105, v89\n"
        "v_cvt_pk_bf16_f32 v76, v70, v38\n"
        "v_smfmac_f32_16x16x64_bf16 v[100:103], v[48:51], v[92:99], v4\n"
        "buffer_store_dwordx3 v[164:166], v13, s[8:11], s37 offen offset:1152 sc0 sc1 nt\n"
        "v_cvt_pk_bf16_f32 v77, v42, v66\n"
        "v_cvt_pk_bf16_f32 v78, v106, v90\n"
        "v_cvt_pk_bf16_f32 v88, v71, v39\n"
        "v_cvt_pk_bf16_f32 v89, v43, v67\n"
        "v_cvt_pk_bf16_f32 v90, v107, v91\n"
        "v_mfma_f32_16x16x32_bf16 v[20:23], v[212:215], v[160:163], 0\n"
        "s_add_u32 s37, s36, 294336\n"
        "ds_read_b64 v[154:155], v11 offset:192\n"
        "v_mfma_f32_16x16x32_bf16 v[20:23], v[208:211], v[128:131], v[20:23]\n"
        "ds_read_b32 v153, v10\n"
        "v_mfma_f32_16x16x32_bf16 v[20:23], v[204:207], v[124:127], v[20:23]\n"
        "v_mfma_f32_16x16x32_bf16 v[28:31], v[216:219], v[124:127], 0\n"
        "v_mfma_f32_16x16x32_bf16 v[28:31], v[220:223], v[128:131], v[28:31]\n"
        "v_mfma_f32_16x16x32_bf16 v[28:31], v[224:227], v[160:163], v[28:31]\n"
        "buffer_store_dwordx3 v[146:148], v12, s[8:11], s37 offen offset:576 sc0 sc1 nt\n"
        "s_add_u32 s37, s36, 392448\n"
        "s_waitcnt lgkmcnt(1)\n"
        "v_add_f32_dpp v155, v34, v35 quad_perm:[0,1,2,3] row_mask:0x1 bank_mask:0xf\n"
        "v_add_f32_dpp v154, v32, v33 quad_perm:[0,1,2,3] row_mask:0x1 bank_mask:0xf\n"
        "v_perm_b32 v86, v21, v20, s63\n"
        "v_mfma_f32_16x16x32_bf16 v[44:47], v[236:239], v[160:163], 0\n"
        "v_perm_b32 v87, v23, v22, s63\n"
        "v_perm_b32 v20, v29, v28, s63\n"
        "v_mfma_f32_16x16x32_bf16 v[44:47], v[232:235], v[128:131], v[44:47]\n"
        "v_perm_b32 v21, v31, v30, s63\n"
        "v_add_f32_dpp v155, v26, v27 quad_perm:[0,1,2,3] row_mask:0x2 bank_mask:0xf\n"
        "v_mfma_f32_16x16x32_bf16 v[44:47], v[228:231], v[124:127], v[44:47]\n"
        "buffer_store_dwordx3 v[150:152], v12, s[8:11], s37 offen sc0 sc1 nt\n"
        "v_add_f32_dpp v154, v24, v25 quad_perm:[0,1,2,3] row_mask:0x2 bank_mask:0xf\n"
        "v_add_f32_dpp v155, v102, v103 quad_perm:[0,1,2,3] row_mask:0x4 bank_mask:0xf\n"
        "v_mul_f32 v165, s52, v155\n"
        "v_add_f32_dpp v154, v100, v101 quad_perm:[0,1,2,3] row_mask:0x4 bank_mask:0xf\n"
        "s_waitcnt lgkmcnt(0)\n"
        "v_fma_f32 v165, -v165, v155, v153\n"
        "v_add_f32_e64 v165, s53, |v165|\n"
        "v_perm_b32 v23, v47, v46, s63\n"
        "v_perm_b32 v22, v45, v44, s63\n"
        "v_mfma_f32_16x16x32_bf16 v[100:103], v[124:127], v[80:83], 0\n"
        "v_rsq_f32 v153, v165\n"
        "v_mfma_f32_16x16x32_bf16 v[100:103], v[128:131], v[84:87], v[100:103]\n"
        "s_add_u32 s37, s36, 392448\n"
        "ds_read_b128 v[104:107], v8 offset:128\n"
        "v_and_b32 v56, s28, v153\n"
        "v_mfma_f32_16x16x32_bf16 v[100:103], v[160:163], v[20:23], v[100:103]\n"
        "ds_read2_b32 v[108:109], v6 offset0:0 offset1:2\n"
        "ds_read2_b32 v[110:111], v6 offset0:8 offset1:10\n"
        "v_mfma_f32_16x16x32_bf16 v[144:147], v[52:55], v[20:23], 0\n"
        "v_and_b32 v164, s28, v153\n"
        "v_sub_f32 v167, v153, v56\n"
        "v_mfma_f32_16x16x32_bf16 v[144:147], v[136:139], v[84:87], v[144:147]\n"
        "v_cvt_pk_bf16_f32 v57, v167, v56\n"
        "v_cvt_pk_bf16_f32 v165, v164, v167\n"
        "v_mfma_f32_16x16x32_bf16 v[144:147], v[132:135], v[80:83], v[144:147]\n"
        "buffer_store_dwordx3 v[72:74], v15, s[8:11], s37 offen offset:768 sc0 sc1 nt\n"
        "v_mul_f32 v154, v154, v153\n"
        "v_readlane_b32 s58, v153, 56\n"
        "s_mov_b64 exec, s[56:57]\n"
        "v_mov_b32 v154, 0xff800000\n"
        "s_mov_b64 exec, -1\n"
        "v_mfma_f32_16x16x4_4b_bf16 v[32:47], v[56:57], v[164:165], 0 blgp:4\n"
        "v_mul_f32 v154, s58, v154\n"
        "v_exp_f32 v154, v154\n"
        "v_mfma_f32_16x16x32_bf16 v[24:27], v[92:95], v[80:83], 0\n"
        "s_add_u32 s37, s36, 490560\n"
        "v_add_f32_dpp v167, v154, v154 row_ror:8 row_mask:0xf bank_mask:0xf\n"
        "v_mul_f32 v154, v154, v153\n"
        "v_mfma_f32_16x16x32_bf16 v[24:27], v[96:99], v[84:87], v[24:27]\n"
        "v_add_f32_dpp v167, v167, v167 row_ror:4 row_mask:0xf bank_mask:0xf\n"
        "v_mfma_f32_16x16x32_bf16 v[24:27], v[116:119], v[20:23], v[24:27]\n"
        "s_waitcnt lgkmcnt(0)\n"
        "v_add_f32_dpp v167, v167, v167 row_ror:2 row_mask:0xf bank_mask:0xf\n"
        "v_mfma_f32_16x16x32_bf16 v[148:151], v[104:107], v[20:23], 0\n"
        "v_mul_f32 v100, v100, v32\n"
        "v_mul_f32 v101, v101, v33\n"
        "v_smfmac_f32_16x16x64_bf16 v[148:151], v[108:111], v[80:87], v4\n"
        "buffer_store_dwordx3 v[156:158], v12, s[8:11], s37 offen offset:192 sc0 sc1 nt\n"
        "v_mul_f32 v102, v102, v34\n"
        "v_mul_f32 v103, v103, v35\n"
        "v_exp_f32 v100, v100\n"
        "v_mul_f32 v144, v144, v36\n"
        "v_mfma_f32_16x16x32_bf16 v[20:23], v[168:171], v[132:135], 0\n"
        "s_add_u32 s37, s36, 490560\n"
        "v_exp_f32 v101, v101\n"
        "v_mfma_f32_16x16x32_bf16 v[20:23], v[172:175], v[136:139], v[20:23]\n"
        "v_exp_f32 v102, v102\n"
        "v_mfma_f32_16x16x32_bf16 v[20:23], v[176:179], v[52:55], v[20:23]\n"
        "v_exp_f32 v103, v103\n"
        "v_add_f32 v151, v100, v101\n"
        "v_mfma_f32_16x16x32_bf16 v[28:31], v[188:191], v[52:55], 0\n"
        "v_add_f32 v151, v151, v102\n"
        "v_add_f32 v151, v151, v103\n"
        "v_mfma_f32_16x16x32_bf16 v[28:31], v[184:187], v[136:139], v[28:31]\n"
        "v_mul_f32 v145, v145, v37\n"
        "v_mul_f32 v146, v146, v38\n"
        "v_mfma_f32_16x16x32_bf16 v[28:31], v[180:183], v[132:135], v[28:31]\n"
        "buffer_store_dwordx3 v[76:78], v14, s[8:11], s37 offen offset:960 sc0 sc1 nt\n"
        "v_perm_b32 v68, v21, v20, s63\n"
        "v_perm_b32 v69, v23, v22, s63\n"
        "v_mul_f32 v147, v147, v39\n"
        "v_exp_f32 v144, v144\n"
        "v_mfma_f32_16x16x32_bf16 v[20:23], v[192:195], v[132:135], 0\n"
        "s_add_u32 s37, s36, 588672\n"
        "v_exp_f32 v145, v145\n"
        "v_mfma_f32_16x16x32_bf16 v[20:23], v[196:199], v[136:139], v[20:23]\n"
        "v_perm_b32 v70, v29, v28, s63\n"
        "v_perm_b32 v71, v31, v30, s63\n"
        "v_mfma_f32_16x16x32_bf16 v[20:23], v[200:203], v[52:55], v[20:23]\n"
        "buffer_store_dwordx3 v[88:90], v12, s[8:11], s37 offen offset:384 sc0 sc1 nt\n"
        "v_exp_f32 v146, v146\n"
        "v_exp_f32 v147, v147\n"
        "v_add_f32 v151, v151, v144\n"
        "v_add_f32 v151, v151, v145\n"
        "v_mfma_f32_16x16x32_bf16 v[28:31], v[212:215], v[52:55], 0\n"
        "v_add_f32 v151, v151, v146\n"
        "v_add_f32 v151, v151, v147\n"
        "v_mfma_f32_16x16x32_bf16 v[28:31], v[208:211], v[136:139], v[28:31]\n"
        "v_perm_b32 v72, v21, v20, s63\n"
        "v_perm_b32 v73, v23, v22, s63\n"
        "v_mfma_f32_16x16x32_bf16 v[28:31], v[204:207], v[132:135], v[28:31]\n"
        "v_mul_f32 v24, v24, v40\n"
        "v_mul_f32 v25, v25, v41\n"
        "v_mul_f32 v26, v26, v42\n"
        "v_mfma_f32_16x16x32_bf16 v[20:23], v[216:219], v[132:135], 0\n"
        "v_mul_f32 v27, v27, v43\n"
        "v_add_f32 v148, v148, v149\n"
        "v_mfma_f32_16x16x32_bf16 v[20:23], v[220:223], v[136:139], v[20:23]\n"
        "v_exp_f32 v24, v24\n"
        "v_mfma_f32_16x16x32_bf16 v[20:23], v[224:227], v[52:55], v[20:23]\n"
        "v_perm_b32 v74, v29, v28, s63\n"
        "v_perm_b32 v75, v31, v30, s63\n"
        "v_add_f32 v151, v151, v24\n"
        "v_mfma_f32_16x16x32_bf16 v[64:67], v[236:239], v[52:55], 0\n"
        "v_exp_f32 v25, v25\n"
        "v_mfma_f32_16x16x32_bf16 v[64:67], v[232:235], v[136:139], v[64:67]\n"
        "v_exp_f32 v26, v26\n"
        "v_mfma_f32_16x16x32_bf16 v[64:67], v[228:231], v[132:135], v[64:67]\n"
        "v_perm_b32 v48, v21, v20, s63\n"
        "v_perm_b32 v49, v23, v22, s63\n"
        "v_mfma_f32_16x16x4_4b_bf16 v[28:43], v[56:57], v[164:165], 0 blgp:5\n"
        "v_exp_f32 v27, v27\n"
        "v_add_f32 v151, v151, v25\n"
        "v_add_f32 v151, v151, v26\n"
        "v_mul_f32 v148, v148, v44\n"
        "v_exp_f32 v148, v148\n"
        "v_perm_b32 v50, v65, v64, s63\n"
        "v_perm_b32 v51, v67, v66, s63\n"
        "v_mfma_f32_16x16x32_bf16 v[58:61], v[124:127], v[68:71], 0\n"
        "v_add_f32 v151, v151, v27\n"
        "v_add_f32_dpp v167, v167, v167 row_ror:1 row_mask:0xf bank_mask:0xf\n"
        "v_mfma_f32_16x16x32_bf16 v[58:61], v[128:131], v[72:75], v[58:61]\n"
        "v_add_f32_dpp v151, v148, v151 quad_perm:[0,1,2,3] row_mask:0x4 bank_mask:0xf\n"
        "v_mfma_f32_16x16x32_bf16 v[58:61], v[160:163], v[48:51], v[58:61]\n"
        "v_mfma_f32_16x16x32_bf16 v[112:115], v[52:55], v[48:51], 0\n"
        "v_permlane32_swap_b32 v151, v167\n"
        "v_mfma_f32_16x16x32_bf16 v[112:115], v[136:139], v[72:75], v[112:115]\n"
        "v_add_f32 v151, v151, v167\n"
        "v_mov_b32 v167, v151\n"
        "v_mfma_f32_16x16x32_bf16 v[112:115], v[132:135], v[68:71], v[112:115]\n"
        "v_mfma_f32_16x16x32_bf16 v[20:23], v[92:95], v[68:71], 0\n"
        "v_permlane16_swap_b32 v151, v167\n"
        "v_mfma_f32_16x16x32_bf16 v[20:23], v[96:99], v[72:75], v[20:23]\n"
        "v_add_f32 v151, v151, v167\n"
        "v_mul_f32 v58, v58, v28\n"
        "v_mfma_f32_16x16x32_bf16 v[20:23], v[116:119], v[48:51], v[20:23]\n"
        "v_rcp_f32 v123, v151\n"
        "v_mul_f32 v59, v59, v29\n"
        "v_and_b32 v158, s28, v123\n"
        "v_mfma_f32_16x16x32_bf16 v[62:65], v[104:107], v[48:51], 0\n"
        "v_sub_f32 v152, v123, v158\n"
        "v_cvt_pk_bf16_f32 v159, v158, v152\n"
        "v_smfmac_f32_16x16x64_bf16 v[62:65], v[108:111], v[68:75], v4\n"
        "v_min3_f32 v167, v151, v123, v153\n"
        "v_mfma_f32_16x16x32_bf16 v[48:51], v[168:171], v[92:95], 0\n"
        "v_mul_f32 v60, v60, v30\n"
        "v_mul_f32 v61, v61, v31\n"
        "v_mfma_f32_16x16x32_bf16 v[48:51], v[172:175], v[96:99], v[48:51]\n"
        "v_exp_f32 v58, v58\n"
        "v_mfma_f32_16x16x32_bf16 v[48:51], v[176:179], v[116:119], v[48:51]\n"
        "v_exp_f32 v59, v59\n"
        "v_exp_f32 v60, v60\n"
        "v_mfma_f32_16x16x32_bf16 v[68:71], v[188:191], v[116:119], 0\n"
        "v_exp_f32 v61, v61\n"
        "v_mfma_f32_16x16x32_bf16 v[68:71], v[184:187], v[96:99], v[68:71]\n"
        "v_add_f32 v155, v58, v59\n"
        "v_add_f32 v155, v155, v60\n"
        "v_mfma_f32_16x16x32_bf16 v[68:71], v[180:183], v[92:95], v[68:71]\n"
        "v_perm_b32 v44, v49, v48, s63\n"
        "v_perm_b32 v45, v51, v50, s63\n"
        "v_add_f32 v155, v155, v61\n"
        "v_mfma_f32_16x16x32_bf16 v[28:31], v[192:195], v[92:95], 0\n"
        "v_mul_f32 v112, v112, v32\n"
        "v_mul_f32 v113, v113, v33\n"
        "v_mfma_f32_16x16x32_bf16 v[28:31], v[196:199], v[96:99], v[28:31]\n"
        "v_mul_f32 v114, v114, v34\n"
        "v_perm_b32 v46, v69, v68, s63\n"
        "v_mfma_f32_16x16x32_bf16 v[28:31], v[200:203], v[116:119], v[28:31]\n"
        "v_perm_b32 v47, v71, v70, s63\n"
        "v_mul_f32 v115, v115, v35\n"
        "v_exp_f32 v112, v112\n"
        "v_mfma_f32_16x16x32_bf16 v[68:71], v[212:215], v[116:119], 0\n"
        "v_exp_f32 v113, v113\n"
        "v_mfma_f32_16x16x32_bf16 v[68:71], v[208:211], v[96:99], v[68:71]\n"
        "v_exp_f32 v114, v114\n"
        "v_mfma_f32_16x16x32_bf16 v[68:71], v[204:207], v[92:95], v[68:71]\n"
        "v_perm_b32 v48, v29, v28, s63\n"
        "v_perm_b32 v49, v31, v30, s63\n"
        "v_add_f32 v155, v155, v112\n"
        "v_mfma_f32_16x16x32_bf16 v[64:67], v[216:219], v[92:95], 0\n"
        "v_exp_f32 v115, v115\n"
        "v_mfma_f32_16x16x32_bf16 v[64:67], v[220:223], v[96:99], v[64:67]\n"
        "v_add_f32 v155, v155, v113\n"
        "v_add_f32 v155, v155, v114\n"
        "v_mfma_f32_16x16x32_bf16 v[64:67], v[224:227], v[116:119], v[64:67]\n"
        "v_perm_b32 v50, v69, v68, s63\n"
        "v_perm_b32 v51, v71, v70, s63\n"
        "v_add_f32 v155, v155, v115\n"
        "v_mul_f32 v20, v20, v36\n"
        "v_mfma_f32_16x16x32_bf16 v[28:31], v[236:239], v[116:119], 0\n"
        "v_mul_f32 v21, v21, v37\n"
        "v_mul_f32 v22, v22, v38\n"
        "v_mfma_f32_16x16x32_bf16 v[28:31], v[232:235], v[96:99], v[28:31]\n"
        "v_perm_b32 v150, v65, v64, s63\n"
        "v_perm_b32 v151, v67, v66, s63\n"
        "v_mfma_f32_16x16x32_bf16 v[28:31], v[228:231], v[92:95], v[28:31]\n"
        "v_exp_f32 v20, v20\n"
        "v_mfma_f32_16x16x4_4b_bf16 v[76:91], v[56:57], v[158:159], 0 blgp:4\n"
        "v_exp_f32 v21, v21\n"
        "v_mul_f32 v23, v23, v39\n"
        "v_exp_f32 v22, v22\n"
        "v_exp_f32 v23, v23\n"
        "v_add_f32 v155, v155, v20\n"
        "v_add_f32 v155, v155, v21\n"
        "v_perm_b32 v152, v29, v28, s63\n"
        "v_perm_b32 v153, v31, v30, s63\n"
        "v_mfma_f32_16x16x32_bf16 v[36:39], v[124:127], v[44:47], 0\n"
        "v_add_f32 v155, v155, v22\n"
        "v_add_f32 v155, v155, v23\n"
        "v_mfma_f32_16x16x32_bf16 v[36:39], v[128:131], v[48:51], v[36:39]\n"
        "v_mul_f32 v100, v100, v76\n"
        "v_mul_f32 v101, v101, v77\n"
        "v_mfma_f32_16x16x32_bf16 v[36:39], v[160:163], v[150:153], v[36:39]\n"
        "v_mul_f32 v102, v102, v78\n"
        "v_mul_f32 v103, v103, v79\n"
        "v_mul_f32 v144, v144, v80\n"
        "v_mul_f32 v145, v145, v81\n"
        "v_mfma_f32_16x16x32_bf16 v[140:143], v[52:55], v[150:153], 0\n"
        "v_mul_f32 v146, v146, v82\n"
        "v_mul_f32 v147, v147, v83\n"
        "v_mfma_f32_16x16x32_bf16 v[140:143], v[136:139], v[48:51], v[140:143]\n"
        "v_mul_f32 v24, v24, v84\n"
        "v_mul_f32 v25, v25, v85\n"
        "v_mfma_f32_16x16x32_bf16 v[140:143], v[132:135], v[44:47], v[140:143]\n"
        "v_add_f32 v62, v62, v63\n"
        "v_mul_f32 v62, v62, v40\n"
        "v_mul_f32 v26, v26, v86\n"
        "v_mfma_f32_16x16x4_4b_bf16 v[64:79], v[56:57], v[164:165], 0 blgp:6\n"
        "v_exp_f32 v62, v62\n"
        "v_mul_f32 v27, v27, v87\n"
        "v_fma_f32 v148, v148, v88, v19\n"
        "v_add_f32_dpp v155, v62, v155 quad_perm:[0,1,2,3] row_mask:0x4 bank_mask:0xf\n"
        "v_perm_b32 v40, v101, v100, s63\n"
        "v_perm_b32 v41, v103, v102, s63\n"
        "v_perm_b32 v42, v145, v144, s63\n"
        "v_perm_b32 v43, v147, v146, s63\n"
        "v_perm_b32 v164, v25, v24, s63\n"
        "v_mfma_f32_16x16x32_bf16 v[156:159], v[92:95], v[44:47], 0\n"
        "v_perm_b32 v166, v27, v26, s63\n"
        "v_perm_b32 v165, 0, v148, s63\n"
        "v_mfma_f32_16x16x32_bf16 v[156:159], v[96:99], v[48:51], v[156:159]\n"
        "v_mul_f32 v36, v36, v64\n"
        "v_mul_f32 v37, v37, v65\n"
        "v_mfma_f32_16x16x32_bf16 v[156:159], v[116:119], v[150:153], v[156:159]\n"
        "v_mul_f32 v38, v38, v66\n"
        "v_mul_f32 v39, v39, v67\n"
        "v_mul_f32 v140, v140, v68\n"
        "v_mfma_f32_16x16x32_bf16 v[92:95], v[104:107], v[150:153], 0\n"
        "ds_read_b128 v[88:91], v17 offset:40064\n"
        "v_mul_f32 v141, v141, v69\n"
        "v_smfmac_f32_16x16x64_bf16 v[92:95], v[108:111], v[44:51], v4\n"
        "ds_read_b128 v[84:87], v17 offset:40128\n"
        "ds_read_b128 v[150:153], v17 offset:40192\n"
        "v_mfma_f32_16x16x32_bf16 v[48:51], v[124:127], v[240:243], 0\n"
        "v_exp_f32 v36, v36\n"
        "v_mfma_f32_16x16x32_bf16 v[48:51], v[128:131], v[244:247], v[48:51]\n"
        "ds_read_b128 v[80:83], v0 offset:5136\n"
        "v_mul_f32 v142, v142, v70\n"
        "v_mfma_f32_16x16x32_bf16 v[48:51], v[160:163], v[248:251], v[48:51]\n"
        "ds_read_b128 v[32:35], v0 offset:4112\n"
        "v_mul_f32 v143, v143, v71\n"
        "v_mfma_f32_16x16x32_bf16 v[24:27], v[52:55], v[248:251], 0\n"
        "v_exp_f32 v37, v37\n"
        "v_mfma_f32_16x16x32_bf16 v[24:27], v[136:139], v[244:247], v[24:27]\n"
        "v_exp_f32 v38, v38\n"
        "v_mfma_f32_16x16x32_bf16 v[24:27], v[132:135], v[240:243], v[24:27]\n"
        "s_waitcnt lgkmcnt(2)\n"
        "v_perm_b32 v44, v49, v48, s63\n"
        "v_perm_b32 v45, v51, v50, s63\n"
        "v_add_f32 v145, v36, v37\n"
        "v_mfma_f32_16x16x32_bf16 v[104:107], v[88:91], v[240:243], 0\n"
        "v_exp_f32 v39, v39\n"
        "v_mfma_f32_16x16x32_bf16 v[104:107], v[84:87], v[244:247], v[104:107]\n"
        "v_add_f32 v145, v145, v38\n"
        "v_perm_b32 v46, v25, v24, s63\n"
        "v_mfma_f32_16x16x32_bf16 v[104:107], v[150:153], v[248:251], v[104:107]\n"
        "s_waitcnt lgkmcnt(0)\n"
        "v_perm_b32 v47, v27, v26, s63\n"
        "v_add_f32 v145, v145, v39\n"
        "v_exp_f32 v140, v140\n"
        "v_mfma_f32_16x16x32_bf16 v[64:67], v[150:153], v[80:83], 0\n"
        "v_exp_f32 v141, v141\n"
        "v_mfma_f32_16x16x32_bf16 v[64:67], v[84:87], v[32:35], v[64:67]\n"
        "ds_read_b128 v[146:149], v0 offset:6176\n"
        "v_perm_b32 v116, v105, v104, s63\n"
        "v_mfma_f32_16x16x32_bf16 v[64:67], v[88:91], v[252:255], v[64:67]\n"
        "ds_read_b128 v[24:27], v0 offset:7200\n"
        "ds_read_b128 v[108:111], v0 offset:8224\n"
        "v_mfma_f32_16x16x32_bf16 v[100:103], v[132:135], v[252:255], 0\n"
        "v_perm_b32 v120, v107, v106, s63\n"
        "v_add_f32 v145, v145, v140\n"
        "v_mfma_f32_16x16x32_bf16 v[100:103], v[136:139], v[32:35], v[100:103]\n"
        "v_exp_f32 v142, v142\n"
        "v_mfma_f32_16x16x32_bf16 v[100:103], v[52:55], v[80:83], v[100:103]\n"
        "v_perm_b32 v94, v65, v64, s63\n"
        "v_perm_b32 v98, v67, v66, s63\n"
        "v_add_f32 v145, v145, v141\n"
        "v_mfma_f32_16x16x32_bf16 v[68:71], v[160:163], v[80:83], 0\n"
        "v_exp_f32 v143, v143\n"
        "v_mfma_f32_16x16x32_bf16 v[68:71], v[128:131], v[32:35], v[68:71]\n"
        "v_add_f32 v145, v145, v142\n"
        "v_add_f32 v145, v145, v143\n"
        "v_mfma_f32_16x16x32_bf16 v[68:71], v[124:127], v[252:255], v[68:71]\n"
        "s_waitcnt lgkmcnt(0)\n"
        "v_perm_b32 v106, v101, v100, s63\n"
        "v_perm_b32 v107, v103, v102, s63\n"
        "v_mul_f32 v156, v156, v72\n"
        "v_mul_f32 v157, v157, v73\n"
        "v_mfma_f32_16x16x32_bf16 v[32:35], v[124:127], v[146:149], 0\n"
        "v_mul_f32 v158, v158, v74\n"
        "v_mul_f32 v159, v159, v75\n"
        "v_mfma_f32_16x16x32_bf16 v[32:35], v[128:131], v[24:27], v[32:35]\n"
        "ds_read_b128 v[48:51], v0 offset:11312\n"
        "v_perm_b32 v104, v69, v68, s63\n"
        "v_mfma_f32_16x16x32_bf16 v[32:35], v[160:163], v[108:111], v[32:35]\n"
        "ds_read_b128 v[80:83], v0 offset:10288\n"
        "ds_read_b128 v[64:67], v0 offset:9264\n"
        "v_mfma_f32_16x16x32_bf16 v[100:103], v[52:55], v[108:111], 0\n"
        "v_perm_b32 v105, v71, v70, s63\n"
        "v_add_f32 v92, v92, v93\n"
        "v_mfma_f32_16x16x32_bf16 v[100:103], v[136:139], v[24:27], v[100:103]\n"
        "v_exp_f32 v156, v156\n"
        "v_mfma_f32_16x16x32_bf16 v[100:103], v[132:135], v[146:149], v[100:103]\n"
        "v_perm_b32 v28, v33, v32, s63\n"
        "v_perm_b32 v29, v35, v34, s63\n"
        "v_add_f32 v145, v145, v156\n"
        "v_mfma_f32_16x16x32_bf16 v[70:73], v[88:91], v[146:149], 0\n"
        "v_exp_f32 v157, v157\n"
        "v_mfma_f32_16x16x32_bf16 v[70:73], v[84:87], v[24:27], v[70:73]\n"
        "v_exp_f32 v158, v158\n"
        "v_mfma_f32_16x16x32_bf16 v[70:73], v[150:153], v[108:111], v[70:73]\n"
        "s_waitcnt lgkmcnt(0)\n"
        "v_perm_b32 v30, v101, v100, s63\n"
        "v_perm_b32 v31, v103, v102, s63\n"
        "v_exp_f32 v159, v159\n"
        "v_mfma_f32_16x16x32_bf16 v[32:35], v[150:153], v[48:51], 0\n"
        "v_add_f32 v145, v145, v157\n"
        "v_add_f32 v145, v145, v158\n"
        "v_mfma_f32_16x16x32_bf16 v[32:35], v[84:87], v[80:83], v[32:35]\n"
        "ds_read_b128 v[100:103], v0 offset:12352\n"
        "v_perm_b32 v144, v71, v70, s63\n"
        "v_mfma_f32_16x16x32_bf16 v[32:35], v[88:91], v[64:67], v[32:35]\n"
        "ds_read_b128 v[108:111], v0 offset:13376\n"
        "ds_read_b128 v[24:27], v0 offset:14400\n"
        "v_mfma_f32_16x16x32_bf16 v[68:71], v[132:135], v[64:67], 0\n"
        "v_perm_b32 v148, v73, v72, s63\n"
        "v_mul_f32 v92, v92, v76\n"
        "v_mfma_f32_16x16x32_bf16 v[68:71], v[136:139], v[80:83], v[68:71]\n"
        "v_add_f32 v145, v145, v159\n"
        "v_readlane_b32 s59, v123, 32\n"
        "v_mfma_f32_16x16x32_bf16 v[68:71], v[52:55], v[48:51], v[68:71]\n"
        "v_perm_b32 v117, v33, v32, s63\n"
        "v_perm_b32 v121, v35, v34, s63\n"
        "v_exp_f32 v92, v92\n"
        "v_mfma_f32_16x16x32_bf16 v[72:75], v[160:163], v[48:51], 0\n"
        "v_mul_f32 v154, s59, v154\n"
        "v_add_f32_dpp v145, v92, v145 quad_perm:[0,1,2,3] row_mask:0x4 bank_mask:0xf\n"
        "v_mfma_f32_16x16x32_bf16 v[72:75], v[128:131], v[80:83], v[72:75]\n"
        "v_perm_b32 v154, 0, v154, s63\n"
        "v_perm_b32 v50, v69, v68, s63\n"
        "v_mfma_f32_16x16x32_bf16 v[72:75], v[124:127], v[64:67], v[72:75]\n"
        "s_waitcnt lgkmcnt(0)\n"
        "v_perm_b32 v51, v71, v70, s63\n"
        "v_permlane32_swap_b32 v155, v145\n"
        "v_mfma_f32_16x16x32_bf16 v[80:83], v[124:127], v[100:103], 0\n"
        "v_add_f32 v155, v155, v145\n"
        "v_mov_b32 v149, v155\n"
        "v_mfma_f32_16x16x32_bf16 v[80:83], v[128:131], v[108:111], v[80:83]\n"
        "ds_read_b128 v[32:35], v0 offset:17488\n"
        "v_perm_b32 v48, v73, v72, s63\n"
        "v_mfma_f32_16x16x32_bf16 v[80:83], v[160:163], v[24:27], v[80:83]\n"
        "ds_read_b128 v[68:71], v0 offset:16464\n"
        "ds_read_b128 v[64:67], v0 offset:15440\n"
        "v_mfma_f32_16x16x32_bf16 v[76:79], v[52:55], v[24:27], 0\n"
        "v_perm_b32 v49, v75, v74, s63\n"
        "v_mfma_f32_16x16x32_bf16 v[76:79], v[136:139], v[108:111], v[76:79]\n"
        "v_permlane16_swap_b32 v155, v149\n"
        "v_mfma_f32_16x16x32_bf16 v[76:79], v[132:135], v[100:103], v[76:79]\n"
        "v_add_f32 v155, v155, v149\n"
        "v_rcp_f32 v147, v155\n"
        "v_mfma_f32_16x16x32_bf16 v[72:75], v[88:91], v[100:103], 0\n"
        "ds_read2_b32 v[122:123], v11 offset0:0 offset1:3\n"
        "v_min3_f32 v167, v167, v155, v147\n"
        "v_mfma_f32_16x16x32_bf16 v[72:75], v[84:87], v[108:111], v[72:75]\n"
        "ds_read_b64_tr_b16 v[118:119], v9 offset:40064\n"
        "v_and_b32 v102, s28, v147\n"
        "v_mfma_f32_16x16x32_bf16 v[72:75], v[150:153], v[24:27], v[72:75]\n"
        "s_waitcnt lgkmcnt(2)\n"
        "ds_read2_b32 v[100:101], v11 offset0:1 offset1:4\n"
        "v_sub_f32 v149, v147, v102\n"
        "v_cvt_pk_bf16_f32 v103, v102, v149\n"
        "v_perm_b32 v108, v81, v80, s63\n"
        "v_mfma_f32_16x16x32_bf16 v[24:27], v[150:153], v[32:35], 0\n"
        "ds_read_b64_tr_b16 v[96:97], v9 offset:40080\n"
        "v_perm_b32 v109, v83, v82, s63\n"
        "v_mfma_f32_16x16x32_bf16 v[24:27], v[84:87], v[68:71], v[24:27]\n"
        "ds_read2_b32 v[150:151], v11 offset0:2 offset1:5\n"
        "v_perm_b32 v110, v77, v76, s63\n"
        "v_mfma_f32_16x16x32_bf16 v[24:27], v[88:91], v[64:67], v[24:27]\n"
        "ds_read_b64_tr_b16 v[146:147], v9 offset:40096\n"
        "v_perm_b32 v111, v79, v78, s63\n"
        "v_perm_b32 v95, v73, v72, s63\n"
        "v_mfma_f32_16x16x4_4b_bf16 v[76:91], v[56:57], v[102:103], 0 blgp:4\n"
        "v_perm_b32 v99, v75, v74, s63\n"
        "v_cmp_nle_f32 vcc, 0x2b800000, v167\n"
        "v_mov_b32_dpp v167, v19 row_shr:4 row_mask:0xf bank_mask:0xf bound_ctrl:0\n"
        "ds_write_b16 v1, v154\n"
        "v_perm_b32 v145, v25, v24, s63\n"
        "v_perm_b32 v149, v27, v26, s63\n"
        "s_andn2_b64 vcc, vcc, s[56:57]\n"
        "s_cbranch_scc1 CREDOB_%=\n"
        "v_mfma_f32_16x16x32_bf16 v[24:27], v[132:135], v[64:67], 0\n"
        "s_mov_b32 exec_hi, 0xffff\n"
        "v_add_u32 v10, -40064, v10\n"
        "s_mov_b32 exec_hi, -1\n"
        "v_mfma_f32_16x16x32_bf16 v[24:27], v[136:139], v[68:71], v[24:27]\n"
        "v_mul_f32 v58, v58, v76\n"
        "v_mul_f32 v59, v59, v77\n"
        "v_mfma_f32_16x16x32_bf16 v[24:27], v[52:55], v[32:35], v[24:27]\n"
        "s_add_u32 s31, s31, 1\n"
        "v_mul_f32 v60, v60, v78\n"
        "v_mul_f32 v61, v61, v79\n"
        "v_mul_f32 v112, v112, v80\n"
        "v_mul_f32 v113, v113, v81\n"
        "v_mfma_f32_16x16x32_bf16 v[52:55], v[160:163], v[32:35], 0\n"
        "s_cmp_ge_u32 s31, s71\n"
        "v_mul_f32 v114, v114, v82\n"
        "v_mul_f32 v115, v115, v83\n"
        "v_mfma_f32_16x16x32_bf16 v[52:55], v[128:131], v[68:71], v[52:55]\n"
        "s_cbranch_scc1 L34B_%=\n"
        "L35B_%=:\n"
        "v_mul_f32 v20, v20, v84\n"
        "v_perm_b32 v34, v25, v24, s63\n"
        "v_mfma_f32_16x16x32_bf16 v[52:55], v[124:127], v[64:67], v[52:55]\n"
        "s_add_u32 s16, s16, 256\n"
        "v_perm_b32 v35, v27, v26, s63\n"
        "v_mul_f32 v21, v21, v85\n"
        "v_mfma_f32_16x16x32_bf16 v[124:127], v[40:43], v[44:47], 0\n"
        "s_waitcnt lgkmcnt(0)\n"
        "v_mul_f32 v22, v22, v86\n"
        "v_mul_f32 v23, v23, v87\n"
        "v_smfmac_f32_16x16x64_bf16 v[124:127], v[164:167], v[116:123], v18\n"
        "s_cmp_ge_u32 s31, s43\n"
        "s_cselect_b32 s39, s22, s16\n"
        "ds_read_b64_tr_b16 v[118:119], v9 offset:40112\n"
        "v_mfma_f32_16x16x4_4b_bf16 v[72:87], v[56:57], v[102:103], 0 blgp:6\n"
        "v_perm_b32 v32, v53, v52, s63\n"
        "v_perm_b32 v33, v55, v54, s63\n"
        "v_fma_f32 v62, v62, v88, v19\n"
        "v_perm_b32 v68, v59, v58, s63\n"
        "v_perm_b32 v69, v61, v60, s63\n"
        "v_perm_b32 v70, v113, v112, s63\n"
        "v_perm_b32 v71, v115, v114, s63\n"
        "v_perm_b32 v24, v21, v20, s63\n"
        "v_perm_b32 v26, v23, v22, s63\n"
        "v_mfma_f32_16x16x32_bf16 v[88:91], v[40:43], v[104:107], 0\n"
        "s_cmp_gt_u32 s31, s43\n"
        "v_perm_b32 v25, 0, v62, s63\n"
        "v_mul_f32 v36, v36, v72\n"
        "v_smfmac_f32_16x16x64_bf16 v[88:91], v[164:167], v[94:101], v18\n"
        "s_cselect_b32 s39, s23, s39\n"
        "ds_read_b64_tr_b16 v[96:97], v9 offset:40128\n"
        "v_mfma_f32_16x16x32_bf16 v[56:59], v[40:43], v[28:31], 0\n"
        "s_mul_hi_u32 s38, s39, 58835169\n"
        "ds_read_b128 v[128:131], v2\n"
        "v_mul_f32 v37, v37, v73\n"
        "v_smfmac_f32_16x16x64_bf16 v[56:59], v[164:167], v[144:151], v18\n"
        "s_mul_i32 s39, s39, 1344\n"
        "ds_read_b64_tr_b16 v[146:147], v9 offset:40144\n"
        "ds_read_b128 v[52:55], v3\n"
        "v_mfma_f32_16x16x32_bf16 v[112:115], v[40:43], v[48:51], 0\n"
        "s_waitcnt lgkmcnt(4)\n"
        "v_mul_f32 v38, v38, v74\n"
        "v_mul_f32 v39, v39, v75\n"
        "v_smfmac_f32_16x16x64_bf16 v[112:115], v[164:167], v[116:123], v18 abid:1\n"
        "s_mul_i32 s38, s38, 588672\n"
        "s_add_u32 s39, s39, s38\n"
        "v_mul_f32 v140, v140, v76\n"
        "v_cvt_pk_bf16_f32 v152, v124, v88\n"
        "v_mul_f32 v141, v141, v77\n"
        "v_mfma_f32_16x16x32_bf16 v[72:75], v[40:43], v[108:111], 0\n"
        "s_add_u32 s39, s39, s17\n"
        "v_mov_b64 v[60:61], 0\n"
        "s_waitcnt lgkmcnt(3)\n"
        "v_smfmac_f32_16x16x64_bf16 v[72:75], v[164:167], v[94:101], v18 abid:1\n"
        "s_mul_i32 s68, s31, 3\n"
        "v_mov_b64 v[62:63], 0\n"
        "v_cvt_pk_bf16_f32 v153, v56, v112\n"
        "v_mfma_f32_16x16x32_bf16 v[64:67], v[40:43], v[32:35], 0\n"
        "s_waitcnt lgkmcnt(1)\n"
        "v_mov_b64 v[20:21], 0\n"
        "v_mov_b64 v[22:23], 0\n"
        "v_smfmac_f32_16x16x64_bf16 v[64:67], v[164:167], v[144:151], v18 abid:1\n"
        "s_waitcnt lgkmcnt(0)\n"
        "s_bitcmp1_b32 s31, 0\n"
        "v_mov_b32_dpp v27, v19 row_shr:4 row_mask:0xf bank_mask:0xf bound_ctrl:0\n"
        "s_cselect_b32 s38, 3, 6\n"
        "v_smfmac_f32_16x16x64_bf16 v[60:63], v[128:131], v[44:51], v4\n"
        "s_add_u32 s68, s68, s38\n"
        "v_mov_b64 v[40:41], 0\n"
        "v_smfmac_f32_16x16x64_bf16 v[60:63], v[52:55], v[116:123], v4 abid:1\n"
        "s_add_u32 s37, s35, 0\n"
        "s_cmp_lt_u32 s31, s14\n"
        "ds_read_b64_tr_b16 v[118:119], v9 offset:43392\n"
        "v_mov_b64 v[42:43], 0\n"
        "v_smfmac_f32_16x16x64_bf16 v[20:23], v[128:131], v[104:111], v4\n"
        "s_cselect_b32 s68, s68, 0\n"
        "v_cvt_pk_bf16_f32 v154, v72, v64\n"
        "v_smfmac_f32_16x16x64_bf16 v[20:23], v[52:55], v[94:101], v4 abid:1\n"
        "s_and_b32 s38, s31, 7\n"
        "ds_read_b64_tr_b16 v[96:97], v9 offset:43408\n"
        "v_cvt_pk_bf16_f32 v160, v125, v89\n"
        "v_smfmac_f32_16x16x64_bf16 v[40:43], v[128:131], v[28:35], v4\n"
        "s_cselect_b32 s30, 1, -7\n"
        "v_cvt_pk_bf16_f32 v88, v126, v90\n"
        "v_smfmac_f32_16x16x64_bf16 v[40:43], v[52:55], v[144:151], v4 abid:1\n"
        "buffer_store_dwordx3 v[152:154], v12, s[8:11], s37 offen sc0 sc1 nt\n"
        "ds_read_b64_tr_b16 v[146:147], v9 offset:43424\n"
        "v_cvt_pk_bf16_f32 v164, v127, v91\n"
        "v_cvt_pk_bf16_f32 v161, v57, v113\n"
        "v_cvt_pk_bf16_f32 v89, v58, v114\n"
        "v_cvt_pk_bf16_f32 v165, v59, v115\n"
        "v_cvt_pk_bf16_f32 v162, v73, v65\n"
        "v_mfma_f32_16x16x32_bf16 v[152:155], v[68:71], v[44:47], 0\n"
        "s_waitcnt lgkmcnt(2)\n"
        "v_cvt_pk_bf16_f32 v90, v74, v66\n"
        "v_cvt_pk_bf16_f32 v166, v75, v67\n"
        "v_smfmac_f32_16x16x64_bf16 v[152:155], v[24:27], v[116:123], v18\n"
        "s_mul_i32 s29, s30, 192\n"
        "s_mul_i32 s30, s30, 392\n"
        "ds_read_b64_tr_b16 v[118:119], v9 offset:43440\n"
        "v_add_f32 v60, v60, v61\n"
        "v_add_f32 v62, v62, v63\n"
        "v_add_f32 v20, v20, v21\n"
        "v_mfma_f32_16x16x32_bf16 v[56:59], v[68:71], v[104:107], 0\n"
        "s_waitcnt lgkmcnt(2)\n"
        "v_add_f32 v22, v22, v23\n"
        "v_cvt_pk_bf16_f32 v112, v60, v20\n"
        "v_smfmac_f32_16x16x64_bf16 v[56:59], v[24:27], v[94:101], v18\n"
        "s_add_u32 s37, s35, 0\n"
        "s_cmp_eq_u32 s69, s18\n"
        "ds_read_b64_tr_b16 v[96:97], v9 offset:43456\n"
        "v_add_f32 v40, v40, v41\n"
        "v_add_f32 v42, v42, v43\n"
        "v_cvt_pk_bf16_f32 v113, v40, v62\n"
        "v_mfma_f32_16x16x32_bf16 v[72:75], v[68:71], v[28:31], 0\n"
        "s_waitcnt lgkmcnt(2)\n"
        "v_cvt_pk_bf16_f32 v114, v22, v42\n"
        "v_mul_f32 v142, v142, v78\n"
        "v_smfmac_f32_16x16x64_bf16 v[72:75], v[24:27], v[144:151], v18\n"
        "buffer_store_dwordx3 v[160:162], v15, s[8:11], s37 offen offset:768 sc0 sc1 nt\n"
        "ds_read_b64_tr_b16 v[146:147], v9 offset:43472\n"
        "s_cselect_b32 s34, -7488, s70\n"
        "v_mul_f32 v143, v143, v79\n"
        "v_mul_f32 v156, v156, v80\n"
        "v_mul_f32 v157, v157, v81\n"
        "v_mul_f32 v158, v158, v82\n"
        "v_mfma_f32_16x16x32_bf16 v[20:23], v[68:71], v[48:51], 0\n"
        "s_waitcnt lgkmcnt(2)\n"
        "s_add_u32 s69, s69, s34\n"
        "v_mul_f32 v159, v159, v83\n"
        "v_smfmac_f32_16x16x64_bf16 v[20:23], v[24:27], v[116:123], v18 abid:1\n"
        "s_add_u32 s37, s35, 98112\n"
        "ds_read_b64_tr_b16 v[118:119], v9 offset:46720\n"
        "v_fma_f32 v92, v92, v84, v19\n"
        "v_mfma_f32_16x16x32_bf16 v[84:87], v[68:71], v[108:111], 0\n"
        "s_waitcnt lgkmcnt(2)\n"
        "v_perm_b32 v80, v37, v36, s63\n"
        "v_perm_b32 v81, v39, v38, s63\n"
        "v_smfmac_f32_16x16x64_bf16 v[84:87], v[24:27], v[94:101], v18 abid:1\n"
        "ds_read_b64_tr_b16 v[96:97], v9 offset:46736\n"
        "v_perm_b32 v82, v141, v140, s63\n"
        "v_mfma_f32_16x16x32_bf16 v[60:63], v[68:71], v[32:35], 0\n"
        "s_waitcnt lgkmcnt(2)\n"
        "v_perm_b32 v83, v143, v142, s63\n"
        "v_perm_b32 v76, v157, v156, s63\n"
        "v_smfmac_f32_16x16x64_bf16 v[60:63], v[24:27], v[144:151], v18 abid:1\n"
        "buffer_store_dwordx3 v[88:90], v12, s[8:11], s37 offen offset:192 sc0 sc1 nt\n"
        "ds_read_b64_tr_b16 v[146:147], v9 offset:46752\n"
        "ds_read_b32 v167, v0 offset:21600\n"
        "v_perm_b32 v78, v159, v158, s63\n"
        "v_perm_b32 v77, 0, v92, s63\n"
        "v_mov_b32_dpp v79, v19 row_shr:4 row_mask:0xf bank_mask:0xf bound_ctrl:0\n"
        "v_mfma_f32_16x16x32_bf16 v[68:71], v[80:83], v[44:47], 0\n"
        "s_waitcnt lgkmcnt(3)\n"
        "v_writelane_b32 v10, s69, 56\n"
        "v_add_u32 v11, s30, v11\n"
        "v_smfmac_f32_16x16x64_bf16 v[68:71], v[76:79], v[116:123], v18\n"
        "s_add_u32 s37, s35, 98112\n"
        "ds_read_b64_tr_b16 v[118:119], v9 offset:46768\n"
        "v_mfma_f32_16x16x32_bf16 v[36:39], v[80:83], v[104:107], 0\n"
        "s_waitcnt lgkmcnt(3)\n"
        "v_smfmac_f32_16x16x64_bf16 v[36:39], v[76:79], v[94:101], v18\n"
        "ds_read_b64_tr_b16 v[96:97], v9 offset:46784\n"
        "v_mfma_f32_16x16x32_bf16 v[40:43], v[80:83], v[28:31], 0\n"
        "s_waitcnt lgkmcnt(3)\n"
        "s_waitcnt lgkmcnt(2)\n"
        "v_readfirstlane_b32 s40, v167\n"
        "v_smfmac_f32_16x16x64_bf16 v[40:43], v[76:79], v[144:151], v18\n"
        "buffer_store_dwordx3 v[164:166], v14, s[8:11], s37 offen offset:960 sc0 sc1 nt\n"
        "ds_read_b64_tr_b16 v[146:147], v9 offset:46800\n"
        "s_add_u32 s37, s35, 588672\n"
        "s_add_u32 s46, s37, 1344\n"
        "s_cmp_lt_i32 s40, s68\n"
        "s_mov_b64 exec, 1\n"
        "ds_inc_u32 v0, v1 offset:21612\n"
        "s_mov_b64 exec, -1\n"
        "s_cbranch_scc1 L38B_%=\n"
        "L39B_%=:\n"
        "ds_read_b128 v[124:127], v7 offset:0\n"
        "v_mfma_f32_16x16x32_bf16 v[64:67], v[80:83], v[48:51], 0\n"
        "s_waitcnt lgkmcnt(4)\n"
        "ds_read_b128 v[132:135], v7 offset:3328\n"
        "v_smfmac_f32_16x16x64_bf16 v[64:67], v[76:79], v[116:123], v18 abid:1\n"
        "buffer_store_dwordx3 v[112:114], v12, s[44:47], s37 offen offset:1152 sc0 sc1 nt\n"
        "ds_read_b128 v[128:131], v7 offset:64\n"
        "ds_read_b128 v[136:139], v7 offset:3392\n"
        "ds_read_b128 v[160:163], v7 offset:128\n"
        "ds_read_b128 v[52:55], v7 offset:3456\n"
        "v_mfma_f32_16x16x32_bf16 v[104:107], v[80:83], v[108:111], 0\n"
        "s_waitcnt lgkmcnt(7)\n"
        "s_mov_b32 s36, s35\n"
        "v_smfmac_f32_16x16x64_bf16 v[104:107], v[76:79], v[94:101], v18 abid:1\n"
        "s_mov_b64 exec, s[54:55]\n"
        "v_add_u32 v16, s29, v16\n"
        "v_add_u32 v8, s34, v8\n"
        "s_mov_b64 exec, -1\n"
        "v_mfma_f32_16x16x32_bf16 v[88:91], v[80:83], v[32:35], 0\n"
        "s_mov_b32 s35, s39\n"
        "v_smfmac_f32_16x16x64_bf16 v[88:91], v[76:79], v[144:151], v18 abid:1\n"
        "s_mov_b64 exec, s[50:51]\n"
        "v_add_u32 v5, s29, v5\n"
        "v_add_u32 v6, s34, v6\n"
        "s_mov_b64 exec, -1\n"
        "s_setprio 0\n"
        "CNEXTB_%=:\n"
        "s_cmp_ge_u32 s31, s14\n"
        "s_cbranch_scc1 CEND_%=\n"
        "s_cmp_gt_i32 s29, 0\n"
        "s_cbranch_scc1 CTOP_%=\n"
        "s_branch CFULL_%=\n"
        "CEND_%=:\n"
        "s_waitcnt lgkmcnt(0)\n"
        "s_nop 15\n"
        "v_cvt_pk_bf16_f32 v248, v152, v56\n"
        "v_cvt_pk_bf16_f32 v249, v72, v20\n"
        "v_cvt_pk_bf16_f32 v250, v84, v60\n"
        "s_add_u32 s37, s36, 196224\n"
        "buffer_store_dwordx3 v[248:250], v12, s[8:11], s37 offen offset:384 sc0 sc1 nt\n"
        "v_cvt_pk_bf16_f32 v252, v153, v57\n"
        "v_cvt_pk_bf16_f32 v253, v73, v21\n"
        "v_cvt_pk_bf16_f32 v254, v85, v61\n"
        "s_add_u32 s37, s36, 196224\n"
        "buffer_store_dwordx3 v[252:254], v13, s[8:11], s37 offen offset:1152 sc0 sc1 nt\n"
        "v_cvt_pk_bf16_f32 v248, v154, v58\n"
        "v_cvt_pk_bf16_f32 v249, v74, v22\n"
        "v_cvt_pk_bf16_f32 v250, v86, v62\n"
        "s_add_u32 s37, s36, 294336\n"
        "buffer_store_dwordx3 v[248:250], v12, s[8:11], s37 offen offset:576 sc0 sc1 nt\n"
        "v_cvt_pk_bf16_f32 v252, v155, v59\n"
        "v_cvt_pk_bf16_f32 v253, v75, v23\n"
        "v_cvt_pk_bf16_f32 v254, v87, v63\n"
        "s_add_u32 s37, s36, 392448\n"
        "buffer_store_dwordx3 v[252:254], v12, s[8:11], s37 offen sc0 sc1 nt\n"
        "v_cvt_pk_bf16_f32 v248, v68, v36\n"
        "v_cvt_pk_bf16_f32 v249, v40, v64\n"
        "v_cvt_pk_bf16_f32 v250, v104, v88\n"
        "s_add_u32 s37, s36, 392448\n"
        "v_cvt_pk_bf16_f32 v252, v69, v37\n"
        "v_cvt_pk_bf16_f32 v253, v41, v65\n"
        "v_cvt_pk_bf16_f32 v254, v105, v89\n"
        "buffer_store_dwordx3 v[248:250], v15, s[8:11], s37 offen offset:768 sc0 sc1 nt\n"
        "s_add_u32 s37, s36, 490560\n"
        "v_cvt_pk_bf16_f32 v246, v70, v38\n"
        "v_cvt_pk_bf16_f32 v247, v42, v66\n"
        "v_cvt_pk_bf16_f32 v248, v106, v90\n"
        "buffer_store_dwordx3 v[252:254], v12, s[8:11], s37 offen offset:192 sc0 sc1 nt\n"
        "s_add_u32 s37, s36, 490560\n"
        "buffer_store_dwordx3 v[246:248], v14, s[8:11], s37 offen offset:960 sc0 sc1 nt\n"
        "s_add_u32 s37, s36, 588672\n"
        "v_cvt_pk_bf16_f32 v252, v71, v39\n"
        "v_cvt_pk_bf16_f32 v253, v43, v67\n"
        "v_cvt_pk_bf16_f32 v254, v107, v91\n"
        "buffer_store_dwordx3 v[252:254], v12, s[8:11], s37 offen offset:384 sc0 sc1 nt\n"
        "s_nop 1\n"
        "s_endpgm\n"
        "L34A_%=:\n"
        "s_nop 7\n"
        "v_mov_b32 v153, 21600\n"
        "L32A_%=:\n"
        "ds_read_b32 v163, v153 offset:64\n"
        "s_waitcnt lgkmcnt(0)\n"
        "v_readfirstlane_b32 s40, v163\n"
        "s_nop 3\n"
        "s_cmp_ge_i32 s40, 1\n"
        "s_cbranch_scc1 L33A_%=\n"
        "s_sleep 1\n"
        "s_sub_u32 s41, s41, 1\n"
        "s_cbranch_scc0 L32A_%=\n"
        "s_endpgm\n"
        "L33A_%=:\n"
        "v_mbcnt_lo_u32_b32 v163, -1, 0\n"
        "v_lshl_add_u32 v153, v163, 2, v153\n"
        "ds_read_b32 v163, v153 offset:68\n"
        "s_mov_b32 s14, s40\n"
        "s_mov_b32 s71, -1\n"
        "s_waitcnt lgkmcnt(0)\n"
        "v_readlane_b32 s22, v163, 0\n"
        "v_readlane_b32 s23, v163, 1\n"
        "s_nop 3\n"
        "s_nop 1\n"
        "s_branch L35A_%=\n"
        "L38A_%=:\n"
        "s_nop 7\n"
        "L36A_%=:\n"
        "ds_read_b32 v167, v0 offset:21604\n"
        "s_waitcnt lgkmcnt(0)\n"
        "v_readfirstlane_b32 s40, v167\n"
        "s_nop 3\n"
        "s_cmp_ge_i32 s40, s68\n"
        "s_cbranch_scc1 L37A_%=\n"
        "s_sleep 1\n"
        "s_sub_u32 s41, s41, 1\n"
        "s_cbranch_scc0 L36A_%=\n"
        "s_endpgm\n"
        "L37A_%=:\n"
        "s_nop 1\n"
        "s_branch L39A_%=\n"
        "L34B_%=:\n"
        "s_nop 7\n"
        "v_mov_b32 v153, 21600\n"
        "L32B_%=:\n"
        "ds_read_b32 v163, v153 offset:64\n"
        "s_waitcnt lgkmcnt(0)\n"
        "v_readfirstlane_b32 s40, v163\n"
        "s_nop 3\n"
        "s_cmp_ge_i32 s40, 1\n"
        "s_cbranch_scc1 L33B_%=\n"
        "s_sleep 1\n"
        "s_sub_u32 s41, s41, 1\n"
        "s_cbranch_scc0 L32B_%=\n"
        "s_endpgm\n"
        "L33B_%=:\n"
        "v_mbcnt_lo_u32_b32 v163, -1, 0\n"
        "v_lshl_add_u32 v153, v163, 2, v153\n"
        "ds_read_b32 v163, v153 offset:68\n"
        "s_mov_b32 s14, s40\n"
        "s_mov_b32 s71, -1\n"
        "s_waitcnt lgkmcnt(0)\n"
        "v_readlane_b32 s22, v163, 0\n"
        "v_readlane_b32 s23, v163, 1\n"
        "s_nop 3\n"
        "s_nop 1\n"
        "s_branch L35B_%=\n"
        "L38B_%=:\n"
        "s_nop 7\n"
        "L36B_%=:\n"
        "ds_read_b32 v167, v0 offset:21600\n"
        "s_waitcnt lgkmcnt(0)\n"
        "v_readfirstlane_b32 s40, v167\n"
        "s_nop 3\n"
        "s_cmp_ge_i32 s40, s68\n"
        "s_cbranch_scc1 L37B_%=\n"
        "s_sleep 1\n"
        "s_sub_u32 s41, s41, 1\n"
        "s_cbranch_scc0 L36B_%=\n"
        "s_endpgm\n"
        "L37B_%=:\n"
        "s_nop 1\n"
        "s_branch L39B_%=\n"
        "CGRP_%=:\n"
        "s_setprio 3\n"
        "CGRQ_%=:\n"
        "s_waitcnt lgkmcnt(0)\n"
        "s_nop 6\n"
        "ds_read_u16 v166, v0 offset:20576\n"
        "s_ashr_i32 s61, s31, 3\n"
        "s_add_u32 s61, s61, 1\n"
        "L40_%=:\n"
        "ds_read_b32 v167, v0 offset:21660\n"
        "s_waitcnt lgkmcnt(0)\n"
        "v_readfirstlane_b32 s40, v167\n"
        "s_nop 3\n"
        "s_cmp_ge_i32 s40, s61\n"
        "s_cbranch_scc1 L41_%=\n"
        "s_sleep 1\n"
        "s_sub_u32 s41, s41, 1\n"
        "s_cbranch_scc0 L40_%=\n"
        "s_endpgm\n"
        "L41_%=:\n"
        "s_sub_u32 s60, s69, 102048\n"
        "v_add_u32 v166, s60, v166\n"
        "v_subrev_u32 v165, 8320, v166\n"
        "v_min_u32 v166, v166, v165\n"
        "v_add_u32 v166, 101856, v166\n"
        "s_mov_b32 exec_lo, 0xff00ff00\n"
        "s_mov_b32 exec_hi, 0xff00ff00\n"
        "v_mov_b32 v166, 18912\n"
        "s_mov_b64 exec, -1\n"
        "ds_read_b128 v[28:31], v166 offset:64\n"
        "ds_read_b128 v[24:27], v166 offset:128\n"
        "ds_read_b128 v[32:35], v166\n"
        "s_waitcnt lgkmcnt(1)\n"
        "s_waitcnt lgkmcnt(0)\n"
        "v_mfma_f32_16x16x32_bf16 v[44:47], v[168:171], v[32:35], 0\n"
        "ds_read_u16 v162, v0 offset:20578\n"
        "v_lshrrev_b32 v157, 8, v0\n"
        "v_mfma_f32_16x16x32_bf16 v[44:47], v[172:175], v[28:31], v[44:47]\n"
        "ds_read_u16 v166, v0 offset:20580\n"
        "v_mfma_f32_16x16x32_bf16 v[44:47], v[176:179], v[24:27], v[44:47]\n"
        "ds_read_u16 v167, v0 offset:20582\n"
        "ds_read_u16 v163, v0 offset:20588\n"
        "ds_read_u16 v151, v0 offset:20590\n"
        "v_mfma_f32_16x16x32_bf16 v[76:79], v[180:183], v[32:35], 0\n"
        "v_mfma_f32_16x16x32_bf16 v[76:79], v[184:187], v[28:31], v[76:79]\n"
        "v_mfma_f32_16x16x32_bf16 v[76:79], v[188:191], v[24:27], v[76:79]\n"
        "v_mfma_f32_16x16x32_bf16 v[48:51], v[192:195], v[32:35], 0\n"
        "s_waitcnt lgkmcnt(4)\n"
        "v_add_u32 v162, s60, v162\n"
        "v_subrev_u32 v165, 8320, v162\n"
        "v_mfma_f32_16x16x32_bf16 v[48:51], v[196:199], v[28:31], v[48:51]\n"
        "v_min_u32 v162, v162, v165\n"
        "v_add_u32 v162, 101856, v162\n"
        "v_mfma_f32_16x16x32_bf16 v[48:51], v[200:203], v[24:27], v[48:51]\n"
        "s_mov_b32 exec_lo, 0\n"
        "v_mov_b32 v162, 18912\n"
        "s_mov_b32 exec_lo, -1\n"
        "ds_read_b64 v[160:161], v162\n"
        "v_perm_b32 v52, v45, v44, s63\n"
        "v_perm_b32 v53, v47, v46, s63\n"
        "v_perm_b32 v54, v77, v76, s63\n"
        "v_perm_b32 v55, v79, v78, s63\n"
        "v_mfma_f32_16x16x32_bf16 v[44:47], v[204:207], v[32:35], 0\n"
        "v_perm_b32 v80, v49, v48, s63\n"
        "v_perm_b32 v81, v51, v50, s63\n"
        "v_mfma_f32_16x16x32_bf16 v[44:47], v[208:211], v[28:31], v[44:47]\n"
        "s_waitcnt lgkmcnt(1)\n"
        "ds_read_b64 v[164:165], v163\n"
        "v_add_u32 v166, s19, v166\n"
        "v_mfma_f32_16x16x32_bf16 v[44:47], v[212:215], v[24:27], v[44:47]\n"
        "ds_read_b64 v[158:159], v162 offset:16\n"
        "v_mad_i32_i24 v157, v157, -8, v166\n"
        "v_add_u32 v151, s19, v151\n"
        "v_add_u32 v167, s19, v167\n"
        "v_mfma_f32_16x16x32_bf16 v[48:51], v[216:219], v[32:35], 0\n"
        "ds_read_b64 v[148:149], v162 offset:32\n"
        "v_mfma_f32_16x16x32_bf16 v[48:51], v[220:223], v[28:31], v[48:51]\n"
        "v_mfma_f32_16x16x32_bf16 v[48:51], v[224:227], v[24:27], v[48:51]\n"
        "s_mov_b32 exec_lo, 0x00ff00ff\n"
        "s_mov_b32 exec_hi, 0x00ff00ff\n"
        "ds_write2_b32 v157, v52, v54 offset0:0 offset1:1\n"
        "ds_write2_b32 v157, v53, v55 offset0:16 offset1:17\n"
        "s_mov_b64 exec, -1\n"
        "v_mfma_f32_16x16x32_bf16 v[76:79], v[228:231], v[32:35], 0\n"
        "v_perm_b32 v82, v45, v44, s63\n"
        "v_perm_b32 v83, v47, v46, s63\n"
        "v_mfma_f32_16x16x32_bf16 v[76:79], v[232:235], v[28:31], v[76:79]\n"
        "v_perm_b32 v44, v49, v48, s63\n"
        "v_perm_b32 v45, v51, v50, s63\n"
        "v_mfma_f32_16x16x32_bf16 v[76:79], v[236:239], v[24:27], v[76:79]\n"
        "s_waitcnt lgkmcnt(2)\n"
        "s_mov_b32 exec_lo, 0x00ff00ff\n"
        "s_mov_b32 exec_hi, 0x00ff00ff\n"
        "ds_write2_b32 v157, v80, v82 offset0:8 offset1:9\n"
        "ds_write2_b32 v157, v81, v83 offset0:24 offset1:25\n"
        "s_mov_b64 exec, -1\n"
        "v_mfma_f32_4x4x4_16b_bf16 v[100:103], v[160:161], v[164:165], 0\n"
        "v_mfma_f32_4x4x4_16b_bf16 v[92:95], v[158:159], v[164:165], 0\n"
        "v_mfma_f32_4x4x4_16b_bf16 v[48:51], v[148:149], v[164:165], 0\n"
        "v_mov_b32 v96, 0x3f803f80\n"
        "v_mov_b32 v97, 0x3f803f80\n"
        "v_mov_b32 v98, 0x3f803f80\n"
        "v_mov_b32 v99, 0x3f803f80\n"
        "v_mfma_f32_16x16x32_bf16 v[124:127], v[32:35], v[240:243], 0\n"
        "v_perm_b32 v46, v77, v76, s63\n"
        "v_perm_b32 v47, v79, v78, s63\n"
        "v_mfma_f32_16x16x32_bf16 v[124:127], v[28:31], v[244:247], v[124:127]\n"
        "ds_read_b128 v[120:123], v0 offset:4112\n"
        "v_mfma_f32_16x16x32_bf16 v[124:127], v[24:27], v[248:251], v[124:127]\n"
        "ds_read_b128 v[108:111], v0 offset:5136\n"
        "v_mfma_f32_16x16x32_bf16 v[76:79], v[96:99], v[32:35], 0\n"
        "v_mfma_f32_16x16x32_bf16 v[76:79], v[96:99], v[28:31], v[76:79]\n"
        "ds_read_b128 v[116:119], v0 offset:6176\n"
        "v_mfma_f32_16x16x32_bf16 v[76:79], v[96:99], v[24:27], v[76:79]\n"
        "ds_read_b128 v[112:115], v0 offset:7200\n"
        "ds_read_b128 v[96:99], v0 offset:8224\n"
        "ds_read_b64 v[160:161], v162 offset:48\n"
        "v_perm_b32 v142, v100, v124, s63\n"
        "v_perm_b32 v144, v101, v125, s63\n"
        "v_perm_b32 v158, v102, v126, s63\n"
        "v_perm_b32 v156, v103, v127, s63\n"
        "v_mfma_f32_16x16x32_bf16 v[128:131], v[32:35], v[52:55], 0\n"
        "v_mfma_f32_16x16x32_bf16 v[128:131], v[28:31], v[80:83], v[128:131]\n"
        "ds_read_b64 v[148:149], v162 offset:64\n"
        "v_mfma_f32_16x16x32_bf16 v[128:131], v[24:27], v[44:47], v[128:131]\n"
        "s_waitcnt lgkmcnt(5)\n"
        "v_mfma_f32_16x16x32_bf16 v[80:83], v[32:35], v[252:255], 0\n"
        "ds_read_b64 v[146:147], v162 offset:80\n"
        "v_mfma_f32_16x16x32_bf16 v[80:83], v[28:31], v[120:123], v[80:83]\n"
        "ds_read_b128 v[52:55], v0 offset:9264\n"
        "v_mfma_f32_16x16x32_bf16 v[80:83], v[24:27], v[108:111], v[80:83]\n"
        "s_waitcnt lgkmcnt(3)\n"
        "ds_read_b128 v[108:111], v0 offset:10288\n"
        "ds_read_b128 v[100:103], v0 offset:11312\n"
        "s_mov_b32 vcc_lo, 0x22222222\n"
        "v_cndmask_b32 v141, v128, v129, vcc\n"
        "s_mov_b32 vcc_lo, 0x44444444\n"
        "v_cndmask_b32 v141, v141, v130, vcc\n"
        "v_mfma_f32_16x16x32_bf16 v[124:127], v[32:35], v[116:119], 0\n"
        "s_mov_b32 vcc_lo, 0x88888888\n"
        "v_cndmask_b32 v141, v141, v131, vcc\n"
        "v_perm_b32 v143, v92, v80, s63\n"
        "v_mfma_f32_16x16x32_bf16 v[124:127], v[28:31], v[112:115], v[124:127]\n"
        "ds_read_b128 v[132:135], v0 offset:12352\n"
        "v_perm_b32 v145, v93, v81, s63\n"
        "v_mfma_f32_16x16x32_bf16 v[124:127], v[24:27], v[96:99], v[124:127]\n"
        "ds_read_b128 v[136:139], v0 offset:13376\n"
        "ds_read_b128 v[128:131], v0 offset:14400\n"
        "v_perm_b32 v159, v94, v82, s63\n"
        "v_perm_b32 v157, v95, v83, s63\n"
        "v_mfma_f32_4x4x4_16b_bf16 v[112:115], v[160:161], v[164:165], 0\n"
        "s_waitcnt lgkmcnt(5)\n"
        "v_mfma_f32_4x4x4_16b_bf16 v[80:83], v[148:149], v[164:165], 0\n"
        "ds_read_b128 v[120:123], v0 offset:15440\n"
        "ds_read_b128 v[116:119], v0 offset:16464\n"
        "ds_read_b128 v[96:99], v0 offset:17488\n"
        "v_mfma_f32_4x4x4_16b_bf16 v[92:95], v[146:147], v[164:165], 0\n"
        "v_perm_b32 v150, v48, v124, s63\n"
        "v_perm_b32 v148, v49, v125, s63\n"
        "v_perm_b32 v164, v50, v126, s63\n"
        "s_waitcnt lgkmcnt(6)\n"
        "v_mfma_f32_16x16x32_bf16 v[160:163], v[32:35], v[52:55], 0\n"
        "v_perm_b32 v146, v51, v127, s63\n"
        "v_mfma_f32_16x16x32_bf16 v[160:163], v[28:31], v[108:111], v[160:163]\n"
        "v_mfma_f32_16x16x32_bf16 v[160:163], v[24:27], v[100:103], v[160:163]\n"
        "s_waitcnt lgkmcnt(3)\n"
        "s_mov_b64 exec, 0x00f0000f\n"
        "ds_write2_b32 v151, v141, v76 offset0:0 offset1:1\n"
        "s_mov_b64 exec, -1\n"
        "v_mfma_f32_16x16x32_bf16 v[48:51], v[32:35], v[132:135], 0\n"
        "ds_read_b128 v[124:127], v7\n"
        "v_mfma_f32_16x16x32_bf16 v[48:51], v[28:31], v[136:139], v[48:51]\n"
        "ds_read_b128 v[132:135], v7 offset:3328\n"
        "v_perm_b32 v151, v112, v160, s63\n"
        "v_mfma_f32_16x16x32_bf16 v[48:51], v[24:27], v[128:131], v[48:51]\n"
        "s_waitcnt lgkmcnt(3)\n"
        "ds_read_b128 v[128:131], v7 offset:64\n"
        "v_perm_b32 v149, v113, v161, s63\n"
        "v_perm_b32 v165, v114, v162, s63\n"
        "v_perm_b32 v147, v115, v163, s63\n"
        "v_mfma_f32_16x16x32_bf16 v[76:79], v[32:35], v[120:123], 0\n"
        "ds_read_b128 v[136:139], v7 offset:3392\n"
        "v_mfma_f32_16x16x32_bf16 v[76:79], v[28:31], v[116:119], v[76:79]\n"
        "ds_read_b128 v[160:163], v7 offset:128\n"
        "v_perm_b32 v120, v80, v48, s63\n"
        "v_mfma_f32_16x16x32_bf16 v[76:79], v[24:27], v[96:99], v[76:79]\n"
        "s_mov_b32 exec_lo, 0x00ff00ff\n"
        "s_mov_b32 exec_hi, 0x00ff00ff\n"
        "ds_write_b128 v166, v[44:47] offset:128\n"
        "s_mov_b64 exec, -1\n"
        "ds_read_b128 v[52:55], v7 offset:3456\n"
        "v_perm_b32 v122, v81, v49, s63\n"
        "v_perm_b32 v140, v82, v50, s63\n"
        "v_perm_b32 v118, v83, v51, s63\n"
        "v_perm_b32 v121, v92, v76, s63\n"
        "v_perm_b32 v123, v93, v77, s63\n"
        "v_perm_b32 v141, v94, v78, s63\n"
        "v_perm_b32 v119, v95, v79, s63\n"
        "s_mov_b32 exec_hi, 0\n"
        "ds_write2_b64 v167, v[142:143], v[144:145] offset0:0 offset1:49\n"
        "ds_write2_b64 v167, v[158:159], v[156:157] offset0:98 offset1:147\n"
        "s_mov_b32 exec_hi, -1\n"
        "s_mov_b32 exec_hi, 0\n"
        "ds_write2_b64 v167, v[150:151], v[148:149] offset0:1 offset1:50\n"
        "ds_write2_b64 v167, v[164:165], v[146:147] offset0:99 offset1:148\n"
        "s_mov_b32 exec_hi, -1\n"
        "s_mov_b32 exec_hi, 0\n"
        "ds_write2_b64 v167, v[120:121], v[122:123] offset0:2 offset1:51\n"
        "ds_write2_b64 v167, v[140:141], v[118:119] offset0:100 offset1:149\n"
        "s_mov_b32 exec_hi, -1\n"
        "s_waitcnt lgkmcnt(0)\n"
        "s_nop 6\n"
        "s_setprio 0\n"
        "s_setpc_b64 s[64:65]\n"
        "L52A_%=:\n"
        "s_nop 7\n"
        "v_mov_b32 v167, 21600\n"
        "L50A_%=:\n"
        "ds_read_b32 v166, v167 offset:64\n"
        "s_waitcnt lgkmcnt(0)\n"
        "v_readfirstlane_b32 s40, v166\n"
        "s_nop 3\n"
        "s_cmp_ge_i32 s40, 1\n"
        "s_cbranch_scc1 L51A_%=\n"
        "s_sleep 1\n"
        "s_sub_u32 s41, s41, 1\n"
        "s_cbranch_scc0 L50A_%=\n"
        "s_endpgm\n"
        "L51A_%=:\n"
        "v_mbcnt_lo_u32_b32 v166, -1, 0\n"
        "v_lshl_add_u32 v167, v166, 2, v167\n"
        "ds_read_b32 v166, v167 offset:68\n"
        "s_mov_b32 s14, s40\n"
        "s_mov_b32 s71, -1\n"
        "s_waitcnt lgkmcnt(0)\n"
        "v_readlane_b32 s22, v166, 0\n"
        "v_readlane_b32 s23, v166, 1\n"
        "s_nop 3\n"
        "s_nop 1\n"
        "s_branch L53A_%=\n"
        "L56A_%=:\n"
        "s_nop 7\n"
        "L54A_%=:\n"
        "ds_read_b32 v167, v0 offset:21604\n"
        "s_waitcnt lgkmcnt(0)\n"
        "v_readfirstlane_b32 s40, v167\n"
        "s_nop 3\n"
        "s_cmp_ge_i32 s40, s68\n"
        "s_cbranch_scc1 L55A_%=\n"
        "s_sleep 1\n"
        "s_sub_u32 s41, s41, 1\n"
        "s_cbranch_scc0 L54A_%=\n"
        "s_endpgm\n"
        "L55A_%=:\n"
        "s_nop 1\n"
        "s_branch L57A_%=\n"
        "L52B_%=:\n"
        "s_nop 7\n"
        "v_mov_b32 v167, 21600\n"
        "L50B_%=:\n"
        "ds_read_b32 v166, v167 offset:64\n"
        "s_waitcnt lgkmcnt(0)\n"
        "v_readfirstlane_b32 s40, v166\n"
        "s_nop 3\n"
        "s_cmp_ge_i32 s40, 1\n"
        "s_cbranch_scc1 L51B_%=\n"
        "s_sleep 1\n"
        "s_sub_u32 s41, s41, 1\n"
        "s_cbranch_scc0 L50B_%=\n"
        "s_endpgm\n"
        "L51B_%=:\n"
        "v_mbcnt_lo_u32_b32 v166, -1, 0\n"
        "v_lshl_add_u32 v167, v166, 2, v167\n"
        "ds_read_b32 v166, v167 offset:68\n"
        "s_mov_b32 s14, s40\n"
        "s_mov_b32 s71, -1\n"
        "s_waitcnt lgkmcnt(0)\n"
        "v_readlane_b32 s22, v166, 0\n"
        "v_readlane_b32 s23, v166, 1\n"
        "s_nop 3\n"
        "s_nop 1\n"
        "s_branch L53B_%=\n"
        "L56B_%=:\n"
        "s_nop 7\n"
        "L54B_%=:\n"
        "ds_read_b32 v167, v0 offset:21600\n"
        "s_waitcnt lgkmcnt(0)\n"
        "v_readfirstlane_b32 s40, v167\n"
        "s_nop 3\n"
        "s_cmp_ge_i32 s40, s68\n"
        "s_cbranch_scc1 L55B_%=\n"
        "s_sleep 1\n"
        "s_sub_u32 s41, s41, 1\n"
        "s_cbranch_scc0 L54B_%=\n"
        "s_endpgm\n"
        "L55B_%=:\n"
        "s_nop 1\n"
        "s_branch L57B_%=\n"
        "CREDOA_%=:\n"
        "s_waitcnt lgkmcnt(0)\n"
        "s_nop 15\n"
        "s_mov_b32 s36, 0x20000000\n"
        "ds_read_b128 v[124:127], v7 offset:0\n"
        "ds_read_b128 v[132:135], v7 offset:3328\n"
        "ds_read_b128 v[128:131], v7 offset:64\n"
        "ds_read_b128 v[136:139], v7 offset:3392\n"
        "ds_read_b128 v[160:163], v7 offset:128\n"
        "ds_read_b128 v[52:55], v7 offset:3456\n"
        "s_waitcnt lgkmcnt(0)\n"
        "s_branch MTOPA_%=\n"
        "CREDOB_%=:\n"
        "s_waitcnt lgkmcnt(0)\n"
        "s_nop 15\n"
        "s_mov_b32 s36, 0x20000000\n"
        "ds_read_b128 v[124:127], v7 offset:40064\n"
        "ds_read_b128 v[132:135], v7 offset:43392\n"
        "ds_read_b128 v[128:131], v7 offset:40128\n"
        "ds_read_b128 v[136:139], v7 offset:43456\n"
        "ds_read_b128 v[160:163], v7 offset:40192\n"
        "ds_read_b128 v[52:55], v7 offset:43520\n"
        "s_waitcnt lgkmcnt(0)\n"
        "s_branch MTOPB_%=\n"
        "MTOPA_%=:\n"
        "s_waitcnt lgkmcnt(5)\n"
        "v_mfma_f32_16x16x32_bf16 v[24:27], v[168:171], v[124:127], 0\n"
        "v_cvt_pk_bf16_f32 v112, v152, v56\n"
        "v_cvt_pk_bf16_f32 v113, v72, v20\n"
        "v_cvt_pk_bf16_f32 v114, v84, v60\n"
        "v_cvt_pk_bf16_f32 v116, v153, v57\n"
        "v_cvt_pk_bf16_f32 v117, v73, v21\n"
        "v_cvt_pk_bf16_f32 v118, v85, v61\n"
        "v_cvt_pk_bf16_f32 v120, v154, v58\n"
        "v_cvt_pk_bf16_f32 v121, v74, v22\n"
        "s_waitcnt lgkmcnt(3)\n"
        "v_mfma_f32_16x16x32_bf16 v[24:27], v[172:175], v[128:131], v[24:27]\n"
        "v_cvt_pk_bf16_f32 v122, v86, v62\n"
        "v_cvt_pk_bf16_f32 v140, v155, v59\n"
        "v_cvt_pk_bf16_f32 v141, v75, v23\n"
        "v_cvt_pk_bf16_f32 v142, v87, v63\n"
        "v_cvt_pk_bf16_f32 v144, v68, v36\n"
        "v_cvt_pk_bf16_f32 v145, v40, v64\n"
        "v_cvt_pk_bf16_f32 v146, v104, v88\n"
        "v_cvt_pk_bf16_f32 v148, v69, v37\n"
        "s_waitcnt lgkmcnt(1)\n"
        "v_mfma_f32_16x16x32_bf16 v[24:27], v[176:179], v[160:163], v[24:27]\n"
        "v_cvt_pk_bf16_f32 v149, v41, v65\n"
        "v_cvt_pk_bf16_f32 v150, v105, v89\n"
        "v_cvt_pk_bf16_f32 v152, v70, v38\n"
        "v_cvt_pk_bf16_f32 v153, v42, v66\n"
        "v_cvt_pk_bf16_f32 v154, v106, v90\n"
        "v_cvt_pk_bf16_f32 v156, v71, v39\n"
        "v_cvt_pk_bf16_f32 v157, v43, v67\n"
        "v_cvt_pk_bf16_f32 v158, v107, v91\n"
        "v_mfma_f32_16x16x32_bf16 v[32:35], v[188:191], v[160:163], 0\n"
        "v_perm_b32 v40, v25, v24, s63\n"
        "v_perm_b32 v41, v27, v26, s63\n"
        "v_mfma_f32_16x16x32_bf16 v[32:35], v[184:187], v[128:131], v[32:35]\n"
        "v_mfma_f32_16x16x32_bf16 v[32:35], v[180:183], v[124:127], v[32:35]\n"
        "s_add_u32 s37, s36, 196224\n"
        "ds_read_b128 v[28:31], v16 offset:128\n"
        "ds_read_b32 v159, v10\n"
        "v_mfma_f32_16x16x32_bf16 v[24:27], v[192:195], v[124:127], 0\n"
        "ds_read_b128 v[20:23], v5\n"
        "v_mfma_f32_16x16x32_bf16 v[24:27], v[196:199], v[128:131], v[24:27]\n"
        "ds_read_b64 v[164:165], v11 offset:192\n"
        "v_mfma_f32_16x16x32_bf16 v[24:27], v[200:203], v[160:163], v[24:27]\n"
        "buffer_store_dwordx3 v[112:114], v12, s[8:11], s37 offen offset:384 sc0 sc1 nt\n"
        "v_perm_b32 v42, v33, v32, s63\n"
        "v_perm_b32 v43, v35, v34, s63\n"
        "s_waitcnt lgkmcnt(3)\n"
        "v_mfma_f32_16x16x32_bf16 v[48:51], v[28:31], v[160:163], 0\n"
        "ds_read_b128 v[60:63], v7 offset:6784\n"
        "ds_read_i16 v167, v0 offset:20584\n"
        "ds_read_i16 v155, v0 offset:20586\n"
        "v_perm_b32 v44, v25, v24, s63\n"
        "v_perm_b32 v45, v27, v26, s63\n"
        "s_waitcnt lgkmcnt(3)\n"
        "v_smfmac_f32_16x16x64_bf16 v[48:51], v[20:23], v[124:131], v4\n"
        "s_add_u32 s37, s36, 196224\n"
        "ds_read_b128 v[32:35], v7 offset:6656\n"
        "ds_read_b128 v[36:39], v7 offset:6720\n"
        "v_mfma_f32_16x16x32_bf16 v[56:59], v[28:31], v[52:55], 0\n"
        "v_smfmac_f32_16x16x64_bf16 v[56:59], v[20:23], v[132:139], v4\n"
        "s_setprio 2\n"
        "s_waitcnt lgkmcnt(4)\n"
        "v_mfma_f32_16x16x32_bf16 v[24:27], v[28:31], v[60:63], 0\n"
        "buffer_store_dwordx3 v[116:118], v13, s[8:11], s37 offen offset:1152 sc0 sc1 nt\n"
        "v_add_f32_dpp v165, v50, v51 quad_perm:[0,1,2,3] row_mask:0x1 bank_mask:0xf\n"
        "s_waitcnt lgkmcnt(2)\n"
        "v_add_u32 v167, v9, v167\n"
        "v_add_f32_dpp v164, v48, v49 quad_perm:[0,1,2,3] row_mask:0x1 bank_mask:0xf\n"
        "v_add_f32_dpp v165, v58, v59 quad_perm:[0,1,2,3] row_mask:0x2 bank_mask:0xf\n"
        "v_add_u32 v155, v9, v155\n"
        "v_add_f32_dpp v164, v56, v57 quad_perm:[0,1,2,3] row_mask:0x2 bank_mask:0xf\n"
        "s_waitcnt lgkmcnt(0)\n"
        "v_smfmac_f32_16x16x64_bf16 v[24:27], v[20:23], v[32:39], v4\n"
        "v_mfma_f32_16x16x32_bf16 v[28:31], v[212:215], v[160:163], 0\n"
        "s_add_u32 s37, s36, 294336\n"
        "v_mfma_f32_16x16x32_bf16 v[28:31], v[208:211], v[128:131], v[28:31]\n"
        "v_mfma_f32_16x16x32_bf16 v[28:31], v[204:207], v[124:127], v[28:31]\n"
        "v_mfma_f32_16x16x32_bf16 v[20:23], v[216:219], v[124:127], 0\n"
        "buffer_store_dwordx3 v[120:122], v12, s[8:11], s37 offen offset:576 sc0 sc1 nt\n"
        "v_mfma_f32_16x16x32_bf16 v[20:23], v[220:223], v[128:131], v[20:23]\n"
        "v_mfma_f32_16x16x32_bf16 v[20:23], v[224:227], v[160:163], v[20:23]\n"
        "s_add_u32 s37, s36, 392448\n"
        "v_add_f32_dpp v165, v26, v27 quad_perm:[0,1,2,3] row_mask:0x4 bank_mask:0xf\n"
        "v_mul_f32 v166, s52, v165\n"
        "v_fma_f32 v166, -v166, v165, v159\n"
        "v_perm_b32 v46, v29, v28, s63\n"
        "v_perm_b32 v47, v31, v30, s63\n"
        "v_add_f32_e64 v166, s53, |v166|\n"
        "v_rsq_f32 v159, v166\n"
        "v_mfma_f32_16x16x32_bf16 v[56:59], v[236:239], v[160:163], 0\n"
        "s_mov_b32 exec_hi, 0xffff\n"
        "ds_write_b32 v10, v159\n"
        "s_mov_b32 exec_hi, -1\n"
        "v_perm_b32 v48, v21, v20, s63\n"
        "v_perm_b32 v49, v23, v22, s63\n"
        "ds_read_b32 v20, v167 offset:0\n"
        "v_add_f32_dpp v164, v24, v25 quad_perm:[0,1,2,3] row_mask:0x4 bank_mask:0xf\n"
        "v_mfma_f32_16x16x32_bf16 v[56:59], v[232:235], v[128:131], v[56:59]\n"
        "s_mov_b32 exec_hi, 0xffff\n"
        "v_add_u32 v10, 40064, v10\n"
        "s_mov_b32 exec_hi, -1\n"
        "ds_read_b32 v21, v167 offset:832\n"
        "ds_read_b32 v22, v167 offset:1664\n"
        "ds_read_b32 v23, v167 offset:2496\n"
        "ds_read_b32 v24, v167 offset:3328\n"
        "v_mul_f32 v164, v164, v159\n"
        "v_mfma_f32_16x16x32_bf16 v[56:59], v[228:231], v[124:127], v[56:59]\n"
        "buffer_store_dwordx3 v[140:142], v12, s[8:11], s37 offen sc0 sc1 nt\n"
        "ds_read_b32 v25, v167 offset:4160\n"
        "ds_read_b32 v26, v167 offset:4992\n"
        "ds_read_b32 v27, v167 offset:5824\n"
        "ds_read_b32 v28, v167 offset:6656\n"
        "ds_read_b32 v29, v167 offset:7488\n"
        "ds_read_b32 v30, v167 offset:8320\n"
        "ds_read_b32 v31, v167 offset:9152\n"
        "v_perm_b32 v50, v57, v56, s63\n"
        "v_perm_b32 v51, v59, v58, s63\n"
        "v_readlane_b32 s58, v159, 56\n"
        "v_mfma_f32_16x16x32_bf16 v[56:59], v[124:127], v[40:43], 0\n"
        "s_mov_b64 exec, s[56:57]\n"
        "v_mov_b32 v164, 0xff800000\n"
        "s_mov_b64 exec, -1\n"
        "ds_read_b32 v165, v155 offset:0\n"
        "ds_read_b32 v166, v155 offset:3328\n"
        "v_max_f32_dpp v151, v164, v164 row_ror:8 row_mask:0xf bank_mask:0xf\n"
        "v_mfma_f32_16x16x32_bf16 v[56:59], v[128:131], v[44:47], v[56:59]\n"
        "s_add_u32 s37, s36, 392448\n"
        "v_max_f32_dpp v151, v151, v151 row_ror:4 row_mask:0xf bank_mask:0xf\n"
        "ds_read_b32 v167, v155 offset:6656\n"
        "v_mfma_f32_16x16x32_bf16 v[56:59], v[160:163], v[48:51], v[56:59]\n"
        "v_max_f32_dpp v151, v151, v151 row_ror:2 row_mask:0xf bank_mask:0xf\n"
        "v_mfma_f32_16x16x32_bf16 v[72:75], v[52:55], v[48:51], 0\n"
        "v_mfma_f32_16x16x32_bf16 v[72:75], v[136:139], v[44:47], v[72:75]\n"
        "buffer_store_dwordx3 v[144:146], v15, s[8:11], s37 offen offset:768 sc0 sc1 nt\n"
        "v_max_f32_dpp v151, v151, v151 row_ror:1 row_mask:0xf bank_mask:0xf\n"
        "v_mfma_f32_16x16x32_bf16 v[72:75], v[132:135], v[40:43], v[72:75]\n"
        "ds_read_b128 v[88:91], v8 offset:128\n"
        "v_mfma_f32_16x16x32_bf16 v[68:71], v[32:35], v[40:43], 0\n"
        "s_add_u32 s37, s36, 490560\n"
        "ds_read2_b32 v[92:93], v6 offset0:0 offset1:2\n"
        "ds_read2_b32 v[94:95], v6 offset0:8 offset1:10\n"
        "s_waitcnt lgkmcnt(3)\n"
        "v_mul_f32 v56, v56, v20\n"
        "v_mul_f32 v57, v57, v21\n"
        "v_mul_f32 v58, v58, v22\n"
        "v_mul_f32 v59, v59, v23\n"
        "v_mul_f32 v72, v72, v24\n"
        "v_mfma_f32_16x16x32_bf16 v[68:71], v[36:39], v[44:47], v[68:71]\n"
        "v_max3_f32 v155, v56, v57, v58\n"
        "v_mul_f32 v73, v73, v25\n"
        "v_mul_f32 v74, v74, v26\n"
        "v_max3_f32 v155, v155, v59, v72\n"
        "v_mul_f32 v75, v75, v27\n"
        "v_max3_f32 v155, v155, v73, v74\n"
        "v_mfma_f32_16x16x32_bf16 v[68:71], v[60:63], v[48:51], v[68:71]\n"
        "s_waitcnt lgkmcnt(2)\n"
        "v_mfma_f32_16x16x32_bf16 v[76:79], v[88:91], v[48:51], 0\n"
        "buffer_store_dwordx3 v[148:150], v12, s[8:11], s37 offen offset:192 sc0 sc1 nt\n"
        "s_waitcnt lgkmcnt(0)\n"
        "v_smfmac_f32_16x16x64_bf16 v[76:79], v[92:95], v[40:47], v4\n"
        "v_mfma_f32_16x16x32_bf16 v[48:51], v[168:171], v[132:135], 0\n"
        "s_add_u32 s37, s36, 490560\n"
        "v_mfma_f32_16x16x32_bf16 v[48:51], v[172:175], v[136:139], v[48:51]\n"
        "v_mul_f32 v68, v68, v28\n"
        "v_mul_f32 v69, v69, v29\n"
        "v_mul_f32 v70, v70, v30\n"
        "v_max3_f32 v155, v155, v75, v68\n"
        "v_mul_f32 v71, v71, v31\n"
        "v_add_f32 v76, v76, v77\n"
        "v_mul_f32 v76, s58, v76\n"
        "v_max3_f32 v155, v155, v69, v70\n"
        "v_mfma_f32_16x16x32_bf16 v[48:51], v[176:179], v[52:55], v[48:51]\n"
        "s_mov_b64 exec, s[48:49]\n"
        "v_mov_b32 v76, 0xff800000\n"
        "s_mov_b64 exec, -1\n"
        "v_max3_f32 v155, v155, v71, v76\n"
        "v_mfma_f32_16x16x32_bf16 v[44:47], v[188:191], v[52:55], 0\n"
        "buffer_store_dwordx3 v[152:154], v14, s[8:11], s37 offen offset:960 sc0 sc1 nt\n"
        "v_permlane32_swap_b32 v155, v151\n"
        "v_max_f32 v155, v155, v151\n"
        "v_perm_b32 v40, v49, v48, s63\n"
        "v_perm_b32 v41, v51, v50, s63\n"
        "v_mov_b32 v154, v155\n"
        "v_mfma_f32_16x16x32_bf16 v[44:47], v[184:187], v[136:139], v[44:47]\n"
        "v_mfma_f32_16x16x32_bf16 v[44:47], v[180:183], v[132:135], v[44:47]\n"
        "s_add_u32 s37, s36, 588672\n"
        "v_permlane16_swap_b32 v155, v154\n"
        "v_max_f32 v155, v155, v154\n"
        "v_mov_b32 v151, v155\n"
        "v_mfma_f32_16x16x32_bf16 v[64:67], v[192:195], v[132:135], 0\n"
        "v_mfma_f32_16x16x32_bf16 v[64:67], v[196:199], v[136:139], v[64:67]\n"
        "v_permlane32_swap_b32 v155, v151\n"
        "v_mul_f32 v155, v155, v165\n"
        "v_perm_b32 v42, v45, v44, s63\n"
        "v_perm_b32 v43, v47, v46, s63\n"
        "v_fma_f32 v56, v56, v165, -v155\n"
        "v_fma_f32 v57, v57, v165, -v155\n"
        "v_fma_f32 v58, v58, v165, -v155\n"
        "v_mfma_f32_16x16x32_bf16 v[64:67], v[200:203], v[52:55], v[64:67]\n"
        "buffer_store_dwordx3 v[156:158], v12, s[8:11], s37 offen offset:384 sc0 sc1 nt\n"
        "v_exp_f32 v56, v56\n"
        "v_exp_f32 v57, v57\n"
        "v_exp_f32 v58, v58\n"
        "v_fma_f32 v59, v59, v165, -v155\n"
        "v_add_f32 v158, v56, v57\n"
        "v_exp_f32 v59, v59\n"
        "v_mfma_f32_16x16x32_bf16 v[48:51], v[212:215], v[52:55], 0\n"
        "v_perm_b32 v44, v65, v64, s63\n"
        "v_perm_b32 v45, v67, v66, s63\n"
        "v_fma_f32 v72, v72, v165, -v155\n"
        "v_add_f32 v158, v158, v58\n"
        "v_exp_f32 v72, v72\n"
        "v_fma_f32 v73, v73, v165, -v155\n"
        "v_add_f32 v158, v158, v59\n"
        "v_mfma_f32_16x16x32_bf16 v[48:51], v[208:211], v[136:139], v[48:51]\n"
        "v_exp_f32 v73, v73\n"
        "v_fma_f32 v74, v74, v165, -v155\n"
        "v_add_f32 v158, v158, v72\n"
        "v_exp_f32 v74, v74\n"
        "v_fma_f32 v75, v75, v165, -v155\n"
        "v_sub_f32 v164, v164, v151\n"
        "v_mfma_f32_16x16x32_bf16 v[48:51], v[204:207], v[132:135], v[48:51]\n"
        "v_add_f32 v158, v158, v73\n"
        "v_exp_f32 v75, v75\n"
        "v_fma_f32 v68, v68, v165, -v155\n"
        "v_mul_f32 v164, s58, v164\n"
        "v_add_f32 v158, v158, v74\n"
        "v_exp_f32 v68, v68\n"
        "v_mfma_f32_16x16x32_bf16 v[64:67], v[216:219], v[132:135], 0\n"
        "v_fma_f32 v69, v69, v165, -v155\n"
        "v_perm_b32 v46, v49, v48, s63\n"
        "v_perm_b32 v47, v51, v50, s63\n"
        "v_exp_f32 v164, v164\n"
        "v_add_f32 v158, v158, v75\n"
        "v_exp_f32 v69, v69\n"
        "v_mfma_f32_16x16x32_bf16 v[64:67], v[220:223], v[136:139], v[64:67]\n"
        "v_fma_f32 v70, v70, v165, -v155\n"
        "v_add_f32 v158, v158, v68\n"
        "v_exp_f32 v70, v70\n"
        "v_fma_f32 v71, v71, v165, -v155\n"
        "v_add_f32_dpp v157, v164, v164 row_ror:8 row_mask:0xf bank_mask:0xf\n"
        "v_add_f32 v158, v158, v69\n"
        "v_fma_f32 v76, v76, v165, -v155\n"
        "v_mfma_f32_16x16x32_bf16 v[64:67], v[224:227], v[52:55], v[64:67]\n"
        "v_exp_f32 v71, v71\n"
        "v_add_f32_dpp v157, v157, v157 row_ror:4 row_mask:0xf bank_mask:0xf\n"
        "v_add_f32 v158, v158, v70\n"
        "v_exp_f32 v76, v76\n"
        "v_add_f32_dpp v157, v157, v157 row_ror:2 row_mask:0xf bank_mask:0xf\n"
        "v_add_f32 v158, v158, v71\n"
        "v_mfma_f32_16x16x32_bf16 v[48:51], v[236:239], v[52:55], 0\n"
        "v_add_f32_dpp v157, v157, v157 row_ror:1 row_mask:0xf bank_mask:0xf\n"
        "v_perm_b32 v80, v65, v64, s63\n"
        "v_perm_b32 v81, v67, v66, s63\n"
        "v_add_f32 v158, v158, v76\n"
        "v_mul_f32 v56, v56, v20\n"
        "v_mul_f32 v57, v57, v21\n"
        "v_permlane32_swap_b32 v158, v157\n"
        "v_mfma_f32_16x16x32_bf16 v[48:51], v[232:235], v[136:139], v[48:51]\n"
        "v_add_f32 v158, v158, v157\n"
        "v_mov_b32 v165, v158\n"
        "v_mul_f32 v58, v58, v22\n"
        "v_mul_f32 v59, v59, v23\n"
        "v_permlane16_swap_b32 v158, v165\n"
        "v_add_f32 v158, v158, v165\n"
        "v_mul_f32 v72, v72, v24\n"
        "v_mfma_f32_16x16x32_bf16 v[48:51], v[228:231], v[132:135], v[48:51]\n"
        "v_rcp_f32 v158, v158\n"
        "v_mul_f32 v73, v73, v25\n"
        "v_mov_b32 v157, v158\n"
        "v_mul_f32 v74, v74, v26\n"
        "v_mul_f32 v75, v75, v27\n"
        "v_permlane32_swap_b32 v158, v157\n"
        "v_mfma_f32_16x16x32_bf16 v[64:67], v[124:127], v[40:43], 0\n"
        "v_mul_f32 v56, v56, v158\n"
        "v_perm_b32 v82, v49, v48, s63\n"
        "v_perm_b32 v83, v51, v50, s63\n"
        "v_mul_f32 v57, v57, v158\n"
        "v_mul_f32 v58, v58, v158\n"
        "v_mul_f32 v59, v59, v158\n"
        "v_mul_f32 v72, v72, v158\n"
        "v_mul_f32 v73, v73, v158\n"
        "v_mfma_f32_16x16x32_bf16 v[64:67], v[128:131], v[44:47], v[64:67]\n"
        "v_mul_f32 v74, v74, v158\n"
        "v_mul_f32 v75, v75, v158\n"
        "v_perm_b32 v48, v57, v56, s63\n"
        "v_perm_b32 v49, v59, v58, s63\n"
        "v_perm_b32 v50, v73, v72, s63\n"
        "v_perm_b32 v51, v75, v74, s63\n"
        "v_mul_f32 v68, v68, v28\n"
        "v_mul_f32 v69, v69, v29\n"
        "v_mfma_f32_16x16x32_bf16 v[64:67], v[160:163], v[80:83], v[64:67]\n"
        "v_mul_f32 v70, v70, v30\n"
        "v_mul_f32 v71, v71, v31\n"
        "v_mul_f32 v76, s58, v76\n"
        "v_mul_f32 v68, v68, v158\n"
        "v_mul_f32 v69, v69, v158\n"
        "v_mul_f32 v70, v70, v158\n"
        "v_mul_f32 v71, v71, v158\n"
        "v_fma_f32 v76, v76, v158, v19\n"
        "v_mfma_f32_16x16x32_bf16 v[84:87], v[52:55], v[80:83], 0\n"
        "v_mul_f32 v64, v64, v20\n"
        "v_mul_f32 v65, v65, v21\n"
        "v_mul_f32 v66, v66, v22\n"
        "v_mul_f32 v67, v67, v23\n"
        "v_max3_f32 v165, v64, v65, v66\n"
        "v_perm_b32 v56, v69, v68, s63\n"
        "v_perm_b32 v58, v71, v70, s63\n"
        "v_perm_b32 v57, 0, v76, s63\n"
        "v_mfma_f32_16x16x32_bf16 v[84:87], v[136:139], v[44:47], v[84:87]\n"
        "v_mul_f32 v164, v164, v159\n"
        "v_mul_f32 v164, v164, v157\n"
        "v_perm_b32 v164, 0, v164, s63\n"
        "ds_write_b16 v1, v164\n"
        "v_mfma_f32_16x16x32_bf16 v[84:87], v[132:135], v[40:43], v[84:87]\n"
        "v_mfma_f32_16x16x32_bf16 v[68:71], v[32:35], v[40:43], 0\n"
        "v_mfma_f32_16x16x32_bf16 v[68:71], v[36:39], v[44:47], v[68:71]\n"
        "v_mfma_f32_16x16x32_bf16 v[68:71], v[60:63], v[80:83], v[68:71]\n"
        "v_mfma_f32_16x16x32_bf16 v[72:75], v[88:91], v[80:83], 0\n"
        "v_smfmac_f32_16x16x64_bf16 v[72:75], v[92:95], v[40:47], v4\n"
        "v_mfma_f32_16x16x32_bf16 v[80:83], v[168:171], v[32:35], 0\n"
        "v_mfma_f32_16x16x32_bf16 v[80:83], v[172:175], v[36:39], v[80:83]\n"
        "v_mfma_f32_16x16x32_bf16 v[80:83], v[176:179], v[60:63], v[80:83]\n"
        "v_mul_f32 v84, v84, v24\n"
        "v_mul_f32 v85, v85, v25\n"
        "v_mul_f32 v86, v86, v26\n"
        "v_max3_f32 v165, v165, v67, v84\n"
        "v_mul_f32 v87, v87, v27\n"
        "v_add_f32 v72, v72, v73\n"
        "v_mul_f32 v68, v68, v28\n"
        "v_mul_f32 v72, s58, v72\n"
        "v_mfma_f32_16x16x32_bf16 v[40:43], v[188:191], v[60:63], 0\n"
        "s_mov_b64 exec, s[48:49]\n"
        "v_mov_b32 v72, 0xff800000\n"
        "s_mov_b64 exec, -1\n"
        "v_perm_b32 v76, v81, v80, s63\n"
        "v_perm_b32 v77, v83, v82, s63\n"
        "v_max3_f32 v165, v165, v85, v86\n"
        "v_mul_f32 v69, v69, v29\n"
        "v_mul_f32 v70, v70, v30\n"
        "v_max3_f32 v165, v165, v87, v68\n"
        "v_mfma_f32_16x16x32_bf16 v[40:43], v[184:187], v[36:39], v[40:43]\n"
        "v_mul_f32 v71, v71, v31\n"
        "v_max3_f32 v165, v165, v69, v70\n"
        "v_max3_f32 v165, v165, v71, v72\n"
        "v_mfma_f32_16x16x32_bf16 v[40:43], v[180:183], v[32:35], v[40:43]\n"
        "v_mfma_f32_16x16x32_bf16 v[100:103], v[192:195], v[32:35], 0\n"
        "v_mfma_f32_16x16x32_bf16 v[100:103], v[196:199], v[36:39], v[100:103]\n"
        "v_mfma_f32_16x16x32_bf16 v[100:103], v[200:203], v[60:63], v[100:103]\n"
        "v_mfma_f32_16x16x32_bf16 v[96:99], v[212:215], v[60:63], 0\n"
        "v_mfma_f32_16x16x32_bf16 v[96:99], v[208:211], v[36:39], v[96:99]\n"
        "v_mfma_f32_16x16x32_bf16 v[96:99], v[204:207], v[32:35], v[96:99]\n"
        "v_mfma_f32_16x16x32_bf16 v[44:47], v[216:219], v[32:35], 0\n"
        "v_mfma_f32_16x16x32_bf16 v[44:47], v[220:223], v[36:39], v[44:47]\n"
        "v_perm_b32 v78, v41, v40, s63\n"
        "v_perm_b32 v79, v43, v42, s63\n"
        "v_mfma_f32_16x16x32_bf16 v[44:47], v[224:227], v[60:63], v[44:47]\n"
        "v_perm_b32 v80, v101, v100, s63\n"
        "v_perm_b32 v81, v103, v102, s63\n"
        "v_mfma_f32_16x16x32_bf16 v[40:43], v[236:239], v[60:63], 0\n"
        "v_perm_b32 v82, v97, v96, s63\n"
        "v_perm_b32 v83, v99, v98, s63\n"
        "v_mfma_f32_16x16x32_bf16 v[40:43], v[232:235], v[36:39], v[40:43]\n"
        "v_mfma_f32_16x16x32_bf16 v[40:43], v[228:231], v[32:35], v[40:43]\n"
        "v_mfma_f32_16x16x32_bf16 v[108:111], v[124:127], v[76:79], 0\n"
        "v_perm_b32 v96, v45, v44, s63\n"
        "v_perm_b32 v97, v47, v46, s63\n"
        "v_mfma_f32_16x16x32_bf16 v[108:111], v[128:131], v[80:83], v[108:111]\n"
        "s_nop 3\n"
        "v_perm_b32 v98, v41, v40, s63\n"
        "v_perm_b32 v99, v43, v42, s63\n"
        "s_nop 1\n"
        "v_mfma_f32_16x16x32_bf16 v[108:111], v[160:163], v[96:99], v[108:111]\n"
        "v_mfma_f32_16x16x32_bf16 v[140:143], v[52:55], v[96:99], 0\n"
        "v_mfma_f32_16x16x32_bf16 v[140:143], v[136:139], v[80:83], v[140:143]\n"
        "v_mfma_f32_16x16x32_bf16 v[140:143], v[132:135], v[76:79], v[140:143]\n"
        "v_mfma_f32_16x16x32_bf16 v[100:103], v[32:35], v[76:79], 0\n"
        "v_mfma_f32_16x16x32_bf16 v[100:103], v[36:39], v[80:83], v[100:103]\n"
        "v_mfma_f32_16x16x32_bf16 v[100:103], v[60:63], v[96:99], v[100:103]\n"
        "v_mfma_f32_16x16x32_bf16 v[44:47], v[88:91], v[96:99], 0\n"
        "v_smfmac_f32_16x16x64_bf16 v[44:47], v[92:95], v[76:83], v4\n"
        "v_mul_f32 v108, v108, v20\n"
        "v_mul_f32 v109, v109, v21\n"
        "v_mul_f32 v110, v110, v22\n"
        "v_mul_f32 v111, v111, v23\n"
        "v_mul_f32 v140, v140, v24\n"
        "v_max3_f32 v159, v108, v109, v110\n"
        "v_mul_f32 v141, v141, v25\n"
        "v_mfma_f32_16x16x32_bf16 v[60:63], v[124:127], v[240:243], 0\n"
        "v_add_f32 v44, v44, v45\n"
        "v_mul_f32 v142, v142, v26\n"
        "v_max3_f32 v159, v159, v111, v140\n"
        "v_mul_f32 v143, v143, v27\n"
        "v_mul_f32 v100, v100, v28\n"
        "v_mul_f32 v44, s58, v44\n"
        "v_max3_f32 v159, v159, v141, v142\n"
        "v_mul_f32 v101, v101, v29\n"
        "v_mfma_f32_16x16x32_bf16 v[60:63], v[128:131], v[244:247], v[60:63]\n"
        "s_mov_b64 exec, s[48:49]\n"
        "v_mov_b32 v44, 0xff800000\n"
        "s_mov_b64 exec, -1\n"
        "v_mul_f32 v102, v102, v30\n"
        "v_max3_f32 v159, v159, v143, v100\n"
        "v_mul_f32 v103, v103, v31\n"
        "v_max3_f32 v159, v159, v101, v102\n"
        "v_max3_f32 v159, v159, v103, v44\n"
        "v_mfma_f32_16x16x32_bf16 v[60:63], v[160:163], v[248:251], v[60:63]\n"
        "ds_read_b128 v[32:35], v17 offset:0\n"
        "v_permlane32_swap_b32 v165, v159\n"
        "v_max_f32 v165, v165, v159\n"
        "v_mov_b32 v164, v165\n"
        "v_mfma_f32_16x16x32_bf16 v[76:79], v[52:55], v[248:251], 0\n"
        "ds_read_b128 v[36:39], v17 offset:64\n"
        "v_permlane16_swap_b32 v165, v164\n"
        "v_max_f32 v165, v165, v164\n"
        "v_perm_b32 v40, v61, v60, s63\n"
        "v_perm_b32 v41, v63, v62, s63\n"
        "v_mov_b32 v159, v165\n"
        "v_mfma_f32_16x16x32_bf16 v[76:79], v[136:139], v[244:247], v[76:79]\n"
        "ds_read_b128 v[60:63], v17 offset:128\n"
        "v_permlane32_swap_b32 v165, v159\n"
        "v_mul_f32 v165, v165, v166\n"
        "v_mul_f32 v159, v159, v167\n"
        "v_fma_f32 v64, v64, v166, -v165\n"
        "v_fma_f32 v65, v65, v166, -v165\n"
        "v_fma_f32 v108, v108, v167, -v159\n"
        "v_mfma_f32_16x16x32_bf16 v[76:79], v[132:135], v[240:243], v[76:79]\n"
        "s_waitcnt lgkmcnt(2)\n"
        "ds_read_b128 v[96:99], v0 offset:5136\n"
        "v_fma_f32 v109, v109, v167, -v159\n"
        "v_exp_f32 v64, v64\n"
        "v_exp_f32 v65, v65\n"
        "v_fma_f32 v66, v66, v166, -v165\n"
        "v_fma_f32 v110, v110, v167, -v159\n"
        "v_mfma_f32_16x16x32_bf16 v[80:83], v[32:35], v[240:243], 0\n"
        "s_waitcnt lgkmcnt(2)\n"
        "ds_read_b128 v[104:107], v0 offset:4112\n"
        "v_perm_b32 v42, v77, v76, s63\n"
        "v_perm_b32 v43, v79, v78, s63\n"
        "v_exp_f32 v108, v108\n"
        "v_exp_f32 v109, v109\n"
        "v_fma_f32 v67, v67, v166, -v165\n"
        "v_mfma_f32_16x16x32_bf16 v[80:83], v[36:39], v[244:247], v[80:83]\n"
        "s_waitcnt lgkmcnt(2)\n"
        "v_exp_f32 v66, v66\n"
        "v_exp_f32 v110, v110\n"
        "v_fma_f32 v111, v111, v167, -v159\n"
        "v_add_f32 v95, v64, v65\n"
        "v_exp_f32 v67, v67\n"
        "v_mfma_f32_16x16x32_bf16 v[80:83], v[60:63], v[248:251], v[80:83]\n"
        "s_waitcnt lgkmcnt(1)\n"
        "v_fma_f32 v84, v84, v166, -v165\n"
        "v_add_f32 v164, v108, v109\n"
        "v_exp_f32 v111, v111\n"
        "v_fma_f32 v140, v140, v167, -v159\n"
        "v_add_f32 v95, v95, v66\n"
        "v_exp_f32 v84, v84\n"
        "v_mfma_f32_16x16x32_bf16 v[112:115], v[60:63], v[96:99], 0\n"
        "s_waitcnt lgkmcnt(0)\n"
        "v_perm_b32 v76, v81, v80, s63\n"
        "v_perm_b32 v80, v83, v82, s63\n"
        "v_fma_f32 v85, v85, v166, -v165\n"
        "v_add_f32 v164, v164, v110\n"
        "v_exp_f32 v140, v140\n"
        "v_fma_f32 v141, v141, v167, -v159\n"
        "v_add_f32 v95, v95, v67\n"
        "v_mfma_f32_16x16x32_bf16 v[112:115], v[36:39], v[104:107], v[112:115]\n"
        "v_exp_f32 v85, v85\n"
        "v_fma_f32 v86, v86, v166, -v165\n"
        "v_add_f32 v164, v164, v111\n"
        "v_exp_f32 v141, v141\n"
        "v_fma_f32 v142, v142, v167, -v159\n"
        "v_add_f32 v95, v95, v84\n"
        "v_mfma_f32_16x16x32_bf16 v[112:115], v[32:35], v[252:255], v[112:115]\n"
        "v_exp_f32 v86, v86\n"
        "v_fma_f32 v87, v87, v166, -v165\n"
        "v_add_f32 v164, v164, v140\n"
        "v_exp_f32 v142, v142\n"
        "v_fma_f32 v143, v143, v167, -v159\n"
        "v_add_f32 v95, v95, v85\n"
        "v_mfma_f32_16x16x32_bf16 v[120:123], v[132:135], v[252:255], 0\n"
        "v_exp_f32 v87, v87\n"
        "v_perm_b32 v88, v113, v112, s63\n"
        "v_perm_b32 v92, v115, v114, s63\n"
        "v_fma_f32 v68, v68, v166, -v165\n"
        "v_add_f32 v164, v164, v141\n"
        "v_exp_f32 v143, v143\n"
        "v_mfma_f32_16x16x32_bf16 v[120:123], v[136:139], v[104:107], v[120:123]\n"
        "v_fma_f32 v100, v100, v167, -v159\n"
        "v_add_f32 v95, v95, v86\n"
        "v_exp_f32 v68, v68\n"
        "v_fma_f32 v69, v69, v166, -v165\n"
        "v_add_f32 v164, v164, v142\n"
        "v_exp_f32 v100, v100\n"
        "v_mfma_f32_16x16x32_bf16 v[120:123], v[52:55], v[96:99], v[120:123]\n"
        "ds_read_b128 v[144:147], v0 offset:6176\n"
        "v_fma_f32 v101, v101, v167, -v159\n"
        "v_add_f32 v95, v95, v87\n"
        "v_exp_f32 v69, v69\n"
        "v_fma_f32 v70, v70, v166, -v165\n"
        "v_add_f32 v164, v164, v143\n"
        "v_fma_f32 v102, v102, v167, -v159\n"
        "v_mfma_f32_16x16x32_bf16 v[148:151], v[160:163], v[96:99], 0\n"
        "ds_read_b128 v[116:119], v0 offset:7200\n"
        "v_perm_b32 v98, v121, v120, s63\n"
        "v_perm_b32 v99, v123, v122, s63\n"
        "v_exp_f32 v101, v101\n"
        "v_add_f32 v95, v95, v68\n"
        "v_exp_f32 v70, v70\n"
        "v_mfma_f32_16x16x32_bf16 v[148:151], v[128:131], v[104:107], v[148:151]\n"
        "ds_read_b128 v[112:115], v0 offset:8224\n"
        "v_fma_f32 v71, v71, v166, -v165\n"
        "v_add_f32 v164, v164, v100\n"
        "v_exp_f32 v102, v102\n"
        "v_fma_f32 v103, v103, v167, -v159\n"
        "v_add_f32 v95, v95, v69\n"
        "v_fma_f32 v72, v72, v166, -v165\n"
        "v_mfma_f32_16x16x32_bf16 v[148:151], v[124:127], v[252:255], v[148:151]\n"
        "s_waitcnt lgkmcnt(2)\n"
        "v_exp_f32 v71, v71\n"
        "v_add_f32 v164, v164, v101\n"
        "v_exp_f32 v103, v103\n"
        "v_fma_f32 v44, v44, v167, -v159\n"
        "v_add_f32 v95, v95, v70\n"
        "v_add_f32 v164, v164, v102\n"
        "v_mfma_f32_16x16x32_bf16 v[120:123], v[124:127], v[144:147], 0\n"
        "s_waitcnt lgkmcnt(1)\n"
        "v_perm_b32 v96, v149, v148, s63\n"
        "v_perm_b32 v97, v151, v150, s63\n"
        "v_exp_f32 v72, v72\n"
        "v_exp_f32 v44, v44\n"
        "v_add_f32 v95, v95, v71\n"
        "v_add_f32 v164, v164, v103\n"
        "v_mfma_f32_16x16x32_bf16 v[120:123], v[128:131], v[116:119], v[120:123]\n"
        "s_waitcnt lgkmcnt(0)\n"
        "v_add_f32 v95, v95, v72\n"
        "v_add_f32 v164, v164, v44\n"
        "v_mul_f32 v64, v64, v20\n"
        "v_mul_f32 v65, v65, v21\n"
        "v_permlane32_swap_b32 v95, v164\n"
        "v_add_f32 v95, v95, v164\n"
        "v_mov_b32 v167, v95\n"
        "v_mfma_f32_16x16x32_bf16 v[120:123], v[160:163], v[112:115], v[120:123]\n"
        "v_mul_f32 v66, v66, v22\n"
        "v_permlane16_swap_b32 v95, v167\n"
        "v_add_f32 v95, v95, v167\n"
        "v_rcp_f32 v95, v95\n"
        "v_mul_f32 v67, v67, v23\n"
        "v_mov_b32 v164, v95\n"
        "v_mfma_f32_16x16x32_bf16 v[156:159], v[52:55], v[112:115], 0\n"
        "v_mul_f32 v84, v84, v24\n"
        "v_perm_b32 v104, v121, v120, s63\n"
        "v_perm_b32 v105, v123, v122, s63\n"
        "v_permlane32_swap_b32 v95, v164\n"
        "v_mul_f32 v85, v85, v25\n"
        "v_mul_f32 v86, v86, v26\n"
        "v_mul_f32 v87, v87, v27\n"
        "v_mfma_f32_16x16x32_bf16 v[156:159], v[136:139], v[116:119], v[156:159]\n"
        "v_mul_f32 v64, v64, v95\n"
        "v_mul_f32 v65, v65, v95\n"
        "v_mul_f32 v66, v66, v95\n"
        "v_mul_f32 v67, v67, v95\n"
        "v_mul_f32 v84, v84, v95\n"
        "v_mul_f32 v85, v85, v95\n"
        "v_mul_f32 v86, v86, v95\n"
        "v_mul_f32 v87, v87, v95\n"
        "v_mfma_f32_16x16x32_bf16 v[156:159], v[132:135], v[144:147], v[156:159]\n"
        "ds_read_b128 v[148:151], v0 offset:11312\n"
        "v_perm_b32 v120, v65, v64, s63\n"
        "v_perm_b32 v121, v67, v66, s63\n"
        "v_perm_b32 v122, v85, v84, s63\n"
        "v_perm_b32 v123, v87, v86, s63\n"
        "v_mul_f32 v68, v68, v28\n"
        "v_mul_f32 v69, v69, v29\n"
        "v_mul_f32 v70, v70, v30\n"
        "v_mfma_f32_16x16x32_bf16 v[152:155], v[32:35], v[144:147], 0\n"
        "ds_read_b128 v[84:87], v0 offset:10288\n"
        "v_perm_b32 v106, v157, v156, s63\n"
        "v_perm_b32 v107, v159, v158, s63\n"
        "v_mul_f32 v71, v71, v31\n"
        "v_mul_f32 v72, s58, v72\n"
        "v_mul_f32 v68, v68, v95\n"
        "v_mul_f32 v69, v69, v95\n"
        "v_mul_f32 v70, v70, v95\n"
        "v_mfma_f32_16x16x32_bf16 v[152:155], v[36:39], v[116:119], v[152:155]\n"
        "ds_read_b128 v[144:147], v0 offset:9264\n"
        "v_mul_f32 v71, v71, v95\n"
        "v_fma_f32 v72, v72, v95, v19\n"
        "v_perm_b32 v64, v69, v68, s63\n"
        "v_perm_b32 v66, v71, v70, s63\n"
        "v_perm_b32 v65, 0, v72, s63\n"
        "v_mul_f32 v108, v108, v20\n"
        "v_mul_f32 v109, v109, v21\n"
        "v_mfma_f32_16x16x32_bf16 v[152:155], v[60:63], v[112:115], v[152:155]\n"
        "s_waitcnt lgkmcnt(2)\n"
        "v_mul_f32 v110, v110, v22\n"
        "v_mul_f32 v111, v111, v23\n"
        "v_mul_f32 v140, v140, v24\n"
        "v_mul_f32 v141, v141, v25\n"
        "v_mul_f32 v142, v142, v26\n"
        "v_mul_f32 v143, v143, v27\n"
        "v_mul_f32 v108, v108, v164\n"
        "v_perm_b32 v112, v153, v152, s63\n"
        "v_mfma_f32_16x16x32_bf16 v[20:23], v[60:63], v[148:151], 0\n"
        "s_waitcnt lgkmcnt(1)\n"
        "v_perm_b32 v116, v155, v154, s63\n"
        "v_mul_f32 v109, v109, v164\n"
        "v_mul_f32 v110, v110, v164\n"
        "v_mul_f32 v111, v111, v164\n"
        "v_mul_f32 v140, v140, v164\n"
        "v_mul_f32 v141, v141, v164\n"
        "v_mul_f32 v142, v142, v164\n"
        "v_mul_f32 v143, v143, v164\n"
        "v_mfma_f32_16x16x32_bf16 v[20:23], v[36:39], v[84:87], v[20:23]\n"
        "s_waitcnt lgkmcnt(0)\n"
        "v_perm_b32 v24, v109, v108, s63\n"
        "v_perm_b32 v25, v111, v110, s63\n"
        "v_perm_b32 v26, v141, v140, s63\n"
        "v_perm_b32 v27, v143, v142, s63\n"
        "v_mul_f32 v100, v100, v28\n"
        "v_mul_f32 v101, v101, v29\n"
        "v_mul_f32 v102, v102, v30\n"
        "v_mul_f32 v103, v103, v31\n"
        "v_mfma_f32_16x16x32_bf16 v[20:23], v[32:35], v[144:147], v[20:23]\n"
        "v_mul_f32 v44, s58, v44\n"
        "v_mul_f32 v100, v100, v164\n"
        "v_mul_f32 v101, v101, v164\n"
        "v_mul_f32 v102, v102, v164\n"
        "v_mul_f32 v103, v103, v164\n"
        "v_fma_f32 v44, v44, v164, v19\n"
        "v_perm_b32 v28, v101, v100, s63\n"
        "v_perm_b32 v30, v103, v102, s63\n"
        "v_mfma_f32_16x16x32_bf16 v[68:71], v[132:135], v[144:147], 0\n"
        "v_perm_b32 v77, v21, v20, s63\n"
        "v_perm_b32 v81, v23, v22, s63\n"
        "v_perm_b32 v29, 0, v44, s63\n"
        "v_mfma_f32_16x16x32_bf16 v[68:71], v[136:139], v[84:87], v[68:71]\n"
        "v_mfma_f32_16x16x32_bf16 v[68:71], v[52:55], v[148:151], v[68:71]\n"
        "ds_read_b128 v[20:23], v0 offset:12352\n"
        "v_mfma_f32_16x16x32_bf16 v[72:75], v[160:163], v[148:151], 0\n"
        "ds_read_b128 v[108:111], v0 offset:13376\n"
        "v_mfma_f32_16x16x32_bf16 v[72:75], v[128:131], v[84:87], v[72:75]\n"
        "ds_read_b128 v[140:143], v0 offset:14400\n"
        "v_mfma_f32_16x16x32_bf16 v[72:75], v[124:127], v[144:147], v[72:75]\n"
        "s_waitcnt lgkmcnt(2)\n"
        "v_mfma_f32_16x16x32_bf16 v[144:147], v[124:127], v[20:23], 0\n"
        "v_perm_b32 v46, v69, v68, s63\n"
        "v_perm_b32 v47, v71, v70, s63\n"
        "s_waitcnt lgkmcnt(1)\n"
        "v_mfma_f32_16x16x32_bf16 v[144:147], v[128:131], v[108:111], v[144:147]\n"
        "s_waitcnt lgkmcnt(0)\n"
        "v_mfma_f32_16x16x32_bf16 v[144:147], v[160:163], v[140:143], v[144:147]\n"
        "v_perm_b32 v44, v73, v72, s63\n"
        "v_perm_b32 v45, v75, v74, s63\n"
        "v_mfma_f32_16x16x32_bf16 v[148:151], v[52:55], v[140:143], 0\n"
        "v_mfma_f32_16x16x32_bf16 v[148:151], v[136:139], v[108:111], v[148:151]\n"
        "v_mfma_f32_16x16x32_bf16 v[148:151], v[132:135], v[20:23], v[148:151]\n"
        "ds_read_b128 v[72:75], v0 offset:17488\n"
        "v_mfma_f32_16x16x32_bf16 v[84:87], v[32:35], v[20:23], 0\n"
        "ds_read_b128 v[68:71], v0 offset:16464\n"
        "v_perm_b32 v100, v145, v144, s63\n"
        "v_perm_b32 v101, v147, v146, s63\n"
        "v_mfma_f32_16x16x32_bf16 v[84:87], v[36:39], v[108:111], v[84:87]\n"
        "ds_read_b128 v[20:23], v0 offset:15440\n"
        "v_mfma_f32_16x16x32_bf16 v[84:87], v[60:63], v[140:143], v[84:87]\n"
        "v_perm_b32 v102, v149, v148, s63\n"
        "v_perm_b32 v103, v151, v150, s63\n"
        "s_waitcnt lgkmcnt(2)\n"
        "v_mfma_f32_16x16x32_bf16 v[108:111], v[60:63], v[72:75], 0\n"
        "s_waitcnt lgkmcnt(1)\n"
        "v_mfma_f32_16x16x32_bf16 v[108:111], v[36:39], v[68:71], v[108:111]\n"
        "s_waitcnt lgkmcnt(0)\n"
        "v_mfma_f32_16x16x32_bf16 v[108:111], v[32:35], v[20:23], v[108:111]\n"
        "ds_read2_b32 v[82:83], v11 offset0:0 offset1:3\n"
        "v_perm_b32 v89, v85, v84, s63\n"
        "v_perm_b32 v93, v87, v86, s63\n"
        "v_mov_b32_dpp v59, v19 row_shr:4 row_mask:0xf bank_mask:0xf bound_ctrl:0\n"
        "v_mfma_f32_16x16x32_bf16 v[36:39], v[132:135], v[20:23], 0\n"
        "ds_read_b64_tr_b16 v[78:79], v9 offset:0\n"
        "v_mfma_f32_16x16x32_bf16 v[36:39], v[136:139], v[68:71], v[36:39]\n"
        "ds_read2_b32 v[94:95], v11 offset0:1 offset1:4\n"
        "v_perm_b32 v113, v109, v108, s63\n"
        "v_perm_b32 v117, v111, v110, s63\n"
        "v_mfma_f32_16x16x32_bf16 v[36:39], v[52:55], v[72:75], v[36:39]\n"
        "s_add_u32 s31, s31, 1\n"
        "ds_read_b64_tr_b16 v[90:91], v9 offset:16\n"
        "v_mfma_f32_16x16x32_bf16 v[32:35], v[160:163], v[72:75], 0\n"
        "s_cmp_ge_u32 s31, s71\n"
        "ds_read2_b32 v[118:119], v11 offset0:2 offset1:5\n"
        "v_mfma_f32_16x16x32_bf16 v[32:35], v[128:131], v[68:71], v[32:35]\n"
        "s_cbranch_scc1 L52A_%=\n"
        "L53A_%=:\n"
        "ds_read_b64_tr_b16 v[114:115], v9 offset:32\n"
        "v_perm_b32 v110, v37, v36, s63\n"
        "v_perm_b32 v111, v39, v38, s63\n"
        "v_mfma_f32_16x16x32_bf16 v[32:35], v[124:127], v[20:23], v[32:35]\n"
        "s_add_u32 s16, s16, 256\n"
        "v_mfma_f32_16x16x32_bf16 v[20:23], v[48:51], v[40:43], 0\n"
        "s_waitcnt lgkmcnt(4)\n"
        "s_cmp_ge_u32 s31, s43\n"
        "v_smfmac_f32_16x16x64_bf16 v[20:23], v[56:59], v[76:83], v18\n"
        "s_cselect_b32 s39, s22, s16\n"
        "v_mfma_f32_16x16x32_bf16 v[36:39], v[48:51], v[96:99], 0\n"
        "s_waitcnt lgkmcnt(2)\n"
        "v_perm_b32 v108, v33, v32, s63\n"
        "v_perm_b32 v109, v35, v34, s63\n"
        "s_cmp_gt_u32 s31, s43\n"
        "v_smfmac_f32_16x16x64_bf16 v[36:39], v[56:59], v[88:95], v18\n"
        "s_cselect_b32 s39, s23, s39\n"
        "ds_read_b64_tr_b16 v[78:79], v9 offset:48\n"
        "v_mfma_f32_16x16x32_bf16 v[32:35], v[48:51], v[104:107], 0\n"
        "s_waitcnt lgkmcnt(1)\n"
        "s_mul_hi_u32 s38, s39, 58835169\n"
        "v_smfmac_f32_16x16x64_bf16 v[32:35], v[56:59], v[112:119], v18\n"
        "s_mul_i32 s39, s39, 1344\n"
        "ds_read_b64_tr_b16 v[90:91], v9 offset:64\n"
        "v_cvt_pk_bf16_f32 v152, v20, v36\n"
        "v_cvt_pk_bf16_f32 v156, v21, v37\n"
        "v_cvt_pk_bf16_f32 v160, v22, v38\n"
        "v_cvt_pk_bf16_f32 v164, v23, v39\n"
        "v_mfma_f32_16x16x32_bf16 v[68:71], v[48:51], v[44:47], 0\n"
        "s_mul_i32 s38, s38, 588672\n"
        "s_waitcnt lgkmcnt(1)\n"
        "v_smfmac_f32_16x16x64_bf16 v[68:71], v[56:59], v[76:83], v18 abid:1\n"
        "s_add_u32 s39, s39, s38\n"
        "ds_read_b64_tr_b16 v[114:115], v9 offset:80\n"
        "v_mfma_f32_16x16x32_bf16 v[60:63], v[48:51], v[100:103], 0\n"
        "s_add_u32 s39, s39, s17\n"
        "ds_read_b128 v[36:39], v2\n"
        "v_mov_b64 v[20:21], 0\n"
        "v_mov_b64 v[22:23], 0\n"
        "s_waitcnt lgkmcnt(2)\n"
        "v_smfmac_f32_16x16x64_bf16 v[60:63], v[56:59], v[88:95], v18 abid:1\n"
        "s_mul_i32 s68, s31, 3\n"
        "ds_read_b128 v[52:55], v3\n"
        "v_cvt_pk_bf16_f32 v153, v32, v68\n"
        "v_cvt_pk_bf16_f32 v157, v33, v69\n"
        "v_cvt_pk_bf16_f32 v161, v34, v70\n"
        "v_cvt_pk_bf16_f32 v165, v35, v71\n"
        "v_mfma_f32_16x16x32_bf16 v[68:71], v[48:51], v[108:111], 0\n"
        "s_bitcmp1_b32 s31, 0\n"
        "v_mov_b64 v[32:33], 0\n"
        "v_mov_b64 v[34:35], 0\n"
        "s_waitcnt lgkmcnt(2)\n"
        "v_smfmac_f32_16x16x64_bf16 v[68:71], v[56:59], v[112:119], v18 abid:1\n"
        "s_cselect_b32 s38, 3, 6\n"
        "s_waitcnt lgkmcnt(1)\n"
        "v_mov_b32_dpp v67, v19 row_shr:4 row_mask:0xf bank_mask:0xf bound_ctrl:0\n"
        "v_smfmac_f32_16x16x64_bf16 v[20:23], v[36:39], v[40:47], v4\n"
        "s_add_u32 s68, s68, s38\n"
        "v_mov_b64 v[48:49], 0\n"
        "v_mov_b64 v[50:51], 0\n"
        "s_waitcnt lgkmcnt(0)\n"
        "v_smfmac_f32_16x16x64_bf16 v[20:23], v[52:55], v[76:83], v4 abid:1\n"
        "s_add_u32 s37, s35, 0\n"
        "s_cmp_lt_u32 s31, s14\n"
        "v_cvt_pk_bf16_f32 v154, v60, v68\n"
        "v_cvt_pk_bf16_f32 v158, v61, v69\n"
        "v_cvt_pk_bf16_f32 v162, v62, v70\n"
        "v_cvt_pk_bf16_f32 v166, v63, v71\n"
        "v_smfmac_f32_16x16x64_bf16 v[32:35], v[36:39], v[96:103], v4\n"
        "s_cselect_b32 s68, s68, 0\n"
        "v_add_f32 v20, v20, v21\n"
        "v_add_f32 v22, v22, v23\n"
        "v_smfmac_f32_16x16x64_bf16 v[32:35], v[52:55], v[88:95], v4 abid:1\n"
        "s_and_b32 s38, s31, 7\n"
        "ds_read_b64_tr_b16 v[78:79], v9 offset:3328\n"
        "v_smfmac_f32_16x16x64_bf16 v[48:51], v[36:39], v[104:111], v4\n"
        "buffer_store_dwordx3 v[152:154], v12, s[8:11], s37 offen sc0 sc1 nt\n"
        "s_cselect_b32 s30, 1, -7\n"
        "v_smfmac_f32_16x16x64_bf16 v[48:51], v[52:55], v[112:119], v4 abid:1\n"
        "s_mul_i32 s29, s30, 192\n"
        "ds_read_b64_tr_b16 v[90:91], v9 offset:3344\n"
        "v_add_f32 v32, v32, v33\n"
        "v_add_f32 v34, v34, v35\n"
        "v_cvt_pk_bf16_f32 v148, v20, v32\n"
        "v_mfma_f32_16x16x32_bf16 v[152:155], v[120:123], v[40:43], 0\n"
        "s_mul_i32 s30, s30, 392\n"
        "s_waitcnt lgkmcnt(1)\n"
        "v_smfmac_f32_16x16x64_bf16 v[152:155], v[64:67], v[76:83], v18\n"
        "s_add_u32 s37, s35, 0\n"
        "s_cmp_eq_u32 s69, s18\n"
        "ds_read_b64_tr_b16 v[114:115], v9 offset:3360\n"
        "v_add_f32 v48, v48, v49\n"
        "v_add_f32 v50, v50, v51\n"
        "v_cvt_pk_bf16_f32 v149, v48, v22\n"
        "v_cvt_pk_bf16_f32 v150, v34, v50\n"
        "v_mfma_f32_16x16x32_bf16 v[56:59], v[120:123], v[96:99], 0\n"
        "s_cselect_b32 s34, -7488, s70\n"
        "s_waitcnt lgkmcnt(1)\n"
        "v_smfmac_f32_16x16x64_bf16 v[56:59], v[64:67], v[88:95], v18\n"
        "s_add_u32 s69, s69, s34\n"
        "ds_read_b64_tr_b16 v[78:79], v9 offset:3376\n"
        "v_writelane_b32 v10, s69, 56\n"
        "v_add_u32 v11, s30, v11\n"
        "s_mov_b64 exec, s[54:55]\n"
        "v_add_u32 v16, s29, v16\n"
        "v_add_u32 v8, s34, v8\n"
        "s_mov_b64 exec, -1\n"
        "v_mfma_f32_16x16x32_bf16 v[72:75], v[120:123], v[104:107], 0\n"
        "buffer_store_dwordx3 v[156:158], v15, s[8:11], s37 offen offset:768 sc0 sc1 nt\n"
        "s_mov_b64 exec, s[50:51]\n"
        "v_add_u32 v5, s29, v5\n"
        "v_add_u32 v6, s34, v6\n"
        "s_mov_b64 exec, -1\n"
        "s_waitcnt lgkmcnt(1)\n"
        "v_smfmac_f32_16x16x64_bf16 v[72:75], v[64:67], v[112:119], v18\n"
        "ds_read_b64_tr_b16 v[90:91], v9 offset:3392\n"
        "v_mov_b32_dpp v31, v19 row_shr:4 row_mask:0xf bank_mask:0xf bound_ctrl:0\n"
        "v_mfma_f32_16x16x32_bf16 v[20:23], v[120:123], v[44:47], 0\n"
        "s_waitcnt lgkmcnt(1)\n"
        "v_smfmac_f32_16x16x64_bf16 v[20:23], v[64:67], v[76:83], v18 abid:1\n"
        "s_add_u32 s37, s35, 98112\n"
        "ds_read_b64_tr_b16 v[114:115], v9 offset:3408\n"
        "v_mfma_f32_16x16x32_bf16 v[84:87], v[120:123], v[100:103], 0\n"
        "s_waitcnt lgkmcnt(1)\n"
        "v_smfmac_f32_16x16x64_bf16 v[84:87], v[64:67], v[88:95], v18 abid:1\n"
        "ds_read_b64_tr_b16 v[78:79], v9 offset:6656\n"
        "v_mfma_f32_16x16x32_bf16 v[60:63], v[120:123], v[108:111], 0\n"
        "buffer_store_dwordx3 v[160:162], v12, s[8:11], s37 offen offset:192 sc0 sc1 nt\n"
        "ds_read_b32 v167, v0 offset:21604\n"
        "s_waitcnt lgkmcnt(2)\n"
        "v_smfmac_f32_16x16x64_bf16 v[60:63], v[64:67], v[112:119], v18 abid:1\n"
        "ds_read_b64_tr_b16 v[90:91], v9 offset:6672\n"
        "v_mfma_f32_16x16x32_bf16 v[68:71], v[24:27], v[40:43], 0\n"
        "s_waitcnt lgkmcnt(2)\n"
        "v_smfmac_f32_16x16x64_bf16 v[68:71], v[28:31], v[76:83], v18\n"
        "s_add_u32 s37, s35, 98112\n"
        "ds_read_b64_tr_b16 v[114:115], v9 offset:6688\n"
        "v_mfma_f32_16x16x32_bf16 v[36:39], v[24:27], v[96:99], 0\n"
        "s_waitcnt lgkmcnt(1)\n"
        "v_smfmac_f32_16x16x64_bf16 v[36:39], v[28:31], v[88:95], v18\n"
        "ds_read_b64_tr_b16 v[78:79], v9 offset:6704\n"
        "v_readfirstlane_b32 s40, v167\n"
        "v_mfma_f32_16x16x32_bf16 v[40:43], v[24:27], v[104:107], 0\n"
        "buffer_store_dwordx3 v[164:166], v14, s[8:11], s37 offen offset:960 sc0 sc1 nt\n"
        "s_add_u32 s37, s35, 588672\n"
        "s_add_u32 s46, s37, 1344\n"
        "s_cmp_lt_i32 s40, s68\n"
        "s_waitcnt lgkmcnt(1)\n"
        "v_smfmac_f32_16x16x64_bf16 v[40:43], v[28:31], v[112:119], v18\n"
        "buffer_store_dwordx3 v[148:150], v12, s[44:47], s37 offen offset:1152 sc0 sc1 nt\n"
        "ds_read_b64_tr_b16 v[90:91], v9 offset:6720\n"
        "v_mfma_f32_16x16x32_bf16 v[64:67], v[24:27], v[44:47], 0\n"
        "s_waitcnt lgkmcnt(1)\n"
        "v_smfmac_f32_16x16x64_bf16 v[64:67], v[28:31], v[76:83], v18 abid:1\n"
        "ds_read_b64_tr_b16 v[114:115], v9 offset:6736\n"
        "s_mov_b64 exec, 1\n"
        "ds_inc_u32 v0, v1 offset:21608\n"
        "s_mov_b64 exec, -1\n"
        "s_cbranch_scc1 L56A_%=\n"
        "L57A_%=:\n"
        "ds_read_b128 v[124:127], v7 offset:40064\n"
        "ds_read_b128 v[132:135], v7 offset:43392\n"
        "ds_read_b128 v[128:131], v7 offset:40128\n"
        "ds_read_b128 v[136:139], v7 offset:43456\n"
        "v_mfma_f32_16x16x32_bf16 v[104:107], v[24:27], v[100:103], 0\n"
        "s_waitcnt lgkmcnt(6)\n"
        "ds_read_b128 v[160:163], v7 offset:40192\n"
        "ds_read_b128 v[52:55], v7 offset:43520\n"
        "s_mov_b32 s36, s35\n"
        "v_smfmac_f32_16x16x64_bf16 v[104:107], v[28:31], v[88:95], v18 abid:1\n"
        "s_mov_b32 s35, s39\n"
        "v_mfma_f32_16x16x32_bf16 v[88:91], v[24:27], v[108:111], 0\n"
        "s_waitcnt lgkmcnt(7)\n"
        "v_smfmac_f32_16x16x64_bf16 v[88:91], v[28:31], v[112:119], v18 abid:1\n"
        "s_setprio 0\n"
        "s_waitcnt lgkmcnt(0)\n"
        "s_nop 15\n"
        "s_branch CNEXTA_%=\n"
        "MTOPB_%=:\n"
        "s_waitcnt lgkmcnt(5)\n"
        "v_mfma_f32_16x16x32_bf16 v[24:27], v[168:171], v[124:127], 0\n"
        "v_cvt_pk_bf16_f32 v112, v152, v56\n"
        "v_cvt_pk_bf16_f32 v113, v72, v20\n"
        "v_cvt_pk_bf16_f32 v114, v84, v60\n"
        "v_cvt_pk_bf16_f32 v116, v153, v57\n"
        "v_cvt_pk_bf16_f32 v117, v73, v21\n"
        "v_cvt_pk_bf16_f32 v118, v85, v61\n"
        "v_cvt_pk_bf16_f32 v120, v154, v58\n"
        "v_cvt_pk_bf16_f32 v121, v74, v22\n"
        "s_waitcnt lgkmcnt(3)\n"
        "v_mfma_f32_16x16x32_bf16 v[24:27], v[172:175], v[128:131], v[24:27]\n"
        "v_cvt_pk_bf16_f32 v122, v86, v62\n"
        "v_cvt_pk_bf16_f32 v140, v155, v59\n"
        "v_cvt_pk_bf16_f32 v141, v75, v23\n"
        "v_cvt_pk_bf16_f32 v142, v87, v63\n"
        "v_cvt_pk_bf16_f32 v144, v68, v36\n"
        "v_cvt_pk_bf16_f32 v145, v40, v64\n"
        "v_cvt_pk_bf16_f32 v146, v104, v88\n"
        "v_cvt_pk_bf16_f32 v148, v69, v37\n"
        "s_waitcnt lgkmcnt(1)\n"
        "v_mfma_f32_16x16x32_bf16 v[24:27], v[176:179], v[160:163], v[24:27]\n"
        "v_cvt_pk_bf16_f32 v149, v41, v65\n"
        "v_cvt_pk_bf16_f32 v150, v105, v89\n"
        "v_cvt_pk_bf16_f32 v152, v70, v38\n"
        "v_cvt_pk_bf16_f32 v153, v42, v66\n"
        "v_cvt_pk_bf16_f32 v154, v106, v90\n"
        "v_cvt_pk_bf16_f32 v156, v71, v39\n"
        "v_cvt_pk_bf16_f32 v157, v43, v67\n"
        "v_cvt_pk_bf16_f32 v158, v107, v91\n"
        "v_mfma_f32_16x16x32_bf16 v[32:35], v[188:191], v[160:163], 0\n"
        "v_perm_b32 v40, v25, v24, s63\n"
        "v_perm_b32 v41, v27, v26, s63\n"
        "v_mfma_f32_16x16x32_bf16 v[32:35], v[184:187], v[128:131], v[32:35]\n"
        "v_mfma_f32_16x16x32_bf16 v[32:35], v[180:183], v[124:127], v[32:35]\n"
        "s_add_u32 s37, s36, 196224\n"
        "ds_read_b128 v[28:31], v16 offset:128\n"
        "ds_read_b32 v159, v10\n"
        "v_mfma_f32_16x16x32_bf16 v[24:27], v[192:195], v[124:127], 0\n"
        "ds_read_b128 v[20:23], v5\n"
        "v_mfma_f32_16x16x32_bf16 v[24:27], v[196:199], v[128:131], v[24:27]\n"
        "ds_read_b64 v[164:165], v11 offset:192\n"
        "v_mfma_f32_16x16x32_bf16 v[24:27], v[200:203], v[160:163], v[24:27]\n"
        "buffer_store_dwordx3 v[112:114], v12, s[8:11], s37 offen offset:384 sc0 sc1 nt\n"
        "v_perm_b32 v42, v33, v32, s63\n"
        "v_perm_b32 v43, v35, v34, s63\n"
        "s_waitcnt lgkmcnt(3)\n"
        "v_mfma_f32_16x16x32_bf16 v[48:51], v[28:31], v[160:163], 0\n"
        "ds_read_b128 v[60:63], v7 offset:46848\n"
        "ds_read_i16 v167, v0 offset:20584\n"
        "ds_read_i16 v155, v0 offset:20586\n"
        "v_perm_b32 v44, v25, v24, s63\n"
        "v_perm_b32 v45, v27, v26, s63\n"
        "s_waitcnt lgkmcnt(3)\n"
        "v_smfmac_f32_16x16x64_bf16 v[48:51], v[20:23], v[124:131], v4\n"
        "s_add_u32 s37, s36, 196224\n"
        "ds_read_b128 v[32:35], v7 offset:46720\n"
        "ds_read_b128 v[36:39], v7 offset:46784\n"
        "v_mfma_f32_16x16x32_bf16 v[56:59], v[28:31], v[52:55], 0\n"
        "v_smfmac_f32_16x16x64_bf16 v[56:59], v[20:23], v[132:139], v4\n"
        "s_setprio 2\n"
        "s_waitcnt lgkmcnt(4)\n"
        "v_mfma_f32_16x16x32_bf16 v[24:27], v[28:31], v[60:63], 0\n"
        "buffer_store_dwordx3 v[116:118], v13, s[8:11], s37 offen offset:1152 sc0 sc1 nt\n"
        "v_add_f32_dpp v165, v50, v51 quad_perm:[0,1,2,3] row_mask:0x1 bank_mask:0xf\n"
        "s_waitcnt lgkmcnt(2)\n"
        "v_add_u32 v167, v9, v167\n"
        "v_add_f32_dpp v164, v48, v49 quad_perm:[0,1,2,3] row_mask:0x1 bank_mask:0xf\n"
        "v_add_f32_dpp v165, v58, v59 quad_perm:[0,1,2,3] row_mask:0x2 bank_mask:0xf\n"
        "v_add_u32 v155, v9, v155\n"
        "v_add_f32_dpp v164, v56, v57 quad_perm:[0,1,2,3] row_mask:0x2 bank_mask:0xf\n"
        "s_waitcnt lgkmcnt(0)\n"
        "v_smfmac_f32_16x16x64_bf16 v[24:27], v[20:23], v[32:39], v4\n"
        "v_mfma_f32_16x16x32_bf16 v[28:31], v[212:215], v[160:163], 0\n"
        "s_add_u32 s37, s36, 294336\n"
        "v_mfma_f32_16x16x32_bf16 v[28:31], v[208:211], v[128:131], v[28:31]\n"
        "v_mfma_f32_16x16x32_bf16 v[28:31], v[204:207], v[124:127], v[28:31]\n"
        "v_mfma_f32_16x16x32_bf16 v[20:23], v[216:219], v[124:127], 0\n"
        "buffer_store_dwordx3 v[120:122], v12, s[8:11], s37 offen offset:576 sc0 sc1 nt\n"
        "v_mfma_f32_16x16x32_bf16 v[20:23], v[220:223], v[128:131], v[20:23]\n"
        "v_mfma_f32_16x16x32_bf16 v[20:23], v[224:227], v[160:163], v[20:23]\n"
        "s_add_u32 s37, s36, 392448\n"
        "v_add_f32_dpp v165, v26, v27 quad_perm:[0,1,2,3] row_mask:0x4 bank_mask:0xf\n"
        "v_mul_f32 v166, s52, v165\n"
        "v_fma_f32 v166, -v166, v165, v159\n"
        "v_perm_b32 v46, v29, v28, s63\n"
        "v_perm_b32 v47, v31, v30, s63\n"
        "v_add_f32_e64 v166, s53, |v166|\n"
        "v_rsq_f32 v159, v166\n"
        "v_mfma_f32_16x16x32_bf16 v[56:59], v[236:239], v[160:163], 0\n"
        "s_mov_b32 exec_hi, 0xffff\n"
        "ds_write_b32 v10, v159\n"
        "s_mov_b32 exec_hi, -1\n"
        "v_perm_b32 v48, v21, v20, s63\n"
        "v_perm_b32 v49, v23, v22, s63\n"
        "ds_read_b32 v20, v167 offset:40064\n"
        "v_add_f32_dpp v164, v24, v25 quad_perm:[0,1,2,3] row_mask:0x4 bank_mask:0xf\n"
        "v_mfma_f32_16x16x32_bf16 v[56:59], v[232:235], v[128:131], v[56:59]\n"
        "s_mov_b32 exec_hi, 0xffff\n"
        "v_add_u32 v10, -40064, v10\n"
        "s_mov_b32 exec_hi, -1\n"
        "ds_read_b32 v21, v167 offset:40896\n"
        "ds_read_b32 v22, v167 offset:41728\n"
        "ds_read_b32 v23, v167 offset:42560\n"
        "ds_read_b32 v24, v167 offset:43392\n"
        "v_mul_f32 v164, v164, v159\n"
        "v_mfma_f32_16x16x32_bf16 v[56:59], v[228:231], v[124:127], v[56:59]\n"
        "buffer_store_dwordx3 v[140:142], v12, s[8:11], s37 offen sc0 sc1 nt\n"
        "ds_read_b32 v25, v167 offset:44224\n"
        "ds_read_b32 v26, v167 offset:45056\n"
        "ds_read_b32 v27, v167 offset:45888\n"
        "ds_read_b32 v28, v167 offset:46720\n"
        "ds_read_b32 v29, v167 offset:47552\n"
        "ds_read_b32 v30, v167 offset:48384\n"
        "ds_read_b32 v31, v167 offset:49216\n"
        "v_perm_b32 v50, v57, v56, s63\n"
        "v_perm_b32 v51, v59, v58, s63\n"
        "v_readlane_b32 s58, v159, 56\n"
        "v_mfma_f32_16x16x32_bf16 v[56:59], v[124:127], v[40:43], 0\n"
        "s_mov_b64 exec, s[56:57]\n"
        "v_mov_b32 v164, 0xff800000\n"
        "s_mov_b64 exec, -1\n"
        "ds_read_b32 v165, v155 offset:40064\n"
        "ds_read_b32 v166, v155 offset:43392\n"
        "v_max_f32_dpp v151, v164, v164 row_ror:8 row_mask:0xf bank_mask:0xf\n"
        "v_mfma_f32_16x16x32_bf16 v[56:59], v[128:131], v[44:47], v[56:59]\n"
        "s_add_u32 s37, s36, 392448\n"
        "v_max_f32_dpp v151, v151, v151 row_ror:4 row_mask:0xf bank_mask:0xf\n"
        "ds_read_b32 v167, v155 offset:46720\n"
        "v_mfma_f32_16x16x32_bf16 v[56:59], v[160:163], v[48:51], v[56:59]\n"
        "v_max_f32_dpp v151, v151, v151 row_ror:2 row_mask:0xf bank_mask:0xf\n"
        "v_mfma_f32_16x16x32_bf16 v[72:75], v[52:55], v[48:51], 0\n"
        "v_mfma_f32_16x16x32_bf16 v[72:75], v[136:139], v[44:47], v[72:75]\n"
        "buffer_store_dwordx3 v[144:146], v15, s[8:11], s37 offen offset:768 sc0 sc1 nt\n"
        "v_max_f32_dpp v151, v151, v151 row_ror:1 row_mask:0xf bank_mask:0xf\n"
        "v_mfma_f32_16x16x32_bf16 v[72:75], v[132:135], v[40:43], v[72:75]\n"
        "ds_read_b128 v[88:91], v8 offset:128\n"
        "v_mfma_f32_16x16x32_bf16 v[68:71], v[32:35], v[40:43], 0\n"
        "s_add_u32 s37, s36, 490560\n"
        "ds_read2_b32 v[92:93], v6 offset0:0 offset1:2\n"
        "ds_read2_b32 v[94:95], v6 offset0:8 offset1:10\n"
        "s_waitcnt lgkmcnt(3)\n"
        "v_mul_f32 v56, v56, v20\n"
        "v_mul_f32 v57, v57, v21\n"
        "v_mul_f32 v58, v58, v22\n"
        "v_mul_f32 v59, v59, v23\n"
        "v_mul_f32 v72, v72, v24\n"
        "v_mfma_f32_16x16x32_bf16 v[68:71], v[36:39], v[44:47], v[68:71]\n"
        "v_max3_f32 v155, v56, v57, v58\n"
        "v_mul_f32 v73, v73, v25\n"
        "v_mul_f32 v74, v74, v26\n"
        "v_max3_f32 v155, v155, v59, v72\n"
        "v_mul_f32 v75, v75, v27\n"
        "v_max3_f32 v155, v155, v73, v74\n"
        "v_mfma_f32_16x16x32_bf16 v[68:71], v[60:63], v[48:51], v[68:71]\n"
        "s_waitcnt lgkmcnt(2)\n"
        "v_mfma_f32_16x16x32_bf16 v[76:79], v[88:91], v[48:51], 0\n"
        "buffer_store_dwordx3 v[148:150], v12, s[8:11], s37 offen offset:192 sc0 sc1 nt\n"
        "s_waitcnt lgkmcnt(0)\n"
        "v_smfmac_f32_16x16x64_bf16 v[76:79], v[92:95], v[40:47], v4\n"
        "v_mfma_f32_16x16x32_bf16 v[48:51], v[168:171], v[132:135], 0\n"
        "s_add_u32 s37, s36, 490560\n"
        "v_mfma_f32_16x16x32_bf16 v[48:51], v[172:175], v[136:139], v[48:51]\n"
        "v_mul_f32 v68, v68, v28\n"
        "v_mul_f32 v69, v69, v29\n"
        "v_mul_f32 v70, v70, v30\n"
        "v_max3_f32 v155, v155, v75, v68\n"
        "v_mul_f32 v71, v71, v31\n"
        "v_add_f32 v76, v76, v77\n"
        "v_mul_f32 v76, s58, v76\n"
        "v_max3_f32 v155, v155, v69, v70\n"
        "v_mfma_f32_16x16x32_bf16 v[48:51], v[176:179], v[52:55], v[48:51]\n"
        "s_mov_b64 exec, s[48:49]\n"
        "v_mov_b32 v76, 0xff800000\n"
        "s_mov_b64 exec, -1\n"
        "v_max3_f32 v155, v155, v71, v76\n"
        "v_mfma_f32_16x16x32_bf16 v[44:47], v[188:191], v[52:55], 0\n"
        "buffer_store_dwordx3 v[152:154], v14, s[8:11], s37 offen offset:960 sc0 sc1 nt\n"
        "v_permlane32_swap_b32 v155, v151\n"
        "v_max_f32 v155, v155, v151\n"
        "v_perm_b32 v40, v49, v48, s63\n"
        "v_perm_b32 v41, v51, v50, s63\n"
        "v_mov_b32 v154, v155\n"
        "v_mfma_f32_16x16x32_bf16 v[44:47], v[184:187], v[136:139], v[44:47]\n"
        "v_mfma_f32_16x16x32_bf16 v[44:47], v[180:183], v[132:135], v[44:47]\n"
        "s_add_u32 s37, s36, 588672\n"
        "v_permlane16_swap_b32 v155, v154\n"
        "v_max_f32 v155, v155, v154\n"
        "v_mov_b32 v151, v155\n"
        "v_mfma_f32_16x16x32_bf16 v[64:67], v[192:195], v[132:135], 0\n"
        "v_mfma_f32_16x16x32_bf16 v[64:67], v[196:199], v[136:139], v[64:67]\n"
        "v_permlane32_swap_b32 v155, v151\n"
        "v_mul_f32 v155, v155, v165\n"
        "v_perm_b32 v42, v45, v44, s63\n"
        "v_perm_b32 v43, v47, v46, s63\n"
        "v_fma_f32 v56, v56, v165, -v155\n"
        "v_fma_f32 v57, v57, v165, -v155\n"
        "v_fma_f32 v58, v58, v165, -v155\n"
        "v_mfma_f32_16x16x32_bf16 v[64:67], v[200:203], v[52:55], v[64:67]\n"
        "buffer_store_dwordx3 v[156:158], v12, s[8:11], s37 offen offset:384 sc0 sc1 nt\n"
        "v_exp_f32 v56, v56\n"
        "v_exp_f32 v57, v57\n"
        "v_exp_f32 v58, v58\n"
        "v_fma_f32 v59, v59, v165, -v155\n"
        "v_add_f32 v158, v56, v57\n"
        "v_exp_f32 v59, v59\n"
        "v_mfma_f32_16x16x32_bf16 v[48:51], v[212:215], v[52:55], 0\n"
        "v_perm_b32 v44, v65, v64, s63\n"
        "v_perm_b32 v45, v67, v66, s63\n"
        "v_fma_f32 v72, v72, v165, -v155\n"
        "v_add_f32 v158, v158, v58\n"
        "v_exp_f32 v72, v72\n"
        "v_fma_f32 v73, v73, v165, -v155\n"
        "v_add_f32 v158, v158, v59\n"
        "v_mfma_f32_16x16x32_bf16 v[48:51], v[208:211], v[136:139], v[48:51]\n"
        "v_exp_f32 v73, v73\n"
        "v_fma_f32 v74, v74, v165, -v155\n"
        "v_add_f32 v158, v158, v72\n"
        "v_exp_f32 v74, v74\n"
        "v_fma_f32 v75, v75, v165, -v155\n"
        "v_sub_f32 v164, v164, v151\n"
        "v_mfma_f32_16x16x32_bf16 v[48:51], v[204:207], v[132:135], v[48:51]\n"
        "v_add_f32 v158, v158, v73\n"
        "v_exp_f32 v75, v75\n"
        "v_fma_f32 v68, v68, v165, -v155\n"
        "v_mul_f32 v164, s58, v164\n"
        "v_add_f32 v158, v158, v74\n"
        "v_exp_f32 v68, v68\n"
        "v_mfma_f32_16x16x32_bf16 v[64:67], v[216:219], v[132:135], 0\n"
        "v_fma_f32 v69, v69, v165, -v155\n"
        "v_perm_b32 v46, v49, v48, s63\n"
        "v_perm_b32 v47, v51, v50, s63\n"
        "v_exp_f32 v164, v164\n"
        "v_add_f32 v158, v158, v75\n"
        "v_exp_f32 v69, v69\n"
        "v_mfma_f32_16x16x32_bf16 v[64:67], v[220:223], v[136:139], v[64:67]\n"
        "v_fma_f32 v70, v70, v165, -v155\n"
        "v_add_f32 v158, v158, v68\n"
        "v_exp_f32 v70, v70\n"
        "v_fma_f32 v71, v71, v165, -v155\n"
        "v_add_f32_dpp v157, v164, v164 row_ror:8 row_mask:0xf bank_mask:0xf\n"
        "v_add_f32 v158, v158, v69\n"
        "v_fma_f32 v76, v76, v165, -v155\n"
        "v_mfma_f32_16x16x32_bf16 v[64:67], v[224:227], v[52:55], v[64:67]\n"
        "v_exp_f32 v71, v71\n"
        "v_add_f32_dpp v157, v157, v157 row_ror:4 row_mask:0xf bank_mask:0xf\n"
        "v_add_f32 v158, v158, v70\n"
        "v_exp_f32 v76, v76\n"
        "v_add_f32_dpp v157, v157, v157 row_ror:2 row_mask:0xf bank_mask:0xf\n"
        "v_add_f32 v158, v158, v71\n"
        "v_mfma_f32_16x16x32_bf16 v[48:51], v[236:239], v[52:55], 0\n"
        "v_add_f32_dpp v157, v157, v157 row_ror:1 row_mask:0xf bank_mask:0xf\n"
        "v_perm_b32 v80, v65, v64, s63\n"
        "v_perm_b32 v81, v67, v66, s63\n"
        "v_add_f32 v158, v158, v76\n"
        "v_mul_f32 v56, v56, v20\n"
        "v_mul_f32 v57, v57, v21\n"
        "v_permlane32_swap_b32 v158, v157\n"
        "v_mfma_f32_16x16x32_bf16 v[48:51], v[232:235], v[136:139], v[48:51]\n"
        "v_add_f32 v158, v158, v157\n"
        "v_mov_b32 v165, v158\n"
        "v_mul_f32 v58, v58, v22\n"
        "v_mul_f32 v59, v59, v23\n"
        "v_permlane16_swap_b32 v158, v165\n"
        "v_add_f32 v158, v158, v165\n"
        "v_mul_f32 v72, v72, v24\n"
        "v_mfma_f32_16x16x32_bf16 v[48:51], v[228:231], v[132:135], v[48:51]\n"
        "v_rcp_f32 v158, v158\n"
        "v_mul_f32 v73, v73, v25\n"
        "v_mov_b32 v157, v158\n"
        "v_mul_f32 v74, v74, v26\n"
        "v_mul_f32 v75, v75, v27\n"
        "v_permlane32_swap_b32 v158, v157\n"
        "v_mfma_f32_16x16x32_bf16 v[64:67], v[124:127], v[40:43], 0\n"
        "v_mul_f32 v56, v56, v158\n"
        "v_perm_b32 v82, v49, v48, s63\n"
        "v_perm_b32 v83, v51, v50, s63\n"
        "v_mul_f32 v57, v57, v158\n"
        "v_mul_f32 v58, v58, v158\n"
        "v_mul_f32 v59, v59, v158\n"
        "v_mul_f32 v72, v72, v158\n"
        "v_mul_f32 v73, v73, v158\n"
        "v_mfma_f32_16x16x32_bf16 v[64:67], v[128:131], v[44:47], v[64:67]\n"
        "v_mul_f32 v74, v74, v158\n"
        "v_mul_f32 v75, v75, v158\n"
        "v_perm_b32 v48, v57, v56, s63\n"
        "v_perm_b32 v49, v59, v58, s63\n"
        "v_perm_b32 v50, v73, v72, s63\n"
        "v_perm_b32 v51, v75, v74, s63\n"
        "v_mul_f32 v68, v68, v28\n"
        "v_mul_f32 v69, v69, v29\n"
        "v_mfma_f32_16x16x32_bf16 v[64:67], v[160:163], v[80:83], v[64:67]\n"
        "v_mul_f32 v70, v70, v30\n"
        "v_mul_f32 v71, v71, v31\n"
        "v_mul_f32 v76, s58, v76\n"
        "v_mul_f32 v68, v68, v158\n"
        "v_mul_f32 v69, v69, v158\n"
        "v_mul_f32 v70, v70, v158\n"
        "v_mul_f32 v71, v71, v158\n"
        "v_fma_f32 v76, v76, v158, v19\n"
        "v_mfma_f32_16x16x32_bf16 v[84:87], v[52:55], v[80:83], 0\n"
        "v_mul_f32 v64, v64, v20\n"
        "v_mul_f32 v65, v65, v21\n"
        "v_mul_f32 v66, v66, v22\n"
        "v_mul_f32 v67, v67, v23\n"
        "v_max3_f32 v165, v64, v65, v66\n"
        "v_perm_b32 v56, v69, v68, s63\n"
        "v_perm_b32 v58, v71, v70, s63\n"
        "v_perm_b32 v57, 0, v76, s63\n"
        "v_mfma_f32_16x16x32_bf16 v[84:87], v[136:139], v[44:47], v[84:87]\n"
        "v_mul_f32 v164, v164, v159\n"
        "v_mul_f32 v164, v164, v157\n"
        "v_perm_b32 v164, 0, v164, s63\n"
        "ds_write_b16 v1, v164\n"
        "v_mfma_f32_16x16x32_bf16 v[84:87], v[132:135], v[40:43], v[84:87]\n"
        "v_mfma_f32_16x16x32_bf16 v[68:71], v[32:35], v[40:43], 0\n"
        "v_mfma_f32_16x16x32_bf16 v[68:71], v[36:39], v[44:47], v[68:71]\n"
        "v_mfma_f32_16x16x32_bf16 v[68:71], v[60:63], v[80:83], v[68:71]\n"
        "v_mfma_f32_16x16x32_bf16 v[72:75], v[88:91], v[80:83], 0\n"
        "v_smfmac_f32_16x16x64_bf16 v[72:75], v[92:95], v[40:47], v4\n"
        "v_mfma_f32_16x16x32_bf16 v[80:83], v[168:171], v[32:35], 0\n"
        "v_mfma_f32_16x16x32_bf16 v[80:83], v[172:175], v[36:39], v[80:83]\n"
        "v_mfma_f32_16x16x32_bf16 v[80:83], v[176:179], v[60:63], v[80:83]\n"
        "v_mul_f32 v84, v84, v24\n"
        "v_mul_f32 v85, v85, v25\n"
        "v_mul_f32 v86, v86, v26\n"
        "v_max3_f32 v165, v165, v67, v84\n"
        "v_mul_f32 v87, v87, v27\n"
        "v_add_f32 v72, v72, v73\n"
        "v_mul_f32 v68, v68, v28\n"
        "v_mul_f32 v72, s58, v72\n"
        "v_mfma_f32_16x16x32_bf16 v[40:43], v[188:191], v[60:63], 0\n"
        "s_mov_b64 exec, s[48:49]\n"
        "v_mov_b32 v72, 0xff800000\n"
        "s_mov_b64 exec, -1\n"
        "v_perm_b32 v76, v81, v80, s63\n"
        "v_perm_b32 v77, v83, v82, s63\n"
        "v_max3_f32 v165, v165, v85, v86\n"
        "v_mul_f32 v69, v69, v29\n"
        "v_mul_f32 v70, v70, v30\n"
        "v_max3_f32 v165, v165, v87, v68\n"
        "v_mfma_f32_16x16x32_bf16 v[40:43], v[184:187], v[36:39], v[40:43]\n"
        "v_mul_f32 v71, v71, v31\n"
        "v_max3_f32 v165, v165, v69, v70\n"
        "v_max3_f32 v165, v165, v71, v72\n"
        "v_mfma_f32_16x16x32_bf16 v[40:43], v[180:183], v[32:35], v[40:43]\n"
        "v_mfma_f32_16x16x32_bf16 v[100:103], v[192:195], v[32:35], 0\n"
        "v_mfma_f32_16x16x32_bf16 v[100:103], v[196:199], v[36:39], v[100:103]\n"
        "v_mfma_f32_16x16x32_bf16 v[100:103], v[200:203], v[60:63], v[100:103]\n"
        "v_mfma_f32_16x16x32_bf16 v[96:99], v[212:215], v[60:63], 0\n"
        "v_mfma_f32_16x16x32_bf16 v[96:99], v[208:211], v[36:39], v[96:99]\n"
        "v_mfma_f32_16x16x32_bf16 v[96:99], v[204:207], v[32:35], v[96:99]\n"
        "v_mfma_f32_16x16x32_bf16 v[44:47], v[216:219], v[32:35], 0\n"
        "v_mfma_f32_16x16x32_bf16 v[44:47], v[220:223], v[36:39], v[44:47]\n"
        "v_perm_b32 v78, v41, v40, s63\n"
        "v_perm_b32 v79, v43, v42, s63\n"
        "v_mfma_f32_16x16x32_bf16 v[44:47], v[224:227], v[60:63], v[44:47]\n"
        "v_perm_b32 v80, v101, v100, s63\n"
        "v_perm_b32 v81, v103, v102, s63\n"
        "v_mfma_f32_16x16x32_bf16 v[40:43], v[236:239], v[60:63], 0\n"
        "v_perm_b32 v82, v97, v96, s63\n"
        "v_perm_b32 v83, v99, v98, s63\n"
        "v_mfma_f32_16x16x32_bf16 v[40:43], v[232:235], v[36:39], v[40:43]\n"
        "v_mfma_f32_16x16x32_bf16 v[40:43], v[228:231], v[32:35], v[40:43]\n"
        "v_mfma_f32_16x16x32_bf16 v[108:111], v[124:127], v[76:79], 0\n"
        "v_perm_b32 v96, v45, v44, s63\n"
        "v_perm_b32 v97, v47, v46, s63\n"
        "v_mfma_f32_16x16x32_bf16 v[108:111], v[128:131], v[80:83], v[108:111]\n"
        "s_nop 3\n"
        "v_perm_b32 v98, v41, v40, s63\n"
        "v_perm_b32 v99, v43, v42, s63\n"
        "s_nop 1\n"
        "v_mfma_f32_16x16x32_bf16 v[108:111], v[160:163], v[96:99], v[108:111]\n"
        "v_mfma_f32_16x16x32_bf16 v[140:143], v[52:55], v[96:99], 0\n"
        "v_mfma_f32_16x16x32_bf16 v[140:143], v[136:139], v[80:83], v[140:143]\n"
        "v_mfma_f32_16x16x32_bf16 v[140:143], v[132:135], v[76:79], v[140:143]\n"
        "v_mfma_f32_16x16x32_bf16 v[100:103], v[32:35], v[76:79], 0\n"
        "v_mfma_f32_16x16x32_bf16 v[100:103], v[36:39], v[80:83], v[100:103]\n"
        "v_mfma_f32_16x16x32_bf16 v[100:103], v[60:63], v[96:99], v[100:103]\n"
        "v_mfma_f32_16x16x32_bf16 v[44:47], v[88:91], v[96:99], 0\n"
        "v_smfmac_f32_16x16x64_bf16 v[44:47], v[92:95], v[76:83], v4\n"
        "v_mul_f32 v108, v108, v20\n"
        "v_mul_f32 v109, v109, v21\n"
        "v_mul_f32 v110, v110, v22\n"
        "v_mul_f32 v111, v111, v23\n"
        "v_mul_f32 v140, v140, v24\n"
        "v_max3_f32 v159, v108, v109, v110\n"
        "v_mul_f32 v141, v141, v25\n"
        "v_mfma_f32_16x16x32_bf16 v[60:63], v[124:127], v[240:243], 0\n"
        "v_add_f32 v44, v44, v45\n"
        "v_mul_f32 v142, v142, v26\n"
        "v_max3_f32 v159, v159, v111, v140\n"
        "v_mul_f32 v143, v143, v27\n"
        "v_mul_f32 v100, v100, v28\n"
        "v_mul_f32 v44, s58, v44\n"
        "v_max3_f32 v159, v159, v141, v142\n"
        "v_mul_f32 v101, v101, v29\n"
        "v_mfma_f32_16x16x32_bf16 v[60:63], v[128:131], v[244:247], v[60:63]\n"
        "s_mov_b64 exec, s[48:49]\n"
        "v_mov_b32 v44, 0xff800000\n"
        "s_mov_b64 exec, -1\n"
        "v_mul_f32 v102, v102, v30\n"
        "v_max3_f32 v159, v159, v143, v100\n"
        "v_mul_f32 v103, v103, v31\n"
        "v_max3_f32 v159, v159, v101, v102\n"
        "v_max3_f32 v159, v159, v103, v44\n"
        "v_mfma_f32_16x16x32_bf16 v[60:63], v[160:163], v[248:251], v[60:63]\n"
        "ds_read_b128 v[32:35], v17 offset:40064\n"
        "v_permlane32_swap_b32 v165, v159\n"
        "v_max_f32 v165, v165, v159\n"
        "v_mov_b32 v164, v165\n"
        "v_mfma_f32_16x16x32_bf16 v[76:79], v[52:55], v[248:251], 0\n"
        "ds_read_b128 v[36:39], v17 offset:40128\n"
        "v_permlane16_swap_b32 v165, v164\n"
        "v_max_f32 v165, v165, v164\n"
        "v_perm_b32 v40, v61, v60, s63\n"
        "v_perm_b32 v41, v63, v62, s63\n"
        "v_mov_b32 v159, v165\n"
        "v_mfma_f32_16x16x32_bf16 v[76:79], v[136:139], v[244:247], v[76:79]\n"
        "ds_read_b128 v[60:63], v17 offset:40192\n"
        "v_permlane32_swap_b32 v165, v159\n"
        "v_mul_f32 v165, v165, v166\n"
        "v_mul_f32 v159, v159, v167\n"
        "v_fma_f32 v64, v64, v166, -v165\n"
        "v_fma_f32 v65, v65, v166, -v165\n"
        "v_fma_f32 v108, v108, v167, -v159\n"
        "v_mfma_f32_16x16x32_bf16 v[76:79], v[132:135], v[240:243], v[76:79]\n"
        "s_waitcnt lgkmcnt(2)\n"
        "ds_read_b128 v[96:99], v0 offset:5136\n"
        "v_fma_f32 v109, v109, v167, -v159\n"
        "v_exp_f32 v64, v64\n"
        "v_exp_f32 v65, v65\n"
        "v_fma_f32 v66, v66, v166, -v165\n"
        "v_fma_f32 v110, v110, v167, -v159\n"
        "v_mfma_f32_16x16x32_bf16 v[80:83], v[32:35], v[240:243], 0\n"
        "s_waitcnt lgkmcnt(2)\n"
        "ds_read_b128 v[104:107], v0 offset:4112\n"
        "v_perm_b32 v42, v77, v76, s63\n"
        "v_perm_b32 v43, v79, v78, s63\n"
        "v_exp_f32 v108, v108\n"
        "v_exp_f32 v109, v109\n"
        "v_fma_f32 v67, v67, v166, -v165\n"
        "v_mfma_f32_16x16x32_bf16 v[80:83], v[36:39], v[244:247], v[80:83]\n"
        "s_waitcnt lgkmcnt(2)\n"
        "v_exp_f32 v66, v66\n"
        "v_exp_f32 v110, v110\n"
        "v_fma_f32 v111, v111, v167, -v159\n"
        "v_add_f32 v95, v64, v65\n"
        "v_exp_f32 v67, v67\n"
        "v_mfma_f32_16x16x32_bf16 v[80:83], v[60:63], v[248:251], v[80:83]\n"
        "s_waitcnt lgkmcnt(1)\n"
        "v_fma_f32 v84, v84, v166, -v165\n"
        "v_add_f32 v164, v108, v109\n"
        "v_exp_f32 v111, v111\n"
        "v_fma_f32 v140, v140, v167, -v159\n"
        "v_add_f32 v95, v95, v66\n"
        "v_exp_f32 v84, v84\n"
        "v_mfma_f32_16x16x32_bf16 v[112:115], v[60:63], v[96:99], 0\n"
        "s_waitcnt lgkmcnt(0)\n"
        "v_perm_b32 v76, v81, v80, s63\n"
        "v_perm_b32 v80, v83, v82, s63\n"
        "v_fma_f32 v85, v85, v166, -v165\n"
        "v_add_f32 v164, v164, v110\n"
        "v_exp_f32 v140, v140\n"
        "v_fma_f32 v141, v141, v167, -v159\n"
        "v_add_f32 v95, v95, v67\n"
        "v_mfma_f32_16x16x32_bf16 v[112:115], v[36:39], v[104:107], v[112:115]\n"
        "v_exp_f32 v85, v85\n"
        "v_fma_f32 v86, v86, v166, -v165\n"
        "v_add_f32 v164, v164, v111\n"
        "v_exp_f32 v141, v141\n"
        "v_fma_f32 v142, v142, v167, -v159\n"
        "v_add_f32 v95, v95, v84\n"
        "v_mfma_f32_16x16x32_bf16 v[112:115], v[32:35], v[252:255], v[112:115]\n"
        "v_exp_f32 v86, v86\n"
        "v_fma_f32 v87, v87, v166, -v165\n"
        "v_add_f32 v164, v164, v140\n"
        "v_exp_f32 v142, v142\n"
        "v_fma_f32 v143, v143, v167, -v159\n"
        "v_add_f32 v95, v95, v85\n"
        "v_mfma_f32_16x16x32_bf16 v[120:123], v[132:135], v[252:255], 0\n"
        "v_exp_f32 v87, v87\n"
        "v_perm_b32 v88, v113, v112, s63\n"
        "v_perm_b32 v92, v115, v114, s63\n"
        "v_fma_f32 v68, v68, v166, -v165\n"
        "v_add_f32 v164, v164, v141\n"
        "v_exp_f32 v143, v143\n"
        "v_mfma_f32_16x16x32_bf16 v[120:123], v[136:139], v[104:107], v[120:123]\n"
        "v_fma_f32 v100, v100, v167, -v159\n"
        "v_add_f32 v95, v95, v86\n"
        "v_exp_f32 v68, v68\n"
        "v_fma_f32 v69, v69, v166, -v165\n"
        "v_add_f32 v164, v164, v142\n"
        "v_exp_f32 v100, v100\n"
        "v_mfma_f32_16x16x32_bf16 v[120:123], v[52:55], v[96:99], v[120:123]\n"
        "ds_read_b128 v[144:147], v0 offset:6176\n"
        "v_fma_f32 v101, v101, v167, -v159\n"
        "v_add_f32 v95, v95, v87\n"
        "v_exp_f32 v69, v69\n"
        "v_fma_f32 v70, v70, v166, -v165\n"
        "v_add_f32 v164, v164, v143\n"
        "v_fma_f32 v102, v102, v167, -v159\n"
        "v_mfma_f32_16x16x32_bf16 v[148:151], v[160:163], v[96:99], 0\n"
        "ds_read_b128 v[116:119], v0 offset:7200\n"
        "v_perm_b32 v98, v121, v120, s63\n"
        "v_perm_b32 v99, v123, v122, s63\n"
        "v_exp_f32 v101, v101\n"
        "v_add_f32 v95, v95, v68\n"
        "v_exp_f32 v70, v70\n"
        "v_mfma_f32_16x16x32_bf16 v[148:151], v[128:131], v[104:107], v[148:151]\n"
        "ds_read_b128 v[112:115], v0 offset:8224\n"
        "v_fma_f32 v71, v71, v166, -v165\n"
        "v_add_f32 v164, v164, v100\n"
        "v_exp_f32 v102, v102\n"
        "v_fma_f32 v103, v103, v167, -v159\n"
        "v_add_f32 v95, v95, v69\n"
        "v_fma_f32 v72, v72, v166, -v165\n"
        "v_mfma_f32_16x16x32_bf16 v[148:151], v[124:127], v[252:255], v[148:151]\n"
        "s_waitcnt lgkmcnt(2)\n"
        "v_exp_f32 v71, v71\n"
        "v_add_f32 v164, v164, v101\n"
        "v_exp_f32 v103, v103\n"
        "v_fma_f32 v44, v44, v167, -v159\n"
        "v_add_f32 v95, v95, v70\n"
        "v_add_f32 v164, v164, v102\n"
        "v_mfma_f32_16x16x32_bf16 v[120:123], v[124:127], v[144:147], 0\n"
        "s_waitcnt lgkmcnt(1)\n"
        "v_perm_b32 v96, v149, v148, s63\n"
        "v_perm_b32 v97, v151, v150, s63\n"
        "v_exp_f32 v72, v72\n"
        "v_exp_f32 v44, v44\n"
        "v_add_f32 v95, v95, v71\n"
        "v_add_f32 v164, v164, v103\n"
        "v_mfma_f32_16x16x32_bf16 v[120:123], v[128:131], v[116:119], v[120:123]\n"
        "s_waitcnt lgkmcnt(0)\n"
        "v_add_f32 v95, v95, v72\n"
        "v_add_f32 v164, v164, v44\n"
        "v_mul_f32 v64, v64, v20\n"
        "v_mul_f32 v65, v65, v21\n"
        "v_permlane32_swap_b32 v95, v164\n"
        "v_add_f32 v95, v95, v164\n"
        "v_mov_b32 v167, v95\n"
        "v_mfma_f32_16x16x32_bf16 v[120:123], v[160:163], v[112:115], v[120:123]\n"
        "v_mul_f32 v66, v66, v22\n"
        "v_permlane16_swap_b32 v95, v167\n"
        "v_add_f32 v95, v95, v167\n"
        "v_rcp_f32 v95, v95\n"
        "v_mul_f32 v67, v67, v23\n"
        "v_mov_b32 v164, v95\n"
        "v_mfma_f32_16x16x32_bf16 v[156:159], v[52:55], v[112:115], 0\n"
        "v_mul_f32 v84, v84, v24\n"
        "v_perm_b32 v104, v121, v120, s63\n"
        "v_perm_b32 v105, v123, v122, s63\n"
        "v_permlane32_swap_b32 v95, v164\n"
        "v_mul_f32 v85, v85, v25\n"
        "v_mul_f32 v86, v86, v26\n"
        "v_mul_f32 v87, v87, v27\n"
        "v_mfma_f32_16x16x32_bf16 v[156:159], v[136:139], v[116:119], v[156:159]\n"
        "v_mul_f32 v64, v64, v95\n"
        "v_mul_f32 v65, v65, v95\n"
        "v_mul_f32 v66, v66, v95\n"
        "v_mul_f32 v67, v67, v95\n"
        "v_mul_f32 v84, v84, v95\n"
        "v_mul_f32 v85, v85, v95\n"
        "v_mul_f32 v86, v86, v95\n"
        "v_mul_f32 v87, v87, v95\n"
        "v_mfma_f32_16x16x32_bf16 v[156:159], v[132:135], v[144:147], v[156:159]\n"
        "ds_read_b128 v[148:151], v0 offset:11312\n"
        "v_perm_b32 v120, v65, v64, s63\n"
        "v_perm_b32 v121, v67, v66, s63\n"
        "v_perm_b32 v122, v85, v84, s63\n"
        "v_perm_b32 v123, v87, v86, s63\n"
        "v_mul_f32 v68, v68, v28\n"
        "v_mul_f32 v69, v69, v29\n"
        "v_mul_f32 v70, v70, v30\n"
        "v_mfma_f32_16x16x32_bf16 v[152:155], v[32:35], v[144:147], 0\n"
        "ds_read_b128 v[84:87], v0 offset:10288\n"
        "v_perm_b32 v106, v157, v156, s63\n"
        "v_perm_b32 v107, v159, v158, s63\n"
        "v_mul_f32 v71, v71, v31\n"
        "v_mul_f32 v72, s58, v72\n"
        "v_mul_f32 v68, v68, v95\n"
        "v_mul_f32 v69, v69, v95\n"
        "v_mul_f32 v70, v70, v95\n"
        "v_mfma_f32_16x16x32_bf16 v[152:155], v[36:39], v[116:119], v[152:155]\n"
        "ds_read_b128 v[144:147], v0 offset:9264\n"
        "v_mul_f32 v71, v71, v95\n"
        "v_fma_f32 v72, v72, v95, v19\n"
        "v_perm_b32 v64, v69, v68, s63\n"
        "v_perm_b32 v66, v71, v70, s63\n"
        "v_perm_b32 v65, 0, v72, s63\n"
        "v_mul_f32 v108, v108, v20\n"
        "v_mul_f32 v109, v109, v21\n"
        "v_mfma_f32_16x16x32_bf16 v[152:155], v[60:63], v[112:115], v[152:155]\n"
        "s_waitcnt lgkmcnt(2)\n"
        "v_mul_f32 v110, v110, v22\n"
        "v_mul_f32 v111, v111, v23\n"
        "v_mul_f32 v140, v140, v24\n"
        "v_mul_f32 v141, v141, v25\n"
        "v_mul_f32 v142, v142, v26\n"
        "v_mul_f32 v143, v143, v27\n"
        "v_mul_f32 v108, v108, v164\n"
        "v_perm_b32 v112, v153, v152, s63\n"
        "v_mfma_f32_16x16x32_bf16 v[20:23], v[60:63], v[148:151], 0\n"
        "s_waitcnt lgkmcnt(1)\n"
        "v_perm_b32 v116, v155, v154, s63\n"
        "v_mul_f32 v109, v109, v164\n"
        "v_mul_f32 v110, v110, v164\n"
        "v_mul_f32 v111, v111, v164\n"
        "v_mul_f32 v140, v140, v164\n"
        "v_mul_f32 v141, v141, v164\n"
        "v_mul_f32 v142, v142, v164\n"
        "v_mul_f32 v143, v143, v164\n"
        "v_mfma_f32_16x16x32_bf16 v[20:23], v[36:39], v[84:87], v[20:23]\n"
        "s_waitcnt lgkmcnt(0)\n"
        "v_perm_b32 v24, v109, v108, s63\n"
        "v_perm_b32 v25, v111, v110, s63\n"
        "v_perm_b32 v26, v141, v140, s63\n"
        "v_perm_b32 v27, v143, v142, s63\n"
        "v_mul_f32 v100, v100, v28\n"
        "v_mul_f32 v101, v101, v29\n"
        "v_mul_f32 v102, v102, v30\n"
        "v_mul_f32 v103, v103, v31\n"
        "v_mfma_f32_16x16x32_bf16 v[20:23], v[32:35], v[144:147], v[20:23]\n"
        "v_mul_f32 v44, s58, v44\n"
        "v_mul_f32 v100, v100, v164\n"
        "v_mul_f32 v101, v101, v164\n"
        "v_mul_f32 v102, v102, v164\n"
        "v_mul_f32 v103, v103, v164\n"
        "v_fma_f32 v44, v44, v164, v19\n"
        "v_perm_b32 v28, v101, v100, s63\n"
        "v_perm_b32 v30, v103, v102, s63\n"
        "v_mfma_f32_16x16x32_bf16 v[68:71], v[132:135], v[144:147], 0\n"
        "v_perm_b32 v77, v21, v20, s63\n"
        "v_perm_b32 v81, v23, v22, s63\n"
        "v_perm_b32 v29, 0, v44, s63\n"
        "v_mfma_f32_16x16x32_bf16 v[68:71], v[136:139], v[84:87], v[68:71]\n"
        "v_mfma_f32_16x16x32_bf16 v[68:71], v[52:55], v[148:151], v[68:71]\n"
        "ds_read_b128 v[20:23], v0 offset:12352\n"
        "v_mfma_f32_16x16x32_bf16 v[72:75], v[160:163], v[148:151], 0\n"
        "ds_read_b128 v[108:111], v0 offset:13376\n"
        "v_mfma_f32_16x16x32_bf16 v[72:75], v[128:131], v[84:87], v[72:75]\n"
        "ds_read_b128 v[140:143], v0 offset:14400\n"
        "v_mfma_f32_16x16x32_bf16 v[72:75], v[124:127], v[144:147], v[72:75]\n"
        "s_waitcnt lgkmcnt(2)\n"
        "v_mfma_f32_16x16x32_bf16 v[144:147], v[124:127], v[20:23], 0\n"
        "v_perm_b32 v46, v69, v68, s63\n"
        "v_perm_b32 v47, v71, v70, s63\n"
        "s_waitcnt lgkmcnt(1)\n"
        "v_mfma_f32_16x16x32_bf16 v[144:147], v[128:131], v[108:111], v[144:147]\n"
        "s_waitcnt lgkmcnt(0)\n"
        "v_mfma_f32_16x16x32_bf16 v[144:147], v[160:163], v[140:143], v[144:147]\n"
        "v_perm_b32 v44, v73, v72, s63\n"
        "v_perm_b32 v45, v75, v74, s63\n"
        "v_mfma_f32_16x16x32_bf16 v[148:151], v[52:55], v[140:143], 0\n"
        "v_mfma_f32_16x16x32_bf16 v[148:151], v[136:139], v[108:111], v[148:151]\n"
        "v_mfma_f32_16x16x32_bf16 v[148:151], v[132:135], v[20:23], v[148:151]\n"
        "ds_read_b128 v[72:75], v0 offset:17488\n"
        "v_mfma_f32_16x16x32_bf16 v[84:87], v[32:35], v[20:23], 0\n"
        "ds_read_b128 v[68:71], v0 offset:16464\n"
        "v_perm_b32 v100, v145, v144, s63\n"
        "v_perm_b32 v101, v147, v146, s63\n"
        "v_mfma_f32_16x16x32_bf16 v[84:87], v[36:39], v[108:111], v[84:87]\n"
        "ds_read_b128 v[20:23], v0 offset:15440\n"
        "v_mfma_f32_16x16x32_bf16 v[84:87], v[60:63], v[140:143], v[84:87]\n"
        "v_perm_b32 v102, v149, v148, s63\n"
        "v_perm_b32 v103, v151, v150, s63\n"
        "s_waitcnt lgkmcnt(2)\n"
        "v_mfma_f32_16x16x32_bf16 v[108:111], v[60:63], v[72:75], 0\n"
        "s_waitcnt lgkmcnt(1)\n"
        "v_mfma_f32_16x16x32_bf16 v[108:111], v[36:39], v[68:71], v[108:111]\n"
        "s_waitcnt lgkmcnt(0)\n"
        "v_mfma_f32_16x16x32_bf16 v[108:111], v[32:35], v[20:23], v[108:111]\n"
        "ds_read2_b32 v[82:83], v11 offset0:0 offset1:3\n"
        "v_perm_b32 v89, v85, v84, s63\n"
        "v_perm_b32 v93, v87, v86, s63\n"
        "v_mov_b32_dpp v59, v19 row_shr:4 row_mask:0xf bank_mask:0xf bound_ctrl:0\n"
        "v_mfma_f32_16x16x32_bf16 v[36:39], v[132:135], v[20:23], 0\n"
        "ds_read_b64_tr_b16 v[78:79], v9 offset:40064\n"
        "v_mfma_f32_16x16x32_bf16 v[36:39], v[136:139], v[68:71], v[36:39]\n"
        "ds_read2_b32 v[94:95], v11 offset0:1 offset1:4\n"
        "v_perm_b32 v113, v109, v108, s63\n"
        "v_perm_b32 v117, v111, v110, s63\n"
        "v_mfma_f32_16x16x32_bf16 v[36:39], v[52:55], v[72:75], v[36:39]\n"
        "s_add_u32 s31, s31, 1\n"
        "ds_read_b64_tr_b16 v[90:91], v9 offset:40080\n"
        "v_mfma_f32_16x16x32_bf16 v[32:35], v[160:163], v[72:75], 0\n"
        "s_cmp_ge_u32 s31, s71\n"
        "ds_read2_b32 v[118:119], v11 offset0:2 offset1:5\n"
        "v_mfma_f32_16x16x32_bf16 v[32:35], v[128:131], v[68:71], v[32:35]\n"
        "s_cbranch_scc1 L52B_%=\n"
        "L53B_%=:\n"
        "ds_read_b64_tr_b16 v[114:115], v9 offset:40096\n"
        "v_perm_b32 v110, v37, v36, s63\n"
        "v_perm_b32 v111, v39, v38, s63\n"
        "v_mfma_f32_16x16x32_bf16 v[32:35], v[124:127], v[20:23], v[32:35]\n"
        "s_add_u32 s16, s16, 256\n"
        "v_mfma_f32_16x16x32_bf16 v[20:23], v[48:51], v[40:43], 0\n"
        "s_waitcnt lgkmcnt(4)\n"
        "s_cmp_ge_u32 s31, s43\n"
        "v_smfmac_f32_16x16x64_bf16 v[20:23], v[56:59], v[76:83], v18\n"
        "s_cselect_b32 s39, s22, s16\n"
        "v_mfma_f32_16x16x32_bf16 v[36:39], v[48:51], v[96:99], 0\n"
        "s_waitcnt lgkmcnt(2)\n"
        "v_perm_b32 v108, v33, v32, s63\n"
        "v_perm_b32 v109, v35, v34, s63\n"
        "s_cmp_gt_u32 s31, s43\n"
        "v_smfmac_f32_16x16x64_bf16 v[36:39], v[56:59], v[88:95], v18\n"
        "s_cselect_b32 s39, s23, s39\n"
        "ds_read_b64_tr_b16 v[78:79], v9 offset:40112\n"
        "v_mfma_f32_16x16x32_bf16 v[32:35], v[48:51], v[104:107], 0\n"
        "s_waitcnt lgkmcnt(1)\n"
        "s_mul_hi_u32 s38, s39, 58835169\n"
        "v_smfmac_f32_16x16x64_bf16 v[32:35], v[56:59], v[112:119], v18\n"
        "s_mul_i32 s39, s39, 1344\n"
        "ds_read_b64_tr_b16 v[90:91], v9 offset:40128\n"
        "v_cvt_pk_bf16_f32 v152, v20, v36\n"
        "v_cvt_pk_bf16_f32 v156, v21, v37\n"
        "v_cvt_pk_bf16_f32 v160, v22, v38\n"
        "v_cvt_pk_bf16_f32 v164, v23, v39\n"
        "v_mfma_f32_16x16x32_bf16 v[68:71], v[48:51], v[44:47], 0\n"
        "s_mul_i32 s38, s38, 588672\n"
        "s_waitcnt lgkmcnt(1)\n"
        "v_smfmac_f32_16x16x64_bf16 v[68:71], v[56:59], v[76:83], v18 abid:1\n"
        "s_add_u32 s39, s39, s38\n"
        "ds_read_b64_tr_b16 v[114:115], v9 offset:40144\n"
        "v_mfma_f32_16x16x32_bf16 v[60:63], v[48:51], v[100:103], 0\n"
        "s_add_u32 s39, s39, s17\n"
        "ds_read_b128 v[36:39], v2\n"
        "v_mov_b64 v[20:21], 0\n"
        "v_mov_b64 v[22:23], 0\n"
        "s_waitcnt lgkmcnt(2)\n"
        "v_smfmac_f32_16x16x64_bf16 v[60:63], v[56:59], v[88:95], v18 abid:1\n"
        "s_mul_i32 s68, s31, 3\n"
        "ds_read_b128 v[52:55], v3\n"
        "v_cvt_pk_bf16_f32 v153, v32, v68\n"
        "v_cvt_pk_bf16_f32 v157, v33, v69\n"
        "v_cvt_pk_bf16_f32 v161, v34, v70\n"
        "v_cvt_pk_bf16_f32 v165, v35, v71\n"
        "v_mfma_f32_16x16x32_bf16 v[68:71], v[48:51], v[108:111], 0\n"
        "s_bitcmp1_b32 s31, 0\n"
        "v_mov_b64 v[32:33], 0\n"
        "v_mov_b64 v[34:35], 0\n"
        "s_waitcnt lgkmcnt(2)\n"
        "v_smfmac_f32_16x16x64_bf16 v[68:71], v[56:59], v[112:119], v18 abid:1\n"
        "s_cselect_b32 s38, 3, 6\n"
        "s_waitcnt lgkmcnt(1)\n"
        "v_mov_b32_dpp v67, v19 row_shr:4 row_mask:0xf bank_mask:0xf bound_ctrl:0\n"
        "v_smfmac_f32_16x16x64_bf16 v[20:23], v[36:39], v[40:47], v4\n"
        "s_add_u32 s68, s68, s38\n"
        "v_mov_b64 v[48:49], 0\n"
        "v_mov_b64 v[50:51], 0\n"
        "s_waitcnt lgkmcnt(0)\n"
        "v_smfmac_f32_16x16x64_bf16 v[20:23], v[52:55], v[76:83], v4 abid:1\n"
        "s_add_u32 s37, s35, 0\n"
        "s_cmp_lt_u32 s31, s14\n"
        "v_cvt_pk_bf16_f32 v154, v60, v68\n"
        "v_cvt_pk_bf16_f32 v158, v61, v69\n"
        "v_cvt_pk_bf16_f32 v162, v62, v70\n"
        "v_cvt_pk_bf16_f32 v166, v63, v71\n"
        "v_smfmac_f32_16x16x64_bf16 v[32:35], v[36:39], v[96:103], v4\n"
        "s_cselect_b32 s68, s68, 0\n"
        "v_add_f32 v20, v20, v21\n"
        "v_add_f32 v22, v22, v23\n"
        "v_smfmac_f32_16x16x64_bf16 v[32:35], v[52:55], v[88:95], v4 abid:1\n"
        "s_and_b32 s38, s31, 7\n"
        "ds_read_b64_tr_b16 v[78:79], v9 offset:43392\n"
        "v_smfmac_f32_16x16x64_bf16 v[48:51], v[36:39], v[104:111], v4\n"
        "buffer_store_dwordx3 v[152:154], v12, s[8:11], s37 offen sc0 sc1 nt\n"
        "s_cselect_b32 s30, 1, -7\n"
        "v_smfmac_f32_16x16x64_bf16 v[48:51], v[52:55], v[112:119], v4 abid:1\n"
        "s_mul_i32 s29, s30, 192\n"
        "ds_read_b64_tr_b16 v[90:91], v9 offset:43408\n"
        "v_add_f32 v32, v32, v33\n"
        "v_add_f32 v34, v34, v35\n"
        "v_cvt_pk_bf16_f32 v148, v20, v32\n"
        "v_mfma_f32_16x16x32_bf16 v[152:155], v[120:123], v[40:43], 0\n"
        "s_mul_i32 s30, s30, 392\n"
        "s_waitcnt lgkmcnt(1)\n"
        "v_smfmac_f32_16x16x64_bf16 v[152:155], v[64:67], v[76:83], v18\n"
        "s_add_u32 s37, s35, 0\n"
        "s_cmp_eq_u32 s69, s18\n"
        "ds_read_b64_tr_b16 v[114:115], v9 offset:43424\n"
        "v_add_f32 v48, v48, v49\n"
        "v_add_f32 v50, v50, v51\n"
        "v_cvt_pk_bf16_f32 v149, v48, v22\n"
        "v_cvt_pk_bf16_f32 v150, v34, v50\n"
        "v_mfma_f32_16x16x32_bf16 v[56:59], v[120:123], v[96:99], 0\n"
        "s_cselect_b32 s34, -7488, s70\n"
        "s_waitcnt lgkmcnt(1)\n"
        "v_smfmac_f32_16x16x64_bf16 v[56:59], v[64:67], v[88:95], v18\n"
        "s_add_u32 s69, s69, s34\n"
        "ds_read_b64_tr_b16 v[78:79], v9 offset:43440\n"
        "v_writelane_b32 v10, s69, 56\n"
        "v_add_u32 v11, s30, v11\n"
        "s_mov_b64 exec, s[54:55]\n"
        "v_add_u32 v16, s29, v16\n"
        "v_add_u32 v8, s34, v8\n"
        "s_mov_b64 exec, -1\n"
        "v_mfma_f32_16x16x32_bf16 v[72:75], v[120:123], v[104:107], 0\n"
        "buffer_store_dwordx3 v[156:158], v15, s[8:11], s37 offen offset:768 sc0 sc1 nt\n"
        "s_mov_b64 exec, s[50:51]\n"
        "v_add_u32 v5, s29, v5\n"
        "v_add_u32 v6, s34, v6\n"
        "s_mov_b64 exec, -1\n"
        "s_waitcnt lgkmcnt(1)\n"
        "v_smfmac_f32_16x16x64_bf16 v[72:75], v[64:67], v[112:119], v18\n"
        "ds_read_b64_tr_b16 v[90:91], v9 offset:43456\n"
        "v_mov_b32_dpp v31, v19 row_shr:4 row_mask:0xf bank_mask:0xf bound_ctrl:0\n"
        "v_mfma_f32_16x16x32_bf16 v[20:23], v[120:123], v[44:47], 0\n"
        "s_waitcnt lgkmcnt(1)\n"
        "v_smfmac_f32_16x16x64_bf16 v[20:23], v[64:67], v[76:83], v18 abid:1\n"
        "s_add_u32 s37, s35, 98112\n"
        "ds_read_b64_tr_b16 v[114:115], v9 offset:43472\n"
        "v_mfma_f32_16x16x32_bf16 v[84:87], v[120:123], v[100:103], 0\n"
        "s_waitcnt lgkmcnt(1)\n"
        "v_smfmac_f32_16x16x64_bf16 v[84:87], v[64:67], v[88:95], v18 abid:1\n"
        "ds_read_b64_tr_b16 v[78:79], v9 offset:46720\n"
        "v_mfma_f32_16x16x32_bf16 v[60:63], v[120:123], v[108:111], 0\n"
        "buffer_store_dwordx3 v[160:162], v12, s[8:11], s37 offen offset:192 sc0 sc1 nt\n"
        "ds_read_b32 v167, v0 offset:21600\n"
        "s_waitcnt lgkmcnt(2)\n"
        "v_smfmac_f32_16x16x64_bf16 v[60:63], v[64:67], v[112:119], v18 abid:1\n"
        "ds_read_b64_tr_b16 v[90:91], v9 offset:46736\n"
        "v_mfma_f32_16x16x32_bf16 v[68:71], v[24:27], v[40:43], 0\n"
        "s_waitcnt lgkmcnt(2)\n"
        "v_smfmac_f32_16x16x64_bf16 v[68:71], v[28:31], v[76:83], v18\n"
        "s_add_u32 s37, s35, 98112\n"
        "ds_read_b64_tr_b16 v[114:115], v9 offset:46752\n"
        "v_mfma_f32_16x16x32_bf16 v[36:39], v[24:27], v[96:99], 0\n"
        "s_waitcnt lgkmcnt(1)\n"
        "v_smfmac_f32_16x16x64_bf16 v[36:39], v[28:31], v[88:95], v18\n"
        "ds_read_b64_tr_b16 v[78:79], v9 offset:46768\n"
        "v_readfirstlane_b32 s40, v167\n"
        "v_mfma_f32_16x16x32_bf16 v[40:43], v[24:27], v[104:107], 0\n"
        "buffer_store_dwordx3 v[164:166], v14, s[8:11], s37 offen offset:960 sc0 sc1 nt\n"
        "s_add_u32 s37, s35, 588672\n"
        "s_add_u32 s46, s37, 1344\n"
        "s_cmp_lt_i32 s40, s68\n"
        "s_waitcnt lgkmcnt(1)\n"
        "v_smfmac_f32_16x16x64_bf16 v[40:43], v[28:31], v[112:119], v18\n"
        "buffer_store_dwordx3 v[148:150], v12, s[44:47], s37 offen offset:1152 sc0 sc1 nt\n"
        "ds_read_b64_tr_b16 v[90:91], v9 offset:46784\n"
        "v_mfma_f32_16x16x32_bf16 v[64:67], v[24:27], v[44:47], 0\n"
        "s_waitcnt lgkmcnt(1)\n"
        "v_smfmac_f32_16x16x64_bf16 v[64:67], v[28:31], v[76:83], v18 abid:1\n"
        "ds_read_b64_tr_b16 v[114:115], v9 offset:46800\n"
        "s_mov_b64 exec, 1\n"
        "ds_inc_u32 v0, v1 offset:21612\n"
        "s_mov_b64 exec, -1\n"
        "s_cbranch_scc1 L56B_%=\n"
        "L57B_%=:\n"
        "ds_read_b128 v[124:127], v7 offset:0\n"
        "ds_read_b128 v[132:135], v7 offset:3328\n"
        "ds_read_b128 v[128:131], v7 offset:64\n"
        "ds_read_b128 v[136:139], v7 offset:3392\n"
        "v_mfma_f32_16x16x32_bf16 v[104:107], v[24:27], v[100:103], 0\n"
        "s_waitcnt lgkmcnt(6)\n"
        "ds_read_b128 v[160:163], v7 offset:128\n"
        "ds_read_b128 v[52:55], v7 offset:3456\n"
        "s_mov_b32 s36, s35\n"
        "v_smfmac_f32_16x16x64_bf16 v[104:107], v[28:31], v[88:95], v18 abid:1\n"
        "s_mov_b32 s35, s39\n"
        "v_mfma_f32_16x16x32_bf16 v[88:91], v[24:27], v[108:111], 0\n"
        "s_waitcnt lgkmcnt(7)\n"
        "v_smfmac_f32_16x16x64_bf16 v[88:91], v[28:31], v[112:119], v18 abid:1\n"
        "s_setprio 0\n"
        "s_waitcnt lgkmcnt(0)\n"
        "s_nop 15\n"
        "s_branch CNEXTB_%=\n"
        "MPRO_%=:\n"
        "s_lshl_b32 s7, s3, 11\n"
        "s_add_u32 s7, s7, 68608\n"
        "s_add_u32 s22, s4, s7\n"
        "s_addc_u32 s23, s5, 0\n"
        "global_load_dwordx4 v[240:243], v57, s[22:23] offset:0\n"
        "global_load_dwordx4 v[244:247], v57, s[22:23] offset:1024\n"
        "v_mov_b32 v250, 21600\n"
        "v_lshl_add_u32 v27, v56, 2, v250\n"
        "v_mov_b32 v28, 0\n"
        "s_mov_b32 exec_hi, 0\n"
        "ds_write_b32 v27, v28\n"
        "s_mov_b32 exec_hi, -1\n"
        "s_mov_b32 s41, 0x400000\n"
        "s_mov_b32 s27, 0x20000\n"
        "s_lshr_b32 s61, s3, 1\n"
        "s_mul_i32 s61, s61, 192\n"
        "s_cmp_lt_u32 s3, 6\n"
        "s_cselect_b32 s61, s61, 0\n"
        "s_cselect_b32 s62, 3, 16\n"
        "s_cselect_b32 s63, 12, 32\n"
        "s_cselect_b32 s26, 3, 1\n"
        "s_cselect_b32 s70, 72, 1\n"
        "s_cselect_b32 s71, 36, 4\n"
        "s_cselect_b32 s72, 6, 8\n"
        "s_mul_i32 s62, s62, 192\n"
        "s_mul_i32 s63, s63, 192\n"
        "s_mul_i32 s70, s70, 192\n"
        "s_mul_i32 s71, s71, 192\n"
        "s_mul_i32 s72, s72, 192\n"
        "s_mul_i32 s26, s26, 18432\n"
        "s_add_u32 s7, s62, s63\n"
        "s_lshl_b32 s29, s63, 1\n"
        "s_add_u32 s60, s7, s63\n"
        "s_bitcmp1_b32 s6, 0\n"
        "s_cselect_b32 s28, s7, 0\n"
        "s_cselect_b32 s29, s29, s62\n"
        "s_cselect_b32 s60, s60, s63\n"
        "s_add_u32 s28, s28, s61\n"
        "s_add_u32 s29, s29, s61\n"
        "s_add_u32 s60, s60, s61\n"
        "v_add_u32 v24, 0, v56\n"
        "v_mul_u32_u24 v21, 171, v24\n"
        "v_lshrrev_b32 v21, 11, v21\n"
        "v_mad_i32_i24 v24, v21, -12, v24\n"
        "v_bfe_u32 v58, v21, 2, 2\n"
        "v_xor_b32 v58, v24, v58\n"
        "v_and_b32 v27, 3, v21\n"
        "v_mul_u32_u24 v27, s70, v27\n"
        "v_lshl_add_u32 v32, v58, 4, v27\n"
        "v_bfe_u32 v27, v21, 2, 1\n"
        "v_mad_u32_u24 v32, v27, s71, v32\n"
        "v_lshrrev_b32 v27, 3, v21\n"
        "v_mad_u32_u24 v32, v27, s72, v32\n"
        "v_and_b32 v27, 7, v21\n"
        "v_mul_u32_u24 v27, s70, v27\n"
        "v_lshl_add_u32 v58, v58, 4, v27\n"
        "v_add_u32 v25, 64, v56\n"
        "v_mul_u32_u24 v22, 171, v25\n"
        "v_lshrrev_b32 v22, 11, v22\n"
        "v_mad_i32_i24 v25, v22, -12, v25\n"
        "v_bfe_u32 v59, v22, 2, 2\n"
        "v_xor_b32 v59, v25, v59\n"
        "v_and_b32 v27, 3, v22\n"
        "v_mul_u32_u24 v27, s70, v27\n"
        "v_lshl_add_u32 v33, v59, 4, v27\n"
        "v_bfe_u32 v27, v22, 2, 1\n"
        "v_mad_u32_u24 v33, v27, s71, v33\n"
        "v_lshrrev_b32 v27, 3, v22\n"
        "v_mad_u32_u24 v33, v27, s72, v33\n"
        "v_and_b32 v27, 7, v22\n"
        "v_mul_u32_u24 v27, s70, v27\n"
        "v_lshl_add_u32 v59, v59, 4, v27\n"
        "v_lshrrev_b32 v27, 3, v22\n"
        "v_mad_u32_u24 v59, v27, s70, v59\n"
        "v_add_u32 v26, 128, v56\n"
        "v_mul_u32_u24 v23, 171, v26\n"
        "v_lshrrev_b32 v23, 11, v23\n"
        "v_mad_i32_i24 v26, v23, -12, v26\n"
        "v_bfe_u32 v60, v23, 2, 2\n"
        "v_xor_b32 v60, v26, v60\n"
        "v_and_b32 v27, 3, v23\n"
        "v_mul_u32_u24 v27, s70, v27\n"
        "v_lshl_add_u32 v34, v60, 4, v27\n"
        "v_bfe_u32 v27, v23, 2, 1\n"
        "v_mad_u32_u24 v34, v27, s71, v34\n"
        "v_lshrrev_b32 v27, 3, v23\n"
        "v_mad_u32_u24 v34, v27, s72, v34\n"
        "v_and_b32 v27, 7, v23\n"
        "v_mul_u32_u24 v27, s70, v27\n"
        "v_lshl_add_u32 v60, v60, 4, v27\n"
        "v_add_u32 v60, s70, v60\n"
        "s_mul_i32 s64, s3, 9216\n"
        "s_add_u32 s64, s64, 21856\n"
        "s_mul_i32 s7, s6, 7168\n"
        "s_add_u32 s7, s7, 6144\n"
        "s_add_u32 s22, s4, s7\n"
        "s_addc_u32 s23, s5, 0\n"
        "s_waitcnt lgkmcnt(0)\n"
        "s_cmp_lt_u32 s3, 6\n"
        "s_cselect_b64 s[24:25], s[50:51], s[52:53]\n"
        "s_and_b32 s25, s25, 0xffff\n"
        "s_cmp_lt_u32 s3, 4\n"
        "s_cbranch_scc0 L78_%=\n"
        "s_add_u32 m0, s64, 0\n"
        "s_nop 0\n"
        "buffer_load_dwordx4 v32, s[24:27], s28 offen lds\n"
        "s_add_u32 m0, s64, 1024\n"
        "s_nop 0\n"
        "buffer_load_dwordx4 v33, s[24:27], s28 offen lds\n"
        "s_add_u32 m0, s64, 2048\n"
        "s_nop 0\n"
        "buffer_load_dwordx4 v34, s[24:27], s28 offen lds\n"
        "s_add_u32 m0, s64, 3072\n"
        "s_nop 0\n"
        "buffer_load_dwordx4 v32, s[24:27], s29 offen lds\n"
        "s_add_u32 m0, s64, 4096\n"
        "s_nop 0\n"
        "buffer_load_dwordx4 v33, s[24:27], s29 offen lds\n"
        "s_add_u32 m0, s64, 5120\n"
        "s_nop 0\n"
        "buffer_load_dwordx4 v34, s[24:27], s29 offen lds\n"
        "s_add_u32 m0, s64, 6144\n"
        "s_nop 0\n"
        "buffer_load_dwordx4 v32, s[24:27], s60 offen lds\n"
        "s_add_u32 m0, s64, 7168\n"
        "s_nop 0\n"
        "buffer_load_dwordx4 v33, s[24:27], s60 offen lds\n"
        "s_add_u32 m0, s64, 8192\n"
        "s_nop 0\n"
        "buffer_load_dwordx4 v34, s[24:27], s60 offen lds\n"
        "s_barrier\n"
        "s_setprio 1\n"
        "s_waitcnt vmcnt(0)\n"
        "s_mov_b64 exec, 1\n"
        "ds_inc_u32 v250, v250 offset:16\n"
        "s_mov_b64 exec, -1\n"
        "L82_%=:\n"
        "ds_read_b32 v251, v250 offset:16\n"
        "s_waitcnt lgkmcnt(0)\n"
        "v_readfirstlane_b32 s40, v251\n"
        "s_nop 3\n"
        "s_cmp_ge_i32 s40, 4\n"
        "s_cbranch_scc1 L83_%=\n"
        "s_nop 0\n"
        "s_sub_u32 s41, s41, 1\n"
        "s_cbranch_scc0 L82_%=\n"
        "s_endpgm\n"
        "L83_%=:\n"
        "s_cmp_eq_u32 s6, 3\n"
        "s_cbranch_scc1 L84_%=\n"
        "s_mov_b32 s34, 0x4162ccfd\n"
        "s_mov_b32 s35, 21624\n"
        "s_mov_b32 s36, 21624\n"
        "s_mov_b32 s37, 21656\n"
        "s_call_b64 s[30:31], FOLDM_%=\n"
        "s_branch L85_%=\n"
        "L84_%=:\n"
        "s_mov_b32 s34, 0xbe173353\n"
        "s_mov_b32 s35, 21656\n"
        "s_mov_b32 s36, 21656\n"
        "s_mov_b32 s37, 21624\n"
        "s_call_b64 s[30:31], FOLDR_%=\n"
        "s_branch L80_%=\n"
        "L85_%=:\n"
        "s_mov_b32 s7, 21728\n"
        "s_mov_b32 s28, s6\n"
        "s_branch L79_%=\n"
        "L78_%=:\n"
        "s_barrier\n"
        "s_add_u32 m0, s64, 0\n"
        "s_nop 0\n"
        "buffer_load_dwordx4 v32, s[24:27], s28 offen lds\n"
        "s_add_u32 m0, s64, 1024\n"
        "s_nop 0\n"
        "buffer_load_dwordx4 v33, s[24:27], s28 offen lds\n"
        "s_add_u32 m0, s64, 2048\n"
        "s_nop 0\n"
        "buffer_load_dwordx4 v34, s[24:27], s28 offen lds\n"
        "s_add_u32 m0, s64, 3072\n"
        "s_nop 0\n"
        "buffer_load_dwordx4 v32, s[24:27], s29 offen lds\n"
        "s_add_u32 m0, s64, 4096\n"
        "s_nop 0\n"
        "buffer_load_dwordx4 v33, s[24:27], s29 offen lds\n"
        "s_add_u32 m0, s64, 5120\n"
        "s_nop 0\n"
        "buffer_load_dwordx4 v34, s[24:27], s29 offen lds\n"
        "s_add_u32 m0, s64, 6144\n"
        "s_nop 0\n"
        "buffer_load_dwordx4 v32, s[24:27], s60 offen lds\n"
        "s_add_u32 m0, s64, 7168\n"
        "s_nop 0\n"
        "buffer_load_dwordx4 v33, s[24:27], s60 offen lds\n"
        "s_add_u32 m0, s64, 8192\n"
        "s_nop 0\n"
        "buffer_load_dwordx4 v34, s[24:27], s60 offen lds\n"
        "s_mov_b64 s[24:25], s[56:57]\n"
        "s_waitcnt vmcnt(0)\n"
        "s_mov_b64 exec, 1\n"
        "ds_inc_u32 v250, v250 offset:20\n"
        "s_mov_b64 exec, -1\n"
        "L86_%=:\n"
        "ds_read_b32 v251, v250 offset:20\n"
        "s_waitcnt lgkmcnt(0)\n"
        "v_readfirstlane_b32 s40, v251\n"
        "s_nop 3\n"
        "s_cmp_ge_i32 s40, 4\n"
        "s_cbranch_scc1 L87_%=\n"
        "s_nop 0\n"
        "s_sub_u32 s41, s41, 1\n"
        "s_cbranch_scc0 L86_%=\n"
        "s_endpgm\n"
        "L87_%=:\n"
        "s_cmp_eq_u32 s6, 0\n"
        "s_cbranch_scc1 L88_%=\n"
        "s_mov_b32 s34, 0x411da57f\n"
        "s_mov_b32 s35, 21632\n"
        "s_mov_b32 s36, 21632\n"
        "s_mov_b32 s37, 21640\n"
        "s_call_b64 s[30:31], FOLDW_%=\n"
        "s_branch L89_%=\n"
        "L88_%=:\n"
        "s_mov_b64 exec, 1\n"
        "ds_inc_u32 v250, v250 offset:32\n"
        "s_mov_b64 exec, -1\n"
        "s_mov_b64 exec, 1\n"
        "ds_inc_u32 v250, v250 offset:40\n"
        "s_mov_b64 exec, -1\n"
        "s_mov_b64 exec, 1\n"
        "v_mov_b32 v27, 0\n"
        "v_mov_b32 v28, 0\n"
        "global_atomic_swap v252, v27, v28, s[24:25] offset:0 sc0\n"
        "s_mov_b64 exec, -1\n"
        "s_branch L80_%=\n"
        "L89_%=:\n"
        "L90_%=:\n"
        "ds_read_b32 v251, v250 offset:40\n"
        "s_waitcnt lgkmcnt(0)\n"
        "v_readfirstlane_b32 s40, v251\n"
        "s_nop 3\n"
        "s_cmp_ge_i32 s40, 4\n"
        "s_cbranch_scc1 L91_%=\n"
        "s_nop 0\n"
        "s_sub_u32 s41, s41, 1\n"
        "s_cbranch_scc0 L90_%=\n"
        "s_endpgm\n"
        "L91_%=:\n"
        "s_mov_b32 s7, -18528\n"
        "s_add_u32 s28, s6, 2\n"
        "L79_%=:\n"
        "s_mul_i32 s29, s28, 6176\n"
        "s_add_u32 s7, s7, s29\n"
        "v_add_u32 v27, s7, v57\n"
        "s_mul_i32 s28, s28, 6144\n"
        "s_add_u32 s28, s28, 10240\n"
        "s_add_u32 s70, s56, s28\n"
        "s_addc_u32 s71, s57, 0\n"
        "ds_read_b128 v[64:67], v27 offset:0\n"
        "ds_read_b128 v[68:71], v27 offset:1024\n"
        "ds_read_b128 v[72:75], v27 offset:2048\n"
        "ds_read_b128 v[76:79], v27 offset:3088\n"
        "ds_read_b128 v[80:83], v27 offset:4112\n"
        "ds_read_b128 v[84:87], v27 offset:5136\n"
        "s_waitcnt lgkmcnt(5)\n"
        "global_store_dwordx4 v57, v[64:67], s[70:71] offset:-2048 sc1\n"
        "s_waitcnt lgkmcnt(4)\n"
        "global_store_dwordx4 v57, v[68:71], s[70:71] offset:-1024 sc1\n"
        "s_waitcnt lgkmcnt(3)\n"
        "global_store_dwordx4 v57, v[72:75], s[70:71] offset:0 sc1\n"
        "s_waitcnt lgkmcnt(2)\n"
        "global_store_dwordx4 v57, v[76:79], s[70:71] offset:1024 sc1\n"
        "s_waitcnt lgkmcnt(1)\n"
        "global_store_dwordx4 v57, v[80:83], s[70:71] offset:2048 sc1\n"
        "s_waitcnt lgkmcnt(0)\n"
        "global_store_dwordx4 v57, v[84:87], s[70:71] offset:3072 sc1\n"
        "L80_%=:\n"
        "s_waitcnt vmcnt(0) lgkmcnt(0)\n"
        "s_barrier\n"
        "s_setprio 0\n"
        "s_cmp_lg_u32 s3, 7\n"
        "s_cbranch_scc1 PRO_%=\n"
        "s_mov_b32 exec_hi, 0\n"
        "v_mbcnt_lo_u32_b32 v27, -1, 0\n"
        "v_lshlrev_b32 v27, 7, v27\n"
        "v_mov_b32 v28, 1\n"
        "global_atomic_add v27, v28, s[24:25] offset:256\n"
        "s_mov_b32 exec_hi, -1\n"
        "s_branch PRO_%=\n"
        :: "{v0}"(tid), "{s[0:1]}"(kap), "{s2}"(bid), "{s[4:5]}"(tab) : CLOBBERS);
}

extern "C" void kernel_launch(void* const* d_in, const int* in_sizes, int n_in, void* d_out, int out_size, void* d_ws, size_t ws_size, hipStream_t stream) {
    (void)in_sizes; (void)n_in; (void)out_size; (void)ws_size;
    (void)hipFuncSetAttribute((const void*)swin_kernel, hipFuncAttributeMaxDynamicSharedMemorySize, LDS_BYTES);
    swin_kernel<<<256, 512, LDS_BYTES, stream>>>((const char*)d_in[0], (const char*)d_in[1], (const char*)d_in[2], (const char*)d_in[3], (const char*)d_in[4], (char*)d_out, (const char*)d_ws);
}
```

```cpp
#define LTOK_ 208
#define FPAD 32
#define NPR 36
#define NWC 22
#define ASP 0
#define SWP 1
#define LK0 1
#define LK1 0
#define LK2 1
#define LK3 1
#include <hip/hip_runtime.h>
#include <hip/hip_bf16.h>

constexpr int TOKB = 192, ROWB = 511 * TOKB;
constexpr unsigned FRAMEB = 511u * ROWB;
constexpr int LTOK = LTOK_;
constexpr int LFRAME = 48 * LTOK + FPAD;
constexpr int LBUF = 4 * LFRAME;
constexpr int LSTASH = 10 * 4 * LTOK;
constexpr int LSTG = 6208;
constexpr int VST = 392, LEXT = 1536 + 8 * VST;
constexpr int WT = 3088;
constexpr int LDS_AUX = (54 - NPR) / 3 * WT, LDS_TAB = LDS_AUX + 1024, LDS_CNT = LDS_TAB + 2048, LDS_BUF = LDS_CNT + 128, LDS_STASH = LDS_BUF + 2 * LBUF,
              LDS_STG = LDS_STASH + LSTASH, LDS_SCRAP = LDS_STG + 4 * LSTG, LDS_EXT = LDS_SCRAP + 512, LDS_BYTES = 163840;
static_assert(NWC >= 18 && NPR % 3 == 0 && LDS_EXT + 4 * LEXT + 256 <= LDS_BYTES && NPR * 1024 <= LBUF, "LDS");
constexpr unsigned RAWM = 18432, RAW_T = LDS_BUF, RAW_TV = RAW_T + 2 * RAWM + 128, RAW_SV = (36 - NPR) / 3 * WT;
static_assert(RAW_TV + 2 * RAWM <= LDS_STASH && RAWM + 6 * WT <= 2 * RAWM + 128, "fold LDS");

constexpr int phi(int v) { return v ^ (v >> 1); }
struct Tab {
    unsigned img[3][64][4];
    unsigned p[4][7][64][4];
    unsigned c[4][2][64][4];
    unsigned d[4][7][64][4];
    unsigned e[8][2][64][4];
    static constexpr void job(unsigned (*q)[64][4], int lane, unsigned L, unsigned R, int mode, int tau, unsigned st0, unsigned st1) {
        const int i = lane & 15, g = lane >> 4;
        for (int a = 0; a < 2; ++a) {
            const unsigned ra = (8 * (i >> 2) + 4 * a + (i & 3)) * 192 + ((g ^ ((2 * (i >> 2) + a) & 3)) * 16);
            q[0][lane][a] = L + ra;
            q[0][lane][2 + a] = mode == 0 ? R + tau * 6144 + ra : mode == 2 ? R + tau * 6144 + (16 * a + i) * 192 + ((g ^ (i >> 2)) * 16)
                                          : R + tau * 64 + (8 * g + 4 * a + (i >> 2)) * 192 + (((i & 3) ^ ((2 * g + a) & 3)) * 16);
        }
        q[1][lane][0] = st0; q[1][lane][1] = st1; q[1][lane][2] = LDS_SCRAP + g * 32;
    }
    static constexpr unsigned scat(bool win, int lane, int tau, int tb) {
        const int i = lane & 15, c = 32 * tau + 8 * (i >> 2) + 4 * tb + (i & 3), r = c / 24, o = c % 24, q = win ? o / 6 : o & 3, t = win ? o % 6 : o >> 2;
        return (win ? RAW_SV : 3 * t < NPR - 18 ? LDS_BUF + 18 * 1024 : -(NPR - 18) / 3 * WT) + t * WT + ((lane & 48) + 4 * q + r) * 16;
    }
    constexpr Tab() : img{}, p{}, c{}, d{}, e{} {
        for (int z = 0; z < 48; ++z) img[0][z >> 2][z & 3] = 0x3f803f80u;
        img[0][12][0] = 0x7f800000u;
        for (int z = 0; z < 8; ++z) img[0][36 + 10 + z][z >> 1] = z & 1 ? 0x3f800000u : 0x3f80u;
        for (int k = 0; k < 4; ++k) for (int lane = 0; lane < 64; ++lane) {
            const int i = lane & 15, g = lane >> 4, rowv = 8 * (i >> 2) + (i & 3);
            unsigned vi[20] = {};
            {
                const int pp = i >> 2, t = i & 3;
                const int t1 = (k + 3 - (SWP ? LK0 : LK1)) & 3;
                if (t1 < 3) job(SWP ? c[k] : p[k], lane, RAW_TV, RAW_TV + RAWM, 1, t1, scat(false, lane, t1, 0), scat(false, lane, t1, 1));
                const unsigned wl = lane * 16, lm = pp == g ? ~0u : 0u, idp = LDS_AUX + 576 + (t == g ? 10 + pp : 0) * 16, sidx = i & 1 ? 0xeeeeddddu : 0x4444ddddu;
                unsigned vA[3] = {};
                for (int m = 0; m < 3; ++m) {
                    const int n = 64 * m + lane, tf = n / 96, q = n % 96 / 12, s = (n % 12) ^ tf;
                    const unsigned cc = tf * FRAMEB + (3 * (s & 3) + (s >> 2)) * 16 + 2048 - 1024 * m;
                    vA[m] = cc + q * TOKB;
                    img[1][lane][m] = (cc + 6 * ROWB + 6 * TOKB) | ((unsigned)(2 * (q & 3) + (q >> 2)) << 28);
                }
                const unsigned vA0 = vA[0], vA1 = vA[1], vA2 = vA[2], vB0 = vA0 + (ROWB - 7 * TOKB), vB1 = vA1 + (ROWB - 7 * TOKB), vB2 = vA2 + (ROWB - 7 * TOKB);
                const unsigned dA = LDS_BUF + t * LFRAME + pp * LTOK + g * 16, rA = LDS_BUF + t * LFRAME + (pp == g ? pp : 4 + g) * LTOK + 192;
                img[1][lane][3] = (2 * pp * (4 * LTOK) + t * LTOK + g * 16) | (((pp == g ? 2 * pp : 2 * g + 1) * (4 * LTOK) + t * LTOK + 192) << 16);
                const unsigned cd0 = k < 3 ? dA + k * 8 * LTOK : LDS_STASH + (img[1][lane][3] & 0xffffu), cr0 = k < 3 ? rA + k * 8 * LTOK : LDS_STASH + (img[1][lane][3] >> 16);
                const unsigned xr = LDS_STG + k * LSTG + (t >> 1) * 3104 + ((t & 1) * 8 + pp) * TOKB + (g ^ (t & 1)) * 16;
                const unsigned axp = LDS_AUX + (t > ASP ? 384 : 0);
                const unsigned pf = g * FRAMEB + (i >> 1) * TOKB + (i & 1) * 188 + 2048;
                vi[0] = xr; vi[1] = lm; vi[2] = rA; vi[3] = dA; vi[4] = vA0; vi[5] = vA1; vi[6] = vA2; vi[7] = vB0; vi[8] = vB1; vi[9] = vB2; vi[10] = sidx; vi[11] = cd0; vi[12] = cr0; vi[13] = wl; vi[14] = idp; vi[15] = pf; vi[16] = axp;
                for (int z = 0; z < 20; ++z) p[k][2 + (z >> 2)][lane][z & 3] = vi[z];
            }
            {
                const int t0 = (k + 3 - (SWP ? LK1 : LK0)) & 3;
                const unsigned s0 = LDS_BUF + t0 * 6144 + lane * 16, s3 = LDS_BUF + k * 2 * WT + lane * 16, rs = RAW_T + 128;
                if (t0 < 3) job(SWP ? p[k] : c[k], lane, RAW_T, RAW_T + RAWM, 2, t0, s0, s0 + 3072); else job(SWP ? p[k] : c[k], lane, RAW_T, RAW_T + RAWM, 1, 0, LDS_SCRAP + rowv * 4, 0);
                if (k < 3) job(e[k], lane, rs, rs + RAWM, 0, k, s3, s3 + WT); else job(e[k], lane, rs, rs + RAWM, 1, 0, LDS_SCRAP + rowv * 4, 0);
                if (k > 0) job(e[4 + k], lane, RAW_TV, RAW_TV + RAWM, 1, k - 1, scat(true, lane, k - 1, 0), scat(true, lane, k - 1, 1));
                const int dg = 16 * (2 * (g & 1) + (g >> 1)), pix = 4 * (i & 3) + phi(i >> 2);
                const unsigned wl = lane * 16, fr = LDS_BUF + k * LFRAME, ext = LDS_EXT + k * LEXT;
                const unsigned ta = fr + pix * LTOK + dg, zr = LDS_AUX + 384 + g * 16, t30 = i & 3 ? zr : LDS_STASH + k * LTOK + dg;
                const unsigned xa = fr + (4 * (i >> 2) + phi(g)) * LTOK + 96 * (i & 1) + 8 * ((i >> 1) & 1);
                const int sg = 8 * ((i >> 1) & 1) + 4 * (i >> 3) + 2 * ((i >> 2) & 1) + (i & 1);
                const unsigned tv = fr + (32 + 4 * (sg & 3) + phi(sg >> 2)) * LTOK + dg, c1f = (i >> 2) == 2 * g ? 0x3f800000u : 0u;
                const unsigned sidx = g < 2 ? 0x0e0e0404u | ((i >> 2) == 2 * g ? (i & 3) * 0x00100010u : (i >> 2) == 2 * g + 1 ? (i & 3) * 0x40004000u : 0u) : 0xeeee4444u;
                const unsigned ga0 = phi(g) * TOKB + 48 * (i & 3) + 12 * (i >> 2), wr = ROWB - 7 * TOKB;
                const unsigned ga1 = ga0 + (phi(g) >= 1 ? wr : 0), ga2 = ga0 + (phi(g) >= 2 ? wr : 0), ga3 = ga0 + (phi(g) >= 3 ? wr : 0);
                const unsigned pb = LDS_SCRAP + k * 128, pr0 = i < 4 && (i >> 1) == (g >> 1) ? pb + (i & 1) * 32 + (g & 1) * 16 : zr, pr1 = i == 0 || i == 2 ? pb + 64 + g * 16 : zr;
                const unsigned pw = g < 2 ? pb + ((i >> 1) & 1) * 32 + (i >> 3) * 16 + ((i >> 2) & 1) * 8 + g * 4 + (i & 1) * 2
                                          : pb + 64 + (i >> 2) * 16 + (g & 1) * 4 + ((i >> 1) & 1) * 8 + (i & 1) * 2 + (lane == 57 ? 6 : 0);
                const unsigned six2 = (i & 1 ? 0xeeeeu : 0x4444u) | ((i & 2 ? 0xeeee0000u : 0x44440000u) & (g < 2 ? 0x0f0f0000u : 0xffff0000u));
                const unsigned exs0 = i & 2 ? LDS_AUX + g * 16 : ext + (i & 1) * 64 + g * 16, t3s0 = i & 2 ? zr : LDS_STASH + k * LTOK + 64 * (g >> 1) + 16 * (g & 1) + 4 * (i & 1);
                const unsigned ex0 = i & 1 ? zr : i & 2 ? LDS_AUX + g * 16 : ext + g * 16, exv0 = ext + 1536 + i * 24;
                const unsigned rla0 = lane < 48 ? fr + (16 * g + pix) * LTOK + 192 : lane == 56 ? LDS_STASH + k * LTOK + 192 : LDS_AUX + 192;
                img[2][lane][0] = ((i & 7) * (4 * LTOK) + dg) | ((((4 * g + (i & 3)) & 7) * (4 * LTOK) + 96 * ((i >> 2) & 1) + 8 * (i >> 3)) << 16);
                img[2][lane][1] = ((i & 7) * 192u + g * 16) | ((1536u + i * 24 + (g & 1) * 4 * VST) << 16);
                img[2][lane][2] = ((fr + phi(g) * LTOK + 192 - xa) & 0xffffu) | ((fr + pix * LTOK + 192 - xa) << 16);
                img[2][lane][3] = (LDS_AUX + 576 + (10 + (i & 3)) * 16) | ((1536u + (i & 7) * VST + 384) << 16);
                vi[0] = wl; vi[1] = pw; vi[2] = pr0; vi[3] = pr1; vi[4] = six2; vi[5] = exs0; vi[6] = t3s0; vi[7] = ta; vi[8] = t30; vi[9] = xa; vi[10] = rla0; vi[11] = exv0; vi[12] = ga0; vi[13] = ga1; vi[14] = ga2; vi[15] = ga3; vi[16] = ex0; vi[17] = tv; vi[18] = sidx; vi[19] = c1f;
                for (int z = 0; z < 20; ++z) d[k][2 + (z >> 2)][lane][z & 3] = vi[z];
            }
        }
    }
};
static __device__ const Tab TAB __attribute__((aligned(128))) = Tab();

#define CLOBBERS "v1", "v2", "v3", "v4", "v5", "v6", "v7", "v8", "v9", "v10", "v11", "v12", "v13", "v14", "v15", "v16", "v17", "v18", "v19", "v20", "v21", "v22", "v23", "v24", "v25", "v26", "v27", "v28", "v29", "v30", "v31", "v32", "v33", "v34", "v35", "v36", "v37", "v38", "v39", "v40", "v41", "v42", "v43", "v44", "v45", "v46", "v47", "v48", "v49", "v50", "v51", "v52", "v53", "v54", "v55", "v56", "v57", "v58", "v59", "v60", "v61", "v62", "v63", "v64", "v65", "v66", "v67", "v68", "v69", "v70", "v71", "v72", "v73", "v74", "v75", "v76", "v77", "v78", "v79", "v80", "v81", "v82", "v83", "v84", "v85", "v86", "v87", "v88", "v89", "v90", "v91", "v92", "v93", "v94", "v95", "v96", "v97", "v98", "v99", "v100", "v101", "v102", "v103", "v104", "v105", "v106", "v107", "v108", "v109", "v110", "v111", "v112", "v113", "v114", "v115", "v116", "v117", "v118", "v119", "v120", "v121", "v122", "v123", "v124", "v125", "v126", "v127", "v128", "v129", "v130", "v131", "v132", "v133", "v134", "v135", "v136", "v137", "v138", "v139", "v140", "v141", "v142", "v143", "v144", "v145", "v146", "v147", "v148", "v149", "v150", "v151", "v152", "v153", "v154", "v155", "v156", "v157", "v158", "v159", "v160", "v161", "v162", "v163", "v164", "v165", "v166", "v167", "v168", "v169", "v170", "v171", "v172", "v173", "v174", "v175", "v176", "v177", "v178", "v179", "v180", "v181", "v182", "v183", "v184", "v185", "v186", "v187", "v188", "v189", "v190", "v191", "v192", "v193", "v194", "v195", "v196", "v197", "v198", "v199", "v200", "v201", "v202", "v203", "v204", "v205", "v206", "v207", "v208", "v209", "v210", "v211", "v212", "v213", "v214", "v215", "v216", "v217", "v218", "v219", "v220", "v221", "v222", "v223", "v224", "v225", "v226", "v227", "v228", "v229", "v230", "v231", "v232", "v233", "v234", "v235", "v236", "v237", "v238", "v239", "v240", "v241", "v242", "v243", "v244", "v245", "v246", "v247", "v248", "v249", "v250", "v251", "v252", "v253", "v254", "v255", "s3", "s6", "s7", "s8", "s9", "s10", "s11", "s12", "s13", "s14", "s15", "s16", "s17", "s18", "s19", "s20", "s21", "s22", "s23", "s24", "s25", "s26", "s27", "s28", "s29", "s30", "s31", "s34", "s35", "s36", "s37", "s38", "s39", "s40", "s41", "s42", "s43", "s44", "s45", "s46", "s47", "s48", "s49", "s50", "s51", "s52", "s53", "s54", "s55", "s56", "s57", "s58", "s59", "s60", "s61", "s62", "s63", "s64", "s65", "s66", "s67", "s68", "s69", "s70", "s71", "s72", "s73", "s74", "s75", "s76", "s77", "vcc", "scc", "memory"
__global__ __launch_bounds__(512, 2) void swin_kernel(const char* x, const char* wqkv_t, const char* wo_t, const char* wqkv_s, const char* wo_s, char* out, const char* ws) {
    const unsigned long long kap = (unsigned long long)__builtin_amdgcn_kernarg_segment_ptr(), tab = (unsigned long long)&TAB;
    const unsigned tid = threadIdx.x, bid = blockIdx.x;
    asm volatile(
        "s_load_dwordx8 s[44:51], s[0:1], 0x0\n"
        "s_load_dwordx4 s[52:55], s[0:1], 0x20\n"
        "s_load_dwordx2 s[56:57], s[0:1], 0x30\n"
        "v_readfirstlane_b32 s3, v0\n"
        "v_and_b32 v56, 63, v0\n"
        "v_lshlrev_b32 v57, 4, v56\n"
        "s_bfe_u32 s3, s3, 0x30006\n"
        "s_and_b32 s6, s3, 3\n"
        "s_cmp_eq_u32 s2, 0\n"
        "s_cbranch_scc1 MPRO_%=\n"
        "PRO_%=:\n"
        "s_mul_i32 s64, s3, 9216\n"
        "s_add_u32 s64, s64, 21728\n"
        "s_mov_b32 s27, 0x20000\n"
        "s_cmp_lt_u32 s3, 4\n"
        "s_cbranch_scc0 PPRO_%=\n"
        "s_cmp_lg_u32 s6, 0\n"
        "s_cbranch_scc1 L75_%=\n"
        "s_mov_b32 exec_hi, 0\n"
        "v_lshlrev_b32 v27, 2, v56\n"
        "v_add_u32 v27, 21600, v27\n"
        "v_mov_b32 v28, 0\n"
        "ds_write_b32 v27, v28\n"
        "v_add_u32 v27, 132608, v27\n"
        "ds_write2_b32 v27, v28, v28 offset1:32\n"
        "s_mov_b64 exec, 0x1ffff\n"
        "s_bfe_u32 s7, s2, 0x50003\n"
        "s_mul_i32 s7, s7, 2144\n"
        "v_lshlrev_b32 v27, 7, v56\n"
        "v_add_u32 v27, s7, v27\n"
        "global_load_dword v252, v27, s[4:5]\n"
        "s_mov_b64 exec, -1\n"
        "L75_%=:\n"
        "s_cmp_lt_u32 s6, 2\n"
        "s_cselect_b32 s65, 3, 1\n"
        "s_cselect_b32 s61, 384, 0\n"
        "s_cselect_b32 s62, 9, 1\n"
        "s_cselect_b32 s63, 3, 4\n"
        "s_cselect_b32 s26, 3, 1\n"
        "s_cselect_b32 s34, 9, 1\n"
        "s_mul_i32 s65, s65, 192\n"
        "s_mul_i32 s62, s62, 3072\n"
        "s_mul_i32 s63, s63, 1536\n"
        "s_mul_i32 s26, s26, 18432\n"
        "s_mul_i32 s34, s34, 1536\n"
        "s_add_u32 s7, s62, s63\n"
        "s_lshl_b32 s29, s63, 1\n"
        "s_add_u32 s60, s7, s63\n"
        "s_bitcmp1_b32 s6, 0\n"
        "s_cselect_b32 s28, s7, 0\n"
        "s_cselect_b32 s29, s29, s62\n"
        "s_cselect_b32 s60, s60, s63\n"
        "s_add_u32 s28, s28, s61\n"
        "s_add_u32 s29, s29, s61\n"
        "s_add_u32 s60, s60, s61\n"
        "v_add_u32 v24, 0, v56\n"
        "v_mul_u32_u24 v21, 171, v24\n"
        "v_lshrrev_b32 v21, 11, v21\n"
        "v_mad_i32_i24 v24, v21, -12, v24\n"
        "v_bfe_u32 v58, v21, 2, 2\n"
        "v_xor_b32 v58, v24, v58\n"
        "v_and_b32 v27, 7, v21\n"
        "v_mul_u32_u24 v27, s65, v27\n"
        "v_lshl_add_u32 v58, v58, 4, v27\n"
        "v_add_u32 v25, 64, v56\n"
        "v_mul_u32_u24 v22, 171, v25\n"
        "v_lshrrev_b32 v22, 11, v22\n"
        "v_mad_i32_i24 v25, v22, -12, v25\n"
        "v_bfe_u32 v59, v22, 2, 2\n"
        "v_xor_b32 v59, v25, v59\n"
        "v_and_b32 v27, 7, v22\n"
        "v_mul_u32_u24 v27, s65, v27\n"
        "v_lshl_add_u32 v59, v59, 4, v27\n"
        "v_lshrrev_b32 v27, 3, v22\n"
        "v_mad_u32_u24 v59, v27, s34, v59\n"
        "v_add_u32 v26, 128, v56\n"
        "v_mul_u32_u24 v23, 171, v26\n"
        "v_lshrrev_b32 v23, 11, v23\n"
        "v_mad_i32_i24 v26, v23, -12, v26\n"
        "v_bfe_u32 v60, v23, 2, 2\n"
        "v_xor_b32 v60, v26, v60\n"
        "v_and_b32 v27, 7, v23\n"
        "v_mul_u32_u24 v27, s65, v27\n"
        "v_lshl_add_u32 v60, v60, 4, v27\n"
        "v_add_u32 v60, s34, v60\n"
        "s_add_u32 s64, s64, 36992\n"
        "s_mul_i32 s7, s6, 2048\n"
        "s_add_u32 s7, s7, 31744\n"
        "s_add_u32 s22, s4, s7\n"
        "s_addc_u32 s23, s5, 0\n"
        "v_mov_b32 v250, 21600\n"
        "s_mov_b32 s41, 0x400000\n"
        "s_waitcnt lgkmcnt(0)\n"
        "s_cmp_lg_u32 s6, 0\n"
        "s_cbranch_scc1 L76_%=\n"
        "s_bitcmp1_b32 s2, 7\n"
        "s_cselect_b64 s[8:9], s[46:47], s[50:51]\n"
        "s_cselect_b64 s[10:11], s[48:49], s[52:53]\n"
        "s_bfe_u32 s7, s2, 0x40003\n"
        "s_cmp_ge_u32 s7, 12\n"
        "s_cselect_b64 s[8:9], s[10:11], s[8:9]\n"
        "s_cselect_b32 s10, 12, 0\n"
        "s_sub_u32 s7, s7, s10\n"
        "s_mul_i32 s7, s7, 4608\n"
        "v_lshlrev_b32 v27, 7, v56\n"
        "v_add_u32 v27, s7, v27\n"
        "s_mov_b32 exec_hi, 0xf\n"
        "global_load_dword v252, v27, s[8:9]\n"
        "s_mov_b32 exec_hi, -1\n"
        "L76_%=:\n"
        "s_sub_u32 s7, s6, 0\n"
        "s_and_b32 s7, s7, 3\n"
        "s_min_u32 s7, s7, 2\n"
        "s_lshl_b32 s7, s7, 10\n"
        "s_add_u32 s73, s7, 18528\n"
        "v_add_u32 v27, s7, v57\n"
        "s_cmp_lt_u32 s6, 2\n"
        "s_cselect_b64 s[24:25], s[46:47], s[48:49]\n"
        "s_and_b32 s25, s25, 0xffff\n"
        "s_barrier\n"
        "s_mov_b32 m0, s73\n"
        "s_nop 0\n"
        "global_load_lds_dwordx4 v27, s[4:5]\n"
        "s_add_u32 m0, s64, 0\n"
        "s_nop 0\n"
        "buffer_load_dwordx4 v58, s[24:27], s28 offen lds\n"
        "s_add_u32 m0, s64, 1024\n"
        "s_nop 0\n"
        "buffer_load_dwordx4 v59, s[24:27], s28 offen lds\n"
        "s_add_u32 m0, s64, 2048\n"
        "s_nop 0\n"
        "buffer_load_dwordx4 v60, s[24:27], s28 offen lds\n"
        "s_add_u32 m0, s64, 3072\n"
        "s_nop 0\n"
        "buffer_load_dwordx4 v58, s[24:27], s29 offen lds\n"
        "s_add_u32 m0, s64, 4096\n"
        "s_nop 0\n"
        "buffer_load_dwordx4 v59, s[24:27], s29 offen lds\n"
        "s_add_u32 m0, s64, 5120\n"
        "s_nop 0\n"
        "buffer_load_dwordx4 v60, s[24:27], s29 offen lds\n"
        "s_add_u32 m0, s64, 6144\n"
        "s_nop 0\n"
        "buffer_load_dwordx4 v58, s[24:27], s60 offen lds\n"
        "s_add_u32 m0, s64, 7168\n"
        "s_nop 0\n"
        "buffer_load_dwordx4 v59, s[24:27], s60 offen lds\n"
        "s_add_u32 m0, s64, 8192\n"
        "s_nop 0\n"
        "buffer_load_dwordx4 v60, s[24:27], s60 offen lds\n"
        "global_load_dwordx4 v[240:243], v57, s[22:23] offset:0\n"
        "global_load_dwordx4 v[244:247], v57, s[22:23] offset:1024\n"
        "s_mov_b64 s[24:25], s[56:57]\n"
        "s_mov_b64 exec, 1\n"
        "v_bfe_u32 v27, s2, 3, 5\n"
        "v_lshlrev_b32 v27, 7, v27\n"
        "global_load_dword v253, v27, s[24:25] offset:256 sc1\n"
        "s_mov_b64 exec, -1\n"
        "s_setprio 2\n"
        "s_and_b32 s7, s2, 7\n"
        "s_lshl_b32 s7, s7, 5\n"
        "s_lshr_b32 s14, s2, 3\n"
        "s_add_u32 s7, s7, s14\n"
        "s_sub_u32 s7, 255, s7\n"
        "s_and_b32 s16, s7, 255\n"
        "s_sub_u32 s7, s7, s16\n"
        "s_mul_i32 s7, s7, 20\n"
        "s_add_u32 s7, s7, s16\n"
        "s_mov_b32 s14, 22\n"
        "s_cmp_eq_u32 s2, 0\n"
        "s_cselect_b32 s43, 19, 20\n"
        "s_mul_i32 s15, s7, 57457\n"
        "s_lshr_b32 s15, s15, 22\n"
        "s_mov_b32 s16, s7\n"
        "s_mul_i32 s15, s15, 588672\n"
        "s_mul_i32 s7, s16, 1344\n"
        "s_add_u32 s15, s15, s7\n"
        "s_mov_b32 s8, s54\n"
        "s_and_b32 s9, s55, 0xffff\n"
        "s_mov_b32 s10, 200540928\n"
        "s_mov_b32 s11, 0x20000\n"
        "s_mov_b64 s[12:13], s[54:55]\n"
        "s_mul_i32 s17, s6, 50135232\n"
        "s_mul_i32 s18, s6, 208\n"
        "s_add_u32 s18, s18, 109536\n"
        "s_mul_i32 s19, s6, 4672\n"
        "s_add_u32 s19, s19, 135520\n"
        "s_mul_i32 s7, s6, 7168\n"
        "s_add_u32 s7, s7, 43008\n"
        "s_add_u32 s22, s4, s7\n"
        "s_addc_u32 s23, s5, 0\n"
        "s_waitcnt vmcnt(0)\n"
        "s_mov_b64 exec, 1\n"
        "ds_inc_u32 v250, v250 offset:20\n"
        "s_mov_b64 exec, -1\n"
        "L77_%=:\n"
        "ds_read_b32 v251, v250 offset:20\n"
        "s_waitcnt lgkmcnt(0)\n"
        "v_readfirstlane_b32 s40, v251\n"
        "s_nop 3\n"
        "s_cmp_ge_i32 s40, 4\n"
        "s_cbranch_scc1 L78_%=\n"
        "s_nop 0\n"
        "s_sub_u32 s41, s41, 1\n"
        "s_cbranch_scc0 L77_%=\n"
        "s_endpgm\n"
        "L78_%=:\n"
        "s_cmp_eq_u32 s6, 1\n"
        "s_cbranch_scc1 L81_%=\n"
        "s_mov_b32 s34, 0x411cc471\n"
        "s_mov_b32 s35, 21632\n"
        "s_mov_b32 s36, 21624\n"
        "s_mov_b32 s37, 21628\n"
        "s_call_b64 s[30:31], FOLDW_%=\n"
        "s_branch L82_%=\n"
        "L81_%=:\n"
        "s_mov_b64 exec, 1\n"
        "ds_inc_u32 v250, v250 offset:32\n"
        "s_mov_b64 exec, -1\n"
        "global_load_dwordx4 v[40:43], v57, s[22:23] offset:-3072\n"
        "global_load_dwordx4 v[44:47], v57, s[22:23] offset:-2048\n"
        "global_load_dwordx4 v[0:3], v57, s[22:23] offset:-1024\n"
        "global_load_dwordx4 v[4:7], v57, s[22:23] offset:0\n"
        "global_load_dwordx4 v[8:11], v57, s[22:23] offset:1024\n"
        "global_load_dwordx4 v[12:15], v57, s[22:23] offset:2048\n"
        "global_load_dwordx4 v[16:19], v57, s[22:23] offset:3072\n"
        "L82_%=:\n"
        "s_setprio 2\n"
        "s_cmp_lg_u32 s6, 1\n"
        "s_cbranch_scc1 PY_%=\n"
        "v_readfirstlane_b32 s26, v253\n"
        "s_cmp_eq_u32 s2, 0\n"
        "s_cbranch_scc1 L80_%=\n"
        "L79_%=:\n"
        "s_mov_b64 exec, 1\n"
        "v_bfe_u32 v27, s2, 3, 5\n"
        "v_lshlrev_b32 v27, 7, v27\n"
        "global_load_dword v253, v27, s[24:25] offset:256 sc1\n"
        "s_mov_b64 exec, -1\n"
        "s_waitcnt vmcnt(0)\n"
        "v_readfirstlane_b32 s7, v253\n"
        "s_nop 3\n"
        "s_cmp_lg_u32 s7, s26\n"
        "s_cbranch_scc1 L80_%=\n"
        "s_sleep 2\n"
        "s_sub_u32 s41, s41, 64\n"
        "s_cbranch_scc0 L79_%=\n"
        "s_endpgm\n"
        "L80_%=:\n"
        "s_mov_b64 exec, 1\n"
        "ds_inc_u32 v250, v250 offset:36\n"
        "s_mov_b64 exec, -1\n"
        "s_branch PX_%=\n"
        "PY_%=:\n"
        "PX_%=:\n"
        "s_mul_i32 s7, s6, 9216\n"
        "s_add_u32 s7, s7, 12288\n"
        "s_add_u32 s70, s56, s7\n"
        "s_addc_u32 s71, s57, 0\n"
        "v_add_u32 v28, 4096, v57\n"
        "L83_%=:\n"
        "ds_read_b32 v251, v250 offset:32\n"
        "s_waitcnt lgkmcnt(0)\n"
        "v_readfirstlane_b32 s40, v251\n"
        "s_nop 3\n"
        "s_cmp_ge_i32 s40, 4\n"
        "s_cbranch_scc1 L84_%=\n"
        "s_nop 0\n"
        "s_sub_u32 s41, s41, 1\n"
        "s_cbranch_scc0 L83_%=\n"
        "s_endpgm\n"
        "L84_%=:\n"
        "L85_%=:\n"
        "ds_read_b32 v251, v250 offset:36\n"
        "s_waitcnt lgkmcnt(0)\n"
        "v_readfirstlane_b32 s40, v251\n"
        "s_nop 3\n"
        "s_cmp_ge_i32 s40, 1\n"
        "s_cbranch_scc1 L86_%=\n"
        "s_sleep 1\n"
        "s_sub_u32 s41, s41, 1\n"
        "s_cbranch_scc0 L85_%=\n"
        "s_endpgm\n"
        "L86_%=:\n"
        "global_load_dwordx4 v[64:67], v57, s[70:71] offset:-4096 sc1\n"
        "global_load_dwordx4 v[68:71], v57, s[70:71] offset:-3072 sc1\n"
        "global_load_dwordx4 v[72:75], v57, s[70:71] offset:-2048 sc1\n"
        "global_load_dwordx4 v[76:79], v57, s[70:71] offset:-1024 sc1\n"
        "global_load_dwordx4 v[80:83], v57, s[70:71] offset:0 sc1\n"
        "global_load_dwordx4 v[84:87], v57, s[70:71] offset:1024 sc1\n"
        "global_load_dwordx4 v[88:91], v57, s[70:71] offset:2048 sc1\n"
        "global_load_dwordx4 v[92:95], v57, s[70:71] offset:3072 sc1\n"
        "global_load_dwordx4 v[96:99], v28, s[70:71] sc1\n"
        "s_cmp_lt_u32 s6, 2\n"
        "s_cselect_b32 s7, 80320, 0\n"
        "s_mul_i32 s28, s6, 9264\n"
        "s_add_u32 s7, s7, s28\n"
        "s_add_u32 s7, s7, -18528\n"
        "v_add_u32 v27, s7, v57\n"
        "s_waitcnt vmcnt(8)\n"
        "ds_write_b128 v27, v[64:67] offset:0\n"
        "s_waitcnt vmcnt(7)\n"
        "ds_write_b128 v27, v[68:71] offset:1024\n"
        "s_waitcnt vmcnt(6)\n"
        "ds_write_b128 v27, v[72:75] offset:2048\n"
        "s_waitcnt vmcnt(5)\n"
        "ds_write_b128 v27, v[76:79] offset:3088\n"
        "s_waitcnt vmcnt(4)\n"
        "ds_write_b128 v27, v[80:83] offset:4112\n"
        "s_waitcnt vmcnt(3)\n"
        "ds_write_b128 v27, v[84:87] offset:5136\n"
        "s_waitcnt vmcnt(2)\n"
        "ds_write_b128 v27, v[88:91] offset:6176\n"
        "s_waitcnt vmcnt(1)\n"
        "ds_write_b128 v27, v[92:95] offset:7200\n"
        "s_waitcnt vmcnt(0)\n"
        "ds_write_b128 v27, v[96:99] offset:8224\n"
        "s_mov_b64 exec, 1\n"
        "ds_inc_u32 v250, v250 offset:52\n"
        "s_mov_b64 exec, -1\n"
        "s_branch CONS_%=\n"
        "FOLDN_%=:\n"
        "ds_read_b128 v[88:91], v242\n"
        "ds_read_b128 v[172:175], v240\n"
        "ds_read_b128 v[180:183], v242 offset:64\n"
        "ds_read_b128 v[96:99], v240 offset:64\n"
        "ds_read_b128 v[184:187], v242 offset:128\n"
        "ds_read_b128 v[168:171], v240 offset:128\n"
        "ds_read_b128 v[156:159], v241\n"
        "ds_read_b128 v[92:95], v241 offset:64\n"
        "ds_read_b128 v[144:147], v241 offset:128\n"
        "ds_read_b128 v[148:151], v240 offset:6144\n"
        "ds_read_b128 v[140:143], v240 offset:6208\n"
        "ds_read_b128 v[100:103], v240 offset:6272\n"
        "ds_read_b128 v[136:139], v241 offset:6144\n"
        "ds_read_b128 v[104:107], v241 offset:6208\n"
        "ds_read_b128 v[108:111], v241 offset:6272\n"
        "s_waitcnt lgkmcnt(13)\n"
        "v_mfma_f32_16x16x32_bf16 v[64:67], v[172:175], v[88:91], 0\n"
        "s_waitcnt lgkmcnt(11)\n"
        "ds_read_b128 v[112:115], v240 offset:12288\n"
        "v_mfma_f32_16x16x32_bf16 v[64:67], v[96:99], v[180:183], v[64:67]\n"
        "s_waitcnt lgkmcnt(8)\n"
        "ds_read_b128 v[116:119], v240 offset:12352\n"
        "v_mfma_f32_16x16x32_bf16 v[64:67], v[168:171], v[184:187], v[64:67]\n"
        "ds_read_b128 v[124:127], v240 offset:12416\n"
        "v_mfma_f32_16x16x32_bf16 v[68:71], v[156:159], v[88:91], 0\n"
        "ds_read_b128 v[120:123], v241 offset:12288\n"
        "v_mfma_f32_16x16x32_bf16 v[68:71], v[92:95], v[180:183], v[68:71]\n"
        "s_waitcnt lgkmcnt(4)\n"
        "ds_read_b128 v[128:131], v241 offset:12352\n"
        "v_mfma_f32_16x16x32_bf16 v[68:71], v[144:147], v[184:187], v[68:71]\n"
        "ds_read_b128 v[132:135], v241 offset:12416\n"
        "v_mfma_f32_16x16x32_bf16 v[72:75], v[148:151], v[88:91], 0\n"
        "ds_read_b128 v[152:155], v243\n"
        "v_mfma_f32_16x16x32_bf16 v[72:75], v[140:143], v[180:183], v[72:75]\n"
        "ds_read_b128 v[160:163], v243 offset:64\n"
        "v_mfma_f32_16x16x32_bf16 v[72:75], v[100:103], v[184:187], v[72:75]\n"
        "ds_read_b128 v[164:167], v243 offset:128\n"
        "v_mfma_f32_16x16x32_bf16 v[76:79], v[136:139], v[88:91], 0\n"
        "v_add_f32 v176, v64, v68\n"
        "v_add_f32 v177, v65, v69\n"
        "v_add_f32 v178, v66, v70\n"
        "v_add_f32 v179, v67, v71\n"
        "v_mfma_f32_16x16x32_bf16 v[76:79], v[104:107], v[180:183], v[76:79]\n"
        "v_mfma_f32_16x16x32_bf16 v[76:79], v[108:111], v[184:187], v[76:79]\n"
        "s_waitcnt lgkmcnt(3)\n"
        "v_add_f32 v176, v176, v72\n"
        "v_add_f32 v177, v177, v73\n"
        "v_add_f32 v178, v178, v74\n"
        "v_add_f32 v179, v179, v75\n"
        "v_mfma_f32_16x16x32_bf16 v[80:83], v[112:115], v[88:91], 0\n"
        "global_load_dwordx4 v[0:3], v57, s[22:23] offset:-1024\n"
        "v_mfma_f32_16x16x32_bf16 v[80:83], v[116:119], v[180:183], v[80:83]\n"
        "v_add_f32 v176, v176, v76\n"
        "v_add_f32 v177, v177, v77\n"
        "v_add_f32 v178, v178, v78\n"
        "v_add_f32 v179, v179, v79\n"
        "v_mfma_f32_16x16x32_bf16 v[80:83], v[124:127], v[184:187], v[80:83]\n"
        "global_load_dwordx4 v[4:7], v57, s[22:23] offset:0\n"
        "v_mfma_f32_16x16x32_bf16 v[84:87], v[120:123], v[88:91], 0\n"
        "v_mfma_f32_16x16x32_bf16 v[84:87], v[128:131], v[180:183], v[84:87]\n"
        "global_load_dwordx4 v[8:11], v57, s[22:23] offset:1024\n"
        "v_mfma_f32_16x16x32_bf16 v[84:87], v[132:135], v[184:187], v[84:87]\n"
        "s_waitcnt lgkmcnt(0)\n"
        "v_mov_b32 v239, s35\n"
        "s_mov_b64 exec, 1\n"
        "ds_inc_u32 v239, v239 offset:0\n"
        "s_mov_b64 exec, -1\n"
        "v_add_f32 v176, v176, v80\n"
        "v_add_f32 v177, v177, v81\n"
        "v_add_f32 v178, v178, v82\n"
        "v_add_f32 v179, v179, v83\n"
        "v_add_f32 v176, v176, v84\n"
        "v_add_f32 v177, v177, v85\n"
        "v_add_f32 v178, v178, v86\n"
        "v_add_f32 v179, v179, v87\n"
        "v_add_f32 v176, v176, v177\n"
        "v_add_f32 v178, v178, v179\n"
        "v_add_f32 v176, v176, v178\n"
        "v_mov_b32 v177, v176\n"
        "v_mfma_f32_16x16x32_bf16 v[88:91], v[172:175], v[152:155], 0\n"
        "global_load_dwordx4 v[12:15], v57, s[22:23] offset:2048\n"
        "v_permlane32_swap_b32 v176, v177\n"
        "v_add_f32 v176, v176, v177\n"
        "v_mov_b32 v177, v176\n"
        "v_mfma_f32_16x16x32_bf16 v[88:91], v[96:99], v[160:163], v[88:91]\n"
        "v_mfma_f32_16x16x32_bf16 v[88:91], v[168:171], v[164:167], v[88:91]\n"
        "global_load_dwordx4 v[16:19], v57, s[22:23] offset:3072\n"
        "v_permlane16_swap_b32 v176, v177\n"
        "v_add_f32 v176, v176, v177\n"
        "v_mul_f32 v176, 0xbc2aaaab, v176\n"
        "v_mul_f32 v176, s34, v176\n"
        "v_fma_f32 v64, v64, s34, v176\n"
        "v_fma_f32 v65, v65, s34, v176\n"
        "v_fma_f32 v66, v66, s34, v176\n"
        "v_fma_f32 v67, v67, s34, v176\n"
        "v_fma_f32 v68, v68, s34, v176\n"
        "v_fma_f32 v69, v69, s34, v176\n"
        "v_fma_f32 v70, v70, s34, v176\n"
        "v_fma_f32 v71, v71, s34, v176\n"
        "v_fma_f32 v72, v72, s34, v176\n"
        "v_fma_f32 v73, v73, s34, v176\n"
        "v_fma_f32 v74, v74, s34, v176\n"
        "v_fma_f32 v75, v75, s34, v176\n"
        "v_fma_f32 v76, v76, s34, v176\n"
        "v_fma_f32 v77, v77, s34, v176\n"
        "v_fma_f32 v78, v78, s34, v176\n"
        "v_fma_f32 v79, v79, s34, v176\n"
        "v_fma_f32 v80, v80, s34, v176\n"
        "v_fma_f32 v81, v81, s34, v176\n"
        "v_fma_f32 v82, v82, s34, v176\n"
        "v_fma_f32 v83, v83, s34, v176\n"
        "v_fma_f32 v84, v84, s34, v176\n"
        "v_fma_f32 v85, v85, s34, v176\n"
        "v_fma_f32 v86, v86, s34, v176\n"
        "v_fma_f32 v87, v87, s34, v176\n"
        "v_mfma_f32_16x16x32_bf16 v[96:99], v[156:159], v[152:155], 0\n"
        "v_mfma_f32_16x16x32_bf16 v[96:99], v[92:95], v[160:163], v[96:99]\n"
        "s_add_u32 m0, s17, 0\n"
        "s_nop 0\n"
        "buffer_load_dwordx4 v29, s[8:11], s74 offen lds\n"
        "v_mfma_f32_16x16x32_bf16 v[96:99], v[144:147], v[164:167], v[96:99]\n"
        "v_mfma_f32_16x16x32_bf16 v[92:95], v[148:151], v[152:155], 0\n"
        "buffer_load_dwordx4 v30, s[8:11], s74 offen offset:1024 lds\n"
        "v_mfma_f32_16x16x32_bf16 v[92:95], v[140:143], v[160:163], v[92:95]\n"
        "v_mfma_f32_16x16x32_bf16 v[92:95], v[100:103], v[164:167], v[92:95]\n"
        "buffer_load_dwordx4 v31, s[8:11], s74 offen offset:2048 lds\n"
        "v_mfma_f32_16x16x32_bf16 v[100:103], v[136:139], v[152:155], 0\n"
        "v_mfma_f32_16x16x32_bf16 v[100:103], v[104:107], v[160:163], v[100:103]\n"
        "s_add_u32 m0, s17, 3104\n"
        "s_nop 0\n"
        "buffer_load_dwordx4 v29, s[8:11], s75 offen lds\n"
        "v_add_f32 v136, v88, v96\n"
        "v_add_f32 v137, v89, v97\n"
        "v_add_f32 v138, v90, v98\n"
        "v_add_f32 v139, v91, v99\n"
        "v_add_f32 v136, v136, v92\n"
        "v_add_f32 v137, v137, v93\n"
        "v_add_f32 v138, v138, v94\n"
        "v_add_f32 v139, v139, v95\n"
        "v_mfma_f32_16x16x32_bf16 v[100:103], v[108:111], v[164:167], v[100:103]\n"
        "v_mfma_f32_16x16x32_bf16 v[104:107], v[112:115], v[152:155], 0\n"
        "buffer_load_dwordx4 v30, s[8:11], s75 offen offset:1024 lds\n"
        "v_mfma_f32_16x16x32_bf16 v[104:107], v[116:119], v[160:163], v[104:107]\n"
        "v_mfma_f32_16x16x32_bf16 v[104:107], v[124:127], v[164:167], v[104:107]\n"
        "buffer_load_dwordx4 v31, s[8:11], s75 offen offset:2048 lds\n"
        "v_mfma_f32_16x16x32_bf16 v[108:111], v[120:123], v[152:155], 0\n"
        "v_mfma_f32_16x16x32_bf16 v[108:111], v[128:131], v[160:163], v[108:111]\n"
        "v_mfma_f32_16x16x32_bf16 v[108:111], v[132:135], v[164:167], v[108:111]\n"
        "v_add_f32 v136, v136, v100\n"
        "v_add_f32 v137, v137, v101\n"
        "v_add_f32 v138, v138, v102\n"
        "v_add_f32 v139, v139, v103\n"
        "v_add_f32 v136, v136, v104\n"
        "v_add_f32 v137, v137, v105\n"
        "v_add_f32 v138, v138, v106\n"
        "v_add_f32 v139, v139, v107\n"
        "v_add_f32 v136, v136, v108\n"
        "v_add_f32 v137, v137, v109\n"
        "v_add_f32 v138, v138, v110\n"
        "v_add_f32 v139, v139, v111\n"
        "v_add_f32 v136, v136, v137\n"
        "v_add_f32 v138, v138, v139\n"
        "v_add_f32 v136, v136, v138\n"
        "v_mov_b32 v137, v136\n"
        "v_mov_b32 v239, s37\n"
        "s_nop 0\n"
        "v_permlane32_swap_b32 v136, v137\n"
        "v_add_f32 v136, v136, v137\n"
        "v_mov_b32 v137, v136\n"
        "s_nop 1\n"
        "v_permlane16_swap_b32 v136, v137\n"
        "v_add_f32 v136, v136, v137\n"
        "v_mul_f32 v136, 0xbc2aaaab, v136\n"
        "v_mul_f32 v136, s34, v136\n"
        "v_fma_f32 v88, v88, s34, v136\n"
        "v_fma_f32 v89, v89, s34, v136\n"
        "v_fma_f32 v90, v90, s34, v136\n"
        "v_fma_f32 v91, v91, s34, v136\n"
        "v_fma_f32 v96, v96, s34, v136\n"
        "v_fma_f32 v97, v97, s34, v136\n"
        "v_fma_f32 v98, v98, s34, v136\n"
        "v_fma_f32 v99, v99, s34, v136\n"
        "v_fma_f32 v92, v92, s34, v136\n"
        "v_fma_f32 v93, v93, s34, v136\n"
        "v_fma_f32 v94, v94, s34, v136\n"
        "v_fma_f32 v95, v95, s34, v136\n"
        "v_fma_f32 v100, v100, s34, v136\n"
        "v_fma_f32 v101, v101, s34, v136\n"
        "v_fma_f32 v102, v102, s34, v136\n"
        "v_fma_f32 v103, v103, s34, v136\n"
        "v_fma_f32 v104, v104, s34, v136\n"
        "v_fma_f32 v105, v105, s34, v136\n"
        "v_fma_f32 v106, v106, s34, v136\n"
        "v_fma_f32 v107, v107, s34, v136\n"
        "v_fma_f32 v108, v108, s34, v136\n"
        "v_fma_f32 v109, v109, s34, v136\n"
        "v_fma_f32 v110, v110, s34, v136\n"
        "v_fma_f32 v111, v111, s34, v136\n"
        "v_mov_b32 v237, s36\n"
        "L66_%=:\n"
        "ds_read_b32 v238, v237 offset:0\n"
        "s_waitcnt lgkmcnt(0)\n"
        "v_readfirstlane_b32 s40, v238\n"
        "s_nop 3\n"
        "s_cmp_ge_i32 s40, 4\n"
        "s_cbranch_scc1 L67_%=\n"
        "s_nop 0\n"
        "s_sub_u32 s41, s41, 1\n"
        "s_cbranch_scc0 L66_%=\n"
        "s_endpgm\n"
        "L67_%=:\n"
        "ds_read_b128 v[132:135], v246\n"
        "ds_read_b128 v[128:131], v246 offset:16\n"
        "ds_read_b128 v[124:127], v246 offset:128\n"
        "ds_read_b128 v[120:123], v246 offset:144\n"
        "ds_read_b128 v[116:119], v246 offset:256\n"
        "ds_read_b128 v[112:115], v246 offset:272\n"
        "s_waitcnt lgkmcnt(5)\n"
        "v_add_f32 v64, v64, v132\n"
        "v_add_f32 v65, v65, v133\n"
        "v_add_f32 v66, v66, v134\n"
        "v_add_f32 v67, v67, v135\n"
        "s_waitcnt lgkmcnt(0)\n"
        "v_add_f32 v68, v68, v128\n"
        "v_add_f32 v69, v69, v129\n"
        "v_add_f32 v70, v70, v130\n"
        "v_add_f32 v71, v71, v131\n"
        "v_add_f32 v88, v88, v132\n"
        "v_add_f32 v89, v89, v133\n"
        "v_add_f32 v90, v90, v134\n"
        "v_add_f32 v91, v91, v135\n"
        "v_add_f32 v96, v96, v128\n"
        "v_add_f32 v97, v97, v129\n"
        "v_add_f32 v98, v98, v130\n"
        "v_add_f32 v99, v99, v131\n"
        "v_add_f32 v72, v72, v124\n"
        "v_add_f32 v73, v73, v125\n"
        "v_add_f32 v74, v74, v126\n"
        "v_add_f32 v75, v75, v127\n"
        "v_add_f32 v76, v76, v120\n"
        "v_add_f32 v77, v77, v121\n"
        "v_add_f32 v78, v78, v122\n"
        "v_add_f32 v79, v79, v123\n"
        "v_add_f32 v92, v92, v124\n"
        "v_add_f32 v93, v93, v125\n"
        "v_add_f32 v94, v94, v126\n"
        "v_add_f32 v95, v95, v127\n"
        "v_add_f32 v100, v100, v120\n"
        "v_add_f32 v101, v101, v121\n"
        "v_add_f32 v102, v102, v122\n"
        "v_add_f32 v103, v103, v123\n"
        "v_add_f32 v80, v80, v116\n"
        "v_add_f32 v81, v81, v117\n"
        "v_add_f32 v82, v82, v118\n"
        "v_add_f32 v83, v83, v119\n"
        "v_add_f32 v84, v84, v112\n"
        "v_add_f32 v85, v85, v113\n"
        "v_add_f32 v86, v86, v114\n"
        "v_add_f32 v87, v87, v115\n"
        "v_add_f32 v104, v104, v116\n"
        "v_add_f32 v105, v105, v117\n"
        "v_add_f32 v106, v106, v118\n"
        "v_add_f32 v107, v107, v119\n"
        "v_add_f32 v108, v108, v112\n"
        "v_add_f32 v109, v109, v113\n"
        "v_add_f32 v110, v110, v114\n"
        "v_add_f32 v111, v111, v115\n"
        "v_cvt_pk_bf16_f32 v112, v64, v65\n"
        "v_cvt_pk_bf16_f32 v113, v66, v67\n"
        "v_cvt_pk_bf16_f32 v114, v68, v69\n"
        "v_cvt_pk_bf16_f32 v115, v70, v71\n"
        "v_cvt_pk_bf16_f32 v64, v88, v89\n"
        "v_cvt_pk_bf16_f32 v65, v90, v91\n"
        "v_cvt_pk_bf16_f32 v66, v96, v97\n"
        "v_cvt_pk_bf16_f32 v67, v98, v99\n"
        "v_cvt_pk_bf16_f32 v68, v72, v73\n"
        "v_cvt_pk_bf16_f32 v69, v74, v75\n"
        "v_cvt_pk_bf16_f32 v70, v76, v77\n"
        "v_cvt_pk_bf16_f32 v71, v78, v79\n"
        "v_cvt_pk_bf16_f32 v72, v92, v93\n"
        "v_cvt_pk_bf16_f32 v73, v94, v95\n"
        "v_cvt_pk_bf16_f32 v74, v100, v101\n"
        "v_cvt_pk_bf16_f32 v75, v102, v103\n"
        "v_cvt_pk_bf16_f32 v76, v80, v81\n"
        "v_cvt_pk_bf16_f32 v77, v82, v83\n"
        "v_cvt_pk_bf16_f32 v78, v84, v85\n"
        "v_cvt_pk_bf16_f32 v79, v86, v87\n"
        "v_cvt_pk_bf16_f32 v80, v104, v105\n"
        "v_cvt_pk_bf16_f32 v81, v106, v107\n"
        "v_cvt_pk_bf16_f32 v82, v108, v109\n"
        "v_cvt_pk_bf16_f32 v83, v110, v111\n"
        "ds_write_b128 v244, v[112:115]\n"
        "ds_write_b128 v245, v[64:67]\n"
        "ds_write_b128 v244, v[68:71] offset:1024\n"
        "ds_write_b128 v245, v[72:75] offset:1024\n"
        "ds_write_b128 v244, v[76:79] offset:2048\n"
        "ds_write_b128 v245, v[80:83] offset:2048\n"
        "s_mov_b64 exec, 1\n"
        "ds_inc_u32 v239, v239 offset:0\n"
        "s_mov_b64 exec, -1\n"
        "s_setpc_b64 s[30:31]\n"
        "FOLDS_%=:\n"
        "ds_read_b64_tr_b16 v[92:93], v242 offset:0\n"
        "ds_read_b64_tr_b16 v[94:95], v243 offset:0\n"
        "ds_read_b64_tr_b16 v[88:89], v242 offset:6144\n"
        "ds_read_b64_tr_b16 v[90:91], v243 offset:6144\n"
        "ds_read_b64_tr_b16 v[84:85], v242 offset:12288\n"
        "ds_read_b64_tr_b16 v[86:87], v243 offset:12288\n"
        "ds_read_b64_tr_b16 v[80:81], v242 offset:8\n"
        "ds_read_b64_tr_b16 v[82:83], v243 offset:8\n"
        "ds_read_b64_tr_b16 v[68:69], v242 offset:6152\n"
        "ds_read_b64_tr_b16 v[70:71], v243 offset:6152\n"
        "ds_read_b64_tr_b16 v[72:73], v242 offset:12296\n"
        "ds_read_b64_tr_b16 v[74:75], v243 offset:12296\n"
        "v_mov_b32 v64, 0x3f803f80\n"
        "v_mov_b32 v65, 0x3f803f80\n"
        "v_mov_b32 v66, 0x3f803f80\n"
        "v_mov_b32 v67, 0x3f803f80\n"
        "ds_read_b64_tr_b16 v[76:77], v242 offset:64\n"
        "ds_read_b64_tr_b16 v[78:79], v243 offset:64\n"
        "s_waitcnt lgkmcnt(12)\n"
        "v_mfma_f32_16x16x32_bf16 v[104:107], v[92:95], v[64:67], 0\n"
        "s_waitcnt lgkmcnt(10)\n"
        "v_mfma_f32_16x16x32_bf16 v[104:107], v[88:91], v[64:67], v[104:107]\n"
        "s_waitcnt lgkmcnt(6)\n"
        "ds_read_b64_tr_b16 v[100:101], v242 offset:6208\n"
        "ds_read_b64_tr_b16 v[102:103], v243 offset:6208\n"
        "v_mfma_f32_16x16x32_bf16 v[104:107], v[84:87], v[64:67], v[104:107]\n"
        "ds_read_b64_tr_b16 v[92:93], v242 offset:12352\n"
        "ds_read_b64_tr_b16 v[94:95], v243 offset:12352\n"
        "v_mfma_f32_16x16x32_bf16 v[108:111], v[80:83], v[64:67], 0\n"
        "s_waitcnt lgkmcnt(6)\n"
        "ds_read_b64_tr_b16 v[96:97], v242 offset:72\n"
        "ds_read_b64_tr_b16 v[98:99], v243 offset:72\n"
        "v_mfma_f32_16x16x32_bf16 v[108:111], v[68:71], v[64:67], v[108:111]\n"
        "ds_read_b64_tr_b16 v[80:81], v242 offset:6216\n"
        "ds_read_b64_tr_b16 v[82:83], v243 offset:6216\n"
        "v_cvt_pk_bf16_f32 v68, v104, v105\n"
        "v_cvt_pk_bf16_f32 v69, v106, v107\n"
        "v_mfma_f32_16x16x32_bf16 v[108:111], v[72:75], v[64:67], v[108:111]\n"
        "s_waitcnt lgkmcnt(8)\n"
        "ds_read_b64_tr_b16 v[84:85], v242 offset:12360\n"
        "ds_read_b64_tr_b16 v[86:87], v243 offset:12360\n"
        "v_mfma_f32_16x16x32_bf16 v[104:107], v[76:79], v[64:67], 0\n"
        "ds_read_b64_tr_b16 v[88:89], v242 offset:128\n"
        "ds_read_b64_tr_b16 v[90:91], v243 offset:128\n"
        "s_waitcnt lgkmcnt(10)\n"
        "v_mfma_f32_16x16x32_bf16 v[104:107], v[100:103], v[64:67], v[104:107]\n"
        "ds_read_b64_tr_b16 v[76:77], v242 offset:6272\n"
        "ds_read_b64_tr_b16 v[78:79], v243 offset:6272\n"
        "v_cvt_pk_bf16_f32 v70, v108, v109\n"
        "v_cvt_pk_bf16_f32 v71, v110, v111\n"
        "s_waitcnt lgkmcnt(9)\n"
        "v_mfma_f32_16x16x32_bf16 v[104:107], v[92:95], v[64:67], v[104:107]\n"
        "s_waitcnt lgkmcnt(8)\n"
        "ds_read_b64_tr_b16 v[100:101], v242 offset:12416\n"
        "ds_read_b64_tr_b16 v[102:103], v243 offset:12416\n"
        "v_mfma_f32_16x16x32_bf16 v[108:111], v[96:99], v[64:67], 0\n"
        "s_waitcnt lgkmcnt(8)\n"
        "ds_read_b64_tr_b16 v[96:97], v242 offset:136\n"
        "ds_read_b64_tr_b16 v[98:99], v243 offset:136\n"
        "v_mfma_f32_16x16x32_bf16 v[108:111], v[80:83], v[64:67], v[108:111]\n"
        "ds_read_b64_tr_b16 v[92:93], v242 offset:6280\n"
        "ds_read_b64_tr_b16 v[94:95], v243 offset:6280\n"
        "v_cvt_pk_bf16_f32 v72, v104, v105\n"
        "v_cvt_pk_bf16_f32 v73, v106, v107\n"
        "s_waitcnt lgkmcnt(8)\n"
        "v_mfma_f32_16x16x32_bf16 v[108:111], v[84:87], v[64:67], v[108:111]\n"
        "ds_read_b64_tr_b16 v[80:81], v242 offset:12424\n"
        "ds_read_b64_tr_b16 v[82:83], v243 offset:12424\n"
        "v_mfma_f32_16x16x32_bf16 v[104:107], v[88:91], v[64:67], 0\n"
        "s_waitcnt lgkmcnt(8)\n"
        "ds_read_b128 v[84:87], v240\n"
        "global_load_dwordx4 v[0:3], v57, s[22:23] offset:-1024\n"
        "v_mfma_f32_16x16x32_bf16 v[104:107], v[76:79], v[64:67], v[104:107]\n"
        "s_waitcnt lgkmcnt(7)\n"
        "ds_read_b128 v[88:91], v241\n"
        "v_cvt_pk_bf16_f32 v74, v108, v109\n"
        "v_cvt_pk_bf16_f32 v75, v110, v111\n"
        "v_mfma_f32_16x16x32_bf16 v[104:107], v[100:103], v[64:67], v[104:107]\n"
        "s_waitcnt lgkmcnt(6)\n"
        "ds_read_b128 v[100:103], v240 offset:6144\n"
        "global_load_dwordx4 v[4:7], v57, s[22:23] offset:0\n"
        "v_mfma_f32_16x16x32_bf16 v[120:123], v[96:99], v[64:67], 0\n"
        "s_waitcnt lgkmcnt(4)\n"
        "ds_read_b128 v[96:99], v241 offset:6144\n"
        "v_mfma_f32_16x16x32_bf16 v[120:123], v[92:95], v[64:67], v[120:123]\n"
        "s_waitcnt lgkmcnt(3)\n"
        "ds_read_b128 v[112:115], v240 offset:12288\n"
        "global_load_dwordx4 v[8:11], v57, s[22:23] offset:1024\n"
        "v_cvt_pk_bf16_f32 v92, v104, v105\n"
        "v_mfma_f32_16x16x32_bf16 v[120:123], v[80:83], v[64:67], v[120:123]\n"
        "ds_read_b128 v[108:111], v241 offset:12288\n"
        "v_cvt_pk_bf16_f32 v93, v106, v107\n"
        "v_mfma_f32_16x16x32_bf16 v[64:67], v[68:71], v[84:87], 0\n"
        "s_waitcnt lgkmcnt(3)\n"
        "ds_read_b128 v[116:119], v240 offset:64\n"
        "global_load_dwordx4 v[12:15], v57, s[22:23] offset:2048\n"
        "v_mfma_f32_16x16x32_bf16 v[76:79], v[68:71], v[88:91], 0\n"
        "ds_read_b128 v[104:107], v241 offset:64\n"
        "v_cvt_pk_bf16_f32 v94, v120, v121\n"
        "v_cvt_pk_bf16_f32 v95, v122, v123\n"
        "v_mfma_f32_16x16x32_bf16 v[80:83], v[68:71], v[100:103], 0\n"
        "s_waitcnt lgkmcnt(3)\n"
        "ds_read_b128 v[100:103], v240 offset:6208\n"
        "global_load_dwordx4 v[16:19], v57, s[22:23] offset:3072\n"
        "v_mfma_f32_16x16x32_bf16 v[84:87], v[68:71], v[96:99], 0\n"
        "ds_read_b128 v[120:123], v241 offset:6208\n"
        "v_mfma_f32_16x16x32_bf16 v[88:91], v[68:71], v[112:115], 0\n"
        "s_waitcnt lgkmcnt(3)\n"
        "ds_read_b128 v[124:127], v240 offset:12352\n"
        "s_add_u32 m0, s17, 0\n"
        "s_nop 0\n"
        "buffer_load_dwordx4 v29, s[8:11], s74 offen lds\n"
        "v_mfma_f32_16x16x32_bf16 v[96:99], v[68:71], v[108:111], 0\n"
        "ds_read_b128 v[112:115], v241 offset:12352\n"
        "v_mfma_f32_16x16x32_bf16 v[64:67], v[72:75], v[116:119], v[64:67]\n"
        "s_waitcnt lgkmcnt(3)\n"
        "ds_read_b128 v[108:111], v240 offset:128\n"
        "buffer_load_dwordx4 v30, s[8:11], s74 offen offset:1024 lds\n"
        "v_mfma_f32_16x16x32_bf16 v[76:79], v[72:75], v[104:107], v[76:79]\n"
        "ds_read_b128 v[116:119], v241 offset:128\n"
        "v_mfma_f32_16x16x32_bf16 v[80:83], v[72:75], v[100:103], v[80:83]\n"
        "s_waitcnt lgkmcnt(3)\n"
        "ds_read_b128 v[100:103], v240 offset:6272\n"
        "buffer_load_dwordx4 v31, s[8:11], s74 offen offset:2048 lds\n"
        "v_mfma_f32_16x16x32_bf16 v[84:87], v[72:75], v[120:123], v[84:87]\n"
        "ds_read_b128 v[68:71], v241 offset:6272\n"
        "v_mfma_f32_16x16x32_bf16 v[88:91], v[72:75], v[124:127], v[88:91]\n"
        "s_waitcnt lgkmcnt(4)\n"
        "ds_read_b128 v[104:107], v240 offset:12416\n"
        "s_add_u32 m0, s17, 3104\n"
        "s_nop 0\n"
        "buffer_load_dwordx4 v29, s[8:11], s75 offen lds\n"
        "v_mfma_f32_16x16x32_bf16 v[96:99], v[72:75], v[112:115], v[96:99]\n"
        "s_waitcnt lgkmcnt(3)\n"
        "ds_read_b128 v[72:75], v241 offset:12416\n"
        "v_mfma_f32_16x16x32_bf16 v[64:67], v[92:95], v[108:111], v[64:67]\n"
        "buffer_load_dwordx4 v30, s[8:11], s75 offen offset:1024 lds\n"
        "v_mfma_f32_16x16x32_bf16 v[76:79], v[92:95], v[116:119], v[76:79]\n"
        "s_waitcnt lgkmcnt(3)\n"
        "v_mov_b32 v239, s35\n"
        "s_mov_b64 exec, 1\n"
        "ds_inc_u32 v239, v239 offset:0\n"
        "s_mov_b64 exec, -1\n"
        "v_mfma_f32_16x16x32_bf16 v[80:83], v[92:95], v[100:103], v[80:83]\n"
        "s_waitcnt lgkmcnt(2)\n"
        "buffer_load_dwordx4 v31, s[8:11], s75 offen offset:2048 lds\n"
        "v_add_f32 v238, v64, v76\n"
        "v_mfma_f32_16x16x32_bf16 v[84:87], v[92:95], v[68:71], v[84:87]\n"
        "v_mfma_f32_16x16x32_bf16 v[88:91], v[92:95], v[104:107], v[88:91]\n"
        "s_waitcnt lgkmcnt(1)\n"
        "v_mfma_f32_16x16x32_bf16 v[96:99], v[92:95], v[72:75], v[96:99]\n"
        "v_mov_b32 v239, s37\n"
        "v_add_f32 v238, v238, v80\n"
        "s_nop 2\n"
        "v_add_f32 v238, v238, v84\n"
        "v_add_f32 v238, v238, v88\n"
        "s_nop 0\n"
        "v_add_f32 v238, v238, v96\n"
        "s_nop 1\n"
        "v_add_f32_dpp v238, v238, v238 row_ror:8 row_mask:0xf bank_mask:0xf\n"
        "s_nop 1\n"
        "v_add_f32_dpp v238, v238, v238 row_ror:4 row_mask:0xf bank_mask:0xf\n"
        "s_nop 1\n"
        "v_add_f32_dpp v238, v238, v238 row_ror:2 row_mask:0xf bank_mask:0xf\n"
        "s_nop 1\n"
        "v_add_f32_dpp v238, v238, v238 row_ror:1 row_mask:0xf bank_mask:0xf\n"
        "v_mul_f32 v238, 0xbc2aaaab, v238\n"
        "v_mul_f32 v238, s34, v238\n"
        "v_fma_f32 v64, v64, s34, v238\n"
        "v_fma_f32 v76, v76, s34, v238\n"
        "v_fma_f32 v80, v80, s34, v238\n"
        "v_fma_f32 v84, v84, s34, v238\n"
        "v_fma_f32 v88, v88, s34, v238\n"
        "v_fma_f32 v96, v96, s34, v238\n"
        "ds_write_b32 v244, v64\n"
        "ds_write_b32 v244, v76 offset:16\n"
        "ds_write_b32 v244, v80 offset:128\n"
        "ds_write_b32 v244, v84 offset:144\n"
        "ds_write_b32 v244, v88 offset:256\n"
        "ds_write_b32 v244, v96 offset:272\n"
        "s_mov_b64 exec, 1\n"
        "ds_inc_u32 v239, v239 offset:0\n"
        "s_mov_b64 exec, -1\n"
        "s_setpc_b64 s[30:31]\n"
        "FOLDM_%=:\n"
        "ds_read_b128 v[88:91], v242\n"
        "ds_read_b128 v[92:95], v240\n"
        "ds_read_b128 v[180:183], v242 offset:64\n"
        "ds_read_b128 v[172:175], v240 offset:64\n"
        "ds_read_b128 v[184:187], v242 offset:128\n"
        "ds_read_b128 v[164:167], v240 offset:128\n"
        "ds_read_b128 v[168:171], v241\n"
        "ds_read_b128 v[100:103], v241 offset:64\n"
        "ds_read_b128 v[104:107], v241 offset:128\n"
        "ds_read_b128 v[140:143], v240 offset:6144\n"
        "ds_read_b128 v[132:135], v240 offset:6208\n"
        "ds_read_b128 v[96:99], v240 offset:6272\n"
        "ds_read_b128 v[136:139], v241 offset:6144\n"
        "ds_read_b128 v[128:131], v241 offset:6208\n"
        "ds_read_b128 v[108:111], v241 offset:6272\n"
        "s_waitcnt lgkmcnt(13)\n"
        "v_mfma_f32_16x16x32_bf16 v[64:67], v[92:95], v[88:91], 0\n"
        "s_waitcnt lgkmcnt(11)\n"
        "ds_read_b128 v[152:155], v240 offset:12288\n"
        "v_mfma_f32_16x16x32_bf16 v[64:67], v[172:175], v[180:183], v[64:67]\n"
        "s_waitcnt lgkmcnt(8)\n"
        "ds_read_b128 v[160:163], v240 offset:12352\n"
        "v_mfma_f32_16x16x32_bf16 v[64:67], v[164:167], v[184:187], v[64:67]\n"
        "ds_read_b128 v[112:115], v240 offset:12416\n"
        "v_mfma_f32_16x16x32_bf16 v[68:71], v[168:171], v[88:91], 0\n"
        "ds_read_b128 v[116:119], v241 offset:12288\n"
        "v_mfma_f32_16x16x32_bf16 v[68:71], v[100:103], v[180:183], v[68:71]\n"
        "s_waitcnt lgkmcnt(4)\n"
        "ds_read_b128 v[120:123], v241 offset:12352\n"
        "v_mfma_f32_16x16x32_bf16 v[68:71], v[104:107], v[184:187], v[68:71]\n"
        "ds_read_b128 v[124:127], v241 offset:12416\n"
        "v_mfma_f32_16x16x32_bf16 v[72:75], v[140:143], v[88:91], 0\n"
        "ds_read_b128 v[144:147], v243\n"
        "v_mfma_f32_16x16x32_bf16 v[72:75], v[132:135], v[180:183], v[72:75]\n"
        "ds_read_b128 v[148:151], v243 offset:64\n"
        "v_mfma_f32_16x16x32_bf16 v[72:75], v[96:99], v[184:187], v[72:75]\n"
        "ds_read_b128 v[156:159], v243 offset:128\n"
        "v_mfma_f32_16x16x32_bf16 v[76:79], v[136:139], v[88:91], 0\n"
        "v_add_f32 v176, v64, v68\n"
        "v_add_f32 v177, v65, v69\n"
        "v_add_f32 v178, v66, v70\n"
        "v_add_f32 v179, v67, v71\n"
        "v_mfma_f32_16x16x32_bf16 v[76:79], v[128:131], v[180:183], v[76:79]\n"
        "v_mfma_f32_16x16x32_bf16 v[76:79], v[108:111], v[184:187], v[76:79]\n"
        "s_waitcnt lgkmcnt(3)\n"
        "v_add_f32 v176, v176, v72\n"
        "v_add_f32 v177, v177, v73\n"
        "v_add_f32 v178, v178, v74\n"
        "v_add_f32 v179, v179, v75\n"
        "v_mfma_f32_16x16x32_bf16 v[80:83], v[152:155], v[88:91], 0\n"
        "v_mfma_f32_16x16x32_bf16 v[80:83], v[160:163], v[180:183], v[80:83]\n"
        "v_mfma_f32_16x16x32_bf16 v[80:83], v[112:115], v[184:187], v[80:83]\n"
        "v_add_f32 v176, v176, v76\n"
        "v_add_f32 v177, v177, v77\n"
        "v_add_f32 v178, v178, v78\n"
        "v_add_f32 v179, v179, v79\n"
        "v_mfma_f32_16x16x32_bf16 v[84:87], v[116:119], v[88:91], 0\n"
        "v_mfma_f32_16x16x32_bf16 v[84:87], v[120:123], v[180:183], v[84:87]\n"
        "v_mfma_f32_16x16x32_bf16 v[84:87], v[124:127], v[184:187], v[84:87]\n"
        "s_waitcnt lgkmcnt(0)\n"
        "v_add_f32 v176, v176, v80\n"
        "v_add_f32 v177, v177, v81\n"
        "v_add_f32 v178, v178, v82\n"
        "v_add_f32 v179, v179, v83\n"
        "v_mov_b32 v239, s35\n"
        "s_mov_b64 exec, 1\n"
        "ds_inc_u32 v239, v239 offset:0\n"
        "s_mov_b64 exec, -1\n"
        "v_add_f32 v176, v176, v84\n"
        "v_add_f32 v177, v177, v85\n"
        "v_add_f32 v178, v178, v86\n"
        "v_add_f32 v179, v179, v87\n"
        "v_add_f32 v176, v176, v177\n"
        "v_add_f32 v178, v178, v179\n"
        "v_add_f32 v176, v176, v178\n"
        "v_mov_b32 v177, v176\n"
        "v_mfma_f32_16x16x32_bf16 v[88:91], v[92:95], v[144:147], 0\n"
        "v_mfma_f32_16x16x32_bf16 v[88:91], v[172:175], v[148:151], v[88:91]\n"
        "v_permlane32_swap_b32 v176, v177\n"
        "v_add_f32 v176, v176, v177\n"
        "v_mov_b32 v177, v176\n"
        "v_mfma_f32_16x16x32_bf16 v[88:91], v[164:167], v[156:159], v[88:91]\n"
        "v_mfma_f32_16x16x32_bf16 v[92:95], v[168:171], v[144:147], 0\n"
        "v_permlane16_swap_b32 v176, v177\n"
        "v_add_f32 v176, v176, v177\n"
        "v_mul_f32 v176, 0xbc2aaaab, v176\n"
        "v_mul_f32 v176, s34, v176\n"
        "v_fma_f32 v64, v64, s34, v176\n"
        "v_fma_f32 v65, v65, s34, v176\n"
        "v_fma_f32 v66, v66, s34, v176\n"
        "v_fma_f32 v67, v67, s34, v176\n"
        "v_fma_f32 v68, v68, s34, v176\n"
        "v_fma_f32 v69, v69, s34, v176\n"
        "v_fma_f32 v70, v70, s34, v176\n"
        "v_fma_f32 v71, v71, s34, v176\n"
        "v_fma_f32 v72, v72, s34, v176\n"
        "v_fma_f32 v73, v73, s34, v176\n"
        "v_fma_f32 v74, v74, s34, v176\n"
        "v_fma_f32 v75, v75, s34, v176\n"
        "v_fma_f32 v76, v76, s34, v176\n"
        "v_fma_f32 v77, v77, s34, v176\n"
        "v_fma_f32 v78, v78, s34, v176\n"
        "v_fma_f32 v79, v79, s34, v176\n"
        "v_fma_f32 v80, v80, s34, v176\n"
        "v_fma_f32 v81, v81, s34, v176\n"
        "v_fma_f32 v82, v82, s34, v176\n"
        "v_fma_f32 v83, v83, s34, v176\n"
        "v_fma_f32 v84, v84, s34, v176\n"
        "v_fma_f32 v85, v85, s34, v176\n"
        "v_fma_f32 v86, v86, s34, v176\n"
        "v_fma_f32 v87, v87, s34, v176\n"
        "v_mfma_f32_16x16x32_bf16 v[92:95], v[100:103], v[148:151], v[92:95]\n"
        "v_mfma_f32_16x16x32_bf16 v[92:95], v[104:107], v[156:159], v[92:95]\n"
        "v_mfma_f32_16x16x32_bf16 v[100:103], v[140:143], v[144:147], 0\n"
        "v_mfma_f32_16x16x32_bf16 v[100:103], v[132:135], v[148:151], v[100:103]\n"
        "v_mfma_f32_16x16x32_bf16 v[100:103], v[96:99], v[156:159], v[100:103]\n"
        "v_mfma_f32_16x16x32_bf16 v[104:107], v[136:139], v[144:147], 0\n"
        "v_mfma_f32_16x16x32_bf16 v[104:107], v[128:131], v[148:151], v[104:107]\n"
        "v_mfma_f32_16x16x32_bf16 v[104:107], v[108:111], v[156:159], v[104:107]\n"
        "v_mfma_f32_16x16x32_bf16 v[96:99], v[152:155], v[144:147], 0\n"
        "v_mfma_f32_16x16x32_bf16 v[96:99], v[160:163], v[148:151], v[96:99]\n"
        "v_add_f32 v128, v88, v92\n"
        "v_add_f32 v129, v89, v93\n"
        "v_add_f32 v130, v90, v94\n"
        "v_add_f32 v131, v91, v95\n"
        "v_add_f32 v128, v128, v100\n"
        "v_add_f32 v129, v129, v101\n"
        "v_add_f32 v130, v130, v102\n"
        "v_add_f32 v131, v131, v103\n"
        "v_add_f32 v128, v128, v104\n"
        "v_add_f32 v129, v129, v105\n"
        "v_add_f32 v130, v130, v106\n"
        "v_add_f32 v131, v131, v107\n"
        "v_mfma_f32_16x16x32_bf16 v[96:99], v[112:115], v[156:159], v[96:99]\n"
        "v_mfma_f32_16x16x32_bf16 v[108:111], v[116:119], v[144:147], 0\n"
        "v_mfma_f32_16x16x32_bf16 v[108:111], v[120:123], v[148:151], v[108:111]\n"
        "v_mfma_f32_16x16x32_bf16 v[108:111], v[124:127], v[156:159], v[108:111]\n"
        "v_mov_b32 v239, s37\n"
        "s_nop 3\n"
        "v_add_f32 v128, v128, v96\n"
        "v_add_f32 v129, v129, v97\n"
        "v_add_f32 v130, v130, v98\n"
        "v_add_f32 v131, v131, v99\n"
        "v_add_f32 v128, v128, v108\n"
        "v_add_f32 v129, v129, v109\n"
        "v_add_f32 v130, v130, v110\n"
        "v_add_f32 v131, v131, v111\n"
        "v_add_f32 v128, v128, v129\n"
        "v_add_f32 v130, v130, v131\n"
        "v_add_f32 v128, v128, v130\n"
        "v_mov_b32 v129, v128\n"
        "s_nop 1\n"
        "v_permlane32_swap_b32 v128, v129\n"
        "v_add_f32 v128, v128, v129\n"
        "v_mov_b32 v129, v128\n"
        "s_nop 1\n"
        "v_permlane16_swap_b32 v128, v129\n"
        "v_add_f32 v128, v128, v129\n"
        "v_mul_f32 v128, 0xbc2aaaab, v128\n"
        "v_mul_f32 v128, s34, v128\n"
        "v_fma_f32 v88, v88, s34, v128\n"
        "v_fma_f32 v89, v89, s34, v128\n"
        "v_fma_f32 v90, v90, s34, v128\n"
        "v_fma_f32 v91, v91, s34, v128\n"
        "v_fma_f32 v92, v92, s34, v128\n"
        "v_fma_f32 v93, v93, s34, v128\n"
        "v_fma_f32 v94, v94, s34, v128\n"
        "v_fma_f32 v95, v95, s34, v128\n"
        "v_fma_f32 v100, v100, s34, v128\n"
        "v_fma_f32 v101, v101, s34, v128\n"
        "v_fma_f32 v102, v102, s34, v128\n"
        "v_fma_f32 v103, v103, s34, v128\n"
        "v_fma_f32 v104, v104, s34, v128\n"
        "v_fma_f32 v105, v105, s34, v128\n"
        "v_fma_f32 v106, v106, s34, v128\n"
        "v_fma_f32 v107, v107, s34, v128\n"
        "v_fma_f32 v96, v96, s34, v128\n"
        "v_fma_f32 v97, v97, s34, v128\n"
        "v_fma_f32 v98, v98, s34, v128\n"
        "v_fma_f32 v99, v99, s34, v128\n"
        "v_fma_f32 v108, v108, s34, v128\n"
        "v_fma_f32 v109, v109, s34, v128\n"
        "v_fma_f32 v110, v110, s34, v128\n"
        "v_fma_f32 v111, v111, s34, v128\n"
        "v_mov_b32 v237, s36\n"
        "L68_%=:\n"
        "ds_read_b32 v238, v237 offset:0\n"
        "s_waitcnt lgkmcnt(0)\n"
        "v_readfirstlane_b32 s40, v238\n"
        "s_nop 3\n"
        "s_cmp_ge_i32 s40, 4\n"
        "s_cbranch_scc1 L69_%=\n"
        "s_nop 0\n"
        "s_sub_u32 s41, s41, 1\n"
        "s_cbranch_scc0 L68_%=\n"
        "s_endpgm\n"
        "L69_%=:\n"
        "ds_read_b128 v[132:135], v246\n"
        "ds_read_b128 v[128:131], v246 offset:16\n"
        "ds_read_b128 v[124:127], v246 offset:128\n"
        "ds_read_b128 v[120:123], v246 offset:144\n"
        "ds_read_b128 v[116:119], v246 offset:256\n"
        "ds_read_b128 v[112:115], v246 offset:272\n"
        "s_waitcnt lgkmcnt(5)\n"
        "v_add_f32 v64, v64, v132\n"
        "v_add_f32 v65, v65, v133\n"
        "v_add_f32 v66, v66, v134\n"
        "v_add_f32 v67, v67, v135\n"
        "s_waitcnt lgkmcnt(0)\n"
        "v_add_f32 v68, v68, v128\n"
        "v_add_f32 v69, v69, v129\n"
        "v_add_f32 v70, v70, v130\n"
        "v_add_f32 v71, v71, v131\n"
        "v_add_f32 v88, v88, v132\n"
        "v_add_f32 v89, v89, v133\n"
        "v_add_f32 v90, v90, v134\n"
        "v_add_f32 v91, v91, v135\n"
        "v_add_f32 v92, v92, v128\n"
        "v_add_f32 v93, v93, v129\n"
        "v_add_f32 v94, v94, v130\n"
        "v_add_f32 v95, v95, v131\n"
        "v_add_f32 v72, v72, v124\n"
        "v_add_f32 v73, v73, v125\n"
        "v_add_f32 v74, v74, v126\n"
        "v_add_f32 v75, v75, v127\n"
        "v_add_f32 v76, v76, v120\n"
        "v_add_f32 v77, v77, v121\n"
        "v_add_f32 v78, v78, v122\n"
        "v_add_f32 v79, v79, v123\n"
        "v_add_f32 v100, v100, v124\n"
        "v_add_f32 v101, v101, v125\n"
        "v_add_f32 v102, v102, v126\n"
        "v_add_f32 v103, v103, v127\n"
        "v_add_f32 v104, v104, v120\n"
        "v_add_f32 v105, v105, v121\n"
        "v_add_f32 v106, v106, v122\n"
        "v_add_f32 v107, v107, v123\n"
        "v_add_f32 v80, v80, v116\n"
        "v_add_f32 v81, v81, v117\n"
        "v_add_f32 v82, v82, v118\n"
        "v_add_f32 v83, v83, v119\n"
        "v_add_f32 v84, v84, v112\n"
        "v_add_f32 v85, v85, v113\n"
        "v_add_f32 v86, v86, v114\n"
        "v_add_f32 v87, v87, v115\n"
        "v_add_f32 v96, v96, v116\n"
        "v_add_f32 v97, v97, v117\n"
        "v_add_f32 v98, v98, v118\n"
        "v_add_f32 v99, v99, v119\n"
        "v_add_f32 v108, v108, v112\n"
        "v_add_f32 v109, v109, v113\n"
        "v_add_f32 v110, v110, v114\n"
        "v_add_f32 v111, v111, v115\n"
        "v_cvt_pk_bf16_f32 v112, v64, v65\n"
        "v_cvt_pk_bf16_f32 v113, v66, v67\n"
        "v_cvt_pk_bf16_f32 v114, v68, v69\n"
        "v_cvt_pk_bf16_f32 v115, v70, v71\n"
        "v_cvt_pk_bf16_f32 v64, v88, v89\n"
        "v_cvt_pk_bf16_f32 v65, v90, v91\n"
        "v_cvt_pk_bf16_f32 v66, v92, v93\n"
        "v_cvt_pk_bf16_f32 v67, v94, v95\n"
        "v_cvt_pk_bf16_f32 v68, v72, v73\n"
        "v_cvt_pk_bf16_f32 v69, v74, v75\n"
        "v_cvt_pk_bf16_f32 v70, v76, v77\n"
        "v_cvt_pk_bf16_f32 v71, v78, v79\n"
        "v_cvt_pk_bf16_f32 v72, v100, v101\n"
        "v_cvt_pk_bf16_f32 v73, v102, v103\n"
        "v_cvt_pk_bf16_f32 v74, v104, v105\n"
        "v_cvt_pk_bf16_f32 v75, v106, v107\n"
        "v_cvt_pk_bf16_f32 v76, v80, v81\n"
        "v_cvt_pk_bf16_f32 v77, v82, v83\n"
        "v_cvt_pk_bf16_f32 v78, v84, v85\n"
        "v_cvt_pk_bf16_f32 v79, v86, v87\n"
        "v_cvt_pk_bf16_f32 v80, v96, v97\n"
        "v_cvt_pk_bf16_f32 v81, v98, v99\n"
        "v_cvt_pk_bf16_f32 v82, v108, v109\n"
        "v_cvt_pk_bf16_f32 v83, v110, v111\n"
        "ds_write_b128 v244, v[112:115]\n"
        "ds_write_b128 v245, v[64:67]\n"
        "ds_write_b128 v244, v[68:71] offset:1024\n"
        "ds_write_b128 v245, v[72:75] offset:1024\n"
        "ds_write_b128 v244, v[76:79] offset:2048\n"
        "ds_write_b128 v245, v[80:83] offset:2048\n"
        "s_mov_b64 exec, 1\n"
        "ds_inc_u32 v239, v239 offset:0\n"
        "s_mov_b64 exec, -1\n"
        "s_setpc_b64 s[30:31]\n"
        "FOLDR_%=:\n"
        "ds_read_b64_tr_b16 v[84:85], v242 offset:0\n"
        "ds_read_b64_tr_b16 v[86:87], v243 offset:0\n"
        "ds_read_b64_tr_b16 v[92:93], v242 offset:6144\n"
        "ds_read_b64_tr_b16 v[94:95], v243 offset:6144\n"
        "ds_read_b64_tr_b16 v[80:81], v242 offset:12288\n"
        "ds_read_b64_tr_b16 v[82:83], v243 offset:12288\n"
        "ds_read_b64_tr_b16 v[76:77], v242 offset:8\n"
        "ds_read_b64_tr_b16 v[78:79], v243 offset:8\n"
        "ds_read_b64_tr_b16 v[68:69], v242 offset:6152\n"
        "ds_read_b64_tr_b16 v[70:71], v243 offset:6152\n"
        "ds_read_b64_tr_b16 v[72:73], v242 offset:12296\n"
        "ds_read_b64_tr_b16 v[74:75], v243 offset:12296\n"
        "v_mov_b32 v64, 0x3f803f80\n"
        "v_mov_b32 v65, 0x3f803f80\n"
        "v_mov_b32 v66, 0x3f803f80\n"
        "v_mov_b32 v67, 0x3f803f80\n"
        "ds_read_b64_tr_b16 v[88:89], v242 offset:64\n"
        "ds_read_b64_tr_b16 v[90:91], v243 offset:64\n"
        "s_waitcnt lgkmcnt(12)\n"
        "v_mfma_f32_16x16x32_bf16 v[104:107], v[84:87], v[64:67], 0\n"
        "s_waitcnt lgkmcnt(10)\n"
        "v_mfma_f32_16x16x32_bf16 v[104:107], v[92:95], v[64:67], v[104:107]\n"
        "s_waitcnt lgkmcnt(6)\n"
        "ds_read_b64_tr_b16 v[96:97], v242 offset:6208\n"
        "ds_read_b64_tr_b16 v[98:99], v243 offset:6208\n"
        "v_mfma_f32_16x16x32_bf16 v[104:107], v[80:83], v[64:67], v[104:107]\n"
        "ds_read_b64_tr_b16 v[92:93], v242 offset:12352\n"
        "ds_read_b64_tr_b16 v[94:95], v243 offset:12352\n"
        "v_mfma_f32_16x16x32_bf16 v[100:103], v[76:79], v[64:67], 0\n"
        "s_waitcnt lgkmcnt(6)\n"
        "ds_read_b64_tr_b16 v[80:81], v242 offset:72\n"
        "ds_read_b64_tr_b16 v[82:83], v243 offset:72\n"
        "v_mfma_f32_16x16x32_bf16 v[100:103], v[68:71], v[64:67], v[100:103]\n"
        "ds_read_b64_tr_b16 v[76:77], v242 offset:6216\n"
        "ds_read_b64_tr_b16 v[78:79], v243 offset:6216\n"
        "v_cvt_pk_bf16_f32 v68, v104, v105\n"
        "v_cvt_pk_bf16_f32 v69, v106, v107\n"
        "v_mfma_f32_16x16x32_bf16 v[100:103], v[72:75], v[64:67], v[100:103]\n"
        "s_waitcnt lgkmcnt(8)\n"
        "ds_read_b64_tr_b16 v[84:85], v242 offset:12360\n"
        "ds_read_b64_tr_b16 v[86:87], v243 offset:12360\n"
        "v_mfma_f32_16x16x32_bf16 v[104:107], v[88:91], v[64:67], 0\n"
        "ds_read_b64_tr_b16 v[72:73], v242 offset:128\n"
        "ds_read_b64_tr_b16 v[74:75], v243 offset:128\n"
        "s_waitcnt lgkmcnt(10)\n"
        "v_mfma_f32_16x16x32_bf16 v[104:107], v[96:99], v[64:67], v[104:107]\n"
        "ds_read_b64_tr_b16 v[88:89], v242 offset:6272\n"
        "ds_read_b64_tr_b16 v[90:91], v243 offset:6272\n"
        "v_cvt_pk_bf16_f32 v70, v100, v101\n"
        "v_cvt_pk_bf16_f32 v71, v102, v103\n"
        "s_waitcnt lgkmcnt(9)\n"
        "v_mfma_f32_16x16x32_bf16 v[104:107], v[92:95], v[64:67], v[104:107]\n"
        "s_waitcnt lgkmcnt(8)\n"
        "ds_read_b64_tr_b16 v[100:101], v242 offset:12416\n"
        "ds_read_b64_tr_b16 v[102:103], v243 offset:12416\n"
        "v_mfma_f32_16x16x32_bf16 v[108:111], v[80:83], v[64:67], 0\n"
        "s_waitcnt lgkmcnt(8)\n"
        "ds_read_b64_tr_b16 v[92:93], v242 offset:136\n"
        "ds_read_b64_tr_b16 v[94:95], v243 offset:136\n"
        "v_mfma_f32_16x16x32_bf16 v[108:111], v[76:79], v[64:67], v[108:111]\n"
        "ds_read_b64_tr_b16 v[96:97], v242 offset:6280\n"
        "ds_read_b64_tr_b16 v[98:99], v243 offset:6280\n"
        "v_cvt_pk_bf16_f32 v80, v104, v105\n"
        "v_cvt_pk_bf16_f32 v81, v106, v107\n"
        "s_waitcnt lgkmcnt(8)\n"
        "v_mfma_f32_16x16x32_bf16 v[108:111], v[84:87], v[64:67], v[108:111]\n"
        "ds_read_b64_tr_b16 v[76:77], v242 offset:12424\n"
        "ds_read_b64_tr_b16 v[78:79], v243 offset:12424\n"
        "v_mfma_f32_16x16x32_bf16 v[116:119], v[72:75], v[64:67], 0\n"
        "s_waitcnt lgkmcnt(8)\n"
        "ds_read_b128 v[72:75], v240\n"
        "v_mfma_f32_16x16x32_bf16 v[116:119], v[88:91], v[64:67], v[116:119]\n"
        "ds_read_b128 v[104:107], v241\n"
        "s_waitcnt lgkmcnt(8)\n"
        "v_mfma_f32_16x16x32_bf16 v[116:119], v[100:103], v[64:67], v[116:119]\n"
        "ds_read_b128 v[84:87], v240 offset:6144\n"
        "v_cvt_pk_bf16_f32 v82, v108, v109\n"
        "v_cvt_pk_bf16_f32 v83, v110, v111\n"
        "s_waitcnt lgkmcnt(7)\n"
        "v_mfma_f32_16x16x32_bf16 v[112:115], v[92:95], v[64:67], 0\n"
        "s_waitcnt lgkmcnt(5)\n"
        "ds_read_b128 v[92:95], v241 offset:6144\n"
        "v_mfma_f32_16x16x32_bf16 v[112:115], v[96:99], v[64:67], v[112:115]\n"
        "ds_read_b128 v[100:103], v240 offset:12288\n"
        "v_cvt_pk_bf16_f32 v88, v116, v117\n"
        "v_cvt_pk_bf16_f32 v89, v118, v119\n"
        "s_waitcnt lgkmcnt(5)\n"
        "v_mfma_f32_16x16x32_bf16 v[112:115], v[76:79], v[64:67], v[112:115]\n"
        "s_waitcnt lgkmcnt(4)\n"
        "ds_read_b128 v[116:119], v241 offset:12288\n"
        "v_mfma_f32_16x16x32_bf16 v[64:67], v[68:71], v[72:75], 0\n"
        "s_waitcnt lgkmcnt(4)\n"
        "ds_read_b128 v[120:123], v240 offset:64\n"
        "v_mfma_f32_16x16x32_bf16 v[72:75], v[68:71], v[104:107], 0\n"
        "ds_read_b128 v[108:111], v241 offset:64\n"
        "s_waitcnt lgkmcnt(5)\n"
        "v_mfma_f32_16x16x32_bf16 v[76:79], v[68:71], v[84:87], 0\n"
        "ds_read_b128 v[104:107], v240 offset:6208\n"
        "v_cvt_pk_bf16_f32 v90, v112, v113\n"
        "v_cvt_pk_bf16_f32 v91, v114, v115\n"
        "s_waitcnt lgkmcnt(5)\n"
        "v_mfma_f32_16x16x32_bf16 v[84:87], v[68:71], v[92:95], 0\n"
        "ds_read_b128 v[124:127], v241 offset:6208\n"
        "s_waitcnt lgkmcnt(5)\n"
        "v_mfma_f32_16x16x32_bf16 v[92:95], v[68:71], v[100:103], 0\n"
        "ds_read_b128 v[112:115], v240 offset:12352\n"
        "s_waitcnt lgkmcnt(5)\n"
        "v_mfma_f32_16x16x32_bf16 v[96:99], v[68:71], v[116:119], 0\n"
        "ds_read_b128 v[100:103], v241 offset:12352\n"
        "s_waitcnt lgkmcnt(5)\n"
        "v_mfma_f32_16x16x32_bf16 v[64:67], v[80:83], v[120:123], v[64:67]\n"
        "ds_read_b128 v[68:71], v240 offset:128\n"
        "s_waitcnt lgkmcnt(5)\n"
        "v_mfma_f32_16x16x32_bf16 v[72:75], v[80:83], v[108:111], v[72:75]\n"
        "ds_read_b128 v[116:119], v241 offset:128\n"
        "s_waitcnt lgkmcnt(5)\n"
        "v_mfma_f32_16x16x32_bf16 v[76:79], v[80:83], v[104:107], v[76:79]\n"
        "ds_read_b128 v[108:111], v240 offset:6272\n"
        "s_waitcnt lgkmcnt(5)\n"
        "v_mfma_f32_16x16x32_bf16 v[84:87], v[80:83], v[124:127], v[84:87]\n"
        "ds_read_b128 v[104:107], v241 offset:6272\n"
        "s_waitcnt lgkmcnt(5)\n"
        "v_mfma_f32_16x16x32_bf16 v[92:95], v[80:83], v[112:115], v[92:95]\n"
        "ds_read_b128 v[120:123], v240 offset:12416\n"
        "s_waitcnt lgkmcnt(5)\n"
        "v_mfma_f32_16x16x32_bf16 v[96:99], v[80:83], v[100:103], v[96:99]\n"
        "ds_read_b128 v[112:115], v241 offset:12416\n"
        "s_waitcnt lgkmcnt(5)\n"
        "v_mfma_f32_16x16x32_bf16 v[64:67], v[88:91], v[68:71], v[64:67]\n"
        "s_waitcnt lgkmcnt(4)\n"
        "v_mfma_f32_16x16x32_bf16 v[72:75], v[88:91], v[116:119], v[72:75]\n"
        "v_mov_b32 v239, s35\n"
        "s_waitcnt lgkmcnt(3)\n"
        "v_mfma_f32_16x16x32_bf16 v[76:79], v[88:91], v[108:111], v[76:79]\n"
        "s_mov_b64 exec, 1\n"
        "ds_inc_u32 v239, v239 offset:0\n"
        "s_mov_b64 exec, -1\n"
        "s_waitcnt lgkmcnt(3)\n"
        "v_mfma_f32_16x16x32_bf16 v[84:87], v[88:91], v[104:107], v[84:87]\n"
        "v_add_f32 v238, v64, v72\n"
        "s_waitcnt lgkmcnt(2)\n"
        "v_mfma_f32_16x16x32_bf16 v[92:95], v[88:91], v[120:123], v[92:95]\n"
        "v_add_f32 v238, v238, v76\n"
        "s_waitcnt lgkmcnt(1)\n"
        "v_mfma_f32_16x16x32_bf16 v[96:99], v[88:91], v[112:115], v[96:99]\n"
        "v_mov_b32 v239, s37\n"
        "s_nop 0\n"
        "v_add_f32 v238, v238, v84\n"
        "s_nop 1\n"
        "v_add_f32 v238, v238, v92\n"
        "s_nop 1\n"
        "v_add_f32 v238, v238, v96\n"
        "s_nop 1\n"
        "v_add_f32_dpp v238, v238, v238 row_ror:8 row_mask:0xf bank_mask:0xf\n"
        "s_nop 1\n"
        "v_add_f32_dpp v238, v238, v238 row_ror:4 row_mask:0xf bank_mask:0xf\n"
        "s_nop 1\n"
        "v_add_f32_dpp v238, v238, v238 row_ror:2 row_mask:0xf bank_mask:0xf\n"
        "s_nop 1\n"
        "v_add_f32_dpp v238, v238, v238 row_ror:1 row_mask:0xf bank_mask:0xf\n"
        "v_mul_f32 v238, 0xbc2aaaab, v238\n"
        "v_mul_f32 v238, s34, v238\n"
        "v_fma_f32 v64, v64, s34, v238\n"
        "v_fma_f32 v72, v72, s34, v238\n"
        "v_fma_f32 v76, v76, s34, v238\n"
        "v_fma_f32 v84, v84, s34, v238\n"
        "v_fma_f32 v92, v92, s34, v238\n"
        "v_fma_f32 v96, v96, s34, v238\n"
        "ds_write_b32 v244, v64\n"
        "ds_write_b32 v244, v72 offset:16\n"
        "ds_write_b32 v244, v76 offset:128\n"
        "ds_write_b32 v244, v84 offset:144\n"
        "ds_write_b32 v244, v92 offset:256\n"
        "ds_write_b32 v244, v96 offset:272\n"
        "s_mov_b64 exec, 1\n"
        "ds_inc_u32 v239, v239 offset:0\n"
        "s_mov_b64 exec, -1\n"
        "s_setpc_b64 s[30:31]\n"
        "FOLDW_%=:\n"
        "ds_read_b64_tr_b16 v[68:69], v242 offset:0\n"
        "ds_read_b64_tr_b16 v[70:71], v243 offset:0\n"
        "ds_read_b128 v[172:175], v240\n"
        "ds_read_b64_tr_b16 v[180:181], v242 offset:6144\n"
        "ds_read_b64_tr_b16 v[182:183], v243 offset:6144\n"
        "ds_read_b128 v[120:123], v240 offset:64\n"
        "ds_read_b64_tr_b16 v[184:185], v242 offset:12288\n"
        "ds_read_b64_tr_b16 v[186:187], v243 offset:12288\n"
        "ds_read_b128 v[168:171], v240 offset:128\n"
        "ds_read_b128 v[160:163], v241\n"
        "ds_read_b128 v[148:151], v241 offset:64\n"
        "ds_read_b128 v[132:135], v241 offset:128\n"
        "ds_read_b128 v[124:127], v240 offset:6144\n"
        "ds_read_b128 v[104:107], v240 offset:6208\n"
        "ds_read_b128 v[92:95], v240 offset:6272\n"
        "ds_read_b128 v[96:99], v241 offset:6144\n"
        "ds_read_b128 v[88:91], v241 offset:6208\n"
        "ds_read_b128 v[84:87], v241 offset:6272\n"
        "s_waitcnt lgkmcnt(15)\n"
        "v_mfma_f32_16x16x32_bf16 v[136:139], v[172:175], v[68:71], 0\n"
        "ds_read_b128 v[100:103], v240 offset:12288\n"
        "s_waitcnt lgkmcnt(12)\n"
        "v_mfma_f32_16x16x32_bf16 v[136:139], v[120:123], v[180:183], v[136:139]\n"
        "s_waitcnt lgkmcnt(10)\n"
        "ds_read_b128 v[108:111], v240 offset:12352\n"
        "v_mfma_f32_16x16x32_bf16 v[136:139], v[168:171], v[184:187], v[136:139]\n"
        "s_waitcnt lgkmcnt(7)\n"
        "ds_read_b128 v[64:67], v240 offset:12416\n"
        "v_mfma_f32_16x16x32_bf16 v[140:143], v[160:163], v[68:71], 0\n"
        "ds_read_b128 v[72:75], v241 offset:12288\n"
        "v_mfma_f32_16x16x32_bf16 v[140:143], v[148:151], v[180:183], v[140:143]\n"
        "ds_read_b128 v[76:79], v241 offset:12352\n"
        "v_mfma_f32_16x16x32_bf16 v[140:143], v[132:135], v[184:187], v[140:143]\n"
        "ds_read_b128 v[80:83], v241 offset:12416\n"
        "v_mfma_f32_16x16x32_bf16 v[144:147], v[124:127], v[68:71], 0\n"
        "s_waitcnt lgkmcnt(6)\n"
        "ds_read_b64_tr_b16 v[112:113], v242 offset:8\n"
        "ds_read_b64_tr_b16 v[114:115], v243 offset:8\n"
        "v_mfma_f32_16x16x32_bf16 v[144:147], v[104:107], v[180:183], v[144:147]\n"
        "ds_read_b64_tr_b16 v[116:117], v242 offset:6152\n"
        "ds_read_b64_tr_b16 v[118:119], v243 offset:6152\n"
        "v_add_f32 v164, v136, v140\n"
        "v_add_f32 v165, v137, v141\n"
        "v_add_f32 v166, v138, v142\n"
        "v_add_f32 v167, v139, v143\n"
        "v_mfma_f32_16x16x32_bf16 v[144:147], v[92:95], v[184:187], v[144:147]\n"
        "ds_read_b64_tr_b16 v[128:129], v242 offset:12296\n"
        "ds_read_b64_tr_b16 v[130:131], v243 offset:12296\n"
        "v_mfma_f32_16x16x32_bf16 v[152:155], v[96:99], v[68:71], 0\n"
        "v_mfma_f32_16x16x32_bf16 v[152:155], v[88:91], v[180:183], v[152:155]\n"
        "v_mfma_f32_16x16x32_bf16 v[152:155], v[84:87], v[184:187], v[152:155]\n"
        "s_waitcnt lgkmcnt(6)\n"
        "v_mfma_f32_16x16x32_bf16 v[156:159], v[100:103], v[68:71], 0\n"
        "v_mfma_f32_16x16x32_bf16 v[156:159], v[108:111], v[180:183], v[156:159]\n"
        "v_add_f32 v164, v164, v144\n"
        "v_add_f32 v165, v165, v145\n"
        "v_add_f32 v166, v166, v146\n"
        "v_add_f32 v167, v167, v147\n"
        "v_mfma_f32_16x16x32_bf16 v[156:159], v[64:67], v[184:187], v[156:159]\n"
        "v_add_f32 v164, v164, v152\n"
        "v_add_f32 v165, v165, v153\n"
        "v_add_f32 v166, v166, v154\n"
        "v_add_f32 v167, v167, v155\n"
        "v_mfma_f32_16x16x32_bf16 v[176:179], v[72:75], v[68:71], 0\n"
        "v_mfma_f32_16x16x32_bf16 v[176:179], v[76:79], v[180:183], v[176:179]\n"
        "v_mfma_f32_16x16x32_bf16 v[176:179], v[80:83], v[184:187], v[176:179]\n"
        "s_waitcnt lgkmcnt(0)\n"
        "v_mov_b32 v239, s35\n"
        "v_add_f32 v164, v164, v156\n"
        "v_add_f32 v165, v165, v157\n"
        "v_add_f32 v166, v166, v158\n"
        "v_add_f32 v167, v167, v159\n"
        "s_mov_b64 exec, 1\n"
        "ds_inc_u32 v239, v239 offset:0\n"
        "s_mov_b64 exec, -1\n"
        "v_add_f32 v164, v164, v176\n"
        "v_add_f32 v165, v165, v177\n"
        "v_add_f32 v166, v166, v178\n"
        "v_mfma_f32_16x16x32_bf16 v[68:71], v[172:175], v[112:115], 0\n"
        "v_add_f32 v167, v167, v179\n"
        "v_add_f32 v164, v164, v165\n"
        "v_add_f32 v166, v166, v167\n"
        "v_add_f32 v164, v164, v166\n"
        "v_mov_b32 v165, v164\n"
        "v_mfma_f32_16x16x32_bf16 v[68:71], v[120:123], v[116:119], v[68:71]\n"
        "v_mfma_f32_16x16x32_bf16 v[68:71], v[168:171], v[128:131], v[68:71]\n"
        "v_permlane32_swap_b32 v164, v165\n"
        "v_add_f32 v164, v164, v165\n"
        "v_mov_b32 v165, v164\n"
        "v_mfma_f32_16x16x32_bf16 v[120:123], v[160:163], v[112:115], 0\n"
        "global_load_dwordx4 v[40:43], v57, s[22:23] offset:-3072\n"
        "v_permlane16_swap_b32 v164, v165\n"
        "v_add_f32 v164, v164, v165\n"
        "v_mul_f32 v164, 0xbc2aaaab, v164\n"
        "v_mul_f32 v164, s34, v164\n"
        "v_fma_f32 v136, v136, s34, v164\n"
        "v_fma_f32 v137, v137, s34, v164\n"
        "v_fma_f32 v138, v138, s34, v164\n"
        "v_fma_f32 v139, v139, s34, v164\n"
        "v_fma_f32 v140, v140, s34, v164\n"
        "v_mfma_f32_16x16x32_bf16 v[120:123], v[148:151], v[116:119], v[120:123]\n"
        "v_fma_f32 v141, v141, s34, v164\n"
        "v_fma_f32 v142, v142, s34, v164\n"
        "v_fma_f32 v143, v143, s34, v164\n"
        "v_fma_f32 v144, v144, s34, v164\n"
        "v_fma_f32 v145, v145, s34, v164\n"
        "v_fma_f32 v146, v146, s34, v164\n"
        "v_fma_f32 v147, v147, s34, v164\n"
        "v_fma_f32 v152, v152, s34, v164\n"
        "v_fma_f32 v153, v153, s34, v164\n"
        "v_fma_f32 v154, v154, s34, v164\n"
        "v_fma_f32 v155, v155, s34, v164\n"
        "v_fma_f32 v156, v156, s34, v164\n"
        "v_fma_f32 v157, v157, s34, v164\n"
        "v_fma_f32 v158, v158, s34, v164\n"
        "v_mfma_f32_16x16x32_bf16 v[120:123], v[132:135], v[128:131], v[120:123]\n"
        "global_load_dwordx4 v[44:47], v57, s[22:23] offset:-2048\n"
        "v_fma_f32 v159, v159, s34, v164\n"
        "v_fma_f32 v176, v176, s34, v164\n"
        "v_fma_f32 v177, v177, s34, v164\n"
        "v_fma_f32 v178, v178, s34, v164\n"
        "v_fma_f32 v179, v179, s34, v164\n"
        "v_cvt_pk_bf16_f32 v132, v136, v137\n"
        "v_cvt_pk_bf16_f32 v133, v138, v139\n"
        "v_add_f32 v160, v68, v120\n"
        "v_add_f32 v161, v69, v121\n"
        "v_add_f32 v162, v70, v122\n"
        "v_add_f32 v163, v71, v123\n"
        "v_cvt_pk_bf16_f32 v134, v140, v141\n"
        "v_mfma_f32_16x16x32_bf16 v[148:151], v[124:127], v[112:115], 0\n"
        "v_cvt_pk_bf16_f32 v135, v142, v143\n"
        "v_cvt_pk_bf16_f32 v124, v144, v145\n"
        "v_cvt_pk_bf16_f32 v125, v146, v147\n"
        "v_cvt_pk_bf16_f32 v126, v152, v153\n"
        "v_cvt_pk_bf16_f32 v127, v154, v155\n"
        "v_cvt_pk_bf16_f32 v136, v156, v157\n"
        "v_cvt_pk_bf16_f32 v137, v158, v159\n"
        "v_cvt_pk_bf16_f32 v138, v176, v177\n"
        "v_cvt_pk_bf16_f32 v139, v178, v179\n"
        "v_mfma_f32_16x16x32_bf16 v[148:151], v[104:107], v[116:119], v[148:151]\n"
        "global_load_dwordx4 v[0:3], v57, s[22:23] offset:-1024\n"
        "v_mfma_f32_16x16x32_bf16 v[148:151], v[92:95], v[128:131], v[148:151]\n"
        "v_mfma_f32_16x16x32_bf16 v[104:107], v[96:99], v[112:115], 0\n"
        "global_load_dwordx4 v[4:7], v57, s[22:23] offset:0\n"
        "v_mfma_f32_16x16x32_bf16 v[104:107], v[88:91], v[116:119], v[104:107]\n"
        "v_mfma_f32_16x16x32_bf16 v[104:107], v[84:87], v[128:131], v[104:107]\n"
        "global_load_dwordx4 v[8:11], v57, s[22:23] offset:1024\n"
        "v_mfma_f32_16x16x32_bf16 v[84:87], v[100:103], v[112:115], 0\n"
        "v_mfma_f32_16x16x32_bf16 v[84:87], v[108:111], v[116:119], v[84:87]\n"
        "global_load_dwordx4 v[12:15], v57, s[22:23] offset:2048\n"
        "v_add_f32 v160, v160, v148\n"
        "v_add_f32 v161, v161, v149\n"
        "v_add_f32 v162, v162, v150\n"
        "v_add_f32 v163, v163, v151\n"
        "v_add_f32 v160, v160, v104\n"
        "v_add_f32 v161, v161, v105\n"
        "v_add_f32 v162, v162, v106\n"
        "v_add_f32 v163, v163, v107\n"
        "v_mfma_f32_16x16x32_bf16 v[84:87], v[64:67], v[128:131], v[84:87]\n"
        "v_mfma_f32_16x16x32_bf16 v[88:91], v[72:75], v[112:115], 0\n"
        "global_load_dwordx4 v[16:19], v57, s[22:23] offset:3072\n"
        "v_mfma_f32_16x16x32_bf16 v[88:91], v[76:79], v[116:119], v[88:91]\n"
        "v_mfma_f32_16x16x32_bf16 v[88:91], v[80:83], v[128:131], v[88:91]\n"
        "v_mov_b32 v239, s37\n"
        "s_nop 2\n"
        "v_add_f32 v160, v160, v84\n"
        "v_add_f32 v161, v161, v85\n"
        "v_add_f32 v162, v162, v86\n"
        "v_add_f32 v163, v163, v87\n"
        "v_add_f32 v160, v160, v88\n"
        "v_add_f32 v161, v161, v89\n"
        "v_add_f32 v162, v162, v90\n"
        "v_add_f32 v163, v163, v91\n"
        "v_add_f32 v160, v160, v161\n"
        "v_add_f32 v162, v162, v163\n"
        "v_add_f32 v160, v160, v162\n"
        "v_mov_b32 v161, v160\n"
        "s_nop 1\n"
        "v_permlane32_swap_b32 v160, v161\n"
        "v_add_f32 v160, v160, v161\n"
        "v_mov_b32 v161, v160\n"
        "s_nop 1\n"
        "v_permlane16_swap_b32 v160, v161\n"
        "v_add_f32 v160, v160, v161\n"
        "v_mul_f32 v160, 0xbc2aaaab, v160\n"
        "v_mul_f32 v160, s34, v160\n"
        "v_fma_f32 v68, v68, s34, v160\n"
        "v_fma_f32 v69, v69, s34, v160\n"
        "v_fma_f32 v70, v70, s34, v160\n"
        "v_fma_f32 v71, v71, s34, v160\n"
        "v_fma_f32 v120, v120, s34, v160\n"
        "v_fma_f32 v121, v121, s34, v160\n"
        "v_fma_f32 v122, v122, s34, v160\n"
        "v_fma_f32 v123, v123, s34, v160\n"
        "v_fma_f32 v148, v148, s34, v160\n"
        "v_fma_f32 v149, v149, s34, v160\n"
        "v_fma_f32 v150, v150, s34, v160\n"
        "v_fma_f32 v151, v151, s34, v160\n"
        "v_fma_f32 v104, v104, s34, v160\n"
        "v_fma_f32 v105, v105, s34, v160\n"
        "v_fma_f32 v106, v106, s34, v160\n"
        "v_fma_f32 v107, v107, s34, v160\n"
        "v_fma_f32 v84, v84, s34, v160\n"
        "v_fma_f32 v85, v85, s34, v160\n"
        "v_fma_f32 v86, v86, s34, v160\n"
        "v_fma_f32 v87, v87, s34, v160\n"
        "v_fma_f32 v88, v88, s34, v160\n"
        "v_fma_f32 v89, v89, s34, v160\n"
        "v_fma_f32 v90, v90, s34, v160\n"
        "v_fma_f32 v91, v91, s34, v160\n"
        "v_cvt_pk_bf16_f32 v64, v68, v69\n"
        "v_cvt_pk_bf16_f32 v65, v70, v71\n"
        "v_cvt_pk_bf16_f32 v66, v120, v121\n"
        "v_cvt_pk_bf16_f32 v67, v122, v123\n"
        "v_cvt_pk_bf16_f32 v68, v148, v149\n"
        "v_cvt_pk_bf16_f32 v69, v150, v151\n"
        "v_cvt_pk_bf16_f32 v70, v104, v105\n"
        "v_cvt_pk_bf16_f32 v71, v106, v107\n"
        "v_cvt_pk_bf16_f32 v72, v84, v85\n"
        "v_cvt_pk_bf16_f32 v73, v86, v87\n"
        "v_cvt_pk_bf16_f32 v74, v88, v89\n"
        "v_cvt_pk_bf16_f32 v75, v90, v91\n"
        "s_nop 1\n"
        "v_mov_b32 v238, s36\n"
        "L70_%=:\n"
        "ds_read_b32 v237, v238 offset:0\n"
        "s_waitcnt lgkmcnt(0)\n"
        "v_readfirstlane_b32 s40, v237\n"
        "s_nop 3\n"
        "s_cmp_ge_i32 s40, 4\n"
        "s_cbranch_scc1 L71_%=\n"
        "s_nop 0\n"
        "s_sub_u32 s41, s41, 1\n"
        "s_cbranch_scc0 L70_%=\n"
        "s_endpgm\n"
        "L71_%=:\n"
        "ds_write_b128 v244, v[132:135]\n"
        "ds_write_b128 v244, v[124:127] offset:1024\n"
        "ds_write_b128 v244, v[136:139] offset:2048\n"
        "ds_write_b128 v245, v[64:67]\n"
        "ds_write_b128 v245, v[68:71] offset:1024\n"
        "ds_write_b128 v245, v[72:75] offset:2048\n"
        "s_mov_b64 exec, 1\n"
        "ds_inc_u32 v239, v239 offset:0\n"
        "s_mov_b64 exec, -1\n"
        "s_setpc_b64 s[30:31]\n"
        "PPRO_%=:\n"
        "s_sub_u32 s64, s64, 36864\n"
        "s_mul_i32 s7, s6, 7168\n"
        "s_add_u32 s7, s7, 6144\n"
        "s_add_u32 s22, s4, s7\n"
        "s_addc_u32 s23, s5, 0\n"
        "global_load_dwordx4 v[240:243], v57, s[22:23] offset:-3072\n"
        "global_load_dwordx4 v[244:247], v57, s[22:23] offset:-2048\n"
        "s_mov_b32 s65, 13824\n"
        "v_add_u32 v24, 0, v56\n"
        "v_mul_u32_u24 v21, 171, v24\n"
        "v_lshrrev_b32 v21, 11, v21\n"
        "v_mad_i32_i24 v24, v21, -12, v24\n"
        "v_bfe_u32 v58, v21, 2, 2\n"
        "v_xor_b32 v58, v24, v58\n"
        "v_and_b32 v27, 7, v21\n"
        "v_mul_u32_u24 v27, 576, v27\n"
        "v_lshl_add_u32 v58, v58, 4, v27\n"
        "v_add_u32 v25, 64, v56\n"
        "v_mul_u32_u24 v22, 171, v25\n"
        "v_lshrrev_b32 v22, 11, v22\n"
        "v_mad_i32_i24 v25, v22, -12, v25\n"
        "v_bfe_u32 v59, v22, 2, 2\n"
        "v_xor_b32 v59, v25, v59\n"
        "v_and_b32 v27, 7, v22\n"
        "v_mul_u32_u24 v27, 576, v27\n"
        "v_lshl_add_u32 v59, v59, 4, v27\n"
        "v_lshrrev_b32 v27, 3, v22\n"
        "v_mad_u32_u24 v59, v27, s65, v59\n"
        "v_add_u32 v26, 128, v56\n"
        "v_mul_u32_u24 v23, 171, v26\n"
        "v_lshrrev_b32 v23, 11, v23\n"
        "v_mad_i32_i24 v26, v23, -12, v26\n"
        "v_bfe_u32 v60, v23, 2, 2\n"
        "v_xor_b32 v60, v26, v60\n"
        "v_and_b32 v27, 7, v23\n"
        "v_mul_u32_u24 v27, 576, v27\n"
        "v_lshl_add_u32 v60, v60, 4, v27\n"
        "v_add_u32 v60, s65, v60\n"
        "s_lshr_b32 s61, s6, 1\n"
        "s_mul_i32 s61, s61, 192\n"
        "s_mov_b32 s62, 27648\n"
        "s_mov_b32 s63, 4608\n"
        "s_add_u32 s7, s62, s63\n"
        "s_lshl_b32 s29, s63, 1\n"
        "s_add_u32 s60, s7, s63\n"
        "s_bitcmp1_b32 s6, 0\n"
        "s_cselect_b32 s28, s7, 0\n"
        "s_cselect_b32 s29, s29, s62\n"
        "s_cselect_b32 s60, s60, s63\n"
        "s_add_u32 s28, s28, s61\n"
        "s_add_u32 s29, s29, s61\n"
        "s_add_u32 s60, s60, s61\n"
        "s_mov_b32 s26, 55296\n"
        "s_waitcnt lgkmcnt(0)\n"
        "s_mov_b32 s24, s46\n"
        "s_and_b32 s25, s47, 0xffff\n"
        "s_add_u32 m0, s64, 0\n"
        "s_nop 0\n"
        "buffer_load_dwordx4 v58, s[24:27], s28 offen lds\n"
        "s_add_u32 m0, s64, 1024\n"
        "s_nop 0\n"
        "buffer_load_dwordx4 v59, s[24:27], s28 offen lds\n"
        "s_add_u32 m0, s64, 2048\n"
        "s_nop 0\n"
        "buffer_load_dwordx4 v60, s[24:27], s28 offen lds\n"
        "s_add_u32 m0, s64, 3072\n"
        "s_nop 0\n"
        "buffer_load_dwordx4 v58, s[24:27], s29 offen lds\n"
        "s_add_u32 m0, s64, 4096\n"
        "s_nop 0\n"
        "buffer_load_dwordx4 v59, s[24:27], s29 offen lds\n"
        "s_add_u32 m0, s64, 5120\n"
        "s_nop 0\n"
        "buffer_load_dwordx4 v60, s[24:27], s29 offen lds\n"
        "s_add_u32 m0, s64, 6144\n"
        "s_nop 0\n"
        "buffer_load_dwordx4 v58, s[24:27], s60 offen lds\n"
        "s_add_u32 m0, s64, 7168\n"
        "s_nop 0\n"
        "buffer_load_dwordx4 v59, s[24:27], s60 offen lds\n"
        "s_add_u32 m0, s64, 8192\n"
        "s_nop 0\n"
        "buffer_load_dwordx4 v60, s[24:27], s60 offen lds\n"
        "s_barrier\n"
        "s_mov_b64 s[24:25], s[56:57]\n"
        "s_and_b32 s7, s2, 7\n"
        "s_lshl_b32 s7, s7, 5\n"
        "s_lshr_b32 s14, s2, 3\n"
        "s_add_u32 s7, s7, s14\n"
        "s_sub_u32 s7, 255, s7\n"
        "s_and_b32 s16, s7, 255\n"
        "s_sub_u32 s7, s7, s16\n"
        "s_mul_i32 s7, s7, 20\n"
        "s_add_u32 s7, s7, s16\n"
        "s_mov_b32 s14, 22\n"
        "s_cmp_eq_u32 s2, 0\n"
        "s_cselect_b32 s43, 19, 20\n"
        "s_mul_i32 s15, s7, 57457\n"
        "s_lshr_b32 s15, s15, 22\n"
        "s_mov_b32 s16, s7\n"
        "s_mul_i32 s15, s15, 588672\n"
        "s_mul_i32 s7, s16, 1344\n"
        "s_add_u32 s15, s15, s7\n"
        "s_mul_i32 s74, s6, 98112\n"
        "s_add_u32 s74, s74, s15\n"
        "s_mov_b32 s36, 96768\n"
        "s_mov_b32 s40, 599187\n"
        "s_cmp_eq_u32 s6, 3\n"
        "s_cselect_b32 s7, s16, s6\n"
        "s_cselect_b32 s36, 588672, s36\n"
        "s_cselect_b32 s74, 589824, s74\n"
        "s_cselect_b32 s37, 512, 1\n"
        "s_cselect_b32 s38, 256, 4\n"
        "s_cselect_b32 s39, 7, 1\n"
        "s_cselect_b32 s40, 57457, s40\n"
        "s_mul_i32 s39, s39, 192\n"
        "s_mov_b32 s35, 50135232\n"
        "v_mov_b32 v29, 2048\n"
        "v_bfe_u32 v27, v21, 2, 1\n"
        "v_and_b32 v21, 3, v21\n"
        "v_mul_u32_u24 v21, s37, v21\n"
        "v_mad_u32_u24 v21, v27, s38, v21\n"
        "v_add_u32 v21, s7, v21\n"
        "v_and_b32 v27, 3, v24\n"
        "v_mad_u32_u24 v29, v27, 48, v29\n"
        "v_lshrrev_b32 v27, 2, v24\n"
        "v_lshl_add_u32 v29, v27, 4, v29\n"
        "v_mul_u32_u24 v27, s40, v21\n"
        "v_mul_u32_u24 v21, s39, v21\n"
        "v_lshrrev_b32 v27, 22, v27\n"
        "v_mul_u32_u24 v27, s36, v27\n"
        "v_add3_u32 v29, v29, v21, v27\n"
        "v_lshrrev_b32 v27, 3, v22\n"
        "v_xor_b32 v25, v25, v27\n"
        "v_mul_lo_u32 v30, v27, s35\n"
        "v_add_u32 v30, 1024, v30\n"
        "v_bfe_u32 v27, v22, 2, 1\n"
        "v_and_b32 v22, 3, v22\n"
        "v_mul_u32_u24 v22, s37, v22\n"
        "v_mad_u32_u24 v22, v27, s38, v22\n"
        "v_add_u32 v22, s7, v22\n"
        "v_and_b32 v27, 3, v25\n"
        "v_mad_u32_u24 v30, v27, 48, v30\n"
        "v_lshrrev_b32 v27, 2, v25\n"
        "v_lshl_add_u32 v30, v27, 4, v30\n"
        "v_mul_u32_u24 v27, s40, v22\n"
        "v_mul_u32_u24 v22, s39, v22\n"
        "v_lshrrev_b32 v27, 22, v27\n"
        "v_mul_u32_u24 v27, s36, v27\n"
        "v_add3_u32 v30, v30, v22, v27\n"
        "v_mov_b32 v31, 50135232\n"
        "v_xor_b32 v26, 1, v26\n"
        "v_bfe_u32 v27, v23, 2, 1\n"
        "v_and_b32 v23, 3, v23\n"
        "v_mul_u32_u24 v23, s37, v23\n"
        "v_mad_u32_u24 v23, v27, s38, v23\n"
        "v_add_u32 v23, s7, v23\n"
        "v_and_b32 v27, 3, v26\n"
        "v_mad_u32_u24 v31, v27, 48, v31\n"
        "v_lshrrev_b32 v27, 2, v26\n"
        "v_lshl_add_u32 v31, v27, 4, v31\n"
        "v_mul_u32_u24 v27, s40, v23\n"
        "v_mul_u32_u24 v23, s39, v23\n"
        "v_lshrrev_b32 v27, 22, v27\n"
        "v_mul_u32_u24 v27, s36, v27\n"
        "v_add3_u32 v31, v31, v23, v27\n"
        "s_mul_i32 s17, s6, 6208\n"
        "s_add_u32 s17, s17, 110176\n"
        "s_mov_b32 s18, s6\n"
        "s_add_u32 s75, s74, 100270464\n"
        "v_mov_b32 v250, 21600\n"
        "s_mov_b32 s41, 0x400000\n"
        "s_sub_u32 s8, s44, 2048\n"
        "s_subb_u32 s9, s45, 0\n"
        "s_and_b32 s9, s9, 0xffff\n"
        "s_mov_b32 s10, 200542976\n"
        "s_mov_b32 s11, 0x20000\n"
        "s_setprio 3\n"
        "s_waitcnt vmcnt(0)\n"
        "s_mov_b64 exec, 1\n"
        "ds_inc_u32 v250, v250 offset:16\n"
        "s_mov_b64 exec, -1\n"
        "L101_%=:\n"
        "ds_read_b32 v251, v250 offset:16\n"
        "s_waitcnt lgkmcnt(0)\n"
        "v_readfirstlane_b32 s40, v251\n"
        "s_nop 3\n"
        "s_cmp_ge_i32 s40, 4\n"
        "s_cbranch_scc1 L102_%=\n"
        "s_nop 0\n"
        "s_sub_u32 s41, s41, 1\n"
        "s_cbranch_scc0 L101_%=\n"
        "s_endpgm\n"
        "L102_%=:\n"
        "s_cmp_eq_u32 s6, 0\n"
        "s_cbranch_scc1 L103_%=\n"
        "s_mov_b32 s34, 0x4162ccfd\n"
        "s_mov_b32 s35, 21624\n"
        "s_mov_b32 s36, 21624\n"
        "s_mov_b32 s37, 21640\n"
        "s_call_b64 s[30:31], FOLDN_%=\n"
        "s_branch L104_%=\n"
        "L103_%=:\n"
        "s_mov_b32 s34, 0xbe173353\n"
        "s_mov_b32 s35, 21656\n"
        "s_mov_b32 s36, 21656\n"
        "s_mov_b32 s37, 21624\n"
        "s_call_b64 s[30:31], FOLDS_%=\n"
        "L104_%=:\n"
        "s_setprio 3\n"
        "s_mov_b32 s44, 0xf0000f\n"
        "s_mov_b32 s45, 0xf0000f00\n"
        "s_mov_b32 s46, 0xff000ff\n"
        "s_mov_b32 s47, 0xf00fff00\n"
        "s_mov_b32 s48, 0x22222222\n"
        "s_mov_b32 s49, 0x22222222\n"
        "s_mov_b32 s50, 0x44444444\n"
        "s_mov_b32 s51, 0x44444444\n"
        "s_mov_b32 s52, 0x88888888\n"
        "s_mov_b32 s53, 0x88888888\n"
        "s_mov_b64 vcc, s[44:45]\n"
        "s_mov_b32 s28, 0x3c2aaaab\n"
        "s_mov_b32 s29, 0x3a7ba882\n"
        "s_mov_b32 s30, 0\n"
        "s_mov_b32 s34, 0\n"
        "s_cmp_eq_u32 s18, 3\n"
        "s_cselect_b32 s31, 6, s18\n"
        "s_mov_b32 s36, s18\n"
        "s_mov_b32 s37, s18\n"
        "s_add_u32 s71, s17, 3104\n"
        "s_mov_b32 s68, 4\n"
        "s_mov_b32 s63, 0x07060302\n"
        "s_mov_b32 s74, 16\n"
        "s_mov_b32 s72, -1\n"
        "s_mov_b32 s41, 0x400000\n"
        "L25_%=:\n"
        "ds_read_b32 v251, v250 offset:40\n"
        "s_waitcnt lgkmcnt(0)\n"
        "v_readfirstlane_b32 s40, v251\n"
        "s_nop 3\n"
        "s_cmp_ge_i32 s40, 3\n"
        "s_cbranch_scc1 L26_%=\n"
        "s_nop 0\n"
        "s_sub_u32 s41, s41, 1\n"
        "s_cbranch_scc0 L25_%=\n"
        "s_endpgm\n"
        "L26_%=:\n"
        "L27_%=:\n"
        "ds_read_b32 v251, v250 offset:20\n"
        "s_waitcnt lgkmcnt(0)\n"
        "v_readfirstlane_b32 s40, v251\n"
        "s_nop 3\n"
        "s_cmp_ge_i32 s40, 4\n"
        "s_cbranch_scc1 L28_%=\n"
        "s_nop 0\n"
        "s_sub_u32 s41, s41, 1\n"
        "s_cbranch_scc0 L27_%=\n"
        "s_endpgm\n"
        "L28_%=:\n"
        "s_waitcnt vmcnt(0)\n"
        "ds_read_b128 v[16:19], v16\n"
        "ds_read_b64 v[94:95], v14\n"
        "ds_read_b64 v[86:87], v14\n"
        "ds_read_b64 v[28:29], v14\n"
        "ds_read_b64 v[90:91], v14\n"
        "v_mov_b32 v102, 21600\n"
        "v_add_u32 v20, 21728, v13\n"
        "ds_read_b128 v[112:115], v20 offset:0\n"
        "ds_read_b128 v[116:119], v20 offset:1024\n"
        "ds_read_b128 v[120:123], v20 offset:2048\n"
        "ds_read_b128 v[124:127], v20 offset:3072\n"
        "ds_read_b128 v[128:131], v20 offset:4096\n"
        "ds_read_b128 v[132:135], v20 offset:5120\n"
        "ds_read_b128 v[136:139], v20 offset:6144\n"
        "ds_read_b128 v[140:143], v20 offset:7168\n"
        "ds_read_b128 v[144:147], v20 offset:8192\n"
        "ds_read_b128 v[148:151], v20 offset:9216\n"
        "ds_read_b128 v[152:155], v20 offset:10240\n"
        "ds_read_b128 v[156:159], v20 offset:11264\n"
        "ds_read_b128 v[160:163], v20 offset:12288\n"
        "ds_read_b128 v[164:167], v20 offset:13312\n"
        "ds_read_b128 v[168:171], v20 offset:14336\n"
        "ds_read_b128 v[172:175], v20 offset:15360\n"
        "ds_read_b128 v[176:179], v20 offset:16384\n"
        "ds_read_b128 v[180:183], v20 offset:17408\n"
        "ds_read_b128 v[60:63], v0 offset:0\n"
        "ds_read_b128 v[66:69], v0 offset:64\n"
        "ds_read_b128 v[72:75], v0 offset:128\n"
        "ds_read_b128 v[42:45], v0 offset:768\n"
        "ds_read_b128 v[48:51], v0 offset:832\n"
        "ds_read_b128 v[54:57], v0 offset:896\n"
        "s_add_u32 s37, s37, 4\n"
        "s_cmp_ge_u32 s37, 49\n"
        "s_cselect_b32 s69, 49, 0\n"
        "s_sub_u32 s37, s37, s69\n"
        "s_sub_u32 s69, s37, 4\n"
        "s_cmp_lt_u32 s69, 4\n"
        "s_subb_u32 s36, s36, -4\n"
        "s_cmp_ge_u32 s36, 6\n"
        "s_cselect_b32 s69, 6, 0\n"
        "s_addc_u32 s34, s34, 0\n"
        "s_sub_u32 s36, s36, s69\n"
        "s_cmp_ge_u32 s34, s74\n"
        "s_cbranch_scc0 L31_%=\n"
        "s_nop 1\n"
        "v_mov_b32 v22, 21600\n"
        "L29_%=:\n"
        "ds_read_b32 v23, v22 offset:64\n"
        "s_waitcnt lgkmcnt(0)\n"
        "v_readfirstlane_b32 s40, v23\n"
        "s_nop 3\n"
        "s_cmp_ge_i32 s40, 1\n"
        "s_cbranch_scc1 L30_%=\n"
        "s_sleep 1\n"
        "s_sub_u32 s41, s41, 1\n"
        "s_cbranch_scc0 L29_%=\n"
        "s_endpgm\n"
        "L30_%=:\n"
        "v_mbcnt_lo_u32_b32 v23, -1, 0\n"
        "v_lshl_add_u32 v22, v23, 2, v22\n"
        "ds_read_b32 v23, v22 offset:68\n"
        "s_mov_b32 s14, s40\n"
        "s_mov_b32 s74, -1\n"
        "s_waitcnt lgkmcnt(0)\n"
        "v_readlane_b32 s22, v23, 0\n"
        "v_readlane_b32 s23, v23, 1\n"
        "s_nop 3\n"
        "L31_%=:\n"
        "s_mul_i32 s38, s34, 256\n"
        "s_add_u32 s38, s38, s16\n"
        "s_cmp_ge_u32 s34, s43\n"
        "s_cselect_b32 s38, s22, s38\n"
        "s_cmp_gt_u32 s34, s43\n"
        "s_cselect_b32 s38, s23, s38\n"
        "s_mul_hi_u32 s70, s38, 58835169\n"
        "s_mul_i32 s38, s38, 1344\n"
        "s_mul_i32 s70, s70, 588672\n"
        "s_add_u32 s38, s38, s70\n"
        "s_mul_i32 s69, s36, 98304\n"
        "s_add_u32 s38, s38, s69\n"
        "s_mov_b32 s35, s36\n"
        "s_mul_i32 s69, s36, -12\n"
        "s_add_u32 s69, s69, 60\n"
        "s_lshl_b64 s[54:55], -1, s69\n"
        "s_lshl_b64 s[56:57], s[54:55], 24\n"
        "s_ashr_i64 s[60:61], s[54:55], 8\n"
        "s_mov_b32 s58, s61\n"
        "s_mov_b32 s59, s56\n"
        "v_cndmask_b32_e64 v15, v4, v7, s[56:57]\n"
        "v_cndmask_b32_e64 v100, v5, v8, s[58:59]\n"
        "v_cndmask_b32_e64 v103, v6, v9, s[60:61]\n"
        "s_cmp_eq_u32 s37, 3\n"
        "s_cbranch_scc0 L32_%=\n"
        "s_nop 1\n"
        "ds_read_b32 v15, v13 offset:19552\n"
        "ds_read_b32 v100, v13 offset:19556\n"
        "ds_read_b32 v103, v13 offset:19560\n"
        "s_mov_b32 s35, 6\n"
        "s_mov_b32 s38, 0\n"
        "s_mov_b32 s69, 1344\n"
        "s_waitcnt lgkmcnt(0)\n"
        "v_lshrrev_b32 v22, 28, v15\n"
        "v_add_u32 v22, s34, v22\n"
        "v_and_b32 v15, 0x0fffffff, v15\n"
        "v_mul_u32_u24 v23, 256, v22\n"
        "v_add_u32 v23, s16, v23\n"
        "v_cmpx_le_u32 vcc, s43, v22\n"
        "v_mov_b32 v23, s22\n"
        "s_mov_b64 exec, -1\n"
        "v_cmpx_lt_u32 vcc, s43, v22\n"
        "v_mov_b32 v23, s23\n"
        "s_mov_b64 exec, -1\n"
        "v_mad_u32_u24 v15, v23, s69, v15\n"
        "v_mul_u32_u24 v23, 57457, v23\n"
        "v_lshrrev_b32 v23, 22, v23\n"
        "v_mul_u32_u24 v23, 588672, v23\n"
        "v_add_u32 v15, v15, v23\n"
        "v_lshrrev_b32 v22, 28, v100\n"
        "v_add_u32 v22, s34, v22\n"
        "v_and_b32 v100, 0x0fffffff, v100\n"
        "v_mul_u32_u24 v23, 256, v22\n"
        "v_add_u32 v23, s16, v23\n"
        "v_cmpx_le_u32 vcc, s43, v22\n"
        "v_mov_b32 v23, s22\n"
        "s_mov_b64 exec, -1\n"
        "v_cmpx_lt_u32 vcc, s43, v22\n"
        "v_mov_b32 v23, s23\n"
        "s_mov_b64 exec, -1\n"
        "v_mad_u32_u24 v100, v23, s69, v100\n"
        "v_mul_u32_u24 v23, 57457, v23\n"
        "v_lshrrev_b32 v23, 22, v23\n"
        "v_mul_u32_u24 v23, 588672, v23\n"
        "v_add_u32 v100, v100, v23\n"
        "v_lshrrev_b32 v22, 28, v103\n"
        "v_add_u32 v22, s34, v22\n"
        "v_and_b32 v103, 0x0fffffff, v103\n"
        "v_mul_u32_u24 v23, 256, v22\n"
        "v_add_u32 v23, s16, v23\n"
        "v_cmpx_le_u32 vcc, s43, v22\n"
        "v_mov_b32 v23, s22\n"
        "s_mov_b64 exec, -1\n"
        "v_cmpx_lt_u32 vcc, s43, v22\n"
        "v_mov_b32 v23, s23\n"
        "s_mov_b64 exec, -1\n"
        "v_mad_u32_u24 v103, v23, s69, v103\n"
        "v_mul_u32_u24 v23, 57457, v23\n"
        "v_lshrrev_b32 v23, 22, v23\n"
        "v_mul_u32_u24 v23, 588672, v23\n"
        "v_add_u32 v103, v103, v23\n"
        "s_nop 4\n"
        "s_mov_b64 vcc, s[44:45]\n"
        "L32_%=:\n"
        "s_cmp_lt_u32 s34, s14\n"
        "s_cselect_b32 s38, s38, 0x80000000\n"
        "s_add_u32 s39, s38, 100270464\n"
        "s_mov_b32 s65, 1\n"
        "s_waitcnt lgkmcnt(0)\n"
        "s_cmp_eq_u32 s18, 0\n"
        "s_cselect_b32 s73, -20, 8\n"
        "s_setprio 1\n"
        "PFST_%=:\n"
        "v_mfma_f32_16x16x32_bf16 v[34:37], v[112:115], v[60:63], 0\n"
        "s_mov_b32 m0, s17\n"
        "v_mfma_f32_16x16x32_bf16 v[34:37], v[116:119], v[66:69], v[34:37]\n"
        "v_mfma_f32_16x16x32_bf16 v[34:37], v[120:123], v[72:75], v[34:37]\n"
        "buffer_load_dwordx4 v15, s[8:11], s38 offen lds\n"
        "v_mfma_f32_16x16x32_bf16 v[38:41], v[132:135], v[72:75], 0\n"
        "v_mfma_f32_16x16x32_bf16 v[38:41], v[128:131], v[66:69], v[38:41]\n"
        "s_setprio 3\n"
        "v_mfma_f32_16x16x32_bf16 v[38:41], v[124:127], v[60:63], v[38:41]\n"
        "buffer_load_dwordx4 v100, s[8:11], s38 offen offset:1024 lds\n"
        "v_mfma_f32_16x16x32_bf16 v[78:81], v[136:139], v[60:63], 0\n"
        "v_mfma_f32_16x16x32_bf16 v[78:81], v[140:143], v[66:69], v[78:81]\n"
        "v_mfma_f32_16x16x32_bf16 v[78:81], v[144:147], v[72:75], v[78:81]\n"
        "v_mfma_f32_16x16x32_bf16 v[82:85], v[156:159], v[72:75], 0\n"
        "v_mfma_f32_16x16x32_bf16 v[82:85], v[152:155], v[66:69], v[82:85]\n"
        "v_mfma_f32_16x16x32_bf16 v[82:85], v[148:151], v[60:63], v[82:85]\n"
        "buffer_load_dwordx4 v103, s[8:11], s38 offen offset:2048 lds\n"
        "s_mov_b32 m0, s71\n"
        "s_cmp_eq_u32 s72, s30\n"
        "v_mfma_f32_16x16x32_bf16 v[104:107], v[160:163], v[60:63], 0\n"
        "s_setprio 3\n"
        "s_cbranch_scc1 F5_%=\n"
        "s_setprio 1\n"
        "F5_%=:\n"
        "v_mfma_f32_16x16x32_bf16 v[104:107], v[164:167], v[66:69], v[104:107]\n"
        "v_mfma_f32_16x16x32_bf16 v[104:107], v[168:171], v[72:75], v[104:107]\n"
        "buffer_load_dwordx4 v15, s[8:11], s39 offen lds\n"
        "v_mfma_f32_16x16x32_bf16 v[108:111], v[180:183], v[72:75], 0\n"
        "v_mfma_f32_16x16x32_bf16 v[108:111], v[176:179], v[66:69], v[108:111]\n"
        "v_mfma_f32_16x16x32_bf16 v[108:111], v[172:175], v[60:63], v[108:111]\n"
        "buffer_load_dwordx4 v100, s[8:11], s39 offen offset:1024 lds\n"
        "v_mfma_f32_16x16x32_bf16 v[20:23], v[60:63], v[60:63], 0\n"
        "v_mfma_f32_16x16x32_bf16 v[20:23], v[66:69], v[66:69], v[20:23]\n"
        "v_mfma_f32_16x16x32_bf16 v[20:23], v[72:75], v[72:75], v[20:23]\n"
        "buffer_load_dwordx4 v103, s[8:11], s39 offen offset:2048 lds\n"
        "v_mov_b64 v[96:97], 0\n"
        "v_mov_b64 v[98:99], 0\n"
        "s_cselect_b32 s64, 1, 0\n"
        "F23_%=:\n"
        "ds_read_b32 v184, v13 offset:21628\n"
        "s_waitcnt lgkmcnt(0)\n"
        "v_readfirstlane_b32 s40, v184\n"
        "s_nop 3\n"
        "s_cmp_ge_i32 s40, 3\n"
        "s_cbranch_scc1 F24_%=\n"
        "s_nop 0\n"
        "s_sub_u32 s41, s41, 1\n"
        "s_cbranch_scc0 F23_%=\n"
        "s_endpgm\n"
        "F24_%=:\n"
        "s_cmp_lg_u32 s64, 0\n"
        "v_mfma_f32_16x16x32_bf16 v[24:27], v[42:45], v[42:45], 0\n"
        "ds_read_b128 v[184:187], v13 offset:40160\n"
        "v_mfma_f32_16x16x32_bf16 v[24:27], v[48:51], v[48:51], v[24:27]\n"
        "ds_read_b128 v[188:191], v13 offset:41184\n"
        "v_mfma_f32_16x16x32_bf16 v[24:27], v[54:57], v[54:57], v[24:27]\n"
        "ds_read_b128 v[192:195], v13 offset:42208\n"
        "v_smfmac_f32_16x16x64_bf16 v[96:99], v[60:63], v[34:41], v10\n"
        "s_nop 0\n"
        "ds_read_b128 v[196:199], v13 offset:43248\n"
        "v_cndmask_b32_e64 v93, v20, v21, s[48:49]\n"
        "v_smfmac_f32_16x16x64_bf16 v[96:99], v[66:69], v[78:85], v10\n"
        "ds_read_b128 v[200:203], v13 offset:44272\n"
        "v_cndmask_b32_e64 v93, v93, v22, s[50:51]\n"
        "v_smfmac_f32_16x16x64_bf16 v[96:99], v[72:75], v[104:111], v10\n"
        "ds_read_b128 v[204:207], v13 offset:45296\n"
        "v_mfma_f32_16x16x32_bf16 v[34:37], v[112:115], v[42:45], 0\n"
        "s_nop 0\n"
        "ds_read_b128 v[208:211], v13 offset:46336\n"
        "v_cndmask_b32_e64 v111, v24, v25, s[48:49]\n"
        "v_cndmask_b32_e64 v111, v111, v26, s[50:51]\n"
        "v_mfma_f32_16x16x32_bf16 v[34:37], v[116:119], v[48:51], v[34:37]\n"
        "s_and_b32 s69, s30, 1\n"
        "ds_read_b128 v[212:215], v13 offset:47360\n"
        "v_cndmask_b32_e64 v93, v93, v23, s[52:53]\n"
        "v_cndmask_b32_e64 v111, v111, v27, s[52:53]\n"
        "v_mfma_f32_16x16x32_bf16 v[34:37], v[120:123], v[54:57], v[34:37]\n"
        "s_lshl2_add_u32 s70, s69, 21600\n"
        "ds_read_b128 v[216:219], v13 offset:48384\n"
        "v_mov_b32 v102, s70\n"
        "v_cndmask_b32_dpp v93, v111, v93, vcc row_ror:4 row_mask:0xf bank_mask:0xf\n"
        "v_mfma_f32_16x16x32_bf16 v[38:41], v[132:135], v[54:57], 0\n"
        "s_mul_i32 s69, s69, 40064\n"
        "ds_read_b128 v[220:223], v13 offset:49424\n"
        "s_mul_i32 s70, s31, 1664\n"
        "v_mfma_f32_16x16x32_bf16 v[38:41], v[128:131], v[48:51], v[38:41]\n"
        "s_add_u32 s69, s69, s70\n"
        "ds_read_b128 v[224:227], v13 offset:50448\n"
        "v_add_u32 v11, s69, v3\n"
        "v_mfma_f32_16x16x32_bf16 v[38:41], v[124:127], v[42:45], v[38:41]\n"
        "ds_read_b128 v[228:231], v13 offset:51472\n"
        "v_add_u32 v12, s69, v2\n"
        "s_lshr_b32 s68, s30, 1\n"
        "v_mfma_f32_16x16x32_bf16 v[104:107], v[136:139], v[42:45], 0\n"
        "s_lshl2_add_u32 s68, s68, 4\n"
        "ds_read_b128 v[232:235], v13 offset:52512\n"
        "s_cmp_eq_u32 s31, 6\n"
        "s_cbranch_scc1 F8_%=\n"
        "F9_%=:\n"
        "v_mfma_f32_16x16x32_bf16 v[104:107], v[140:143], v[48:51], v[104:107]\n"
        "s_mov_b32 s72, s30\n"
        "ds_read_b128 v[236:239], v13 offset:53536\n"
        "s_mov_b32 s30, s34\n"
        "v_mfma_f32_16x16x32_bf16 v[104:107], v[144:147], v[54:57], v[104:107]\n"
        "s_mov_b32 s31, s35\n"
        "ds_read_b128 v[240:243], v13 offset:54560\n"
        "s_add_u32 s37, s37, 4\n"
        "v_mfma_f32_16x16x32_bf16 v[108:111], v[156:159], v[54:57], 0\n"
        "s_cmp_ge_u32 s37, 49\n"
        "ds_read_b128 v[244:247], v13 offset:55600\n"
        "s_cselect_b32 s69, 49, 0\n"
        "v_mfma_f32_16x16x32_bf16 v[108:111], v[152:155], v[48:51], v[108:111]\n"
        "s_sub_u32 s37, s37, s69\n"
        "ds_read_b128 v[248:251], v13 offset:56624\n"
        "s_sub_u32 s69, s37, 4\n"
        "s_cmp_lt_u32 s69, 4\n"
        "v_mfma_f32_16x16x32_bf16 v[108:111], v[148:151], v[42:45], v[108:111]\n"
        "s_subb_u32 s36, s36, -4\n"
        "ds_read_b128 v[252:255], v13 offset:57648\n"
        "s_cmp_ge_u32 s36, 6\n"
        "v_mfma_f32_16x16x32_bf16 v[76:79], v[160:163], v[42:45], 0\n"
        "s_cselect_b32 s69, 6, 0\n"
        "s_addc_u32 s34, s34, 0\n"
        "v_mfma_f32_16x16x32_bf16 v[76:79], v[164:167], v[48:51], v[76:79]\n"
        "s_sub_u32 s36, s36, s69\n"
        "s_cmp_ge_u32 s34, s74\n"
        "v_mfma_f32_16x16x32_bf16 v[76:79], v[168:171], v[54:57], v[76:79]\n"
        "s_cbranch_scc1 F10_%=\n"
        "F11_%=:\n"
        "s_mul_i32 s38, s34, 256\n"
        "v_mfma_f32_16x16x32_bf16 v[80:83], v[180:183], v[54:57], 0\n"
        "s_add_u32 s38, s38, s16\n"
        "s_cmp_ge_u32 s34, s43\n"
        "s_cselect_b32 s38, s22, s38\n"
        "v_mfma_f32_16x16x32_bf16 v[80:83], v[176:179], v[48:51], v[80:83]\n"
        "s_cmp_gt_u32 s34, s43\n"
        "s_cselect_b32 s38, s23, s38\n"
        "v_mfma_f32_16x16x32_bf16 v[80:83], v[172:175], v[42:45], v[80:83]\n"
        "s_mul_hi_u32 s70, s38, 58835169\n"
        "s_mul_i32 s38, s38, 1344\n"
        "v_mfma_f32_16x16x32_bf16 v[20:23], v[16:19], v[60:63], 0\n"
        "s_mul_i32 s70, s70, 588672\n"
        "s_add_u32 s38, s38, s70\n"
        "v_mfma_f32_16x16x32_bf16 v[20:23], v[16:19], v[66:69], v[20:23]\n"
        "s_mul_i32 s69, s36, 98304\n"
        "s_add_u32 s38, s38, s69\n"
        "s_mov_b32 s35, s36\n"
        "v_mfma_f32_16x16x32_bf16 v[20:23], v[16:19], v[72:75], v[20:23]\n"
        "s_mul_i32 s69, s36, -12\n"
        "v_mov_b64 v[30:31], 0\n"
        "v_mov_b64 v[32:33], 0\n"
        "v_mfma_f32_16x16x32_bf16 v[24:27], v[16:19], v[42:45], 0\n"
        "s_add_u32 s69, s69, 60\n"
        "s_lshl_b64 s[54:55], -1, s69\n"
        "s_lshl_b64 s[56:57], s[54:55], 24\n"
        "v_mfma_f32_16x16x32_bf16 v[24:27], v[16:19], v[48:51], v[24:27]\n"
        "s_ashr_i64 s[60:61], s[54:55], 8\n"
        "s_mov_b32 s58, s61\n"
        "v_mfma_f32_16x16x32_bf16 v[24:27], v[16:19], v[54:57], v[24:27]\n"
        "s_mov_b32 s59, s56\n"
        "v_cndmask_b32_e64 v15, v4, v7, s[56:57]\n"
        "v_smfmac_f32_16x16x64_bf16 v[30:33], v[42:45], v[34:41], v10\n"
        "v_cndmask_b32_e64 v100, v5, v8, s[58:59]\n"
        "v_smfmac_f32_16x16x64_bf16 v[30:33], v[48:51], v[104:111], v10\n"
        "v_cndmask_b32_e64 v103, v6, v9, s[60:61]\n"
        "v_smfmac_f32_16x16x64_bf16 v[30:33], v[54:57], v[76:83], v10\n"
        "s_cmp_eq_u32 s37, 3\n"
        "s_cbranch_scc1 F12_%=\n"
        "F13_%=:\n"
        "v_cndmask_b32_dpp v110, v24, v20, vcc row_ror:4 row_mask:0xf bank_mask:0xf\n"
        "s_mov_b64 exec, 1\n"
        "ds_inc_u32 v13, v0 offset:21608\n"
        "s_mov_b64 exec, -1\n"
        "s_waitcnt lgkmcnt(0)\n"
        "v_mfma_f32_16x16x32_bf16 v[36:39], v[54:57], v[192:195], 0\n"
        "s_cmp_lt_u32 s34, s14\n"
        "v_mul_f32 v111, s28, v110\n"
        "v_fma_f32 v111, -v111, v110, v93\n"
        "v_mfma_f32_16x16x32_bf16 v[36:39], v[48:51], v[188:191], v[36:39]\n"
        "s_cselect_b32 s38, s38, 0x80000000\n"
        "s_add_u32 s39, s38, 100270464\n"
        "v_add_f32_e64 v111, s29, |v111|\n"
        "v_mfma_f32_16x16x32_bf16 v[36:39], v[42:45], v[184:187], v[36:39]\n"
        "v_rsq_f32 v84, v111\n"
        "v_cndmask_b32_dpp v104, v30, v96, vcc row_ror:4 row_mask:0xf bank_mask:0xf\n"
        "v_mfma_f32_16x16x32_bf16 v[24:27], v[72:75], v[192:195], 0\n"
        "v_cndmask_b32_e64 v84, 0, v84, s[46:47]\n"
        "v_cndmask_b32_dpp v105, v31, v97, vcc row_ror:4 row_mask:0xf bank_mask:0xf\n"
        "v_mfma_f32_16x16x32_bf16 v[24:27], v[66:69], v[188:191], v[24:27]\n"
        "v_cndmask_b32_dpp v85, v32, v98, vcc row_ror:4 row_mask:0xf bank_mask:0xf\n"
        "v_mul_f32_dpp v104, v84, v104 quad_perm:[0,0,0,0] row_mask:0xf bank_mask:0xf\n"
        "v_mfma_f32_16x16x32_bf16 v[24:27], v[60:63], v[184:187], v[24:27]\n"
        "v_mul_f32_dpp v105, v84, v105 quad_perm:[1,1,1,1] row_mask:0xf bank_mask:0xf\n"
        "v_mul_f32_dpp v85, v84, v85 quad_perm:[2,2,2,2] row_mask:0xf bank_mask:0xf\n"
        "v_mfma_f32_16x16x32_bf16 v[20:23], v[60:63], v[220:223], 0\n"
        "v_cndmask_b32_dpp v106, v33, v99, vcc row_ror:4 row_mask:0xf bank_mask:0xf\n"
        "v_max3_f32 v107, v104, v105, v85\n"
        "v_mfma_f32_16x16x32_bf16 v[20:23], v[66:69], v[224:227], v[20:23]\n"
        "v_mul_f32_dpp v106, v84, v106 quad_perm:[3,3,3,3] row_mask:0xf bank_mask:0xf\n"
        "v_max_f32 v107, v107, v106\n"
        "v_mfma_f32_16x16x32_bf16 v[20:23], v[72:75], v[228:231], v[20:23]\n"
        "v_mul_f32 v107, v107, v84\n"
        "v_fma_f32 v104, v104, v84, -v107\n"
        "v_fma_f32 v105, v105, v84, -v107\n"
        "v_mfma_f32_16x16x32_bf16 v[32:35], v[42:45], v[220:223], 0\n"
        "v_fma_f32 v85, v85, v84, -v107\n"
        "v_fma_f32 v106, v106, v84, -v107\n"
        "v_mfma_f32_16x16x32_bf16 v[32:35], v[48:51], v[224:227], v[32:35]\n"
        "v_exp_f32 v104, v104\n"
        "v_mfma_f32_16x16x32_bf16 v[32:35], v[54:57], v[228:231], v[32:35]\n"
        "v_exp_f32 v105, v105\n"
        "v_mfma_f32_16x16x32_bf16 v[108:111], v[54:57], v[204:207], 0\n"
        "v_exp_f32 v85, v85\n"
        "v_mfma_f32_16x16x32_bf16 v[108:111], v[48:51], v[200:203], v[108:111]\n"
        "v_exp_f32 v106, v106\n"
        "v_mfma_f32_16x16x32_bf16 v[108:111], v[42:45], v[196:199], v[108:111]\n"
        "v_add_f32 v107, v104, v105\n"
        "v_add_f32 v101, v85, v106\n"
        "v_add_f32 v107, v107, v101\n"
        "v_mfma_f32_16x16x32_bf16 v[80:83], v[72:75], v[204:207], 0\n"
        "v_rcp_f32 v107, v107\n"
        "v_mfma_f32_16x16x32_bf16 v[80:83], v[66:69], v[200:203], v[80:83]\n"
        "v_mul_f32_dpp v104, v84, v104 quad_perm:[0,0,0,0] row_mask:0xf bank_mask:0xf\n"
        "v_mul_f32_dpp v105, v84, v105 quad_perm:[1,1,1,1] row_mask:0xf bank_mask:0xf\n"
        "v_mfma_f32_16x16x32_bf16 v[80:83], v[60:63], v[196:199], v[80:83]\n"
        "v_mul_f32_dpp v85, v84, v85 quad_perm:[2,2,2,2] row_mask:0xf bank_mask:0xf\n"
        "v_mul_f32_dpp v106, v84, v106 quad_perm:[3,3,3,3] row_mask:0xf bank_mask:0xf\n"
        "v_mul_f32 v104, v104, v107\n"
        "v_mfma_f32_16x16x32_bf16 v[76:79], v[60:63], v[232:235], 0\n"
        "v_mul_f32 v105, v105, v107\n"
        "v_mul_f32 v85, v85, v107\n"
        "v_mfma_f32_16x16x32_bf16 v[76:79], v[66:69], v[236:239], v[76:79]\n"
        "v_mul_f32 v106, v106, v107\n"
        "v_cvt_pk_bf16_f32 v99, v104, v105\n"
        "v_mfma_f32_16x16x32_bf16 v[76:79], v[72:75], v[240:243], v[76:79]\n"
        "v_cvt_pk_bf16_f32 v101, v85, v106\n"
        "v_and_b32 v58, v1, v99\n"
        "v_and_b32 v59, v1, v101\n"
        "v_mfma_f32_16x16x32_bf16 v[104:107], v[42:45], v[232:235], 0\n"
        "v_cvt_pk_bf16_f32 v92, v24, v25\n"
        "v_cvt_pk_bf16_f32 v93, v26, v27\n"
        "v_mfma_f32_16x16x32_bf16 v[104:107], v[48:51], v[236:239], v[104:107]\n"
        "v_and_b32_dpp v40, v99, v1 row_ror:12 row_mask:0xf bank_mask:0xf\n"
        "v_and_b32_dpp v41, v101, v1 row_ror:12 row_mask:0xf bank_mask:0xf\n"
        "v_mfma_f32_16x16x32_bf16 v[104:107], v[54:57], v[240:243], v[104:107]\n"
        "v_cvt_pk_bf16_f32 v84, v36, v37\n"
        "v_cvt_pk_bf16_f32 v85, v38, v39\n"
        "v_mov_b64 v[70:71], v[58:59]\n"
        "v_mfma_f32_16x16x32_bf16 v[36:39], v[92:95], v[58:61], 0\n"
        "v_cvt_pk_bf16_f32 v96, v20, v21\n"
        "v_cvt_pk_bf16_f32 v97, v22, v23\n"
        "v_mfma_f32_16x16x32_bf16 v[22:25], v[84:87], v[40:43], 0\n"
        "v_mov_b64 v[52:53], v[40:41]\n"
        "v_cvt_pk_bf16_f32 v88, v32, v33\n"
        "v_cvt_pk_bf16_f32 v89, v34, v35\n"
        "v_mfma_f32_16x16x32_bf16 v[32:35], v[94:97], v[68:71], 0\n"
        "v_mov_b64 v[64:65], v[58:59]\n"
        "v_cvt_pk_bf16_f32 v30, v80, v81\n"
        "v_mfma_f32_16x16x32_bf16 v[96:99], v[86:89], v[50:53], 0\n"
        "v_cvt_pk_bf16_f32 v31, v82, v83\n"
        "v_mov_b64 v[46:47], v[40:41]\n"
        "v_mfma_f32_16x16x32_bf16 v[80:83], v[54:57], v[216:219], 0\n"
        "v_cvt_pk_bf16_f32 v92, v108, v109\n"
        "v_cvt_pk_bf16_f32 v93, v110, v111\n"
        "v_mfma_f32_16x16x32_bf16 v[80:83], v[48:51], v[212:215], v[80:83]\n"
        "v_cvt_pk_bf16_f32 v26, v76, v77\n"
        "v_cvt_pk_bf16_f32 v27, v78, v79\n"
        "v_mfma_f32_16x16x32_bf16 v[80:83], v[42:45], v[208:211], v[80:83]\n"
        "v_cvt_pk_bf16_f32 v88, v104, v105\n"
        "v_cvt_pk_bf16_f32 v89, v106, v107\n"
        "v_mfma_f32_16x16x32_bf16 v[108:111], v[72:75], v[216:219], 0\n"
        "v_mov_b64 v[76:77], v[58:59]\n"
        "v_mov_b64 v[58:59], v[40:41]\n"
        "v_mfma_f32_16x16x32_bf16 v[108:111], v[66:69], v[212:215], v[108:111]\n"
        "v_cvt_pk_bf16_f32 v104, v36, v37\n"
        "v_cvt_pk_bf16_f32 v105, v38, v39\n"
        "v_mfma_f32_16x16x32_bf16 v[108:111], v[60:63], v[208:211], v[108:111]\n"
        "v_cvt_pk_bf16_f32 v84, v80, v81\n"
        "v_cvt_pk_bf16_f32 v85, v82, v83\n"
        "v_mfma_f32_16x16x32_bf16 v[80:83], v[28:31], v[62:65], 0\n"
        "v_cvt_pk_bf16_f32 v106, v32, v33\n"
        "v_cvt_pk_bf16_f32 v107, v34, v35\n"
        "v_mfma_f32_16x16x32_bf16 v[32:35], v[90:93], v[44:47], 0\n"
        "v_cvt_pk_bf16_f32 v20, v22, v23\n"
        "v_cvt_pk_bf16_f32 v21, v24, v25\n"
        "v_mfma_f32_16x16x32_bf16 v[38:41], v[60:63], v[244:247], 0\n"
        "v_cvt_pk_bf16_f32 v92, v108, v109\n"
        "v_cvt_pk_bf16_f32 v93, v110, v111\n"
        "v_mfma_f32_16x16x32_bf16 v[38:41], v[66:69], v[248:251], v[38:41]\n"
        "v_cvt_pk_bf16_f32 v108, v80, v81\n"
        "v_cvt_pk_bf16_f32 v109, v82, v83\n"
        "v_mfma_f32_16x16x32_bf16 v[38:41], v[72:75], v[252:255], v[38:41]\n"
        "v_cvt_pk_bf16_f32 v22, v96, v97\n"
        "v_cvt_pk_bf16_f32 v23, v98, v99\n"
        "v_mfma_f32_16x16x32_bf16 v[80:83], v[42:45], v[244:247], 0\n"
        "v_cvt_pk_bf16_f32 v24, v32, v33\n"
        "v_cvt_pk_bf16_f32 v25, v34, v35\n"
        "v_mfma_f32_16x16x32_bf16 v[80:83], v[48:51], v[248:251], v[80:83]\n"
        "v_mfma_f32_16x16x32_bf16 v[80:83], v[54:57], v[252:255], v[80:83]\n"
        "v_mfma_f32_16x16x32_bf16 v[34:37], v[26:29], v[70:73], 0\n"
        "v_cvt_pk_bf16_f32 v96, v38, v39\n"
        "v_cvt_pk_bf16_f32 v97, v40, v41\n"
        "v_mfma_f32_16x16x32_bf16 v[38:41], v[88:91], v[52:55], 0\n"
        "v_mfma_f32_16x16x32_bf16 v[30:33], v[92:95], v[64:67], 0\n"
        "s_waitcnt vmcnt(0)\n"
        "ds_read_b128 v[60:63], v0\n"
        "ds_read_b128 v[42:45], v0 offset:768\n"
        "ds_read_b128 v[66:69], v0 offset:64\n"
        "v_cvt_pk_bf16_f32 v88, v80, v81\n"
        "v_mfma_f32_16x16x32_bf16 v[78:81], v[84:87], v[46:49], 0\n"
        "s_add_u32 s73, s73, 1\n"
        "ds_read_b128 v[48:51], v0 offset:832\n"
        "v_cvt_pk_bf16_f32 v89, v82, v83\n"
        "v_cvt_pk_bf16_f32 v110, v34, v35\n"
        "v_cvt_pk_bf16_f32 v111, v36, v37\n"
        "v_mfma_f32_16x16x32_bf16 v[82:85], v[94:97], v[74:77], 0\n"
        "s_cmp_lt_u32 s73, 4\n"
        "ds_read_b128 v[72:75], v0 offset:128\n"
        "v_cvt_pk_bf16_f32 v96, v30, v31\n"
        "v_cvt_pk_bf16_f32 v97, v32, v33\n"
        "v_cvt_pk_bf16_f32 v26, v38, v39\n"
        "v_mfma_f32_16x16x32_bf16 v[34:37], v[86:89], v[56:59], 0\n"
        "s_cbranch_scc1 F19_%=\n"
        "F20_%=:\n"
        "ds_read_b128 v[54:57], v0 offset:896\n"
        "v_cvt_pk_bf16_f32 v98, v82, v83\n"
        "v_cvt_pk_bf16_f32 v99, v84, v85\n"
        "v_cvt_pk_bf16_f32 v27, v40, v41\n"
        "v_mfma_f32_16x16x32_bf16 v[82:85], v[104:107], v[104:107], 0\n"
        "v_cvt_pk_bf16_f32 v30, v78, v79\n"
        "v_cvt_pk_bf16_f32 v31, v80, v81\n"
        "v_mfma_f32_16x16x32_bf16 v[82:85], v[108:111], v[108:111], v[82:85]\n"
        "v_cvt_pk_bf16_f32 v32, v34, v35\n"
        "v_cvt_pk_bf16_f32 v33, v36, v37\n"
        "v_mfma_f32_16x16x32_bf16 v[82:85], v[96:99], v[96:99], v[82:85]\n"
        "v_mfma_f32_16x16x32_bf16 v[76:79], v[20:23], v[20:23], 0\n"
        "v_mfma_f32_16x16x32_bf16 v[76:79], v[24:27], v[24:27], v[76:79]\n"
        "v_mfma_f32_16x16x32_bf16 v[76:79], v[30:33], v[30:33], v[76:79]\n"
        "s_cmp_lt_u32 s30, s14\n"
        "s_branch PTOP_%=\n"
        ".p2align 6\n"
        "s_nop 0\n"
        "s_nop 0\n"
        "s_nop 0\n"
        "s_nop 0\n"
        "PTOP_%=:\n"
        "s_waitcnt lgkmcnt(5)\n"
        "v_mfma_f32_16x16x32_bf16 v[34:37], v[112:115], v[60:63], 0\n"
        "s_mov_b32 m0, s17\n"
        "v_cndmask_b32_e64 v88, v82, v83, s[48:49]\n"
        "s_nop 1\n"
        "v_cndmask_b32_e64 v89, v76, v77, s[48:49]\n"
        "s_waitcnt lgkmcnt(3)\n"
        "v_mfma_f32_16x16x32_bf16 v[34:37], v[116:119], v[66:69], v[34:37]\n"
        "ds_read_b32 v101, v102 offset:8\n"
        "v_cndmask_b32_e64 v88, v88, v84, s[50:51]\n"
        "s_waitcnt lgkmcnt(2)\n"
        "v_mfma_f32_16x16x32_bf16 v[34:37], v[120:123], v[72:75], v[34:37]\n"
        "s_waitcnt lgkmcnt(1)\n"
        "buffer_load_dwordx4 v15, s[8:11], s38 offen lds\n"
        "v_mfma_f32_16x16x32_bf16 v[38:41], v[132:135], v[72:75], 0\n"
        "v_cndmask_b32_e64 v89, v89, v78, s[50:51]\n"
        "v_cndmask_b32_e64 v88, v88, v85, s[52:53]\n"
        "v_mfma_f32_16x16x32_bf16 v[38:41], v[128:131], v[66:69], v[38:41]\n"
        "s_setprio 3 \n"
        "v_cndmask_b32_e64 v89, v89, v79, s[52:53]\n"
        "v_mfma_f32_16x16x32_bf16 v[38:41], v[124:127], v[60:63], v[38:41]\n"
        "buffer_load_dwordx4 v100, s[8:11], s38 offen offset:1024 lds\n"
        "v_mfma_f32_16x16x32_bf16 v[78:81], v[136:139], v[60:63], 0\n"
        "s_waitcnt lgkmcnt(0)\n"
        "v_readfirstlane_b32 s40, v101\n"
        "v_cndmask_b32_dpp v88, v89, v88, vcc row_ror:4 row_mask:0xf bank_mask:0xf\n"
        "v_mfma_f32_16x16x32_bf16 v[78:81], v[140:143], v[66:69], v[78:81]\n"
        "s_cmp_lt_i32 s40, s68\n"
        "v_mfma_f32_16x16x32_bf16 v[78:81], v[144:147], v[72:75], v[78:81]\n"
        "s_cbranch_scc1 L3_%=\n"
        "L4_%=:\n"
        "ds_write_b128 v11, v[104:107]\n"
        "s_mov_b64 exec, s[46:47]\n"
        "ds_write_b32 v12, v88\n"
        "s_mov_b64 exec, -1\n"
        "v_mfma_f32_16x16x32_bf16 v[82:85], v[156:159], v[72:75], 0\n"
        "v_mfma_f32_16x16x32_bf16 v[82:85], v[152:155], v[66:69], v[82:85]\n"
        "v_mfma_f32_16x16x32_bf16 v[82:85], v[148:151], v[60:63], v[82:85]\n"
        "buffer_load_dwordx4 v103, s[8:11], s38 offen offset:2048 lds\n"
        "s_mov_b32 m0, s71\n"
        "ds_write_b128 v11, v[20:23] offset:832\n"
        "s_cmp_eq_u32 s72, s30\n"
        "v_mfma_f32_16x16x32_bf16 v[104:107], v[160:163], v[60:63], 0\n"
        "s_setprio 3\n"
        "s_cbranch_scc1 L5_%=\n"
        "s_setprio 1\n"
        "L5_%=:\n"
        "v_mfma_f32_16x16x32_bf16 v[104:107], v[164:167], v[66:69], v[104:107]\n"
        "v_mfma_f32_16x16x32_bf16 v[104:107], v[168:171], v[72:75], v[104:107]\n"
        "buffer_load_dwordx4 v15, s[8:11], s39 offen lds\n"
        "ds_write_b128 v11, v[108:111] offset:64\n"
        "v_mfma_f32_16x16x32_bf16 v[108:111], v[180:183], v[72:75], 0\n"
        "v_mfma_f32_16x16x32_bf16 v[108:111], v[176:179], v[66:69], v[108:111]\n"
        "v_mfma_f32_16x16x32_bf16 v[108:111], v[172:175], v[60:63], v[108:111]\n"
        "buffer_load_dwordx4 v100, s[8:11], s39 offen offset:1024 lds\n"
        "ds_write_b128 v11, v[24:27] offset:896\n"
        "v_mfma_f32_16x16x32_bf16 v[20:23], v[60:63], v[60:63], 0\n"
        "v_mfma_f32_16x16x32_bf16 v[20:23], v[66:69], v[66:69], v[20:23]\n"
        "v_mfma_f32_16x16x32_bf16 v[20:23], v[72:75], v[72:75], v[20:23]\n"
        "buffer_load_dwordx4 v103, s[8:11], s39 offen offset:2048 lds\n"
        "ds_write_b128 v11, v[96:99] offset:128\n"
        "v_mov_b64 v[96:97], 0\n"
        "v_mov_b64 v[98:99], 0\n"
        "v_mfma_f32_16x16x32_bf16 v[24:27], v[42:45], v[42:45], 0\n"
        "v_mfma_f32_16x16x32_bf16 v[24:27], v[48:51], v[48:51], v[24:27]\n"
        "v_mfma_f32_16x16x32_bf16 v[24:27], v[54:57], v[54:57], v[24:27]\n"
        "ds_write_b128 v11, v[30:33] offset:960\n"
        "v_smfmac_f32_16x16x64_bf16 v[96:99], v[60:63], v[34:41], v10\n"
        "v_cndmask_b32_e64 v93, v20, v21, s[48:49]\n"
        "v_smfmac_f32_16x16x64_bf16 v[96:99], v[66:69], v[78:85], v10\n"
        "v_cndmask_b32_e64 v93, v93, v22, s[50:51]\n"
        "v_smfmac_f32_16x16x64_bf16 v[96:99], v[72:75], v[104:111], v10\n"
        "s_mov_b64 exec, 2\n"
        "ds_inc_u32 v102, v0 offset:0\n"
        "s_mov_b64 exec, -1\n"
        "v_mfma_f32_16x16x32_bf16 v[34:37], v[112:115], v[42:45], 0\n"
        "v_cndmask_b32_e64 v111, v24, v25, s[48:49]\n"
        "v_cndmask_b32_e64 v111, v111, v26, s[50:51]\n"
        "v_mfma_f32_16x16x32_bf16 v[34:37], v[116:119], v[48:51], v[34:37]\n"
        "s_and_b32 s69, s30, 1\n"
        "v_cndmask_b32_e64 v93, v93, v23, s[52:53]\n"
        "v_cndmask_b32_e64 v111, v111, v27, s[52:53]\n"
        "v_mfma_f32_16x16x32_bf16 v[34:37], v[120:123], v[54:57], v[34:37]\n"
        "s_lshl2_add_u32 s70, s69, 21600\n"
        "v_mov_b32 v102, s70\n"
        "v_cndmask_b32_dpp v93, v111, v93, vcc row_ror:4 row_mask:0xf bank_mask:0xf\n"
        "v_mfma_f32_16x16x32_bf16 v[38:41], v[132:135], v[54:57], 0\n"
        "s_mul_i32 s69, s69, 40064\n"
        "s_mul_i32 s70, s31, 1664\n"
        "v_mfma_f32_16x16x32_bf16 v[38:41], v[128:131], v[48:51], v[38:41]\n"
        "s_add_u32 s69, s69, s70\n"
        "v_add_u32 v11, s69, v3\n"
        "v_mfma_f32_16x16x32_bf16 v[38:41], v[124:127], v[42:45], v[38:41]\n"
        "v_add_u32 v12, s69, v2\n"
        "s_lshr_b32 s68, s30, 1\n"
        "v_mfma_f32_16x16x32_bf16 v[104:107], v[136:139], v[42:45], 0\n"
        "s_lshl2_add_u32 s68, s68, 4\n"
        "s_cmp_eq_u32 s31, 6\n"
        "s_cbranch_scc1 L8_%=\n"
        "L9_%=:\n"
        "v_mfma_f32_16x16x32_bf16 v[104:107], v[140:143], v[48:51], v[104:107]\n"
        "s_mov_b32 s72, s30\n"
        "s_mov_b32 s30, s34\n"
        "v_mfma_f32_16x16x32_bf16 v[104:107], v[144:147], v[54:57], v[104:107]\n"
        "s_mov_b32 s31, s35\n"
        "s_add_u32 s37, s37, 4\n"
        "v_mfma_f32_16x16x32_bf16 v[108:111], v[156:159], v[54:57], 0\n"
        "s_cmp_ge_u32 s37, 49\n"
        "s_cselect_b32 s69, 49, 0\n"
        "v_mfma_f32_16x16x32_bf16 v[108:111], v[152:155], v[48:51], v[108:111]\n"
        "s_sub_u32 s37, s37, s69\n"
        "s_sub_u32 s69, s37, 4\n"
        "s_cmp_lt_u32 s69, 4\n"
        "v_mfma_f32_16x16x32_bf16 v[108:111], v[148:151], v[42:45], v[108:111]\n"
        "s_subb_u32 s36, s36, -4\n"
        "s_cmp_ge_u32 s36, 6\n"
        "v_mfma_f32_16x16x32_bf16 v[76:79], v[160:163], v[42:45], 0\n"
        "s_cselect_b32 s69, 6, 0\n"
        "s_addc_u32 s34, s34, 0\n"
        "v_mfma_f32_16x16x32_bf16 v[76:79], v[164:167], v[48:51], v[76:79]\n"
        "s_sub_u32 s36, s36, s69\n"
        "s_cmp_ge_u32 s34, s74\n"
        "v_mfma_f32_16x16x32_bf16 v[76:79], v[168:171], v[54:57], v[76:79]\n"
        "s_cbranch_scc1 L10_%=\n"
        "L11_%=:\n"
        "s_mul_i32 s38, s34, 256\n"
        "v_mfma_f32_16x16x32_bf16 v[80:83], v[180:183], v[54:57], 0\n"
        "s_add_u32 s38, s38, s16\n"
        "s_cmp_ge_u32 s34, s43\n"
        "s_cselect_b32 s38, s22, s38\n"
        "v_mfma_f32_16x16x32_bf16 v[80:83], v[176:179], v[48:51], v[80:83]\n"
        "s_cmp_gt_u32 s34, s43\n"
        "s_cselect_b32 s38, s23, s38\n"
        "v_mfma_f32_16x16x32_bf16 v[80:83], v[172:175], v[42:45], v[80:83]\n"
        "s_mul_hi_u32 s70, s38, 58835169\n"
        "s_mul_i32 s38, s38, 1344\n"
        "v_mfma_f32_16x16x32_bf16 v[20:23], v[16:19], v[60:63], 0\n"
        "s_mul_i32 s70, s70, 588672\n"
        "s_add_u32 s38, s38, s70\n"
        "v_mfma_f32_16x16x32_bf16 v[20:23], v[16:19], v[66:69], v[20:23]\n"
        "s_mul_i32 s69, s36, 98304\n"
        "s_add_u32 s38, s38, s69\n"
        "s_mov_b32 s35, s36\n"
        "v_mfma_f32_16x16x32_bf16 v[20:23], v[16:19], v[72:75], v[20:23]\n"
        "s_mul_i32 s69, s36, -12\n"
        "v_mov_b64 v[30:31], 0\n"
        "v_mov_b64 v[32:33], 0\n"
        "v_mfma_f32_16x16x32_bf16 v[24:27], v[16:19], v[42:45], 0\n"
        "s_add_u32 s69, s69, 60\n"
        "s_lshl_b64 s[54:55], -1, s69\n"
        "s_lshl_b64 s[56:57], s[54:55], 24\n"
        "v_mfma_f32_16x16x32_bf16 v[24:27], v[16:19], v[48:51], v[24:27]\n"
        "s_ashr_i64 s[60:61], s[54:55], 8\n"
        "s_mov_b32 s58, s61\n"
        "v_mfma_f32_16x16x32_bf16 v[24:27], v[16:19], v[54:57], v[24:27]\n"
        "s_mov_b32 s59, s56\n"
        "v_cndmask_b32_e64 v15, v4, v7, s[56:57]\n"
        "v_smfmac_f32_16x16x64_bf16 v[30:33], v[42:45], v[34:41], v10\n"
        "v_cndmask_b32_e64 v100, v5, v8, s[58:59]\n"
        "v_smfmac_f32_16x16x64_bf16 v[30:33], v[48:51], v[104:111], v10\n"
        "v_cndmask_b32_e64 v103, v6, v9, s[60:61]\n"
        "v_smfmac_f32_16x16x64_bf16 v[30:33], v[54:57], v[76:83], v10\n"
        "s_cmp_eq_u32 s37, 3\n"
        "s_cbranch_scc1 L12_%=\n"
        "L13_%=:\n"
        "v_cndmask_b32_dpp v110, v24, v20, vcc row_ror:4 row_mask:0xf bank_mask:0xf\n"
        "v_mfma_f32_16x16x32_bf16 v[36:39], v[54:57], v[192:195], 0\n"
        "s_cmp_lt_u32 s34, s14\n"
        "v_mul_f32 v111, s28, v110\n"
        "v_fma_f32 v111, -v111, v110, v93\n"
        "v_mfma_f32_16x16x32_bf16 v[36:39], v[48:51], v[188:191], v[36:39]\n"
        "s_cselect_b32 s38, s38, 0x80000000\n"
        "s_add_u32 s39, s38, 100270464\n"
        "v_add_f32_e64 v111, s29, |v111|\n"
        "v_mfma_f32_16x16x32_bf16 v[36:39], v[42:45], v[184:187], v[36:39]\n"
        "v_rsq_f32 v84, v111\n"
        "v_cndmask_b32_dpp v104, v30, v96, vcc row_ror:4 row_mask:0xf bank_mask:0xf\n"
        "v_mfma_f32_16x16x32_bf16 v[24:27], v[72:75], v[192:195], 0\n"
        "v_cndmask_b32_e64 v84, 0, v84, s[46:47]\n"
        "v_cndmask_b32_dpp v105, v31, v97, vcc row_ror:4 row_mask:0xf bank_mask:0xf\n"
        "v_mfma_f32_16x16x32_bf16 v[24:27], v[66:69], v[188:191], v[24:27]\n"
        "v_cndmask_b32_dpp v85, v32, v98, vcc row_ror:4 row_mask:0xf bank_mask:0xf\n"
        "v_mul_f32_dpp v104, v84, v104 quad_perm:[0,0,0,0] row_mask:0xf bank_mask:0xf\n"
        "v_mfma_f32_16x16x32_bf16 v[24:27], v[60:63], v[184:187], v[24:27]\n"
        "v_mul_f32_dpp v105, v84, v105 quad_perm:[1,1,1,1] row_mask:0xf bank_mask:0xf\n"
        "v_mul_f32_dpp v85, v84, v85 quad_perm:[2,2,2,2] row_mask:0xf bank_mask:0xf\n"
        "v_mfma_f32_16x16x32_bf16 v[20:23], v[60:63], v[220:223], 0\n"
        "v_cndmask_b32_dpp v106, v33, v99, vcc row_ror:4 row_mask:0xf bank_mask:0xf\n"
        "v_max3_f32 v107, v104, v105, v85\n"
        "v_mfma_f32_16x16x32_bf16 v[20:23], v[66:69], v[224:227], v[20:23]\n"
        "v_mul_f32_dpp v106, v84, v106 quad_perm:[3,3,3,3] row_mask:0xf bank_mask:0xf\n"
        "v_max_f32 v107, v107, v106\n"
        "v_mfma_f32_16x16x32_bf16 v[20:23], v[72:75], v[228:231], v[20:23]\n"
        "v_mul_f32 v107, v107, v84\n"
        "v_fma_f32 v104, v104, v84, -v107\n"
        "v_fma_f32 v105, v105, v84, -v107\n"
        "v_mfma_f32_16x16x32_bf16 v[32:35], v[42:45], v[220:223], 0\n"
        "v_fma_f32 v85, v85, v84, -v107\n"
        "v_fma_f32 v106, v106, v84, -v107\n"
        "v_mfma_f32_16x16x32_bf16 v[32:35], v[48:51], v[224:227], v[32:35]\n"
        "v_exp_f32 v104, v104\n"
        "v_mfma_f32_16x16x32_bf16 v[32:35], v[54:57], v[228:231], v[32:35]\n"
        "v_exp_f32 v105, v105\n"
        "v_mfma_f32_16x16x32_bf16 v[108:111], v[54:57], v[204:207], 0\n"
        "v_exp_f32 v85, v85\n"
        "v_mfma_f32_16x16x32_bf16 v[108:111], v[48:51], v[200:203], v[108:111]\n"
        "v_exp_f32 v106, v106\n"
        "v_mfma_f32_16x16x32_bf16 v[108:111], v[42:45], v[196:199], v[108:111]\n"
        "v_add_f32 v107, v104, v105\n"
        "v_add_f32 v101, v85, v106\n"
        "v_add_f32 v107, v107, v101\n"
        "v_mfma_f32_16x16x32_bf16 v[80:83], v[72:75], v[204:207], 0\n"
        "v_rcp_f32 v107, v107\n"
        "v_mfma_f32_16x16x32_bf16 v[80:83], v[66:69], v[200:203], v[80:83]\n"
        "v_mul_f32_dpp v104, v84, v104 quad_perm:[0,0,0,0] row_mask:0xf bank_mask:0xf\n"
        "v_mul_f32_dpp v105, v84, v105 quad_perm:[1,1,1,1] row_mask:0xf bank_mask:0xf\n"
        "v_mfma_f32_16x16x32_bf16 v[80:83], v[60:63], v[196:199], v[80:83]\n"
        "v_mul_f32_dpp v85, v84, v85 quad_perm:[2,2,2,2] row_mask:0xf bank_mask:0xf\n"
        "v_mul_f32_dpp v106, v84, v106 quad_perm:[3,3,3,3] row_mask:0xf bank_mask:0xf\n"
        "v_mul_f32 v104, v104, v107\n"
        "v_mfma_f32_16x16x32_bf16 v[76:79], v[60:63], v[232:235], 0\n"
        "v_mul_f32 v105, v105, v107\n"
        "v_mul_f32 v85, v85, v107\n"
        "v_mfma_f32_16x16x32_bf16 v[76:79], v[66:69], v[236:239], v[76:79]\n"
        "v_mul_f32 v106, v106, v107\n"
        "v_cvt_pk_bf16_f32 v99, v104, v105\n"
        "v_mfma_f32_16x16x32_bf16 v[76:79], v[72:75], v[240:243], v[76:79]\n"
        "v_cvt_pk_bf16_f32 v101, v85, v106\n"
        "v_and_b32 v58, v1, v99\n"
        "v_and_b32 v59, v1, v101\n"
        "v_mfma_f32_16x16x32_bf16 v[104:107], v[42:45], v[232:235], 0\n"
        "v_cvt_pk_bf16_f32 v92, v24, v25\n"
        "v_cvt_pk_bf16_f32 v93, v26, v27\n"
        "v_mfma_f32_16x16x32_bf16 v[104:107], v[48:51], v[236:239], v[104:107]\n"
        "v_and_b32_dpp v40, v99, v1 row_ror:12 row_mask:0xf bank_mask:0xf\n"
        "v_and_b32_dpp v41, v101, v1 row_ror:12 row_mask:0xf bank_mask:0xf\n"
        "v_mfma_f32_16x16x32_bf16 v[104:107], v[54:57], v[240:243], v[104:107]\n"
        "v_cvt_pk_bf16_f32 v84, v36, v37\n"
        "v_cvt_pk_bf16_f32 v85, v38, v39\n"
        "v_mov_b64 v[70:71], v[58:59]\n"
        "v_mfma_f32_16x16x32_bf16 v[36:39], v[92:95], v[58:61], 0\n"
        "v_cvt_pk_bf16_f32 v96, v20, v21\n"
        "v_cvt_pk_bf16_f32 v97, v22, v23\n"
        "v_mfma_f32_16x16x32_bf16 v[22:25], v[84:87], v[40:43], 0\n"
        "v_mov_b64 v[52:53], v[40:41]\n"
        "v_cvt_pk_bf16_f32 v88, v32, v33\n"
        "v_cvt_pk_bf16_f32 v89, v34, v35\n"
        "v_mfma_f32_16x16x32_bf16 v[32:35], v[94:97], v[68:71], 0\n"
        "v_mov_b64 v[64:65], v[58:59]\n"
        "v_cvt_pk_bf16_f32 v30, v80, v81\n"
        "v_mfma_f32_16x16x32_bf16 v[96:99], v[86:89], v[50:53], 0\n"
        "v_cvt_pk_bf16_f32 v31, v82, v83\n"
        "v_mov_b64 v[46:47], v[40:41]\n"
        "v_mfma_f32_16x16x32_bf16 v[80:83], v[54:57], v[216:219], 0\n"
        "v_cvt_pk_bf16_f32 v92, v108, v109\n"
        "v_cvt_pk_bf16_f32 v93, v110, v111\n"
        "v_mfma_f32_16x16x32_bf16 v[80:83], v[48:51], v[212:215], v[80:83]\n"
        "v_cvt_pk_bf16_f32 v26, v76, v77\n"
        "v_cvt_pk_bf16_f32 v27, v78, v79\n"
        "v_mfma_f32_16x16x32_bf16 v[80:83], v[42:45], v[208:211], v[80:83]\n"
        "v_cvt_pk_bf16_f32 v88, v104, v105\n"
        "v_cvt_pk_bf16_f32 v89, v106, v107\n"
        "v_mfma_f32_16x16x32_bf16 v[108:111], v[72:75], v[216:219], 0\n"
        "v_mov_b64 v[76:77], v[58:59]\n"
        "v_mov_b64 v[58:59], v[40:41]\n"
        "v_mfma_f32_16x16x32_bf16 v[108:111], v[66:69], v[212:215], v[108:111]\n"
        "v_cvt_pk_bf16_f32 v104, v36, v37\n"
        "v_cvt_pk_bf16_f32 v105, v38, v39\n"
        "v_mfma_f32_16x16x32_bf16 v[108:111], v[60:63], v[208:211], v[108:111]\n"
        "v_cvt_pk_bf16_f32 v84, v80, v81\n"
        "v_cvt_pk_bf16_f32 v85, v82, v83\n"
        "v_mfma_f32_16x16x32_bf16 v[80:83], v[28:31], v[62:65], 0\n"
        "v_cvt_pk_bf16_f32 v106, v32, v33\n"
        "v_cvt_pk_bf16_f32 v107, v34, v35\n"
        "v_mfma_f32_16x16x32_bf16 v[32:35], v[90:93], v[44:47], 0\n"
        "v_cvt_pk_bf16_f32 v20, v22, v23\n"
        "v_cvt_pk_bf16_f32 v21, v24, v25\n"
        "v_mfma_f32_16x16x32_bf16 v[38:41], v[60:63], v[244:247], 0\n"
        "v_cvt_pk_bf16_f32 v92, v108, v109\n"
        "v_cvt_pk_bf16_f32 v93, v110, v111\n"
        "v_mfma_f32_16x16x32_bf16 v[38:41], v[66:69], v[248:251], v[38:41]\n"
        "v_cvt_pk_bf16_f32 v108, v80, v81\n"
        "v_cvt_pk_bf16_f32 v109, v82, v83\n"
        "v_mfma_f32_16x16x32_bf16 v[38:41], v[72:75], v[252:255], v[38:41]\n"
        "v_cvt_pk_bf16_f32 v22, v96, v97\n"
        "v_cvt_pk_bf16_f32 v23, v98, v99\n"
        "v_mfma_f32_16x16x32_bf16 v[80:83], v[42:45], v[244:247], 0\n"
        "v_cvt_pk_bf16_f32 v24, v32, v33\n"
        "v_cvt_pk_bf16_f32 v25, v34, v35\n"
        "v_mfma_f32_16x16x32_bf16 v[80:83], v[48:51], v[248:251], v[80:83]\n"
        "v_mfma_f32_16x16x32_bf16 v[80:83], v[54:57], v[252:255], v[80:83]\n"
        "v_mfma_f32_16x16x32_bf16 v[34:37], v[26:29], v[70:73], 0\n"
        "v_cvt_pk_bf16_f32 v96, v38, v39\n"
        "v_cvt_pk_bf16_f32 v97, v40, v41\n"
        "v_mfma_f32_16x16x32_bf16 v[38:41], v[88:91], v[52:55], 0\n"
        "v_mfma_f32_16x16x32_bf16 v[30:33], v[92:95], v[64:67], 0\n"
        "s_waitcnt vmcnt(0)\n"
        "ds_read_b128 v[60:63], v0\n"
        "ds_read_b128 v[42:45], v0 offset:768\n"
        "ds_read_b128 v[66:69], v0 offset:64\n"
        "v_cvt_pk_bf16_f32 v88, v80, v81\n"
        "v_mfma_f32_16x16x32_bf16 v[78:81], v[84:87], v[46:49], 0\n"
        "s_add_u32 s73, s73, 1\n"
        "ds_read_b128 v[48:51], v0 offset:832\n"
        "v_cvt_pk_bf16_f32 v89, v82, v83\n"
        "v_cvt_pk_bf16_f32 v110, v34, v35\n"
        "v_cvt_pk_bf16_f32 v111, v36, v37\n"
        "v_mfma_f32_16x16x32_bf16 v[82:85], v[94:97], v[74:77], 0\n"
        "s_cmp_lt_u32 s73, 4\n"
        "ds_read_b128 v[72:75], v0 offset:128\n"
        "v_cvt_pk_bf16_f32 v96, v30, v31\n"
        "v_cvt_pk_bf16_f32 v97, v32, v33\n"
        "v_cvt_pk_bf16_f32 v26, v38, v39\n"
        "v_mfma_f32_16x16x32_bf16 v[34:37], v[86:89], v[56:59], 0\n"
        "s_cbranch_scc1 L19_%=\n"
        "L20_%=:\n"
        "ds_read_b128 v[54:57], v0 offset:896\n"
        "v_cvt_pk_bf16_f32 v98, v82, v83\n"
        "v_cvt_pk_bf16_f32 v99, v84, v85\n"
        "v_cvt_pk_bf16_f32 v27, v40, v41\n"
        "v_mfma_f32_16x16x32_bf16 v[82:85], v[104:107], v[104:107], 0\n"
        "v_cvt_pk_bf16_f32 v30, v78, v79\n"
        "v_cvt_pk_bf16_f32 v31, v80, v81\n"
        "v_mfma_f32_16x16x32_bf16 v[82:85], v[108:111], v[108:111], v[82:85]\n"
        "v_cvt_pk_bf16_f32 v32, v34, v35\n"
        "v_cvt_pk_bf16_f32 v33, v36, v37\n"
        "v_mfma_f32_16x16x32_bf16 v[82:85], v[96:99], v[96:99], v[82:85]\n"
        "v_mfma_f32_16x16x32_bf16 v[76:79], v[20:23], v[20:23], 0\n"
        "v_mfma_f32_16x16x32_bf16 v[76:79], v[24:27], v[24:27], v[76:79]\n"
        "v_mfma_f32_16x16x32_bf16 v[76:79], v[30:33], v[30:33], v[76:79]\n"
        "s_cmp_lt_u32 s30, s14\n"
        "s_cbranch_scc1 PTOP_%=\n"
        "s_waitcnt lgkmcnt(0)\n"
        "s_nop 15\n"
        "L21_%=:\n"
        "ds_read_b32 v101, v102 offset:8\n"
        "s_waitcnt lgkmcnt(0)\n"
        "v_readfirstlane_b32 s40, v101\n"
        "s_nop 3\n"
        "s_cmp_ge_i32 s40, s68\n"
        "s_cbranch_scc1 L22_%=\n"
        "s_sleep 1\n"
        "s_sub_u32 s41, s41, 1\n"
        "s_cbranch_scc0 L21_%=\n"
        "s_endpgm\n"
        "L22_%=:\n"
        "v_cndmask_b32_e64 v101, v82, v83, s[48:49]\n"
        "v_cndmask_b32_e64 v93, v76, v77, s[48:49]\n"
        "v_cndmask_b32_e64 v101, v101, v84, s[50:51]\n"
        "v_cndmask_b32_e64 v93, v93, v78, s[50:51]\n"
        "v_cndmask_b32_e64 v101, v101, v85, s[52:53]\n"
        "v_cndmask_b32_e64 v93, v93, v79, s[52:53]\n"
        "ds_write_b128 v11, v[104:107]\n"
        "ds_write_b128 v11, v[20:23] offset:832\n"
        "v_cndmask_b32_dpp v101, v93, v101, vcc row_ror:4 row_mask:0xf bank_mask:0xf\n"
        "ds_write_b128 v11, v[108:111] offset:64\n"
        "ds_write_b128 v11, v[24:27] offset:896\n"
        "ds_write_b128 v11, v[96:99] offset:128\n"
        "ds_write_b128 v11, v[30:33] offset:960\n"
        "s_mov_b64 exec, s[46:47]\n"
        "ds_write_b32 v12, v101\n"
        "s_mov_b64 exec, -1\n"
        "s_mov_b64 exec, 2\n"
        "ds_inc_u32 v102, v0 offset:0\n"
        "s_mov_b64 exec, -1\n"
        "s_xor_b32 s40, s2, 0\n"
        "s_or_b32 s40, s18, s40\n"
        "s_cbranch_scc1 L35_%=\n"
        "L34_%=:\n"
        "s_mov_b32 exec_hi, 0\n"
        "v_mbcnt_lo_u32_b32 v20, -1, 0\n"
        "v_lshlrev_b32 v20, 7, v20\n"
        "v_mov_b32 v21, 1\n"
        "global_atomic_add v20, v21, s[24:25] offset:256\n"
        "s_mov_b32 exec_hi, -1\n"
        "s_mov_b64 exec, 1\n"
        "v_mov_b32 v20, 0\n"
        "v_mov_b32 v21, 0\n"
        "global_atomic_add v22, v20, v21, s[24:25] offset:0 sc0\n"
        "s_mov_b64 exec, -1\n"
        "s_waitcnt vmcnt(0)\n"
        "v_readfirstlane_b32 s40, v22\n"
        "s_nop 3\n"
        "s_cmp_ge_u32 s40, 512\n"
        "s_cbranch_scc1 L35_%=\n"
        "s_sleep 8\n"
        "s_sub_u32 s41, s41, 64\n"
        "s_cbranch_scc0 L34_%=\n"
        "L35_%=:\n"
        "s_endpgm\n"
        "L3_%=:\n"
        "s_nop 7\n"
        "L1_%=:\n"
        "ds_read_b32 v101, v102 offset:8\n"
        "s_waitcnt lgkmcnt(0)\n"
        "v_readfirstlane_b32 s40, v101\n"
        "s_nop 3\n"
        "s_cmp_ge_i32 s40, s68\n"
        "s_cbranch_scc1 L2_%=\n"
        "s_sleep 1\n"
        "s_sub_u32 s41, s41, 1\n"
        "s_cbranch_scc0 L1_%=\n"
        "s_endpgm\n"
        "L2_%=:\n"
        "s_nop 1\n"
        "s_branch L4_%=\n"
        "L8_%=:\n"
        "s_nop 7\n"
        "ds_read_u16 v11, v13 offset:19564\n"
        "ds_read_u16 v12, v13 offset:19566\n"
        "s_cmp_ge_u32 s30, 10\n"
        "s_cselect_b32 s69, 10, 0\n"
        "s_cmp_ge_u32 s30, 20\n"
        "s_cselect_b32 s69, 20, s69\n"
        "s_sub_u32 s69, s30, s69\n"
        "s_mul_i32 s69, s69, 832\n"
        "s_waitcnt lgkmcnt(0)\n"
        "v_add_u32 v11, s69, v11\n"
        "v_subrev_u32 v101, 8320, v11\n"
        "v_min_u32 v11, v11, v101\n"
        "v_add_u32 v11, 101856, v11\n"
        "v_add_u32 v12, s69, v12\n"
        "v_subrev_u32 v101, 8320, v12\n"
        "v_min_u32 v12, v12, v101\n"
        "v_add_u32 v12, 101856, v12\n"
        "s_mov_b32 s69, 21660\n"
        "v_writelane_b32 v102, s69, 1\n"
        "s_nop 1\n"
        "s_branch L9_%=\n"
        "L10_%=:\n"
        "s_nop 7\n"
        "v_mov_b32 v101, 21600\n"
        "L6_%=:\n"
        "ds_read_b32 v92, v101 offset:64\n"
        "s_waitcnt lgkmcnt(0)\n"
        "v_readfirstlane_b32 s40, v92\n"
        "s_nop 3\n"
        "s_cmp_ge_i32 s40, 1\n"
        "s_cbranch_scc1 L7_%=\n"
        "s_sleep 1\n"
        "s_sub_u32 s41, s41, 1\n"
        "s_cbranch_scc0 L6_%=\n"
        "s_endpgm\n"
        "L7_%=:\n"
        "v_mbcnt_lo_u32_b32 v92, -1, 0\n"
        "v_lshl_add_u32 v101, v92, 2, v101\n"
        "ds_read_b32 v92, v101 offset:68\n"
        "s_mov_b32 s14, s40\n"
        "s_mov_b32 s74, -1\n"
        "s_waitcnt lgkmcnt(0)\n"
        "v_readlane_b32 s22, v92, 0\n"
        "v_readlane_b32 s23, v92, 1\n"
        "s_nop 3\n"
        "s_nop 1\n"
        "s_branch L11_%=\n"
        "L12_%=:\n"
        "s_nop 7\n"
        "ds_read_b32 v15, v13 offset:19552\n"
        "ds_read_b32 v100, v13 offset:19556\n"
        "ds_read_b32 v103, v13 offset:19560\n"
        "s_mov_b32 s35, 6\n"
        "s_mov_b32 s38, 0\n"
        "s_mov_b32 s69, 1344\n"
        "s_waitcnt lgkmcnt(0)\n"
        "v_lshrrev_b32 v101, 28, v15\n"
        "v_add_u32 v101, s34, v101\n"
        "v_and_b32 v15, 0x0fffffff, v15\n"
        "v_mul_u32_u24 v92, 256, v101\n"
        "v_add_u32 v92, s16, v92\n"
        "v_cmpx_le_u32 vcc, s43, v101\n"
        "v_mov_b32 v92, s22\n"
        "s_mov_b64 exec, -1\n"
        "v_cmpx_lt_u32 vcc, s43, v101\n"
        "v_mov_b32 v92, s23\n"
        "s_mov_b64 exec, -1\n"
        "v_mad_u32_u24 v15, v92, s69, v15\n"
        "v_mul_u32_u24 v92, 57457, v92\n"
        "v_lshrrev_b32 v92, 22, v92\n"
        "v_mul_u32_u24 v92, 588672, v92\n"
        "v_add_u32 v15, v15, v92\n"
        "v_lshrrev_b32 v101, 28, v100\n"
        "v_add_u32 v101, s34, v101\n"
        "v_and_b32 v100, 0x0fffffff, v100\n"
        "v_mul_u32_u24 v92, 256, v101\n"
        "v_add_u32 v92, s16, v92\n"
        "v_cmpx_le_u32 vcc, s43, v101\n"
        "v_mov_b32 v92, s22\n"
        "s_mov_b64 exec, -1\n"
        "v_cmpx_lt_u32 vcc, s43, v101\n"
        "v_mov_b32 v92, s23\n"
        "s_mov_b64 exec, -1\n"
        "v_mad_u32_u24 v100, v92, s69, v100\n"
        "v_mul_u32_u24 v92, 57457, v92\n"
        "v_lshrrev_b32 v92, 22, v92\n"
        "v_mul_u32_u24 v92, 588672, v92\n"
        "v_add_u32 v100, v100, v92\n"
        "v_lshrrev_b32 v101, 28, v103\n"
        "v_add_u32 v101, s34, v101\n"
        "v_and_b32 v103, 0x0fffffff, v103\n"
        "v_mul_u32_u24 v92, 256, v101\n"
        "v_add_u32 v92, s16, v92\n"
        "v_cmpx_le_u32 vcc, s43, v101\n"
        "v_mov_b32 v92, s22\n"
        "s_mov_b64 exec, -1\n"
        "v_cmpx_lt_u32 vcc, s43, v101\n"
        "v_mov_b32 v92, s23\n"
        "s_mov_b64 exec, -1\n"
        "v_mad_u32_u24 v103, v92, s69, v103\n"
        "v_mul_u32_u24 v92, 57457, v92\n"
        "v_lshrrev_b32 v92, 22, v92\n"
        "v_mul_u32_u24 v92, 588672, v92\n"
        "v_add_u32 v103, v103, v92\n"
        "s_nop 4\n"
        "s_mov_b64 vcc, s[44:45]\n"
        "s_nop 1\n"
        "s_branch L13_%=\n"
        "L19_%=:\n"
        "s_nop 7\n"
        "v_readfirstlane_b32 s62, v14\n"
        "s_cmp_eq_u32 s73, 1\n"
        "s_cbranch_scc1 L17_%=\n"
        "s_cmp_ge_u32 s73, 2\n"
        "s_cbranch_scc1 L15_%=\n"
        "s_mov_b32 s27, s43\n"
        "s_cmp_lg_u32 s2, 0\n"
        "s_cbranch_scc1 L16_%=\n"
        "s_mov_b32 exec_hi, 0\n"
        "v_mbcnt_lo_u32_b32 v101, -1, 0\n"
        "v_lshlrev_b32 v101, 7, v101\n"
        "v_mov_b32 v92, 1\n"
        "global_atomic_add v101, v92, s[24:25] offset:256\n"
        "s_mov_b32 exec_hi, -1\n"
        "s_branch L16_%=\n"
        "L15_%=:\n"
        "s_cmp_lt_u32 s62, 210\n"
        "s_addc_u32 s27, s27, 0\n"
        "s_min_u32 s62, s62, 209\n"
        "s_add_u32 s62, s62, 5119\n"
        "s_lshl2_add_u32 s64, s73, 21600\n"
        "v_mov_b32 v101, s64\n"
        "v_mov_b32 v92, s62\n"
        "ds_write_b32 v101, v92 offset:60\n"
        "s_waitcnt lgkmcnt(0)\n"
        "s_cmp_lt_u32 s73, 3\n"
        "s_cbranch_scc1 L17_%=\n"
        "v_mov_b32 v92, s27\n"
        "ds_write_b32 v101, v92 offset:52\n"
        "s_waitcnt lgkmcnt(0)\n"
        "s_branch L16_%=\n"
        "L17_%=:\n"
        "s_mov_b64 exec, 1\n"
        "v_mov_b32 v101, 0\n"
        "v_mov_b32 v92, 1\n"
        "global_atomic_add v14, v101, v92, s[24:25] offset:0 sc0\n"
        "s_mov_b64 exec, -1\n"
        "L16_%=:\n"
        "s_nop 1\n"
        "s_branch L20_%=\n"
        "F8_%=:\n"
        "s_nop 7\n"
        "ds_read_u16 v11, v13 offset:19564\n"
        "ds_read_u16 v12, v13 offset:19566\n"
        "s_cmp_ge_u32 s30, 10\n"
        "s_cselect_b32 s69, 10, 0\n"
        "s_cmp_ge_u32 s30, 20\n"
        "s_cselect_b32 s69, 20, s69\n"
        "s_sub_u32 s69, s30, s69\n"
        "s_mul_i32 s69, s69, 832\n"
        "s_waitcnt lgkmcnt(0)\n"
        "v_add_u32 v11, s69, v11\n"
        "v_subrev_u32 v101, 8320, v11\n"
        "v_min_u32 v11, v11, v101\n"
        "v_add_u32 v11, 101856, v11\n"
        "v_add_u32 v12, s69, v12\n"
        "v_subrev_u32 v101, 8320, v12\n"
        "v_min_u32 v12, v12, v101\n"
        "v_add_u32 v12, 101856, v12\n"
        "s_mov_b32 s69, 21660\n"
        "v_writelane_b32 v102, s69, 1\n"
        "s_nop 1\n"
        "s_branch F9_%=\n"
        "F10_%=:\n"
        "s_nop 7\n"
        "v_mov_b32 v101, 21600\n"
        "F6_%=:\n"
        "ds_read_b32 v92, v101 offset:64\n"
        "s_waitcnt lgkmcnt(0)\n"
        "v_readfirstlane_b32 s40, v92\n"
        "s_nop 3\n"
        "s_cmp_ge_i32 s40, 1\n"
        "s_cbranch_scc1 F7_%=\n"
        "s_sleep 1\n"
        "s_sub_u32 s41, s41, 1\n"
        "s_cbranch_scc0 F6_%=\n"
        "s_endpgm\n"
        "F7_%=:\n"
        "v_mbcnt_lo_u32_b32 v92, -1, 0\n"
        "v_lshl_add_u32 v101, v92, 2, v101\n"
        "ds_read_b32 v92, v101 offset:68\n"
        "s_mov_b32 s14, s40\n"
        "s_mov_b32 s74, -1\n"
        "s_waitcnt lgkmcnt(0)\n"
        "v_readlane_b32 s22, v92, 0\n"
        "v_readlane_b32 s23, v92, 1\n"
        "s_nop 3\n"
        "s_nop 1\n"
        "s_branch F11_%=\n"
        "F12_%=:\n"
        "s_nop 7\n"
        "ds_read_b32 v15, v13 offset:19552\n"
        "ds_read_b32 v100, v13 offset:19556\n"
        "ds_read_b32 v103, v13 offset:19560\n"
        "s_mov_b32 s35, 6\n"
        "s_mov_b32 s38, 0\n"
        "s_mov_b32 s69, 1344\n"
        "s_waitcnt lgkmcnt(0)\n"
        "v_lshrrev_b32 v101, 28, v15\n"
        "v_add_u32 v101, s34, v101\n"
        "v_and_b32 v15, 0x0fffffff, v15\n"
        "v_mul_u32_u24 v92, 256, v101\n"
        "v_add_u32 v92, s16, v92\n"
        "v_cmpx_le_u32 vcc, s43, v101\n"
        "v_mov_b32 v92, s22\n"
        "s_mov_b64 exec, -1\n"
        "v_cmpx_lt_u32 vcc, s43, v101\n"
        "v_mov_b32 v92, s23\n"
        "s_mov_b64 exec, -1\n"
        "v_mad_u32_u24 v15, v92, s69, v15\n"
        "v_mul_u32_u24 v92, 57457, v92\n"
        "v_lshrrev_b32 v92, 22, v92\n"
        "v_mul_u32_u24 v92, 588672, v92\n"
        "v_add_u32 v15, v15, v92\n"
        "v_lshrrev_b32 v101, 28, v100\n"
        "v_add_u32 v101, s34, v101\n"
        "v_and_b32 v100, 0x0fffffff, v100\n"
        "v_mul_u32_u24 v92, 256, v101\n"
        "v_add_u32 v92, s16, v92\n"
        "v_cmpx_le_u32 vcc, s43, v101\n"
        "v_mov_b32 v92, s22\n"
        "s_mov_b64 exec, -1\n"
        "v_cmpx_lt_u32 vcc, s43, v101\n"
        "v_mov_b32 v92, s23\n"
        "s_mov_b64 exec, -1\n"
        "v_mad_u32_u24 v100, v92, s69, v100\n"
        "v_mul_u32_u24 v92, 57457, v92\n"
        "v_lshrrev_b32 v92, 22, v92\n"
        "v_mul_u32_u24 v92, 588672, v92\n"
        "v_add_u32 v100, v100, v92\n"
        "v_lshrrev_b32 v101, 28, v103\n"
        "v_add_u32 v101, s34, v101\n"
        "v_and_b32 v103, 0x0fffffff, v103\n"
        "v_mul_u32_u24 v92, 256, v101\n"
        "v_add_u32 v92, s16, v92\n"
        "v_cmpx_le_u32 vcc, s43, v101\n"
        "v_mov_b32 v92, s22\n"
        "s_mov_b64 exec, -1\n"
        "v_cmpx_lt_u32 vcc, s43, v101\n"
        "v_mov_b32 v92, s23\n"
        "s_mov_b64 exec, -1\n"
        "v_mad_u32_u24 v103, v92, s69, v103\n"
        "v_mul_u32_u24 v92, 57457, v92\n"
        "v_lshrrev_b32 v92, 22, v92\n"
        "v_mul_u32_u24 v92, 588672, v92\n"
        "v_add_u32 v103, v103, v92\n"
        "s_nop 4\n"
        "s_mov_b64 vcc, s[44:45]\n"
        "s_nop 1\n"
        "s_branch F13_%=\n"
        "F19_%=:\n"
        "s_nop 7\n"
        "v_readfirstlane_b32 s62, v14\n"
        "s_cmp_eq_u32 s73, 1\n"
        "s_cbranch_scc1 F17_%=\n"
        "s_cmp_ge_u32 s73, 2\n"
        "s_cbranch_scc1 F15_%=\n"
        "s_mov_b32 s27, s43\n"
        "s_cmp_lg_u32 s2, 0\n"
        "s_cbranch_scc1 F16_%=\n"
        "s_mov_b32 exec_hi, 0\n"
        "v_mbcnt_lo_u32_b32 v101, -1, 0\n"
        "v_lshlrev_b32 v101, 7, v101\n"
        "v_mov_b32 v92, 1\n"
        "global_atomic_add v101, v92, s[24:25] offset:256\n"
        "s_mov_b32 exec_hi, -1\n"
        "s_branch F16_%=\n"
        "F15_%=:\n"
        "s_cmp_lt_u32 s62, 210\n"
        "s_addc_u32 s27, s27, 0\n"
        "s_min_u32 s62, s62, 209\n"
        "s_add_u32 s62, s62, 5119\n"
        "s_lshl2_add_u32 s64, s73, 21600\n"
        "v_mov_b32 v101, s64\n"
        "v_mov_b32 v92, s62\n"
        "ds_write_b32 v101, v92 offset:60\n"
        "s_waitcnt lgkmcnt(0)\n"
        "s_cmp_lt_u32 s73, 3\n"
        "s_cbranch_scc1 F17_%=\n"
        "v_mov_b32 v92, s27\n"
        "ds_write_b32 v101, v92 offset:52\n"
        "s_waitcnt lgkmcnt(0)\n"
        "s_branch F16_%=\n"
        "F17_%=:\n"
        "s_mov_b64 exec, 1\n"
        "v_mov_b32 v101, 0\n"
        "v_mov_b32 v92, 1\n"
        "global_atomic_add v14, v101, v92, s[24:25] offset:0 sc0\n"
        "s_mov_b64 exec, -1\n"
        "F16_%=:\n"
        "s_nop 1\n"
        "s_branch F20_%=\n"
        "CONS_%=:\n"
        "s_mov_b32 s41, 0x400000\n"
        "s_mov_b32 s48, 0xffffffff\n"
        "s_mov_b32 s49, 0xffff0000\n"
        "s_mov_b32 s50, 0x33333333\n"
        "s_mov_b32 s51, 0x33333333\n"
        "s_mov_b32 s52, 0x3c2aaaab\n"
        "s_mov_b32 s53, 0x3a7ba882\n"
        "s_mov_b32 s56, 0x0\n"
        "s_mov_b32 s57, 0xfeff0000\n"
        "s_mov_b32 s54, 0x11111111\n"
        "s_mov_b32 s55, 0x11111111\n"
        "s_mov_b32 s63, 0x07060302\n"
        "s_mov_b64 s[44:45], s[12:13]\n"
        "s_and_b32 s45, s45, 0xffff\n"
        "s_mov_b32 s47, 0x20000\n"
        "s_mov_b32 s70, 832\n"
        "s_mov_b32 s71, 17\n"
        "s_sub_u32 s69, s18, 7488\n"
        "s_mov_b32 s28, 0xffff0000\n"
        "s_mov_b32 s31, 0\n"
        "s_add_u32 s35, s15, s17\n"
        "s_mov_b32 s36, 0x20000000\n"
        "L62_%=:\n"
        "ds_read_b32 v251, v250 offset:52\n"
        "s_waitcnt lgkmcnt(0)\n"
        "v_readfirstlane_b32 s40, v251\n"
        "s_nop 3\n"
        "s_cmp_ge_i32 s40, 4\n"
        "s_cbranch_scc1 L63_%=\n"
        "s_nop 0\n"
        "s_sub_u32 s41, s41, 1\n"
        "s_cbranch_scc0 L62_%=\n"
        "s_endpgm\n"
        "L63_%=:\n"
        "s_waitcnt vmcnt(0)\n"
        "v_add_u32 v24, 2, v1\n"
        "v_mov_b32 v25, 0x3f80\n"
        "s_mov_b32 exec_lo, 0\n"
        "s_mov_b32 exec_hi, 0x1000000\n"
        "ds_write_b16 v24, v25\n"
        "s_mov_b64 exec, -1\n"
        "v_add_u32 v24, 61792, v0\n"
        "ds_read_b128 v[168:171], v24 offset:0\n"
        "ds_read_b128 v[172:175], v24 offset:1024\n"
        "ds_read_b128 v[176:179], v24 offset:2048\n"
        "ds_read_b128 v[180:183], v24 offset:3088\n"
        "ds_read_b128 v[184:187], v24 offset:4112\n"
        "ds_read_b128 v[188:191], v24 offset:5136\n"
        "ds_read_b128 v[192:195], v24 offset:6176\n"
        "ds_read_b128 v[196:199], v24 offset:7200\n"
        "ds_read_b128 v[200:203], v24 offset:8224\n"
        "ds_read_b128 v[204:207], v24 offset:9264\n"
        "ds_read_b128 v[208:211], v24 offset:10288\n"
        "ds_read_b128 v[212:215], v24 offset:11312\n"
        "ds_read_b128 v[216:219], v24 offset:12352\n"
        "ds_read_b128 v[220:223], v24 offset:13376\n"
        "ds_read_b128 v[224:227], v24 offset:14400\n"
        "ds_read_b128 v[228:231], v24 offset:15440\n"
        "ds_read_b128 v[232:235], v24 offset:16464\n"
        "ds_read_b128 v[236:239], v24 offset:17488\n"
        "ds_read_b128 v[240:243], v0 offset:0\n"
        "ds_read_b128 v[244:247], v0 offset:1024\n"
        "ds_read_b128 v[248:251], v0 offset:2048\n"
        "ds_read_b128 v[252:255], v0 offset:3088\n"
        "v_mov_b32 v25, 21600\n"
        "s_waitcnt lgkmcnt(0)\n"
        "s_mov_b64 exec, 1\n"
        "ds_inc_u32 v25, v1 offset:12\n"
        "s_mov_b64 exec, -1\n"
        "s_setprio 2\n"
        "s_call_b64 s[64:65], CGRQ_%=\n"
        "s_setprio 0\n"
        "L64_%=:\n"
        "ds_read_b32 v24, v0 offset:21600\n"
        "s_waitcnt lgkmcnt(0)\n"
        "v_readfirstlane_b32 s40, v24\n"
        "s_nop 3\n"
        "s_cmp_ge_i32 s40, 6\n"
        "s_cbranch_scc1 L65_%=\n"
        "s_nop 0\n"
        "s_sub_u32 s41, s41, 1\n"
        "s_cbranch_scc0 L64_%=\n"
        "s_endpgm\n"
        "L65_%=:\n"
        "ds_read_b128 v[124:127], v7 offset:0\n"
        "ds_read_b128 v[132:135], v7 offset:3328\n"
        "ds_read_b128 v[128:131], v7 offset:64\n"
        "ds_read_b128 v[136:139], v7 offset:3392\n"
        "ds_read_b128 v[160:163], v7 offset:128\n"
        "ds_read_b128 v[52:55], v7 offset:3456\n"
        "s_waitcnt lgkmcnt(0)\n"
        "s_branch CTOP_%=\n"
        ".p2align 6\n"
        "s_nop 0\n"
        "s_nop 0\n"
        "s_nop 0\n"
        "s_nop 0\n"
        "s_nop 0\n"
        "s_nop 0\n"
        "s_nop 0\n"
        "s_nop 0\n"
        "s_nop 0\n"
        "s_nop 0\n"
        "s_nop 0\n"
        "s_nop 0\n"
        "s_nop 0\n"
        "s_nop 0\n"
        "s_nop 0\n"
        "CFULL_%=:\n"
        "s_call_b64 s[64:65], CGRP_%=\n"
        "CTOP_%=:\n"
        "s_waitcnt lgkmcnt(5)\n"
        "v_mfma_f32_16x16x32_bf16 v[24:27], v[168:171], v[124:127], 0\n"
        "v_cvt_pk_bf16_f32 v156, v152, v56\n"
        "v_cvt_pk_bf16_f32 v157, v72, v20\n"
        "s_waitcnt lgkmcnt(3)\n"
        "v_mfma_f32_16x16x32_bf16 v[24:27], v[172:175], v[128:131], v[24:27]\n"
        "ds_read_b128 v[44:47], v16 offset:128\n"
        "v_cvt_pk_bf16_f32 v158, v84, v60\n"
        "s_waitcnt lgkmcnt(2)\n"
        "v_mfma_f32_16x16x32_bf16 v[24:27], v[176:179], v[160:163], v[24:27]\n"
        "ds_read_b128 v[48:51], v5\n"
        "v_cvt_pk_bf16_f32 v164, v153, v57\n"
        "v_mfma_f32_16x16x32_bf16 v[32:35], v[188:191], v[160:163], 0\n"
        "v_cvt_pk_bf16_f32 v165, v73, v21\n"
        "v_cvt_pk_bf16_f32 v166, v85, v61\n"
        "v_mfma_f32_16x16x32_bf16 v[32:35], v[184:187], v[128:131], v[32:35]\n"
        "v_cvt_pk_bf16_f32 v146, v154, v58\n"
        "v_cvt_pk_bf16_f32 v147, v74, v22\n"
        "v_mfma_f32_16x16x32_bf16 v[32:35], v[180:183], v[124:127], v[32:35]\n"
        "s_add_u32 s37, s36, 196224\n"
        "ds_read_b128 v[92:95], v7 offset:6656\n"
        "ds_read_b128 v[96:99], v7 offset:6720\n"
        "ds_read_b128 v[116:119], v7 offset:6784\n"
        "v_mfma_f32_16x16x32_bf16 v[76:79], v[192:195], v[124:127], 0\n"
        "v_perm_b32 v80, v25, v24, s63\n"
        "v_perm_b32 v81, v27, v26, s63\n"
        "v_mfma_f32_16x16x32_bf16 v[76:79], v[196:199], v[128:131], v[76:79]\n"
        "v_perm_b32 v82, v33, v32, s63\n"
        "v_perm_b32 v83, v35, v34, s63\n"
        "v_mfma_f32_16x16x32_bf16 v[76:79], v[200:203], v[160:163], v[76:79]\n"
        "buffer_store_dwordx3 v[156:158], v12, s[8:11], s37 offen offset:384 sc0 sc1 nt\n"
        "v_cvt_pk_bf16_f32 v148, v86, v62\n"
        "v_cvt_pk_bf16_f32 v150, v155, v59\n"
        "v_cvt_pk_bf16_f32 v151, v75, v23\n"
        "v_cvt_pk_bf16_f32 v152, v87, v63\n"
        "s_waitcnt lgkmcnt(3)\n"
        "v_mfma_f32_16x16x32_bf16 v[32:35], v[44:47], v[160:163], 0\n"
        "v_cvt_pk_bf16_f32 v72, v68, v36\n"
        "v_perm_b32 v84, v77, v76, s63\n"
        "v_smfmac_f32_16x16x64_bf16 v[32:35], v[48:51], v[124:131], v4\n"
        "s_add_u32 s37, s36, 196224\n"
        "v_perm_b32 v85, v79, v78, s63\n"
        "v_mfma_f32_16x16x32_bf16 v[24:27], v[44:47], v[52:55], 0\n"
        "v_cvt_pk_bf16_f32 v73, v40, v64\n"
        "v_cvt_pk_bf16_f32 v74, v104, v88\n"
        "v_smfmac_f32_16x16x64_bf16 v[24:27], v[48:51], v[132:139], v4\n"
        "s_waitcnt lgkmcnt(0)\n"
        "s_setprio 2\n"
        "v_cvt_pk_bf16_f32 v156, v69, v37\n"
        "v_cvt_pk_bf16_f32 v157, v41, v65\n"
        "v_mfma_f32_16x16x32_bf16 v[100:103], v[44:47], v[116:119], 0\n"
        "v_cvt_pk_bf16_f32 v158, v105, v89\n"
        "v_cvt_pk_bf16_f32 v76, v70, v38\n"
        "v_smfmac_f32_16x16x64_bf16 v[100:103], v[48:51], v[92:99], v4\n"
        "buffer_store_dwordx3 v[164:166], v13, s[8:11], s37 offen offset:1152 sc0 sc1 nt\n"
        "v_cvt_pk_bf16_f32 v77, v42, v66\n"
        "v_cvt_pk_bf16_f32 v78, v106, v90\n"
        "v_cvt_pk_bf16_f32 v88, v71, v39\n"
        "v_cvt_pk_bf16_f32 v89, v43, v67\n"
        "v_cvt_pk_bf16_f32 v90, v107, v91\n"
        "v_mfma_f32_16x16x32_bf16 v[20:23], v[212:215], v[160:163], 0\n"
        "s_add_u32 s37, s36, 294336\n"
        "ds_read_b64 v[154:155], v11 offset:192\n"
        "v_mfma_f32_16x16x32_bf16 v[20:23], v[208:211], v[128:131], v[20:23]\n"
        "ds_read_b32 v153, v10\n"
        "v_mfma_f32_16x16x32_bf16 v[20:23], v[204:207], v[124:127], v[20:23]\n"
        "v_mfma_f32_16x16x32_bf16 v[28:31], v[216:219], v[124:127], 0\n"
        "v_mfma_f32_16x16x32_bf16 v[28:31], v[220:223], v[128:131], v[28:31]\n"
        "v_mfma_f32_16x16x32_bf16 v[28:31], v[224:227], v[160:163], v[28:31]\n"
        "buffer_store_dwordx3 v[146:148], v12, s[8:11], s37 offen offset:576 sc0 sc1 nt\n"
        "s_add_u32 s37, s36, 392448\n"
        "s_waitcnt lgkmcnt(1)\n"
        "v_add_f32_dpp v155, v34, v35 quad_perm:[0,1,2,3] row_mask:0x1 bank_mask:0xf\n"
        "v_add_f32_dpp v154, v32, v33 quad_perm:[0,1,2,3] row_mask:0x1 bank_mask:0xf\n"
        "v_perm_b32 v86, v21, v20, s63\n"
        "v_mfma_f32_16x16x32_bf16 v[44:47], v[236:239], v[160:163], 0\n"
        "v_perm_b32 v87, v23, v22, s63\n"
        "v_perm_b32 v20, v29, v28, s63\n"
        "v_mfma_f32_16x16x32_bf16 v[44:47], v[232:235], v[128:131], v[44:47]\n"
        "v_perm_b32 v21, v31, v30, s63\n"
        "v_add_f32_dpp v155, v26, v27 quad_perm:[0,1,2,3] row_mask:0x2 bank_mask:0xf\n"
        "v_mfma_f32_16x16x32_bf16 v[44:47], v[228:231], v[124:127], v[44:47]\n"
        "buffer_store_dwordx3 v[150:152], v12, s[8:11], s37 offen sc0 sc1 nt\n"
        "v_add_f32_dpp v154, v24, v25 quad_perm:[0,1,2,3] row_mask:0x2 bank_mask:0xf\n"
        "v_add_f32_dpp v155, v102, v103 quad_perm:[0,1,2,3] row_mask:0x4 bank_mask:0xf\n"
        "v_mul_f32 v165, s52, v155\n"
        "v_add_f32_dpp v154, v100, v101 quad_perm:[0,1,2,3] row_mask:0x4 bank_mask:0xf\n"
        "s_waitcnt lgkmcnt(0)\n"
        "v_fma_f32 v165, -v165, v155, v153\n"
        "v_add_f32_e64 v165, s53, |v165|\n"
        "v_perm_b32 v23, v47, v46, s63\n"
        "v_perm_b32 v22, v45, v44, s63\n"
        "v_mfma_f32_16x16x32_bf16 v[100:103], v[124:127], v[80:83], 0\n"
        "v_rsq_f32 v153, v165\n"
        "v_mfma_f32_16x16x32_bf16 v[100:103], v[128:131], v[84:87], v[100:103]\n"
        "s_add_u32 s37, s36, 392448\n"
        "ds_read_b128 v[104:107], v8 offset:128\n"
        "v_and_b32 v56, s28, v153\n"
        "v_mfma_f32_16x16x32_bf16 v[100:103], v[160:163], v[20:23], v[100:103]\n"
        "ds_read2_b32 v[108:109], v6 offset0:0 offset1:2\n"
        "ds_read2_b32 v[110:111], v6 offset0:8 offset1:10\n"
        "v_mfma_f32_16x16x32_bf16 v[144:147], v[52:55], v[20:23], 0\n"
        "v_and_b32 v164, s28, v153\n"
        "v_sub_f32 v167, v153, v56\n"
        "v_mfma_f32_16x16x32_bf16 v[144:147], v[136:139], v[84:87], v[144:147]\n"
        "v_cvt_pk_bf16_f32 v57, v167, v56\n"
        "v_cvt_pk_bf16_f32 v165, v164, v167\n"
        "v_mfma_f32_16x16x32_bf16 v[144:147], v[132:135], v[80:83], v[144:147]\n"
        "buffer_store_dwordx3 v[72:74], v15, s[8:11], s37 offen offset:768 sc0 sc1 nt\n"
        "v_mul_f32 v154, v154, v153\n"
        "v_readlane_b32 s58, v153, 56\n"
        "s_mov_b64 exec, s[56:57]\n"
        "v_mov_b32 v154, 0xff800000\n"
        "s_mov_b64 exec, -1\n"
        "v_mfma_f32_16x16x4_4b_bf16 v[32:47], v[56:57], v[164:165], 0 blgp:4\n"
        "v_mul_f32 v154, s58, v154\n"
        "v_exp_f32 v154, v154\n"
        "v_mfma_f32_16x16x32_bf16 v[24:27], v[92:95], v[80:83], 0\n"
        "s_add_u32 s37, s36, 490560\n"
        "v_add_f32_dpp v167, v154, v154 row_ror:8 row_mask:0xf bank_mask:0xf\n"
        "v_mul_f32 v154, v154, v153\n"
        "v_mfma_f32_16x16x32_bf16 v[24:27], v[96:99], v[84:87], v[24:27]\n"
        "v_add_f32_dpp v167, v167, v167 row_ror:4 row_mask:0xf bank_mask:0xf\n"
        "v_mfma_f32_16x16x32_bf16 v[24:27], v[116:119], v[20:23], v[24:27]\n"
        "s_waitcnt lgkmcnt(0)\n"
        "v_add_f32_dpp v167, v167, v167 row_ror:2 row_mask:0xf bank_mask:0xf\n"
        "v_mfma_f32_16x16x32_bf16 v[148:151], v[104:107], v[20:23], 0\n"
        "v_mul_f32 v100, v100, v32\n"
        "v_mul_f32 v101, v101, v33\n"
        "v_smfmac_f32_16x16x64_bf16 v[148:151], v[108:111], v[80:87], v4\n"
        "buffer_store_dwordx3 v[156:158], v12, s[8:11], s37 offen offset:192 sc0 sc1 nt\n"
        "v_mul_f32 v102, v102, v34\n"
        "v_mul_f32 v103, v103, v35\n"
        "v_exp_f32 v100, v100\n"
        "v_mul_f32 v144, v144, v36\n"
        "v_mfma_f32_16x16x32_bf16 v[20:23], v[168:171], v[132:135], 0\n"
        "s_add_u32 s37, s36, 490560\n"
        "v_exp_f32 v101, v101\n"
        "v_mfma_f32_16x16x32_bf16 v[20:23], v[172:175], v[136:139], v[20:23]\n"
        "v_exp_f32 v102, v102\n"
        "v_mfma_f32_16x16x32_bf16 v[20:23], v[176:179], v[52:55], v[20:23]\n"
        "v_exp_f32 v103, v103\n"
        "v_add_f32 v151, v100, v101\n"
        "v_mfma_f32_16x16x32_bf16 v[28:31], v[188:191], v[52:55], 0\n"
        "v_add_f32 v151, v151, v102\n"
        "v_add_f32 v151, v151, v103\n"
        "v_mfma_f32_16x16x32_bf16 v[28:31], v[184:187], v[136:139], v[28:31]\n"
        "v_mul_f32 v145, v145, v37\n"
        "v_mul_f32 v146, v146, v38\n"
        "v_mfma_f32_16x16x32_bf16 v[28:31], v[180:183], v[132:135], v[28:31]\n"
        "buffer_store_dwordx3 v[76:78], v14, s[8:11], s37 offen offset:960 sc0 sc1 nt\n"
        "v_perm_b32 v68, v21, v20, s63\n"
        "v_perm_b32 v69, v23, v22, s63\n"
        "v_mul_f32 v147, v147, v39\n"
        "v_exp_f32 v144, v144\n"
        "v_mfma_f32_16x16x32_bf16 v[20:23], v[192:195], v[132:135], 0\n"
        "s_add_u32 s37, s36, 588672\n"
        "v_exp_f32 v145, v145\n"
        "v_mfma_f32_16x16x32_bf16 v[20:23], v[196:199], v[136:139], v[20:23]\n"
        "v_perm_b32 v70, v29, v28, s63\n"
        "v_perm_b32 v71, v31, v30, s63\n"
        "v_mfma_f32_16x16x32_bf16 v[20:23], v[200:203], v[52:55], v[20:23]\n"
        "buffer_store_dwordx3 v[88:90], v12, s[8:11], s37 offen offset:384 sc0 sc1 nt\n"
        "v_exp_f32 v146, v146\n"
        "v_exp_f32 v147, v147\n"
        "v_add_f32 v151, v151, v144\n"
        "v_add_f32 v151, v151, v145\n"
        "v_mfma_f32_16x16x32_bf16 v[28:31], v[212:215], v[52:55], 0\n"
        "v_add_f32 v151, v151, v146\n"
        "v_add_f32 v151, v151, v147\n"
        "v_mfma_f32_16x16x32_bf16 v[28:31], v[208:211], v[136:139], v[28:31]\n"
        "v_perm_b32 v72, v21, v20, s63\n"
        "v_perm_b32 v73, v23, v22, s63\n"
        "v_mfma_f32_16x16x32_bf16 v[28:31], v[204:207], v[132:135], v[28:31]\n"
        "v_mul_f32 v24, v24, v40\n"
        "v_mul_f32 v25, v25, v41\n"
        "v_mul_f32 v26, v26, v42\n"
        "v_mfma_f32_16x16x32_bf16 v[20:23], v[216:219], v[132:135], 0\n"
        "v_mul_f32 v27, v27, v43\n"
        "v_add_f32 v148, v148, v149\n"
        "v_mfma_f32_16x16x32_bf16 v[20:23], v[220:223], v[136:139], v[20:23]\n"
        "v_exp_f32 v24, v24\n"
        "v_mfma_f32_16x16x32_bf16 v[20:23], v[224:227], v[52:55], v[20:23]\n"
        "v_perm_b32 v74, v29, v28, s63\n"
        "v_perm_b32 v75, v31, v30, s63\n"
        "v_add_f32 v151, v151, v24\n"
        "v_mfma_f32_16x16x32_bf16 v[64:67], v[236:239], v[52:55], 0\n"
        "v_exp_f32 v25, v25\n"
        "v_mfma_f32_16x16x32_bf16 v[64:67], v[232:235], v[136:139], v[64:67]\n"
        "v_exp_f32 v26, v26\n"
        "v_mfma_f32_16x16x32_bf16 v[64:67], v[228:231], v[132:135], v[64:67]\n"
        "v_perm_b32 v48, v21, v20, s63\n"
        "v_perm_b32 v49, v23, v22, s63\n"
        "v_mfma_f32_16x16x4_4b_bf16 v[28:43], v[56:57], v[164:165], 0 blgp:5\n"
        "v_exp_f32 v27, v27\n"
        "v_add_f32 v151, v151, v25\n"
        "v_add_f32 v151, v151, v26\n"
        "v_mul_f32 v148, v148, v44\n"
        "v_exp_f32 v148, v148\n"
        "v_perm_b32 v50, v65, v64, s63\n"
        "v_perm_b32 v51, v67, v66, s63\n"
        "v_mfma_f32_16x16x32_bf16 v[58:61], v[124:127], v[68:71], 0\n"
        "v_add_f32 v151, v151, v27\n"
        "v_add_f32_dpp v167, v167, v167 row_ror:1 row_mask:0xf bank_mask:0xf\n"
        "v_mfma_f32_16x16x32_bf16 v[58:61], v[128:131], v[72:75], v[58:61]\n"
        "v_add_f32_dpp v151, v148, v151 quad_perm:[0,1,2,3] row_mask:0x4 bank_mask:0xf\n"
        "v_mfma_f32_16x16x32_bf16 v[58:61], v[160:163], v[48:51], v[58:61]\n"
        "v_mfma_f32_16x16x32_bf16 v[112:115], v[52:55], v[48:51], 0\n"
        "v_permlane32_swap_b32 v151, v167\n"
        "v_mfma_f32_16x16x32_bf16 v[112:115], v[136:139], v[72:75], v[112:115]\n"
        "v_add_f32 v151, v151, v167\n"
        "v_mov_b32 v167, v151\n"
        "v_mfma_f32_16x16x32_bf16 v[112:115], v[132:135], v[68:71], v[112:115]\n"
        "v_mfma_f32_16x16x32_bf16 v[20:23], v[92:95], v[68:71], 0\n"
        "v_permlane16_swap_b32 v151, v167\n"
        "v_mfma_f32_16x16x32_bf16 v[20:23], v[96:99], v[72:75], v[20:23]\n"
        "v_add_f32 v151, v151, v167\n"
        "v_mul_f32 v58, v58, v28\n"
        "v_mfma_f32_16x16x32_bf16 v[20:23], v[116:119], v[48:51], v[20:23]\n"
        "v_rcp_f32 v123, v151\n"
        "v_mul_f32 v59, v59, v29\n"
        "v_and_b32 v158, s28, v123\n"
        "v_mfma_f32_16x16x32_bf16 v[62:65], v[104:107], v[48:51], 0\n"
        "v_sub_f32 v152, v123, v158\n"
        "v_cvt_pk_bf16_f32 v159, v158, v152\n"
        "v_smfmac_f32_16x16x64_bf16 v[62:65], v[108:111], v[68:75], v4\n"
        "v_min3_f32 v167, v151, v123, v153\n"
        "v_mfma_f32_16x16x32_bf16 v[48:51], v[168:171], v[92:95], 0\n"
        "v_mul_f32 v60, v60, v30\n"
        "v_mul_f32 v61, v61, v31\n"
        "v_mfma_f32_16x16x32_bf16 v[48:51], v[172:175], v[96:99], v[48:51]\n"
        "v_exp_f32 v58, v58\n"
        "v_mfma_f32_16x16x32_bf16 v[48:51], v[176:179], v[116:119], v[48:51]\n"
        "v_exp_f32 v59, v59\n"
        "v_exp_f32 v60, v60\n"
        "v_mfma_f32_16x16x32_bf16 v[68:71], v[188:191], v[116:119], 0\n"
        "v_exp_f32 v61, v61\n"
        "v_mfma_f32_16x16x32_bf16 v[68:71], v[184:187], v[96:99], v[68:71]\n"
        "v_add_f32 v155, v58, v59\n"
        "v_add_f32 v155, v155, v60\n"
        "v_mfma_f32_16x16x32_bf16 v[68:71], v[180:183], v[92:95], v[68:71]\n"
        "v_perm_b32 v44, v49, v48, s63\n"
        "v_perm_b32 v45, v51, v50, s63\n"
        "v_add_f32 v155, v155, v61\n"
        "v_mfma_f32_16x16x32_bf16 v[28:31], v[192:195], v[92:95], 0\n"
        "v_mul_f32 v112, v112, v32\n"
        "v_mul_f32 v113, v113, v33\n"
        "v_mfma_f32_16x16x32_bf16 v[28:31], v[196:199], v[96:99], v[28:31]\n"
        "v_mul_f32 v114, v114, v34\n"
        "v_perm_b32 v46, v69, v68, s63\n"
        "v_mfma_f32_16x16x32_bf16 v[28:31], v[200:203], v[116:119], v[28:31]\n"
        "v_perm_b32 v47, v71, v70, s63\n"
        "v_mul_f32 v115, v115, v35\n"
        "v_exp_f32 v112, v112\n"
        "v_mfma_f32_16x16x32_bf16 v[68:71], v[212:215], v[116:119], 0\n"
        "v_exp_f32 v113, v113\n"
        "v_mfma_f32_16x16x32_bf16 v[68:71], v[208:211], v[96:99], v[68:71]\n"
        "v_exp_f32 v114, v114\n"
        "v_mfma_f32_16x16x32_bf16 v[68:71], v[204:207], v[92:95], v[68:71]\n"
        "v_perm_b32 v48, v29, v28, s63\n"
        "v_perm_b32 v49, v31, v30, s63\n"
        "v_add_f32 v155, v155, v112\n"
        "v_mfma_f32_16x16x32_bf16 v[64:67], v[216:219], v[92:95], 0\n"
        "v_exp_f32 v115, v115\n"
        "v_mfma_f32_16x16x32_bf16 v[64:67], v[220:223], v[96:99], v[64:67]\n"
        "v_add_f32 v155, v155, v113\n"
        "v_add_f32 v155, v155, v114\n"
        "v_mfma_f32_16x16x32_bf16 v[64:67], v[224:227], v[116:119], v[64:67]\n"
        "v_perm_b32 v50, v69, v68, s63\n"
        "v_perm_b32 v51, v71, v70, s63\n"
        "v_add_f32 v155, v155, v115\n"
        "v_mul_f32 v20, v20, v36\n"
        "v_mfma_f32_16x16x32_bf16 v[28:31], v[236:239], v[116:119], 0\n"
        "v_mul_f32 v21, v21, v37\n"
        "v_mul_f32 v22, v22, v38\n"
        "v_mfma_f32_16x16x32_bf16 v[28:31], v[232:235], v[96:99], v[28:31]\n"
        "v_perm_b32 v150, v65, v64, s63\n"
        "v_perm_b32 v151, v67, v66, s63\n"
        "v_mfma_f32_16x16x32_bf16 v[28:31], v[228:231], v[92:95], v[28:31]\n"
        "v_exp_f32 v20, v20\n"
        "v_mfma_f32_16x16x4_4b_bf16 v[76:91], v[56:57], v[158:159], 0 blgp:4\n"
        "v_exp_f32 v21, v21\n"
        "v_mul_f32 v23, v23, v39\n"
        "v_exp_f32 v22, v22\n"
        "v_exp_f32 v23, v23\n"
        "v_add_f32 v155, v155, v20\n"
        "v_add_f32 v155, v155, v21\n"
        "v_perm_b32 v152, v29, v28, s63\n"
        "v_perm_b32 v153, v31, v30, s63\n"
        "v_mfma_f32_16x16x32_bf16 v[36:39], v[124:127], v[44:47], 0\n"
        "v_add_f32 v155, v155, v22\n"
        "v_add_f32 v155, v155, v23\n"
        "v_mfma_f32_16x16x32_bf16 v[36:39], v[128:131], v[48:51], v[36:39]\n"
        "v_mul_f32 v100, v100, v76\n"
        "v_mul_f32 v101, v101, v77\n"
        "v_mfma_f32_16x16x32_bf16 v[36:39], v[160:163], v[150:153], v[36:39]\n"
        "v_mul_f32 v102, v102, v78\n"
        "v_mul_f32 v103, v103, v79\n"
        "v_mul_f32 v144, v144, v80\n"
        "v_mul_f32 v145, v145, v81\n"
        "v_mfma_f32_16x16x32_bf16 v[140:143], v[52:55], v[150:153], 0\n"
        "v_mul_f32 v146, v146, v82\n"
        "v_mul_f32 v147, v147, v83\n"
        "v_mfma_f32_16x16x32_bf16 v[140:143], v[136:139], v[48:51], v[140:143]\n"
        "v_mul_f32 v24, v24, v84\n"
        "v_mul_f32 v25, v25, v85\n"
        "v_mfma_f32_16x16x32_bf16 v[140:143], v[132:135], v[44:47], v[140:143]\n"
        "v_add_f32 v62, v62, v63\n"
        "v_mul_f32 v62, v62, v40\n"
        "v_mul_f32 v26, v26, v86\n"
        "v_mfma_f32_16x16x4_4b_bf16 v[64:79], v[56:57], v[164:165], 0 blgp:6\n"
        "v_exp_f32 v62, v62\n"
        "v_mul_f32 v27, v27, v87\n"
        "v_fma_f32 v148, v148, v88, v19\n"
        "v_add_f32_dpp v155, v62, v155 quad_perm:[0,1,2,3] row_mask:0x4 bank_mask:0xf\n"
        "v_perm_b32 v40, v101, v100, s63\n"
        "v_perm_b32 v41, v103, v102, s63\n"
        "v_perm_b32 v42, v145, v144, s63\n"
        "v_perm_b32 v43, v147, v146, s63\n"
        "v_perm_b32 v164, v25, v24, s63\n"
        "v_mfma_f32_16x16x32_bf16 v[156:159], v[92:95], v[44:47], 0\n"
        "v_perm_b32 v166, v27, v26, s63\n"
        "v_perm_b32 v165, 0, v148, s63\n"
        "v_mfma_f32_16x16x32_bf16 v[156:159], v[96:99], v[48:51], v[156:159]\n"
        "v_mul_f32 v36, v36, v64\n"
        "v_mul_f32 v37, v37, v65\n"
        "v_mfma_f32_16x16x32_bf16 v[156:159], v[116:119], v[150:153], v[156:159]\n"
        "v_mul_f32 v38, v38, v66\n"
        "v_mul_f32 v39, v39, v67\n"
        "v_mul_f32 v140, v140, v68\n"
        "v_mfma_f32_16x16x32_bf16 v[92:95], v[104:107], v[150:153], 0\n"
        "ds_read_b128 v[88:91], v17 offset:0\n"
        "v_mul_f32 v141, v141, v69\n"
        "v_smfmac_f32_16x16x64_bf16 v[92:95], v[108:111], v[44:51], v4\n"
        "ds_read_b128 v[84:87], v17 offset:64\n"
        "ds_read_b128 v[150:153], v17 offset:128\n"
        "v_mfma_f32_16x16x32_bf16 v[48:51], v[124:127], v[240:243], 0\n"
        "v_exp_f32 v36, v36\n"
        "v_mfma_f32_16x16x32_bf16 v[48:51], v[128:131], v[244:247], v[48:51]\n"
        "ds_read_b128 v[80:83], v0 offset:5136\n"
        "v_mul_f32 v142, v142, v70\n"
        "v_mfma_f32_16x16x32_bf16 v[48:51], v[160:163], v[248:251], v[48:51]\n"
        "ds_read_b128 v[32:35], v0 offset:4112\n"
        "v_mul_f32 v143, v143, v71\n"
        "v_mfma_f32_16x16x32_bf16 v[24:27], v[52:55], v[248:251], 0\n"
        "v_exp_f32 v37, v37\n"
        "v_mfma_f32_16x16x32_bf16 v[24:27], v[136:139], v[244:247], v[24:27]\n"
        "v_exp_f32 v38, v38\n"
        "v_mfma_f32_16x16x32_bf16 v[24:27], v[132:135], v[240:243], v[24:27]\n"
        "s_waitcnt lgkmcnt(2)\n"
        "v_perm_b32 v44, v49, v48, s63\n"
        "v_perm_b32 v45, v51, v50, s63\n"
        "v_add_f32 v145, v36, v37\n"
        "v_mfma_f32_16x16x32_bf16 v[104:107], v[88:91], v[240:243], 0\n"
        "v_exp_f32 v39, v39\n"
        "v_mfma_f32_16x16x32_bf16 v[104:107], v[84:87], v[244:247], v[104:107]\n"
        "v_add_f32 v145, v145, v38\n"
        "v_perm_b32 v46, v25, v24, s63\n"
        "v_mfma_f32_16x16x32_bf16 v[104:107], v[150:153], v[248:251], v[104:107]\n"
        "s_waitcnt lgkmcnt(0)\n"
        "v_perm_b32 v47, v27, v26, s63\n"
        "v_add_f32 v145, v145, v39\n"
        "v_exp_f32 v140, v140\n"
        "v_mfma_f32_16x16x32_bf16 v[64:67], v[150:153], v[80:83], 0\n"
        "v_exp_f32 v141, v141\n"
        "v_mfma_f32_16x16x32_bf16 v[64:67], v[84:87], v[32:35], v[64:67]\n"
        "ds_read_b128 v[146:149], v0 offset:6176\n"
        "v_perm_b32 v116, v105, v104, s63\n"
        "v_mfma_f32_16x16x32_bf16 v[64:67], v[88:91], v[252:255], v[64:67]\n"
        "ds_read_b128 v[24:27], v0 offset:7200\n"
        "ds_read_b128 v[108:111], v0 offset:8224\n"
        "v_mfma_f32_16x16x32_bf16 v[100:103], v[132:135], v[252:255], 0\n"
        "v_perm_b32 v120, v107, v106, s63\n"
        "v_add_f32 v145, v145, v140\n"
        "v_mfma_f32_16x16x32_bf16 v[100:103], v[136:139], v[32:35], v[100:103]\n"
        "v_exp_f32 v142, v142\n"
        "v_mfma_f32_16x16x32_bf16 v[100:103], v[52:55], v[80:83], v[100:103]\n"
        "v_perm_b32 v94, v65, v64, s63\n"
        "v_perm_b32 v98, v67, v66, s63\n"
        "v_add_f32 v145, v145, v141\n"
        "v_mfma_f32_16x16x32_bf16 v[68:71], v[160:163], v[80:83], 0\n"
        "v_exp_f32 v143, v143\n"
        "v_mfma_f32_16x16x32_bf16 v[68:71], v[128:131], v[32:35], v[68:71]\n"
        "v_add_f32 v145, v145, v142\n"
        "v_add_f32 v145, v145, v143\n"
        "v_mfma_f32_16x16x32_bf16 v[68:71], v[124:127], v[252:255], v[68:71]\n"
        "s_waitcnt lgkmcnt(0)\n"
        "v_perm_b32 v106, v101, v100, s63\n"
        "v_perm_b32 v107, v103, v102, s63\n"
        "v_mul_f32 v156, v156, v72\n"
        "v_mul_f32 v157, v157, v73\n"
        "v_mfma_f32_16x16x32_bf16 v[32:35], v[124:127], v[146:149], 0\n"
        "v_mul_f32 v158, v158, v74\n"
        "v_mul_f32 v159, v159, v75\n"
        "v_mfma_f32_16x16x32_bf16 v[32:35], v[128:131], v[24:27], v[32:35]\n"
        "ds_read_b128 v[48:51], v0 offset:11312\n"
        "v_perm_b32 v104, v69, v68, s63\n"
        "v_mfma_f32_16x16x32_bf16 v[32:35], v[160:163], v[108:111], v[32:35]\n"
        "ds_read_b128 v[80:83], v0 offset:10288\n"
        "ds_read_b128 v[64:67], v0 offset:9264\n"
        "v_mfma_f32_16x16x32_bf16 v[100:103], v[52:55], v[108:111], 0\n"
        "v_perm_b32 v105, v71, v70, s63\n"
        "v_add_f32 v92, v92, v93\n"
        "v_mfma_f32_16x16x32_bf16 v[100:103], v[136:139], v[24:27], v[100:103]\n"
        "v_exp_f32 v156, v156\n"
        "v_mfma_f32_16x16x32_bf16 v[100:103], v[132:135], v[146:149], v[100:103]\n"
        "v_perm_b32 v28, v33, v32, s63\n"
        "v_perm_b32 v29, v35, v34, s63\n"
        "v_add_f32 v145, v145, v156\n"
        "v_mfma_f32_16x16x32_bf16 v[70:73], v[88:91], v[146:149], 0\n"
        "v_exp_f32 v157, v157\n"
        "v_mfma_f32_16x16x32_bf16 v[70:73], v[84:87], v[24:27], v[70:73]\n"
        "v_exp_f32 v158, v158\n"
        "v_mfma_f32_16x16x32_bf16 v[70:73], v[150:153], v[108:111], v[70:73]\n"
        "s_waitcnt lgkmcnt(0)\n"
        "v_perm_b32 v30, v101, v100, s63\n"
        "v_perm_b32 v31, v103, v102, s63\n"
        "v_exp_f32 v159, v159\n"
        "v_mfma_f32_16x16x32_bf16 v[32:35], v[150:153], v[48:51], 0\n"
        "v_add_f32 v145, v145, v157\n"
        "v_add_f32 v145, v145, v158\n"
        "v_mfma_f32_16x16x32_bf16 v[32:35], v[84:87], v[80:83], v[32:35]\n"
        "ds_read_b128 v[100:103], v0 offset:12352\n"
        "v_perm_b32 v144, v71, v70, s63\n"
        "v_mfma_f32_16x16x32_bf16 v[32:35], v[88:91], v[64:67], v[32:35]\n"
        "ds_read_b128 v[108:111], v0 offset:13376\n"
        "ds_read_b128 v[24:27], v0 offset:14400\n"
        "v_mfma_f32_16x16x32_bf16 v[68:71], v[132:135], v[64:67], 0\n"
        "v_perm_b32 v148, v73, v72, s63\n"
        "v_mul_f32 v92, v92, v76\n"
        "v_mfma_f32_16x16x32_bf16 v[68:71], v[136:139], v[80:83], v[68:71]\n"
        "v_add_f32 v145, v145, v159\n"
        "v_readlane_b32 s59, v123, 32\n"
        "v_mfma_f32_16x16x32_bf16 v[68:71], v[52:55], v[48:51], v[68:71]\n"
        "v_perm_b32 v117, v33, v32, s63\n"
        "v_perm_b32 v121, v35, v34, s63\n"
        "v_exp_f32 v92, v92\n"
        "v_mfma_f32_16x16x32_bf16 v[72:75], v[160:163], v[48:51], 0\n"
        "v_mul_f32 v154, s59, v154\n"
        "v_add_f32_dpp v145, v92, v145 quad_perm:[0,1,2,3] row_mask:0x4 bank_mask:0xf\n"
        "v_mfma_f32_16x16x32_bf16 v[72:75], v[128:131], v[80:83], v[72:75]\n"
        "v_perm_b32 v154, 0, v154, s63\n"
        "v_perm_b32 v50, v69, v68, s63\n"
        "v_mfma_f32_16x16x32_bf16 v[72:75], v[124:127], v[64:67], v[72:75]\n"
        "s_waitcnt lgkmcnt(0)\n"
        "v_perm_b32 v51, v71, v70, s63\n"
        "v_permlane32_swap_b32 v155, v145\n"
        "v_mfma_f32_16x16x32_bf16 v[80:83], v[124:127], v[100:103], 0\n"
        "v_add_f32 v155, v155, v145\n"
        "v_mov_b32 v149, v155\n"
        "v_mfma_f32_16x16x32_bf16 v[80:83], v[128:131], v[108:111], v[80:83]\n"
        "ds_read_b128 v[32:35], v0 offset:17488\n"
        "v_perm_b32 v48, v73, v72, s63\n"
        "v_mfma_f32_16x16x32_bf16 v[80:83], v[160:163], v[24:27], v[80:83]\n"
        "ds_read_b128 v[68:71], v0 offset:16464\n"
        "ds_read_b128 v[64:67], v0 offset:15440\n"
        "v_mfma_f32_16x16x32_bf16 v[76:79], v[52:55], v[24:27], 0\n"
        "v_perm_b32 v49, v75, v74, s63\n"
        "v_mfma_f32_16x16x32_bf16 v[76:79], v[136:139], v[108:111], v[76:79]\n"
        "v_permlane16_swap_b32 v155, v149\n"
        "v_mfma_f32_16x16x32_bf16 v[76:79], v[132:135], v[100:103], v[76:79]\n"
        "v_add_f32 v155, v155, v149\n"
        "v_rcp_f32 v147, v155\n"
        "v_mfma_f32_16x16x32_bf16 v[72:75], v[88:91], v[100:103], 0\n"
        "ds_read2_b32 v[122:123], v11 offset0:0 offset1:3\n"
        "v_min3_f32 v167, v167, v155, v147\n"
        "v_mfma_f32_16x16x32_bf16 v[72:75], v[84:87], v[108:111], v[72:75]\n"
        "ds_read_b64_tr_b16 v[118:119], v9 offset:0\n"
        "v_and_b32 v102, s28, v147\n"
        "v_mfma_f32_16x16x32_bf16 v[72:75], v[150:153], v[24:27], v[72:75]\n"
        "s_waitcnt lgkmcnt(2)\n"
        "ds_read2_b32 v[100:101], v11 offset0:1 offset1:4\n"
        "v_sub_f32 v149, v147, v102\n"
        "v_cvt_pk_bf16_f32 v103, v102, v149\n"
        "v_perm_b32 v108, v81, v80, s63\n"
        "v_mfma_f32_16x16x32_bf16 v[24:27], v[150:153], v[32:35], 0\n"
        "ds_read_b64_tr_b16 v[96:97], v9 offset:16\n"
        "v_perm_b32 v109, v83, v82, s63\n"
        "v_mfma_f32_16x16x32_bf16 v[24:27], v[84:87], v[68:71], v[24:27]\n"
        "ds_read2_b32 v[150:151], v11 offset0:2 offset1:5\n"
        "v_perm_b32 v110, v77, v76, s63\n"
        "v_mfma_f32_16x16x32_bf16 v[24:27], v[88:91], v[64:67], v[24:27]\n"
        "ds_read_b64_tr_b16 v[146:147], v9 offset:32\n"
        "v_perm_b32 v111, v79, v78, s63\n"
        "v_perm_b32 v95, v73, v72, s63\n"
        "v_mfma_f32_16x16x4_4b_bf16 v[76:91], v[56:57], v[102:103], 0 blgp:4\n"
        "v_perm_b32 v99, v75, v74, s63\n"
        "v_cmp_nle_f32 vcc, 0x2b800000, v167\n"
        "v_mov_b32_dpp v167, v19 row_shr:4 row_mask:0xf bank_mask:0xf bound_ctrl:0\n"
        "ds_write_b16 v1, v154\n"
        "v_perm_b32 v145, v25, v24, s63\n"
        "v_perm_b32 v149, v27, v26, s63\n"
        "s_andn2_b64 vcc, vcc, s[56:57]\n"
        "s_cbranch_scc1 CREDOA_%=\n"
        "v_mfma_f32_16x16x32_bf16 v[24:27], v[132:135], v[64:67], 0\n"
        "s_mov_b32 exec_hi, 0xffff\n"
        "v_add_u32 v10, 40064, v10\n"
        "s_mov_b32 exec_hi, -1\n"
        "v_mfma_f32_16x16x32_bf16 v[24:27], v[136:139], v[68:71], v[24:27]\n"
        "v_mul_f32 v58, v58, v76\n"
        "v_mul_f32 v59, v59, v77\n"
        "v_mfma_f32_16x16x32_bf16 v[24:27], v[52:55], v[32:35], v[24:27]\n"
        "s_add_u32 s31, s31, 1\n"
        "v_mul_f32 v60, v60, v78\n"
        "v_mul_f32 v61, v61, v79\n"
        "v_mul_f32 v112, v112, v80\n"
        "v_mul_f32 v113, v113, v81\n"
        "v_mfma_f32_16x16x32_bf16 v[52:55], v[160:163], v[32:35], 0\n"
        "s_cmp_ge_u32 s31, s71\n"
        "v_mul_f32 v114, v114, v82\n"
        "v_mul_f32 v115, v115, v83\n"
        "v_mfma_f32_16x16x32_bf16 v[52:55], v[128:131], v[68:71], v[52:55]\n"
        "s_cbranch_scc1 L38A_%=\n"
        "L39A_%=:\n"
        "v_mul_f32 v20, v20, v84\n"
        "v_perm_b32 v34, v25, v24, s63\n"
        "v_mfma_f32_16x16x32_bf16 v[52:55], v[124:127], v[64:67], v[52:55]\n"
        "s_add_u32 s16, s16, 256\n"
        "v_perm_b32 v35, v27, v26, s63\n"
        "v_mul_f32 v21, v21, v85\n"
        "v_mfma_f32_16x16x32_bf16 v[124:127], v[40:43], v[44:47], 0\n"
        "s_waitcnt lgkmcnt(0)\n"
        "v_mul_f32 v22, v22, v86\n"
        "v_mul_f32 v23, v23, v87\n"
        "v_smfmac_f32_16x16x64_bf16 v[124:127], v[164:167], v[116:123], v18\n"
        "s_cmp_ge_u32 s31, s43\n"
        "s_cselect_b32 s39, s22, s16\n"
        "ds_read_b64_tr_b16 v[118:119], v9 offset:48\n"
        "v_mfma_f32_16x16x4_4b_bf16 v[72:87], v[56:57], v[102:103], 0 blgp:6\n"
        "v_perm_b32 v32, v53, v52, s63\n"
        "v_perm_b32 v33, v55, v54, s63\n"
        "v_fma_f32 v62, v62, v88, v19\n"
        "v_perm_b32 v68, v59, v58, s63\n"
        "v_perm_b32 v69, v61, v60, s63\n"
        "v_perm_b32 v70, v113, v112, s63\n"
        "v_perm_b32 v71, v115, v114, s63\n"
        "v_perm_b32 v24, v21, v20, s63\n"
        "v_perm_b32 v26, v23, v22, s63\n"
        "v_mfma_f32_16x16x32_bf16 v[88:91], v[40:43], v[104:107], 0\n"
        "s_cmp_gt_u32 s31, s43\n"
        "v_perm_b32 v25, 0, v62, s63\n"
        "v_mul_f32 v36, v36, v72\n"
        "v_smfmac_f32_16x16x64_bf16 v[88:91], v[164:167], v[94:101], v18\n"
        "s_cselect_b32 s39, s23, s39\n"
        "ds_read_b64_tr_b16 v[96:97], v9 offset:64\n"
        "v_mfma_f32_16x16x32_bf16 v[56:59], v[40:43], v[28:31], 0\n"
        "s_mul_hi_u32 s38, s39, 58835169\n"
        "ds_read_b128 v[128:131], v2\n"
        "v_mul_f32 v37, v37, v73\n"
        "v_smfmac_f32_16x16x64_bf16 v[56:59], v[164:167], v[144:151], v18\n"
        "s_mul_i32 s39, s39, 1344\n"
        "ds_read_b64_tr_b16 v[146:147], v9 offset:80\n"
        "ds_read_b128 v[52:55], v3\n"
        "v_mfma_f32_16x16x32_bf16 v[112:115], v[40:43], v[48:51], 0\n"
        "s_waitcnt lgkmcnt(4)\n"
        "v_mul_f32 v38, v38, v74\n"
        "v_mul_f32 v39, v39, v75\n"
        "v_smfmac_f32_16x16x64_bf16 v[112:115], v[164:167], v[116:123], v18 abid:1\n"
        "s_mul_i32 s38, s38, 588672\n"
        "s_add_u32 s39, s39, s38\n"
        "v_mul_f32 v140, v140, v76\n"
        "v_cvt_pk_bf16_f32 v152, v124, v88\n"
        "v_mul_f32 v141, v141, v77\n"
        "v_mfma_f32_16x16x32_bf16 v[72:75], v[40:43], v[108:111], 0\n"
        "s_add_u32 s39, s39, s17\n"
        "v_mov_b64 v[60:61], 0\n"
        "s_waitcnt lgkmcnt(3)\n"
        "v_smfmac_f32_16x16x64_bf16 v[72:75], v[164:167], v[94:101], v18 abid:1\n"
        "s_mul_i32 s68, s31, 3\n"
        "v_mov_b64 v[62:63], 0\n"
        "v_cvt_pk_bf16_f32 v153, v56, v112\n"
        "v_mfma_f32_16x16x32_bf16 v[64:67], v[40:43], v[32:35], 0\n"
        "s_waitcnt lgkmcnt(1)\n"
        "v_mov_b64 v[20:21], 0\n"
        "v_mov_b64 v[22:23], 0\n"
        "v_smfmac_f32_16x16x64_bf16 v[64:67], v[164:167], v[144:151], v18 abid:1\n"
        "s_waitcnt lgkmcnt(0)\n"
        "s_bitcmp1_b32 s31, 0\n"
        "v_mov_b32_dpp v27, v19 row_shr:4 row_mask:0xf bank_mask:0xf bound_ctrl:0\n"
        "s_cselect_b32 s38, 3, 6\n"
        "v_smfmac_f32_16x16x64_bf16 v[60:63], v[128:131], v[44:51], v4\n"
        "s_add_u32 s68, s68, s38\n"
        "v_mov_b64 v[40:41], 0\n"
        "v_smfmac_f32_16x16x64_bf16 v[60:63], v[52:55], v[116:123], v4 abid:1\n"
        "s_add_u32 s37, s35, 0\n"
        "s_cmp_lt_u32 s31, s14\n"
        "ds_read_b64_tr_b16 v[118:119], v9 offset:3328\n"
        "v_mov_b64 v[42:43], 0\n"
        "v_smfmac_f32_16x16x64_bf16 v[20:23], v[128:131], v[104:111], v4\n"
        "s_cselect_b32 s68, s68, 0\n"
        "v_cvt_pk_bf16_f32 v154, v72, v64\n"
        "v_smfmac_f32_16x16x64_bf16 v[20:23], v[52:55], v[94:101], v4 abid:1\n"
        "s_and_b32 s38, s31, 7\n"
        "ds_read_b64_tr_b16 v[96:97], v9 offset:3344\n"
        "v_cvt_pk_bf16_f32 v160, v125, v89\n"
        "v_smfmac_f32_16x16x64_bf16 v[40:43], v[128:131], v[28:35], v4\n"
        "s_cselect_b32 s30, 1, -7\n"
        "v_cvt_pk_bf16_f32 v88, v126, v90\n"
        "v_smfmac_f32_16x16x64_bf16 v[40:43], v[52:55], v[144:151], v4 abid:1\n"
        "buffer_store_dwordx3 v[152:154], v12, s[8:11], s37 offen sc0 sc1 nt\n"
        "ds_read_b64_tr_b16 v[146:147], v9 offset:3360\n"
        "v_cvt_pk_bf16_f32 v164, v127, v91\n"
        "v_cvt_pk_bf16_f32 v161, v57, v113\n"
        "v_cvt_pk_bf16_f32 v89, v58, v114\n"
        "v_cvt_pk_bf16_f32 v165, v59, v115\n"
        "v_cvt_pk_bf16_f32 v162, v73, v65\n"
        "v_mfma_f32_16x16x32_bf16 v[152:155], v[68:71], v[44:47], 0\n"
        "s_waitcnt lgkmcnt(2)\n"
        "v_cvt_pk_bf16_f32 v90, v74, v66\n"
        "v_cvt_pk_bf16_f32 v166, v75, v67\n"
        "v_smfmac_f32_16x16x64_bf16 v[152:155], v[24:27], v[116:123], v18\n"
        "s_mul_i32 s29, s30, 192\n"
        "s_mul_i32 s30, s30, 392\n"
        "ds_read_b64_tr_b16 v[118:119], v9 offset:3376\n"
        "v_add_f32 v60, v60, v61\n"
        "v_add_f32 v62, v62, v63\n"
        "v_add_f32 v20, v20, v21\n"
        "v_mfma_f32_16x16x32_bf16 v[56:59], v[68:71], v[104:107], 0\n"
        "s_waitcnt lgkmcnt(2)\n"
        "v_add_f32 v22, v22, v23\n"
        "v_cvt_pk_bf16_f32 v112, v60, v20\n"
        "v_smfmac_f32_16x16x64_bf16 v[56:59], v[24:27], v[94:101], v18\n"
        "s_add_u32 s37, s35, 0\n"
        "s_cmp_eq_u32 s69, s18\n"
        "ds_read_b64_tr_b16 v[96:97], v9 offset:3392\n"
        "v_add_f32 v40, v40, v41\n"
        "v_add_f32 v42, v42, v43\n"
        "v_cvt_pk_bf16_f32 v113, v40, v62\n"
        "v_mfma_f32_16x16x32_bf16 v[72:75], v[68:71], v[28:31], 0\n"
        "s_waitcnt lgkmcnt(2)\n"
        "v_cvt_pk_bf16_f32 v114, v22, v42\n"
        "v_mul_f32 v142, v142, v78\n"
        "v_smfmac_f32_16x16x64_bf16 v[72:75], v[24:27], v[144:151], v18\n"
        "buffer_store_dwordx3 v[160:162], v15, s[8:11], s37 offen offset:768 sc0 sc1 nt\n"
        "ds_read_b64_tr_b16 v[146:147], v9 offset:3408\n"
        "s_cselect_b32 s34, -7488, s70\n"
        "v_mul_f32 v143, v143, v79\n"
        "v_mul_f32 v156, v156, v80\n"
        "v_mul_f32 v157, v157, v81\n"
        "v_mul_f32 v158, v158, v82\n"
        "v_mfma_f32_16x16x32_bf16 v[20:23], v[68:71], v[48:51], 0\n"
        "s_waitcnt lgkmcnt(2)\n"
        "s_add_u32 s69, s69, s34\n"
        "v_mul_f32 v159, v159, v83\n"
        "v_smfmac_f32_16x16x64_bf16 v[20:23], v[24:27], v[116:123], v18 abid:1\n"
        "s_add_u32 s37, s35, 98112\n"
        "ds_read_b64_tr_b16 v[118:119], v9 offset:6656\n"
        "v_fma_f32 v92, v92, v84, v19\n"
        "v_mfma_f32_16x16x32_bf16 v[84:87], v[68:71], v[108:111], 0\n"
        "s_waitcnt lgkmcnt(2)\n"
        "v_perm_b32 v80, v37, v36, s63\n"
        "v_perm_b32 v81, v39, v38, s63\n"
        "v_smfmac_f32_16x16x64_bf16 v[84:87], v[24:27], v[94:101], v18 abid:1\n"
        "ds_read_b64_tr_b16 v[96:97], v9 offset:6672\n"
        "v_perm_b32 v82, v141, v140, s63\n"
        "v_mfma_f32_16x16x32_bf16 v[60:63], v[68:71], v[32:35], 0\n"
        "s_waitcnt lgkmcnt(2)\n"
        "v_perm_b32 v83, v143, v142, s63\n"
        "v_perm_b32 v76, v157, v156, s63\n"
        "v_smfmac_f32_16x16x64_bf16 v[60:63], v[24:27], v[144:151], v18 abid:1\n"
        "buffer_store_dwordx3 v[88:90], v12, s[8:11], s37 offen offset:192 sc0 sc1 nt\n"
        "ds_read_b64_tr_b16 v[146:147], v9 offset:6688\n"
        "ds_read_b32 v167, v0 offset:21604\n"
        "v_perm_b32 v78, v159, v158, s63\n"
        "v_perm_b32 v77, 0, v92, s63\n"
        "v_mov_b32_dpp v79, v19 row_shr:4 row_mask:0xf bank_mask:0xf bound_ctrl:0\n"
        "v_mfma_f32_16x16x32_bf16 v[68:71], v[80:83], v[44:47], 0\n"
        "s_waitcnt lgkmcnt(3)\n"
        "v_writelane_b32 v10, s69, 56\n"
        "v_add_u32 v11, s30, v11\n"
        "v_smfmac_f32_16x16x64_bf16 v[68:71], v[76:79], v[116:123], v18\n"
        "s_add_u32 s37, s35, 98112\n"
        "ds_read_b64_tr_b16 v[118:119], v9 offset:6704\n"
        "v_mfma_f32_16x16x32_bf16 v[36:39], v[80:83], v[104:107], 0\n"
        "s_waitcnt lgkmcnt(3)\n"
        "v_smfmac_f32_16x16x64_bf16 v[36:39], v[76:79], v[94:101], v18\n"
        "ds_read_b64_tr_b16 v[96:97], v9 offset:6720\n"
        "v_mfma_f32_16x16x32_bf16 v[40:43], v[80:83], v[28:31], 0\n"
        "s_waitcnt lgkmcnt(3)\n"
        "s_waitcnt lgkmcnt(2)\n"
        "v_readfirstlane_b32 s40, v167\n"
        "v_smfmac_f32_16x16x64_bf16 v[40:43], v[76:79], v[144:151], v18\n"
        "buffer_store_dwordx3 v[164:166], v14, s[8:11], s37 offen offset:960 sc0 sc1 nt\n"
        "ds_read_b64_tr_b16 v[146:147], v9 offset:6736\n"
        "s_add_u32 s37, s35, 588672\n"
        "s_add_u32 s46, s37, 1344\n"
        "s_cmp_lt_i32 s40, s68\n"
        "s_mov_b64 exec, 1\n"
        "ds_inc_u32 v0, v1 offset:21608\n"
        "s_mov_b64 exec, -1\n"
        "s_cbranch_scc1 L42A_%=\n"
        "L43A_%=:\n"
        "ds_read_b128 v[124:127], v7 offset:40064\n"
        "v_mfma_f32_16x16x32_bf16 v[64:67], v[80:83], v[48:51], 0\n"
        "s_waitcnt lgkmcnt(4)\n"
        "ds_read_b128 v[132:135], v7 offset:43392\n"
        "v_smfmac_f32_16x16x64_bf16 v[64:67], v[76:79], v[116:123], v18 abid:1\n"
        "buffer_store_dwordx3 v[112:114], v12, s[44:47], s37 offen offset:1152 sc0 sc1 nt\n"
        "ds_read_b128 v[128:131], v7 offset:40128\n"
        "ds_read_b128 v[136:139], v7 offset:43456\n"
        "ds_read_b128 v[160:163], v7 offset:40192\n"
        "ds_read_b128 v[52:55], v7 offset:43520\n"
        "v_mfma_f32_16x16x32_bf16 v[104:107], v[80:83], v[108:111], 0\n"
        "s_waitcnt lgkmcnt(7)\n"
        "s_mov_b32 s36, s35\n"
        "v_smfmac_f32_16x16x64_bf16 v[104:107], v[76:79], v[94:101], v18 abid:1\n"
        "s_mov_b64 exec, s[54:55]\n"
        "v_add_u32 v16, s29, v16\n"
        "v_add_u32 v8, s34, v8\n"
        "s_mov_b64 exec, -1\n"
        "v_mfma_f32_16x16x32_bf16 v[88:91], v[80:83], v[32:35], 0\n"
        "s_mov_b32 s35, s39\n"
        "v_smfmac_f32_16x16x64_bf16 v[88:91], v[76:79], v[144:151], v18 abid:1\n"
        "s_mov_b64 exec, s[50:51]\n"
        "v_add_u32 v5, s29, v5\n"
        "v_add_u32 v6, s34, v6\n"
        "s_mov_b64 exec, -1\n"
        "s_setprio 0\n"
        "CNEXTA_%=:\n"
        "s_cmp_ge_u32 s31, s14\n"
        "s_cbranch_scc1 CEND_%=\n"
        "s_waitcnt lgkmcnt(5)\n"
        "v_mfma_f32_16x16x32_bf16 v[24:27], v[168:171], v[124:127], 0\n"
        "v_cvt_pk_bf16_f32 v156, v152, v56\n"
        "v_cvt_pk_bf16_f32 v157, v72, v20\n"
        "s_waitcnt lgkmcnt(3)\n"
        "v_mfma_f32_16x16x32_bf16 v[24:27], v[172:175], v[128:131], v[24:27]\n"
        "ds_read_b128 v[44:47], v16 offset:128\n"
        "v_cvt_pk_bf16_f32 v158, v84, v60\n"
        "s_waitcnt lgkmcnt(2)\n"
        "v_mfma_f32_16x16x32_bf16 v[24:27], v[176:179], v[160:163], v[24:27]\n"
        "ds_read_b128 v[48:51], v5\n"
        "v_cvt_pk_bf16_f32 v164, v153, v57\n"
        "v_mfma_f32_16x16x32_bf16 v[32:35], v[188:191], v[160:163], 0\n"
        "v_cvt_pk_bf16_f32 v165, v73, v21\n"
        "v_cvt_pk_bf16_f32 v166, v85, v61\n"
        "v_mfma_f32_16x16x32_bf16 v[32:35], v[184:187], v[128:131], v[32:35]\n"
        "v_cvt_pk_bf16_f32 v146, v154, v58\n"
        "v_cvt_pk_bf16_f32 v147, v74, v22\n"
        "v_mfma_f32_16x16x32_bf16 v[32:35], v[180:183], v[124:127], v[32:35]\n"
        "s_add_u32 s37, s36, 196224\n"
        "ds_read_b128 v[92:95], v7 offset:46720\n"
        "ds_read_b128 v[96:99], v7 offset:46784\n"
        "ds_read_b128 v[116:119], v7 offset:46848\n"
        "v_mfma_f32_16x16x32_bf16 v[76:79], v[192:195], v[124:127], 0\n"
        "v_perm_b32 v80, v25, v24, s63\n"
        "v_perm_b32 v81, v27, v26, s63\n"
        "v_mfma_f32_16x16x32_bf16 v[76:79], v[196:199], v[128:131], v[76:79]\n"
        "v_perm_b32 v82, v33, v32, s63\n"
        "v_perm_b32 v83, v35, v34, s63\n"
        "v_mfma_f32_16x16x32_bf16 v[76:79], v[200:203], v[160:163], v[76:79]\n"
        "buffer_store_dwordx3 v[156:158], v12, s[8:11], s37 offen offset:384 sc0 sc1 nt\n"
        "v_cvt_pk_bf16_f32 v148, v86, v62\n"
        "v_cvt_pk_bf16_f32 v150, v155, v59\n"
        "v_cvt_pk_bf16_f32 v151, v75, v23\n"
        "v_cvt_pk_bf16_f32 v152, v87, v63\n"
        "s_waitcnt lgkmcnt(3)\n"
        "v_mfma_f32_16x16x32_bf16 v[32:35], v[44:47], v[160:163], 0\n"
        "v_cvt_pk_bf16_f32 v72, v68, v36\n"
        "v_perm_b32 v84, v77, v76, s63\n"
        "v_smfmac_f32_16x16x64_bf16 v[32:35], v[48:51], v[124:131], v4\n"
        "s_add_u32 s37, s36, 196224\n"
        "v_perm_b32 v85, v79, v78, s63\n"
        "v_mfma_f32_16x16x32_bf16 v[24:27], v[44:47], v[52:55], 0\n"
        "v_cvt_pk_bf16_f32 v73, v40, v64\n"
        "v_cvt_pk_bf16_f32 v74, v104, v88\n"
        "v_smfmac_f32_16x16x64_bf16 v[24:27], v[48:51], v[132:139], v4\n"
        "s_waitcnt lgkmcnt(0)\n"
        "s_setprio 2\n"
        "v_cvt_pk_bf16_f32 v156, v69, v37\n"
        "v_cvt_pk_bf16_f32 v157, v41, v65\n"
        "v_mfma_f32_16x16x32_bf16 v[100:103], v[44:47], v[116:119], 0\n"
        "v_cvt_pk_bf16_f32 v158, v105, v89\n"
        "v_cvt_pk_bf16_f32 v76, v70, v38\n"
        "v_smfmac_f32_16x16x64_bf16 v[100:103], v[48:51], v[92:99], v4\n"
        "buffer_store_dwordx3 v[164:166], v13, s[8:11], s37 offen offset:1152 sc0 sc1 nt\n"
        "v_cvt_pk_bf16_f32 v77, v42, v66\n"
        "v_cvt_pk_bf16_f32 v78, v106, v90\n"
        "v_cvt_pk_bf16_f32 v88, v71, v39\n"
        "v_cvt_pk_bf16_f32 v89, v43, v67\n"
        "v_cvt_pk_bf16_f32 v90, v107, v91\n"
        "v_mfma_f32_16x16x32_bf16 v[20:23], v[212:215], v[160:163], 0\n"
        "s_add_u32 s37, s36, 294336\n"
        "ds_read_b64 v[154:155], v11 offset:192\n"
        "v_mfma_f32_16x16x32_bf16 v[20:23], v[208:211], v[128:131], v[20:23]\n"
        "ds_read_b32 v153, v10\n"
        "v_mfma_f32_16x16x32_bf16 v[20:23], v[204:207], v[124:127], v[20:23]\n"
        "v_mfma_f32_16x16x32_bf16 v[28:31], v[216:219], v[124:127], 0\n"
        "v_mfma_f32_16x16x32_bf16 v[28:31], v[220:223], v[128:131], v[28:31]\n"
        "v_mfma_f32_16x16x32_bf16 v[28:31], v[224:227], v[160:163], v[28:31]\n"
        "buffer_store_dwordx3 v[146:148], v12, s[8:11], s37 offen offset:576 sc0 sc1 nt\n"
        "s_add_u32 s37, s36, 392448\n"
        "s_waitcnt lgkmcnt(1)\n"
        "v_add_f32_dpp v155, v34, v35 quad_perm:[0,1,2,3] row_mask:0x1 bank_mask:0xf\n"
        "v_add_f32_dpp v154, v32, v33 quad_perm:[0,1,2,3] row_mask:0x1 bank_mask:0xf\n"
        "v_perm_b32 v86, v21, v20, s63\n"
        "v_mfma_f32_16x16x32_bf16 v[44:47], v[236:239], v[160:163], 0\n"
        "v_perm_b32 v87, v23, v22, s63\n"
        "v_perm_b32 v20, v29, v28, s63\n"
        "v_mfma_f32_16x16x32_bf16 v[44:47], v[232:235], v[128:131], v[44:47]\n"
        "v_perm_b32 v21, v31, v30, s63\n"
        "v_add_f32_dpp v155, v26, v27 quad_perm:[0,1,2,3] row_mask:0x2 bank_mask:0xf\n"
        "v_mfma_f32_16x16x32_bf16 v[44:47], v[228:231], v[124:127], v[44:47]\n"
        "buffer_store_dwordx3 v[150:152], v12, s[8:11], s37 offen sc0 sc1 nt\n"
        "v_add_f32_dpp v154, v24, v25 quad_perm:[0,1,2,3] row_mask:0x2 bank_mask:0xf\n"
        "v_add_f32_dpp v155, v102, v103 quad_perm:[0,1,2,3] row_mask:0x4 bank_mask:0xf\n"
        "v_mul_f32 v165, s52, v155\n"
        "v_add_f32_dpp v154, v100, v101 quad_perm:[0,1,2,3] row_mask:0x4 bank_mask:0xf\n"
        "s_waitcnt lgkmcnt(0)\n"
        "v_fma_f32 v165, -v165, v155, v153\n"
        "v_add_f32_e64 v165, s53, |v165|\n"
        "v_perm_b32 v23, v47, v46, s63\n"
        "v_perm_b32 v22, v45, v44, s63\n"
        "v_mfma_f32_16x16x32_bf16 v[100:103], v[124:127], v[80:83], 0\n"
        "v_rsq_f32 v153, v165\n"
        "v_mfma_f32_16x16x32_bf16 v[100:103], v[128:131], v[84:87], v[100:103]\n"
        "s_add_u32 s37, s36, 392448\n"
        "ds_read_b128 v[104:107], v8 offset:128\n"
        "v_and_b32 v56, s28, v153\n"
        "v_mfma_f32_16x16x32_bf16 v[100:103], v[160:163], v[20:23], v[100:103]\n"
        "ds_read2_b32 v[108:109], v6 offset0:0 offset1:2\n"
        "ds_read2_b32 v[110:111], v6 offset0:8 offset1:10\n"
        "v_mfma_f32_16x16x32_bf16 v[144:147], v[52:55], v[20:23], 0\n"
        "v_and_b32 v164, s28, v153\n"
        "v_sub_f32 v167, v153, v56\n"
        "v_mfma_f32_16x16x32_bf16 v[144:147], v[136:139], v[84:87], v[144:147]\n"
        "v_cvt_pk_bf16_f32 v57, v167, v56\n"
        "v_cvt_pk_bf16_f32 v165, v164, v167\n"
        "v_mfma_f32_16x16x32_bf16 v[144:147], v[132:135], v[80:83], v[144:147]\n"
        "buffer_store_dwordx3 v[72:74], v15, s[8:11], s37 offen offset:768 sc0 sc1 nt\n"
        "v_mul_f32 v154, v154, v153\n"
        "v_readlane_b32 s58, v153, 56\n"
        "s_mov_b64 exec, s[56:57]\n"
        "v_mov_b32 v154, 0xff800000\n"
        "s_mov_b64 exec, -1\n"
        "v_mfma_f32_16x16x4_4b_bf16 v[32:47], v[56:57], v[164:165], 0 blgp:4\n"
        "v_mul_f32 v154, s58, v154\n"
        "v_exp_f32 v154, v154\n"
        "v_mfma_f32_16x16x32_bf16 v[24:27], v[92:95], v[80:83], 0\n"
        "s_add_u32 s37, s36, 490560\n"
        "v_add_f32_dpp v167, v154, v154 row_ror:8 row_mask:0xf bank_mask:0xf\n"
        "v_mul_f32 v154, v154, v153\n"
        "v_mfma_f32_16x16x32_bf16 v[24:27], v[96:99], v[84:87], v[24:27]\n"
        "v_add_f32_dpp v167, v167, v167 row_ror:4 row_mask:0xf bank_mask:0xf\n"
        "v_mfma_f32_16x16x32_bf16 v[24:27], v[116:119], v[20:23], v[24:27]\n"
        "s_waitcnt lgkmcnt(0)\n"
        "v_add_f32_dpp v167, v167, v167 row_ror:2 row_mask:0xf bank_mask:0xf\n"
        "v_mfma_f32_16x16x32_bf16 v[148:151], v[104:107], v[20:23], 0\n"
        "v_mul_f32 v100, v100, v32\n"
        "v_mul_f32 v101, v101, v33\n"
        "v_smfmac_f32_16x16x64_bf16 v[148:151], v[108:111], v[80:87], v4\n"
        "buffer_store_dwordx3 v[156:158], v12, s[8:11], s37 offen offset:192 sc0 sc1 nt\n"
        "v_mul_f32 v102, v102, v34\n"
        "v_mul_f32 v103, v103, v35\n"
        "v_exp_f32 v100, v100\n"
        "v_mul_f32 v144, v144, v36\n"
        "v_mfma_f32_16x16x32_bf16 v[20:23], v[168:171], v[132:135], 0\n"
        "s_add_u32 s37, s36, 490560\n"
        "v_exp_f32 v101, v101\n"
        "v_mfma_f32_16x16x32_bf16 v[20:23], v[172:175], v[136:139], v[20:23]\n"
        "v_exp_f32 v102, v102\n"
        "v_mfma_f32_16x16x32_bf16 v[20:23], v[176:179], v[52:55], v[20:23]\n"
        "v_exp_f32 v103, v103\n"
        "v_add_f32 v151, v100, v101\n"
        "v_mfma_f32_16x16x32_bf16 v[28:31], v[188:191], v[52:55], 0\n"
        "v_add_f32 v151, v151, v102\n"
        "v_add_f32 v151, v151, v103\n"
        "v_mfma_f32_16x16x32_bf16 v[28:31], v[184:187], v[136:139], v[28:31]\n"
        "v_mul_f32 v145, v145, v37\n"
        "v_mul_f32 v146, v146, v38\n"
        "v_mfma_f32_16x16x32_bf16 v[28:31], v[180:183], v[132:135], v[28:31]\n"
        "buffer_store_dwordx3 v[76:78], v14, s[8:11], s37 offen offset:960 sc0 sc1 nt\n"
        "v_perm_b32 v68, v21, v20, s63\n"
        "v_perm_b32 v69, v23, v22, s63\n"
        "v_mul_f32 v147, v147, v39\n"
        "v_exp_f32 v144, v144\n"
        "v_mfma_f32_16x16x32_bf16 v[20:23], v[192:195], v[132:135], 0\n"
        "s_add_u32 s37, s36, 588672\n"
        "v_exp_f32 v145, v145\n"
        "v_mfma_f32_16x16x32_bf16 v[20:23], v[196:199], v[136:139], v[20:23]\n"
        "v_perm_b32 v70, v29, v28, s63\n"
        "v_perm_b32 v71, v31, v30, s63\n"
        "v_mfma_f32_16x16x32_bf16 v[20:23], v[200:203], v[52:55], v[20:23]\n"
        "buffer_store_dwordx3 v[88:90], v12, s[8:11], s37 offen offset:384 sc0 sc1 nt\n"
        "v_exp_f32 v146, v146\n"
        "v_exp_f32 v147, v147\n"
        "v_add_f32 v151, v151, v144\n"
        "v_add_f32 v151, v151, v145\n"
        "v_mfma_f32_16x16x32_bf16 v[28:31], v[212:215], v[52:55], 0\n"
        "v_add_f32 v151, v151, v146\n"
        "v_add_f32 v151, v151, v147\n"
        "v_mfma_f32_16x16x32_bf16 v[28:31], v[208:211], v[136:139], v[28:31]\n"
        "v_perm_b32 v72, v21, v20, s63\n"
        "v_perm_b32 v73, v23, v22, s63\n"
        "v_mfma_f32_16x16x32_bf16 v[28:31], v[204:207], v[132:135], v[28:31]\n"
        "v_mul_f32 v24, v24, v40\n"
        "v_mul_f32 v25, v25, v41\n"
        "v_mul_f32 v26, v26, v42\n"
        "v_mfma_f32_16x16x32_bf16 v[20:23], v[216:219], v[132:135], 0\n"
        "v_mul_f32 v27, v27, v43\n"
        "v_add_f32 v148, v148, v149\n"
        "v_mfma_f32_16x16x32_bf16 v[20:23], v[220:223], v[136:139], v[20:23]\n"
        "v_exp_f32 v24, v24\n"
        "v_mfma_f32_16x16x32_bf16 v[20:23], v[224:227], v[52:55], v[20:23]\n"
        "v_perm_b32 v74, v29, v28, s63\n"
        "v_perm_b32 v75, v31, v30, s63\n"
        "v_add_f32 v151, v151, v24\n"
        "v_mfma_f32_16x16x32_bf16 v[64:67], v[236:239], v[52:55], 0\n"
        "v_exp_f32 v25, v25\n"
        "v_mfma_f32_16x16x32_bf16 v[64:67], v[232:235], v[136:139], v[64:67]\n"
        "v_exp_f32 v26, v26\n"
        "v_mfma_f32_16x16x32_bf16 v[64:67], v[228:231], v[132:135], v[64:67]\n"
        "v_perm_b32 v48, v21, v20, s63\n"
        "v_perm_b32 v49, v23, v22, s63\n"
        "v_mfma_f32_16x16x4_4b_bf16 v[28:43], v[56:57], v[164:165], 0 blgp:5\n"
        "v_exp_f32 v27, v27\n"
        "v_add_f32 v151, v151, v25\n"
        "v_add_f32 v151, v151, v26\n"
        "v_mul_f32 v148, v148, v44\n"
        "v_exp_f32 v148, v148\n"
        "v_perm_b32 v50, v65, v64, s63\n"
        "v_perm_b32 v51, v67, v66, s63\n"
        "v_mfma_f32_16x16x32_bf16 v[58:61], v[124:127], v[68:71], 0\n"
        "v_add_f32 v151, v151, v27\n"
        "v_add_f32_dpp v167, v167, v167 row_ror:1 row_mask:0xf bank_mask:0xf\n"
        "v_mfma_f32_16x16x32_bf16 v[58:61], v[128:131], v[72:75], v[58:61]\n"
        "v_add_f32_dpp v151, v148, v151 quad_perm:[0,1,2,3] row_mask:0x4 bank_mask:0xf\n"
        "v_mfma_f32_16x16x32_bf16 v[58:61], v[160:163], v[48:51], v[58:61]\n"
        "v_mfma_f32_16x16x32_bf16 v[112:115], v[52:55], v[48:51], 0\n"
        "v_permlane32_swap_b32 v151, v167\n"
        "v_mfma_f32_16x16x32_bf16 v[112:115], v[136:139], v[72:75], v[112:115]\n"
        "v_add_f32 v151, v151, v167\n"
        "v_mov_b32 v167, v151\n"
        "v_mfma_f32_16x16x32_bf16 v[112:115], v[132:135], v[68:71], v[112:115]\n"
        "v_mfma_f32_16x16x32_bf16 v[20:23], v[92:95], v[68:71], 0\n"
        "v_permlane16_swap_b32 v151, v167\n"
        "v_mfma_f32_16x16x32_bf16 v[20:23], v[96:99], v[72:75], v[20:23]\n"
        "v_add_f32 v151, v151, v167\n"
        "v_mul_f32 v58, v58, v28\n"
        "v_mfma_f32_16x16x32_bf16 v[20:23], v[116:119], v[48:51], v[20:23]\n"
        "v_rcp_f32 v123, v151\n"
        "v_mul_f32 v59, v59, v29\n"
        "v_and_b32 v158, s28, v123\n"
        "v_mfma_f32_16x16x32_bf16 v[62:65], v[104:107], v[48:51], 0\n"
        "v_sub_f32 v152, v123, v158\n"
        "v_cvt_pk_bf16_f32 v159, v158, v152\n"
        "v_smfmac_f32_16x16x64_bf16 v[62:65], v[108:111], v[68:75], v4\n"
        "v_min3_f32 v167, v151, v123, v153\n"
        "v_mfma_f32_16x16x32_bf16 v[48:51], v[168:171], v[92:95], 0\n"
        "v_mul_f32 v60, v60, v30\n"
        "v_mul_f32 v61, v61, v31\n"
        "v_mfma_f32_16x16x32_bf16 v[48:51], v[172:175], v[96:99], v[48:51]\n"
        "v_exp_f32 v58, v58\n"
        "v_mfma_f32_16x16x32_bf16 v[48:51], v[176:179], v[116:119], v[48:51]\n"
        "v_exp_f32 v59, v59\n"
        "v_exp_f32 v60, v60\n"
        "v_mfma_f32_16x16x32_bf16 v[68:71], v[188:191], v[116:119], 0\n"
        "v_exp_f32 v61, v61\n"
        "v_mfma_f32_16x16x32_bf16 v[68:71], v[184:187], v[96:99], v[68:71]\n"
        "v_add_f32 v155, v58, v59\n"
        "v_add_f32 v155, v155, v60\n"
        "v_mfma_f32_16x16x32_bf16 v[68:71], v[180:183], v[92:95], v[68:71]\n"
        "v_perm_b32 v44, v49, v48, s63\n"
        "v_perm_b32 v45, v51, v50, s63\n"
        "v_add_f32 v155, v155, v61\n"
        "v_mfma_f32_16x16x32_bf16 v[28:31], v[192:195], v[92:95], 0\n"
        "v_mul_f32 v112, v112, v32\n"
        "v_mul_f32 v113, v113, v33\n"
        "v_mfma_f32_16x16x32_bf16 v[28:31], v[196:199], v[96:99], v[28:31]\n"
        "v_mul_f32 v114, v114, v34\n"
        "v_perm_b32 v46, v69, v68, s63\n"
        "v_mfma_f32_16x16x32_bf16 v[28:31], v[200:203], v[116:119], v[28:31]\n"
        "v_perm_b32 v47, v71, v70, s63\n"
        "v_mul_f32 v115, v115, v35\n"
        "v_exp_f32 v112, v112\n"
        "v_mfma_f32_16x16x32_bf16 v[68:71], v[212:215], v[116:119], 0\n"
        "v_exp_f32 v113, v113\n"
        "v_mfma_f32_16x16x32_bf16 v[68:71], v[208:211], v[96:99], v[68:71]\n"
        "v_exp_f32 v114, v114\n"
        "v_mfma_f32_16x16x32_bf16 v[68:71], v[204:207], v[92:95], v[68:71]\n"
        "v_perm_b32 v48, v29, v28, s63\n"
        "v_perm_b32 v49, v31, v30, s63\n"
        "v_add_f32 v155, v155, v112\n"
        "v_mfma_f32_16x16x32_bf16 v[64:67], v[216:219], v[92:95], 0\n"
        "v_exp_f32 v115, v115\n"
        "v_mfma_f32_16x16x32_bf16 v[64:67], v[220:223], v[96:99], v[64:67]\n"
        "v_add_f32 v155, v155, v113\n"
        "v_add_f32 v155, v155, v114\n"
        "v_mfma_f32_16x16x32_bf16 v[64:67], v[224:227], v[116:119], v[64:67]\n"
        "v_perm_b32 v50, v69, v68, s63\n"
        "v_perm_b32 v51, v71, v70, s63\n"
        "v_add_f32 v155, v155, v115\n"
        "v_mul_f32 v20, v20, v36\n"
        "v_mfma_f32_16x16x32_bf16 v[28:31], v[236:239], v[116:119], 0\n"
        "v_mul_f32 v21, v21, v37\n"
        "v_mul_f32 v22, v22, v38\n"
        "v_mfma_f32_16x16x32_bf16 v[28:31], v[232:235], v[96:99], v[28:31]\n"
        "v_perm_b32 v150, v65, v64, s63\n"
        "v_perm_b32 v151, v67, v66, s63\n"
        "v_mfma_f32_16x16x32_bf16 v[28:31], v[228:231], v[92:95], v[28:31]\n"
        "v_exp_f32 v20, v20\n"
        "v_mfma_f32_16x16x4_4b_bf16 v[76:91], v[56:57], v[158:159], 0 blgp:4\n"
        "v_exp_f32 v21, v21\n"
        "v_mul_f32 v23, v23, v39\n"
        "v_exp_f32 v22, v22\n"
        "v_exp_f32 v23, v23\n"
        "v_add_f32 v155, v155, v20\n"
        "v_add_f32 v155, v155, v21\n"
        "v_perm_b32 v152, v29, v28, s63\n"
        "v_perm_b32 v153, v31, v30, s63\n"
        "v_mfma_f32_16x16x32_bf16 v[36:39], v[124:127], v[44:47], 0\n"
        "v_add_f32 v155, v155, v22\n"
        "v_add_f32 v155, v155, v23\n"
        "v_mfma_f32_16x16x32_bf16 v[36:39], v[128:131], v[48:51], v[36:39]\n"
        "v_mul_f32 v100, v100, v76\n"
        "v_mul_f32 v101, v101, v77\n"
        "v_mfma_f32_16x16x32_bf16 v[36:39], v[160:163], v[150:153], v[36:39]\n"
        "v_mul_f32 v102, v102, v78\n"
        "v_mul_f32 v103, v103, v79\n"
        "v_mul_f32 v144, v144, v80\n"
        "v_mul_f32 v145, v145, v81\n"
        "v_mfma_f32_16x16x32_bf16 v[140:143], v[52:55], v[150:153], 0\n"
        "v_mul_f32 v146, v146, v82\n"
        "v_mul_f32 v147, v147, v83\n"
        "v_mfma_f32_16x16x32_bf16 v[140:143], v[136:139], v[48:51], v[140:143]\n"
        "v_mul_f32 v24, v24, v84\n"
        "v_mul_f32 v25, v25, v85\n"
        "v_mfma_f32_16x16x32_bf16 v[140:143], v[132:135], v[44:47], v[140:143]\n"
        "v_add_f32 v62, v62, v63\n"
        "v_mul_f32 v62, v62, v40\n"
        "v_mul_f32 v26, v26, v86\n"
        "v_mfma_f32_16x16x4_4b_bf16 v[64:79], v[56:57], v[164:165], 0 blgp:6\n"
        "v_exp_f32 v62, v62\n"
        "v_mul_f32 v27, v27, v87\n"
        "v_fma_f32 v148, v148, v88, v19\n"
        "v_add_f32_dpp v155, v62, v155 quad_perm:[0,1,2,3] row_mask:0x4 bank_mask:0xf\n"
        "v_perm_b32 v40, v101, v100, s63\n"
        "v_perm_b32 v41, v103, v102, s63\n"
        "v_perm_b32 v42, v145, v144, s63\n"
        "v_perm_b32 v43, v147, v146, s63\n"
        "v_perm_b32 v164, v25, v24, s63\n"
        "v_mfma_f32_16x16x32_bf16 v[156:159], v[92:95], v[44:47], 0\n"
        "v_perm_b32 v166, v27, v26, s63\n"
        "v_perm_b32 v165, 0, v148, s63\n"
        "v_mfma_f32_16x16x32_bf16 v[156:159], v[96:99], v[48:51], v[156:159]\n"
        "v_mul_f32 v36, v36, v64\n"
        "v_mul_f32 v37, v37, v65\n"
        "v_mfma_f32_16x16x32_bf16 v[156:159], v[116:119], v[150:153], v[156:159]\n"
        "v_mul_f32 v38, v38, v66\n"
        "v_mul_f32 v39, v39, v67\n"
        "v_mul_f32 v140, v140, v68\n"
        "v_mfma_f32_16x16x32_bf16 v[92:95], v[104:107], v[150:153], 0\n"
        "ds_read_b128 v[88:91], v17 offset:40064\n"
        "v_mul_f32 v141, v141, v69\n"
        "v_smfmac_f32_16x16x64_bf16 v[92:95], v[108:111], v[44:51], v4\n"
        "ds_read_b128 v[84:87], v17 offset:40128\n"
        "ds_read_b128 v[150:153], v17 offset:40192\n"
        "v_mfma_f32_16x16x32_bf16 v[48:51], v[124:127], v[240:243], 0\n"
        "v_exp_f32 v36, v36\n"
        "v_mfma_f32_16x16x32_bf16 v[48:51], v[128:131], v[244:247], v[48:51]\n"
        "ds_read_b128 v[80:83], v0 offset:5136\n"
        "v_mul_f32 v142, v142, v70\n"
        "v_mfma_f32_16x16x32_bf16 v[48:51], v[160:163], v[248:251], v[48:51]\n"
        "ds_read_b128 v[32:35], v0 offset:4112\n"
        "v_mul_f32 v143, v143, v71\n"
        "v_mfma_f32_16x16x32_bf16 v[24:27], v[52:55], v[248:251], 0\n"
        "v_exp_f32 v37, v37\n"
        "v_mfma_f32_16x16x32_bf16 v[24:27], v[136:139], v[244:247], v[24:27]\n"
        "v_exp_f32 v38, v38\n"
        "v_mfma_f32_16x16x32_bf16 v[24:27], v[132:135], v[240:243], v[24:27]\n"
        "s_waitcnt lgkmcnt(2)\n"
        "v_perm_b32 v44, v49, v48, s63\n"
        "v_perm_b32 v45, v51, v50, s63\n"
        "v_add_f32 v145, v36, v37\n"
        "v_mfma_f32_16x16x32_bf16 v[104:107], v[88:91], v[240:243], 0\n"
        "v_exp_f32 v39, v39\n"
        "v_mfma_f32_16x16x32_bf16 v[104:107], v[84:87], v[244:247], v[104:107]\n"
        "v_add_f32 v145, v145, v38\n"
        "v_perm_b32 v46, v25, v24, s63\n"
        "v_mfma_f32_16x16x32_bf16 v[104:107], v[150:153], v[248:251], v[104:107]\n"
        "s_waitcnt lgkmcnt(0)\n"
        "v_perm_b32 v47, v27, v26, s63\n"
        "v_add_f32 v145, v145, v39\n"
        "v_exp_f32 v140, v140\n"
        "v_mfma_f32_16x16x32_bf16 v[64:67], v[150:153], v[80:83], 0\n"
        "v_exp_f32 v141, v141\n"
        "v_mfma_f32_16x16x32_bf16 v[64:67], v[84:87], v[32:35], v[64:67]\n"
        "ds_read_b128 v[146:149], v0 offset:6176\n"
        "v_perm_b32 v116, v105, v104, s63\n"
        "v_mfma_f32_16x16x32_bf16 v[64:67], v[88:91], v[252:255], v[64:67]\n"
        "ds_read_b128 v[24:27], v0 offset:7200\n"
        "ds_read_b128 v[108:111], v0 offset:8224\n"
        "v_mfma_f32_16x16x32_bf16 v[100:103], v[132:135], v[252:255], 0\n"
        "v_perm_b32 v120, v107, v106, s63\n"
        "v_add_f32 v145, v145, v140\n"
        "v_mfma_f32_16x16x32_bf16 v[100:103], v[136:139], v[32:35], v[100:103]\n"
        "v_exp_f32 v142, v142\n"
        "v_mfma_f32_16x16x32_bf16 v[100:103], v[52:55], v[80:83], v[100:103]\n"
        "v_perm_b32 v94, v65, v64, s63\n"
        "v_perm_b32 v98, v67, v66, s63\n"
        "v_add_f32 v145, v145, v141\n"
        "v_mfma_f32_16x16x32_bf16 v[68:71], v[160:163], v[80:83], 0\n"
        "v_exp_f32 v143, v143\n"
        "v_mfma_f32_16x16x32_bf16 v[68:71], v[128:131], v[32:35], v[68:71]\n"
        "v_add_f32 v145, v145, v142\n"
        "v_add_f32 v145, v145, v143\n"
        "v_mfma_f32_16x16x32_bf16 v[68:71], v[124:127], v[252:255], v[68:71]\n"
        "s_waitcnt lgkmcnt(0)\n"
        "v_perm_b32 v106, v101, v100, s63\n"
        "v_perm_b32 v107, v103, v102, s63\n"
        "v_mul_f32 v156, v156, v72\n"
        "v_mul_f32 v157, v157, v73\n"
        "v_mfma_f32_16x16x32_bf16 v[32:35], v[124:127], v[146:149], 0\n"
        "v_mul_f32 v158, v158, v74\n"
        "v_mul_f32 v159, v159, v75\n"
        "v_mfma_f32_16x16x32_bf16 v[32:35], v[128:131], v[24:27], v[32:35]\n"
        "ds_read_b128 v[48:51], v0 offset:11312\n"
        "v_perm_b32 v104, v69, v68, s63\n"
        "v_mfma_f32_16x16x32_bf16 v[32:35], v[160:163], v[108:111], v[32:35]\n"
        "ds_read_b128 v[80:83], v0 offset:10288\n"
        "ds_read_b128 v[64:67], v0 offset:9264\n"
        "v_mfma_f32_16x16x32_bf16 v[100:103], v[52:55], v[108:111], 0\n"
        "v_perm_b32 v105, v71, v70, s63\n"
        "v_add_f32 v92, v92, v93\n"
        "v_mfma_f32_16x16x32_bf16 v[100:103], v[136:139], v[24:27], v[100:103]\n"
        "v_exp_f32 v156, v156\n"
        "v_mfma_f32_16x16x32_bf16 v[100:103], v[132:135], v[146:149], v[100:103]\n"
        "v_perm_b32 v28, v33, v32, s63\n"
        "v_perm_b32 v29, v35, v34, s63\n"
        "v_add_f32 v145, v145, v156\n"
        "v_mfma_f32_16x16x32_bf16 v[70:73], v[88:91], v[146:149], 0\n"
        "v_exp_f32 v157, v157\n"
        "v_mfma_f32_16x16x32_bf16 v[70:73], v[84:87], v[24:27], v[70:73]\n"
        "v_exp_f32 v158, v158\n"
        "v_mfma_f32_16x16x32_bf16 v[70:73], v[150:153], v[108:111], v[70:73]\n"
        "s_waitcnt lgkmcnt(0)\n"
        "v_perm_b32 v30, v101, v100, s63\n"
        "v_perm_b32 v31, v103, v102, s63\n"
        "v_exp_f32 v159, v159\n"
        "v_mfma_f32_16x16x32_bf16 v[32:35], v[150:153], v[48:51], 0\n"
        "v_add_f32 v145, v145, v157\n"
        "v_add_f32 v145, v145, v158\n"
        "v_mfma_f32_16x16x32_bf16 v[32:35], v[84:87], v[80:83], v[32:35]\n"
        "ds_read_b128 v[100:103], v0 offset:12352\n"
        "v_perm_b32 v144, v71, v70, s63\n"
        "v_mfma_f32_16x16x32_bf16 v[32:35], v[88:91], v[64:67], v[32:35]\n"
        "ds_read_b128 v[108:111], v0 offset:13376\n"
        "ds_read_b128 v[24:27], v0 offset:14400\n"
        "v_mfma_f32_16x16x32_bf16 v[68:71], v[132:135], v[64:67], 0\n"
        "v_perm_b32 v148, v73, v72, s63\n"
        "v_mul_f32 v92, v92, v76\n"
        "v_mfma_f32_16x16x32_bf16 v[68:71], v[136:139], v[80:83], v[68:71]\n"
        "v_add_f32 v145, v145, v159\n"
        "v_readlane_b32 s59, v123, 32\n"
        "v_mfma_f32_16x16x32_bf16 v[68:71], v[52:55], v[48:51], v[68:71]\n"
        "v_perm_b32 v117, v33, v32, s63\n"
        "v_perm_b32 v121, v35, v34, s63\n"
        "v_exp_f32 v92, v92\n"
        "v_mfma_f32_16x16x32_bf16 v[72:75], v[160:163], v[48:51], 0\n"
        "v_mul_f32 v154, s59, v154\n"
        "v_add_f32_dpp v145, v92, v145 quad_perm:[0,1,2,3] row_mask:0x4 bank_mask:0xf\n"
        "v_mfma_f32_16x16x32_bf16 v[72:75], v[128:131], v[80:83], v[72:75]\n"
        "v_perm_b32 v154, 0, v154, s63\n"
        "v_perm_b32 v50, v69, v68, s63\n"
        "v_mfma_f32_16x16x32_bf16 v[72:75], v[124:127], v[64:67], v[72:75]\n"
        "s_waitcnt lgkmcnt(0)\n"
        "v_perm_b32 v51, v71, v70, s63\n"
        "v_permlane32_swap_b32 v155, v145\n"
        "v_mfma_f32_16x16x32_bf16 v[80:83], v[124:127], v[100:103], 0\n"
        "v_add_f32 v155, v155, v145\n"
        "v_mov_b32 v149, v155\n"
        "v_mfma_f32_16x16x32_bf16 v[80:83], v[128:131], v[108:111], v[80:83]\n"
        "ds_read_b128 v[32:35], v0 offset:17488\n"
        "v_perm_b32 v48, v73, v72, s63\n"
        "v_mfma_f32_16x16x32_bf16 v[80:83], v[160:163], v[24:27], v[80:83]\n"
        "ds_read_b128 v[68:71], v0 offset:16464\n"
        "ds_read_b128 v[64:67], v0 offset:15440\n"
        "v_mfma_f32_16x16x32_bf16 v[76:79], v[52:55], v[24:27], 0\n"
        "v_perm_b32 v49, v75, v74, s63\n"
        "v_mfma_f32_16x16x32_bf16 v[76:79], v[136:139], v[108:111], v[76:79]\n"
        "v_permlane16_swap_b32 v155, v149\n"
        "v_mfma_f32_16x16x32_bf16 v[76:79], v[132:135], v[100:103], v[76:79]\n"
        "v_add_f32 v155, v155, v149\n"
        "v_rcp_f32 v147, v155\n"
        "v_mfma_f32_16x16x32_bf16 v[72:75], v[88:91], v[100:103], 0\n"
        "ds_read2_b32 v[122:123], v11 offset0:0 offset1:3\n"
        "v_min3_f32 v167, v167, v155, v147\n"
        "v_mfma_f32_16x16x32_bf16 v[72:75], v[84:87], v[108:111], v[72:75]\n"
        "ds_read_b64_tr_b16 v[118:119], v9 offset:40064\n"
        "v_and_b32 v102, s28, v147\n"
        "v_mfma_f32_16x16x32_bf16 v[72:75], v[150:153], v[24:27], v[72:75]\n"
        "s_waitcnt lgkmcnt(2)\n"
        "ds_read2_b32 v[100:101], v11 offset0:1 offset1:4\n"
        "v_sub_f32 v149, v147, v102\n"
        "v_cvt_pk_bf16_f32 v103, v102, v149\n"
        "v_perm_b32 v108, v81, v80, s63\n"
        "v_mfma_f32_16x16x32_bf16 v[24:27], v[150:153], v[32:35], 0\n"
        "ds_read_b64_tr_b16 v[96:97], v9 offset:40080\n"
        "v_perm_b32 v109, v83, v82, s63\n"
        "v_mfma_f32_16x16x32_bf16 v[24:27], v[84:87], v[68:71], v[24:27]\n"
        "ds_read2_b32 v[150:151], v11 offset0:2 offset1:5\n"
        "v_perm_b32 v110, v77, v76, s63\n"
        "v_mfma_f32_16x16x32_bf16 v[24:27], v[88:91], v[64:67], v[24:27]\n"
        "ds_read_b64_tr_b16 v[146:147], v9 offset:40096\n"
        "v_perm_b32 v111, v79, v78, s63\n"
        "v_perm_b32 v95, v73, v72, s63\n"
        "v_mfma_f32_16x16x4_4b_bf16 v[76:91], v[56:57], v[102:103], 0 blgp:4\n"
        "v_perm_b32 v99, v75, v74, s63\n"
        "v_cmp_nle_f32 vcc, 0x2b800000, v167\n"
        "v_mov_b32_dpp v167, v19 row_shr:4 row_mask:0xf bank_mask:0xf bound_ctrl:0\n"
        "ds_write_b16 v1, v154\n"
        "v_perm_b32 v145, v25, v24, s63\n"
        "v_perm_b32 v149, v27, v26, s63\n"
        "s_andn2_b64 vcc, vcc, s[56:57]\n"
        "s_cbranch_scc1 CREDOB_%=\n"
        "v_mfma_f32_16x16x32_bf16 v[24:27], v[132:135], v[64:67], 0\n"
        "s_mov_b32 exec_hi, 0xffff\n"
        "v_add_u32 v10, -40064, v10\n"
        "s_mov_b32 exec_hi, -1\n"
        "v_mfma_f32_16x16x32_bf16 v[24:27], v[136:139], v[68:71], v[24:27]\n"
        "v_mul_f32 v58, v58, v76\n"
        "v_mul_f32 v59, v59, v77\n"
        "v_mfma_f32_16x16x32_bf16 v[24:27], v[52:55], v[32:35], v[24:27]\n"
        "s_add_u32 s31, s31, 1\n"
        "v_mul_f32 v60, v60, v78\n"
        "v_mul_f32 v61, v61, v79\n"
        "v_mul_f32 v112, v112, v80\n"
        "v_mul_f32 v113, v113, v81\n"
        "v_mfma_f32_16x16x32_bf16 v[52:55], v[160:163], v[32:35], 0\n"
        "s_cmp_ge_u32 s31, s71\n"
        "v_mul_f32 v114, v114, v82\n"
        "v_mul_f32 v115, v115, v83\n"
        "v_mfma_f32_16x16x32_bf16 v[52:55], v[128:131], v[68:71], v[52:55]\n"
        "s_cbranch_scc1 L38B_%=\n"
        "L39B_%=:\n"
        "v_mul_f32 v20, v20, v84\n"
        "v_perm_b32 v34, v25, v24, s63\n"
        "v_mfma_f32_16x16x32_bf16 v[52:55], v[124:127], v[64:67], v[52:55]\n"
        "s_add_u32 s16, s16, 256\n"
        "v_perm_b32 v35, v27, v26, s63\n"
        "v_mul_f32 v21, v21, v85\n"
        "v_mfma_f32_16x16x32_bf16 v[124:127], v[40:43], v[44:47], 0\n"
        "s_waitcnt lgkmcnt(0)\n"
        "v_mul_f32 v22, v22, v86\n"
        "v_mul_f32 v23, v23, v87\n"
        "v_smfmac_f32_16x16x64_bf16 v[124:127], v[164:167], v[116:123], v18\n"
        "s_cmp_ge_u32 s31, s43\n"
        "s_cselect_b32 s39, s22, s16\n"
        "ds_read_b64_tr_b16 v[118:119], v9 offset:40112\n"
        "v_mfma_f32_16x16x4_4b_bf16 v[72:87], v[56:57], v[102:103], 0 blgp:6\n"
        "v_perm_b32 v32, v53, v52, s63\n"
        "v_perm_b32 v33, v55, v54, s63\n"
        "v_fma_f32 v62, v62, v88, v19\n"
        "v_perm_b32 v68, v59, v58, s63\n"
        "v_perm_b32 v69, v61, v60, s63\n"
        "v_perm_b32 v70, v113, v112, s63\n"
        "v_perm_b32 v71, v115, v114, s63\n"
        "v_perm_b32 v24, v21, v20, s63\n"
        "v_perm_b32 v26, v23, v22, s63\n"
        "v_mfma_f32_16x16x32_bf16 v[88:91], v[40:43], v[104:107], 0\n"
        "s_cmp_gt_u32 s31, s43\n"
        "v_perm_b32 v25, 0, v62, s63\n"
        "v_mul_f32 v36, v36, v72\n"
        "v_smfmac_f32_16x16x64_bf16 v[88:91], v[164:167], v[94:101], v18\n"
        "s_cselect_b32 s39, s23, s39\n"
        "ds_read_b64_tr_b16 v[96:97], v9 offset:40128\n"
        "v_mfma_f32_16x16x32_bf16 v[56:59], v[40:43], v[28:31], 0\n"
        "s_mul_hi_u32 s38, s39, 58835169\n"
        "ds_read_b128 v[128:131], v2\n"
        "v_mul_f32 v37, v37, v73\n"
        "v_smfmac_f32_16x16x64_bf16 v[56:59], v[164:167], v[144:151], v18\n"
        "s_mul_i32 s39, s39, 1344\n"
        "ds_read_b64_tr_b16 v[146:147], v9 offset:40144\n"
        "ds_read_b128 v[52:55], v3\n"
        "v_mfma_f32_16x16x32_bf16 v[112:115], v[40:43], v[48:51], 0\n"
        "s_waitcnt lgkmcnt(4)\n"
        "v_mul_f32 v38, v38, v74\n"
        "v_mul_f32 v39, v39, v75\n"
        "v_smfmac_f32_16x16x64_bf16 v[112:115], v[164:167], v[116:123], v18 abid:1\n"
        "s_mul_i32 s38, s38, 588672\n"
        "s_add_u32 s39, s39, s38\n"
        "v_mul_f32 v140, v140, v76\n"
        "v_cvt_pk_bf16_f32 v152, v124, v88\n"
        "v_mul_f32 v141, v141, v77\n"
        "v_mfma_f32_16x16x32_bf16 v[72:75], v[40:43], v[108:111], 0\n"
        "s_add_u32 s39, s39, s17\n"
        "v_mov_b64 v[60:61], 0\n"
        "s_waitcnt lgkmcnt(3)\n"
        "v_smfmac_f32_16x16x64_bf16 v[72:75], v[164:167], v[94:101], v18 abid:1\n"
        "s_mul_i32 s68, s31, 3\n"
        "v_mov_b64 v[62:63], 0\n"
        "v_cvt_pk_bf16_f32 v153, v56, v112\n"
        "v_mfma_f32_16x16x32_bf16 v[64:67], v[40:43], v[32:35], 0\n"
        "s_waitcnt lgkmcnt(1)\n"
        "v_mov_b64 v[20:21], 0\n"
        "v_mov_b64 v[22:23], 0\n"
        "v_smfmac_f32_16x16x64_bf16 v[64:67], v[164:167], v[144:151], v18 abid:1\n"
        "s_waitcnt lgkmcnt(0)\n"
        "s_bitcmp1_b32 s31, 0\n"
        "v_mov_b32_dpp v27, v19 row_shr:4 row_mask:0xf bank_mask:0xf bound_ctrl:0\n"
        "s_cselect_b32 s38, 3, 6\n"
        "v_smfmac_f32_16x16x64_bf16 v[60:63], v[128:131], v[44:51], v4\n"
        "s_add_u32 s68, s68, s38\n"
        "v_mov_b64 v[40:41], 0\n"
        "v_smfmac_f32_16x16x64_bf16 v[60:63], v[52:55], v[116:123], v4 abid:1\n"
        "s_add_u32 s37, s35, 0\n"
        "s_cmp_lt_u32 s31, s14\n"
        "ds_read_b64_tr_b16 v[118:119], v9 offset:43392\n"
        "v_mov_b64 v[42:43], 0\n"
        "v_smfmac_f32_16x16x64_bf16 v[20:23], v[128:131], v[104:111], v4\n"
        "s_cselect_b32 s68, s68, 0\n"
        "v_cvt_pk_bf16_f32 v154, v72, v64\n"
        "v_smfmac_f32_16x16x64_bf16 v[20:23], v[52:55], v[94:101], v4 abid:1\n"
        "s_and_b32 s38, s31, 7\n"
        "ds_read_b64_tr_b16 v[96:97], v9 offset:43408\n"
        "v_cvt_pk_bf16_f32 v160, v125, v89\n"
        "v_smfmac_f32_16x16x64_bf16 v[40:43], v[128:131], v[28:35], v4\n"
        "s_cselect_b32 s30, 1, -7\n"
        "v_cvt_pk_bf16_f32 v88, v126, v90\n"
        "v_smfmac_f32_16x16x64_bf16 v[40:43], v[52:55], v[144:151], v4 abid:1\n"
        "buffer_store_dwordx3 v[152:154], v12, s[8:11], s37 offen sc0 sc1 nt\n"
        "ds_read_b64_tr_b16 v[146:147], v9 offset:43424\n"
        "v_cvt_pk_bf16_f32 v164, v127, v91\n"
        "v_cvt_pk_bf16_f32 v161, v57, v113\n"
        "v_cvt_pk_bf16_f32 v89, v58, v114\n"
        "v_cvt_pk_bf16_f32 v165, v59, v115\n"
        "v_cvt_pk_bf16_f32 v162, v73, v65\n"
        "v_mfma_f32_16x16x32_bf16 v[152:155], v[68:71], v[44:47], 0\n"
        "s_waitcnt lgkmcnt(2)\n"
        "v_cvt_pk_bf16_f32 v90, v74, v66\n"
        "v_cvt_pk_bf16_f32 v166, v75, v67\n"
        "v_smfmac_f32_16x16x64_bf16 v[152:155], v[24:27], v[116:123], v18\n"
        "s_mul_i32 s29, s30, 192\n"
        "s_mul_i32 s30, s30, 392\n"
        "ds_read_b64_tr_b16 v[118:119], v9 offset:43440\n"
        "v_add_f32 v60, v60, v61\n"
        "v_add_f32 v62, v62, v63\n"
        "v_add_f32 v20, v20, v21\n"
        "v_mfma_f32_16x16x32_bf16 v[56:59], v[68:71], v[104:107], 0\n"
        "s_waitcnt lgkmcnt(2)\n"
        "v_add_f32 v22, v22, v23\n"
        "v_cvt_pk_bf16_f32 v112, v60, v20\n"
        "v_smfmac_f32_16x16x64_bf16 v[56:59], v[24:27], v[94:101], v18\n"
        "s_add_u32 s37, s35, 0\n"
        "s_cmp_eq_u32 s69, s18\n"
        "ds_read_b64_tr_b16 v[96:97], v9 offset:43456\n"
        "v_add_f32 v40, v40, v41\n"
        "v_add_f32 v42, v42, v43\n"
        "v_cvt_pk_bf16_f32 v113, v40, v62\n"
        "v_mfma_f32_16x16x32_bf16 v[72:75], v[68:71], v[28:31], 0\n"
        "s_waitcnt lgkmcnt(2)\n"
        "v_cvt_pk_bf16_f32 v114, v22, v42\n"
        "v_mul_f32 v142, v142, v78\n"
        "v_smfmac_f32_16x16x64_bf16 v[72:75], v[24:27], v[144:151], v18\n"
        "buffer_store_dwordx3 v[160:162], v15, s[8:11], s37 offen offset:768 sc0 sc1 nt\n"
        "ds_read_b64_tr_b16 v[146:147], v9 offset:43472\n"
        "s_cselect_b32 s34, -7488, s70\n"
        "v_mul_f32 v143, v143, v79\n"
        "v_mul_f32 v156, v156, v80\n"
        "v_mul_f32 v157, v157, v81\n"
        "v_mul_f32 v158, v158, v82\n"
        "v_mfma_f32_16x16x32_bf16 v[20:23], v[68:71], v[48:51], 0\n"
        "s_waitcnt lgkmcnt(2)\n"
        "s_add_u32 s69, s69, s34\n"
        "v_mul_f32 v159, v159, v83\n"
        "v_smfmac_f32_16x16x64_bf16 v[20:23], v[24:27], v[116:123], v18 abid:1\n"
        "s_add_u32 s37, s35, 98112\n"
        "ds_read_b64_tr_b16 v[118:119], v9 offset:46720\n"
        "v_fma_f32 v92, v92, v84, v19\n"
        "v_mfma_f32_16x16x32_bf16 v[84:87], v[68:71], v[108:111], 0\n"
        "s_waitcnt lgkmcnt(2)\n"
        "v_perm_b32 v80, v37, v36, s63\n"
        "v_perm_b32 v81, v39, v38, s63\n"
        "v_smfmac_f32_16x16x64_bf16 v[84:87], v[24:27], v[94:101], v18 abid:1\n"
        "ds_read_b64_tr_b16 v[96:97], v9 offset:46736\n"
        "v_perm_b32 v82, v141, v140, s63\n"
        "v_mfma_f32_16x16x32_bf16 v[60:63], v[68:71], v[32:35], 0\n"
        "s_waitcnt lgkmcnt(2)\n"
        "v_perm_b32 v83, v143, v142, s63\n"
        "v_perm_b32 v76, v157, v156, s63\n"
        "v_smfmac_f32_16x16x64_bf16 v[60:63], v[24:27], v[144:151], v18 abid:1\n"
        "buffer_store_dwordx3 v[88:90], v12, s[8:11], s37 offen offset:192 sc0 sc1 nt\n"
        "ds_read_b64_tr_b16 v[146:147], v9 offset:46752\n"
        "ds_read_b32 v167, v0 offset:21600\n"
        "v_perm_b32 v78, v159, v158, s63\n"
        "v_perm_b32 v77, 0, v92, s63\n"
        "v_mov_b32_dpp v79, v19 row_shr:4 row_mask:0xf bank_mask:0xf bound_ctrl:0\n"
        "v_mfma_f32_16x16x32_bf16 v[68:71], v[80:83], v[44:47], 0\n"
        "s_waitcnt lgkmcnt(3)\n"
        "v_writelane_b32 v10, s69, 56\n"
        "v_add_u32 v11, s30, v11\n"
        "v_smfmac_f32_16x16x64_bf16 v[68:71], v[76:79], v[116:123], v18\n"
        "s_add_u32 s37, s35, 98112\n"
        "ds_read_b64_tr_b16 v[118:119], v9 offset:46768\n"
        "v_mfma_f32_16x16x32_bf16 v[36:39], v[80:83], v[104:107], 0\n"
        "s_waitcnt lgkmcnt(3)\n"
        "v_smfmac_f32_16x16x64_bf16 v[36:39], v[76:79], v[94:101], v18\n"
        "ds_read_b64_tr_b16 v[96:97], v9 offset:46784\n"
        "v_mfma_f32_16x16x32_bf16 v[40:43], v[80:83], v[28:31], 0\n"
        "s_waitcnt lgkmcnt(3)\n"
        "s_waitcnt lgkmcnt(2)\n"
        "v_readfirstlane_b32 s40, v167\n"
        "v_smfmac_f32_16x16x64_bf16 v[40:43], v[76:79], v[144:151], v18\n"
        "buffer_store_dwordx3 v[164:166], v14, s[8:11], s37 offen offset:960 sc0 sc1 nt\n"
        "ds_read_b64_tr_b16 v[146:147], v9 offset:46800\n"
        "s_add_u32 s37, s35, 588672\n"
        "s_add_u32 s46, s37, 1344\n"
        "s_cmp_lt_i32 s40, s68\n"
        "s_mov_b64 exec, 1\n"
        "ds_inc_u32 v0, v1 offset:21612\n"
        "s_mov_b64 exec, -1\n"
        "s_cbranch_scc1 L42B_%=\n"
        "L43B_%=:\n"
        "ds_read_b128 v[124:127], v7 offset:0\n"
        "v_mfma_f32_16x16x32_bf16 v[64:67], v[80:83], v[48:51], 0\n"
        "s_waitcnt lgkmcnt(4)\n"
        "ds_read_b128 v[132:135], v7 offset:3328\n"
        "v_smfmac_f32_16x16x64_bf16 v[64:67], v[76:79], v[116:123], v18 abid:1\n"
        "buffer_store_dwordx3 v[112:114], v12, s[44:47], s37 offen offset:1152 sc0 sc1 nt\n"
        "ds_read_b128 v[128:131], v7 offset:64\n"
        "ds_read_b128 v[136:139], v7 offset:3392\n"
        "ds_read_b128 v[160:163], v7 offset:128\n"
        "ds_read_b128 v[52:55], v7 offset:3456\n"
        "v_mfma_f32_16x16x32_bf16 v[104:107], v[80:83], v[108:111], 0\n"
        "s_waitcnt lgkmcnt(7)\n"
        "s_mov_b32 s36, s35\n"
        "v_smfmac_f32_16x16x64_bf16 v[104:107], v[76:79], v[94:101], v18 abid:1\n"
        "s_mov_b64 exec, s[54:55]\n"
        "v_add_u32 v16, s29, v16\n"
        "v_add_u32 v8, s34, v8\n"
        "s_mov_b64 exec, -1\n"
        "v_mfma_f32_16x16x32_bf16 v[88:91], v[80:83], v[32:35], 0\n"
        "s_mov_b32 s35, s39\n"
        "v_smfmac_f32_16x16x64_bf16 v[88:91], v[76:79], v[144:151], v18 abid:1\n"
        "s_mov_b64 exec, s[50:51]\n"
        "v_add_u32 v5, s29, v5\n"
        "v_add_u32 v6, s34, v6\n"
        "s_mov_b64 exec, -1\n"
        "s_setprio 0\n"
        "CNEXTB_%=:\n"
        "s_cmp_ge_u32 s31, s14\n"
        "s_cbranch_scc1 CEND_%=\n"
        "s_cmp_gt_i32 s29, 0\n"
        "s_cbranch_scc1 CTOP_%=\n"
        "s_branch CFULL_%=\n"
        "CEND_%=:\n"
        "s_waitcnt lgkmcnt(0)\n"
        "s_nop 15\n"
        "v_cvt_pk_bf16_f32 v248, v152, v56\n"
        "v_cvt_pk_bf16_f32 v249, v72, v20\n"
        "v_cvt_pk_bf16_f32 v250, v84, v60\n"
        "s_add_u32 s37, s36, 196224\n"
        "buffer_store_dwordx3 v[248:250], v12, s[8:11], s37 offen offset:384 sc0 sc1 nt\n"
        "v_cvt_pk_bf16_f32 v252, v153, v57\n"
        "v_cvt_pk_bf16_f32 v253, v73, v21\n"
        "v_cvt_pk_bf16_f32 v254, v85, v61\n"
        "s_add_u32 s37, s36, 196224\n"
        "buffer_store_dwordx3 v[252:254], v13, s[8:11], s37 offen offset:1152 sc0 sc1 nt\n"
        "v_cvt_pk_bf16_f32 v248, v154, v58\n"
        "v_cvt_pk_bf16_f32 v249, v74, v22\n"
        "v_cvt_pk_bf16_f32 v250, v86, v62\n"
        "s_add_u32 s37, s36, 294336\n"
        "buffer_store_dwordx3 v[248:250], v12, s[8:11], s37 offen offset:576 sc0 sc1 nt\n"
        "v_cvt_pk_bf16_f32 v252, v155, v59\n"
        "v_cvt_pk_bf16_f32 v253, v75, v23\n"
        "v_cvt_pk_bf16_f32 v254, v87, v63\n"
        "s_add_u32 s37, s36, 392448\n"
        "buffer_store_dwordx3 v[252:254], v12, s[8:11], s37 offen sc0 sc1 nt\n"
        "v_cvt_pk_bf16_f32 v248, v68, v36\n"
        "v_cvt_pk_bf16_f32 v249, v40, v64\n"
        "v_cvt_pk_bf16_f32 v250, v104, v88\n"
        "s_add_u32 s37, s36, 392448\n"
        "v_cvt_pk_bf16_f32 v252, v69, v37\n"
        "v_cvt_pk_bf16_f32 v253, v41, v65\n"
        "v_cvt_pk_bf16_f32 v254, v105, v89\n"
        "buffer_store_dwordx3 v[248:250], v15, s[8:11], s37 offen offset:768 sc0 sc1 nt\n"
        "s_add_u32 s37, s36, 490560\n"
        "v_cvt_pk_bf16_f32 v246, v70, v38\n"
        "v_cvt_pk_bf16_f32 v247, v42, v66\n"
        "v_cvt_pk_bf16_f32 v248, v106, v90\n"
        "buffer_store_dwordx3 v[252:254], v12, s[8:11], s37 offen offset:192 sc0 sc1 nt\n"
        "s_add_u32 s37, s36, 490560\n"
        "buffer_store_dwordx3 v[246:248], v14, s[8:11], s37 offen offset:960 sc0 sc1 nt\n"
        "s_add_u32 s37, s36, 588672\n"
        "v_cvt_pk_bf16_f32 v252, v71, v39\n"
        "v_cvt_pk_bf16_f32 v253, v43, v67\n"
        "v_cvt_pk_bf16_f32 v254, v107, v91\n"
        "buffer_store_dwordx3 v[252:254], v12, s[8:11], s37 offen offset:384 sc0 sc1 nt\n"
        "s_nop 1\n"
        "s_endpgm\n"
        "L38A_%=:\n"
        "s_nop 7\n"
        "v_mov_b32 v153, 21600\n"
        "L36A_%=:\n"
        "ds_read_b32 v163, v153 offset:64\n"
        "s_waitcnt lgkmcnt(0)\n"
        "v_readfirstlane_b32 s40, v163\n"
        "s_nop 3\n"
        "s_cmp_ge_i32 s40, 1\n"
        "s_cbranch_scc1 L37A_%=\n"
        "s_sleep 1\n"
        "s_sub_u32 s41, s41, 1\n"
        "s_cbranch_scc0 L36A_%=\n"
        "s_endpgm\n"
        "L37A_%=:\n"
        "v_mbcnt_lo_u32_b32 v163, -1, 0\n"
        "v_lshl_add_u32 v153, v163, 2, v153\n"
        "ds_read_b32 v163, v153 offset:68\n"
        "s_mov_b32 s14, s40\n"
        "s_mov_b32 s71, -1\n"
        "s_waitcnt lgkmcnt(0)\n"
        "v_readlane_b32 s22, v163, 0\n"
        "v_readlane_b32 s23, v163, 1\n"
        "s_nop 3\n"
        "s_nop 1\n"
        "s_branch L39A_%=\n"
        "L42A_%=:\n"
        "s_nop 7\n"
        "L40A_%=:\n"
        "ds_read_b32 v167, v0 offset:21604\n"
        "s_waitcnt lgkmcnt(0)\n"
        "v_readfirstlane_b32 s40, v167\n"
        "s_nop 3\n"
        "s_cmp_ge_i32 s40, s68\n"
        "s_cbranch_scc1 L41A_%=\n"
        "s_sleep 1\n"
        "s_sub_u32 s41, s41, 1\n"
        "s_cbranch_scc0 L40A_%=\n"
        "s_endpgm\n"
        "L41A_%=:\n"
        "s_nop 1\n"
        "s_branch L43A_%=\n"
        "L38B_%=:\n"
        "s_nop 7\n"
        "v_mov_b32 v153, 21600\n"
        "L36B_%=:\n"
        "ds_read_b32 v163, v153 offset:64\n"
        "s_waitcnt lgkmcnt(0)\n"
        "v_readfirstlane_b32 s40, v163\n"
        "s_nop 3\n"
        "s_cmp_ge_i32 s40, 1\n"
        "s_cbranch_scc1 L37B_%=\n"
        "s_sleep 1\n"
        "s_sub_u32 s41, s41, 1\n"
        "s_cbranch_scc0 L36B_%=\n"
        "s_endpgm\n"
        "L37B_%=:\n"
        "v_mbcnt_lo_u32_b32 v163, -1, 0\n"
        "v_lshl_add_u32 v153, v163, 2, v153\n"
        "ds_read_b32 v163, v153 offset:68\n"
        "s_mov_b32 s14, s40\n"
        "s_mov_b32 s71, -1\n"
        "s_waitcnt lgkmcnt(0)\n"
        "v_readlane_b32 s22, v163, 0\n"
        "v_readlane_b32 s23, v163, 1\n"
        "s_nop 3\n"
        "s_nop 1\n"
        "s_branch L39B_%=\n"
        "L42B_%=:\n"
        "s_nop 7\n"
        "L40B_%=:\n"
        "ds_read_b32 v167, v0 offset:21600\n"
        "s_waitcnt lgkmcnt(0)\n"
        "v_readfirstlane_b32 s40, v167\n"
        "s_nop 3\n"
        "s_cmp_ge_i32 s40, s68\n"
        "s_cbranch_scc1 L41B_%=\n"
        "s_sleep 1\n"
        "s_sub_u32 s41, s41, 1\n"
        "s_cbranch_scc0 L40B_%=\n"
        "s_endpgm\n"
        "L41B_%=:\n"
        "s_nop 1\n"
        "s_branch L43B_%=\n"
        "CGRP_%=:\n"
        "s_setprio 3\n"
        "CGRQ_%=:\n"
        "s_waitcnt lgkmcnt(0)\n"
        "s_nop 6\n"
        "ds_read_u16 v166, v0 offset:20576\n"
        "s_ashr_i32 s61, s31, 3\n"
        "s_add_u32 s61, s61, 1\n"
        "L44_%=:\n"
        "ds_read_b32 v167, v0 offset:21660\n"
        "s_waitcnt lgkmcnt(0)\n"
        "v_readfirstlane_b32 s40, v167\n"
        "s_nop 3\n"
        "s_cmp_ge_i32 s40, s61\n"
        "s_cbranch_scc1 L45_%=\n"
        "s_sleep 1\n"
        "s_sub_u32 s41, s41, 1\n"
        "s_cbranch_scc0 L44_%=\n"
        "s_endpgm\n"
        "L45_%=:\n"
        "s_sub_u32 s60, s69, 102048\n"
        "v_add_u32 v166, s60, v166\n"
        "v_subrev_u32 v165, 8320, v166\n"
        "v_min_u32 v166, v166, v165\n"
        "v_add_u32 v166, 101856, v166\n"
        "s_mov_b32 exec_lo, 0xff00ff00\n"
        "s_mov_b32 exec_hi, 0xff00ff00\n"
        "v_mov_b32 v166, 18912\n"
        "s_mov_b64 exec, -1\n"
        "ds_read_b128 v[28:31], v166 offset:64\n"
        "ds_read_b128 v[24:27], v166 offset:128\n"
        "ds_read_b128 v[32:35], v166\n"
        "s_waitcnt lgkmcnt(1)\n"
        "s_waitcnt lgkmcnt(0)\n"
        "v_mfma_f32_16x16x32_bf16 v[44:47], v[168:171], v[32:35], 0\n"
        "ds_read_u16 v162, v0 offset:20578\n"
        "v_lshrrev_b32 v157, 8, v0\n"
        "v_mfma_f32_16x16x32_bf16 v[44:47], v[172:175], v[28:31], v[44:47]\n"
        "ds_read_u16 v166, v0 offset:20580\n"
        "v_mfma_f32_16x16x32_bf16 v[44:47], v[176:179], v[24:27], v[44:47]\n"
        "ds_read_u16 v167, v0 offset:20582\n"
        "ds_read_u16 v163, v0 offset:20588\n"
        "ds_read_u16 v151, v0 offset:20590\n"
        "v_mfma_f32_16x16x32_bf16 v[76:79], v[180:183], v[32:35], 0\n"
        "v_mfma_f32_16x16x32_bf16 v[76:79], v[184:187], v[28:31], v[76:79]\n"
        "v_mfma_f32_16x16x32_bf16 v[76:79], v[188:191], v[24:27], v[76:79]\n"
        "v_mfma_f32_16x16x32_bf16 v[48:51], v[192:195], v[32:35], 0\n"
        "s_waitcnt lgkmcnt(4)\n"
        "v_add_u32 v162, s60, v162\n"
        "v_subrev_u32 v165, 8320, v162\n"
        "v_mfma_f32_16x16x32_bf16 v[48:51], v[196:199], v[28:31], v[48:51]\n"
        "v_min_u32 v162, v162, v165\n"
        "v_add_u32 v162, 101856, v162\n"
        "v_mfma_f32_16x16x32_bf16 v[48:51], v[200:203], v[24:27], v[48:51]\n"
        "s_mov_b32 exec_lo, 0\n"
        "v_mov_b32 v162, 18912\n"
        "s_mov_b32 exec_lo, -1\n"
        "ds_read_b64 v[160:161], v162\n"
        "v_perm_b32 v52, v45, v44, s63\n"
        "v_perm_b32 v53, v47, v46, s63\n"
        "v_perm_b32 v54, v77, v76, s63\n"
        "v_perm_b32 v55, v79, v78, s63\n"
        "v_mfma_f32_16x16x32_bf16 v[44:47], v[204:207], v[32:35], 0\n"
        "v_perm_b32 v80, v49, v48, s63\n"
        "v_perm_b32 v81, v51, v50, s63\n"
        "v_mfma_f32_16x16x32_bf16 v[44:47], v[208:211], v[28:31], v[44:47]\n"
        "s_waitcnt lgkmcnt(1)\n"
        "ds_read_b64 v[164:165], v163\n"
        "v_add_u32 v166, s19, v166\n"
        "v_mfma_f32_16x16x32_bf16 v[44:47], v[212:215], v[24:27], v[44:47]\n"
        "ds_read_b64 v[158:159], v162 offset:16\n"
        "v_mad_i32_i24 v157, v157, -8, v166\n"
        "v_add_u32 v151, s19, v151\n"
        "v_add_u32 v167, s19, v167\n"
        "v_mfma_f32_16x16x32_bf16 v[48:51], v[216:219], v[32:35], 0\n"
        "ds_read_b64 v[148:149], v162 offset:32\n"
        "v_mfma_f32_16x16x32_bf16 v[48:51], v[220:223], v[28:31], v[48:51]\n"
        "v_mfma_f32_16x16x32_bf16 v[48:51], v[224:227], v[24:27], v[48:51]\n"
        "s_mov_b32 exec_lo, 0x00ff00ff\n"
        "s_mov_b32 exec_hi, 0x00ff00ff\n"
        "ds_write2_b32 v157, v52, v54 offset0:0 offset1:1\n"
        "ds_write2_b32 v157, v53, v55 offset0:16 offset1:17\n"
        "s_mov_b64 exec, -1\n"
        "v_mfma_f32_16x16x32_bf16 v[76:79], v[228:231], v[32:35], 0\n"
        "v_perm_b32 v82, v45, v44, s63\n"
        "v_perm_b32 v83, v47, v46, s63\n"
        "v_mfma_f32_16x16x32_bf16 v[76:79], v[232:235], v[28:31], v[76:79]\n"
        "v_perm_b32 v44, v49, v48, s63\n"
        "v_perm_b32 v45, v51, v50, s63\n"
        "v_mfma_f32_16x16x32_bf16 v[76:79], v[236:239], v[24:27], v[76:79]\n"
        "s_waitcnt lgkmcnt(2)\n"
        "s_mov_b32 exec_lo, 0x00ff00ff\n"
        "s_mov_b32 exec_hi, 0x00ff00ff\n"
        "ds_write2_b32 v157, v80, v82 offset0:8 offset1:9\n"
        "ds_write2_b32 v157, v81, v83 offset0:24 offset1:25\n"
        "s_mov_b64 exec, -1\n"
        "v_mfma_f32_4x4x4_16b_bf16 v[100:103], v[160:161], v[164:165], 0\n"
        "v_mfma_f32_4x4x4_16b_bf16 v[92:95], v[158:159], v[164:165], 0\n"
        "v_mfma_f32_4x4x4_16b_bf16 v[48:51], v[148:149], v[164:165], 0\n"
        "v_mov_b32 v96, 0x3f803f80\n"
        "v_mov_b32 v97, 0x3f803f80\n"
        "v_mov_b32 v98, 0x3f803f80\n"
        "v_mov_b32 v99, 0x3f803f80\n"
        "v_mfma_f32_16x16x32_bf16 v[124:127], v[32:35], v[240:243], 0\n"
        "v_perm_b32 v46, v77, v76, s63\n"
        "v_perm_b32 v47, v79, v78, s63\n"
        "v_mfma_f32_16x16x32_bf16 v[124:127], v[28:31], v[244:247], v[124:127]\n"
        "ds_read_b128 v[120:123], v0 offset:4112\n"
        "v_mfma_f32_16x16x32_bf16 v[124:127], v[24:27], v[248:251], v[124:127]\n"
        "ds_read_b128 v[108:111], v0 offset:5136\n"
        "v_mfma_f32_16x16x32_bf16 v[76:79], v[96:99], v[32:35], 0\n"
        "v_mfma_f32_16x16x32_bf16 v[76:79], v[96:99], v[28:31], v[76:79]\n"
        "ds_read_b128 v[116:119], v0 offset:6176\n"
        "v_mfma_f32_16x16x32_bf16 v[76:79], v[96:99], v[24:27], v[76:79]\n"
        "ds_read_b128 v[112:115], v0 offset:7200\n"
        "ds_read_b128 v[96:99], v0 offset:8224\n"
        "ds_read_b64 v[160:161], v162 offset:48\n"
        "v_perm_b32 v142, v100, v124, s63\n"
        "v_perm_b32 v144, v101, v125, s63\n"
        "v_perm_b32 v158, v102, v126, s63\n"
        "v_perm_b32 v156, v103, v127, s63\n"
        "v_mfma_f32_16x16x32_bf16 v[128:131], v[32:35], v[52:55], 0\n"
        "v_mfma_f32_16x16x32_bf16 v[128:131], v[28:31], v[80:83], v[128:131]\n"
        "ds_read_b64 v[148:149], v162 offset:64\n"
        "v_mfma_f32_16x16x32_bf16 v[128:131], v[24:27], v[44:47], v[128:131]\n"
        "s_waitcnt lgkmcnt(5)\n"
        "v_mfma_f32_16x16x32_bf16 v[80:83], v[32:35], v[252:255], 0\n"
        "ds_read_b64 v[146:147], v162 offset:80\n"
        "v_mfma_f32_16x16x32_bf16 v[80:83], v[28:31], v[120:123], v[80:83]\n"
        "ds_read_b128 v[52:55], v0 offset:9264\n"
        "v_mfma_f32_16x16x32_bf16 v[80:83], v[24:27], v[108:111], v[80:83]\n"
        "s_waitcnt lgkmcnt(3)\n"
        "ds_read_b128 v[108:111], v0 offset:10288\n"
        "ds_read_b128 v[100:103], v0 offset:11312\n"
        "s_mov_b32 vcc_lo, 0x22222222\n"
        "v_cndmask_b32 v141, v128, v129, vcc\n"
        "s_mov_b32 vcc_lo, 0x44444444\n"
        "v_cndmask_b32 v141, v141, v130, vcc\n"
        "v_mfma_f32_16x16x32_bf16 v[124:127], v[32:35], v[116:119], 0\n"
        "s_mov_b32 vcc_lo, 0x88888888\n"
        "v_cndmask_b32 v141, v141, v131, vcc\n"
        "v_perm_b32 v143, v92, v80, s63\n"
        "v_mfma_f32_16x16x32_bf16 v[124:127], v[28:31], v[112:115], v[124:127]\n"
        "ds_read_b128 v[132:135], v0 offset:12352\n"
        "v_perm_b32 v145, v93, v81, s63\n"
        "v_mfma_f32_16x16x32_bf16 v[124:127], v[24:27], v[96:99], v[124:127]\n"
        "ds_read_b128 v[136:139], v0 offset:13376\n"
        "ds_read_b128 v[128:131], v0 offset:14400\n"
        "v_perm_b32 v159, v94, v82, s63\n"
        "v_perm_b32 v157, v95, v83, s63\n"
        "v_mfma_f32_4x4x4_16b_bf16 v[112:115], v[160:161], v[164:165], 0\n"
        "s_waitcnt lgkmcnt(5)\n"
        "v_mfma_f32_4x4x4_16b_bf16 v[80:83], v[148:149], v[164:165], 0\n"
        "ds_read_b128 v[120:123], v0 offset:15440\n"
        "ds_read_b128 v[116:119], v0 offset:16464\n"
        "ds_read_b128 v[96:99], v0 offset:17488\n"
        "v_mfma_f32_4x4x4_16b_bf16 v[92:95], v[146:147], v[164:165], 0\n"
        "v_perm_b32 v150, v48, v124, s63\n"
        "v_perm_b32 v148, v49, v125, s63\n"
        "v_perm_b32 v164, v50, v126, s63\n"
        "s_waitcnt lgkmcnt(6)\n"
        "v_mfma_f32_16x16x32_bf16 v[160:163], v[32:35], v[52:55], 0\n"
        "v_perm_b32 v146, v51, v127, s63\n"
        "v_mfma_f32_16x16x32_bf16 v[160:163], v[28:31], v[108:111], v[160:163]\n"
        "v_mfma_f32_16x16x32_bf16 v[160:163], v[24:27], v[100:103], v[160:163]\n"
        "s_waitcnt lgkmcnt(3)\n"
        "s_mov_b64 exec, 0x00f0000f\n"
        "ds_write2_b32 v151, v141, v76 offset0:0 offset1:1\n"
        "s_mov_b64 exec, -1\n"
        "v_mfma_f32_16x16x32_bf16 v[48:51], v[32:35], v[132:135], 0\n"
        "ds_read_b128 v[124:127], v7\n"
        "v_mfma_f32_16x16x32_bf16 v[48:51], v[28:31], v[136:139], v[48:51]\n"
        "ds_read_b128 v[132:135], v7 offset:3328\n"
        "v_perm_b32 v151, v112, v160, s63\n"
        "v_mfma_f32_16x16x32_bf16 v[48:51], v[24:27], v[128:131], v[48:51]\n"
        "s_waitcnt lgkmcnt(3)\n"
        "ds_read_b128 v[128:131], v7 offset:64\n"
        "v_perm_b32 v149, v113, v161, s63\n"
        "v_perm_b32 v165, v114, v162, s63\n"
        "v_perm_b32 v147, v115, v163, s63\n"
        "v_mfma_f32_16x16x32_bf16 v[76:79], v[32:35], v[120:123], 0\n"
        "ds_read_b128 v[136:139], v7 offset:3392\n"
        "v_mfma_f32_16x16x32_bf16 v[76:79], v[28:31], v[116:119], v[76:79]\n"
        "ds_read_b128 v[160:163], v7 offset:128\n"
        "v_perm_b32 v120, v80, v48, s63\n"
        "v_mfma_f32_16x16x32_bf16 v[76:79], v[24:27], v[96:99], v[76:79]\n"
        "s_mov_b32 exec_lo, 0x00ff00ff\n"
        "s_mov_b32 exec_hi, 0x00ff00ff\n"
        "ds_write_b128 v166, v[44:47] offset:128\n"
        "s_mov_b64 exec, -1\n"
        "ds_read_b128 v[52:55], v7 offset:3456\n"
        "v_perm_b32 v122, v81, v49, s63\n"
        "v_perm_b32 v140, v82, v50, s63\n"
        "v_perm_b32 v118, v83, v51, s63\n"
        "v_perm_b32 v121, v92, v76, s63\n"
        "v_perm_b32 v123, v93, v77, s63\n"
        "v_perm_b32 v141, v94, v78, s63\n"
        "v_perm_b32 v119, v95, v79, s63\n"
        "s_mov_b32 exec_hi, 0\n"
        "ds_write2_b64 v167, v[142:143], v[144:145] offset0:0 offset1:49\n"
        "ds_write2_b64 v167, v[158:159], v[156:157] offset0:98 offset1:147\n"
        "s_mov_b32 exec_hi, -1\n"
        "s_mov_b32 exec_hi, 0\n"
        "ds_write2_b64 v167, v[150:151], v[148:149] offset0:1 offset1:50\n"
        "ds_write2_b64 v167, v[164:165], v[146:147] offset0:99 offset1:148\n"
        "s_mov_b32 exec_hi, -1\n"
        "s_mov_b32 exec_hi, 0\n"
        "ds_write2_b64 v167, v[120:121], v[122:123] offset0:2 offset1:51\n"
        "ds_write2_b64 v167, v[140:141], v[118:119] offset0:100 offset1:149\n"
        "s_mov_b32 exec_hi, -1\n"
        "s_waitcnt lgkmcnt(0)\n"
        "s_nop 6\n"
        "s_setprio 0\n"
        "s_setpc_b64 s[64:65]\n"
        "L56A_%=:\n"
        "s_nop 7\n"
        "v_mov_b32 v167, 21600\n"
        "L54A_%=:\n"
        "ds_read_b32 v166, v167 offset:64\n"
        "s_waitcnt lgkmcnt(0)\n"
        "v_readfirstlane_b32 s40, v166\n"
        "s_nop 3\n"
        "s_cmp_ge_i32 s40, 1\n"
        "s_cbranch_scc1 L55A_%=\n"
        "s_sleep 1\n"
        "s_sub_u32 s41, s41, 1\n"
        "s_cbranch_scc0 L54A_%=\n"
        "s_endpgm\n"
        "L55A_%=:\n"
        "v_mbcnt_lo_u32_b32 v166, -1, 0\n"
        "v_lshl_add_u32 v167, v166, 2, v167\n"
        "ds_read_b32 v166, v167 offset:68\n"
        "s_mov_b32 s14, s40\n"
        "s_mov_b32 s71, -1\n"
        "s_waitcnt lgkmcnt(0)\n"
        "v_readlane_b32 s22, v166, 0\n"
        "v_readlane_b32 s23, v166, 1\n"
        "s_nop 3\n"
        "s_nop 1\n"
        "s_branch L57A_%=\n"
        "L60A_%=:\n"
        "s_nop 7\n"
        "L58A_%=:\n"
        "ds_read_b32 v167, v0 offset:21604\n"
        "s_waitcnt lgkmcnt(0)\n"
        "v_readfirstlane_b32 s40, v167\n"
        "s_nop 3\n"
        "s_cmp_ge_i32 s40, s68\n"
        "s_cbranch_scc1 L59A_%=\n"
        "s_sleep 1\n"
        "s_sub_u32 s41, s41, 1\n"
        "s_cbranch_scc0 L58A_%=\n"
        "s_endpgm\n"
        "L59A_%=:\n"
        "s_nop 1\n"
        "s_branch L61A_%=\n"
        "L56B_%=:\n"
        "s_nop 7\n"
        "v_mov_b32 v167, 21600\n"
        "L54B_%=:\n"
        "ds_read_b32 v166, v167 offset:64\n"
        "s_waitcnt lgkmcnt(0)\n"
        "v_readfirstlane_b32 s40, v166\n"
        "s_nop 3\n"
        "s_cmp_ge_i32 s40, 1\n"
        "s_cbranch_scc1 L55B_%=\n"
        "s_sleep 1\n"
        "s_sub_u32 s41, s41, 1\n"
        "s_cbranch_scc0 L54B_%=\n"
        "s_endpgm\n"
        "L55B_%=:\n"
        "v_mbcnt_lo_u32_b32 v166, -1, 0\n"
        "v_lshl_add_u32 v167, v166, 2, v167\n"
        "ds_read_b32 v166, v167 offset:68\n"
        "s_mov_b32 s14, s40\n"
        "s_mov_b32 s71, -1\n"
        "s_waitcnt lgkmcnt(0)\n"
        "v_readlane_b32 s22, v166, 0\n"
        "v_readlane_b32 s23, v166, 1\n"
        "s_nop 3\n"
        "s_nop 1\n"
        "s_branch L57B_%=\n"
        "L60B_%=:\n"
        "s_nop 7\n"
        "L58B_%=:\n"
        "ds_read_b32 v167, v0 offset:21600\n"
        "s_waitcnt lgkmcnt(0)\n"
        "v_readfirstlane_b32 s40, v167\n"
        "s_nop 3\n"
        "s_cmp_ge_i32 s40, s68\n"
        "s_cbranch_scc1 L59B_%=\n"
        "s_sleep 1\n"
        "s_sub_u32 s41, s41, 1\n"
        "s_cbranch_scc0 L58B_%=\n"
        "s_endpgm\n"
        "L59B_%=:\n"
        "s_nop 1\n"
        "s_branch L61B_%=\n"
        "CREDOA_%=:\n"
        "s_waitcnt lgkmcnt(0)\n"
        "s_nop 15\n"
        "s_mov_b32 s36, 0x20000000\n"
        "ds_read_b128 v[124:127], v7 offset:0\n"
        "ds_read_b128 v[132:135], v7 offset:3328\n"
        "ds_read_b128 v[128:131], v7 offset:64\n"
        "ds_read_b128 v[136:139], v7 offset:3392\n"
        "ds_read_b128 v[160:163], v7 offset:128\n"
        "ds_read_b128 v[52:55], v7 offset:3456\n"
        "s_waitcnt lgkmcnt(0)\n"
        "s_branch MTOPA_%=\n"
        "CREDOB_%=:\n"
        "s_waitcnt lgkmcnt(0)\n"
        "s_nop 15\n"
        "s_mov_b32 s36, 0x20000000\n"
        "ds_read_b128 v[124:127], v7 offset:40064\n"
        "ds_read_b128 v[132:135], v7 offset:43392\n"
        "ds_read_b128 v[128:131], v7 offset:40128\n"
        "ds_read_b128 v[136:139], v7 offset:43456\n"
        "ds_read_b128 v[160:163], v7 offset:40192\n"
        "ds_read_b128 v[52:55], v7 offset:43520\n"
        "s_waitcnt lgkmcnt(0)\n"
        "s_branch MTOPB_%=\n"
        "MTOPA_%=:\n"
        "s_waitcnt lgkmcnt(5)\n"
        "v_mfma_f32_16x16x32_bf16 v[24:27], v[168:171], v[124:127], 0\n"
        "v_cvt_pk_bf16_f32 v112, v152, v56\n"
        "v_cvt_pk_bf16_f32 v113, v72, v20\n"
        "v_cvt_pk_bf16_f32 v114, v84, v60\n"
        "v_cvt_pk_bf16_f32 v116, v153, v57\n"
        "v_cvt_pk_bf16_f32 v117, v73, v21\n"
        "v_cvt_pk_bf16_f32 v118, v85, v61\n"
        "v_cvt_pk_bf16_f32 v120, v154, v58\n"
        "v_cvt_pk_bf16_f32 v121, v74, v22\n"
        "s_waitcnt lgkmcnt(3)\n"
        "v_mfma_f32_16x16x32_bf16 v[24:27], v[172:175], v[128:131], v[24:27]\n"
        "v_cvt_pk_bf16_f32 v122, v86, v62\n"
        "v_cvt_pk_bf16_f32 v140, v155, v59\n"
        "v_cvt_pk_bf16_f32 v141, v75, v23\n"
        "v_cvt_pk_bf16_f32 v142, v87, v63\n"
        "v_cvt_pk_bf16_f32 v144, v68, v36\n"
        "v_cvt_pk_bf16_f32 v145, v40, v64\n"
        "v_cvt_pk_bf16_f32 v146, v104, v88\n"
        "v_cvt_pk_bf16_f32 v148, v69, v37\n"
        "s_waitcnt lgkmcnt(1)\n"
        "v_mfma_f32_16x16x32_bf16 v[24:27], v[176:179], v[160:163], v[24:27]\n"
        "v_cvt_pk_bf16_f32 v149, v41, v65\n"
        "v_cvt_pk_bf16_f32 v150, v105, v89\n"
        "v_cvt_pk_bf16_f32 v152, v70, v38\n"
        "v_cvt_pk_bf16_f32 v153, v42, v66\n"
        "v_cvt_pk_bf16_f32 v154, v106, v90\n"
        "v_cvt_pk_bf16_f32 v156, v71, v39\n"
        "v_cvt_pk_bf16_f32 v157, v43, v67\n"
        "v_cvt_pk_bf16_f32 v158, v107, v91\n"
        "v_mfma_f32_16x16x32_bf16 v[32:35], v[188:191], v[160:163], 0\n"
        "v_perm_b32 v40, v25, v24, s63\n"
        "v_perm_b32 v41, v27, v26, s63\n"
        "v_mfma_f32_16x16x32_bf16 v[32:35], v[184:187], v[128:131], v[32:35]\n"
        "v_mfma_f32_16x16x32_bf16 v[32:35], v[180:183], v[124:127], v[32:35]\n"
        "s_add_u32 s37, s36, 196224\n"
        "ds_read_b128 v[28:31], v16 offset:128\n"
        "ds_read_b32 v159, v10\n"
        "v_mfma_f32_16x16x32_bf16 v[24:27], v[192:195], v[124:127], 0\n"
        "ds_read_b128 v[20:23], v5\n"
        "v_mfma_f32_16x16x32_bf16 v[24:27], v[196:199], v[128:131], v[24:27]\n"
        "ds_read_b64 v[164:165], v11 offset:192\n"
        "v_mfma_f32_16x16x32_bf16 v[24:27], v[200:203], v[160:163], v[24:27]\n"
        "buffer_store_dwordx3 v[112:114], v12, s[8:11], s37 offen offset:384 sc0 sc1 nt\n"
        "v_perm_b32 v42, v33, v32, s63\n"
        "v_perm_b32 v43, v35, v34, s63\n"
        "s_waitcnt lgkmcnt(3)\n"
        "v_mfma_f32_16x16x32_bf16 v[48:51], v[28:31], v[160:163], 0\n"
        "ds_read_b128 v[60:63], v7 offset:6784\n"
        "ds_read_i16 v167, v0 offset:20584\n"
        "ds_read_i16 v155, v0 offset:20586\n"
        "v_perm_b32 v44, v25, v24, s63\n"
        "v_perm_b32 v45, v27, v26, s63\n"
        "s_waitcnt lgkmcnt(3)\n"
        "v_smfmac_f32_16x16x64_bf16 v[48:51], v[20:23], v[124:131], v4\n"
        "s_add_u32 s37, s36, 196224\n"
        "ds_read_b128 v[32:35], v7 offset:6656\n"
        "ds_read_b128 v[36:39], v7 offset:6720\n"
        "v_mfma_f32_16x16x32_bf16 v[56:59], v[28:31], v[52:55], 0\n"
        "v_smfmac_f32_16x16x64_bf16 v[56:59], v[20:23], v[132:139], v4\n"
        "s_setprio 2\n"
        "s_waitcnt lgkmcnt(4)\n"
        "v_mfma_f32_16x16x32_bf16 v[24:27], v[28:31], v[60:63], 0\n"
        "buffer_store_dwordx3 v[116:118], v13, s[8:11], s37 offen offset:1152 sc0 sc1 nt\n"
        "v_add_f32_dpp v165, v50, v51 quad_perm:[0,1,2,3] row_mask:0x1 bank_mask:0xf\n"
        "s_waitcnt lgkmcnt(2)\n"
        "v_add_u32 v167, v9, v167\n"
        "v_add_f32_dpp v164, v48, v49 quad_perm:[0,1,2,3] row_mask:0x1 bank_mask:0xf\n"
        "v_add_f32_dpp v165, v58, v59 quad_perm:[0,1,2,3] row_mask:0x2 bank_mask:0xf\n"
        "v_add_u32 v155, v9, v155\n"
        "v_add_f32_dpp v164, v56, v57 quad_perm:[0,1,2,3] row_mask:0x2 bank_mask:0xf\n"
        "s_waitcnt lgkmcnt(0)\n"
        "v_smfmac_f32_16x16x64_bf16 v[24:27], v[20:23], v[32:39], v4\n"
        "v_mfma_f32_16x16x32_bf16 v[28:31], v[212:215], v[160:163], 0\n"
        "s_add_u32 s37, s36, 294336\n"
        "v_mfma_f32_16x16x32_bf16 v[28:31], v[208:211], v[128:131], v[28:31]\n"
        "v_mfma_f32_16x16x32_bf16 v[28:31], v[204:207], v[124:127], v[28:31]\n"
        "v_mfma_f32_16x16x32_bf16 v[20:23], v[216:219], v[124:127], 0\n"
        "buffer_store_dwordx3 v[120:122], v12, s[8:11], s37 offen offset:576 sc0 sc1 nt\n"
        "v_mfma_f32_16x16x32_bf16 v[20:23], v[220:223], v[128:131], v[20:23]\n"
        "v_mfma_f32_16x16x32_bf16 v[20:23], v[224:227], v[160:163], v[20:23]\n"
        "s_add_u32 s37, s36, 392448\n"
        "v_add_f32_dpp v165, v26, v27 quad_perm:[0,1,2,3] row_mask:0x4 bank_mask:0xf\n"
        "v_mul_f32 v166, s52, v165\n"
        "v_fma_f32 v166, -v166, v165, v159\n"
        "v_perm_b32 v46, v29, v28, s63\n"
        "v_perm_b32 v47, v31, v30, s63\n"
        "v_add_f32_e64 v166, s53, |v166|\n"
        "v_rsq_f32 v159, v166\n"
        "v_mfma_f32_16x16x32_bf16 v[56:59], v[236:239], v[160:163], 0\n"
        "s_mov_b32 exec_hi, 0xffff\n"
        "ds_write_b32 v10, v159\n"
        "s_mov_b32 exec_hi, -1\n"
        "v_perm_b32 v48, v21, v20, s63\n"
        "v_perm_b32 v49, v23, v22, s63\n"
        "ds_read_b32 v20, v167 offset:0\n"
        "v_add_f32_dpp v164, v24, v25 quad_perm:[0,1,2,3] row_mask:0x4 bank_mask:0xf\n"
        "v_mfma_f32_16x16x32_bf16 v[56:59], v[232:235], v[128:131], v[56:59]\n"
        "s_mov_b32 exec_hi, 0xffff\n"
        "v_add_u32 v10, 40064, v10\n"
        "s_mov_b32 exec_hi, -1\n"
        "ds_read_b32 v21, v167 offset:832\n"
        "ds_read_b32 v22, v167 offset:1664\n"
        "ds_read_b32 v23, v167 offset:2496\n"
        "ds_read_b32 v24, v167 offset:3328\n"
        "v_mul_f32 v164, v164, v159\n"
        "v_mfma_f32_16x16x32_bf16 v[56:59], v[228:231], v[124:127], v[56:59]\n"
        "buffer_store_dwordx3 v[140:142], v12, s[8:11], s37 offen sc0 sc1 nt\n"
        "ds_read_b32 v25, v167 offset:4160\n"
        "ds_read_b32 v26, v167 offset:4992\n"
        "ds_read_b32 v27, v167 offset:5824\n"
        "ds_read_b32 v28, v167 offset:6656\n"
        "ds_read_b32 v29, v167 offset:7488\n"
        "ds_read_b32 v30, v167 offset:8320\n"
        "ds_read_b32 v31, v167 offset:9152\n"
        "v_perm_b32 v50, v57, v56, s63\n"
        "v_perm_b32 v51, v59, v58, s63\n"
        "v_readlane_b32 s58, v159, 56\n"
        "v_mfma_f32_16x16x32_bf16 v[56:59], v[124:127], v[40:43], 0\n"
        "s_mov_b64 exec, s[56:57]\n"
        "v_mov_b32 v164, 0xff800000\n"
        "s_mov_b64 exec, -1\n"
        "ds_read_b32 v165, v155 offset:0\n"
        "ds_read_b32 v166, v155 offset:3328\n"
        "v_max_f32_dpp v151, v164, v164 row_ror:8 row_mask:0xf bank_mask:0xf\n"
        "v_mfma_f32_16x16x32_bf16 v[56:59], v[128:131], v[44:47], v[56:59]\n"
        "s_add_u32 s37, s36, 392448\n"
        "v_max_f32_dpp v151, v151, v151 row_ror:4 row_mask:0xf bank_mask:0xf\n"
        "ds_read_b32 v167, v155 offset:6656\n"
        "v_mfma_f32_16x16x32_bf16 v[56:59], v[160:163], v[48:51], v[56:59]\n"
        "v_max_f32_dpp v151, v151, v151 row_ror:2 row_mask:0xf bank_mask:0xf\n"
        "v_mfma_f32_16x16x32_bf16 v[72:75], v[52:55], v[48:51], 0\n"
        "v_mfma_f32_16x16x32_bf16 v[72:75], v[136:139], v[44:47], v[72:75]\n"
        "buffer_store_dwordx3 v[144:146], v15, s[8:11], s37 offen offset:768 sc0 sc1 nt\n"
        "v_max_f32_dpp v151, v151, v151 row_ror:1 row_mask:0xf bank_mask:0xf\n"
        "v_mfma_f32_16x16x32_bf16 v[72:75], v[132:135], v[40:43], v[72:75]\n"
        "ds_read_b128 v[88:91], v8 offset:128\n"
        "v_mfma_f32_16x16x32_bf16 v[68:71], v[32:35], v[40:43], 0\n"
        "s_add_u32 s37, s36, 490560\n"
        "ds_read2_b32 v[92:93], v6 offset0:0 offset1:2\n"
        "ds_read2_b32 v[94:95], v6 offset0:8 offset1:10\n"
        "s_waitcnt lgkmcnt(3)\n"
        "v_mul_f32 v56, v56, v20\n"
        "v_mul_f32 v57, v57, v21\n"
        "v_mul_f32 v58, v58, v22\n"
        "v_mul_f32 v59, v59, v23\n"
        "v_mul_f32 v72, v72, v24\n"
        "v_mfma_f32_16x16x32_bf16 v[68:71], v[36:39], v[44:47], v[68:71]\n"
        "v_max3_f32 v155, v56, v57, v58\n"
        "v_mul_f32 v73, v73, v25\n"
        "v_mul_f32 v74, v74, v26\n"
        "v_max3_f32 v155, v155, v59, v72\n"
        "v_mul_f32 v75, v75, v27\n"
        "v_max3_f32 v155, v155, v73, v74\n"
        "v_mfma_f32_16x16x32_bf16 v[68:71], v[60:63], v[48:51], v[68:71]\n"
        "s_waitcnt lgkmcnt(2)\n"
        "v_mfma_f32_16x16x32_bf16 v[76:79], v[88:91], v[48:51], 0\n"
        "buffer_store_dwordx3 v[148:150], v12, s[8:11], s37 offen offset:192 sc0 sc1 nt\n"
        "s_waitcnt lgkmcnt(0)\n"
        "v_smfmac_f32_16x16x64_bf16 v[76:79], v[92:95], v[40:47], v4\n"
        "v_mfma_f32_16x16x32_bf16 v[48:51], v[168:171], v[132:135], 0\n"
        "s_add_u32 s37, s36, 490560\n"
        "v_mfma_f32_16x16x32_bf16 v[48:51], v[172:175], v[136:139], v[48:51]\n"
        "v_mul_f32 v68, v68, v28\n"
        "v_mul_f32 v69, v69, v29\n"
        "v_mul_f32 v70, v70, v30\n"
        "v_max3_f32 v155, v155, v75, v68\n"
        "v_mul_f32 v71, v71, v31\n"
        "v_add_f32 v76, v76, v77\n"
        "v_mul_f32 v76, s58, v76\n"
        "v_max3_f32 v155, v155, v69, v70\n"
        "v_mfma_f32_16x16x32_bf16 v[48:51], v[176:179], v[52:55], v[48:51]\n"
        "s_mov_b64 exec, s[48:49]\n"
        "v_mov_b32 v76, 0xff800000\n"
        "s_mov_b64 exec, -1\n"
        "v_max3_f32 v155, v155, v71, v76\n"
        "v_mfma_f32_16x16x32_bf16 v[44:47], v[188:191], v[52:55], 0\n"
        "buffer_store_dwordx3 v[152:154], v14, s[8:11], s37 offen offset:960 sc0 sc1 nt\n"
        "v_permlane32_swap_b32 v155, v151\n"
        "v_max_f32 v155, v155, v151\n"
        "v_perm_b32 v40, v49, v48, s63\n"
        "v_perm_b32 v41, v51, v50, s63\n"
        "v_mov_b32 v154, v155\n"
        "v_mfma_f32_16x16x32_bf16 v[44:47], v[184:187], v[136:139], v[44:47]\n"
        "v_mfma_f32_16x16x32_bf16 v[44:47], v[180:183], v[132:135], v[44:47]\n"
        "s_add_u32 s37, s36, 588672\n"
        "v_permlane16_swap_b32 v155, v154\n"
        "v_max_f32 v155, v155, v154\n"
        "v_mov_b32 v151, v155\n"
        "v_mfma_f32_16x16x32_bf16 v[64:67], v[192:195], v[132:135], 0\n"
        "v_mfma_f32_16x16x32_bf16 v[64:67], v[196:199], v[136:139], v[64:67]\n"
        "v_permlane32_swap_b32 v155, v151\n"
        "v_mul_f32 v155, v155, v165\n"
        "v_perm_b32 v42, v45, v44, s63\n"
        "v_perm_b32 v43, v47, v46, s63\n"
        "v_fma_f32 v56, v56, v165, -v155\n"
        "v_fma_f32 v57, v57, v165, -v155\n"
        "v_fma_f32 v58, v58, v165, -v155\n"
        "v_mfma_f32_16x16x32_bf16 v[64:67], v[200:203], v[52:55], v[64:67]\n"
        "buffer_store_dwordx3 v[156:158], v12, s[8:11], s37 offen offset:384 sc0 sc1 nt\n"
        "v_exp_f32 v56, v56\n"
        "v_exp_f32 v57, v57\n"
        "v_exp_f32 v58, v58\n"
        "v_fma_f32 v59, v59, v165, -v155\n"
        "v_add_f32 v158, v56, v57\n"
        "v_exp_f32 v59, v59\n"
        "v_mfma_f32_16x16x32_bf16 v[48:51], v[212:215], v[52:55], 0\n"
        "v_perm_b32 v44, v65, v64, s63\n"
        "v_perm_b32 v45, v67, v66, s63\n"
        "v_fma_f32 v72, v72, v165, -v155\n"
        "v_add_f32 v158, v158, v58\n"
        "v_exp_f32 v72, v72\n"
        "v_fma_f32 v73, v73, v165, -v155\n"
        "v_add_f32 v158, v158, v59\n"
        "v_mfma_f32_16x16x32_bf16 v[48:51], v[208:211], v[136:139], v[48:51]\n"
        "v_exp_f32 v73, v73\n"
        "v_fma_f32 v74, v74, v165, -v155\n"
        "v_add_f32 v158, v158, v72\n"
        "v_exp_f32 v74, v74\n"
        "v_fma_f32 v75, v75, v165, -v155\n"
        "v_sub_f32 v164, v164, v151\n"
        "v_mfma_f32_16x16x32_bf16 v[48:51], v[204:207], v[132:135], v[48:51]\n"
        "v_add_f32 v158, v158, v73\n"
        "v_exp_f32 v75, v75\n"
        "v_fma_f32 v68, v68, v165, -v155\n"
        "v_mul_f32 v164, s58, v164\n"
        "v_add_f32 v158, v158, v74\n"
        "v_exp_f32 v68, v68\n"
        "v_mfma_f32_16x16x32_bf16 v[64:67], v[216:219], v[132:135], 0\n"
        "v_fma_f32 v69, v69, v165, -v155\n"
        "v_perm_b32 v46, v49, v48, s63\n"
        "v_perm_b32 v47, v51, v50, s63\n"
        "v_exp_f32 v164, v164\n"
        "v_add_f32 v158, v158, v75\n"
        "v_exp_f32 v69, v69\n"
        "v_mfma_f32_16x16x32_bf16 v[64:67], v[220:223], v[136:139], v[64:67]\n"
        "v_fma_f32 v70, v70, v165, -v155\n"
        "v_add_f32 v158, v158, v68\n"
        "v_exp_f32 v70, v70\n"
        "v_fma_f32 v71, v71, v165, -v155\n"
        "v_add_f32_dpp v157, v164, v164 row_ror:8 row_mask:0xf bank_mask:0xf\n"
        "v_add_f32 v158, v158, v69\n"
        "v_fma_f32 v76, v76, v165, -v155\n"
        "v_mfma_f32_16x16x32_bf16 v[64:67], v[224:227], v[52:55], v[64:67]\n"
        "v_exp_f32 v71, v71\n"
        "v_add_f32_dpp v157, v157, v157 row_ror:4 row_mask:0xf bank_mask:0xf\n"
        "v_add_f32 v158, v158, v70\n"
        "v_exp_f32 v76, v76\n"
        "v_add_f32_dpp v157, v157, v157 row_ror:2 row_mask:0xf bank_mask:0xf\n"
        "v_add_f32 v158, v158, v71\n"
        "v_mfma_f32_16x16x32_bf16 v[48:51], v[236:239], v[52:55], 0\n"
        "v_add_f32_dpp v157, v157, v157 row_ror:1 row_mask:0xf bank_mask:0xf\n"
        "v_perm_b32 v80, v65, v64, s63\n"
        "v_perm_b32 v81, v67, v66, s63\n"
        "v_add_f32 v158, v158, v76\n"
        "v_mul_f32 v56, v56, v20\n"
        "v_mul_f32 v57, v57, v21\n"
        "v_permlane32_swap_b32 v158, v157\n"
        "v_mfma_f32_16x16x32_bf16 v[48:51], v[232:235], v[136:139], v[48:51]\n"
        "v_add_f32 v158, v158, v157\n"
        "v_mov_b32 v165, v158\n"
        "v_mul_f32 v58, v58, v22\n"
        "v_mul_f32 v59, v59, v23\n"
        "v_permlane16_swap_b32 v158, v165\n"
        "v_add_f32 v158, v158, v165\n"
        "v_mul_f32 v72, v72, v24\n"
        "v_mfma_f32_16x16x32_bf16 v[48:51], v[228:231], v[132:135], v[48:51]\n"
        "v_rcp_f32 v158, v158\n"
        "v_mul_f32 v73, v73, v25\n"
        "v_mov_b32 v157, v158\n"
        "v_mul_f32 v74, v74, v26\n"
        "v_mul_f32 v75, v75, v27\n"
        "v_permlane32_swap_b32 v158, v157\n"
        "v_mfma_f32_16x16x32_bf16 v[64:67], v[124:127], v[40:43], 0\n"
        "v_mul_f32 v56, v56, v158\n"
        "v_perm_b32 v82, v49, v48, s63\n"
        "v_perm_b32 v83, v51, v50, s63\n"
        "v_mul_f32 v57, v57, v158\n"
        "v_mul_f32 v58, v58, v158\n"
        "v_mul_f32 v59, v59, v158\n"
        "v_mul_f32 v72, v72, v158\n"
        "v_mul_f32 v73, v73, v158\n"
        "v_mfma_f32_16x16x32_bf16 v[64:67], v[128:131], v[44:47], v[64:67]\n"
        "v_mul_f32 v74, v74, v158\n"
        "v_mul_f32 v75, v75, v158\n"
        "v_perm_b32 v48, v57, v56, s63\n"
        "v_perm_b32 v49, v59, v58, s63\n"
        "v_perm_b32 v50, v73, v72, s63\n"
        "v_perm_b32 v51, v75, v74, s63\n"
        "v_mul_f32 v68, v68, v28\n"
        "v_mul_f32 v69, v69, v29\n"
        "v_mfma_f32_16x16x32_bf16 v[64:67], v[160:163], v[80:83], v[64:67]\n"
        "v_mul_f32 v70, v70, v30\n"
        "v_mul_f32 v71, v71, v31\n"
        "v_mul_f32 v76, s58, v76\n"
        "v_mul_f32 v68, v68, v158\n"
        "v_mul_f32 v69, v69, v158\n"
        "v_mul_f32 v70, v70, v158\n"
        "v_mul_f32 v71, v71, v158\n"
        "v_fma_f32 v76, v76, v158, v19\n"
        "v_mfma_f32_16x16x32_bf16 v[84:87], v[52:55], v[80:83], 0\n"
        "v_mul_f32 v64, v64, v20\n"
        "v_mul_f32 v65, v65, v21\n"
        "v_mul_f32 v66, v66, v22\n"
        "v_mul_f32 v67, v67, v23\n"
        "v_max3_f32 v165, v64, v65, v66\n"
        "v_perm_b32 v56, v69, v68, s63\n"
        "v_perm_b32 v58, v71, v70, s63\n"
        "v_perm_b32 v57, 0, v76, s63\n"
        "v_mfma_f32_16x16x32_bf16 v[84:87], v[136:139], v[44:47], v[84:87]\n"
        "v_mul_f32 v164, v164, v159\n"
        "v_mul_f32 v164, v164, v157\n"
        "v_perm_b32 v164, 0, v164, s63\n"
        "ds_write_b16 v1, v164\n"
        "v_mfma_f32_16x16x32_bf16 v[84:87], v[132:135], v[40:43], v[84:87]\n"
        "v_mfma_f32_16x16x32_bf16 v[68:71], v[32:35], v[40:43], 0\n"
        "v_mfma_f32_16x16x32_bf16 v[68:71], v[36:39], v[44:47], v[68:71]\n"
        "v_mfma_f32_16x16x32_bf16 v[68:71], v[60:63], v[80:83], v[68:71]\n"
        "v_mfma_f32_16x16x32_bf16 v[72:75], v[88:91], v[80:83], 0\n"
        "v_smfmac_f32_16x16x64_bf16 v[72:75], v[92:95], v[40:47], v4\n"
        "v_mfma_f32_16x16x32_bf16 v[80:83], v[168:171], v[32:35], 0\n"
        "v_mfma_f32_16x16x32_bf16 v[80:83], v[172:175], v[36:39], v[80:83]\n"
        "v_mfma_f32_16x16x32_bf16 v[80:83], v[176:179], v[60:63], v[80:83]\n"
        "v_mul_f32 v84, v84, v24\n"
        "v_mul_f32 v85, v85, v25\n"
        "v_mul_f32 v86, v86, v26\n"
        "v_max3_f32 v165, v165, v67, v84\n"
        "v_mul_f32 v87, v87, v27\n"
        "v_add_f32 v72, v72, v73\n"
        "v_mul_f32 v68, v68, v28\n"
        "v_mul_f32 v72, s58, v72\n"
        "v_mfma_f32_16x16x32_bf16 v[40:43], v[188:191], v[60:63], 0\n"
        "s_mov_b64 exec, s[48:49]\n"
        "v_mov_b32 v72, 0xff800000\n"
        "s_mov_b64 exec, -1\n"
        "v_perm_b32 v76, v81, v80, s63\n"
        "v_perm_b32 v77, v83, v82, s63\n"
        "v_max3_f32 v165, v165, v85, v86\n"
        "v_mul_f32 v69, v69, v29\n"
        "v_mul_f32 v70, v70, v30\n"
        "v_max3_f32 v165, v165, v87, v68\n"
        "v_mfma_f32_16x16x32_bf16 v[40:43], v[184:187], v[36:39], v[40:43]\n"
        "v_mul_f32 v71, v71, v31\n"
        "v_max3_f32 v165, v165, v69, v70\n"
        "v_max3_f32 v165, v165, v71, v72\n"
        "v_mfma_f32_16x16x32_bf16 v[40:43], v[180:183], v[32:35], v[40:43]\n"
        "v_mfma_f32_16x16x32_bf16 v[100:103], v[192:195], v[32:35], 0\n"
        "v_mfma_f32_16x16x32_bf16 v[100:103], v[196:199], v[36:39], v[100:103]\n"
        "v_mfma_f32_16x16x32_bf16 v[100:103], v[200:203], v[60:63], v[100:103]\n"
        "v_mfma_f32_16x16x32_bf16 v[96:99], v[212:215], v[60:63], 0\n"
        "v_mfma_f32_16x16x32_bf16 v[96:99], v[208:211], v[36:39], v[96:99]\n"
        "v_mfma_f32_16x16x32_bf16 v[96:99], v[204:207], v[32:35], v[96:99]\n"
        "v_mfma_f32_16x16x32_bf16 v[44:47], v[216:219], v[32:35], 0\n"
        "v_mfma_f32_16x16x32_bf16 v[44:47], v[220:223], v[36:39], v[44:47]\n"
        "v_perm_b32 v78, v41, v40, s63\n"
        "v_perm_b32 v79, v43, v42, s63\n"
        "v_mfma_f32_16x16x32_bf16 v[44:47], v[224:227], v[60:63], v[44:47]\n"
        "v_perm_b32 v80, v101, v100, s63\n"
        "v_perm_b32 v81, v103, v102, s63\n"
        "v_mfma_f32_16x16x32_bf16 v[40:43], v[236:239], v[60:63], 0\n"
        "v_perm_b32 v82, v97, v96, s63\n"
        "v_perm_b32 v83, v99, v98, s63\n"
        "v_mfma_f32_16x16x32_bf16 v[40:43], v[232:235], v[36:39], v[40:43]\n"
        "v_mfma_f32_16x16x32_bf16 v[40:43], v[228:231], v[32:35], v[40:43]\n"
        "v_mfma_f32_16x16x32_bf16 v[108:111], v[124:127], v[76:79], 0\n"
        "v_perm_b32 v96, v45, v44, s63\n"
        "v_perm_b32 v97, v47, v46, s63\n"
        "v_mfma_f32_16x16x32_bf16 v[108:111], v[128:131], v[80:83], v[108:111]\n"
        "s_nop 3\n"
        "v_perm_b32 v98, v41, v40, s63\n"
        "v_perm_b32 v99, v43, v42, s63\n"
        "s_nop 1\n"
        "v_mfma_f32_16x16x32_bf16 v[108:111], v[160:163], v[96:99], v[108:111]\n"
        "v_mfma_f32_16x16x32_bf16 v[140:143], v[52:55], v[96:99], 0\n"
        "v_mfma_f32_16x16x32_bf16 v[140:143], v[136:139], v[80:83], v[140:143]\n"
        "v_mfma_f32_16x16x32_bf16 v[140:143], v[132:135], v[76:79], v[140:143]\n"
        "v_mfma_f32_16x16x32_bf16 v[100:103], v[32:35], v[76:79], 0\n"
        "v_mfma_f32_16x16x32_bf16 v[100:103], v[36:39], v[80:83], v[100:103]\n"
        "v_mfma_f32_16x16x32_bf16 v[100:103], v[60:63], v[96:99], v[100:103]\n"
        "v_mfma_f32_16x16x32_bf16 v[44:47], v[88:91], v[96:99], 0\n"
        "v_smfmac_f32_16x16x64_bf16 v[44:47], v[92:95], v[76:83], v4\n"
        "v_mul_f32 v108, v108, v20\n"
        "v_mul_f32 v109, v109, v21\n"
        "v_mul_f32 v110, v110, v22\n"
        "v_mul_f32 v111, v111, v23\n"
        "v_mul_f32 v140, v140, v24\n"
        "v_max3_f32 v159, v108, v109, v110\n"
        "v_mul_f32 v141, v141, v25\n"
        "v_mfma_f32_16x16x32_bf16 v[60:63], v[124:127], v[240:243], 0\n"
        "v_add_f32 v44, v44, v45\n"
        "v_mul_f32 v142, v142, v26\n"
        "v_max3_f32 v159, v159, v111, v140\n"
        "v_mul_f32 v143, v143, v27\n"
        "v_mul_f32 v100, v100, v28\n"
        "v_mul_f32 v44, s58, v44\n"
        "v_max3_f32 v159, v159, v141, v142\n"
        "v_mul_f32 v101, v101, v29\n"
        "v_mfma_f32_16x16x32_bf16 v[60:63], v[128:131], v[244:247], v[60:63]\n"
        "s_mov_b64 exec, s[48:49]\n"
        "v_mov_b32 v44, 0xff800000\n"
        "s_mov_b64 exec, -1\n"
        "v_mul_f32 v102, v102, v30\n"
        "v_max3_f32 v159, v159, v143, v100\n"
        "v_mul_f32 v103, v103, v31\n"
        "v_max3_f32 v159, v159, v101, v102\n"
        "v_max3_f32 v159, v159, v103, v44\n"
        "v_mfma_f32_16x16x32_bf16 v[60:63], v[160:163], v[248:251], v[60:63]\n"
        "ds_read_b128 v[32:35], v17 offset:0\n"
        "v_permlane32_swap_b32 v165, v159\n"
        "v_max_f32 v165, v165, v159\n"
        "v_mov_b32 v164, v165\n"
        "v_mfma_f32_16x16x32_bf16 v[76:79], v[52:55], v[248:251], 0\n"
        "ds_read_b128 v[36:39], v17 offset:64\n"
        "v_permlane16_swap_b32 v165, v164\n"
        "v_max_f32 v165, v165, v164\n"
        "v_perm_b32 v40, v61, v60, s63\n"
        "v_perm_b32 v41, v63, v62, s63\n"
        "v_mov_b32 v159, v165\n"
        "v_mfma_f32_16x16x32_bf16 v[76:79], v[136:139], v[244:247], v[76:79]\n"
        "ds_read_b128 v[60:63], v17 offset:128\n"
        "v_permlane32_swap_b32 v165, v159\n"
        "v_mul_f32 v165, v165, v166\n"
        "v_mul_f32 v159, v159, v167\n"
        "v_fma_f32 v64, v64, v166, -v165\n"
        "v_fma_f32 v65, v65, v166, -v165\n"
        "v_fma_f32 v108, v108, v167, -v159\n"
        "v_mfma_f32_16x16x32_bf16 v[76:79], v[132:135], v[240:243], v[76:79]\n"
        "s_waitcnt lgkmcnt(2)\n"
        "ds_read_b128 v[96:99], v0 offset:5136\n"
        "v_fma_f32 v109, v109, v167, -v159\n"
        "v_exp_f32 v64, v64\n"
        "v_exp_f32 v65, v65\n"
        "v_fma_f32 v66, v66, v166, -v165\n"
        "v_fma_f32 v110, v110, v167, -v159\n"
        "v_mfma_f32_16x16x32_bf16 v[80:83], v[32:35], v[240:243], 0\n"
        "s_waitcnt lgkmcnt(2)\n"
        "ds_read_b128 v[104:107], v0 offset:4112\n"
        "v_perm_b32 v42, v77, v76, s63\n"
        "v_perm_b32 v43, v79, v78, s63\n"
        "v_exp_f32 v108, v108\n"
        "v_exp_f32 v109, v109\n"
        "v_fma_f32 v67, v67, v166, -v165\n"
        "v_mfma_f32_16x16x32_bf16 v[80:83], v[36:39], v[244:247], v[80:83]\n"
        "s_waitcnt lgkmcnt(2)\n"
        "v_exp_f32 v66, v66\n"
        "v_exp_f32 v110, v110\n"
        "v_fma_f32 v111, v111, v167, -v159\n"
        "v_add_f32 v95, v64, v65\n"
        "v_exp_f32 v67, v67\n"
        "v_mfma_f32_16x16x32_bf16 v[80:83], v[60:63], v[248:251], v[80:83]\n"
        "s_waitcnt lgkmcnt(1)\n"
        "v_fma_f32 v84, v84, v166, -v165\n"
        "v_add_f32 v164, v108, v109\n"
        "v_exp_f32 v111, v111\n"
        "v_fma_f32 v140, v140, v167, -v159\n"
        "v_add_f32 v95, v95, v66\n"
        "v_exp_f32 v84, v84\n"
        "v_mfma_f32_16x16x32_bf16 v[112:115], v[60:63], v[96:99], 0\n"
        "s_waitcnt lgkmcnt(0)\n"
        "v_perm_b32 v76, v81, v80, s63\n"
        "v_perm_b32 v80, v83, v82, s63\n"
        "v_fma_f32 v85, v85, v166, -v165\n"
        "v_add_f32 v164, v164, v110\n"
        "v_exp_f32 v140, v140\n"
        "v_fma_f32 v141, v141, v167, -v159\n"
        "v_add_f32 v95, v95, v67\n"
        "v_mfma_f32_16x16x32_bf16 v[112:115], v[36:39], v[104:107], v[112:115]\n"
        "v_exp_f32 v85, v85\n"
        "v_fma_f32 v86, v86, v166, -v165\n"
        "v_add_f32 v164, v164, v111\n"
        "v_exp_f32 v141, v141\n"
        "v_fma_f32 v142, v142, v167, -v159\n"
        "v_add_f32 v95, v95, v84\n"
        "v_mfma_f32_16x16x32_bf16 v[112:115], v[32:35], v[252:255], v[112:115]\n"
        "v_exp_f32 v86, v86\n"
        "v_fma_f32 v87, v87, v166, -v165\n"
        "v_add_f32 v164, v164, v140\n"
        "v_exp_f32 v142, v142\n"
        "v_fma_f32 v143, v143, v167, -v159\n"
        "v_add_f32 v95, v95, v85\n"
        "v_mfma_f32_16x16x32_bf16 v[120:123], v[132:135], v[252:255], 0\n"
        "v_exp_f32 v87, v87\n"
        "v_perm_b32 v88, v113, v112, s63\n"
        "v_perm_b32 v92, v115, v114, s63\n"
        "v_fma_f32 v68, v68, v166, -v165\n"
        "v_add_f32 v164, v164, v141\n"
        "v_exp_f32 v143, v143\n"
        "v_mfma_f32_16x16x32_bf16 v[120:123], v[136:139], v[104:107], v[120:123]\n"
        "v_fma_f32 v100, v100, v167, -v159\n"
        "v_add_f32 v95, v95, v86\n"
        "v_exp_f32 v68, v68\n"
        "v_fma_f32 v69, v69, v166, -v165\n"
        "v_add_f32 v164, v164, v142\n"
        "v_exp_f32 v100, v100\n"
        "v_mfma_f32_16x16x32_bf16 v[120:123], v[52:55], v[96:99], v[120:123]\n"
        "ds_read_b128 v[144:147], v0 offset:6176\n"
        "v_fma_f32 v101, v101, v167, -v159\n"
        "v_add_f32 v95, v95, v87\n"
        "v_exp_f32 v69, v69\n"
        "v_fma_f32 v70, v70, v166, -v165\n"
        "v_add_f32 v164, v164, v143\n"
        "v_fma_f32 v102, v102, v167, -v159\n"
        "v_mfma_f32_16x16x32_bf16 v[148:151], v[160:163], v[96:99], 0\n"
        "ds_read_b128 v[116:119], v0 offset:7200\n"
        "v_perm_b32 v98, v121, v120, s63\n"
        "v_perm_b32 v99, v123, v122, s63\n"
        "v_exp_f32 v101, v101\n"
        "v_add_f32 v95, v95, v68\n"
        "v_exp_f32 v70, v70\n"
        "v_mfma_f32_16x16x32_bf16 v[148:151], v[128:131], v[104:107], v[148:151]\n"
        "ds_read_b128 v[112:115], v0 offset:8224\n"
        "v_fma_f32 v71, v71, v166, -v165\n"
        "v_add_f32 v164, v164, v100\n"
        "v_exp_f32 v102, v102\n"
        "v_fma_f32 v103, v103, v167, -v159\n"
        "v_add_f32 v95, v95, v69\n"
        "v_fma_f32 v72, v72, v166, -v165\n"
        "v_mfma_f32_16x16x32_bf16 v[148:151], v[124:127], v[252:255], v[148:151]\n"
        "s_waitcnt lgkmcnt(2)\n"
        "v_exp_f32 v71, v71\n"
        "v_add_f32 v164, v164, v101\n"
        "v_exp_f32 v103, v103\n"
        "v_fma_f32 v44, v44, v167, -v159\n"
        "v_add_f32 v95, v95, v70\n"
        "v_add_f32 v164, v164, v102\n"
        "v_mfma_f32_16x16x32_bf16 v[120:123], v[124:127], v[144:147], 0\n"
        "s_waitcnt lgkmcnt(1)\n"
        "v_perm_b32 v96, v149, v148, s63\n"
        "v_perm_b32 v97, v151, v150, s63\n"
        "v_exp_f32 v72, v72\n"
        "v_exp_f32 v44, v44\n"
        "v_add_f32 v95, v95, v71\n"
        "v_add_f32 v164, v164, v103\n"
        "v_mfma_f32_16x16x32_bf16 v[120:123], v[128:131], v[116:119], v[120:123]\n"
        "s_waitcnt lgkmcnt(0)\n"
        "v_add_f32 v95, v95, v72\n"
        "v_add_f32 v164, v164, v44\n"
        "v_mul_f32 v64, v64, v20\n"
        "v_mul_f32 v65, v65, v21\n"
        "v_permlane32_swap_b32 v95, v164\n"
        "v_add_f32 v95, v95, v164\n"
        "v_mov_b32 v167, v95\n"
        "v_mfma_f32_16x16x32_bf16 v[120:123], v[160:163], v[112:115], v[120:123]\n"
        "v_mul_f32 v66, v66, v22\n"
        "v_permlane16_swap_b32 v95, v167\n"
        "v_add_f32 v95, v95, v167\n"
        "v_rcp_f32 v95, v95\n"
        "v_mul_f32 v67, v67, v23\n"
        "v_mov_b32 v164, v95\n"
        "v_mfma_f32_16x16x32_bf16 v[156:159], v[52:55], v[112:115], 0\n"
        "v_mul_f32 v84, v84, v24\n"
        "v_perm_b32 v104, v121, v120, s63\n"
        "v_perm_b32 v105, v123, v122, s63\n"
        "v_permlane32_swap_b32 v95, v164\n"
        "v_mul_f32 v85, v85, v25\n"
        "v_mul_f32 v86, v86, v26\n"
        "v_mul_f32 v87, v87, v27\n"
        "v_mfma_f32_16x16x32_bf16 v[156:159], v[136:139], v[116:119], v[156:159]\n"
        "v_mul_f32 v64, v64, v95\n"
        "v_mul_f32 v65, v65, v95\n"
        "v_mul_f32 v66, v66, v95\n"
        "v_mul_f32 v67, v67, v95\n"
        "v_mul_f32 v84, v84, v95\n"
        "v_mul_f32 v85, v85, v95\n"
        "v_mul_f32 v86, v86, v95\n"
        "v_mul_f32 v87, v87, v95\n"
        "v_mfma_f32_16x16x32_bf16 v[156:159], v[132:135], v[144:147], v[156:159]\n"
        "ds_read_b128 v[148:151], v0 offset:11312\n"
        "v_perm_b32 v120, v65, v64, s63\n"
        "v_perm_b32 v121, v67, v66, s63\n"
        "v_perm_b32 v122, v85, v84, s63\n"
        "v_perm_b32 v123, v87, v86, s63\n"
        "v_mul_f32 v68, v68, v28\n"
        "v_mul_f32 v69, v69, v29\n"
        "v_mul_f32 v70, v70, v30\n"
        "v_mfma_f32_16x16x32_bf16 v[152:155], v[32:35], v[144:147], 0\n"
        "ds_read_b128 v[84:87], v0 offset:10288\n"
        "v_perm_b32 v106, v157, v156, s63\n"
        "v_perm_b32 v107, v159, v158, s63\n"
        "v_mul_f32 v71, v71, v31\n"
        "v_mul_f32 v72, s58, v72\n"
        "v_mul_f32 v68, v68, v95\n"
        "v_mul_f32 v69, v69, v95\n"
        "v_mul_f32 v70, v70, v95\n"
        "v_mfma_f32_16x16x32_bf16 v[152:155], v[36:39], v[116:119], v[152:155]\n"
        "ds_read_b128 v[144:147], v0 offset:9264\n"
        "v_mul_f32 v71, v71, v95\n"
        "v_fma_f32 v72, v72, v95, v19\n"
        "v_perm_b32 v64, v69, v68, s63\n"
        "v_perm_b32 v66, v71, v70, s63\n"
        "v_perm_b32 v65, 0, v72, s63\n"
        "v_mul_f32 v108, v108, v20\n"
        "v_mul_f32 v109, v109, v21\n"
        "v_mfma_f32_16x16x32_bf16 v[152:155], v[60:63], v[112:115], v[152:155]\n"
        "s_waitcnt lgkmcnt(2)\n"
        "v_mul_f32 v110, v110, v22\n"
        "v_mul_f32 v111, v111, v23\n"
        "v_mul_f32 v140, v140, v24\n"
        "v_mul_f32 v141, v141, v25\n"
        "v_mul_f32 v142, v142, v26\n"
        "v_mul_f32 v143, v143, v27\n"
        "v_mul_f32 v108, v108, v164\n"
        "v_perm_b32 v112, v153, v152, s63\n"
        "v_mfma_f32_16x16x32_bf16 v[20:23], v[60:63], v[148:151], 0\n"
        "s_waitcnt lgkmcnt(1)\n"
        "v_perm_b32 v116, v155, v154, s63\n"
        "v_mul_f32 v109, v109, v164\n"
        "v_mul_f32 v110, v110, v164\n"
        "v_mul_f32 v111, v111, v164\n"
        "v_mul_f32 v140, v140, v164\n"
        "v_mul_f32 v141, v141, v164\n"
        "v_mul_f32 v142, v142, v164\n"
        "v_mul_f32 v143, v143, v164\n"
        "v_mfma_f32_16x16x32_bf16 v[20:23], v[36:39], v[84:87], v[20:23]\n"
        "s_waitcnt lgkmcnt(0)\n"
        "v_perm_b32 v24, v109, v108, s63\n"
        "v_perm_b32 v25, v111, v110, s63\n"
        "v_perm_b32 v26, v141, v140, s63\n"
        "v_perm_b32 v27, v143, v142, s63\n"
        "v_mul_f32 v100, v100, v28\n"
        "v_mul_f32 v101, v101, v29\n"
        "v_mul_f32 v102, v102, v30\n"
        "v_mul_f32 v103, v103, v31\n"
        "v_mfma_f32_16x16x32_bf16 v[20:23], v[32:35], v[144:147], v[20:23]\n"
        "v_mul_f32 v44, s58, v44\n"
        "v_mul_f32 v100, v100, v164\n"
        "v_mul_f32 v101, v101, v164\n"
        "v_mul_f32 v102, v102, v164\n"
        "v_mul_f32 v103, v103, v164\n"
        "v_fma_f32 v44, v44, v164, v19\n"
        "v_perm_b32 v28, v101, v100, s63\n"
        "v_perm_b32 v30, v103, v102, s63\n"
        "v_mfma_f32_16x16x32_bf16 v[68:71], v[132:135], v[144:147], 0\n"
        "v_perm_b32 v77, v21, v20, s63\n"
        "v_perm_b32 v81, v23, v22, s63\n"
        "v_perm_b32 v29, 0, v44, s63\n"
        "v_mfma_f32_16x16x32_bf16 v[68:71], v[136:139], v[84:87], v[68:71]\n"
        "v_mfma_f32_16x16x32_bf16 v[68:71], v[52:55], v[148:151], v[68:71]\n"
        "ds_read_b128 v[20:23], v0 offset:12352\n"
        "v_mfma_f32_16x16x32_bf16 v[72:75], v[160:163], v[148:151], 0\n"
        "ds_read_b128 v[108:111], v0 offset:13376\n"
        "v_mfma_f32_16x16x32_bf16 v[72:75], v[128:131], v[84:87], v[72:75]\n"
        "ds_read_b128 v[140:143], v0 offset:14400\n"
        "v_mfma_f32_16x16x32_bf16 v[72:75], v[124:127], v[144:147], v[72:75]\n"
        "s_waitcnt lgkmcnt(2)\n"
        "v_mfma_f32_16x16x32_bf16 v[144:147], v[124:127], v[20:23], 0\n"
        "v_perm_b32 v46, v69, v68, s63\n"
        "v_perm_b32 v47, v71, v70, s63\n"
        "s_waitcnt lgkmcnt(1)\n"
        "v_mfma_f32_16x16x32_bf16 v[144:147], v[128:131], v[108:111], v[144:147]\n"
        "s_waitcnt lgkmcnt(0)\n"
        "v_mfma_f32_16x16x32_bf16 v[144:147], v[160:163], v[140:143], v[144:147]\n"
        "v_perm_b32 v44, v73, v72, s63\n"
        "v_perm_b32 v45, v75, v74, s63\n"
        "v_mfma_f32_16x16x32_bf16 v[148:151], v[52:55], v[140:143], 0\n"
        "v_mfma_f32_16x16x32_bf16 v[148:151], v[136:139], v[108:111], v[148:151]\n"
        "v_mfma_f32_16x16x32_bf16 v[148:151], v[132:135], v[20:23], v[148:151]\n"
        "ds_read_b128 v[72:75], v0 offset:17488\n"
        "v_mfma_f32_16x16x32_bf16 v[84:87], v[32:35], v[20:23], 0\n"
        "ds_read_b128 v[68:71], v0 offset:16464\n"
        "v_perm_b32 v100, v145, v144, s63\n"
        "v_perm_b32 v101, v147, v146, s63\n"
        "v_mfma_f32_16x16x32_bf16 v[84:87], v[36:39], v[108:111], v[84:87]\n"
        "ds_read_b128 v[20:23], v0 offset:15440\n"
        "v_mfma_f32_16x16x32_bf16 v[84:87], v[60:63], v[140:143], v[84:87]\n"
        "v_perm_b32 v102, v149, v148, s63\n"
        "v_perm_b32 v103, v151, v150, s63\n"
        "s_waitcnt lgkmcnt(2)\n"
        "v_mfma_f32_16x16x32_bf16 v[108:111], v[60:63], v[72:75], 0\n"
        "s_waitcnt lgkmcnt(1)\n"
        "v_mfma_f32_16x16x32_bf16 v[108:111], v[36:39], v[68:71], v[108:111]\n"
        "s_waitcnt lgkmcnt(0)\n"
        "v_mfma_f32_16x16x32_bf16 v[108:111], v[32:35], v[20:23], v[108:111]\n"
        "ds_read2_b32 v[82:83], v11 offset0:0 offset1:3\n"
        "v_perm_b32 v89, v85, v84, s63\n"
        "v_perm_b32 v93, v87, v86, s63\n"
        "v_mov_b32_dpp v59, v19 row_shr:4 row_mask:0xf bank_mask:0xf bound_ctrl:0\n"
        "v_mfma_f32_16x16x32_bf16 v[36:39], v[132:135], v[20:23], 0\n"
        "ds_read_b64_tr_b16 v[78:79], v9 offset:0\n"
        "v_mfma_f32_16x16x32_bf16 v[36:39], v[136:139], v[68:71], v[36:39]\n"
        "ds_read2_b32 v[94:95], v11 offset0:1 offset1:4\n"
        "v_perm_b32 v113, v109, v108, s63\n"
        "v_perm_b32 v117, v111, v110, s63\n"
        "v_mfma_f32_16x16x32_bf16 v[36:39], v[52:55], v[72:75], v[36:39]\n"
        "s_add_u32 s31, s31, 1\n"
        "ds_read_b64_tr_b16 v[90:91], v9 offset:16\n"
        "v_mfma_f32_16x16x32_bf16 v[32:35], v[160:163], v[72:75], 0\n"
        "s_cmp_ge_u32 s31, s71\n"
        "ds_read2_b32 v[118:119], v11 offset0:2 offset1:5\n"
        "v_mfma_f32_16x16x32_bf16 v[32:35], v[128:131], v[68:71], v[32:35]\n"
        "s_cbranch_scc1 L56A_%=\n"
        "L57A_%=:\n"
        "ds_read_b64_tr_b16 v[114:115], v9 offset:32\n"
        "v_perm_b32 v110, v37, v36, s63\n"
        "v_perm_b32 v111, v39, v38, s63\n"
        "v_mfma_f32_16x16x32_bf16 v[32:35], v[124:127], v[20:23], v[32:35]\n"
        "s_add_u32 s16, s16, 256\n"
        "v_mfma_f32_16x16x32_bf16 v[20:23], v[48:51], v[40:43], 0\n"
        "s_waitcnt lgkmcnt(4)\n"
        "s_cmp_ge_u32 s31, s43\n"
        "v_smfmac_f32_16x16x64_bf16 v[20:23], v[56:59], v[76:83], v18\n"
        "s_cselect_b32 s39, s22, s16\n"
        "v_mfma_f32_16x16x32_bf16 v[36:39], v[48:51], v[96:99], 0\n"
        "s_waitcnt lgkmcnt(2)\n"
        "v_perm_b32 v108, v33, v32, s63\n"
        "v_perm_b32 v109, v35, v34, s63\n"
        "s_cmp_gt_u32 s31, s43\n"
        "v_smfmac_f32_16x16x64_bf16 v[36:39], v[56:59], v[88:95], v18\n"
        "s_cselect_b32 s39, s23, s39\n"
        "ds_read_b64_tr_b16 v[78:79], v9 offset:48\n"
        "v_mfma_f32_16x16x32_bf16 v[32:35], v[48:51], v[104:107], 0\n"
        "s_waitcnt lgkmcnt(1)\n"
        "s_mul_hi_u32 s38, s39, 58835169\n"
        "v_smfmac_f32_16x16x64_bf16 v[32:35], v[56:59], v[112:119], v18\n"
        "s_mul_i32 s39, s39, 1344\n"
        "ds_read_b64_tr_b16 v[90:91], v9 offset:64\n"
        "v_cvt_pk_bf16_f32 v152, v20, v36\n"
        "v_cvt_pk_bf16_f32 v156, v21, v37\n"
        "v_cvt_pk_bf16_f32 v160, v22, v38\n"
        "v_cvt_pk_bf16_f32 v164, v23, v39\n"
        "v_mfma_f32_16x16x32_bf16 v[68:71], v[48:51], v[44:47], 0\n"
        "s_mul_i32 s38, s38, 588672\n"
        "s_waitcnt lgkmcnt(1)\n"
        "v_smfmac_f32_16x16x64_bf16 v[68:71], v[56:59], v[76:83], v18 abid:1\n"
        "s_add_u32 s39, s39, s38\n"
        "ds_read_b64_tr_b16 v[114:115], v9 offset:80\n"
        "v_mfma_f32_16x16x32_bf16 v[60:63], v[48:51], v[100:103], 0\n"
        "s_add_u32 s39, s39, s17\n"
        "ds_read_b128 v[36:39], v2\n"
        "v_mov_b64 v[20:21], 0\n"
        "v_mov_b64 v[22:23], 0\n"
        "s_waitcnt lgkmcnt(2)\n"
        "v_smfmac_f32_16x16x64_bf16 v[60:63], v[56:59], v[88:95], v18 abid:1\n"
        "s_mul_i32 s68, s31, 3\n"
        "ds_read_b128 v[52:55], v3\n"
        "v_cvt_pk_bf16_f32 v153, v32, v68\n"
        "v_cvt_pk_bf16_f32 v157, v33, v69\n"
        "v_cvt_pk_bf16_f32 v161, v34, v70\n"
        "v_cvt_pk_bf16_f32 v165, v35, v71\n"
        "v_mfma_f32_16x16x32_bf16 v[68:71], v[48:51], v[108:111], 0\n"
        "s_bitcmp1_b32 s31, 0\n"
        "v_mov_b64 v[32:33], 0\n"
        "v_mov_b64 v[34:35], 0\n"
        "s_waitcnt lgkmcnt(2)\n"
        "v_smfmac_f32_16x16x64_bf16 v[68:71], v[56:59], v[112:119], v18 abid:1\n"
        "s_cselect_b32 s38, 3, 6\n"
        "s_waitcnt lgkmcnt(1)\n"
        "v_mov_b32_dpp v67, v19 row_shr:4 row_mask:0xf bank_mask:0xf bound_ctrl:0\n"
        "v_smfmac_f32_16x16x64_bf16 v[20:23], v[36:39], v[40:47], v4\n"
        "s_add_u32 s68, s68, s38\n"
        "v_mov_b64 v[48:49], 0\n"
        "v_mov_b64 v[50:51], 0\n"
        "s_waitcnt lgkmcnt(0)\n"
        "v_smfmac_f32_16x16x64_bf16 v[20:23], v[52:55], v[76:83], v4 abid:1\n"
        "s_add_u32 s37, s35, 0\n"
        "s_cmp_lt_u32 s31, s14\n"
        "v_cvt_pk_bf16_f32 v154, v60, v68\n"
        "v_cvt_pk_bf16_f32 v158, v61, v69\n"
        "v_cvt_pk_bf16_f32 v162, v62, v70\n"
        "v_cvt_pk_bf16_f32 v166, v63, v71\n"
        "v_smfmac_f32_16x16x64_bf16 v[32:35], v[36:39], v[96:103], v4\n"
        "s_cselect_b32 s68, s68, 0\n"
        "v_add_f32 v20, v20, v21\n"
        "v_add_f32 v22, v22, v23\n"
        "v_smfmac_f32_16x16x64_bf16 v[32:35], v[52:55], v[88:95], v4 abid:1\n"
        "s_and_b32 s38, s31, 7\n"
        "ds_read_b64_tr_b16 v[78:79], v9 offset:3328\n"
        "v_smfmac_f32_16x16x64_bf16 v[48:51], v[36:39], v[104:111], v4\n"
        "buffer_store_dwordx3 v[152:154], v12, s[8:11], s37 offen sc0 sc1 nt\n"
        "s_cselect_b32 s30, 1, -7\n"
        "v_smfmac_f32_16x16x64_bf16 v[48:51], v[52:55], v[112:119], v4 abid:1\n"
        "s_mul_i32 s29, s30, 192\n"
        "ds_read_b64_tr_b16 v[90:91], v9 offset:3344\n"
        "v_add_f32 v32, v32, v33\n"
        "v_add_f32 v34, v34, v35\n"
        "v_cvt_pk_bf16_f32 v148, v20, v32\n"
        "v_mfma_f32_16x16x32_bf16 v[152:155], v[120:123], v[40:43], 0\n"
        "s_mul_i32 s30, s30, 392\n"
        "s_waitcnt lgkmcnt(1)\n"
        "v_smfmac_f32_16x16x64_bf16 v[152:155], v[64:67], v[76:83], v18\n"
        "s_add_u32 s37, s35, 0\n"
        "s_cmp_eq_u32 s69, s18\n"
        "ds_read_b64_tr_b16 v[114:115], v9 offset:3360\n"
        "v_add_f32 v48, v48, v49\n"
        "v_add_f32 v50, v50, v51\n"
        "v_cvt_pk_bf16_f32 v149, v48, v22\n"
        "v_cvt_pk_bf16_f32 v150, v34, v50\n"
        "v_mfma_f32_16x16x32_bf16 v[56:59], v[120:123], v[96:99], 0\n"
        "s_cselect_b32 s34, -7488, s70\n"
        "s_waitcnt lgkmcnt(1)\n"
        "v_smfmac_f32_16x16x64_bf16 v[56:59], v[64:67], v[88:95], v18\n"
        "s_add_u32 s69, s69, s34\n"
        "ds_read_b64_tr_b16 v[78:79], v9 offset:3376\n"
        "v_writelane_b32 v10, s69, 56\n"
        "v_add_u32 v11, s30, v11\n"
        "s_mov_b64 exec, s[54:55]\n"
        "v_add_u32 v16, s29, v16\n"
        "v_add_u32 v8, s34, v8\n"
        "s_mov_b64 exec, -1\n"
        "v_mfma_f32_16x16x32_bf16 v[72:75], v[120:123], v[104:107], 0\n"
        "buffer_store_dwordx3 v[156:158], v15, s[8:11], s37 offen offset:768 sc0 sc1 nt\n"
        "s_mov_b64 exec, s[50:51]\n"
        "v_add_u32 v5, s29, v5\n"
        "v_add_u32 v6, s34, v6\n"
        "s_mov_b64 exec, -1\n"
        "s_waitcnt lgkmcnt(1)\n"
        "v_smfmac_f32_16x16x64_bf16 v[72:75], v[64:67], v[112:119], v18\n"
        "ds_read_b64_tr_b16 v[90:91], v9 offset:3392\n"
        "v_mov_b32_dpp v31, v19 row_shr:4 row_mask:0xf bank_mask:0xf bound_ctrl:0\n"
        "v_mfma_f32_16x16x32_bf16 v[20:23], v[120:123], v[44:47], 0\n"
        "s_waitcnt lgkmcnt(1)\n"
        "v_smfmac_f32_16x16x64_bf16 v[20:23], v[64:67], v[76:83], v18 abid:1\n"
        "s_add_u32 s37, s35, 98112\n"
        "ds_read_b64_tr_b16 v[114:115], v9 offset:3408\n"
        "v_mfma_f32_16x16x32_bf16 v[84:87], v[120:123], v[100:103], 0\n"
        "s_waitcnt lgkmcnt(1)\n"
        "v_smfmac_f32_16x16x64_bf16 v[84:87], v[64:67], v[88:95], v18 abid:1\n"
        "ds_read_b64_tr_b16 v[78:79], v9 offset:6656\n"
        "v_mfma_f32_16x16x32_bf16 v[60:63], v[120:123], v[108:111], 0\n"
        "buffer_store_dwordx3 v[160:162], v12, s[8:11], s37 offen offset:192 sc0 sc1 nt\n"
        "ds_read_b32 v167, v0 offset:21604\n"
        "s_waitcnt lgkmcnt(2)\n"
        "v_smfmac_f32_16x16x64_bf16 v[60:63], v[64:67], v[112:119], v18 abid:1\n"
        "ds_read_b64_tr_b16 v[90:91], v9 offset:6672\n"
        "v_mfma_f32_16x16x32_bf16 v[68:71], v[24:27], v[40:43], 0\n"
        "s_waitcnt lgkmcnt(2)\n"
        "v_smfmac_f32_16x16x64_bf16 v[68:71], v[28:31], v[76:83], v18\n"
        "s_add_u32 s37, s35, 98112\n"
        "ds_read_b64_tr_b16 v[114:115], v9 offset:6688\n"
        "v_mfma_f32_16x16x32_bf16 v[36:39], v[24:27], v[96:99], 0\n"
        "s_waitcnt lgkmcnt(1)\n"
        "v_smfmac_f32_16x16x64_bf16 v[36:39], v[28:31], v[88:95], v18\n"
        "ds_read_b64_tr_b16 v[78:79], v9 offset:6704\n"
        "v_readfirstlane_b32 s40, v167\n"
        "v_mfma_f32_16x16x32_bf16 v[40:43], v[24:27], v[104:107], 0\n"
        "buffer_store_dwordx3 v[164:166], v14, s[8:11], s37 offen offset:960 sc0 sc1 nt\n"
        "s_add_u32 s37, s35, 588672\n"
        "s_add_u32 s46, s37, 1344\n"
        "s_cmp_lt_i32 s40, s68\n"
        "s_waitcnt lgkmcnt(1)\n"
        "v_smfmac_f32_16x16x64_bf16 v[40:43], v[28:31], v[112:119], v18\n"
        "buffer_store_dwordx3 v[148:150], v12, s[44:47], s37 offen offset:1152 sc0 sc1 nt\n"
        "ds_read_b64_tr_b16 v[90:91], v9 offset:6720\n"
        "v_mfma_f32_16x16x32_bf16 v[64:67], v[24:27], v[44:47], 0\n"
        "s_waitcnt lgkmcnt(1)\n"
        "v_smfmac_f32_16x16x64_bf16 v[64:67], v[28:31], v[76:83], v18 abid:1\n"
        "ds_read_b64_tr_b16 v[114:115], v9 offset:6736\n"
        "s_mov_b64 exec, 1\n"
        "ds_inc_u32 v0, v1 offset:21608\n"
        "s_mov_b64 exec, -1\n"
        "s_cbranch_scc1 L60A_%=\n"
        "L61A_%=:\n"
        "ds_read_b128 v[124:127], v7 offset:40064\n"
        "ds_read_b128 v[132:135], v7 offset:43392\n"
        "ds_read_b128 v[128:131], v7 offset:40128\n"
        "ds_read_b128 v[136:139], v7 offset:43456\n"
        "v_mfma_f32_16x16x32_bf16 v[104:107], v[24:27], v[100:103], 0\n"
        "s_waitcnt lgkmcnt(6)\n"
        "ds_read_b128 v[160:163], v7 offset:40192\n"
        "ds_read_b128 v[52:55], v7 offset:43520\n"
        "s_mov_b32 s36, s35\n"
        "v_smfmac_f32_16x16x64_bf16 v[104:107], v[28:31], v[88:95], v18 abid:1\n"
        "s_mov_b32 s35, s39\n"
        "v_mfma_f32_16x16x32_bf16 v[88:91], v[24:27], v[108:111], 0\n"
        "s_waitcnt lgkmcnt(7)\n"
        "v_smfmac_f32_16x16x64_bf16 v[88:91], v[28:31], v[112:119], v18 abid:1\n"
        "s_setprio 0\n"
        "s_waitcnt lgkmcnt(0)\n"
        "s_nop 15\n"
        "s_branch CNEXTA_%=\n"
        "MTOPB_%=:\n"
        "s_waitcnt lgkmcnt(5)\n"
        "v_mfma_f32_16x16x32_bf16 v[24:27], v[168:171], v[124:127], 0\n"
        "v_cvt_pk_bf16_f32 v112, v152, v56\n"
        "v_cvt_pk_bf16_f32 v113, v72, v20\n"
        "v_cvt_pk_bf16_f32 v114, v84, v60\n"
        "v_cvt_pk_bf16_f32 v116, v153, v57\n"
        "v_cvt_pk_bf16_f32 v117, v73, v21\n"
        "v_cvt_pk_bf16_f32 v118, v85, v61\n"
        "v_cvt_pk_bf16_f32 v120, v154, v58\n"
        "v_cvt_pk_bf16_f32 v121, v74, v22\n"
        "s_waitcnt lgkmcnt(3)\n"
        "v_mfma_f32_16x16x32_bf16 v[24:27], v[172:175], v[128:131], v[24:27]\n"
        "v_cvt_pk_bf16_f32 v122, v86, v62\n"
        "v_cvt_pk_bf16_f32 v140, v155, v59\n"
        "v_cvt_pk_bf16_f32 v141, v75, v23\n"
        "v_cvt_pk_bf16_f32 v142, v87, v63\n"
        "v_cvt_pk_bf16_f32 v144, v68, v36\n"
        "v_cvt_pk_bf16_f32 v145, v40, v64\n"
        "v_cvt_pk_bf16_f32 v146, v104, v88\n"
        "v_cvt_pk_bf16_f32 v148, v69, v37\n"
        "s_waitcnt lgkmcnt(1)\n"
        "v_mfma_f32_16x16x32_bf16 v[24:27], v[176:179], v[160:163], v[24:27]\n"
        "v_cvt_pk_bf16_f32 v149, v41, v65\n"
        "v_cvt_pk_bf16_f32 v150, v105, v89\n"
        "v_cvt_pk_bf16_f32 v152, v70, v38\n"
        "v_cvt_pk_bf16_f32 v153, v42, v66\n"
        "v_cvt_pk_bf16_f32 v154, v106, v90\n"
        "v_cvt_pk_bf16_f32 v156, v71, v39\n"
        "v_cvt_pk_bf16_f32 v157, v43, v67\n"
        "v_cvt_pk_bf16_f32 v158, v107, v91\n"
        "v_mfma_f32_16x16x32_bf16 v[32:35], v[188:191], v[160:163], 0\n"
        "v_perm_b32 v40, v25, v24, s63\n"
        "v_perm_b32 v41, v27, v26, s63\n"
        "v_mfma_f32_16x16x32_bf16 v[32:35], v[184:187], v[128:131], v[32:35]\n"
        "v_mfma_f32_16x16x32_bf16 v[32:35], v[180:183], v[124:127], v[32:35]\n"
        "s_add_u32 s37, s36, 196224\n"
        "ds_read_b128 v[28:31], v16 offset:128\n"
        "ds_read_b32 v159, v10\n"
        "v_mfma_f32_16x16x32_bf16 v[24:27], v[192:195], v[124:127], 0\n"
        "ds_read_b128 v[20:23], v5\n"
        "v_mfma_f32_16x16x32_bf16 v[24:27], v[196:199], v[128:131], v[24:27]\n"
        "ds_read_b64 v[164:165], v11 offset:192\n"
        "v_mfma_f32_16x16x32_bf16 v[24:27], v[200:203], v[160:163], v[24:27]\n"
        "buffer_store_dwordx3 v[112:114], v12, s[8:11], s37 offen offset:384 sc0 sc1 nt\n"
        "v_perm_b32 v42, v33, v32, s63\n"
        "v_perm_b32 v43, v35, v34, s63\n"
        "s_waitcnt lgkmcnt(3)\n"
        "v_mfma_f32_16x16x32_bf16 v[48:51], v[28:31], v[160:163], 0\n"
        "ds_read_b128 v[60:63], v7 offset:46848\n"
        "ds_read_i16 v167, v0 offset:20584\n"
        "ds_read_i16 v155, v0 offset:20586\n"
        "v_perm_b32 v44, v25, v24, s63\n"
        "v_perm_b32 v45, v27, v26, s63\n"
        "s_waitcnt lgkmcnt(3)\n"
        "v_smfmac_f32_16x16x64_bf16 v[48:51], v[20:23], v[124:131], v4\n"
        "s_add_u32 s37, s36, 196224\n"
        "ds_read_b128 v[32:35], v7 offset:46720\n"
        "ds_read_b128 v[36:39], v7 offset:46784\n"
        "v_mfma_f32_16x16x32_bf16 v[56:59], v[28:31], v[52:55], 0\n"
        "v_smfmac_f32_16x16x64_bf16 v[56:59], v[20:23], v[132:139], v4\n"
        "s_setprio 2\n"
        "s_waitcnt lgkmcnt(4)\n"
        "v_mfma_f32_16x16x32_bf16 v[24:27], v[28:31], v[60:63], 0\n"
        "buffer_store_dwordx3 v[116:118], v13, s[8:11], s37 offen offset:1152 sc0 sc1 nt\n"
        "v_add_f32_dpp v165, v50, v51 quad_perm:[0,1,2,3] row_mask:0x1 bank_mask:0xf\n"
        "s_waitcnt lgkmcnt(2)\n"
        "v_add_u32 v167, v9, v167\n"
        "v_add_f32_dpp v164, v48, v49 quad_perm:[0,1,2,3] row_mask:0x1 bank_mask:0xf\n"
        "v_add_f32_dpp v165, v58, v59 quad_perm:[0,1,2,3] row_mask:0x2 bank_mask:0xf\n"
        "v_add_u32 v155, v9, v155\n"
        "v_add_f32_dpp v164, v56, v57 quad_perm:[0,1,2,3] row_mask:0x2 bank_mask:0xf\n"
        "s_waitcnt lgkmcnt(0)\n"
        "v_smfmac_f32_16x16x64_bf16 v[24:27], v[20:23], v[32:39], v4\n"
        "v_mfma_f32_16x16x32_bf16 v[28:31], v[212:215], v[160:163], 0\n"
        "s_add_u32 s37, s36, 294336\n"
        "v_mfma_f32_16x16x32_bf16 v[28:31], v[208:211], v[128:131], v[28:31]\n"
        "v_mfma_f32_16x16x32_bf16 v[28:31], v[204:207], v[124:127], v[28:31]\n"
        "v_mfma_f32_16x16x32_bf16 v[20:23], v[216:219], v[124:127], 0\n"
        "buffer_store_dwordx3 v[120:122], v12, s[8:11], s37 offen offset:576 sc0 sc1 nt\n"
        "v_mfma_f32_16x16x32_bf16 v[20:23], v[220:223], v[128:131], v[20:23]\n"
        "v_mfma_f32_16x16x32_bf16 v[20:23], v[224:227], v[160:163], v[20:23]\n"
        "s_add_u32 s37, s36, 392448\n"
        "v_add_f32_dpp v165, v26, v27 quad_perm:[0,1,2,3] row_mask:0x4 bank_mask:0xf\n"
        "v_mul_f32 v166, s52, v165\n"
        "v_fma_f32 v166, -v166, v165, v159\n"
        "v_perm_b32 v46, v29, v28, s63\n"
        "v_perm_b32 v47, v31, v30, s63\n"
        "v_add_f32_e64 v166, s53, |v166|\n"
        "v_rsq_f32 v159, v166\n"
        "v_mfma_f32_16x16x32_bf16 v[56:59], v[236:239], v[160:163], 0\n"
        "s_mov_b32 exec_hi, 0xffff\n"
        "ds_write_b32 v10, v159\n"
        "s_mov_b32 exec_hi, -1\n"
        "v_perm_b32 v48, v21, v20, s63\n"
        "v_perm_b32 v49, v23, v22, s63\n"
        "ds_read_b32 v20, v167 offset:40064\n"
        "v_add_f32_dpp v164, v24, v25 quad_perm:[0,1,2,3] row_mask:0x4 bank_mask:0xf\n"
        "v_mfma_f32_16x16x32_bf16 v[56:59], v[232:235], v[128:131], v[56:59]\n"
        "s_mov_b32 exec_hi, 0xffff\n"
        "v_add_u32 v10, -40064, v10\n"
        "s_mov_b32 exec_hi, -1\n"
        "ds_read_b32 v21, v167 offset:40896\n"
        "ds_read_b32 v22, v167 offset:41728\n"
        "ds_read_b32 v23, v167 offset:42560\n"
        "ds_read_b32 v24, v167 offset:43392\n"
        "v_mul_f32 v164, v164, v159\n"
        "v_mfma_f32_16x16x32_bf16 v[56:59], v[228:231], v[124:127], v[56:59]\n"
        "buffer_store_dwordx3 v[140:142], v12, s[8:11], s37 offen sc0 sc1 nt\n"
        "ds_read_b32 v25, v167 offset:44224\n"
        "ds_read_b32 v26, v167 offset:45056\n"
        "ds_read_b32 v27, v167 offset:45888\n"
        "ds_read_b32 v28, v167 offset:46720\n"
        "ds_read_b32 v29, v167 offset:47552\n"
        "ds_read_b32 v30, v167 offset:48384\n"
        "ds_read_b32 v31, v167 offset:49216\n"
        "v_perm_b32 v50, v57, v56, s63\n"
        "v_perm_b32 v51, v59, v58, s63\n"
        "v_readlane_b32 s58, v159, 56\n"
        "v_mfma_f32_16x16x32_bf16 v[56:59], v[124:127], v[40:43], 0\n"
        "s_mov_b64 exec, s[56:57]\n"
        "v_mov_b32 v164, 0xff800000\n"
        "s_mov_b64 exec, -1\n"
        "ds_read_b32 v165, v155 offset:40064\n"
        "ds_read_b32 v166, v155 offset:43392\n"
        "v_max_f32_dpp v151, v164, v164 row_ror:8 row_mask:0xf bank_mask:0xf\n"
        "v_mfma_f32_16x16x32_bf16 v[56:59], v[128:131], v[44:47], v[56:59]\n"
        "s_add_u32 s37, s36, 392448\n"
        "v_max_f32_dpp v151, v151, v151 row_ror:4 row_mask:0xf bank_mask:0xf\n"
        "ds_read_b32 v167, v155 offset:46720\n"
        "v_mfma_f32_16x16x32_bf16 v[56:59], v[160:163], v[48:51], v[56:59]\n"
        "v_max_f32_dpp v151, v151, v151 row_ror:2 row_mask:0xf bank_mask:0xf\n"
        "v_mfma_f32_16x16x32_bf16 v[72:75], v[52:55], v[48:51], 0\n"
        "v_mfma_f32_16x16x32_bf16 v[72:75], v[136:139], v[44:47], v[72:75]\n"
        "buffer_store_dwordx3 v[144:146], v15, s[8:11], s37 offen offset:768 sc0 sc1 nt\n"
        "v_max_f32_dpp v151, v151, v151 row_ror:1 row_mask:0xf bank_mask:0xf\n"
        "v_mfma_f32_16x16x32_bf16 v[72:75], v[132:135], v[40:43], v[72:75]\n"
        "ds_read_b128 v[88:91], v8 offset:128\n"
        "v_mfma_f32_16x16x32_bf16 v[68:71], v[32:35], v[40:43], 0\n"
        "s_add_u32 s37, s36, 490560\n"
        "ds_read2_b32 v[92:93], v6 offset0:0 offset1:2\n"
        "ds_read2_b32 v[94:95], v6 offset0:8 offset1:10\n"
        "s_waitcnt lgkmcnt(3)\n"
        "v_mul_f32 v56, v56, v20\n"
        "v_mul_f32 v57, v57, v21\n"
        "v_mul_f32 v58, v58, v22\n"
        "v_mul_f32 v59, v59, v23\n"
        "v_mul_f32 v72, v72, v24\n"
        "v_mfma_f32_16x16x32_bf16 v[68:71], v[36:39], v[44:47], v[68:71]\n"
        "v_max3_f32 v155, v56, v57, v58\n"
        "v_mul_f32 v73, v73, v25\n"
        "v_mul_f32 v74, v74, v26\n"
        "v_max3_f32 v155, v155, v59, v72\n"
        "v_mul_f32 v75, v75, v27\n"
        "v_max3_f32 v155, v155, v73, v74\n"
        "v_mfma_f32_16x16x32_bf16 v[68:71], v[60:63], v[48:51], v[68:71]\n"
        "s_waitcnt lgkmcnt(2)\n"
        "v_mfma_f32_16x16x32_bf16 v[76:79], v[88:91], v[48:51], 0\n"
        "buffer_store_dwordx3 v[148:150], v12, s[8:11], s37 offen offset:192 sc0 sc1 nt\n"
        "s_waitcnt lgkmcnt(0)\n"
        "v_smfmac_f32_16x16x64_bf16 v[76:79], v[92:95], v[40:47], v4\n"
        "v_mfma_f32_16x16x32_bf16 v[48:51], v[168:171], v[132:135], 0\n"
        "s_add_u32 s37, s36, 490560\n"
        "v_mfma_f32_16x16x32_bf16 v[48:51], v[172:175], v[136:139], v[48:51]\n"
        "v_mul_f32 v68, v68, v28\n"
        "v_mul_f32 v69, v69, v29\n"
        "v_mul_f32 v70, v70, v30\n"
        "v_max3_f32 v155, v155, v75, v68\n"
        "v_mul_f32 v71, v71, v31\n"
        "v_add_f32 v76, v76, v77\n"
        "v_mul_f32 v76, s58, v76\n"
        "v_max3_f32 v155, v155, v69, v70\n"
        "v_mfma_f32_16x16x32_bf16 v[48:51], v[176:179], v[52:55], v[48:51]\n"
        "s_mov_b64 exec, s[48:49]\n"
        "v_mov_b32 v76, 0xff800000\n"
        "s_mov_b64 exec, -1\n"
        "v_max3_f32 v155, v155, v71, v76\n"
        "v_mfma_f32_16x16x32_bf16 v[44:47], v[188:191], v[52:55], 0\n"
        "buffer_store_dwordx3 v[152:154], v14, s[8:11], s37 offen offset:960 sc0 sc1 nt\n"
        "v_permlane32_swap_b32 v155, v151\n"
        "v_max_f32 v155, v155, v151\n"
        "v_perm_b32 v40, v49, v48, s63\n"
        "v_perm_b32 v41, v51, v50, s63\n"
        "v_mov_b32 v154, v155\n"
        "v_mfma_f32_16x16x32_bf16 v[44:47], v[184:187], v[136:139], v[44:47]\n"
        "v_mfma_f32_16x16x32_bf16 v[44:47], v[180:183], v[132:135], v[44:47]\n"
        "s_add_u32 s37, s36, 588672\n"
        "v_permlane16_swap_b32 v155, v154\n"
        "v_max_f32 v155, v155, v154\n"
        "v_mov_b32 v151, v155\n"
        "v_mfma_f32_16x16x32_bf16 v[64:67], v[192:195], v[132:135], 0\n"
        "v_mfma_f32_16x16x32_bf16 v[64:67], v[196:199], v[136:139], v[64:67]\n"
        "v_permlane32_swap_b32 v155, v151\n"
        "v_mul_f32 v155, v155, v165\n"
        "v_perm_b32 v42, v45, v44, s63\n"
        "v_perm_b32 v43, v47, v46, s63\n"
        "v_fma_f32 v56, v56, v165, -v155\n"
        "v_fma_f32 v57, v57, v165, -v155\n"
        "v_fma_f32 v58, v58, v165, -v155\n"
        "v_mfma_f32_16x16x32_bf16 v[64:67], v[200:203], v[52:55], v[64:67]\n"
        "buffer_store_dwordx3 v[156:158], v12, s[8:11], s37 offen offset:384 sc0 sc1 nt\n"
        "v_exp_f32 v56, v56\n"
        "v_exp_f32 v57, v57\n"
        "v_exp_f32 v58, v58\n"
        "v_fma_f32 v59, v59, v165, -v155\n"
        "v_add_f32 v158, v56, v57\n"
        "v_exp_f32 v59, v59\n"
        "v_mfma_f32_16x16x32_bf16 v[48:51], v[212:215], v[52:55], 0\n"
        "v_perm_b32 v44, v65, v64, s63\n"
        "v_perm_b32 v45, v67, v66, s63\n"
        "v_fma_f32 v72, v72, v165, -v155\n"
        "v_add_f32 v158, v158, v58\n"
        "v_exp_f32 v72, v72\n"
        "v_fma_f32 v73, v73, v165, -v155\n"
        "v_add_f32 v158, v158, v59\n"
        "v_mfma_f32_16x16x32_bf16 v[48:51], v[208:211], v[136:139], v[48:51]\n"
        "v_exp_f32 v73, v73\n"
        "v_fma_f32 v74, v74, v165, -v155\n"
        "v_add_f32 v158, v158, v72\n"
        "v_exp_f32 v74, v74\n"
        "v_fma_f32 v75, v75, v165, -v155\n"
        "v_sub_f32 v164, v164, v151\n"
        "v_mfma_f32_16x16x32_bf16 v[48:51], v[204:207], v[132:135], v[48:51]\n"
        "v_add_f32 v158, v158, v73\n"
        "v_exp_f32 v75, v75\n"
        "v_fma_f32 v68, v68, v165, -v155\n"
        "v_mul_f32 v164, s58, v164\n"
        "v_add_f32 v158, v158, v74\n"
        "v_exp_f32 v68, v68\n"
        "v_mfma_f32_16x16x32_bf16 v[64:67], v[216:219], v[132:135], 0\n"
        "v_fma_f32 v69, v69, v165, -v155\n"
        "v_perm_b32 v46, v49, v48, s63\n"
        "v_perm_b32 v47, v51, v50, s63\n"
        "v_exp_f32 v164, v164\n"
        "v_add_f32 v158, v158, v75\n"
        "v_exp_f32 v69, v69\n"
        "v_mfma_f32_16x16x32_bf16 v[64:67], v[220:223], v[136:139], v[64:67]\n"
        "v_fma_f32 v70, v70, v165, -v155\n"
        "v_add_f32 v158, v158, v68\n"
        "v_exp_f32 v70, v70\n"
        "v_fma_f32 v71, v71, v165, -v155\n"
        "v_add_f32_dpp v157, v164, v164 row_ror:8 row_mask:0xf bank_mask:0xf\n"
        "v_add_f32 v158, v158, v69\n"
        "v_fma_f32 v76, v76, v165, -v155\n"
        "v_mfma_f32_16x16x32_bf16 v[64:67], v[224:227], v[52:55], v[64:67]\n"
        "v_exp_f32 v71, v71\n"
        "v_add_f32_dpp v157, v157, v157 row_ror:4 row_mask:0xf bank_mask:0xf\n"
        "v_add_f32 v158, v158, v70\n"
        "v_exp_f32 v76, v76\n"
        "v_add_f32_dpp v157, v157, v157 row_ror:2 row_mask:0xf bank_mask:0xf\n"
        "v_add_f32 v158, v158, v71\n"
        "v_mfma_f32_16x16x32_bf16 v[48:51], v[236:239], v[52:55], 0\n"
        "v_add_f32_dpp v157, v157, v157 row_ror:1 row_mask:0xf bank_mask:0xf\n"
        "v_perm_b32 v80, v65, v64, s63\n"
        "v_perm_b32 v81, v67, v66, s63\n"
        "v_add_f32 v158, v158, v76\n"
        "v_mul_f32 v56, v56, v20\n"
        "v_mul_f32 v57, v57, v21\n"
        "v_permlane32_swap_b32 v158, v157\n"
        "v_mfma_f32_16x16x32_bf16 v[48:51], v[232:235], v[136:139], v[48:51]\n"
        "v_add_f32 v158, v158, v157\n"
        "v_mov_b32 v165, v158\n"
        "v_mul_f32 v58, v58, v22\n"
        "v_mul_f32 v59, v59, v23\n"
        "v_permlane16_swap_b32 v158, v165\n"
        "v_add_f32 v158, v158, v165\n"
        "v_mul_f32 v72, v72, v24\n"
        "v_mfma_f32_16x16x32_bf16 v[48:51], v[228:231], v[132:135], v[48:51]\n"
        "v_rcp_f32 v158, v158\n"
        "v_mul_f32 v73, v73, v25\n"
        "v_mov_b32 v157, v158\n"
        "v_mul_f32 v74, v74, v26\n"
        "v_mul_f32 v75, v75, v27\n"
        "v_permlane32_swap_b32 v158, v157\n"
        "v_mfma_f32_16x16x32_bf16 v[64:67], v[124:127], v[40:43], 0\n"
        "v_mul_f32 v56, v56, v158\n"
        "v_perm_b32 v82, v49, v48, s63\n"
        "v_perm_b32 v83, v51, v50, s63\n"
        "v_mul_f32 v57, v57, v158\n"
        "v_mul_f32 v58, v58, v158\n"
        "v_mul_f32 v59, v59, v158\n"
        "v_mul_f32 v72, v72, v158\n"
        "v_mul_f32 v73, v73, v158\n"
        "v_mfma_f32_16x16x32_bf16 v[64:67], v[128:131], v[44:47], v[64:67]\n"
        "v_mul_f32 v74, v74, v158\n"
        "v_mul_f32 v75, v75, v158\n"
        "v_perm_b32 v48, v57, v56, s63\n"
        "v_perm_b32 v49, v59, v58, s63\n"
        "v_perm_b32 v50, v73, v72, s63\n"
        "v_perm_b32 v51, v75, v74, s63\n"
        "v_mul_f32 v68, v68, v28\n"
        "v_mul_f32 v69, v69, v29\n"
        "v_mfma_f32_16x16x32_bf16 v[64:67], v[160:163], v[80:83], v[64:67]\n"
        "v_mul_f32 v70, v70, v30\n"
        "v_mul_f32 v71, v71, v31\n"
        "v_mul_f32 v76, s58, v76\n"
        "v_mul_f32 v68, v68, v158\n"
        "v_mul_f32 v69, v69, v158\n"
        "v_mul_f32 v70, v70, v158\n"
        "v_mul_f32 v71, v71, v158\n"
        "v_fma_f32 v76, v76, v158, v19\n"
        "v_mfma_f32_16x16x32_bf16 v[84:87], v[52:55], v[80:83], 0\n"
        "v_mul_f32 v64, v64, v20\n"
        "v_mul_f32 v65, v65, v21\n"
        "v_mul_f32 v66, v66, v22\n"
        "v_mul_f32 v67, v67, v23\n"
        "v_max3_f32 v165, v64, v65, v66\n"
        "v_perm_b32 v56, v69, v68, s63\n"
        "v_perm_b32 v58, v71, v70, s63\n"
        "v_perm_b32 v57, 0, v76, s63\n"
        "v_mfma_f32_16x16x32_bf16 v[84:87], v[136:139], v[44:47], v[84:87]\n"
        "v_mul_f32 v164, v164, v159\n"
        "v_mul_f32 v164, v164, v157\n"
        "v_perm_b32 v164, 0, v164, s63\n"
        "ds_write_b16 v1, v164\n"
        "v_mfma_f32_16x16x32_bf16 v[84:87], v[132:135], v[40:43], v[84:87]\n"
        "v_mfma_f32_16x16x32_bf16 v[68:71], v[32:35], v[40:43], 0\n"
        "v_mfma_f32_16x16x32_bf16 v[68:71], v[36:39], v[44:47], v[68:71]\n"
        "v_mfma_f32_16x16x32_bf16 v[68:71], v[60:63], v[80:83], v[68:71]\n"
        "v_mfma_f32_16x16x32_bf16 v[72:75], v[88:91], v[80:83], 0\n"
        "v_smfmac_f32_16x16x64_bf16 v[72:75], v[92:95], v[40:47], v4\n"
        "v_mfma_f32_16x16x32_bf16 v[80:83], v[168:171], v[32:35], 0\n"
        "v_mfma_f32_16x16x32_bf16 v[80:83], v[172:175], v[36:39], v[80:83]\n"
        "v_mfma_f32_16x16x32_bf16 v[80:83], v[176:179], v[60:63], v[80:83]\n"
        "v_mul_f32 v84, v84, v24\n"
        "v_mul_f32 v85, v85, v25\n"
        "v_mul_f32 v86, v86, v26\n"
        "v_max3_f32 v165, v165, v67, v84\n"
        "v_mul_f32 v87, v87, v27\n"
        "v_add_f32 v72, v72, v73\n"
        "v_mul_f32 v68, v68, v28\n"
        "v_mul_f32 v72, s58, v72\n"
        "v_mfma_f32_16x16x32_bf16 v[40:43], v[188:191], v[60:63], 0\n"
        "s_mov_b64 exec, s[48:49]\n"
        "v_mov_b32 v72, 0xff800000\n"
        "s_mov_b64 exec, -1\n"
        "v_perm_b32 v76, v81, v80, s63\n"
        "v_perm_b32 v77, v83, v82, s63\n"
        "v_max3_f32 v165, v165, v85, v86\n"
        "v_mul_f32 v69, v69, v29\n"
        "v_mul_f32 v70, v70, v30\n"
        "v_max3_f32 v165, v165, v87, v68\n"
        "v_mfma_f32_16x16x32_bf16 v[40:43], v[184:187], v[36:39], v[40:43]\n"
        "v_mul_f32 v71, v71, v31\n"
        "v_max3_f32 v165, v165, v69, v70\n"
        "v_max3_f32 v165, v165, v71, v72\n"
        "v_mfma_f32_16x16x32_bf16 v[40:43], v[180:183], v[32:35], v[40:43]\n"
        "v_mfma_f32_16x16x32_bf16 v[100:103], v[192:195], v[32:35], 0\n"
        "v_mfma_f32_16x16x32_bf16 v[100:103], v[196:199], v[36:39], v[100:103]\n"
        "v_mfma_f32_16x16x32_bf16 v[100:103], v[200:203], v[60:63], v[100:103]\n"
        "v_mfma_f32_16x16x32_bf16 v[96:99], v[212:215], v[60:63], 0\n"
        "v_mfma_f32_16x16x32_bf16 v[96:99], v[208:211], v[36:39], v[96:99]\n"
        "v_mfma_f32_16x16x32_bf16 v[96:99], v[204:207], v[32:35], v[96:99]\n"
        "v_mfma_f32_16x16x32_bf16 v[44:47], v[216:219], v[32:35], 0\n"
        "v_mfma_f32_16x16x32_bf16 v[44:47], v[220:223], v[36:39], v[44:47]\n"
        "v_perm_b32 v78, v41, v40, s63\n"
        "v_perm_b32 v79, v43, v42, s63\n"
        "v_mfma_f32_16x16x32_bf16 v[44:47], v[224:227], v[60:63], v[44:47]\n"
        "v_perm_b32 v80, v101, v100, s63\n"
        "v_perm_b32 v81, v103, v102, s63\n"
        "v_mfma_f32_16x16x32_bf16 v[40:43], v[236:239], v[60:63], 0\n"
        "v_perm_b32 v82, v97, v96, s63\n"
        "v_perm_b32 v83, v99, v98, s63\n"
        "v_mfma_f32_16x16x32_bf16 v[40:43], v[232:235], v[36:39], v[40:43]\n"
        "v_mfma_f32_16x16x32_bf16 v[40:43], v[228:231], v[32:35], v[40:43]\n"
        "v_mfma_f32_16x16x32_bf16 v[108:111], v[124:127], v[76:79], 0\n"
        "v_perm_b32 v96, v45, v44, s63\n"
        "v_perm_b32 v97, v47, v46, s63\n"
        "v_mfma_f32_16x16x32_bf16 v[108:111], v[128:131], v[80:83], v[108:111]\n"
        "s_nop 3\n"
        "v_perm_b32 v98, v41, v40, s63\n"
        "v_perm_b32 v99, v43, v42, s63\n"
        "s_nop 1\n"
        "v_mfma_f32_16x16x32_bf16 v[108:111], v[160:163], v[96:99], v[108:111]\n"
        "v_mfma_f32_16x16x32_bf16 v[140:143], v[52:55], v[96:99], 0\n"
        "v_mfma_f32_16x16x32_bf16 v[140:143], v[136:139], v[80:83], v[140:143]\n"
        "v_mfma_f32_16x16x32_bf16 v[140:143], v[132:135], v[76:79], v[140:143]\n"
        "v_mfma_f32_16x16x32_bf16 v[100:103], v[32:35], v[76:79], 0\n"
        "v_mfma_f32_16x16x32_bf16 v[100:103], v[36:39], v[80:83], v[100:103]\n"
        "v_mfma_f32_16x16x32_bf16 v[100:103], v[60:63], v[96:99], v[100:103]\n"
        "v_mfma_f32_16x16x32_bf16 v[44:47], v[88:91], v[96:99], 0\n"
        "v_smfmac_f32_16x16x64_bf16 v[44:47], v[92:95], v[76:83], v4\n"
        "v_mul_f32 v108, v108, v20\n"
        "v_mul_f32 v109, v109, v21\n"
        "v_mul_f32 v110, v110, v22\n"
        "v_mul_f32 v111, v111, v23\n"
        "v_mul_f32 v140, v140, v24\n"
        "v_max3_f32 v159, v108, v109, v110\n"
        "v_mul_f32 v141, v141, v25\n"
        "v_mfma_f32_16x16x32_bf16 v[60:63], v[124:127], v[240:243], 0\n"
        "v_add_f32 v44, v44, v45\n"
        "v_mul_f32 v142, v142, v26\n"
        "v_max3_f32 v159, v159, v111, v140\n"
        "v_mul_f32 v143, v143, v27\n"
        "v_mul_f32 v100, v100, v28\n"
        "v_mul_f32 v44, s58, v44\n"
        "v_max3_f32 v159, v159, v141, v142\n"
        "v_mul_f32 v101, v101, v29\n"
        "v_mfma_f32_16x16x32_bf16 v[60:63], v[128:131], v[244:247], v[60:63]\n"
        "s_mov_b64 exec, s[48:49]\n"
        "v_mov_b32 v44, 0xff800000\n"
        "s_mov_b64 exec, -1\n"
        "v_mul_f32 v102, v102, v30\n"
        "v_max3_f32 v159, v159, v143, v100\n"
        "v_mul_f32 v103, v103, v31\n"
        "v_max3_f32 v159, v159, v101, v102\n"
        "v_max3_f32 v159, v159, v103, v44\n"
        "v_mfma_f32_16x16x32_bf16 v[60:63], v[160:163], v[248:251], v[60:63]\n"
        "ds_read_b128 v[32:35], v17 offset:40064\n"
        "v_permlane32_swap_b32 v165, v159\n"
        "v_max_f32 v165, v165, v159\n"
        "v_mov_b32 v164, v165\n"
        "v_mfma_f32_16x16x32_bf16 v[76:79], v[52:55], v[248:251], 0\n"
        "ds_read_b128 v[36:39], v17 offset:40128\n"
        "v_permlane16_swap_b32 v165, v164\n"
        "v_max_f32 v165, v165, v164\n"
        "v_perm_b32 v40, v61, v60, s63\n"
        "v_perm_b32 v41, v63, v62, s63\n"
        "v_mov_b32 v159, v165\n"
        "v_mfma_f32_16x16x32_bf16 v[76:79], v[136:139], v[244:247], v[76:79]\n"
        "ds_read_b128 v[60:63], v17 offset:40192\n"
        "v_permlane32_swap_b32 v165, v159\n"
        "v_mul_f32 v165, v165, v166\n"
        "v_mul_f32 v159, v159, v167\n"
        "v_fma_f32 v64, v64, v166, -v165\n"
        "v_fma_f32 v65, v65, v166, -v165\n"
        "v_fma_f32 v108, v108, v167, -v159\n"
        "v_mfma_f32_16x16x32_bf16 v[76:79], v[132:135], v[240:243], v[76:79]\n"
        "s_waitcnt lgkmcnt(2)\n"
        "ds_read_b128 v[96:99], v0 offset:5136\n"
        "v_fma_f32 v109, v109, v167, -v159\n"
        "v_exp_f32 v64, v64\n"
        "v_exp_f32 v65, v65\n"
        "v_fma_f32 v66, v66, v166, -v165\n"
        "v_fma_f32 v110, v110, v167, -v159\n"
        "v_mfma_f32_16x16x32_bf16 v[80:83], v[32:35], v[240:243], 0\n"
        "s_waitcnt lgkmcnt(2)\n"
        "ds_read_b128 v[104:107], v0 offset:4112\n"
        "v_perm_b32 v42, v77, v76, s63\n"
        "v_perm_b32 v43, v79, v78, s63\n"
        "v_exp_f32 v108, v108\n"
        "v_exp_f32 v109, v109\n"
        "v_fma_f32 v67, v67, v166, -v165\n"
        "v_mfma_f32_16x16x32_bf16 v[80:83], v[36:39], v[244:247], v[80:83]\n"
        "s_waitcnt lgkmcnt(2)\n"
        "v_exp_f32 v66, v66\n"
        "v_exp_f32 v110, v110\n"
        "v_fma_f32 v111, v111, v167, -v159\n"
        "v_add_f32 v95, v64, v65\n"
        "v_exp_f32 v67, v67\n"
        "v_mfma_f32_16x16x32_bf16 v[80:83], v[60:63], v[248:251], v[80:83]\n"
        "s_waitcnt lgkmcnt(1)\n"
        "v_fma_f32 v84, v84, v166, -v165\n"
        "v_add_f32 v164, v108, v109\n"
        "v_exp_f32 v111, v111\n"
        "v_fma_f32 v140, v140, v167, -v159\n"
        "v_add_f32 v95, v95, v66\n"
        "v_exp_f32 v84, v84\n"
        "v_mfma_f32_16x16x32_bf16 v[112:115], v[60:63], v[96:99], 0\n"
        "s_waitcnt lgkmcnt(0)\n"
        "v_perm_b32 v76, v81, v80, s63\n"
        "v_perm_b32 v80, v83, v82, s63\n"
        "v_fma_f32 v85, v85, v166, -v165\n"
        "v_add_f32 v164, v164, v110\n"
        "v_exp_f32 v140, v140\n"
        "v_fma_f32 v141, v141, v167, -v159\n"
        "v_add_f32 v95, v95, v67\n"
        "v_mfma_f32_16x16x32_bf16 v[112:115], v[36:39], v[104:107], v[112:115]\n"
        "v_exp_f32 v85, v85\n"
        "v_fma_f32 v86, v86, v166, -v165\n"
        "v_add_f32 v164, v164, v111\n"
        "v_exp_f32 v141, v141\n"
        "v_fma_f32 v142, v142, v167, -v159\n"
        "v_add_f32 v95, v95, v84\n"
        "v_mfma_f32_16x16x32_bf16 v[112:115], v[32:35], v[252:255], v[112:115]\n"
        "v_exp_f32 v86, v86\n"
        "v_fma_f32 v87, v87, v166, -v165\n"
        "v_add_f32 v164, v164, v140\n"
        "v_exp_f32 v142, v142\n"
        "v_fma_f32 v143, v143, v167, -v159\n"
        "v_add_f32 v95, v95, v85\n"
        "v_mfma_f32_16x16x32_bf16 v[120:123], v[132:135], v[252:255], 0\n"
        "v_exp_f32 v87, v87\n"
        "v_perm_b32 v88, v113, v112, s63\n"
        "v_perm_b32 v92, v115, v114, s63\n"
        "v_fma_f32 v68, v68, v166, -v165\n"
        "v_add_f32 v164, v164, v141\n"
        "v_exp_f32 v143, v143\n"
        "v_mfma_f32_16x16x32_bf16 v[120:123], v[136:139], v[104:107], v[120:123]\n"
        "v_fma_f32 v100, v100, v167, -v159\n"
        "v_add_f32 v95, v95, v86\n"
        "v_exp_f32 v68, v68\n"
        "v_fma_f32 v69, v69, v166, -v165\n"
        "v_add_f32 v164, v164, v142\n"
        "v_exp_f32 v100, v100\n"
        "v_mfma_f32_16x16x32_bf16 v[120:123], v[52:55], v[96:99], v[120:123]\n"
        "ds_read_b128 v[144:147], v0 offset:6176\n"
        "v_fma_f32 v101, v101, v167, -v159\n"
        "v_add_f32 v95, v95, v87\n"
        "v_exp_f32 v69, v69\n"
        "v_fma_f32 v70, v70, v166, -v165\n"
        "v_add_f32 v164, v164, v143\n"
        "v_fma_f32 v102, v102, v167, -v159\n"
        "v_mfma_f32_16x16x32_bf16 v[148:151], v[160:163], v[96:99], 0\n"
        "ds_read_b128 v[116:119], v0 offset:7200\n"
        "v_perm_b32 v98, v121, v120, s63\n"
        "v_perm_b32 v99, v123, v122, s63\n"
        "v_exp_f32 v101, v101\n"
        "v_add_f32 v95, v95, v68\n"
        "v_exp_f32 v70, v70\n"
        "v_mfma_f32_16x16x32_bf16 v[148:151], v[128:131], v[104:107], v[148:151]\n"
        "ds_read_b128 v[112:115], v0 offset:8224\n"
        "v_fma_f32 v71, v71, v166, -v165\n"
        "v_add_f32 v164, v164, v100\n"
        "v_exp_f32 v102, v102\n"
        "v_fma_f32 v103, v103, v167, -v159\n"
        "v_add_f32 v95, v95, v69\n"
        "v_fma_f32 v72, v72, v166, -v165\n"
        "v_mfma_f32_16x16x32_bf16 v[148:151], v[124:127], v[252:255], v[148:151]\n"
        "s_waitcnt lgkmcnt(2)\n"
        "v_exp_f32 v71, v71\n"
        "v_add_f32 v164, v164, v101\n"
        "v_exp_f32 v103, v103\n"
        "v_fma_f32 v44, v44, v167, -v159\n"
        "v_add_f32 v95, v95, v70\n"
        "v_add_f32 v164, v164, v102\n"
        "v_mfma_f32_16x16x32_bf16 v[120:123], v[124:127], v[144:147], 0\n"
        "s_waitcnt lgkmcnt(1)\n"
        "v_perm_b32 v96, v149, v148, s63\n"
        "v_perm_b32 v97, v151, v150, s63\n"
        "v_exp_f32 v72, v72\n"
        "v_exp_f32 v44, v44\n"
        "v_add_f32 v95, v95, v71\n"
        "v_add_f32 v164, v164, v103\n"
        "v_mfma_f32_16x16x32_bf16 v[120:123], v[128:131], v[116:119], v[120:123]\n"
        "s_waitcnt lgkmcnt(0)\n"
        "v_add_f32 v95, v95, v72\n"
        "v_add_f32 v164, v164, v44\n"
        "v_mul_f32 v64, v64, v20\n"
        "v_mul_f32 v65, v65, v21\n"
        "v_permlane32_swap_b32 v95, v164\n"
        "v_add_f32 v95, v95, v164\n"
        "v_mov_b32 v167, v95\n"
        "v_mfma_f32_16x16x32_bf16 v[120:123], v[160:163], v[112:115], v[120:123]\n"
        "v_mul_f32 v66, v66, v22\n"
        "v_permlane16_swap_b32 v95, v167\n"
        "v_add_f32 v95, v95, v167\n"
        "v_rcp_f32 v95, v95\n"
        "v_mul_f32 v67, v67, v23\n"
        "v_mov_b32 v164, v95\n"
        "v_mfma_f32_16x16x32_bf16 v[156:159], v[52:55], v[112:115], 0\n"
        "v_mul_f32 v84, v84, v24\n"
        "v_perm_b32 v104, v121, v120, s63\n"
        "v_perm_b32 v105, v123, v122, s63\n"
        "v_permlane32_swap_b32 v95, v164\n"
        "v_mul_f32 v85, v85, v25\n"
        "v_mul_f32 v86, v86, v26\n"
        "v_mul_f32 v87, v87, v27\n"
        "v_mfma_f32_16x16x32_bf16 v[156:159], v[136:139], v[116:119], v[156:159]\n"
        "v_mul_f32 v64, v64, v95\n"
        "v_mul_f32 v65, v65, v95\n"
        "v_mul_f32 v66, v66, v95\n"
        "v_mul_f32 v67, v67, v95\n"
        "v_mul_f32 v84, v84, v95\n"
        "v_mul_f32 v85, v85, v95\n"
        "v_mul_f32 v86, v86, v95\n"
        "v_mul_f32 v87, v87, v95\n"
        "v_mfma_f32_16x16x32_bf16 v[156:159], v[132:135], v[144:147], v[156:159]\n"
        "ds_read_b128 v[148:151], v0 offset:11312\n"
        "v_perm_b32 v120, v65, v64, s63\n"
        "v_perm_b32 v121, v67, v66, s63\n"
        "v_perm_b32 v122, v85, v84, s63\n"
        "v_perm_b32 v123, v87, v86, s63\n"
        "v_mul_f32 v68, v68, v28\n"
        "v_mul_f32 v69, v69, v29\n"
        "v_mul_f32 v70, v70, v30\n"
        "v_mfma_f32_16x16x32_bf16 v[152:155], v[32:35], v[144:147], 0\n"
        "ds_read_b128 v[84:87], v0 offset:10288\n"
        "v_perm_b32 v106, v157, v156, s63\n"
        "v_perm_b32 v107, v159, v158, s63\n"
        "v_mul_f32 v71, v71, v31\n"
        "v_mul_f32 v72, s58, v72\n"
        "v_mul_f32 v68, v68, v95\n"
        "v_mul_f32 v69, v69, v95\n"
        "v_mul_f32 v70, v70, v95\n"
        "v_mfma_f32_16x16x32_bf16 v[152:155], v[36:39], v[116:119], v[152:155]\n"
        "ds_read_b128 v[144:147], v0 offset:9264\n"
        "v_mul_f32 v71, v71, v95\n"
        "v_fma_f32 v72, v72, v95, v19\n"
        "v_perm_b32 v64, v69, v68, s63\n"
        "v_perm_b32 v66, v71, v70, s63\n"
        "v_perm_b32 v65, 0, v72, s63\n"
        "v_mul_f32 v108, v108, v20\n"
        "v_mul_f32 v109, v109, v21\n"
        "v_mfma_f32_16x16x32_bf16 v[152:155], v[60:63], v[112:115], v[152:155]\n"
        "s_waitcnt lgkmcnt(2)\n"
        "v_mul_f32 v110, v110, v22\n"
        "v_mul_f32 v111, v111, v23\n"
        "v_mul_f32 v140, v140, v24\n"
        "v_mul_f32 v141, v141, v25\n"
        "v_mul_f32 v142, v142, v26\n"
        "v_mul_f32 v143, v143, v27\n"
        "v_mul_f32 v108, v108, v164\n"
        "v_perm_b32 v112, v153, v152, s63\n"
        "v_mfma_f32_16x16x32_bf16 v[20:23], v[60:63], v[148:151], 0\n"
        "s_waitcnt lgkmcnt(1)\n"
        "v_perm_b32 v116, v155, v154, s63\n"
        "v_mul_f32 v109, v109, v164\n"
        "v_mul_f32 v110, v110, v164\n"
        "v_mul_f32 v111, v111, v164\n"
        "v_mul_f32 v140, v140, v164\n"
        "v_mul_f32 v141, v141, v164\n"
        "v_mul_f32 v142, v142, v164\n"
        "v_mul_f32 v143, v143, v164\n"
        "v_mfma_f32_16x16x32_bf16 v[20:23], v[36:39], v[84:87], v[20:23]\n"
        "s_waitcnt lgkmcnt(0)\n"
        "v_perm_b32 v24, v109, v108, s63\n"
        "v_perm_b32 v25, v111, v110, s63\n"
        "v_perm_b32 v26, v141, v140, s63\n"
        "v_perm_b32 v27, v143, v142, s63\n"
        "v_mul_f32 v100, v100, v28\n"
        "v_mul_f32 v101, v101, v29\n"
        "v_mul_f32 v102, v102, v30\n"
        "v_mul_f32 v103, v103, v31\n"
        "v_mfma_f32_16x16x32_bf16 v[20:23], v[32:35], v[144:147], v[20:23]\n"
        "v_mul_f32 v44, s58, v44\n"
        "v_mul_f32 v100, v100, v164\n"
        "v_mul_f32 v101, v101, v164\n"
        "v_mul_f32 v102, v102, v164\n"
        "v_mul_f32 v103, v103, v164\n"
        "v_fma_f32 v44, v44, v164, v19\n"
        "v_perm_b32 v28, v101, v100, s63\n"
        "v_perm_b32 v30, v103, v102, s63\n"
        "v_mfma_f32_16x16x32_bf16 v[68:71], v[132:135], v[144:147], 0\n"
        "v_perm_b32 v77, v21, v20, s63\n"
        "v_perm_b32 v81, v23, v22, s63\n"
        "v_perm_b32 v29, 0, v44, s63\n"
        "v_mfma_f32_16x16x32_bf16 v[68:71], v[136:139], v[84:87], v[68:71]\n"
        "v_mfma_f32_16x16x32_bf16 v[68:71], v[52:55], v[148:151], v[68:71]\n"
        "ds_read_b128 v[20:23], v0 offset:12352\n"
        "v_mfma_f32_16x16x32_bf16 v[72:75], v[160:163], v[148:151], 0\n"
        "ds_read_b128 v[108:111], v0 offset:13376\n"
        "v_mfma_f32_16x16x32_bf16 v[72:75], v[128:131], v[84:87], v[72:75]\n"
        "ds_read_b128 v[140:143], v0 offset:14400\n"
        "v_mfma_f32_16x16x32_bf16 v[72:75], v[124:127], v[144:147], v[72:75]\n"
        "s_waitcnt lgkmcnt(2)\n"
        "v_mfma_f32_16x16x32_bf16 v[144:147], v[124:127], v[20:23], 0\n"
        "v_perm_b32 v46, v69, v68, s63\n"
        "v_perm_b32 v47, v71, v70, s63\n"
        "s_waitcnt lgkmcnt(1)\n"
        "v_mfma_f32_16x16x32_bf16 v[144:147], v[128:131], v[108:111], v[144:147]\n"
        "s_waitcnt lgkmcnt(0)\n"
        "v_mfma_f32_16x16x32_bf16 v[144:147], v[160:163], v[140:143], v[144:147]\n"
        "v_perm_b32 v44, v73, v72, s63\n"
        "v_perm_b32 v45, v75, v74, s63\n"
        "v_mfma_f32_16x16x32_bf16 v[148:151], v[52:55], v[140:143], 0\n"
        "v_mfma_f32_16x16x32_bf16 v[148:151], v[136:139], v[108:111], v[148:151]\n"
        "v_mfma_f32_16x16x32_bf16 v[148:151], v[132:135], v[20:23], v[148:151]\n"
        "ds_read_b128 v[72:75], v0 offset:17488\n"
        "v_mfma_f32_16x16x32_bf16 v[84:87], v[32:35], v[20:23], 0\n"
        "ds_read_b128 v[68:71], v0 offset:16464\n"
        "v_perm_b32 v100, v145, v144, s63\n"
        "v_perm_b32 v101, v147, v146, s63\n"
        "v_mfma_f32_16x16x32_bf16 v[84:87], v[36:39], v[108:111], v[84:87]\n"
        "ds_read_b128 v[20:23], v0 offset:15440\n"
        "v_mfma_f32_16x16x32_bf16 v[84:87], v[60:63], v[140:143], v[84:87]\n"
        "v_perm_b32 v102, v149, v148, s63\n"
        "v_perm_b32 v103, v151, v150, s63\n"
        "s_waitcnt lgkmcnt(2)\n"
        "v_mfma_f32_16x16x32_bf16 v[108:111], v[60:63], v[72:75], 0\n"
        "s_waitcnt lgkmcnt(1)\n"
        "v_mfma_f32_16x16x32_bf16 v[108:111], v[36:39], v[68:71], v[108:111]\n"
        "s_waitcnt lgkmcnt(0)\n"
        "v_mfma_f32_16x16x32_bf16 v[108:111], v[32:35], v[20:23], v[108:111]\n"
        "ds_read2_b32 v[82:83], v11 offset0:0 offset1:3\n"
        "v_perm_b32 v89, v85, v84, s63\n"
        "v_perm_b32 v93, v87, v86, s63\n"
        "v_mov_b32_dpp v59, v19 row_shr:4 row_mask:0xf bank_mask:0xf bound_ctrl:0\n"
        "v_mfma_f32_16x16x32_bf16 v[36:39], v[132:135], v[20:23], 0\n"
        "ds_read_b64_tr_b16 v[78:79], v9 offset:40064\n"
        "v_mfma_f32_16x16x32_bf16 v[36:39], v[136:139], v[68:71], v[36:39]\n"
        "ds_read2_b32 v[94:95], v11 offset0:1 offset1:4\n"
        "v_perm_b32 v113, v109, v108, s63\n"
        "v_perm_b32 v117, v111, v110, s63\n"
        "v_mfma_f32_16x16x32_bf16 v[36:39], v[52:55], v[72:75], v[36:39]\n"
        "s_add_u32 s31, s31, 1\n"
        "ds_read_b64_tr_b16 v[90:91], v9 offset:40080\n"
        "v_mfma_f32_16x16x32_bf16 v[32:35], v[160:163], v[72:75], 0\n"
        "s_cmp_ge_u32 s31, s71\n"
        "ds_read2_b32 v[118:119], v11 offset0:2 offset1:5\n"
        "v_mfma_f32_16x16x32_bf16 v[32:35], v[128:131], v[68:71], v[32:35]\n"
        "s_cbranch_scc1 L56B_%=\n"
        "L57B_%=:\n"
        "ds_read_b64_tr_b16 v[114:115], v9 offset:40096\n"
        "v_perm_b32 v110, v37, v36, s63\n"
        "v_perm_b32 v111, v39, v38, s63\n"
        "v_mfma_f32_16x16x32_bf16 v[32:35], v[124:127], v[20:23], v[32:35]\n"
        "s_add_u32 s16, s16, 256\n"
        "v_mfma_f32_16x16x32_bf16 v[20:23], v[48:51], v[40:43], 0\n"
        "s_waitcnt lgkmcnt(4)\n"
        "s_cmp_ge_u32 s31, s43\n"
        "v_smfmac_f32_16x16x64_bf16 v[20:23], v[56:59], v[76:83], v18\n"
        "s_cselect_b32 s39, s22, s16\n"
        "v_mfma_f32_16x16x32_bf16 v[36:39], v[48:51], v[96:99], 0\n"
        "s_waitcnt lgkmcnt(2)\n"
        "v_perm_b32 v108, v33, v32, s63\n"
        "v_perm_b32 v109, v35, v34, s63\n"
        "s_cmp_gt_u32 s31, s43\n"
        "v_smfmac_f32_16x16x64_bf16 v[36:39], v[56:59], v[88:95], v18\n"
        "s_cselect_b32 s39, s23, s39\n"
        "ds_read_b64_tr_b16 v[78:79], v9 offset:40112\n"
        "v_mfma_f32_16x16x32_bf16 v[32:35], v[48:51], v[104:107], 0\n"
        "s_waitcnt lgkmcnt(1)\n"
        "s_mul_hi_u32 s38, s39, 58835169\n"
        "v_smfmac_f32_16x16x64_bf16 v[32:35], v[56:59], v[112:119], v18\n"
        "s_mul_i32 s39, s39, 1344\n"
        "ds_read_b64_tr_b16 v[90:91], v9 offset:40128\n"
        "v_cvt_pk_bf16_f32 v152, v20, v36\n"
        "v_cvt_pk_bf16_f32 v156, v21, v37\n"
        "v_cvt_pk_bf16_f32 v160, v22, v38\n"
        "v_cvt_pk_bf16_f32 v164, v23, v39\n"
        "v_mfma_f32_16x16x32_bf16 v[68:71], v[48:51], v[44:47], 0\n"
        "s_mul_i32 s38, s38, 588672\n"
        "s_waitcnt lgkmcnt(1)\n"
        "v_smfmac_f32_16x16x64_bf16 v[68:71], v[56:59], v[76:83], v18 abid:1\n"
        "s_add_u32 s39, s39, s38\n"
        "ds_read_b64_tr_b16 v[114:115], v9 offset:40144\n"
        "v_mfma_f32_16x16x32_bf16 v[60:63], v[48:51], v[100:103], 0\n"
        "s_add_u32 s39, s39, s17\n"
        "ds_read_b128 v[36:39], v2\n"
        "v_mov_b64 v[20:21], 0\n"
        "v_mov_b64 v[22:23], 0\n"
        "s_waitcnt lgkmcnt(2)\n"
        "v_smfmac_f32_16x16x64_bf16 v[60:63], v[56:59], v[88:95], v18 abid:1\n"
        "s_mul_i32 s68, s31, 3\n"
        "ds_read_b128 v[52:55], v3\n"
        "v_cvt_pk_bf16_f32 v153, v32, v68\n"
        "v_cvt_pk_bf16_f32 v157, v33, v69\n"
        "v_cvt_pk_bf16_f32 v161, v34, v70\n"
        "v_cvt_pk_bf16_f32 v165, v35, v71\n"
        "v_mfma_f32_16x16x32_bf16 v[68:71], v[48:51], v[108:111], 0\n"
        "s_bitcmp1_b32 s31, 0\n"
        "v_mov_b64 v[32:33], 0\n"
        "v_mov_b64 v[34:35], 0\n"
        "s_waitcnt lgkmcnt(2)\n"
        "v_smfmac_f32_16x16x64_bf16 v[68:71], v[56:59], v[112:119], v18 abid:1\n"
        "s_cselect_b32 s38, 3, 6\n"
        "s_waitcnt lgkmcnt(1)\n"
        "v_mov_b32_dpp v67, v19 row_shr:4 row_mask:0xf bank_mask:0xf bound_ctrl:0\n"
        "v_smfmac_f32_16x16x64_bf16 v[20:23], v[36:39], v[40:47], v4\n"
        "s_add_u32 s68, s68, s38\n"
        "v_mov_b64 v[48:49], 0\n"
        "v_mov_b64 v[50:51], 0\n"
        "s_waitcnt lgkmcnt(0)\n"
        "v_smfmac_f32_16x16x64_bf16 v[20:23], v[52:55], v[76:83], v4 abid:1\n"
        "s_add_u32 s37, s35, 0\n"
        "s_cmp_lt_u32 s31, s14\n"
        "v_cvt_pk_bf16_f32 v154, v60, v68\n"
        "v_cvt_pk_bf16_f32 v158, v61, v69\n"
        "v_cvt_pk_bf16_f32 v162, v62, v70\n"
        "v_cvt_pk_bf16_f32 v166, v63, v71\n"
        "v_smfmac_f32_16x16x64_bf16 v[32:35], v[36:39], v[96:103], v4\n"
        "s_cselect_b32 s68, s68, 0\n"
        "v_add_f32 v20, v20, v21\n"
        "v_add_f32 v22, v22, v23\n"
        "v_smfmac_f32_16x16x64_bf16 v[32:35], v[52:55], v[88:95], v4 abid:1\n"
        "s_and_b32 s38, s31, 7\n"
        "ds_read_b64_tr_b16 v[78:79], v9 offset:43392\n"
        "v_smfmac_f32_16x16x64_bf16 v[48:51], v[36:39], v[104:111], v4\n"
        "buffer_store_dwordx3 v[152:154], v12, s[8:11], s37 offen sc0 sc1 nt\n"
        "s_cselect_b32 s30, 1, -7\n"
        "v_smfmac_f32_16x16x64_bf16 v[48:51], v[52:55], v[112:119], v4 abid:1\n"
        "s_mul_i32 s29, s30, 192\n"
        "ds_read_b64_tr_b16 v[90:91], v9 offset:43408\n"
        "v_add_f32 v32, v32, v33\n"
        "v_add_f32 v34, v34, v35\n"
        "v_cvt_pk_bf16_f32 v148, v20, v32\n"
        "v_mfma_f32_16x16x32_bf16 v[152:155], v[120:123], v[40:43], 0\n"
        "s_mul_i32 s30, s30, 392\n"
        "s_waitcnt lgkmcnt(1)\n"
        "v_smfmac_f32_16x16x64_bf16 v[152:155], v[64:67], v[76:83], v18\n"
        "s_add_u32 s37, s35, 0\n"
        "s_cmp_eq_u32 s69, s18\n"
        "ds_read_b64_tr_b16 v[114:115], v9 offset:43424\n"
        "v_add_f32 v48, v48, v49\n"
        "v_add_f32 v50, v50, v51\n"
        "v_cvt_pk_bf16_f32 v149, v48, v22\n"
        "v_cvt_pk_bf16_f32 v150, v34, v50\n"
        "v_mfma_f32_16x16x32_bf16 v[56:59], v[120:123], v[96:99], 0\n"
        "s_cselect_b32 s34, -7488, s70\n"
        "s_waitcnt lgkmcnt(1)\n"
        "v_smfmac_f32_16x16x64_bf16 v[56:59], v[64:67], v[88:95], v18\n"
        "s_add_u32 s69, s69, s34\n"
        "ds_read_b64_tr_b16 v[78:79], v9 offset:43440\n"
        "v_writelane_b32 v10, s69, 56\n"
        "v_add_u32 v11, s30, v11\n"
        "s_mov_b64 exec, s[54:55]\n"
        "v_add_u32 v16, s29, v16\n"
        "v_add_u32 v8, s34, v8\n"
        "s_mov_b64 exec, -1\n"
        "v_mfma_f32_16x16x32_bf16 v[72:75], v[120:123], v[104:107], 0\n"
        "buffer_store_dwordx3 v[156:158], v15, s[8:11], s37 offen offset:768 sc0 sc1 nt\n"
        "s_mov_b64 exec, s[50:51]\n"
        "v_add_u32 v5, s29, v5\n"
        "v_add_u32 v6, s34, v6\n"
        "s_mov_b64 exec, -1\n"
        "s_waitcnt lgkmcnt(1)\n"
        "v_smfmac_f32_16x16x64_bf16 v[72:75], v[64:67], v[112:119], v18\n"
        "ds_read_b64_tr_b16 v[90:91], v9 offset:43456\n"
        "v_mov_b32_dpp v31, v19 row_shr:4 row_mask:0xf bank_mask:0xf bound_ctrl:0\n"
        "v_mfma_f32_16x16x32_bf16 v[20:23], v[120:123], v[44:47], 0\n"
        "s_waitcnt lgkmcnt(1)\n"
        "v_smfmac_f32_16x16x64_bf16 v[20:23], v[64:67], v[76:83], v18 abid:1\n"
        "s_add_u32 s37, s35, 98112\n"
        "ds_read_b64_tr_b16 v[114:115], v9 offset:43472\n"
        "v_mfma_f32_16x16x32_bf16 v[84:87], v[120:123], v[100:103], 0\n"
        "s_waitcnt lgkmcnt(1)\n"
        "v_smfmac_f32_16x16x64_bf16 v[84:87], v[64:67], v[88:95], v18 abid:1\n"
        "ds_read_b64_tr_b16 v[78:79], v9 offset:46720\n"
        "v_mfma_f32_16x16x32_bf16 v[60:63], v[120:123], v[108:111], 0\n"
        "buffer_store_dwordx3 v[160:162], v12, s[8:11], s37 offen offset:192 sc0 sc1 nt\n"
        "ds_read_b32 v167, v0 offset:21600\n"
        "s_waitcnt lgkmcnt(2)\n"
        "v_smfmac_f32_16x16x64_bf16 v[60:63], v[64:67], v[112:119], v18 abid:1\n"
        "ds_read_b64_tr_b16 v[90:91], v9 offset:46736\n"
        "v_mfma_f32_16x16x32_bf16 v[68:71], v[24:27], v[40:43], 0\n"
        "s_waitcnt lgkmcnt(2)\n"
        "v_smfmac_f32_16x16x64_bf16 v[68:71], v[28:31], v[76:83], v18\n"
        "s_add_u32 s37, s35, 98112\n"
        "ds_read_b64_tr_b16 v[114:115], v9 offset:46752\n"
        "v_mfma_f32_16x16x32_bf16 v[36:39], v[24:27], v[96:99], 0\n"
        "s_waitcnt lgkmcnt(1)\n"
        "v_smfmac_f32_16x16x64_bf16 v[36:39], v[28:31], v[88:95], v18\n"
        "ds_read_b64_tr_b16 v[78:79], v9 offset:46768\n"
        "v_readfirstlane_b32 s40, v167\n"
        "v_mfma_f32_16x16x32_bf16 v[40:43], v[24:27], v[104:107], 0\n"
        "buffer_store_dwordx3 v[164:166], v14, s[8:11], s37 offen offset:960 sc0 sc1 nt\n"
        "s_add_u32 s37, s35, 588672\n"
        "s_add_u32 s46, s37, 1344\n"
        "s_cmp_lt_i32 s40, s68\n"
        "s_waitcnt lgkmcnt(1)\n"
        "v_smfmac_f32_16x16x64_bf16 v[40:43], v[28:31], v[112:119], v18\n"
        "buffer_store_dwordx3 v[148:150], v12, s[44:47], s37 offen offset:1152 sc0 sc1 nt\n"
        "ds_read_b64_tr_b16 v[90:91], v9 offset:46784\n"
        "v_mfma_f32_16x16x32_bf16 v[64:67], v[24:27], v[44:47], 0\n"
        "s_waitcnt lgkmcnt(1)\n"
        "v_smfmac_f32_16x16x64_bf16 v[64:67], v[28:31], v[76:83], v18 abid:1\n"
        "ds_read_b64_tr_b16 v[114:115], v9 offset:46800\n"
        "s_mov_b64 exec, 1\n"
        "ds_inc_u32 v0, v1 offset:21612\n"
        "s_mov_b64 exec, -1\n"
        "s_cbranch_scc1 L60B_%=\n"
        "L61B_%=:\n"
        "ds_read_b128 v[124:127], v7 offset:0\n"
        "ds_read_b128 v[132:135], v7 offset:3328\n"
        "ds_read_b128 v[128:131], v7 offset:64\n"
        "ds_read_b128 v[136:139], v7 offset:3392\n"
        "v_mfma_f32_16x16x32_bf16 v[104:107], v[24:27], v[100:103], 0\n"
        "s_waitcnt lgkmcnt(6)\n"
        "ds_read_b128 v[160:163], v7 offset:128\n"
        "ds_read_b128 v[52:55], v7 offset:3456\n"
        "s_mov_b32 s36, s35\n"
        "v_smfmac_f32_16x16x64_bf16 v[104:107], v[28:31], v[88:95], v18 abid:1\n"
        "s_mov_b32 s35, s39\n"
        "v_mfma_f32_16x16x32_bf16 v[88:91], v[24:27], v[108:111], 0\n"
        "s_waitcnt lgkmcnt(7)\n"
        "v_smfmac_f32_16x16x64_bf16 v[88:91], v[28:31], v[112:119], v18 abid:1\n"
        "s_setprio 0\n"
        "s_waitcnt lgkmcnt(0)\n"
        "s_nop 15\n"
        "s_branch CNEXTB_%=\n"
        "MPRO_%=:\n"
        "s_lshl_b32 s7, s3, 11\n"
        "s_add_u32 s7, s7, 68608\n"
        "s_add_u32 s22, s4, s7\n"
        "s_addc_u32 s23, s5, 0\n"
        "global_load_dwordx4 v[240:243], v57, s[22:23] offset:0\n"
        "global_load_dwordx4 v[244:247], v57, s[22:23] offset:1024\n"
        "v_mov_b32 v250, 21600\n"
        "v_lshl_add_u32 v27, v56, 2, v250\n"
        "v_mov_b32 v28, 0\n"
        "s_mov_b32 exec_hi, 0\n"
        "ds_write_b32 v27, v28\n"
        "s_mov_b32 exec_hi, -1\n"
        "s_mov_b32 s41, 0x400000\n"
        "s_mov_b32 s27, 0x20000\n"
        "s_lshr_b32 s61, s3, 1\n"
        "s_mul_i32 s61, s61, 192\n"
        "s_cmp_lt_u32 s3, 6\n"
        "s_cselect_b32 s61, s61, 0\n"
        "s_cselect_b32 s62, 3, 16\n"
        "s_cselect_b32 s63, 12, 32\n"
        "s_cselect_b32 s26, 3, 1\n"
        "s_cselect_b32 s70, 72, 1\n"
        "s_cselect_b32 s71, 36, 4\n"
        "s_cselect_b32 s72, 6, 8\n"
        "s_mul_i32 s62, s62, 192\n"
        "s_mul_i32 s63, s63, 192\n"
        "s_mul_i32 s70, s70, 192\n"
        "s_mul_i32 s71, s71, 192\n"
        "s_mul_i32 s72, s72, 192\n"
        "s_mul_i32 s26, s26, 18432\n"
        "s_add_u32 s7, s62, s63\n"
        "s_lshl_b32 s29, s63, 1\n"
        "s_add_u32 s60, s7, s63\n"
        "s_bitcmp1_b32 s6, 0\n"
        "s_cselect_b32 s28, s7, 0\n"
        "s_cselect_b32 s29, s29, s62\n"
        "s_cselect_b32 s60, s60, s63\n"
        "s_add_u32 s28, s28, s61\n"
        "s_add_u32 s29, s29, s61\n"
        "s_add_u32 s60, s60, s61\n"
        "v_add_u32 v24, 0, v56\n"
        "v_mul_u32_u24 v21, 171, v24\n"
        "v_lshrrev_b32 v21, 11, v21\n"
        "v_mad_i32_i24 v24, v21, -12, v24\n"
        "v_bfe_u32 v58, v21, 2, 2\n"
        "v_xor_b32 v58, v24, v58\n"
        "v_and_b32 v27, 3, v21\n"
        "v_mul_u32_u24 v27, s70, v27\n"
        "v_lshl_add_u32 v32, v58, 4, v27\n"
        "v_bfe_u32 v27, v21, 2, 1\n"
        "v_mad_u32_u24 v32, v27, s71, v32\n"
        "v_lshrrev_b32 v27, 3, v21\n"
        "v_mad_u32_u24 v32, v27, s72, v32\n"
        "v_and_b32 v27, 7, v21\n"
        "v_mul_u32_u24 v27, s70, v27\n"
        "v_lshl_add_u32 v58, v58, 4, v27\n"
        "v_add_u32 v25, 64, v56\n"
        "v_mul_u32_u24 v22, 171, v25\n"
        "v_lshrrev_b32 v22, 11, v22\n"
        "v_mad_i32_i24 v25, v22, -12, v25\n"
        "v_bfe_u32 v59, v22, 2, 2\n"
        "v_xor_b32 v59, v25, v59\n"
        "v_and_b32 v27, 3, v22\n"
        "v_mul_u32_u24 v27, s70, v27\n"
        "v_lshl_add_u32 v33, v59, 4, v27\n"
        "v_bfe_u32 v27, v22, 2, 1\n"
        "v_mad_u32_u24 v33, v27, s71, v33\n"
        "v_lshrrev_b32 v27, 3, v22\n"
        "v_mad_u32_u24 v33, v27, s72, v33\n"
        "v_and_b32 v27, 7, v22\n"
        "v_mul_u32_u24 v27, s70, v27\n"
        "v_lshl_add_u32 v59, v59, 4, v27\n"
        "v_lshrrev_b32 v27, 3, v22\n"
        "v_mad_u32_u24 v59, v27, s70, v59\n"
        "v_add_u32 v26, 128, v56\n"
        "v_mul_u32_u24 v23, 171, v26\n"
        "v_lshrrev_b32 v23, 11, v23\n"
        "v_mad_i32_i24 v26, v23, -12, v26\n"
        "v_bfe_u32 v60, v23, 2, 2\n"
        "v_xor_b32 v60, v26, v60\n"
        "v_and_b32 v27, 3, v23\n"
        "v_mul_u32_u24 v27, s70, v27\n"
        "v_lshl_add_u32 v34, v60, 4, v27\n"
        "v_bfe_u32 v27, v23, 2, 1\n"
        "v_mad_u32_u24 v34, v27, s71, v34\n"
        "v_lshrrev_b32 v27, 3, v23\n"
        "v_mad_u32_u24 v34, v27, s72, v34\n"
        "v_and_b32 v27, 7, v23\n"
        "v_mul_u32_u24 v27, s70, v27\n"
        "v_lshl_add_u32 v60, v60, 4, v27\n"
        "v_add_u32 v60, s70, v60\n"
        "s_mul_i32 s64, s3, 9216\n"
        "s_add_u32 s64, s64, 21856\n"
        "s_mul_i32 s7, s6, 7168\n"
        "s_add_u32 s7, s7, 6144\n"
        "s_add_u32 s22, s4, s7\n"
        "s_addc_u32 s23, s5, 0\n"
        "s_waitcnt lgkmcnt(0)\n"
        "s_cmp_lt_u32 s3, 6\n"
        "s_cselect_b64 s[24:25], s[50:51], s[52:53]\n"
        "s_and_b32 s25, s25, 0xffff\n"
        "s_cmp_lt_u32 s3, 4\n"
        "s_cbranch_scc0 L87_%=\n"
        "s_add_u32 m0, s64, 0\n"
        "s_nop 0\n"
        "buffer_load_dwordx4 v32, s[24:27], s28 offen lds\n"
        "s_add_u32 m0, s64, 1024\n"
        "s_nop 0\n"
        "buffer_load_dwordx4 v33, s[24:27], s28 offen lds\n"
        "s_add_u32 m0, s64, 2048\n"
        "s_nop 0\n"
        "buffer_load_dwordx4 v34, s[24:27], s28 offen lds\n"
        "s_add_u32 m0, s64, 3072\n"
        "s_nop 0\n"
        "buffer_load_dwordx4 v32, s[24:27], s29 offen lds\n"
        "s_add_u32 m0, s64, 4096\n"
        "s_nop 0\n"
        "buffer_load_dwordx4 v33, s[24:27], s29 offen lds\n"
        "s_add_u32 m0, s64, 5120\n"
        "s_nop 0\n"
        "buffer_load_dwordx4 v34, s[24:27], s29 offen lds\n"
        "s_add_u32 m0, s64, 6144\n"
        "s_nop 0\n"
        "buffer_load_dwordx4 v32, s[24:27], s60 offen lds\n"
        "s_add_u32 m0, s64, 7168\n"
        "s_nop 0\n"
        "buffer_load_dwordx4 v33, s[24:27], s60 offen lds\n"
        "s_add_u32 m0, s64, 8192\n"
        "s_nop 0\n"
        "buffer_load_dwordx4 v34, s[24:27], s60 offen lds\n"
        "s_barrier\n"
        "s_setprio 1\n"
        "s_waitcnt vmcnt(0)\n"
        "s_mov_b64 exec, 1\n"
        "ds_inc_u32 v250, v250 offset:16\n"
        "s_mov_b64 exec, -1\n"
        "L91_%=:\n"
        "ds_read_b32 v251, v250 offset:16\n"
        "s_waitcnt lgkmcnt(0)\n"
        "v_readfirstlane_b32 s40, v251\n"
        "s_nop 3\n"
        "s_cmp_ge_i32 s40, 4\n"
        "s_cbranch_scc1 L92_%=\n"
        "s_nop 0\n"
        "s_sub_u32 s41, s41, 1\n"
        "s_cbranch_scc0 L91_%=\n"
        "s_endpgm\n"
        "L92_%=:\n"
        "s_cmp_eq_u32 s6, 3\n"
        "s_cbranch_scc1 L93_%=\n"
        "s_mov_b32 s34, 0x4162ccfd\n"
        "s_mov_b32 s35, 21624\n"
        "s_mov_b32 s36, 21624\n"
        "s_mov_b32 s37, 21656\n"
        "s_call_b64 s[30:31], FOLDM_%=\n"
        "s_branch L94_%=\n"
        "L93_%=:\n"
        "s_mov_b32 s34, 0xbe173353\n"
        "s_mov_b32 s35, 21656\n"
        "s_mov_b32 s36, 21656\n"
        "s_mov_b32 s37, 21624\n"
        "s_call_b64 s[30:31], FOLDR_%=\n"
        "s_branch L89_%=\n"
        "L94_%=:\n"
        "s_mov_b32 s7, 21728\n"
        "s_mov_b32 s28, s6\n"
        "s_branch L88_%=\n"
        "L87_%=:\n"
        "s_barrier\n"
        "s_add_u32 m0, s64, 0\n"
        "s_nop 0\n"
        "buffer_load_dwordx4 v32, s[24:27], s28 offen lds\n"
        "s_add_u32 m0, s64, 1024\n"
        "s_nop 0\n"
        "buffer_load_dwordx4 v33, s[24:27], s28 offen lds\n"
        "s_add_u32 m0, s64, 2048\n"
        "s_nop 0\n"
        "buffer_load_dwordx4 v34, s[24:27], s28 offen lds\n"
        "s_add_u32 m0, s64, 3072\n"
        "s_nop 0\n"
        "buffer_load_dwordx4 v32, s[24:27], s29 offen lds\n"
        "s_add_u32 m0, s64, 4096\n"
        "s_nop 0\n"
        "buffer_load_dwordx4 v33, s[24:27], s29 offen lds\n"
        "s_add_u32 m0, s64, 5120\n"
        "s_nop 0\n"
        "buffer_load_dwordx4 v34, s[24:27], s29 offen lds\n"
        "s_add_u32 m0, s64, 6144\n"
        "s_nop 0\n"
        "buffer_load_dwordx4 v32, s[24:27], s60 offen lds\n"
        "s_add_u32 m0, s64, 7168\n"
        "s_nop 0\n"
        "buffer_load_dwordx4 v33, s[24:27], s60 offen lds\n"
        "s_add_u32 m0, s64, 8192\n"
        "s_nop 0\n"
        "buffer_load_dwordx4 v34, s[24:27], s60 offen lds\n"
        "s_setprio 0\n"
        "s_mov_b64 s[24:25], s[56:57]\n"
        "s_waitcnt vmcnt(0)\n"
        "s_mov_b64 exec, 1\n"
        "ds_inc_u32 v250, v250 offset:20\n"
        "s_mov_b64 exec, -1\n"
        "L95_%=:\n"
        "ds_read_b32 v251, v250 offset:20\n"
        "s_waitcnt lgkmcnt(0)\n"
        "v_readfirstlane_b32 s40, v251\n"
        "s_nop 3\n"
        "s_cmp_ge_i32 s40, 4\n"
        "s_cbranch_scc1 L96_%=\n"
        "s_nop 0\n"
        "s_sub_u32 s41, s41, 1\n"
        "s_cbranch_scc0 L95_%=\n"
        "s_endpgm\n"
        "L96_%=:\n"
        "s_cmp_eq_u32 s6, 0\n"
        "s_cbranch_scc1 L97_%=\n"
        "s_mov_b32 s34, 0x411da57f\n"
        "s_mov_b32 s35, 21632\n"
        "s_mov_b32 s36, 21632\n"
        "s_mov_b32 s37, 21640\n"
        "s_call_b64 s[30:31], FOLDW_%=\n"
        "s_branch L98_%=\n"
        "L97_%=:\n"
        "s_mov_b64 exec, 1\n"
        "ds_inc_u32 v250, v250 offset:32\n"
        "s_mov_b64 exec, -1\n"
        "s_mov_b64 exec, 1\n"
        "ds_inc_u32 v250, v250 offset:40\n"
        "s_mov_b64 exec, -1\n"
        "s_mov_b64 exec, 1\n"
        "v_mov_b32 v27, 0\n"
        "v_mov_b32 v28, 0\n"
        "global_atomic_swap v252, v27, v28, s[24:25] offset:0 sc0\n"
        "s_mov_b64 exec, -1\n"
        "s_branch L89_%=\n"
        "L98_%=:\n"
        "L99_%=:\n"
        "ds_read_b32 v251, v250 offset:40\n"
        "s_waitcnt lgkmcnt(0)\n"
        "v_readfirstlane_b32 s40, v251\n"
        "s_nop 3\n"
        "s_cmp_ge_i32 s40, 4\n"
        "s_cbranch_scc1 L100_%=\n"
        "s_nop 0\n"
        "s_sub_u32 s41, s41, 1\n"
        "s_cbranch_scc0 L99_%=\n"
        "s_endpgm\n"
        "L100_%=:\n"
        "s_mov_b32 s7, -18528\n"
        "s_add_u32 s28, s6, 2\n"
        "L88_%=:\n"
        "s_mul_i32 s29, s28, 6176\n"
        "s_add_u32 s7, s7, s29\n"
        "v_add_u32 v27, s7, v57\n"
        "s_mul_i32 s28, s28, 6144\n"
        "s_add_u32 s28, s28, 10240\n"
        "s_add_u32 s70, s56, s28\n"
        "s_addc_u32 s71, s57, 0\n"
        "ds_read_b128 v[64:67], v27 offset:0\n"
        "ds_read_b128 v[68:71], v27 offset:1024\n"
        "ds_read_b128 v[72:75], v27 offset:2048\n"
        "ds_read_b128 v[76:79], v27 offset:3088\n"
        "ds_read_b128 v[80:83], v27 offset:4112\n"
        "ds_read_b128 v[84:87], v27 offset:5136\n"
        "s_waitcnt lgkmcnt(5)\n"
        "global_store_dwordx4 v57, v[64:67], s[70:71] offset:-2048 sc1\n"
        "s_waitcnt lgkmcnt(4)\n"
        "global_store_dwordx4 v57, v[68:71], s[70:71] offset:-1024 sc1\n"
        "s_waitcnt lgkmcnt(3)\n"
        "global_store_dwordx4 v57, v[72:75], s[70:71] offset:0 sc1\n"
        "s_waitcnt lgkmcnt(2)\n"
        "global_store_dwordx4 v57, v[76:79], s[70:71] offset:1024 sc1\n"
        "s_waitcnt lgkmcnt(1)\n"
        "global_store_dwordx4 v57, v[80:83], s[70:71] offset:2048 sc1\n"
        "s_waitcnt lgkmcnt(0)\n"
        "global_store_dwordx4 v57, v[84:87], s[70:71] offset:3072 sc1\n"
        "L89_%=:\n"
        "s_waitcnt vmcnt(0) lgkmcnt(0)\n"
        "s_barrier\n"
        "s_setprio 0\n"
        "s_cmp_lg_u32 s3, 7\n"
        "s_cbranch_scc1 PRO_%=\n"
        "s_mov_b32 exec_hi, 0\n"
        "v_mbcnt_lo_u32_b32 v27, -1, 0\n"
        "v_lshlrev_b32 v27, 7, v27\n"
        "v_mov_b32 v28, 1\n"
        "global_atomic_add v27, v28, s[24:25] offset:256\n"
        "s_mov_b32 exec_hi, -1\n"
        "s_branch PRO_%=\n"
        :: "{v0}"(tid), "{s[0:1]}"(kap), "{s2}"(bid), "{s[4:5]}"(tab) : CLOBBERS);
}

extern "C" void kernel_launch(void* const* d_in, const int* in_sizes, int n_in, void* d_out, int out_size, void* d_ws, size_t ws_size, hipStream_t stream) {
    (void)in_sizes; (void)n_in; (void)out_size; (void)ws_size;
    (void)hipFuncSetAttribute((const void*)swin_kernel, hipFuncAttributeMaxDynamicSharedMemorySize, LDS_BYTES);
    swin_kernel<<<256, 512, LDS_BYTES, stream>>>((const char*)d_in[0], (const char*)d_in[1], (const char*)d_in[2], (const char*)d_in[3], (const char*)d_in[4], (char*)d_out, (const char*)d_ws);
}
```
